```python
import torch

def _softmax(x, axis=-1):
    return torch.softmax(x, dim=axis)

def _gelu(x):
    return torch.nn.functional.gelu(x, approximate='tanh')

def _silu(x):
    return torch.nn.functional.silu(x)

def _sigmoid(x):
    return torch.sigmoid(x)

def _layernorm(x, axis=-1, eps=1e-05):
    m = x.mean(dim=axis, keepdim=True)
    v = x.var(dim=axis, keepdim=True, unbiased=False)
    return (x - m) / torch.sqrt(v + eps)

def _scatter_add(tgt, idx, src, axis=0):
    return tgt.clone().index_add_(axis, idx, src)

def _segment_sum(src, seg, n):
    out = torch.zeros((n,) + src.shape[1:], dtype=src.dtype)
    return out.index_add_(0, seg, src)

def _pad(x, pw):
    flat = tuple((v for pair in reversed(pw) for v in pair))
    return torch.nn.functional.pad(x, flat)
kernel_name = 'standalone__neighborhood_nat__vision'

def setup_inputs():

    _w = lambda a: (lambda t: t.to(torch.bfloat16) if t.is_floating_point() else t)(torch.as_tensor(a))
    H = W = 224
    C = 192
    return {'x': _w((lambda *s: torch.randn(*s, dtype=torch.bfloat16) * 0.2)(1, H, W, C)), 'wqkv': _w((lambda *s: torch.randn(*s, dtype=torch.bfloat16) * 0.2)(C, 3 * C)), 'wo': _w((lambda *s: torch.randn(*s, dtype=torch.bfloat16) * 0.05)(C, C))}

def reference(x, wqkv, wo):
    B, H, W, C = x.shape
    ks, dil = (7, 2)
    qkv = x @ wqkv
    q, k, v = (qkv[..., :C].clone(), qkv[..., C:2 * C].clone(), qkv[..., 2 * C:].clone())
    half = (ks - 1) // 2 * dil
    ir = torch.arange(H)
    ic = torch.arange(W)
    off = torch.arange(ks) * dil
    r0 = torch.clip(ir - half, 0, H - 1 - (ks - 1) * dil)
    c0 = torch.clip(ic - half, 0, W - 1 - (ks - 1) * dil)
    ri = r0[:, None] + off[None, :]
    ci = c0[:, None] + off[None, :]
    nidx = (ri[:, None, :, None] * W + ci[None, :, None, :]).reshape(1, H * W * ks * ks, 1)
    kf = k.reshape(B, H * W, C)
    vf = v.reshape(B, H * W, C)
    kn = (lambda x, i, axis: torch.take_along_dim(x, i, dim=axis))(kf, nidx, 1).reshape(B, H * W, ks * ks, C)
    vn = (lambda x, i, axis: torch.take_along_dim(x, i, dim=axis))(vf, nidx, 1).reshape(B, H * W, ks * ks, C)
    qf = q.reshape(B, H * W, C)
    scale = C ** (-0.5)
    logits = torch.einsum('bnc,bnkc->bnk', qf, kn) * scale
    p = _softmax(logits, axis=-1)
    o = torch.einsum('bnk,bnkc->bnc', p, vn) @ wo
    return o.reshape(B, H, W, C)
```

```cpp
#include <hip/hip_runtime.h>
#include <stdint.h>

typedef unsigned short u16;
typedef unsigned long long u64;
typedef __attribute__((ext_vector_type(4))) float f32x4;
typedef __attribute__((ext_vector_type(4))) unsigned u32x4;
typedef __attribute__((ext_vector_type(2))) unsigned u32x2;
typedef __attribute__((ext_vector_type(8))) unsigned u32x8;

#define DEVINL __device__ __forceinline__
#define LAMBDA_INL __attribute__((always_inline))
#define UNI(x) __builtin_amdgcn_readfirstlane((int)(x))

static constexpr int IMW = 224;
static constexpr int PIXB = 384;
static constexpr int IROWB = IMW * PIXB;
static constexpr int RP = 8896;
static constexpr int LQ = 14 * RP, LZ = LQ + 3 * 6144, LF = LZ + 3 * 6144;
static constexpr int LDS_TOT = 163840;
static constexpr unsigned OOB = 0x100000u;
static constexpr int WS_W = 8192, WS_FL = WS_W + 144 * 1024, NREP = 8, WS_ACK = 393216;

DEVINL int pixad(int c) { return 800 * ((c + 1) >> 1) + 384 * ((c + 1) & 1) - 384; }
struct AxisMap { int o, d, x, last; };
DEVINL int axis_at(const AxisMap& m, int v) {
    const int reg = m.o + m.d * v, ext = m.x + 2 * min(v - m.last - 1, 2), sel = (m.last - v) >> 31;
    return (ext & sel) | (reg & ~sel);
}
DEVINL int scol(int i) { return (int)((0xD5B3E6C4A2F79180ull >> (4 * i)) & 15); }

struct AttC { unsigned sb, v0, v1, pk; };
DEVINL AttC att_const(int lane, int H, int cedge) {
    const int i16 = lane & 15, g = lane >> 4, e = (i16 + 4) >> 3 & 1, qj = i16 - 4 * ((i16 + 4) >> 3), sc = scol(i16);
    const int ws0 = (cedge ? min(8 * H + qj, 9) : 8 * H + qj) - 8 * H;
    AttC c;
    c.pk = 8 | e << 8;
#pragma unroll
    for (int r = 0; r < 4; ++r) {
        const unsigned ok = (unsigned)(scol(4 * g + r) - ws0) <= 6u;
        c.pk |= ok << (4 + r) | (r & 1 ? ok << (r - 1) : 0u);
    }
    c.sb = pixad(8 * H + (sc < 14 ? sc : sc - 8)) + 16 * g;
    c.v0 = pixad(8 * H + scol(4 * (g >> 1) + ((lane >> 2) & 3))) + (g & 1) * RP + 8 * (lane & 3);
    c.v1 = pixad(8 * H + scol(8 + 4 * (g >> 1) + ((lane >> 2) & 3))) + (g & 1) * RP + 8 * (lane & 3);
    return c;
}

__global__ __launch_bounds__(512, 2) void na_main(const u16* __restrict__ x_, const u16* __restrict__ wqkv_,
                                                  const u16* __restrict__ wo_, u16* __restrict__ out_, char* ws_) {
    __shared__ __attribute__((aligned(1024))) char smem[LDS_TOT];
    int tid = threadIdx.x, bid = blockIdx.x;
    u32x8 ka;
    u32x2 ka4;
    u32x4 ax, xo;
    unsigned xo4, t0;
    asm volatile(
    "s_load_dwordx8 s[8:15], s[0:1], 0x0\n"
    "s_load_dwordx2 s[16:17], s[0:1], 0x20\n"
    "s_memtime s[4:5]\n"
    "v_readfirstlane_b32 s7, v0\n"
    "s_lshr_b32 s24, s2, 3\n"
    "v_and_b32 v6, 63, v0\n"
    "s_getpc_b64 s[28:29]\n"
    ".Le_pc:\n"
    "s_lshl_b32 s25, s24, 10\n"
    "s_mov_b32 m0, 162816\n"
    "s_cmp_lg_u32 s7, 128\n"
    "s_cbranch_scc1 .Le_ncp\n"
    "v_lshl_add_u32 v7, v6, 4, s25\n"
    "global_load_lds_dwordx4 v7, s[28:29]\n"
    ".Le_ncp:\n"
    "s_lshr_b32 s27, s2, 6\n"
    "s_and_b32 s26, s24, 7\n"
    "s_lshr_b32 s7, s7, 6\n"
    "s_add_u32 s3, s27, 14\n"
    "s_cmp_eq_u32 s26, 7\n"
    "s_cselect_b32 s3, s3, 99\n"
    "s_cmp_lt_u32 s24, 16\n"
    "s_cselect_b32 s3, s24, s3\n"
    "s_cmp_lg_u32 s7, 7\n"
    "s_cbranch_scc1 .Le_np\n"
    "s_cmp_ge_u32 s3, 18\n"
    "s_cbranch_scc1 .Le_b\n"
    "s_and_b32 s23, s2, 3\n"
    "s_mul_i32 s23, s23, 18\n"
    "s_add_u32 s3, s3, s23\n"
    "s_mul_i32 s24, s3, 43\n"
    "s_lshr_b32 s24, s24, 8\n"
    "s_mul_i32 s25, s24, 6\n"
    "s_sub_u32 s25, s3, s25\n"
    "v_lshrrev_b32 v4, 2, v6\n"
    "v_lshrrev_b32 v5, 4, v6\n"
    "v_sub_u32 v3, 0, v5\n"
    "v_xor_b32 v3, v3, v6\n"
    "v_and_b32 v3, 3, v3\n"
    "v_lshlrev_b32 v3, 4, v3\n"
    "s_mul_i32 s25, s25, 36864\n"
    "s_movk_i32 s23, 1152\n"
    "s_lshr_b32 s31, s24, 1\n"
    "s_and_b32 s24, s24, 1\n"
    "v_mov_b32 v9, 0\n"
    "s_mov_b32 m0, 124544\n"
    "s_bitcmp1_b32 s2, 2\n"
    "s_cbranch_scc1 .Le_p2\n"
    "v_lshl_add_u32 v7, v5, 2, v4\n"
    "v_mad_u32_u24 v7, v7, s23, v3\n"
    "s_lshl3_add_u32 s24, s31, s24\n"
    "s_mul_i32 s24, s24, 4608\n"
    "s_addk_i32 s24, 384\n"
    "s_mov_b32 s23, 6\n"
    "s_waitcnt lgkmcnt(0)\n"
    "s_add_u32 s26, s10, s25\n"
    "s_addc_u32 s27, s11, 0\n"
    "global_load_dwordx2 v[12:13], v9, s[16:17] sc1\n"
    "s_add_u32 s28, s26, 4608\n"
    "s_addc_u32 s29, s27, 0\n"
    "s_add_u32 s30, s10, s24\n"
    "s_addc_u32 s31, s11, 0\n"
    ".Le_l1:\n"
    "global_load_lds_dwordx4 v7, s[26:27]\n"
    "s_addk_i32 m0, 1024\n"
    "s_sub_u32 s23, s23, 1\n"
    "global_load_lds_dwordx4 v7, s[28:29]\n"
    "s_addk_i32 m0, 1024\n"
    "s_nop 0\n"
    "global_load_lds_dwordx4 v7, s[30:31]\n"
    "s_addk_i32 m0, 1024\n"
    "s_cmp_lg_u32 s23, 0\n"
    "v_add_u32 v7, 64, v7\n"
    "s_cbranch_scc1 .Le_l1\n"
    "s_branch .Le_p1\n"
    ".Le_np:\n"
    "s_cmp_ge_u32 s7, 4\n"
    "s_cbranch_scc1 .Le_a\n"
    "s_and_b32 s25, s2, 1\n"
    "s_and_b32 s23, s7, 1\n"
    "s_lshl2_add_u32 s29, s25, s27\n"
    "s_mul_i32 s22, s23, 486\n"
    "s_cmp_eq_u32 s26, 7\n"
    "s_cselect_b32 s26, s29, s26\n"
    "s_cselect_b32 s31, 1, s27\n"
    "s_cselect_b32 s18, 1, 0\n"
    "v_lshl_add_u32 v1, s23, 6, v6\n"
    "v_add_u32 v5, s22, v6\n"
    "s_movk_i32 s22, 1311\n"
    "v_add_u32 v2, 64 + 24 + 128, v1\n"
    "v_add_u32 v3, 64 + 24 + 256, v1\n"
    "v_add_u32 v4, 64 + 24 + 384, v1\n"
    "v_add_u32 v1, 64 + 24, v1\n"
    "v_add_u32 v5, 24, v5\n"
    "v_mul_u32_u24 v7, s22, v1\n"
    "s_lshl_b32 s29, s26, 5\n"
    "v_mul_u32_u24 v8, s22, v2\n"
    "s_sub_u32 s20, 222, s29\n"
    "v_mul_u32_u24 v9, s22, v3\n"
    "s_add_u32 s29, s29, 1\n"
    "v_mul_u32_u24 v10, s22, v4\n"
    "s_bitcmp1_b32 s2, 1\n"
    "v_mul_u32_u24 v11, s22, v5\n"
    "s_cselect_b32 s20, s29, s20\n"
    "v_not_b32 v12, 49\n"
    "v_lshrrev_b32 v7, 16, v7\n"
    "s_cselect_b32 s21, 2, -2\n"
    "v_lshrrev_b32 v8, 16, v8\n"
    "s_cselect_b32 s30, 218, 1\n"
    "v_lshrrev_b32 v9, 16, v9\n"
    "v_lshrrev_b32 v10, 16, v10\n"
    "v_lshrrev_b32 v11, 16, v11\n"
    "s_sub_u32 s29, s20, s21\n"
    "s_mul_i32 s29, s29, 384\n"
    "s_mul_i32 s22, s21, 768\n"
    "v_mad_i32_i24 v1, v7, v12, v1\n"
    "s_lshl_b32 s28, s27, 3\n"
    "v_mad_i32_i24 v2, v8, v12, v2\n"
    "s_lshr_b32 s28, 0x1837321d, s28\n"
    "v_mad_i32_i24 v3, v9, v12, v3\n"
    "s_and_b32 s28, s28, 0xff\n"
    "v_mad_i32_i24 v4, v10, v12, v4\n"
    "v_mad_i32_i24 v5, v11, v12, v5\n"
    "v_mov_b32 v18, s29\n"
    "s_add_u32 s29, s21, -1\n"
    "s_mul_i32 s29, s29, 384\n"
    "v_add_u32 v19, s29, v18\n"
    "v_min_u32 v1, 47, v1\n"
    "s_mul_i32 s29, s25, 50\n"
    "v_min_u32 v2, 47, v2\n"
    "s_add_u32 s28, s28, s29\n"
    "v_min_u32 v3, 47, v3\n"
    "s_cmp_lg_u32 s18, 0\n"
    "v_min_u32 v4, 47, v4\n"
    "s_cselect_b32 s28, 0, s28\n"
    "v_min_u32 v5, 47, v5\n"
    "v_cmp_lt_u32 s[42:43], 23, v1\n"
    "v_cmp_lt_u32 s[44:45], 23, v2\n"
    "v_cmp_lt_u32 s[46:47], 23, v3\n"
    "v_cmp_lt_u32 s[48:49], 23, v4\n"
    "v_cmp_lt_u32 s[50:51], 23, v5\n"
    "s_lshl_b32 s28, s28, 1\n"
    "s_sub_u32 s18, 222, s28\n"
    "s_add_u32 s28, s28, 1\n"
    "s_lshr_b32 s29, s2, 2\n"
    "v_cndmask_b32 v13, v18, v19, s[42:43]\n"
    "v_cndmask_b32 v14, v18, v19, s[44:45]\n"
    "v_cndmask_b32 v15, v18, v19, s[46:47]\n"
    "v_cndmask_b32 v16, v18, v19, s[48:49]\n"
    "v_cndmask_b32 v17, v18, v19, s[50:51]\n"
    "s_xor_b32 s31, s29, s31\n"
    "s_bitcmp1_b32 s2, 2\n"
    "s_cselect_b32 s18, s28, s18\n"
    "v_mad_i32_i24 v13, v7, s22, v13\n"
    "v_mad_i32_i24 v14, v8, s22, v14\n"
    "v_mad_i32_i24 v15, v9, s22, v15\n"
    "v_mad_i32_i24 v16, v10, s22, v16\n"
    "v_mad_i32_i24 v17, v11, s22, v17\n"
    "s_bitcmp1_b32 s31, 0\n"
    "s_cselect_b32 s19, -2, 2\n"
    "s_cmp_eq_u32 s26, 6\n"
    "s_cbranch_scc1 .Le_ce\n"
    ".Le_cd:\n"
    "v_lshl_add_u32 v1, v1, 4, v13\n"
    "v_lshl_add_u32 v2, v2, 4, v14\n"
    "v_lshl_add_u32 v3, v3, 4, v15\n"
    "v_lshl_add_u32 v4, v4, 4, v16\n"
    "v_lshl_add_u32 v5, v5, 4, v17\n"
    "s_cmp_ge_u32 s7, 2\n"
    "s_cbranch_scc1 .Le_b\n"
    "s_cmp_eq_u32 s26, 7\n"
    "s_cbranch_scc1 .Le_b\n"
    "s_mul_i32 s25, s19, 3\n"
    "s_add_u32 s25, s25, s18\n"
    "s_mul_i32 s25, s25, 86016\n"
    "s_mul_i32 s28, s19, 86016\n"
    "s_lshl_b32 s24, s23, 10\n"
    "s_addk_i32 s24, 27712\n"
    "s_mov_b32 s29, 7\n"
    "s_cmp_lt_u32 s3, 18\n"
    "s_cselect_b32 s31, 4, -1\n"
    "s_bitcmp1_b32 s2, 2\n"
    "s_cselect_b32 s31, -1, s31\n"
    "s_barrier\n"
    "s_waitcnt lgkmcnt(0)\n"
    ".Le_xl:\n"
    "s_cmp_eq_u32 s29, s31\n"
    "s_cbranch_scc1 .Le_h\n"
    ".Le_go:\n"
    "s_mov_b32 m0, s24\n"
    "s_add_u32 s26, s8, s25\n"
    "s_addc_u32 s27, s9, 0\n"
    "global_load_lds_dwordx4 v1, s[26:27]\n"
    "s_addk_i32 m0, 2048\n"
    "s_add_u32 s25, s25, s28\n"
    "global_load_lds_dwordx4 v2, s[26:27]\n"
    "s_addk_i32 m0, 2048\n"
    "s_add_u32 s24, s24, 8896\n"
    "global_load_lds_dwordx4 v3, s[26:27]\n"
    "s_addk_i32 m0, 2048\n"
    "s_cmp_lg_u32 s23, 0\n"
    "s_cbranch_scc1 .Le_x3\n"
    "global_load_lds_dwordx4 v4, s[26:27]\n"
    ".Le_x3:\n"
    "s_sub_u32 s29, s29, 1\n"
    "s_cmp_lg_u32 s29, 0\n"
    "s_cbranch_scc1 .Le_xl\n"
    "s_branch .Le_w\n"
    ".Le_h:\n"
    "v_mov_b32 v16, 161456\n"
    "ds_read_b32 v7, v16\n"
    "s_waitcnt lgkmcnt(0)\n"
    "v_cmp_gt_u32 vcc, 1, v7\n"
    "s_cbranch_vccnz .Le_h\n"
    "s_branch .Le_go\n"
    ".Le_a:\n"
    "s_cmp_lg_u32 s7, 4\n"
    "s_cbranch_scc1 .Le_nf\n"
    "s_cmp_lt_u32 s3, 18\n"
    "s_cselect_b32 s23, 0, 8\n"
    "v_lshrrev_b32 v10, 1, v6\n"
    "v_cmp_eq_u32 vcc, 1, v10\n"
    "v_lshrrev_b32 v7, 2, v6\n"
    "v_lshrrev_b32 v8, v7, s23\n"
    "v_mov_b32 v9, 0\n"
    "v_and_b32 v8, 1, v8\n"
    "v_cndmask_b32 v8, v8, 7, vcc\n"
    "v_cmp_eq_u32 vcc, 5, v10\n"
    "v_lshlrev_b32 v7, 2, v6\n"
    "v_add_u32 v7, 161408, v7\n"
    "v_cndmask_b32 v8, v8, 15, vcc\n"
    "ds_write_b32 v7, v9 offset:64\n"
    "ds_write_b32 v7, v9 offset:768\n"
    "ds_write_b32 v7, v8\n"
    "s_waitcnt lgkmcnt(0)\n"
    "s_branch .Le_b\n"
    ".Le_nf:\n"
    "s_cmp_lg_u32 s7, 5\n"
    "s_cbranch_scc1 .Le_b\n"
    "s_add_u32 s25, s25, 32768\n"
    "s_mov_b32 s23, .Lg_last-.Le_pc\n"
    "s_max_u32 s23, s23, .Lh_last-.Le_pc\n"
    "s_max_u32 s23, s23, .La_last-.Le_pc\n"
    "s_max_u32 s23, s23, .Ls_last-.Le_pc\n"
    "s_min_u32 s25, s25, s23\n"
    "v_lshl_add_u32 v7, v6, 4, s25\n"
    "global_load_lds_dwordx4 v7, s[28:29]\n"
    "s_branch .Le_b\n"
    ".Le_ce:\n"
    "s_movk_i32 s29, 384\n"
    "v_cndmask_b32 v20, 0, 1, s[42:43]\n"
    "v_lshl_add_u32 v21, v7, 1, v20\n"
    "v_cmp_lt_u32 vcc, 16, v21\n"
    "v_add_u32 v22, -17, v21\n"
    "v_min_u32 v22, 2, v22\n"
    "v_lshl_add_u32 v22, v22, 1, s30\n"
    "v_sub_u32 v22, v22, v20\n"
    "v_mul_i32_i24 v22, s29, v22\n"
    "v_cndmask_b32 v13, v13, v22, vcc\n"
    "v_cndmask_b32 v20, 0, 1, s[44:45]\n"
    "v_lshl_add_u32 v21, v8, 1, v20\n"
    "v_cmp_lt_u32 vcc, 16, v21\n"
    "v_add_u32 v22, -17, v21\n"
    "v_min_u32 v22, 2, v22\n"
    "v_lshl_add_u32 v22, v22, 1, s30\n"
    "v_sub_u32 v22, v22, v20\n"
    "v_mul_i32_i24 v22, s29, v22\n"
    "v_cndmask_b32 v14, v14, v22, vcc\n"
    "v_cndmask_b32 v20, 0, 1, s[46:47]\n"
    "v_lshl_add_u32 v21, v9, 1, v20\n"
    "v_cmp_lt_u32 vcc, 16, v21\n"
    "v_add_u32 v22, -17, v21\n"
    "v_min_u32 v22, 2, v22\n"
    "v_lshl_add_u32 v22, v22, 1, s30\n"
    "v_sub_u32 v22, v22, v20\n"
    "v_mul_i32_i24 v22, s29, v22\n"
    "v_cndmask_b32 v15, v15, v22, vcc\n"
    "v_cndmask_b32 v20, 0, 1, s[48:49]\n"
    "v_lshl_add_u32 v21, v10, 1, v20\n"
    "v_cmp_lt_u32 vcc, 16, v21\n"
    "v_add_u32 v22, -17, v21\n"
    "v_min_u32 v22, 2, v22\n"
    "v_lshl_add_u32 v22, v22, 1, s30\n"
    "v_sub_u32 v22, v22, v20\n"
    "v_mul_i32_i24 v22, s29, v22\n"
    "v_cndmask_b32 v16, v16, v22, vcc\n"
    "v_cndmask_b32 v20, 0, 1, s[50:51]\n"
    "v_lshl_add_u32 v21, v11, 1, v20\n"
    "v_cmp_lt_u32 vcc, 16, v21\n"
    "v_add_u32 v22, -17, v21\n"
    "v_min_u32 v22, 2, v22\n"
    "v_lshl_add_u32 v22, v22, 1, s30\n"
    "v_sub_u32 v22, v22, v20\n"
    "v_mul_i32_i24 v22, s29, v22\n"
    "v_cndmask_b32 v17, v17, v22, vcc\n"
    "s_branch .Le_cd\n"
    ".Le_p1:\n"
    "s_barrier\n"
    "v_bfe_u32 v8, v6, 2, 2\n"
    "v_sub_u32 v8, 0, v8\n"
    "v_xor_b32 v8, v8, v5\n"
    "v_and_b32 v8, 3, v8\n"
    "v_lshlrev_b32 v8, 4, v8\n"
    "v_and_b32 v10, 15, v6\n"
    "v_lshl_add_u32 v8, v10, 6, v8\n"
    "v_add_u32 v8, 124544, v8\n"
    "s_waitcnt vmcnt(9)\n"
    "ds_read_b128 v[20:23], v8 offset:0\n"
    "ds_read_b128 v[24:27], v8 offset:1024\n"
    "ds_read_b128 v[28:31], v8 offset:2048\n"
    "ds_read_b128 v[32:35], v8 offset:3072\n"
    "ds_read_b128 v[36:39], v8 offset:4096\n"
    "ds_read_b128 v[40:43], v8 offset:5120\n"
    "ds_read_b128 v[44:47], v8 offset:6144\n"
    "ds_read_b128 v[48:51], v8 offset:7168\n"
    "ds_read_b128 v[52:55], v8 offset:8192\n"
    "s_waitcnt vmcnt(0)\n"
    "ds_read_b128 v[56:59], v8 offset:9216\n"
    "ds_read_b128 v[60:63], v8 offset:10240\n"
    "ds_read_b128 v[64:67], v8 offset:11264\n"
    "ds_read_b128 v[68:71], v8 offset:12288\n"
    "ds_read_b128 v[72:75], v8 offset:13312\n"
    "ds_read_b128 v[76:79], v8 offset:14336\n"
    "ds_read_b128 v[80:83], v8 offset:15360\n"
    "ds_read_b128 v[84:87], v8 offset:16384\n"
    "ds_read_b128 v[88:91], v8 offset:17408\n"
    "s_waitcnt lgkmcnt(14)\n"
    "v_mfma_f32_16x16x32_bf16 v[92:95], v[20:23], v[28:31], 0\n"
    "v_mfma_f32_16x16x32_bf16 v[96:99], v[24:27], v[28:31], 0\n"
    "s_waitcnt lgkmcnt(12)\n"
    "v_mfma_f32_16x16x32_bf16 v[92:95], v[32:35], v[40:43], v[92:95]\n"
    "v_mfma_f32_16x16x32_bf16 v[96:99], v[36:39], v[40:43], v[96:99]\n"
    "s_waitcnt lgkmcnt(9)\n"
    "v_mfma_f32_16x16x32_bf16 v[92:95], v[44:47], v[52:55], v[92:95]\n"
    "v_mfma_f32_16x16x32_bf16 v[96:99], v[48:51], v[52:55], v[96:99]\n"
    "s_waitcnt lgkmcnt(6)\n"
    "v_mfma_f32_16x16x32_bf16 v[92:95], v[56:59], v[64:67], v[92:95]\n"
    "v_mfma_f32_16x16x32_bf16 v[96:99], v[60:63], v[64:67], v[96:99]\n"
    "s_waitcnt lgkmcnt(3)\n"
    "v_mfma_f32_16x16x32_bf16 v[92:95], v[68:71], v[76:79], v[92:95]\n"
    "v_mfma_f32_16x16x32_bf16 v[96:99], v[72:75], v[76:79], v[96:99]\n"
    "s_waitcnt lgkmcnt(0)\n"
    "v_mfma_f32_16x16x32_bf16 v[92:95], v[80:83], v[88:91], v[92:95]\n"
    "v_mfma_f32_16x16x32_bf16 v[96:99], v[84:87], v[88:91], v[96:99]\n"
    ".Le_pub:\n"
    "s_lshl_b32 s23, s3, 10\n"
    "s_add_u32 s23, s23, 8192\n"
    "v_lshl_add_u32 v9, v6, 4, s23\n"
    "s_lshl_b32 s23, s3, 7\n"
    "s_add_u32 s23, s23, 155648\n"
    "v_add_u32 v12, 0x7f4a7c15, v12\n"
    "v_add_u32 v13, 0x9e3779b9, v13\n"
    "v_and_b32 v11, 7, v6\n"
    "v_mov_b32 v17, 1\n"
    "v_cvt_pk_bf16_f32 v92, v92, v93\n"
    "v_cvt_pk_bf16_f32 v93, v94, v95\n"
    "v_cvt_pk_bf16_f32 v94, v96, v97\n"
    "v_cvt_pk_bf16_f32 v95, v98, v99\n"
    "v_add_u32 v92, v92, v12\n"
    "global_store_dwordx4 v9, v[92:95], s[16:17] sc1\n"
    "v_add_u32 v14, v92, v93\n"
    "v_add_u32 v15, v94, v95\n"
    "v_lshlrev_b32 v18, 2, v11\n"
    "v_and_b32 v18, 12, v18\n"
    "v_add_u32 v18, 161456, v18\n"
    "v_lshrrev_b32 v10, 3, v6\n"
    "ds_write_b32 v18, v17\n"
    "v_mul_u32_u24 v10, 18432, v10\n"
    "v_lshl_add_u32 v10, v11, 4, v10\n"
    "v_add_u32 v10, s23, v10\n"
    "v_permlane32_swap_b32 v14, v15\n"
    "v_add_u32 v14, v14, v15\n"
    "s_nop 1\n"
    "v_add_u32_dpp v14, v14, v14 row_ror:8 row_mask:0xf bank_mask:0xf\n"
    "s_nop 1\n"
    "v_add_u32_dpp v14, v14, v14 row_ror:4 row_mask:0xf bank_mask:0xf\n"
    "s_nop 1\n"
    "v_add_u32_dpp v14, v14, v14 row_ror:2 row_mask:0xf bank_mask:0xf\n"
    "s_nop 1\n"
    "v_add_u32_dpp v14, v14, v14 row_ror:1 row_mask:0xf bank_mask:0xf\n"
    "v_mov_b32 v15, v14\n"
    "s_nop 1\n"
    "v_permlane16_swap_b32 v14, v15\n"
    "v_add_u32 v14, v14, v15\n"
    "v_mov_b32 v15, v14\n"
    "s_nop 1\n"
    "v_permlane32_swap_b32 v14, v15\n"
    "global_store_dwordx4 v10, v[12:15], s[16:17] sc1\n"
    "s_branch .Le_w\n"
    ".Le_p2:\n"
    "v_bfe_u32 v8, v6, 2, 2\n"
    "v_and_b32 v7, 1, v5\n"
    "v_lshl_add_u32 v7, v7, 4, v8\n"
    "v_lshrrev_b32 v10, 5, v6\n"
    "v_lshl_add_u32 v7, v10, 3, v7\n"
    "v_mad_u32_u24 v7, v7, s23, v3\n"
    "v_and_b32 v11, 3, v6\n"
    "v_mul_u32_u24 v10, 24, v4\n"
    "v_add_lshl_u32 v10, v10, v11, 4\n"
    "s_lshl_b32 s31, s31, 6\n"
    "s_add_u32 s25, s25, 768\n"
    "s_add_u32 s3, s3, 72\n"
    "s_waitcnt lgkmcnt(0)\n"
    "s_add_u32 s26, s10, s25\n"
    "s_addc_u32 s27, s11, 0\n"
    "global_load_dwordx2 v[12:13], v9, s[16:17] sc1\n"
    "s_add_u32 s28, s26, 4608\n"
    "s_addc_u32 s29, s27, 0\n"
    "s_add_u32 s30, s12, s31\n"
    "s_addc_u32 s31, s13, 0\n"
    "s_mov_b32 s23, 6\n"
    ".Le_l2:\n"
    "global_load_lds_dwordx4 v7, s[26:27]\n"
    "s_addk_i32 m0, 1024\n"
    "s_sub_u32 s23, s23, 1\n"
    "global_load_lds_dwordx4 v7, s[28:29]\n"
    "s_addk_i32 m0, 1024\n"
    "v_add_u32 v7, 64, v7\n"
    "global_load_lds_dwordx4 v10, s[30:31]\n"
    "s_addk_i32 m0, 1024\n"
    "v_add_u32 v10, 6144, v10\n"
    "global_load_lds_dwordx4 v10, s[30:31]\n"
    "s_addk_i32 m0, 1024\n"
    "v_add_u32 v10, 6144, v10\n"
    "s_cmp_lg_u32 s23, 0\n"
    "s_cbranch_scc1 .Le_l2\n"
    "s_barrier\n"
    "v_lshl_add_u32 v7, v5, 3, v8\n"
    "v_lshlrev_b32 v7, 6, v7\n"
    "v_lshl_add_u32 v7, v11, 4, v7\n"
    "v_bfe_u32 v8, v6, 2, 2\n"
    "v_sub_u32 v8, 0, v8\n"
    "v_xor_b32 v8, v8, v5\n"
    "v_and_b32 v8, 3, v8\n"
    "v_lshlrev_b32 v8, 4, v8\n"
    "v_and_b32 v10, 15, v6\n"
    "v_lshl_add_u32 v8, v10, 6, v8\n"
    "v_add_u32 v8, 124544, v8\n"
    "s_lshl_b32 s24, s24, 3\n"
    "s_add_u32 s24, s24, 126592\n"
    "v_add_u32 v7, s24, v7\n"
    "s_waitcnt vmcnt(0)\n"
    "ds_read_b128 v[20:23], v8 offset:0\n"
    "ds_read_b128 v[24:27], v8 offset:1024\n"
    "ds_read_b64_tr_b16 v[28:29], v7 offset:0\n"
    "ds_read_b64_tr_b16 v[30:31], v7 offset:256\n"
    "ds_read_b128 v[32:35], v8 offset:4096\n"
    "ds_read_b128 v[36:39], v8 offset:5120\n"
    "ds_read_b64_tr_b16 v[40:41], v7 offset:4096\n"
    "ds_read_b64_tr_b16 v[42:43], v7 offset:4352\n"
    "ds_read_b128 v[44:47], v8 offset:8192\n"
    "ds_read_b128 v[48:51], v8 offset:9216\n"
    "ds_read_b64_tr_b16 v[52:53], v7 offset:8192\n"
    "ds_read_b64_tr_b16 v[54:55], v7 offset:8448\n"
    "ds_read_b128 v[56:59], v8 offset:12288\n"
    "ds_read_b128 v[60:63], v8 offset:13312\n"
    "ds_read_b64_tr_b16 v[64:65], v7 offset:12288\n"
    "ds_read_b64_tr_b16 v[66:67], v7 offset:12544\n"
    "ds_read_b128 v[68:71], v8 offset:16384\n"
    "ds_read_b128 v[72:75], v8 offset:17408\n"
    "ds_read_b64_tr_b16 v[76:77], v7 offset:16384\n"
    "ds_read_b64_tr_b16 v[78:79], v7 offset:16640\n"
    "ds_read_b128 v[80:83], v8 offset:20480\n"
    "ds_read_b128 v[84:87], v8 offset:21504\n"
    "ds_read_b64_tr_b16 v[88:89], v7 offset:20480\n"
    "ds_read_b64_tr_b16 v[90:91], v7 offset:20736\n"
    "s_waitcnt lgkmcnt(14)\n"
    "v_mfma_f32_16x16x32_bf16 v[92:95], v[20:23], v[28:31], 0\n"
    "v_mfma_f32_16x16x32_bf16 v[96:99], v[24:27], v[28:31], 0\n"
    "v_mfma_f32_16x16x32_bf16 v[92:95], v[32:35], v[40:43], v[92:95]\n"
    "v_mfma_f32_16x16x32_bf16 v[96:99], v[36:39], v[40:43], v[96:99]\n"
    "s_waitcnt lgkmcnt(12)\n"
    "v_mfma_f32_16x16x32_bf16 v[92:95], v[44:47], v[52:55], v[92:95]\n"
    "v_mfma_f32_16x16x32_bf16 v[96:99], v[48:51], v[52:55], v[96:99]\n"
    "s_waitcnt lgkmcnt(8)\n"
    "v_mfma_f32_16x16x32_bf16 v[92:95], v[56:59], v[64:67], v[92:95]\n"
    "v_mfma_f32_16x16x32_bf16 v[96:99], v[60:63], v[64:67], v[96:99]\n"
    "s_waitcnt lgkmcnt(4)\n"
    "v_mfma_f32_16x16x32_bf16 v[92:95], v[68:71], v[76:79], v[92:95]\n"
    "v_mfma_f32_16x16x32_bf16 v[96:99], v[72:75], v[76:79], v[96:99]\n"
    "s_waitcnt lgkmcnt(0)\n"
    "v_mfma_f32_16x16x32_bf16 v[92:95], v[80:83], v[88:91], v[92:95]\n"
    "v_mfma_f32_16x16x32_bf16 v[96:99], v[84:87], v[88:91], v[96:99]\n"
    "s_branch .Le_pub\n"
    ".Le_b:\n"
    "s_barrier\n"
    ".Le_w:\n"
    "s_waitcnt lgkmcnt(0)\n"
        :
          "={s[8:15]}"(ka), "={s[16:17]}"(ka4), "={s4}"(t0), "={s18}"(ax[0]), "={s19}"(ax[1]), "={s20}"(ax[2]), "={s21}"(ax[3]),
          "={v1}"(xo[0]), "={v2}"(xo[1]), "={v3}"(xo[2]), "={v4}"(xo[3]), "={v5}"(xo4), "+{v0}"(tid), "+{s2}"(bid)
        : "{s[0:1]}"(__builtin_amdgcn_kernarg_segment_ptr())
        : "memory", "scc", "vcc", "s3", "s5", "s7", "s22", "s42", "s43", "s44", "s45", "s46", "s47", "s48", "s49", "s50", "s51", "s23", "s24", "s25", "s26", "s27", "s28", "s29", "s30", "s31",
          "v6", "v7", "v8", "v9", "v10", "v11", "v12", "v13", "v14", "v15", "v16", "v17", "v18", "v19", "v20", "v21", "v22", "v23", "v24", "v25", "v26", "v27", "v28", "v29", "v30", "v31", "v32", "v33", "v34", "v35", "v36", "v37", "v38", "v39", "v40", "v41", "v42", "v43", "v44", "v45", "v46", "v47", "v48", "v49", "v50", "v51", "v52", "v53", "v54", "v55", "v56", "v57", "v58", "v59", "v60", "v61", "v62", "v63", "v64", "v65", "v66", "v67", "v68", "v69", "v70", "v71", "v72", "v73", "v74", "v75", "v76", "v77", "v78", "v79", "v80", "v81", "v82", "v83", "v84", "v85", "v86", "v87", "v88", "v89", "v90", "v91", "v92", "v93", "v94", "v95", "v96", "v97", "v98", "v99");
    auto mkptr = [](unsigned lo, unsigned hi) LAMBDA_INL { return (char*)(((u64)(unsigned)UNI(hi) << 32) | (unsigned)UNI(lo)); };
    const char* xb = mkptr(ka[0], ka[1]);
    char* ws = mkptr(ka4[0], ka4[1]);
    bid = UNI(bid);
    int lane = tid & 63;
    const int wave = __builtin_amdgcn_readfirstlane(tid >> 6);
    const int tq = bid >> 6, ts = (bid >> 3) & 7, strip = ts == 7 ? tq + 4 * (bid & 1) : ts;
    const int t12 = tq < 2 || ts == 7, hs = tq == 3 && ts != 7, redge = tq == 1 && ts != 7 && (bid & 1), cedge = strip == 6;
    const AxisMap rm = {UNI(ax[0]), UNI(ax[1]), bid & 4 ? 218 : 1, redge ? 11 : 99};
    const AxisMap cm = {UNI(ax[2]), UNI(ax[3]), bid & 2 ? 218 : 1, cedge ? 15 : 99};
    const unsigned vf = LF + 4 * (lane & 3);
    const u32x4 fl = {vf, lane < 2 ? LF + 4 * (wave & 1) + 8 * lane : OOB, lane < 1 ? LF + 4 * wave : OOB, 0};
    if (wave < 2 && strip != 7) {
        asm volatile("s_nop 7\n"
    "s_cmp_eq_u32 s50, 1\n"
    "s_cbranch_scc1 .Li_s1\n"
    "s_waitcnt vmcnt(20)\n"
    "v_mov_b32 v227, 1\n"
    "ds_write_b32 v226, v227 offset:0\n"
    "s_waitcnt vmcnt(12)\n"
    "v_mov_b32 v227, 2\n"
    "ds_write_b32 v226, v227 offset:0\n"
    "s_mul_i32 s56, s45, 2\n"
    "s_add_u32 s56, s56, s44\n"
    "s_add_u32 s60, s42, s56\n"
    "s_addc_u32 s61, s43, 0\n"
    "s_mov_b32 m0, 18816\n"
    "s_nop 0\n"
    "global_load_lds_dwordx4 v230, s[60:61]\n"
    "s_mov_b32 m0, 20864\n"
    "s_nop 0\n"
    "global_load_lds_dwordx4 v231, s[60:61]\n"
    "s_mov_b32 m0, 22912\n"
    "s_nop 0\n"
    "global_load_lds_dwordx4 v232, s[60:61]\n"
    "s_mov_b32 m0, 24960\n"
    "s_nop 0\n"
    "global_load_lds_dwordx4 v233, s[60:61]\n"
    "s_mov_b32 m0, 17792\n"
    "s_nop 0\n"
    "global_load_lds_dwordx4 v234, s[60:61]\n"
    "s_mul_i32 s56, s45, 1\n"
    "s_add_u32 s56, s56, s44\n"
    "s_add_u32 s60, s42, s56\n"
    "s_addc_u32 s61, s43, 0\n"
    "s_mov_b32 m0, 9920\n"
    "s_nop 0\n"
    "global_load_lds_dwordx4 v230, s[60:61]\n"
    "s_mov_b32 m0, 11968\n"
    "s_nop 0\n"
    "global_load_lds_dwordx4 v231, s[60:61]\n"
    "s_mov_b32 m0, 14016\n"
    "s_nop 0\n"
    "global_load_lds_dwordx4 v232, s[60:61]\n"
    "s_mov_b32 m0, 16064\n"
    "s_nop 0\n"
    "global_load_lds_dwordx4 v233, s[60:61]\n"
    "s_mov_b32 m0, 8896\n"
    "s_nop 0\n"
    "global_load_lds_dwordx4 v234, s[60:61]\n"
    "s_mul_i32 s56, s45, 0\n"
    "s_add_u32 s56, s56, s44\n"
    "s_add_u32 s60, s42, s56\n"
    "s_addc_u32 s61, s43, 0\n"
    "s_mov_b32 m0, 1024\n"
    "s_nop 0\n"
    "global_load_lds_dwordx4 v230, s[60:61]\n"
    "s_mov_b32 m0, 3072\n"
    "s_nop 0\n"
    "global_load_lds_dwordx4 v231, s[60:61]\n"
    "s_mov_b32 m0, 5120\n"
    "s_nop 0\n"
    "global_load_lds_dwordx4 v232, s[60:61]\n"
    "s_mov_b32 m0, 7168\n"
    "s_nop 0\n"
    "global_load_lds_dwordx4 v233, s[60:61]\n"
    "s_mov_b32 m0, 0\n"
    "s_nop 0\n"
    "global_load_lds_dwordx4 v234, s[60:61]\n"
    "s_mul_i32 s56, s45, 3\n"
    "s_add_u32 s56, s56, s44\n"
    "s_add_u32 s60, s42, s56\n"
    "s_addc_u32 s61, s43, 0\n"
    "s_mov_b32 m0, 26688\n"
    "s_nop 0\n"
    "global_load_lds_dwordx4 v234, s[60:61]\n"
    "s_mul_i32 s56, s45, 4\n"
    "s_add_u32 s56, s56, s44\n"
    "s_add_u32 s60, s42, s56\n"
    "s_addc_u32 s61, s43, 0\n"
    "s_mov_b32 m0, 35584\n"
    "s_nop 0\n"
    "global_load_lds_dwordx4 v234, s[60:61]\n"
    "s_mul_i32 s56, s45, 5\n"
    "s_add_u32 s56, s56, s44\n"
    "s_add_u32 s60, s42, s56\n"
    "s_addc_u32 s61, s43, 0\n"
    "s_mov_b32 m0, 44480\n"
    "s_nop 0\n"
    "global_load_lds_dwordx4 v234, s[60:61]\n"
    "s_mul_i32 s56, s45, 6\n"
    "s_add_u32 s56, s56, s44\n"
    "s_add_u32 s60, s42, s56\n"
    "s_addc_u32 s61, s43, 0\n"
    "s_mov_b32 m0, 53376\n"
    "s_nop 0\n"
    "global_load_lds_dwordx4 v234, s[60:61]\n"
    "s_mul_i32 s56, s45, 7\n"
    "s_add_u32 s56, s56, s44\n"
    "s_add_u32 s60, s42, s56\n"
    "s_addc_u32 s61, s43, 0\n"
    "s_mov_b32 m0, 62272\n"
    "s_nop 0\n"
    "global_load_lds_dwordx4 v234, s[60:61]\n"
    "s_mul_i32 s56, s45, 8\n"
    "s_add_u32 s56, s56, s44\n"
    "s_add_u32 s60, s42, s56\n"
    "s_addc_u32 s61, s43, 0\n"
    "s_mov_b32 m0, 71168\n"
    "s_nop 0\n"
    "global_load_lds_dwordx4 v234, s[60:61]\n"
    "s_mul_i32 s56, s45, 9\n"
    "s_add_u32 s56, s56, s44\n"
    "s_add_u32 s60, s42, s56\n"
    "s_addc_u32 s61, s43, 0\n"
    "s_mov_b32 m0, 80064\n"
    "s_nop 0\n"
    "global_load_lds_dwordx4 v234, s[60:61]\n"
    "s_waitcnt vmcnt(26)\n"
    "v_mov_b32 v227, 3\n"
    "ds_write_b32 v226, v227 offset:0\n"
    "s_waitcnt vmcnt(22)\n"
    "s_waitcnt vmcnt(2)\n"
    "v_mov_b32 v227, 4\n"
    "ds_write_b32 v226, v227 offset:0\n"
    "s_waitcnt vmcnt(0)\n"
    "v_mov_b32 v227, 5\n"
    "ds_write_b32 v226, v227 offset:0\n"
    "s_branch .Li_sd\n"
    ".Li_s1:\n"
    "s_waitcnt vmcnt(15)\n"
    "v_mov_b32 v227, 1\n"
    "ds_write_b32 v226, v227 offset:0\n"
    "s_waitcnt vmcnt(9)\n"
    "v_mov_b32 v227, 2\n"
    "ds_write_b32 v226, v227 offset:0\n"
    "s_mul_i32 s56, s45, 2\n"
    "s_add_u32 s56, s56, s44\n"
    "s_add_u32 s60, s42, s56\n"
    "s_addc_u32 s61, s43, 0\n"
    "s_mov_b32 m0, 19840\n"
    "s_nop 0\n"
    "global_load_lds_dwordx4 v230, s[60:61]\n"
    "s_mov_b32 m0, 21888\n"
    "s_nop 0\n"
    "global_load_lds_dwordx4 v231, s[60:61]\n"
    "s_mov_b32 m0, 23936\n"
    "s_nop 0\n"
    "global_load_lds_dwordx4 v232, s[60:61]\n"
    "s_mov_b32 m0, 25568\n"
    "s_nop 0\n"
    "global_load_lds_dwordx4 v234, s[60:61]\n"
    "s_mul_i32 s56, s45, 1\n"
    "s_add_u32 s56, s56, s44\n"
    "s_add_u32 s60, s42, s56\n"
    "s_addc_u32 s61, s43, 0\n"
    "s_mov_b32 m0, 10944\n"
    "s_nop 0\n"
    "global_load_lds_dwordx4 v230, s[60:61]\n"
    "s_mov_b32 m0, 12992\n"
    "s_nop 0\n"
    "global_load_lds_dwordx4 v231, s[60:61]\n"
    "s_mov_b32 m0, 15040\n"
    "s_nop 0\n"
    "global_load_lds_dwordx4 v232, s[60:61]\n"
    "s_mov_b32 m0, 16672\n"
    "s_nop 0\n"
    "global_load_lds_dwordx4 v234, s[60:61]\n"
    "s_mul_i32 s56, s45, 0\n"
    "s_add_u32 s56, s56, s44\n"
    "s_add_u32 s60, s42, s56\n"
    "s_addc_u32 s61, s43, 0\n"
    "s_mov_b32 m0, 2048\n"
    "s_nop 0\n"
    "global_load_lds_dwordx4 v230, s[60:61]\n"
    "s_mov_b32 m0, 4096\n"
    "s_nop 0\n"
    "global_load_lds_dwordx4 v231, s[60:61]\n"
    "s_mov_b32 m0, 6144\n"
    "s_nop 0\n"
    "global_load_lds_dwordx4 v232, s[60:61]\n"
    "s_mov_b32 m0, 7776\n"
    "s_nop 0\n"
    "global_load_lds_dwordx4 v234, s[60:61]\n"
    "s_mul_i32 s56, s45, 3\n"
    "s_add_u32 s56, s56, s44\n"
    "s_add_u32 s60, s42, s56\n"
    "s_addc_u32 s61, s43, 0\n"
    "s_mov_b32 m0, 34464\n"
    "s_nop 0\n"
    "global_load_lds_dwordx4 v234, s[60:61]\n"
    "s_mul_i32 s56, s45, 4\n"
    "s_add_u32 s56, s56, s44\n"
    "s_add_u32 s60, s42, s56\n"
    "s_addc_u32 s61, s43, 0\n"
    "s_mov_b32 m0, 43360\n"
    "s_nop 0\n"
    "global_load_lds_dwordx4 v234, s[60:61]\n"
    "s_mul_i32 s56, s45, 5\n"
    "s_add_u32 s56, s56, s44\n"
    "s_add_u32 s60, s42, s56\n"
    "s_addc_u32 s61, s43, 0\n"
    "s_mov_b32 m0, 52256\n"
    "s_nop 0\n"
    "global_load_lds_dwordx4 v234, s[60:61]\n"
    "s_mul_i32 s56, s45, 6\n"
    "s_add_u32 s56, s56, s44\n"
    "s_add_u32 s60, s42, s56\n"
    "s_addc_u32 s61, s43, 0\n"
    "s_mov_b32 m0, 61152\n"
    "s_nop 0\n"
    "global_load_lds_dwordx4 v234, s[60:61]\n"
    "s_mul_i32 s56, s45, 7\n"
    "s_add_u32 s56, s56, s44\n"
    "s_add_u32 s60, s42, s56\n"
    "s_addc_u32 s61, s43, 0\n"
    "s_mov_b32 m0, 70048\n"
    "s_nop 0\n"
    "global_load_lds_dwordx4 v234, s[60:61]\n"
    "s_mul_i32 s56, s45, 8\n"
    "s_add_u32 s56, s56, s44\n"
    "s_add_u32 s60, s42, s56\n"
    "s_addc_u32 s61, s43, 0\n"
    "s_mov_b32 m0, 78944\n"
    "s_nop 0\n"
    "global_load_lds_dwordx4 v234, s[60:61]\n"
    "s_mul_i32 s56, s45, 9\n"
    "s_add_u32 s56, s56, s44\n"
    "s_add_u32 s60, s42, s56\n"
    "s_addc_u32 s61, s43, 0\n"
    "s_mov_b32 m0, 87840\n"
    "s_nop 0\n"
    "global_load_lds_dwordx4 v234, s[60:61]\n"
    "s_waitcnt vmcnt(22)\n"
    "v_mov_b32 v227, 3\n"
    "ds_write_b32 v226, v227 offset:0\n"
    "s_waitcnt vmcnt(19)\n"
    "s_waitcnt vmcnt(2)\n"
    "v_mov_b32 v227, 4\n"
    "ds_write_b32 v226, v227 offset:0\n"
    "s_waitcnt vmcnt(0)\n"
    "v_mov_b32 v227, 5\n"
    "ds_write_b32 v226, v227 offset:0\n"
    ".Li_sd:\n"
    "s_setprio 3\n"
    "s_waitcnt lgkmcnt(0)\n"
            : "+v"(lane)
            : "{v[224:227]}"(fl), "{v[230:233]}"(xo), "{v234}"(xo4), "{s[42:43]}"(xb), "{s44}"(UNI(rm.o * IROWB)), "{s45}"(UNI(rm.d * IROWB)), "{s50}"(wave)
            : "memory", "s56", "s60", "s61", "s90", "s91", "s92", "s93", "s94", "s95", "scc");
    }
    const int i16 = lane & 15, g = lane >> 4;
    const int e = (i16 + 4) >> 3 & 1, qj = i16 - 4 * ((i16 + 4) >> 3);

    if (strip == 7) {
        asm volatile("s_nop 7\n"
    "s_cmp_gt_u32 s44, 2\n"
    "s_cbranch_scc1 .Ls_end\n"
    "v_mov_b32 v1, 0\n"
    "global_load_dwordx2 v[4:5], v1, s[40:41] sc1\n"
    "v_and_b32 v2, 7, v0\n"
    "v_lshlrev_b32 v2, 4, v2\n"
    "s_waitcnt vmcnt(0)\n"
    "v_add_u32 v6, 0x7f4a7c15, v4\n"
    "v_add_u32 v7, 0x9e3779b9, v5\n"
    "v_add_u32 v4, 0x7f4a7c15, v4\n"
    "v_add_u32 v5, 0x9e3779b9, v5\n"
    "s_cmp_eq_u32 s44, 0\n"
    "s_cbranch_scc1 .Ls_l\n"
    "v_add_u32 v2, s48, v2\n"
    "global_store_dwordx4 v2, v[4:7], s[40:41] sc1\n"
    "s_endpgm\n"
    ".Ls_l:\n"
    "s_cmp_eq_u32 s45, 0\n"
    "s_cbranch_scc1 .Ls_end\n"
    "v_lshlrev_b32 v3, 10, v0\n"
    "v_add_u32 v3, 393216, v3\n"
    "s_mov_b32 s52, 0x10000\n"
    "s_sleep 110\n"
    ".Ls_p:\n"
    "s_sleep 8\n"
    "global_load_dwordx2 v[8:9], v3, s[40:41] offset:0 sc1\n"
    "global_load_dwordx2 v[10:11], v3, s[40:41] offset:128 sc1\n"
    "global_load_dwordx2 v[12:13], v3, s[40:41] offset:256 sc1\n"
    "global_load_dwordx2 v[14:15], v3, s[40:41] offset:384 sc1\n"
    "global_load_dwordx2 v[16:17], v3, s[40:41] offset:512 sc1\n"
    "global_load_dwordx2 v[18:19], v3, s[40:41] offset:640 sc1\n"
    "global_load_dwordx2 v[20:21], v3, s[40:41] offset:768 sc1\n"
    "global_load_dwordx2 v[22:23], v3, s[40:41] offset:896 sc1\n"
    "s_mov_b64 s[46:47], 0\n"
    "s_waitcnt vmcnt(0)\n"
    "v_cmp_ne_u64 vcc, v[4:5], v[8:9]\n"
    "s_or_b64 s[46:47], s[46:47], vcc\n"
    "v_cmp_ne_u64 vcc, v[4:5], v[10:11]\n"
    "s_or_b64 s[46:47], s[46:47], vcc\n"
    "v_cmp_ne_u64 vcc, v[4:5], v[12:13]\n"
    "s_or_b64 s[46:47], s[46:47], vcc\n"
    "v_cmp_ne_u64 vcc, v[4:5], v[14:15]\n"
    "s_or_b64 s[46:47], s[46:47], vcc\n"
    "v_cmp_ne_u64 vcc, v[4:5], v[16:17]\n"
    "s_or_b64 s[46:47], s[46:47], vcc\n"
    "v_cmp_ne_u64 vcc, v[4:5], v[18:19]\n"
    "s_or_b64 s[46:47], s[46:47], vcc\n"
    "v_cmp_ne_u64 vcc, v[4:5], v[20:21]\n"
    "s_or_b64 s[46:47], s[46:47], vcc\n"
    "v_cmp_ne_u64 vcc, v[4:5], v[22:23]\n"
    "s_or_b64 s[46:47], s[46:47], vcc\n"
    "s_cbranch_scc0 .Ls_c\n"
    "s_sub_u32 s52, s52, 1\n"
    "s_cmp_lg_u32 s52, 0\n"
    "s_cbranch_scc1 .Ls_p\n"
    ".Ls_c:\n"
    "global_store_dwordx4 v2, v[4:7], s[40:41] sc1\n"
    ".Ls_end:\n"
    "s_endpgm\n"
    ".Ls_last:\n"
            :: "{v0}"(lane), "{s[40:41]}"(ws), "{s44}"(wave), "{s45}"(UNI(bid == 253)), "{s48}"(UNI(WS_ACK + (2 * bid + wave - 1) * 128))
            : "memory", "v23", "s46", "s47", "s52", "vcc", "scc");
    } else if (wave < 4) {
        const int h = wave & 1;
        const unsigned l16 = lane * 16;
        const int nf = min(lane, 35) + 36 * h;
        const unsigned z0 = LQ + (lane & 3) * 256 + (4 * ((2 * (g >> 1)) ^ (lane & 3)) + ((lane >> 2) & 3)) * 16 + (g & 1) * 8;
        const u32x2 vz = {z0, z0 ^ 64};
        const unsigned vi = LF + 320 + 4 * ((i16 + 8) & 15);
        const unsigned c0 = axis_at(cm, 3 + qj) * PIXB + 16 * g, c1 = axis_at(cm, 11 + qj) * PIXB + 16 * g;
        const u32x2 dd = {(unsigned)((axis_at(rm, 11 + e) - axis_at(rm, 9 + e)) * IROWB), (unsigned)((axis_at(rm, 13 + e) - axis_at(rm, 11 + e)) * IROWB)};
        const unsigned d56 = 2 * rm.d * IROWB + (hs ? c1 - c0 : 0);
        const u32x4 srd = {(unsigned)UNI(ka[6]), (unsigned)UNI(ka[7]) & 0xffffu, (unsigned)(IMW * IROWB), 0x00020000u};
        if (wave >= 2) {
            const unsigned b = pixad(3 + qj) + e * RP + 16 * g;
            const u32x2 va = {b, b + 3200};
            const u32x2 vb = {(unsigned)(e ? -9 * RP : 5 * RP), LQ + l16 + h * 3072};
            const u32x2 lf = {l16, (unsigned)(WS_FL + ((bid & (NREP - 1)) * 144 + nf) * 128)};
            constexpr int N0 = 8, UE = (N0 + 1) / 2, UO = N0 / 2;
            u32x4 to;
            if (h) {
                to = (u32x4){axis_at(rm, 3 + 2 * UE + e) * IROWB + c0, axis_at(rm, 3 + 2 * UO + e) * IROWB + c1, dd[0], dd[1]};
            } else {
                const AttC c = att_const(lane, hs, cedge);
                to = (u32x4){c.sb, c.v0, c.v1, c.pk};
            }
            asm volatile("s_nop 7\n"
    "s_mov_b32 s52, 0x40000\n"
    "s_load_dwordx2 s[78:79], s[40:41], 0x0\n"
    "s_mul_i32 s53, s50, 36864\n"
    "s_add_u32 s57, s53, 11264\n"
    "s_add_u32 s64, s40, s57\n"
    "s_addc_u32 s65, s41, 0\n"
    "s_add_u32 s57, s53, 17408\n"
    "s_add_u32 s66, s40, s57\n"
    "s_addc_u32 s67, s41, 0\n"
    "s_add_u32 s57, s53, 23552\n"
    "s_add_u32 s68, s40, s57\n"
    "s_addc_u32 s69, s41, 0\n"
    "s_add_u32 s57, s53, 29696\n"
    "s_add_u32 s70, s40, s57\n"
    "s_addc_u32 s71, s41, 0\n"
    "s_add_u32 s57, s53, 35840\n"
    "s_add_u32 s72, s40, s57\n"
    "s_addc_u32 s73, s41, 0\n"
    "s_add_u32 s57, s53, 41984\n"
    "s_add_u32 s74, s40, s57\n"
    "s_addc_u32 s75, s41, 0\n"
    "v_add_u32 v4, 71168, v2\n"
    "v_add_u32 v5, 71168, v3\n"
    "s_lshl_b32 s54, s50, 10\n"
    "s_add_u32 s54, s54, 1024\n"
    "s_mul_i32 s56, s50, 7776\n"
    "s_cmp_eq_u32 s50, 0\n"
    "s_cbranch_scc1 .Lg_x0\n"
    "v_mov_b32 v233, v234\n"
    ".Lg_x0:\n"
    "s_max_u32 s55, s56, 7168\n"
    ".Lg_dl:\n"
    "s_sleep 1\n"
    "s_memtime s[76:77]\n"
    "s_waitcnt lgkmcnt(0)\n"
    "s_sub_u32 s76, s76, s59\n"
    "s_cmp_lt_i32 s76, 0\n"
    "s_cbranch_scc1 .Lg_dl\n"
    "s_waitcnt lgkmcnt(0)\n"
    "s_add_u32 s78, s78, 0x7f4a7c15\n"
    "s_add_u32 s79, s79, 0x9e3779b9\n"
    "v_mov_b64 v[240:241], s[78:79]\n"
    "s_mov_b32 s51, 0x4000\n"
    ".Lg_retry:\n"
    "v_mov_b64 v[244:245], v[240:241]\n"
    "v_mov_b64 v[246:247], 0\n"
    "v_mov_b64 v[242:243], 0\n"
    "s_mov_b32 exec_hi, 15\n"
    "global_load_dwordx4 v[244:247], v236, s[40:41] sc1\n"
    "s_mov_b32 exec_hi, -1\n"
    "ds_read_b32 v228, v224 offset:48\n"
    "s_waitcnt lgkmcnt(0)\n"
    "v_cmp_gt_u32 vcc, 1, v228\n"
    "s_cbranch_vccnz .Lg1\n"
    ".Lg1b:\n"
    "global_load_dwordx4 v[8:11], v235, s[64:65] offset:-3072 sc1\n"
    "global_load_dwordx4 v[32:35], v235, s[66:67] offset:-3072 sc1\n"
    "global_load_dwordx4 v[12:15], v235, s[64:65] offset:-2048 sc1\n"
    "global_load_dwordx4 v[36:39], v235, s[66:67] offset:-2048 sc1\n"
    "global_load_dwordx4 v[16:19], v235, s[64:65] offset:-1024 sc1\n"
    "global_load_dwordx4 v[40:43], v235, s[66:67] offset:-1024 sc1\n"
    "global_load_dwordx4 v[20:23], v235, s[64:65] offset:0 sc1\n"
    "global_load_dwordx4 v[44:47], v235, s[66:67] offset:0 sc1\n"
    "global_load_dwordx4 v[24:27], v235, s[64:65] offset:1024 sc1\n"
    "global_load_dwordx4 v[48:51], v235, s[66:67] offset:1024 sc1\n"
    "global_load_dwordx4 v[28:31], v235, s[64:65] offset:2048 sc1\n"
    "global_load_dwordx4 v[52:55], v235, s[66:67] offset:2048 sc1\n"
    "global_load_dwordx4 v[56:59], v235, s[68:69] offset:-3072 sc1\n"
    "global_load_dwordx4 v[80:83], v235, s[70:71] offset:-3072 sc1\n"
    "global_load_dwordx4 v[60:63], v235, s[68:69] offset:-2048 sc1\n"
    "global_load_dwordx4 v[84:87], v235, s[70:71] offset:-2048 sc1\n"
    "global_load_dwordx4 v[64:67], v235, s[68:69] offset:-1024 sc1\n"
    "global_load_dwordx4 v[88:91], v235, s[70:71] offset:-1024 sc1\n"
    "global_load_dwordx4 v[68:71], v235, s[68:69] offset:0 sc1\n"
    "global_load_dwordx4 v[92:95], v235, s[70:71] offset:0 sc1\n"
    "ds_read_b32 v228, v224 offset:0\n"
    "s_waitcnt lgkmcnt(0)\n"
    "v_cmp_gt_u32 vcc, 1, v228\n"
    "s_cbranch_vccnz .Lg2\n"
    ".Lg2b:\n"
    "ds_read_b128 v[152:155], v2 offset:26688\n"
    "ds_read_b128 v[156:159], v2 offset:26752\n"
    "ds_read_b128 v[160:163], v2 offset:26816\n"
    "ds_read_b128 v[164:167], v2 offset:26880\n"
    "ds_read_b128 v[168:171], v2 offset:26944\n"
    "ds_read_b128 v[172:175], v2 offset:27008\n"
    "s_waitcnt vmcnt(18)\n"
    "v_add3_u32 v242, v242, v8, v9\n"
    "v_sub_u32 v8, v8, v240\n"
    "v_add3_u32 v243, v243, v10, v11\n"
    "v_add3_u32 v242, v242, v32, v33\n"
    "v_sub_u32 v32, v32, v240\n"
    "v_add3_u32 v243, v243, v34, v35\n"
    "s_waitcnt vmcnt(16)\n"
    "v_add3_u32 v242, v242, v12, v13\n"
    "v_sub_u32 v12, v12, v240\n"
    "v_add3_u32 v243, v243, v14, v15\n"
    "s_waitcnt lgkmcnt(5)\n"
    "v_mfma_f32_16x16x32_bf16 v[200:203], v[8:11], v[152:155], 0\n"
    "global_load_dwordx4 v[72:75], v235, s[68:69] offset:1024 sc1\n"
    "v_add3_u32 v242, v242, v36, v37\n"
    "v_sub_u32 v36, v36, v240\n"
    "v_add3_u32 v243, v243, v38, v39\n"
    "v_mfma_f32_16x16x32_bf16 v[204:207], v[32:35], v[152:155], 0\n"
    "global_load_dwordx4 v[96:99], v235, s[70:71] offset:1024 sc1\n"
    "s_waitcnt vmcnt(16)\n"
    "v_add3_u32 v242, v242, v16, v17\n"
    "v_sub_u32 v16, v16, v240\n"
    "v_add3_u32 v243, v243, v18, v19\n"
    "s_waitcnt lgkmcnt(4)\n"
    "v_mfma_f32_16x16x32_bf16 v[200:203], v[12:15], v[156:159], v[200:203]\n"
    "global_load_dwordx4 v[76:79], v235, s[68:69] offset:2048 sc1\n"
    "v_add3_u32 v242, v242, v40, v41\n"
    "v_sub_u32 v40, v40, v240\n"
    "v_add3_u32 v243, v243, v42, v43\n"
    "v_mfma_f32_16x16x32_bf16 v[204:207], v[36:39], v[156:159], v[204:207]\n"
    "global_load_dwordx4 v[100:103], v235, s[70:71] offset:2048 sc1\n"
    "s_waitcnt vmcnt(16)\n"
    "v_add3_u32 v242, v242, v20, v21\n"
    "v_sub_u32 v20, v20, v240\n"
    "v_add3_u32 v243, v243, v22, v23\n"
    "s_waitcnt lgkmcnt(3)\n"
    "v_mfma_f32_16x16x32_bf16 v[200:203], v[16:19], v[160:163], v[200:203]\n"
    "global_load_dwordx4 v[104:107], v235, s[72:73] offset:-3072 sc1\n"
    "v_add3_u32 v242, v242, v44, v45\n"
    "v_sub_u32 v44, v44, v240\n"
    "v_add3_u32 v243, v243, v46, v47\n"
    "v_mfma_f32_16x16x32_bf16 v[204:207], v[40:43], v[160:163], v[204:207]\n"
    "global_load_dwordx4 v[128:131], v235, s[74:75] offset:-3072 sc1\n"
    "s_waitcnt vmcnt(16)\n"
    "v_add3_u32 v242, v242, v24, v25\n"
    "v_sub_u32 v24, v24, v240\n"
    "v_add3_u32 v243, v243, v26, v27\n"
    "s_waitcnt lgkmcnt(2)\n"
    "v_mfma_f32_16x16x32_bf16 v[200:203], v[20:23], v[164:167], v[200:203]\n"
    "global_load_dwordx4 v[108:111], v235, s[72:73] offset:-2048 sc1\n"
    "v_add3_u32 v242, v242, v48, v49\n"
    "v_sub_u32 v48, v48, v240\n"
    "v_add3_u32 v243, v243, v50, v51\n"
    "v_mfma_f32_16x16x32_bf16 v[204:207], v[44:47], v[164:167], v[204:207]\n"
    "global_load_dwordx4 v[132:135], v235, s[74:75] offset:-2048 sc1\n"
    "s_waitcnt vmcnt(16)\n"
    "v_add3_u32 v242, v242, v28, v29\n"
    "v_sub_u32 v28, v28, v240\n"
    "v_add3_u32 v243, v243, v30, v31\n"
    "s_waitcnt lgkmcnt(1)\n"
    "v_mfma_f32_16x16x32_bf16 v[200:203], v[24:27], v[168:171], v[200:203]\n"
    "global_load_dwordx4 v[112:115], v235, s[72:73] offset:-1024 sc1\n"
    "v_add3_u32 v242, v242, v52, v53\n"
    "v_sub_u32 v52, v52, v240\n"
    "v_add3_u32 v243, v243, v54, v55\n"
    "v_mfma_f32_16x16x32_bf16 v[204:207], v[48:51], v[168:171], v[204:207]\n"
    "global_load_dwordx4 v[136:139], v235, s[74:75] offset:-1024 sc1\n"
    "s_waitcnt lgkmcnt(0)\n"
    "v_mfma_f32_16x16x32_bf16 v[200:203], v[28:31], v[172:175], v[200:203]\n"
    "global_load_dwordx4 v[116:119], v235, s[72:73] offset:0 sc1\n"
    "v_mfma_f32_16x16x32_bf16 v[204:207], v[52:55], v[172:175], v[204:207]\n"
    "global_load_dwordx4 v[140:143], v235, s[74:75] offset:0 sc1\n"
    "s_waitcnt vmcnt(18)\n"
    "v_add3_u32 v242, v242, v56, v57\n"
    "v_sub_u32 v56, v56, v240\n"
    "v_add3_u32 v243, v243, v58, v59\n"
    "v_add3_u32 v242, v242, v80, v81\n"
    "v_sub_u32 v80, v80, v240\n"
    "v_add3_u32 v243, v243, v82, v83\n"
    "s_waitcnt vmcnt(16)\n"
    "v_add3_u32 v242, v242, v60, v61\n"
    "v_sub_u32 v60, v60, v240\n"
    "v_add3_u32 v243, v243, v62, v63\n"
    "v_mfma_f32_16x16x32_bf16 v[208:211], v[56:59], v[152:155], 0\n"
    "global_load_dwordx4 v[120:123], v235, s[72:73] offset:1024 sc1\n"
    "v_add3_u32 v242, v242, v84, v85\n"
    "v_sub_u32 v84, v84, v240\n"
    "v_add3_u32 v243, v243, v86, v87\n"
    "v_mfma_f32_16x16x32_bf16 v[212:215], v[80:83], v[152:155], 0\n"
    "global_load_dwordx4 v[144:147], v235, s[74:75] offset:1024 sc1\n"
    "s_waitcnt vmcnt(16)\n"
    "v_add3_u32 v242, v242, v64, v65\n"
    "v_sub_u32 v64, v64, v240\n"
    "v_add3_u32 v243, v243, v66, v67\n"
    "v_mfma_f32_16x16x32_bf16 v[208:211], v[60:63], v[156:159], v[208:211]\n"
    "global_load_dwordx4 v[124:127], v235, s[72:73] offset:2048 sc1\n"
    "v_add3_u32 v242, v242, v88, v89\n"
    "v_sub_u32 v88, v88, v240\n"
    "v_add3_u32 v243, v243, v90, v91\n"
    "v_mfma_f32_16x16x32_bf16 v[212:215], v[84:87], v[156:159], v[212:215]\n"
    "global_load_dwordx4 v[148:151], v235, s[74:75] offset:2048 sc1\n"
    "s_waitcnt vmcnt(16)\n"
    "v_add3_u32 v242, v242, v68, v69\n"
    "v_sub_u32 v68, v68, v240\n"
    "v_add3_u32 v243, v243, v70, v71\n"
    "v_mfma_f32_16x16x32_bf16 v[208:211], v[64:67], v[160:163], v[208:211]\n"
    "v_mov_b32 v227, 1\n"
    "v_add3_u32 v242, v242, v92, v93\n"
    "v_sub_u32 v92, v92, v240\n"
    "v_add3_u32 v243, v243, v94, v95\n"
    "v_mfma_f32_16x16x32_bf16 v[212:215], v[88:91], v[160:163], v[212:215]\n"
    "ds_write_b32 v225, v227 offset:256\n"
    "s_waitcnt vmcnt(14)\n"
    "v_add3_u32 v242, v242, v72, v73\n"
    "v_sub_u32 v72, v72, v240\n"
    "v_add3_u32 v243, v243, v74, v75\n"
    "v_mfma_f32_16x16x32_bf16 v[208:211], v[68:71], v[164:167], v[208:211]\n"
    "v_cvt_pk_bf16_f32 v200, v200, v201\n"
    "v_add3_u32 v242, v242, v96, v97\n"
    "v_sub_u32 v96, v96, v240\n"
    "v_add3_u32 v243, v243, v98, v99\n"
    "v_mfma_f32_16x16x32_bf16 v[212:215], v[92:95], v[164:167], v[212:215]\n"
    "v_cvt_pk_bf16_f32 v201, v202, v203\n"
    "s_waitcnt vmcnt(12)\n"
    "v_add3_u32 v242, v242, v76, v77\n"
    "v_sub_u32 v76, v76, v240\n"
    "v_add3_u32 v243, v243, v78, v79\n"
    "v_mfma_f32_16x16x32_bf16 v[208:211], v[72:75], v[168:171], v[208:211]\n"
    "v_cvt_pk_bf16_f32 v202, v204, v205\n"
    "v_add3_u32 v242, v242, v100, v101\n"
    "v_sub_u32 v100, v100, v240\n"
    "v_add3_u32 v243, v243, v102, v103\n"
    "v_mfma_f32_16x16x32_bf16 v[212:215], v[96:99], v[168:171], v[212:215]\n"
    "v_cvt_pk_bf16_f32 v203, v206, v207\n"
    "v_mfma_f32_16x16x32_bf16 v[208:211], v[76:79], v[172:175], v[208:211]\n"
    "ds_write_b128 v7, v[200:203] offset:0\n"
    "v_mfma_f32_16x16x32_bf16 v[212:215], v[100:103], v[172:175], v[212:215]\n"
    "s_waitcnt vmcnt(10)\n"
    "v_add3_u32 v242, v242, v104, v105\n"
    "v_sub_u32 v104, v104, v240\n"
    "v_add3_u32 v243, v243, v106, v107\n"
    "v_add3_u32 v242, v242, v128, v129\n"
    "v_sub_u32 v128, v128, v240\n"
    "v_add3_u32 v243, v243, v130, v131\n"
    "s_waitcnt vmcnt(8)\n"
    "v_add3_u32 v242, v242, v108, v109\n"
    "v_sub_u32 v108, v108, v240\n"
    "v_add3_u32 v243, v243, v110, v111\n"
    "v_mfma_f32_16x16x32_bf16 v[216:219], v[104:107], v[152:155], 0\n"
    "ds_read_b128 v[176:179], v3 offset:26688\n"
    "v_add3_u32 v242, v242, v132, v133\n"
    "v_sub_u32 v132, v132, v240\n"
    "v_add3_u32 v243, v243, v134, v135\n"
    "v_mfma_f32_16x16x32_bf16 v[220:223], v[128:131], v[152:155], 0\n"
    "ds_read_b128 v[180:183], v3 offset:26752\n"
    "s_waitcnt vmcnt(6)\n"
    "v_add3_u32 v242, v242, v112, v113\n"
    "v_sub_u32 v112, v112, v240\n"
    "v_add3_u32 v243, v243, v114, v115\n"
    "v_mfma_f32_16x16x32_bf16 v[216:219], v[108:111], v[156:159], v[216:219]\n"
    "ds_read_b128 v[184:187], v3 offset:26816\n"
    "v_add3_u32 v242, v242, v136, v137\n"
    "v_sub_u32 v136, v136, v240\n"
    "v_add3_u32 v243, v243, v138, v139\n"
    "v_mfma_f32_16x16x32_bf16 v[220:223], v[132:135], v[156:159], v[220:223]\n"
    "ds_read_b128 v[188:191], v3 offset:26880\n"
    "s_waitcnt vmcnt(4)\n"
    "v_add3_u32 v242, v242, v116, v117\n"
    "v_sub_u32 v116, v116, v240\n"
    "v_add3_u32 v243, v243, v118, v119\n"
    "v_mfma_f32_16x16x32_bf16 v[216:219], v[112:115], v[160:163], v[216:219]\n"
    "ds_read_b128 v[192:195], v3 offset:26944\n"
    "v_add3_u32 v242, v242, v140, v141\n"
    "v_sub_u32 v140, v140, v240\n"
    "v_add3_u32 v243, v243, v142, v143\n"
    "v_mfma_f32_16x16x32_bf16 v[220:223], v[136:139], v[160:163], v[220:223]\n"
    "ds_read_b128 v[196:199], v3 offset:27008\n"
    "s_waitcnt vmcnt(2)\n"
    "v_add3_u32 v242, v242, v120, v121\n"
    "v_sub_u32 v120, v120, v240\n"
    "v_add3_u32 v243, v243, v122, v123\n"
    "v_mfma_f32_16x16x32_bf16 v[216:219], v[116:119], v[164:167], v[216:219]\n"
    "v_cvt_pk_bf16_f32 v208, v208, v209\n"
    "v_add3_u32 v242, v242, v144, v145\n"
    "v_sub_u32 v144, v144, v240\n"
    "v_add3_u32 v243, v243, v146, v147\n"
    "v_mfma_f32_16x16x32_bf16 v[220:223], v[140:143], v[164:167], v[220:223]\n"
    "v_cvt_pk_bf16_f32 v209, v210, v211\n"
    "s_waitcnt vmcnt(0)\n"
    "v_add3_u32 v242, v242, v124, v125\n"
    "v_sub_u32 v124, v124, v240\n"
    "v_add3_u32 v243, v243, v126, v127\n"
    "v_mfma_f32_16x16x32_bf16 v[216:219], v[120:123], v[168:171], v[216:219]\n"
    "v_cvt_pk_bf16_f32 v210, v212, v213\n"
    "v_add3_u32 v242, v242, v148, v149\n"
    "v_sub_u32 v148, v148, v240\n"
    "v_add3_u32 v243, v243, v150, v151\n"
    "v_mfma_f32_16x16x32_bf16 v[220:223], v[144:147], v[168:171], v[220:223]\n"
    "v_cvt_pk_bf16_f32 v211, v214, v215\n"
    "v_mfma_f32_16x16x32_bf16 v[216:219], v[124:127], v[172:175], v[216:219]\n"
    "ds_write_b128 v7, v[208:211] offset:1024\n"
    "v_mfma_f32_16x16x32_bf16 v[220:223], v[148:151], v[172:175], v[220:223]\n"
    "v_cmp_ne_u64 s[76:77], v[240:241], v[244:245]\n"
    "v_sub_u32 v242, v242, v246\n"
    "v_sub_u32 v243, v243, v247\n"
    "s_nop 1\n"
    "v_permlane32_swap_b32 v242, v243\n"
    "v_add_u32 v242, v242, v243\n"
    "v_cvt_pk_bf16_f32 v216, v216, v217\n"
    "v_cvt_pk_bf16_f32 v217, v218, v219\n"
    "v_add_u32_dpp v242, v242, v242 row_ror:8 row_mask:0xf bank_mask:0xf\n"
    "v_cvt_pk_bf16_f32 v218, v220, v221\n"
    "v_cvt_pk_bf16_f32 v219, v222, v223\n"
    "v_add_u32_dpp v242, v242, v242 row_ror:4 row_mask:0xf bank_mask:0xf\n"
    "ds_write_b128 v7, v[216:219] offset:2048\n"
    "s_nop 0\n"
    "v_add_u32_dpp v242, v242, v242 row_ror:2 row_mask:0xf bank_mask:0xf\n"
    "s_nop 1\n"
    "v_add_u32_dpp v242, v242, v242 row_ror:1 row_mask:0xf bank_mask:0xf\n"
    "v_mov_b32 v243, v242\n"
    "s_nop 1\n"
    "v_permlane16_swap_b32 v242, v243\n"
    "v_add_u32 v242, v242, v243\n"
    "v_cmp_ne_u32 vcc, 0, v242\n"
    "v_mov_b32 v227, 1\n"
    "s_or_b64 vcc, vcc, s[76:77]\n"
    "s_cbranch_scc1 .Lg_fail\n"
    ".Lg_ok:\n"
    "ds_write_b32 v225, v227 offset:16\n"
    "s_waitcnt lgkmcnt(3)\n"
    "v_mfma_f32_16x16x32_bf16 v[200:203], v[8:11], v[176:179], 0\n"
    "v_mfma_f32_16x16x32_bf16 v[204:207], v[32:35], v[176:179], 0\n"
    "v_mfma_f32_16x16x32_bf16 v[200:203], v[12:15], v[180:183], v[200:203]\n"
    "v_mfma_f32_16x16x32_bf16 v[204:207], v[36:39], v[180:183], v[204:207]\n"
    "v_mfma_f32_16x16x32_bf16 v[200:203], v[16:19], v[184:187], v[200:203]\n"
    "v_mfma_f32_16x16x32_bf16 v[204:207], v[40:43], v[184:187], v[204:207]\n"
    "v_mfma_f32_16x16x32_bf16 v[200:203], v[20:23], v[188:191], v[200:203]\n"
    "v_mfma_f32_16x16x32_bf16 v[204:207], v[44:47], v[188:191], v[204:207]\n"
    "v_mfma_f32_16x16x32_bf16 v[200:203], v[24:27], v[192:195], v[200:203]\n"
    "v_mfma_f32_16x16x32_bf16 v[204:207], v[48:51], v[192:195], v[204:207]\n"
    "v_mfma_f32_16x16x32_bf16 v[200:203], v[28:31], v[196:199], v[200:203]\n"
    "v_mfma_f32_16x16x32_bf16 v[204:207], v[52:55], v[196:199], v[204:207]\n"
    "v_mfma_f32_16x16x32_bf16 v[208:211], v[56:59], v[176:179], 0\n"
    "v_mfma_f32_16x16x32_bf16 v[212:215], v[80:83], v[176:179], 0\n"
    "ds_read_b32 v229, v224 offset:0\n"
    "v_mfma_f32_16x16x32_bf16 v[208:211], v[60:63], v[180:183], v[208:211]\n"
    "v_mfma_f32_16x16x32_bf16 v[212:215], v[84:87], v[180:183], v[212:215]\n"
    "v_mfma_f32_16x16x32_bf16 v[208:211], v[64:67], v[184:187], v[208:211]\n"
    "v_mfma_f32_16x16x32_bf16 v[212:215], v[88:91], v[184:187], v[212:215]\n"
    "v_cvt_pk_bf16_f32 v200, v200, v201\n"
    "v_cvt_pk_bf16_f32 v201, v202, v203\n"
    "v_mfma_f32_16x16x32_bf16 v[208:211], v[68:71], v[188:191], v[208:211]\n"
    "v_cvt_pk_bf16_f32 v202, v204, v205\n"
    "v_cvt_pk_bf16_f32 v203, v206, v207\n"
    "v_mfma_f32_16x16x32_bf16 v[212:215], v[92:95], v[188:191], v[212:215]\n"
    "ds_write_b128 v7, v[200:203] offset:6144\n"
    "v_mfma_f32_16x16x32_bf16 v[208:211], v[72:75], v[192:195], v[208:211]\n"
    "v_mfma_f32_16x16x32_bf16 v[212:215], v[96:99], v[192:195], v[212:215]\n"
    "s_waitcnt lgkmcnt(1)\n"
    "v_cmp_gt_u32 vcc, 2, v229\n"
    "v_mfma_f32_16x16x32_bf16 v[208:211], v[76:79], v[196:199], v[208:211]\n"
    "v_mfma_f32_16x16x32_bf16 v[212:215], v[100:103], v[196:199], v[212:215]\n"
    "s_cbranch_vccnz .Lg3\n"
    ".Lg3b:\n"
    "v_mfma_f32_16x16x32_bf16 v[216:219], v[104:107], v[176:179], 0\n"
    "ds_read_b128 v[152:155], v2 offset:44480\n"
    "ds_read_b128 v[156:159], v2 offset:44544\n"
    "v_mfma_f32_16x16x32_bf16 v[220:223], v[128:131], v[176:179], 0\n"
    "ds_read_b128 v[160:163], v2 offset:44608\n"
    "ds_read_b128 v[164:167], v2 offset:44672\n"
    "v_mfma_f32_16x16x32_bf16 v[216:219], v[108:111], v[180:183], v[216:219]\n"
    "ds_read_b128 v[168:171], v2 offset:44736\n"
    "ds_read_b128 v[172:175], v2 offset:44800\n"
    "v_mfma_f32_16x16x32_bf16 v[220:223], v[132:135], v[180:183], v[220:223]\n"
    "v_cvt_pk_bf16_f32 v208, v208, v209\n"
    "v_cvt_pk_bf16_f32 v209, v210, v211\n"
    "v_mfma_f32_16x16x32_bf16 v[216:219], v[112:115], v[184:187], v[216:219]\n"
    "v_cvt_pk_bf16_f32 v210, v212, v213\n"
    "v_cvt_pk_bf16_f32 v211, v214, v215\n"
    "v_mfma_f32_16x16x32_bf16 v[220:223], v[136:139], v[184:187], v[220:223]\n"
    "ds_write_b128 v7, v[208:211] offset:7168\n"
    "v_mfma_f32_16x16x32_bf16 v[216:219], v[116:119], v[188:191], v[216:219]\n"
    "v_mfma_f32_16x16x32_bf16 v[220:223], v[140:143], v[188:191], v[220:223]\n"
    "v_mfma_f32_16x16x32_bf16 v[216:219], v[120:123], v[192:195], v[216:219]\n"
    "v_mfma_f32_16x16x32_bf16 v[220:223], v[144:147], v[192:195], v[220:223]\n"
    "v_mfma_f32_16x16x32_bf16 v[216:219], v[124:127], v[196:199], v[216:219]\n"
    "v_mfma_f32_16x16x32_bf16 v[220:223], v[148:151], v[196:199], v[220:223]\n"
    "s_waitcnt lgkmcnt(1)\n"
    "v_mfma_f32_16x16x32_bf16 v[200:203], v[8:11], v[152:155], 0\n"
    "v_mov_b32 v227, 2\n"
    "v_mfma_f32_16x16x32_bf16 v[204:207], v[32:35], v[152:155], 0\n"
    "v_mfma_f32_16x16x32_bf16 v[200:203], v[12:15], v[156:159], v[200:203]\n"
    "v_mfma_f32_16x16x32_bf16 v[204:207], v[36:39], v[156:159], v[204:207]\n"
    "v_mfma_f32_16x16x32_bf16 v[200:203], v[16:19], v[160:163], v[200:203]\n"
    "v_cvt_pk_bf16_f32 v216, v216, v217\n"
    "v_cvt_pk_bf16_f32 v217, v218, v219\n"
    "v_mfma_f32_16x16x32_bf16 v[204:207], v[40:43], v[160:163], v[204:207]\n"
    "v_cvt_pk_bf16_f32 v218, v220, v221\n"
    "v_cvt_pk_bf16_f32 v219, v222, v223\n"
    "v_mfma_f32_16x16x32_bf16 v[200:203], v[20:23], v[164:167], v[200:203]\n"
    "ds_write_b128 v7, v[216:219] offset:8192\n"
    "v_mfma_f32_16x16x32_bf16 v[204:207], v[44:47], v[164:167], v[204:207]\n"
    "ds_write_b32 v225, v227 offset:16\n"
    "v_mfma_f32_16x16x32_bf16 v[200:203], v[24:27], v[168:171], v[200:203]\n"
    "v_mfma_f32_16x16x32_bf16 v[204:207], v[48:51], v[168:171], v[204:207]\n"
    "v_mfma_f32_16x16x32_bf16 v[200:203], v[28:31], v[172:175], v[200:203]\n"
    "v_mfma_f32_16x16x32_bf16 v[204:207], v[52:55], v[172:175], v[204:207]\n"
    "v_mfma_f32_16x16x32_bf16 v[208:211], v[56:59], v[152:155], 0\n"
    "v_mfma_f32_16x16x32_bf16 v[212:215], v[80:83], v[152:155], 0\n"
    "v_mfma_f32_16x16x32_bf16 v[208:211], v[60:63], v[156:159], v[208:211]\n"
    "v_mfma_f32_16x16x32_bf16 v[212:215], v[84:87], v[156:159], v[212:215]\n"
    "v_mfma_f32_16x16x32_bf16 v[208:211], v[64:67], v[160:163], v[208:211]\n"
    "v_mfma_f32_16x16x32_bf16 v[212:215], v[88:91], v[160:163], v[212:215]\n"
    "v_mfma_f32_16x16x32_bf16 v[208:211], v[68:71], v[164:167], v[208:211]\n"
    "v_cvt_pk_bf16_f32 v200, v200, v201\n"
    "v_cvt_pk_bf16_f32 v201, v202, v203\n"
    "v_mfma_f32_16x16x32_bf16 v[212:215], v[92:95], v[164:167], v[212:215]\n"
    "v_cvt_pk_bf16_f32 v202, v204, v205\n"
    "v_cvt_pk_bf16_f32 v203, v206, v207\n"
    "v_mfma_f32_16x16x32_bf16 v[208:211], v[72:75], v[168:171], v[208:211]\n"
    "ds_write_b128 v7, v[200:203] offset:12288\n"
    "v_mfma_f32_16x16x32_bf16 v[212:215], v[96:99], v[168:171], v[212:215]\n"
    "v_mfma_f32_16x16x32_bf16 v[208:211], v[76:79], v[172:175], v[208:211]\n"
    "v_mfma_f32_16x16x32_bf16 v[212:215], v[100:103], v[172:175], v[212:215]\n"
    "v_mfma_f32_16x16x32_bf16 v[216:219], v[104:107], v[152:155], 0\n"
    "ds_read_b128 v[176:179], v3 offset:44480\n"
    "ds_read_b128 v[180:183], v3 offset:44544\n"
    "v_mfma_f32_16x16x32_bf16 v[220:223], v[128:131], v[152:155], 0\n"
    "ds_read_b128 v[184:187], v3 offset:44608\n"
    "ds_read_b128 v[188:191], v3 offset:44672\n"
    "v_mfma_f32_16x16x32_bf16 v[216:219], v[108:111], v[156:159], v[216:219]\n"
    "ds_read_b128 v[192:195], v3 offset:44736\n"
    "ds_read_b128 v[196:199], v3 offset:44800\n"
    "v_mfma_f32_16x16x32_bf16 v[220:223], v[132:135], v[156:159], v[220:223]\n"
    "v_cvt_pk_bf16_f32 v208, v208, v209\n"
    "v_cvt_pk_bf16_f32 v209, v210, v211\n"
    "v_mfma_f32_16x16x32_bf16 v[216:219], v[112:115], v[160:163], v[216:219]\n"
    "v_cvt_pk_bf16_f32 v210, v212, v213\n"
    "v_cvt_pk_bf16_f32 v211, v214, v215\n"
    "v_mfma_f32_16x16x32_bf16 v[220:223], v[136:139], v[160:163], v[220:223]\n"
    "ds_write_b128 v7, v[208:211] offset:13312\n"
    "v_mfma_f32_16x16x32_bf16 v[216:219], v[116:119], v[164:167], v[216:219]\n"
    "v_mfma_f32_16x16x32_bf16 v[220:223], v[140:143], v[164:167], v[220:223]\n"
    "v_mfma_f32_16x16x32_bf16 v[216:219], v[120:123], v[168:171], v[216:219]\n"
    "v_mfma_f32_16x16x32_bf16 v[220:223], v[144:147], v[168:171], v[220:223]\n"
    "v_mfma_f32_16x16x32_bf16 v[216:219], v[124:127], v[172:175], v[216:219]\n"
    "v_mfma_f32_16x16x32_bf16 v[220:223], v[148:151], v[172:175], v[220:223]\n"
    "s_waitcnt lgkmcnt(1)\n"
    "v_mfma_f32_16x16x32_bf16 v[200:203], v[8:11], v[176:179], 0\n"
    "v_mov_b32 v227, 3\n"
    "v_mfma_f32_16x16x32_bf16 v[204:207], v[32:35], v[176:179], 0\n"
    "v_mfma_f32_16x16x32_bf16 v[200:203], v[12:15], v[180:183], v[200:203]\n"
    "v_mfma_f32_16x16x32_bf16 v[204:207], v[36:39], v[180:183], v[204:207]\n"
    "v_mfma_f32_16x16x32_bf16 v[200:203], v[16:19], v[184:187], v[200:203]\n"
    "v_cvt_pk_bf16_f32 v216, v216, v217\n"
    "v_cvt_pk_bf16_f32 v217, v218, v219\n"
    "v_mfma_f32_16x16x32_bf16 v[204:207], v[40:43], v[184:187], v[204:207]\n"
    "v_cvt_pk_bf16_f32 v218, v220, v221\n"
    "v_cvt_pk_bf16_f32 v219, v222, v223\n"
    "v_mfma_f32_16x16x32_bf16 v[200:203], v[20:23], v[188:191], v[200:203]\n"
    "ds_write_b128 v7, v[216:219] offset:14336\n"
    "v_mfma_f32_16x16x32_bf16 v[204:207], v[44:47], v[188:191], v[204:207]\n"
    "ds_write_b32 v225, v227 offset:16\n"
    "ds_read_b32 v228, v224 offset:192\n"
    "v_mfma_f32_16x16x32_bf16 v[200:203], v[24:27], v[192:195], v[200:203]\n"
    "v_mfma_f32_16x16x32_bf16 v[204:207], v[48:51], v[192:195], v[204:207]\n"
    "v_mfma_f32_16x16x32_bf16 v[200:203], v[28:31], v[196:199], v[200:203]\n"
    "v_mfma_f32_16x16x32_bf16 v[204:207], v[52:55], v[196:199], v[204:207]\n"
    "v_mfma_f32_16x16x32_bf16 v[208:211], v[56:59], v[176:179], 0\n"
    "v_mfma_f32_16x16x32_bf16 v[212:215], v[80:83], v[176:179], 0\n"
    "ds_read_b32 v229, v224 offset:0\n"
    "v_mfma_f32_16x16x32_bf16 v[208:211], v[60:63], v[180:183], v[208:211]\n"
    "v_mfma_f32_16x16x32_bf16 v[212:215], v[84:87], v[180:183], v[212:215]\n"
    "s_waitcnt lgkmcnt(1)\n"
    "v_cmp_gt_u32 vcc, 1, v228\n"
    "v_mfma_f32_16x16x32_bf16 v[208:211], v[64:67], v[184:187], v[208:211]\n"
    "v_cvt_pk_bf16_f32 v200, v200, v201\n"
    "s_cbranch_vccnz .Lg4\n"
    ".Lg4b:\n"
    "v_mfma_f32_16x16x32_bf16 v[212:215], v[88:91], v[184:187], v[212:215]\n"
    "v_cvt_pk_bf16_f32 v201, v202, v203\n"
    "v_cvt_pk_bf16_f32 v202, v204, v205\n"
    "v_mfma_f32_16x16x32_bf16 v[208:211], v[68:71], v[188:191], v[208:211]\n"
    "v_cvt_pk_bf16_f32 v203, v206, v207\n"
    "v_mfma_f32_16x16x32_bf16 v[212:215], v[92:95], v[188:191], v[212:215]\n"
    "ds_write_b128 v7, v[200:203] offset:0\n"
    "v_mfma_f32_16x16x32_bf16 v[208:211], v[72:75], v[192:195], v[208:211]\n"
    "v_mfma_f32_16x16x32_bf16 v[212:215], v[96:99], v[192:195], v[212:215]\n"
    "s_waitcnt lgkmcnt(1)\n"
    "v_cmp_gt_u32 vcc, 3, v229\n"
    "v_mfma_f32_16x16x32_bf16 v[208:211], v[76:79], v[196:199], v[208:211]\n"
    "v_mfma_f32_16x16x32_bf16 v[212:215], v[100:103], v[196:199], v[212:215]\n"
    "s_cbranch_vccnz .Lg5\n"
    ".Lg5b:\n"
    "v_mfma_f32_16x16x32_bf16 v[216:219], v[104:107], v[176:179], 0\n"
    "ds_read_b128 v[152:155], v2 offset:62272\n"
    "ds_read_b128 v[156:159], v2 offset:62336\n"
    "v_mfma_f32_16x16x32_bf16 v[220:223], v[128:131], v[176:179], 0\n"
    "ds_read_b128 v[160:163], v2 offset:62400\n"
    "ds_read_b128 v[164:167], v2 offset:62464\n"
    "v_mfma_f32_16x16x32_bf16 v[216:219], v[108:111], v[180:183], v[216:219]\n"
    "ds_read_b128 v[168:171], v2 offset:62528\n"
    "ds_read_b128 v[172:175], v2 offset:62592\n"
    "v_mfma_f32_16x16x32_bf16 v[220:223], v[132:135], v[180:183], v[220:223]\n"
    "v_cvt_pk_bf16_f32 v208, v208, v209\n"
    "v_cvt_pk_bf16_f32 v209, v210, v211\n"
    "v_mfma_f32_16x16x32_bf16 v[216:219], v[112:115], v[184:187], v[216:219]\n"
    "v_cvt_pk_bf16_f32 v210, v212, v213\n"
    "v_cvt_pk_bf16_f32 v211, v214, v215\n"
    "v_mfma_f32_16x16x32_bf16 v[220:223], v[136:139], v[184:187], v[220:223]\n"
    "ds_write_b128 v7, v[208:211] offset:1024\n"
    "v_mfma_f32_16x16x32_bf16 v[216:219], v[116:119], v[188:191], v[216:219]\n"
    "v_mfma_f32_16x16x32_bf16 v[220:223], v[140:143], v[188:191], v[220:223]\n"
    "v_mfma_f32_16x16x32_bf16 v[216:219], v[120:123], v[192:195], v[216:219]\n"
    "v_mfma_f32_16x16x32_bf16 v[220:223], v[144:147], v[192:195], v[220:223]\n"
    "v_mfma_f32_16x16x32_bf16 v[216:219], v[124:127], v[196:199], v[216:219]\n"
    "v_mfma_f32_16x16x32_bf16 v[220:223], v[148:151], v[196:199], v[220:223]\n"
    "s_waitcnt lgkmcnt(1)\n"
    "v_mfma_f32_16x16x32_bf16 v[200:203], v[8:11], v[152:155], 0\n"
    "v_mov_b32 v227, 4\n"
    "v_mfma_f32_16x16x32_bf16 v[204:207], v[32:35], v[152:155], 0\n"
    "v_mfma_f32_16x16x32_bf16 v[200:203], v[12:15], v[156:159], v[200:203]\n"
    "v_mfma_f32_16x16x32_bf16 v[204:207], v[36:39], v[156:159], v[204:207]\n"
    "v_mfma_f32_16x16x32_bf16 v[200:203], v[16:19], v[160:163], v[200:203]\n"
    "v_cvt_pk_bf16_f32 v216, v216, v217\n"
    "v_cvt_pk_bf16_f32 v217, v218, v219\n"
    "v_mfma_f32_16x16x32_bf16 v[204:207], v[40:43], v[160:163], v[204:207]\n"
    "v_cvt_pk_bf16_f32 v218, v220, v221\n"
    "v_cvt_pk_bf16_f32 v219, v222, v223\n"
    "v_mfma_f32_16x16x32_bf16 v[200:203], v[20:23], v[164:167], v[200:203]\n"
    "ds_write_b128 v7, v[216:219] offset:2048\n"
    "v_mfma_f32_16x16x32_bf16 v[204:207], v[44:47], v[164:167], v[204:207]\n"
    "ds_write_b32 v225, v227 offset:16\n"
    "ds_read_b32 v228, v224 offset:208\n"
    "v_mfma_f32_16x16x32_bf16 v[200:203], v[24:27], v[168:171], v[200:203]\n"
    "ds_read_b32 v239, v224 offset:64\n"
    "v_mfma_f32_16x16x32_bf16 v[204:207], v[48:51], v[168:171], v[204:207]\n"
    "v_mfma_f32_16x16x32_bf16 v[200:203], v[28:31], v[172:175], v[200:203]\n"
    "v_mfma_f32_16x16x32_bf16 v[204:207], v[52:55], v[172:175], v[204:207]\n"
    "v_mfma_f32_16x16x32_bf16 v[208:211], v[56:59], v[152:155], 0\n"
    "v_mfma_f32_16x16x32_bf16 v[212:215], v[80:83], v[152:155], 0\n"
    "v_mfma_f32_16x16x32_bf16 v[208:211], v[60:63], v[156:159], v[208:211]\n"
    "v_mfma_f32_16x16x32_bf16 v[212:215], v[84:87], v[156:159], v[212:215]\n"
    "s_waitcnt lgkmcnt(1)\n"
    "v_cmp_gt_u32 vcc, 1, v228\n"
    "v_mfma_f32_16x16x32_bf16 v[208:211], v[64:67], v[160:163], v[208:211]\n"
    "v_cvt_pk_bf16_f32 v200, v200, v201\n"
    "s_cbranch_vccnz .Lg6\n"
    ".Lg6b:\n"
    "v_mfma_f32_16x16x32_bf16 v[212:215], v[88:91], v[160:163], v[212:215]\n"
    "v_cvt_pk_bf16_f32 v201, v202, v203\n"
    "v_cvt_pk_bf16_f32 v202, v204, v205\n"
    "v_mfma_f32_16x16x32_bf16 v[208:211], v[68:71], v[164:167], v[208:211]\n"
    "v_cvt_pk_bf16_f32 v203, v206, v207\n"
    "v_mfma_f32_16x16x32_bf16 v[212:215], v[92:95], v[164:167], v[212:215]\n"
    "ds_write_b128 v7, v[200:203] offset:6144\n"
    "v_mfma_f32_16x16x32_bf16 v[208:211], v[72:75], v[168:171], v[208:211]\n"
    "v_mfma_f32_16x16x32_bf16 v[212:215], v[96:99], v[168:171], v[212:215]\n"
    "v_mfma_f32_16x16x32_bf16 v[208:211], v[76:79], v[172:175], v[208:211]\n"
    "v_mfma_f32_16x16x32_bf16 v[212:215], v[100:103], v[172:175], v[212:215]\n"
    "v_mfma_f32_16x16x32_bf16 v[216:219], v[104:107], v[152:155], 0\n"
    "ds_read_b128 v[176:179], v3 offset:62272\n"
    "ds_read_b128 v[180:183], v3 offset:62336\n"
    "v_mfma_f32_16x16x32_bf16 v[220:223], v[128:131], v[152:155], 0\n"
    "ds_read_b128 v[184:187], v3 offset:62400\n"
    "ds_read_b128 v[188:191], v3 offset:62464\n"
    "v_mfma_f32_16x16x32_bf16 v[216:219], v[108:111], v[156:159], v[216:219]\n"
    "ds_read_b128 v[192:195], v3 offset:62528\n"
    "ds_read_b128 v[196:199], v3 offset:62592\n"
    "v_mfma_f32_16x16x32_bf16 v[220:223], v[132:135], v[156:159], v[220:223]\n"
    "v_cvt_pk_bf16_f32 v208, v208, v209\n"
    "v_cvt_pk_bf16_f32 v209, v210, v211\n"
    "v_mfma_f32_16x16x32_bf16 v[216:219], v[112:115], v[160:163], v[216:219]\n"
    "v_cvt_pk_bf16_f32 v210, v212, v213\n"
    "v_cvt_pk_bf16_f32 v211, v214, v215\n"
    "v_mfma_f32_16x16x32_bf16 v[220:223], v[136:139], v[160:163], v[220:223]\n"
    "ds_write_b128 v7, v[208:211] offset:7168\n"
    "v_mfma_f32_16x16x32_bf16 v[216:219], v[116:119], v[164:167], v[216:219]\n"
    "s_waitcnt lgkmcnt(8)\n"
    "v_cmp_gt_u32 vcc, 1, v239\n"
    "v_mfma_f32_16x16x32_bf16 v[220:223], v[140:143], v[164:167], v[220:223]\n"
    "v_mfma_f32_16x16x32_bf16 v[216:219], v[120:123], v[168:171], v[216:219]\n"
    "s_cbranch_vccnz .Lg7\n"
    ".Lg7b:\n"
    "v_mfma_f32_16x16x32_bf16 v[220:223], v[144:147], v[168:171], v[220:223]\n"
    "v_mfma_f32_16x16x32_bf16 v[216:219], v[124:127], v[172:175], v[216:219]\n"
    "v_mfma_f32_16x16x32_bf16 v[220:223], v[148:151], v[172:175], v[220:223]\n"
    "s_waitcnt lgkmcnt(1)\n"
    "v_mfma_f32_16x16x32_bf16 v[200:203], v[8:11], v[176:179], 0\n"
    "v_mov_b32 v227, 5\n"
    "v_mfma_f32_16x16x32_bf16 v[204:207], v[32:35], v[176:179], 0\n"
    "v_mfma_f32_16x16x32_bf16 v[200:203], v[12:15], v[180:183], v[200:203]\n"
    "v_mfma_f32_16x16x32_bf16 v[204:207], v[36:39], v[180:183], v[204:207]\n"
    "v_mfma_f32_16x16x32_bf16 v[200:203], v[16:19], v[184:187], v[200:203]\n"
    "v_cvt_pk_bf16_f32 v216, v216, v217\n"
    "v_cvt_pk_bf16_f32 v217, v218, v219\n"
    "v_mfma_f32_16x16x32_bf16 v[204:207], v[40:43], v[184:187], v[204:207]\n"
    "v_cvt_pk_bf16_f32 v218, v220, v221\n"
    "v_cvt_pk_bf16_f32 v219, v222, v223\n"
    "v_mfma_f32_16x16x32_bf16 v[200:203], v[20:23], v[188:191], v[200:203]\n"
    "ds_write_b128 v7, v[216:219] offset:8192\n"
    "v_mfma_f32_16x16x32_bf16 v[204:207], v[44:47], v[188:191], v[204:207]\n"
    "ds_write_b32 v225, v227 offset:16\n"
    "ds_read_b32 v228, v224 offset:224\n"
    "v_mfma_f32_16x16x32_bf16 v[200:203], v[24:27], v[192:195], v[200:203]\n"
    "ds_read_b32 v239, v224 offset:80\n"
    "v_mfma_f32_16x16x32_bf16 v[204:207], v[48:51], v[192:195], v[204:207]\n"
    "v_mfma_f32_16x16x32_bf16 v[200:203], v[28:31], v[196:199], v[200:203]\n"
    "v_mfma_f32_16x16x32_bf16 v[204:207], v[52:55], v[196:199], v[204:207]\n"
    "v_mfma_f32_16x16x32_bf16 v[208:211], v[56:59], v[176:179], 0\n"
    "v_mfma_f32_16x16x32_bf16 v[212:215], v[80:83], v[176:179], 0\n"
    "ds_read_b32 v229, v224 offset:0\n"
    "v_mfma_f32_16x16x32_bf16 v[208:211], v[60:63], v[180:183], v[208:211]\n"
    "v_mfma_f32_16x16x32_bf16 v[212:215], v[84:87], v[180:183], v[212:215]\n"
    "s_waitcnt lgkmcnt(2)\n"
    "v_cmp_gt_u32 vcc, 1, v228\n"
    "v_mfma_f32_16x16x32_bf16 v[208:211], v[64:67], v[184:187], v[208:211]\n"
    "v_cvt_pk_bf16_f32 v200, v200, v201\n"
    "s_cbranch_vccnz .Lg8\n"
    ".Lg8b:\n"
    "v_mfma_f32_16x16x32_bf16 v[212:215], v[88:91], v[184:187], v[212:215]\n"
    "v_cvt_pk_bf16_f32 v201, v202, v203\n"
    "v_cvt_pk_bf16_f32 v202, v204, v205\n"
    "v_mfma_f32_16x16x32_bf16 v[208:211], v[68:71], v[188:191], v[208:211]\n"
    "v_cvt_pk_bf16_f32 v203, v206, v207\n"
    "v_mfma_f32_16x16x32_bf16 v[212:215], v[92:95], v[188:191], v[212:215]\n"
    "ds_write_b128 v7, v[200:203] offset:12288\n"
    "v_mfma_f32_16x16x32_bf16 v[208:211], v[72:75], v[192:195], v[208:211]\n"
    "v_mfma_f32_16x16x32_bf16 v[212:215], v[96:99], v[192:195], v[212:215]\n"
    "s_waitcnt lgkmcnt(1)\n"
    "v_cmp_gt_u32 vcc, 6, v229\n"
    "v_mfma_f32_16x16x32_bf16 v[208:211], v[76:79], v[196:199], v[208:211]\n"
    "v_mfma_f32_16x16x32_bf16 v[212:215], v[100:103], v[196:199], v[212:215]\n"
    "s_cbranch_vccnz .Lg9\n"
    ".Lg9b:\n"
    "v_mfma_f32_16x16x32_bf16 v[216:219], v[104:107], v[176:179], 0\n"
    "ds_read_b128 v[152:155], v4 offset:8896\n"
    "ds_read_b128 v[156:159], v4 offset:8960\n"
    "v_mfma_f32_16x16x32_bf16 v[220:223], v[128:131], v[176:179], 0\n"
    "ds_read_b128 v[160:163], v4 offset:9024\n"
    "ds_read_b128 v[164:167], v4 offset:9088\n"
    "v_mfma_f32_16x16x32_bf16 v[216:219], v[108:111], v[180:183], v[216:219]\n"
    "ds_read_b128 v[168:171], v4 offset:9152\n"
    "ds_read_b128 v[172:175], v4 offset:9216\n"
    "v_mfma_f32_16x16x32_bf16 v[220:223], v[132:135], v[180:183], v[220:223]\n"
    "v_cvt_pk_bf16_f32 v208, v208, v209\n"
    "v_cvt_pk_bf16_f32 v209, v210, v211\n"
    "v_mfma_f32_16x16x32_bf16 v[216:219], v[112:115], v[184:187], v[216:219]\n"
    "v_cvt_pk_bf16_f32 v210, v212, v213\n"
    "v_cvt_pk_bf16_f32 v211, v214, v215\n"
    "v_mfma_f32_16x16x32_bf16 v[220:223], v[136:139], v[184:187], v[220:223]\n"
    "ds_write_b128 v7, v[208:211] offset:13312\n"
    "v_mfma_f32_16x16x32_bf16 v[216:219], v[116:119], v[188:191], v[216:219]\n"
    "v_cmp_gt_u32 vcc, 1, v239\n"
    "v_mfma_f32_16x16x32_bf16 v[220:223], v[140:143], v[188:191], v[220:223]\n"
    "s_cbranch_vccnz .Lg10\n"
    ".Lg10b:\n"
    "v_mfma_f32_16x16x32_bf16 v[216:219], v[120:123], v[192:195], v[216:219]\n"
    "s_add_u32 s60, s42, s44\n"
    "s_addc_u32 s61, s43, 0\n"
    "v_mfma_f32_16x16x32_bf16 v[220:223], v[144:147], v[192:195], v[220:223]\n"
    "s_add_u32 m0, s54, 0\n"
    "v_mfma_f32_16x16x32_bf16 v[216:219], v[124:127], v[196:199], v[216:219]\n"
    "global_load_lds_dwordx4 v230, s[60:61]\n"
    "s_add_u32 m0, s54, 2048\n"
    "v_mfma_f32_16x16x32_bf16 v[220:223], v[148:151], v[196:199], v[220:223]\n"
    "global_load_lds_dwordx4 v231, s[60:61]\n"
    "s_add_u32 m0, s54, 4096\n"
    "s_waitcnt lgkmcnt(1)\n"
    "v_mfma_f32_16x16x32_bf16 v[200:203], v[8:11], v[152:155], 0\n"
    "v_mov_b32 v227, 6\n"
    "v_cvt_pk_bf16_f32 v216, v216, v217\n"
    "v_mfma_f32_16x16x32_bf16 v[204:207], v[32:35], v[152:155], 0\n"
    "v_cvt_pk_bf16_f32 v217, v218, v219\n"
    "v_cvt_pk_bf16_f32 v218, v220, v221\n"
    "v_mfma_f32_16x16x32_bf16 v[200:203], v[12:15], v[156:159], v[200:203]\n"
    "v_cvt_pk_bf16_f32 v219, v222, v223\n"
    "v_mfma_f32_16x16x32_bf16 v[204:207], v[36:39], v[156:159], v[204:207]\n"
    "ds_write_b128 v7, v[216:219] offset:14336\n"
    "v_mfma_f32_16x16x32_bf16 v[200:203], v[16:19], v[160:163], v[200:203]\n"
    "ds_write_b32 v225, v227 offset:16\n"
    "ds_read_b32 v228, v224 offset:240\n"
    "v_mfma_f32_16x16x32_bf16 v[204:207], v[40:43], v[160:163], v[204:207]\n"
    "ds_read_b32 v239, v224 offset:96\n"
    "global_load_lds_dwordx4 v232, s[60:61]\n"
    "v_mfma_f32_16x16x32_bf16 v[200:203], v[20:23], v[164:167], v[200:203]\n"
    "s_add_u32 m0, s55, 0\n"
    "v_mfma_f32_16x16x32_bf16 v[204:207], v[44:47], v[164:167], v[204:207]\n"
    "global_load_lds_dwordx4 v233, s[60:61]\n"
    "s_add_u32 m0, s56, 0\n"
    "v_mfma_f32_16x16x32_bf16 v[200:203], v[24:27], v[168:171], v[200:203]\n"
    "global_load_lds_dwordx4 v234, s[60:61]\n"
    "v_mfma_f32_16x16x32_bf16 v[204:207], v[48:51], v[168:171], v[204:207]\n"
    "s_add_u32 s60, s42, s45\n"
    "s_addc_u32 s61, s43, 0\n"
    "v_mfma_f32_16x16x32_bf16 v[200:203], v[28:31], v[172:175], v[200:203]\n"
    "s_add_u32 m0, s54, 8896\n"
    "v_mfma_f32_16x16x32_bf16 v[204:207], v[52:55], v[172:175], v[204:207]\n"
    "global_load_lds_dwordx4 v230, s[60:61]\n"
    "s_add_u32 m0, s54, 10944\n"
    "v_mfma_f32_16x16x32_bf16 v[208:211], v[56:59], v[152:155], 0\n"
    "s_waitcnt lgkmcnt(1)\n"
    "v_cmp_gt_u32 vcc, 1, v228\n"
    "v_mfma_f32_16x16x32_bf16 v[212:215], v[80:83], v[152:155], 0\n"
    "v_cvt_pk_bf16_f32 v200, v200, v201\n"
    "s_cbranch_vccnz .Lg11\n"
    ".Lg11b:\n"
    "v_mfma_f32_16x16x32_bf16 v[208:211], v[60:63], v[156:159], v[208:211]\n"
    "v_cvt_pk_bf16_f32 v201, v202, v203\n"
    "v_cvt_pk_bf16_f32 v202, v204, v205\n"
    "v_mfma_f32_16x16x32_bf16 v[212:215], v[84:87], v[156:159], v[212:215]\n"
    "v_cvt_pk_bf16_f32 v203, v206, v207\n"
    "v_mfma_f32_16x16x32_bf16 v[208:211], v[64:67], v[160:163], v[208:211]\n"
    "ds_write_b128 v7, v[200:203] offset:0\n"
    "v_mfma_f32_16x16x32_bf16 v[212:215], v[88:91], v[160:163], v[212:215]\n"
    "global_load_lds_dwordx4 v231, s[60:61]\n"
    "s_add_u32 m0, s54, 12992\n"
    "v_mfma_f32_16x16x32_bf16 v[208:211], v[68:71], v[164:167], v[208:211]\n"
    "global_load_lds_dwordx4 v232, s[60:61]\n"
    "s_add_u32 m0, s55, 8896\n"
    "v_mfma_f32_16x16x32_bf16 v[212:215], v[92:95], v[164:167], v[212:215]\n"
    "global_load_lds_dwordx4 v233, s[60:61]\n"
    "s_add_u32 m0, s56, 8896\n"
    "v_mfma_f32_16x16x32_bf16 v[208:211], v[72:75], v[168:171], v[208:211]\n"
    "global_load_lds_dwordx4 v234, s[60:61]\n"
    "v_mfma_f32_16x16x32_bf16 v[212:215], v[96:99], v[168:171], v[212:215]\n"
    "v_mfma_f32_16x16x32_bf16 v[208:211], v[76:79], v[172:175], v[208:211]\n"
    "v_mfma_f32_16x16x32_bf16 v[212:215], v[100:103], v[172:175], v[212:215]\n"
    "v_mfma_f32_16x16x32_bf16 v[216:219], v[104:107], v[152:155], 0\n"
    "ds_read_b128 v[176:179], v5 offset:8896\n"
    "ds_read_b128 v[180:183], v5 offset:8960\n"
    "v_mfma_f32_16x16x32_bf16 v[220:223], v[128:131], v[152:155], 0\n"
    "ds_read_b128 v[184:187], v5 offset:9024\n"
    "ds_read_b128 v[188:191], v5 offset:9088\n"
    "v_mfma_f32_16x16x32_bf16 v[216:219], v[108:111], v[156:159], v[216:219]\n"
    "ds_read_b128 v[192:195], v5 offset:9152\n"
    "ds_read_b128 v[196:199], v5 offset:9216\n"
    "v_mfma_f32_16x16x32_bf16 v[220:223], v[132:135], v[156:159], v[220:223]\n"
    "v_cvt_pk_bf16_f32 v208, v208, v209\n"
    "v_cvt_pk_bf16_f32 v209, v210, v211\n"
    "v_mfma_f32_16x16x32_bf16 v[216:219], v[112:115], v[160:163], v[216:219]\n"
    "v_cvt_pk_bf16_f32 v210, v212, v213\n"
    "v_cvt_pk_bf16_f32 v211, v214, v215\n"
    "v_mfma_f32_16x16x32_bf16 v[220:223], v[136:139], v[160:163], v[220:223]\n"
    "ds_write_b128 v7, v[208:211] offset:1024\n"
    "v_mfma_f32_16x16x32_bf16 v[216:219], v[116:119], v[164:167], v[216:219]\n"
    "s_waitcnt lgkmcnt(8)\n"
    "v_cmp_gt_u32 vcc, 1, v239\n"
    "v_mfma_f32_16x16x32_bf16 v[220:223], v[140:143], v[164:167], v[220:223]\n"
    "v_mfma_f32_16x16x32_bf16 v[216:219], v[120:123], v[168:171], v[216:219]\n"
    "s_cbranch_vccnz .Lg12\n"
    ".Lg12b:\n"
    "v_mfma_f32_16x16x32_bf16 v[220:223], v[144:147], v[168:171], v[220:223]\n"
    "v_mfma_f32_16x16x32_bf16 v[216:219], v[124:127], v[172:175], v[216:219]\n"
    "v_mfma_f32_16x16x32_bf16 v[220:223], v[148:151], v[172:175], v[220:223]\n"
    "s_waitcnt lgkmcnt(1)\n"
    "v_mfma_f32_16x16x32_bf16 v[200:203], v[8:11], v[176:179], 0\n"
    "v_mov_b32 v227, 7\n"
    "v_mfma_f32_16x16x32_bf16 v[204:207], v[32:35], v[176:179], 0\n"
    "v_mfma_f32_16x16x32_bf16 v[200:203], v[12:15], v[180:183], v[200:203]\n"
    "v_mfma_f32_16x16x32_bf16 v[204:207], v[36:39], v[180:183], v[204:207]\n"
    "v_mfma_f32_16x16x32_bf16 v[200:203], v[16:19], v[184:187], v[200:203]\n"
    "v_cvt_pk_bf16_f32 v216, v216, v217\n"
    "v_cvt_pk_bf16_f32 v217, v218, v219\n"
    "v_mfma_f32_16x16x32_bf16 v[204:207], v[40:43], v[184:187], v[204:207]\n"
    "v_cvt_pk_bf16_f32 v218, v220, v221\n"
    "v_cvt_pk_bf16_f32 v219, v222, v223\n"
    "v_mfma_f32_16x16x32_bf16 v[200:203], v[20:23], v[188:191], v[200:203]\n"
    "ds_write_b128 v7, v[216:219] offset:2048\n"
    "v_mfma_f32_16x16x32_bf16 v[204:207], v[44:47], v[188:191], v[204:207]\n"
    "ds_write_b32 v225, v227 offset:16\n"
    "ds_read_b32 v228, v224 offset:192\n"
    "v_mfma_f32_16x16x32_bf16 v[200:203], v[24:27], v[192:195], v[200:203]\n"
    "ds_read_b32 v239, v224 offset:112\n"
    "v_mfma_f32_16x16x32_bf16 v[204:207], v[48:51], v[192:195], v[204:207]\n"
    "v_mfma_f32_16x16x32_bf16 v[200:203], v[28:31], v[196:199], v[200:203]\n"
    "v_mfma_f32_16x16x32_bf16 v[204:207], v[52:55], v[196:199], v[204:207]\n"
    "v_mfma_f32_16x16x32_bf16 v[208:211], v[56:59], v[176:179], 0\n"
    "v_mfma_f32_16x16x32_bf16 v[212:215], v[80:83], v[176:179], 0\n"
    "ds_read_b32 v229, v224 offset:0\n"
    "v_mfma_f32_16x16x32_bf16 v[208:211], v[60:63], v[180:183], v[208:211]\n"
    "v_mfma_f32_16x16x32_bf16 v[212:215], v[84:87], v[180:183], v[212:215]\n"
    "s_waitcnt lgkmcnt(2)\n"
    "v_cmp_gt_u32 vcc, 2, v228\n"
    "v_mfma_f32_16x16x32_bf16 v[208:211], v[64:67], v[184:187], v[208:211]\n"
    "v_cvt_pk_bf16_f32 v200, v200, v201\n"
    "s_cbranch_vccnz .Lg13\n"
    ".Lg13b:\n"
    "v_mfma_f32_16x16x32_bf16 v[212:215], v[88:91], v[184:187], v[212:215]\n"
    "v_cvt_pk_bf16_f32 v201, v202, v203\n"
    "v_cvt_pk_bf16_f32 v202, v204, v205\n"
    "v_mfma_f32_16x16x32_bf16 v[208:211], v[68:71], v[188:191], v[208:211]\n"
    "v_cvt_pk_bf16_f32 v203, v206, v207\n"
    "v_mfma_f32_16x16x32_bf16 v[212:215], v[92:95], v[188:191], v[212:215]\n"
    "ds_write_b128 v7, v[200:203] offset:6144\n"
    "v_mfma_f32_16x16x32_bf16 v[208:211], v[72:75], v[192:195], v[208:211]\n"
    "v_mfma_f32_16x16x32_bf16 v[212:215], v[96:99], v[192:195], v[212:215]\n"
    "s_waitcnt lgkmcnt(1)\n"
    "v_cmp_gt_u32 vcc, 7, v229\n"
    "v_mfma_f32_16x16x32_bf16 v[208:211], v[76:79], v[196:199], v[208:211]\n"
    "v_mfma_f32_16x16x32_bf16 v[212:215], v[100:103], v[196:199], v[212:215]\n"
    "s_cbranch_vccnz .Lg14\n"
    ".Lg14b:\n"
    "v_mfma_f32_16x16x32_bf16 v[216:219], v[104:107], v[176:179], 0\n"
    "ds_read_b128 v[152:155], v4 offset:26688\n"
    "ds_read_b128 v[156:159], v4 offset:26752\n"
    "v_mfma_f32_16x16x32_bf16 v[220:223], v[128:131], v[176:179], 0\n"
    "ds_read_b128 v[160:163], v4 offset:26816\n"
    "ds_read_b128 v[164:167], v4 offset:26880\n"
    "v_mfma_f32_16x16x32_bf16 v[216:219], v[108:111], v[180:183], v[216:219]\n"
    "ds_read_b128 v[168:171], v4 offset:26944\n"
    "ds_read_b128 v[172:175], v4 offset:27008\n"
    "v_mfma_f32_16x16x32_bf16 v[220:223], v[132:135], v[180:183], v[220:223]\n"
    "v_cvt_pk_bf16_f32 v208, v208, v209\n"
    "v_cvt_pk_bf16_f32 v209, v210, v211\n"
    "v_mfma_f32_16x16x32_bf16 v[216:219], v[112:115], v[184:187], v[216:219]\n"
    "v_cvt_pk_bf16_f32 v210, v212, v213\n"
    "v_cvt_pk_bf16_f32 v211, v214, v215\n"
    "v_mfma_f32_16x16x32_bf16 v[220:223], v[136:139], v[184:187], v[220:223]\n"
    "ds_write_b128 v7, v[208:211] offset:7168\n"
    "v_mfma_f32_16x16x32_bf16 v[216:219], v[116:119], v[188:191], v[216:219]\n"
    "v_cmp_gt_u32 vcc, 1, v239\n"
    "v_mfma_f32_16x16x32_bf16 v[220:223], v[140:143], v[188:191], v[220:223]\n"
    "s_cbranch_vccnz .Lg15\n"
    ".Lg15b:\n"
    "v_mfma_f32_16x16x32_bf16 v[216:219], v[120:123], v[192:195], v[216:219]\n"
    "s_add_u32 s60, s42, s46\n"
    "s_addc_u32 s61, s43, 0\n"
    "v_mfma_f32_16x16x32_bf16 v[220:223], v[144:147], v[192:195], v[220:223]\n"
    "s_add_u32 m0, s54, 17792\n"
    "v_mfma_f32_16x16x32_bf16 v[216:219], v[124:127], v[196:199], v[216:219]\n"
    "global_load_lds_dwordx4 v230, s[60:61]\n"
    "s_add_u32 m0, s54, 19840\n"
    "v_mfma_f32_16x16x32_bf16 v[220:223], v[148:151], v[196:199], v[220:223]\n"
    "global_load_lds_dwordx4 v231, s[60:61]\n"
    "s_add_u32 m0, s54, 21888\n"
    "s_waitcnt lgkmcnt(1)\n"
    "global_load_lds_dwordx4 v232, s[60:61]\n"
    "s_add_u32 m0, s55, 17792\n"
    "s_nop 0\n"
    "global_load_lds_dwordx4 v233, s[60:61]\n"
    "s_add_u32 m0, s56, 17792\n"
    "s_nop 0\n"
    "global_load_lds_dwordx4 v234, s[60:61]\n"
    "s_add_u32 s60, s42, s47\n"
    "s_addc_u32 s61, s43, 0\n"
    "s_add_u32 m0, s54, 26688\n"
    "s_nop 0\n"
    "global_load_lds_dwordx4 v230, s[60:61]\n"
    "s_add_u32 m0, s54, 28736\n"
    "s_nop 0\n"
    "global_load_lds_dwordx4 v231, s[60:61]\n"
    "s_add_u32 m0, s54, 30784\n"
    "s_nop 0\n"
    "global_load_lds_dwordx4 v232, s[60:61]\n"
    "s_add_u32 m0, s55, 26688\n"
    "s_nop 0\n"
    "global_load_lds_dwordx4 v233, s[60:61]\n"
    "s_add_u32 m0, s56, 26688\n"
    "s_nop 0\n"
    "global_load_lds_dwordx4 v234, s[60:61]\n"
    "v_mfma_f32_16x16x32_bf16 v[200:203], v[8:11], v[152:155], 0\n"
    "v_cvt_pk_bf16_f32 v216, v216, v217\n"
    "v_cvt_pk_bf16_f32 v217, v218, v219\n"
    "v_mfma_f32_16x16x32_bf16 v[204:207], v[32:35], v[152:155], 0\n"
    "v_cvt_pk_bf16_f32 v218, v220, v221\n"
    "v_cvt_pk_bf16_f32 v219, v222, v223\n"
    "v_mfma_f32_16x16x32_bf16 v[200:203], v[12:15], v[156:159], v[200:203]\n"
    "ds_write_b128 v7, v[216:219] offset:8192\n"
    "v_mfma_f32_16x16x32_bf16 v[204:207], v[36:39], v[156:159], v[204:207]\n"
    "v_mov_b32 v227, 8\n"
    "v_mfma_f32_16x16x32_bf16 v[200:203], v[16:19], v[160:163], v[200:203]\n"
    "ds_write_b32 v225, v227 offset:16\n"
    "ds_read_b32 v228, v224 offset:208\n"
    "v_mfma_f32_16x16x32_bf16 v[204:207], v[40:43], v[160:163], v[204:207]\n"
    "ds_read_b32 v239, v224 offset:64\n"
    "s_waitcnt vmcnt(10)\n"
    "v_mfma_f32_16x16x32_bf16 v[200:203], v[20:23], v[164:167], v[200:203]\n"
    "v_mov_b32 v227, 8\n"
    "v_mfma_f32_16x16x32_bf16 v[204:207], v[44:47], v[164:167], v[204:207]\n"
    "ds_write_b32 v226, v227 offset:0\n"
    "v_mfma_f32_16x16x32_bf16 v[200:203], v[24:27], v[168:171], v[200:203]\n"
    "v_mfma_f32_16x16x32_bf16 v[204:207], v[48:51], v[168:171], v[204:207]\n"
    "v_mfma_f32_16x16x32_bf16 v[200:203], v[28:31], v[172:175], v[200:203]\n"
    "v_mfma_f32_16x16x32_bf16 v[204:207], v[52:55], v[172:175], v[204:207]\n"
    "v_mfma_f32_16x16x32_bf16 v[208:211], v[56:59], v[152:155], 0\n"
    "s_waitcnt lgkmcnt(2)\n"
    "v_cmp_gt_u32 vcc, 2, v228\n"
    "v_mfma_f32_16x16x32_bf16 v[212:215], v[80:83], v[152:155], 0\n"
    "v_mfma_f32_16x16x32_bf16 v[208:211], v[60:63], v[156:159], v[208:211]\n"
    "s_cbranch_vccnz .Lg16\n"
    ".Lg16b:\n"
    "v_mfma_f32_16x16x32_bf16 v[212:215], v[84:87], v[156:159], v[212:215]\n"
    "v_cvt_pk_bf16_f32 v200, v200, v201\n"
    "v_cvt_pk_bf16_f32 v201, v202, v203\n"
    "v_mfma_f32_16x16x32_bf16 v[208:211], v[64:67], v[160:163], v[208:211]\n"
    "v_cvt_pk_bf16_f32 v202, v204, v205\n"
    "v_cvt_pk_bf16_f32 v203, v206, v207\n"
    "v_mfma_f32_16x16x32_bf16 v[212:215], v[88:91], v[160:163], v[212:215]\n"
    "ds_write_b128 v7, v[200:203] offset:12288\n"
    "v_mfma_f32_16x16x32_bf16 v[208:211], v[68:71], v[164:167], v[208:211]\n"
    "v_mfma_f32_16x16x32_bf16 v[212:215], v[92:95], v[164:167], v[212:215]\n"
    "v_mfma_f32_16x16x32_bf16 v[208:211], v[72:75], v[168:171], v[208:211]\n"
    "v_mfma_f32_16x16x32_bf16 v[212:215], v[96:99], v[168:171], v[212:215]\n"
    "v_mfma_f32_16x16x32_bf16 v[208:211], v[76:79], v[172:175], v[208:211]\n"
    "v_mfma_f32_16x16x32_bf16 v[212:215], v[100:103], v[172:175], v[212:215]\n"
    "v_mfma_f32_16x16x32_bf16 v[216:219], v[104:107], v[152:155], 0\n"
    "ds_read_b128 v[176:179], v5 offset:26688\n"
    "ds_read_b128 v[180:183], v5 offset:26752\n"
    "v_mfma_f32_16x16x32_bf16 v[220:223], v[128:131], v[152:155], 0\n"
    "ds_read_b128 v[184:187], v5 offset:26816\n"
    "ds_read_b128 v[188:191], v5 offset:26880\n"
    "v_mfma_f32_16x16x32_bf16 v[216:219], v[108:111], v[156:159], v[216:219]\n"
    "ds_read_b128 v[192:195], v5 offset:26944\n"
    "ds_read_b128 v[196:199], v5 offset:27008\n"
    "v_mfma_f32_16x16x32_bf16 v[220:223], v[132:135], v[156:159], v[220:223]\n"
    "v_cvt_pk_bf16_f32 v208, v208, v209\n"
    "v_cvt_pk_bf16_f32 v209, v210, v211\n"
    "v_mfma_f32_16x16x32_bf16 v[216:219], v[112:115], v[160:163], v[216:219]\n"
    "v_cvt_pk_bf16_f32 v210, v212, v213\n"
    "v_cvt_pk_bf16_f32 v211, v214, v215\n"
    "v_mfma_f32_16x16x32_bf16 v[220:223], v[136:139], v[160:163], v[220:223]\n"
    "ds_write_b128 v7, v[208:211] offset:13312\n"
    "v_mfma_f32_16x16x32_bf16 v[216:219], v[116:119], v[164:167], v[216:219]\n"
    "s_waitcnt lgkmcnt(9)\n"
    "v_cmp_gt_u32 vcc, 2, v239\n"
    "v_mfma_f32_16x16x32_bf16 v[220:223], v[140:143], v[164:167], v[220:223]\n"
    "v_mfma_f32_16x16x32_bf16 v[216:219], v[120:123], v[168:171], v[216:219]\n"
    "s_cbranch_vccnz .Lg17\n"
    ".Lg17b:\n"
    "v_mfma_f32_16x16x32_bf16 v[220:223], v[144:147], v[168:171], v[220:223]\n"
    "v_mfma_f32_16x16x32_bf16 v[216:219], v[124:127], v[172:175], v[216:219]\n"
    "v_mfma_f32_16x16x32_bf16 v[220:223], v[148:151], v[172:175], v[220:223]\n"
    "s_waitcnt lgkmcnt(1)\n"
    "v_mfma_f32_16x16x32_bf16 v[200:203], v[8:11], v[176:179], 0\n"
    "v_mov_b32 v227, 9\n"
    "v_mfma_f32_16x16x32_bf16 v[204:207], v[32:35], v[176:179], 0\n"
    "v_mfma_f32_16x16x32_bf16 v[200:203], v[12:15], v[180:183], v[200:203]\n"
    "v_mfma_f32_16x16x32_bf16 v[204:207], v[36:39], v[180:183], v[204:207]\n"
    "v_mfma_f32_16x16x32_bf16 v[200:203], v[16:19], v[184:187], v[200:203]\n"
    "v_cvt_pk_bf16_f32 v216, v216, v217\n"
    "v_cvt_pk_bf16_f32 v217, v218, v219\n"
    "v_mfma_f32_16x16x32_bf16 v[204:207], v[40:43], v[184:187], v[204:207]\n"
    "v_cvt_pk_bf16_f32 v218, v220, v221\n"
    "v_cvt_pk_bf16_f32 v219, v222, v223\n"
    "v_mfma_f32_16x16x32_bf16 v[200:203], v[20:23], v[188:191], v[200:203]\n"
    "ds_write_b128 v7, v[216:219] offset:14336\n"
    "v_mfma_f32_16x16x32_bf16 v[204:207], v[44:47], v[188:191], v[204:207]\n"
    "ds_write_b32 v225, v227 offset:16\n"
    "ds_read_b32 v228, v224 offset:224\n"
    "v_mfma_f32_16x16x32_bf16 v[200:203], v[24:27], v[192:195], v[200:203]\n"
    "ds_read_b32 v239, v224 offset:80\n"
    "v_mfma_f32_16x16x32_bf16 v[204:207], v[48:51], v[192:195], v[204:207]\n"
    "v_mfma_f32_16x16x32_bf16 v[200:203], v[28:31], v[196:199], v[200:203]\n"
    "v_mfma_f32_16x16x32_bf16 v[204:207], v[52:55], v[196:199], v[204:207]\n"
    "v_mfma_f32_16x16x32_bf16 v[208:211], v[56:59], v[176:179], 0\n"
    "v_mfma_f32_16x16x32_bf16 v[212:215], v[80:83], v[176:179], 0\n"
    "ds_read_b32 v229, v224 offset:0\n"
    "v_mfma_f32_16x16x32_bf16 v[208:211], v[60:63], v[180:183], v[208:211]\n"
    "v_mfma_f32_16x16x32_bf16 v[212:215], v[84:87], v[180:183], v[212:215]\n"
    "s_waitcnt lgkmcnt(2)\n"
    "v_cmp_gt_u32 vcc, 2, v228\n"
    "v_mfma_f32_16x16x32_bf16 v[208:211], v[64:67], v[184:187], v[208:211]\n"
    "v_cvt_pk_bf16_f32 v200, v200, v201\n"
    "s_cbranch_vccnz .Lg18\n"
    ".Lg18b:\n"
    "v_mfma_f32_16x16x32_bf16 v[212:215], v[88:91], v[184:187], v[212:215]\n"
    "v_cvt_pk_bf16_f32 v201, v202, v203\n"
    "v_cvt_pk_bf16_f32 v202, v204, v205\n"
    "v_mfma_f32_16x16x32_bf16 v[208:211], v[68:71], v[188:191], v[208:211]\n"
    "v_cvt_pk_bf16_f32 v203, v206, v207\n"
    "v_mfma_f32_16x16x32_bf16 v[212:215], v[92:95], v[188:191], v[212:215]\n"
    "ds_write_b128 v7, v[200:203] offset:0\n"
    "v_mfma_f32_16x16x32_bf16 v[208:211], v[72:75], v[192:195], v[208:211]\n"
    "v_mfma_f32_16x16x32_bf16 v[212:215], v[96:99], v[192:195], v[212:215]\n"
    "s_waitcnt lgkmcnt(1)\n"
    "v_cmp_gt_u32 vcc, 8, v229\n"
    "v_mfma_f32_16x16x32_bf16 v[208:211], v[76:79], v[196:199], v[208:211]\n"
    "v_mfma_f32_16x16x32_bf16 v[212:215], v[100:103], v[196:199], v[212:215]\n"
    "s_cbranch_vccnz .Lg19\n"
    ".Lg19b:\n"
    "v_mfma_f32_16x16x32_bf16 v[216:219], v[104:107], v[176:179], 0\n"
    "v_add_u32 v4, v6, v4\n"
    "v_cmp_gt_u32 vcc, 2, v239\n"
    "v_mfma_f32_16x16x32_bf16 v[220:223], v[128:131], v[176:179], 0\n"
    "ds_read_b128 v[152:155], v4 offset:0\n"
    "ds_read_b128 v[156:159], v4 offset:64\n"
    "v_mfma_f32_16x16x32_bf16 v[216:219], v[108:111], v[180:183], v[216:219]\n"
    "ds_read_b128 v[160:163], v4 offset:128\n"
    "ds_read_b128 v[164:167], v4 offset:192\n"
    "v_mfma_f32_16x16x32_bf16 v[220:223], v[132:135], v[180:183], v[220:223]\n"
    "ds_read_b128 v[168:171], v4 offset:256\n"
    "ds_read_b128 v[172:175], v4 offset:320\n"
    "v_mfma_f32_16x16x32_bf16 v[216:219], v[112:115], v[184:187], v[216:219]\n"
    "v_cvt_pk_bf16_f32 v208, v208, v209\n"
    "v_cvt_pk_bf16_f32 v209, v210, v211\n"
    "v_mfma_f32_16x16x32_bf16 v[220:223], v[136:139], v[184:187], v[220:223]\n"
    "v_cvt_pk_bf16_f32 v210, v212, v213\n"
    "v_cvt_pk_bf16_f32 v211, v214, v215\n"
    "v_mfma_f32_16x16x32_bf16 v[216:219], v[116:119], v[188:191], v[216:219]\n"
    "ds_write_b128 v7, v[208:211] offset:1024\n"
    "v_mfma_f32_16x16x32_bf16 v[220:223], v[140:143], v[188:191], v[220:223]\n"
    "s_cbranch_vccnz .Lg20\n"
    ".Lg20b:\n"
    "v_mfma_f32_16x16x32_bf16 v[216:219], v[120:123], v[192:195], v[216:219]\n"
    "s_add_u32 s60, s42, s48\n"
    "s_addc_u32 s61, s43, 0\n"
    "v_mfma_f32_16x16x32_bf16 v[220:223], v[144:147], v[192:195], v[220:223]\n"
    "s_add_u32 m0, s54, 35584\n"
    "v_mfma_f32_16x16x32_bf16 v[216:219], v[124:127], v[196:199], v[216:219]\n"
    "global_load_lds_dwordx4 v230, s[60:61]\n"
    "s_add_u32 m0, s54, 37632\n"
    "v_mfma_f32_16x16x32_bf16 v[220:223], v[148:151], v[196:199], v[220:223]\n"
    "global_load_lds_dwordx4 v231, s[60:61]\n"
    "s_add_u32 m0, s54, 39680\n"
    "s_waitcnt lgkmcnt(1)\n"
    "global_load_lds_dwordx4 v232, s[60:61]\n"
    "s_add_u32 m0, s55, 35584\n"
    "s_nop 0\n"
    "global_load_lds_dwordx4 v233, s[60:61]\n"
    "s_add_u32 m0, s56, 35584\n"
    "s_nop 0\n"
    "global_load_lds_dwordx4 v234, s[60:61]\n"
    "s_add_u32 s60, s42, s49\n"
    "s_addc_u32 s61, s43, 0\n"
    "s_add_u32 m0, s54, 44480\n"
    "s_nop 0\n"
    "global_load_lds_dwordx4 v230, s[60:61]\n"
    "s_add_u32 m0, s54, 46528\n"
    "s_nop 0\n"
    "global_load_lds_dwordx4 v231, s[60:61]\n"
    "s_add_u32 m0, s54, 48576\n"
    "s_nop 0\n"
    "global_load_lds_dwordx4 v232, s[60:61]\n"
    "s_add_u32 m0, s55, 44480\n"
    "s_nop 0\n"
    "global_load_lds_dwordx4 v233, s[60:61]\n"
    "s_add_u32 m0, s56, 44480\n"
    "s_nop 0\n"
    "global_load_lds_dwordx4 v234, s[60:61]\n"
    "v_mfma_f32_16x16x32_bf16 v[200:203], v[8:11], v[152:155], 0\n"
    "v_cvt_pk_bf16_f32 v216, v216, v217\n"
    "v_cvt_pk_bf16_f32 v217, v218, v219\n"
    "v_mfma_f32_16x16x32_bf16 v[204:207], v[32:35], v[152:155], 0\n"
    "v_cvt_pk_bf16_f32 v218, v220, v221\n"
    "v_cvt_pk_bf16_f32 v219, v222, v223\n"
    "v_mfma_f32_16x16x32_bf16 v[200:203], v[12:15], v[156:159], v[200:203]\n"
    "ds_write_b128 v7, v[216:219] offset:2048\n"
    "v_mfma_f32_16x16x32_bf16 v[204:207], v[36:39], v[156:159], v[204:207]\n"
    "v_mov_b32 v227, 10\n"
    "v_mfma_f32_16x16x32_bf16 v[200:203], v[16:19], v[160:163], v[200:203]\n"
    "ds_write_b32 v225, v227 offset:16\n"
    "ds_read_b32 v228, v224 offset:240\n"
    "v_mfma_f32_16x16x32_bf16 v[204:207], v[40:43], v[160:163], v[204:207]\n"
    "s_waitcnt vmcnt(10)\n"
    "v_mov_b32 v227, 9\n"
    "v_mfma_f32_16x16x32_bf16 v[200:203], v[20:23], v[164:167], v[200:203]\n"
    "ds_write_b32 v226, v227 offset:0\n"
    "v_mfma_f32_16x16x32_bf16 v[204:207], v[44:47], v[164:167], v[204:207]\n"
    "v_mfma_f32_16x16x32_bf16 v[200:203], v[24:27], v[168:171], v[200:203]\n"
    "v_mfma_f32_16x16x32_bf16 v[204:207], v[48:51], v[168:171], v[204:207]\n"
    "v_mfma_f32_16x16x32_bf16 v[200:203], v[28:31], v[172:175], v[200:203]\n"
    "v_mfma_f32_16x16x32_bf16 v[204:207], v[52:55], v[172:175], v[204:207]\n"
    "v_mfma_f32_16x16x32_bf16 v[208:211], v[56:59], v[152:155], 0\n"
    "s_waitcnt lgkmcnt(1)\n"
    "v_cmp_gt_u32 vcc, 2, v228\n"
    "v_mfma_f32_16x16x32_bf16 v[212:215], v[80:83], v[152:155], 0\n"
    "v_mfma_f32_16x16x32_bf16 v[208:211], v[60:63], v[156:159], v[208:211]\n"
    "s_cbranch_vccnz .Lg21\n"
    ".Lg21b:\n"
    "v_mfma_f32_16x16x32_bf16 v[212:215], v[84:87], v[156:159], v[212:215]\n"
    "v_cvt_pk_bf16_f32 v200, v200, v201\n"
    "v_cvt_pk_bf16_f32 v201, v202, v203\n"
    "v_mfma_f32_16x16x32_bf16 v[208:211], v[64:67], v[160:163], v[208:211]\n"
    "v_cvt_pk_bf16_f32 v202, v204, v205\n"
    "v_cvt_pk_bf16_f32 v203, v206, v207\n"
    "v_mfma_f32_16x16x32_bf16 v[212:215], v[88:91], v[160:163], v[212:215]\n"
    "ds_write_b128 v7, v[200:203] offset:6144\n"
    "v_mfma_f32_16x16x32_bf16 v[208:211], v[68:71], v[164:167], v[208:211]\n"
    "v_mfma_f32_16x16x32_bf16 v[212:215], v[92:95], v[164:167], v[212:215]\n"
    "v_mfma_f32_16x16x32_bf16 v[208:211], v[72:75], v[168:171], v[208:211]\n"
    "v_mfma_f32_16x16x32_bf16 v[212:215], v[96:99], v[168:171], v[212:215]\n"
    "v_mfma_f32_16x16x32_bf16 v[208:211], v[76:79], v[172:175], v[208:211]\n"
    "v_mfma_f32_16x16x32_bf16 v[212:215], v[100:103], v[172:175], v[212:215]\n"
    "v_mfma_f32_16x16x32_bf16 v[216:219], v[104:107], v[152:155], 0\n"
    "v_add_u32 v5, v6, v5\n"
    "v_mfma_f32_16x16x32_bf16 v[220:223], v[128:131], v[152:155], 0\n"
    "ds_read_b128 v[176:179], v5 offset:0\n"
    "ds_read_b128 v[180:183], v5 offset:64\n"
    "v_mfma_f32_16x16x32_bf16 v[216:219], v[108:111], v[156:159], v[216:219]\n"
    "ds_read_b128 v[184:187], v5 offset:128\n"
    "ds_read_b128 v[188:191], v5 offset:192\n"
    "v_mfma_f32_16x16x32_bf16 v[220:223], v[132:135], v[156:159], v[220:223]\n"
    "ds_read_b128 v[192:195], v5 offset:256\n"
    "ds_read_b128 v[196:199], v5 offset:320\n"
    "v_mfma_f32_16x16x32_bf16 v[216:219], v[112:115], v[160:163], v[216:219]\n"
    "v_cvt_pk_bf16_f32 v208, v208, v209\n"
    "v_cvt_pk_bf16_f32 v209, v210, v211\n"
    "v_mfma_f32_16x16x32_bf16 v[220:223], v[136:139], v[160:163], v[220:223]\n"
    "v_cvt_pk_bf16_f32 v210, v212, v213\n"
    "v_cvt_pk_bf16_f32 v211, v214, v215\n"
    "v_mfma_f32_16x16x32_bf16 v[216:219], v[116:119], v[164:167], v[216:219]\n"
    "ds_write_b128 v7, v[208:211] offset:7168\n"
    "v_mfma_f32_16x16x32_bf16 v[220:223], v[140:143], v[164:167], v[220:223]\n"
    "v_mfma_f32_16x16x32_bf16 v[216:219], v[120:123], v[168:171], v[216:219]\n"
    "v_mfma_f32_16x16x32_bf16 v[220:223], v[144:147], v[168:171], v[220:223]\n"
    "v_mfma_f32_16x16x32_bf16 v[216:219], v[124:127], v[172:175], v[216:219]\n"
    "v_mfma_f32_16x16x32_bf16 v[220:223], v[148:151], v[172:175], v[220:223]\n"
    "s_waitcnt lgkmcnt(1)\n"
    "s_lshl1_add_u32 s53, s58, s50\n"
    "s_cmp_eq_u32 s53, 2\n"
    "s_cbranch_scc1 .Lg_e12a\n"
    "s_cmp_eq_u32 s53, 3\n"
    "s_cbranch_scc1 .Lg_e12b\n"
    "v_mfma_f32_16x16x32_bf16 v[200:203], v[8:11], v[176:179], 0\n"
    "v_cvt_pk_bf16_f32 v216, v216, v217\n"
    "v_cvt_pk_bf16_f32 v217, v218, v219\n"
    "v_mfma_f32_16x16x32_bf16 v[204:207], v[32:35], v[176:179], 0\n"
    "v_cvt_pk_bf16_f32 v218, v220, v221\n"
    "v_cvt_pk_bf16_f32 v219, v222, v223\n"
    "v_mfma_f32_16x16x32_bf16 v[200:203], v[12:15], v[180:183], v[200:203]\n"
    "ds_write_b128 v7, v[216:219] offset:8192\n"
    "v_mfma_f32_16x16x32_bf16 v[204:207], v[36:39], v[180:183], v[204:207]\n"
    "v_mov_b32 v227, 11\n"
    "v_mfma_f32_16x16x32_bf16 v[200:203], v[16:19], v[184:187], v[200:203]\n"
    "ds_write_b32 v225, v227 offset:16\n"
    "ds_read_b32 v228, v224 offset:192\n"
    "v_mfma_f32_16x16x32_bf16 v[204:207], v[40:43], v[184:187], v[204:207]\n"
    "v_mfma_f32_16x16x32_bf16 v[200:203], v[20:23], v[188:191], v[200:203]\n"
    "v_mfma_f32_16x16x32_bf16 v[204:207], v[44:47], v[188:191], v[204:207]\n"
    "v_mfma_f32_16x16x32_bf16 v[200:203], v[24:27], v[192:195], v[200:203]\n"
    "v_mfma_f32_16x16x32_bf16 v[204:207], v[48:51], v[192:195], v[204:207]\n"
    "v_mfma_f32_16x16x32_bf16 v[200:203], v[28:31], v[196:199], v[200:203]\n"
    "v_mfma_f32_16x16x32_bf16 v[204:207], v[52:55], v[196:199], v[204:207]\n"
    "v_mfma_f32_16x16x32_bf16 v[208:211], v[56:59], v[176:179], 0\n"
    "s_waitcnt lgkmcnt(0)\n"
    "v_cmp_gt_u32 vcc, 3, v228\n"
    "v_mfma_f32_16x16x32_bf16 v[212:215], v[80:83], v[176:179], 0\n"
    "ds_read_b32 v229, v224 offset:0\n"
    "s_cbranch_vccnz .Lg24\n"
    ".Lg24b:\n"
    "v_mfma_f32_16x16x32_bf16 v[208:211], v[60:63], v[180:183], v[208:211]\n"
    "v_cvt_pk_bf16_f32 v200, v200, v201\n"
    "v_cvt_pk_bf16_f32 v201, v202, v203\n"
    "v_mfma_f32_16x16x32_bf16 v[212:215], v[84:87], v[180:183], v[212:215]\n"
    "v_cvt_pk_bf16_f32 v202, v204, v205\n"
    "v_cvt_pk_bf16_f32 v203, v206, v207\n"
    "v_mfma_f32_16x16x32_bf16 v[208:211], v[64:67], v[184:187], v[208:211]\n"
    "ds_write_b128 v7, v[200:203] offset:12288\n"
    "v_mfma_f32_16x16x32_bf16 v[212:215], v[88:91], v[184:187], v[212:215]\n"
    "v_mfma_f32_16x16x32_bf16 v[208:211], v[68:71], v[188:191], v[208:211]\n"
    "v_mfma_f32_16x16x32_bf16 v[212:215], v[92:95], v[188:191], v[212:215]\n"
    "v_mfma_f32_16x16x32_bf16 v[208:211], v[72:75], v[192:195], v[208:211]\n"
    "v_mfma_f32_16x16x32_bf16 v[212:215], v[96:99], v[192:195], v[212:215]\n"
    "s_waitcnt lgkmcnt(1)\n"
    "v_cmp_gt_u32 vcc, 9, v229\n"
    "v_mfma_f32_16x16x32_bf16 v[208:211], v[76:79], v[196:199], v[208:211]\n"
    "v_mfma_f32_16x16x32_bf16 v[212:215], v[100:103], v[196:199], v[212:215]\n"
    "s_cbranch_vccnz .Lg25\n"
    ".Lg25b:\n"
    "v_mfma_f32_16x16x32_bf16 v[216:219], v[104:107], v[176:179], 0\n"
    "v_add_u32 v2, s84, v2\n"
    "v_mfma_f32_16x16x32_bf16 v[220:223], v[128:131], v[176:179], 0\n"
    "ds_read_b128 v[152:155], v2 offset:8896\n"
    "ds_read_b128 v[156:159], v2 offset:8960\n"
    "v_mfma_f32_16x16x32_bf16 v[216:219], v[108:111], v[180:183], v[216:219]\n"
    "ds_read_b128 v[160:163], v2 offset:9024\n"
    "ds_read_b128 v[164:167], v2 offset:9088\n"
    "v_mfma_f32_16x16x32_bf16 v[220:223], v[132:135], v[180:183], v[220:223]\n"
    "ds_read_b128 v[168:171], v2 offset:9152\n"
    "ds_read_b128 v[172:175], v2 offset:9216\n"
    "v_mfma_f32_16x16x32_bf16 v[216:219], v[112:115], v[184:187], v[216:219]\n"
    "v_cvt_pk_bf16_f32 v208, v208, v209\n"
    "v_cvt_pk_bf16_f32 v209, v210, v211\n"
    "v_mfma_f32_16x16x32_bf16 v[220:223], v[136:139], v[184:187], v[220:223]\n"
    "v_cvt_pk_bf16_f32 v210, v212, v213\n"
    "v_cvt_pk_bf16_f32 v211, v214, v215\n"
    "v_mfma_f32_16x16x32_bf16 v[216:219], v[116:119], v[188:191], v[216:219]\n"
    "ds_write_b128 v7, v[208:211] offset:13312\n"
    "v_mfma_f32_16x16x32_bf16 v[220:223], v[140:143], v[188:191], v[220:223]\n"
    "v_mfma_f32_16x16x32_bf16 v[216:219], v[120:123], v[192:195], v[216:219]\n"
    "v_mfma_f32_16x16x32_bf16 v[220:223], v[144:147], v[192:195], v[220:223]\n"
    "v_mfma_f32_16x16x32_bf16 v[216:219], v[124:127], v[196:199], v[216:219]\n"
    "v_mfma_f32_16x16x32_bf16 v[220:223], v[148:151], v[196:199], v[220:223]\n"
    "s_waitcnt lgkmcnt(1)\n"
    "s_cmp_eq_u32 s50, 0\n"
    "s_cbranch_scc1 .Lg_e13a\n"
    "v_mfma_f32_16x16x32_bf16 v[200:203], v[8:11], v[152:155], 0\n"
    "v_mov_b32 v227, 12\n"
    "v_mfma_f32_16x16x32_bf16 v[204:207], v[32:35], v[152:155], 0\n"
    "v_mfma_f32_16x16x32_bf16 v[200:203], v[12:15], v[156:159], v[200:203]\n"
    "v_cvt_pk_bf16_f32 v216, v216, v217\n"
    "v_cvt_pk_bf16_f32 v217, v218, v219\n"
    "v_mfma_f32_16x16x32_bf16 v[204:207], v[36:39], v[156:159], v[204:207]\n"
    "v_cvt_pk_bf16_f32 v218, v220, v221\n"
    "v_cvt_pk_bf16_f32 v219, v222, v223\n"
    "v_mfma_f32_16x16x32_bf16 v[200:203], v[16:19], v[160:163], v[200:203]\n"
    "ds_write_b128 v7, v[216:219] offset:14336\n"
    "v_mfma_f32_16x16x32_bf16 v[204:207], v[40:43], v[160:163], v[204:207]\n"
    "ds_write_b32 v225, v227 offset:16\n"
    "ds_read_b32 v228, v224 offset:208\n"
    "v_mfma_f32_16x16x32_bf16 v[200:203], v[20:23], v[164:167], v[200:203]\n"
    "s_waitcnt vmcnt(0)\n"
    "v_mov_b32 v227, 10\n"
    "v_mfma_f32_16x16x32_bf16 v[204:207], v[44:47], v[164:167], v[204:207]\n"
    "ds_write_b32 v226, v227 offset:0\n"
    "v_mfma_f32_16x16x32_bf16 v[200:203], v[24:27], v[168:171], v[200:203]\n"
    "v_mfma_f32_16x16x32_bf16 v[204:207], v[48:51], v[168:171], v[204:207]\n"
    "v_mfma_f32_16x16x32_bf16 v[200:203], v[28:31], v[172:175], v[200:203]\n"
    "v_mfma_f32_16x16x32_bf16 v[204:207], v[52:55], v[172:175], v[204:207]\n"
    "v_mfma_f32_16x16x32_bf16 v[208:211], v[56:59], v[152:155], 0\n"
    "s_add_u32 s60, s40, 109568\n"
    "s_addc_u32 s61, s41, 0\n"
    "v_mfma_f32_16x16x32_bf16 v[212:215], v[80:83], v[152:155], 0\n"
    "s_waitcnt lgkmcnt(1)\n"
    "v_cmp_gt_u32 vcc, 3, v228\n"
    "v_mfma_f32_16x16x32_bf16 v[208:211], v[60:63], v[156:159], v[208:211]\n"
    "v_cvt_pk_bf16_f32 v200, v200, v201\n"
    "s_cbranch_vccnz .Lg27\n"
    ".Lg27b:\n"
    "v_mfma_f32_16x16x32_bf16 v[212:215], v[84:87], v[156:159], v[212:215]\n"
    "v_cvt_pk_bf16_f32 v201, v202, v203\n"
    "v_cvt_pk_bf16_f32 v202, v204, v205\n"
    "v_mfma_f32_16x16x32_bf16 v[208:211], v[64:67], v[160:163], v[208:211]\n"
    "v_cvt_pk_bf16_f32 v203, v206, v207\n"
    "v_mfma_f32_16x16x32_bf16 v[212:215], v[88:91], v[160:163], v[212:215]\n"
    "ds_write_b128 v7, v[200:203] offset:0\n"
    "v_mfma_f32_16x16x32_bf16 v[208:211], v[68:71], v[164:167], v[208:211]\n"
    "s_add_u32 s66, s60, 6144\n"
    "s_addc_u32 s67, s61, 0\n"
    "v_mfma_f32_16x16x32_bf16 v[212:215], v[92:95], v[164:167], v[212:215]\n"
    "global_load_dwordx4 v[8:11], v235, s[60:61] offset:-3072 sc1\n"
    "global_load_dwordx4 v[32:35], v235, s[66:67] offset:-3072 sc1\n"
    "v_mfma_f32_16x16x32_bf16 v[208:211], v[72:75], v[168:171], v[208:211]\n"
    "global_load_dwordx4 v[12:15], v235, s[60:61] offset:-2048 sc1\n"
    "global_load_dwordx4 v[36:39], v235, s[66:67] offset:-2048 sc1\n"
    "v_mfma_f32_16x16x32_bf16 v[212:215], v[96:99], v[168:171], v[212:215]\n"
    "global_load_dwordx4 v[16:19], v235, s[60:61] offset:-1024 sc1\n"
    "global_load_dwordx4 v[40:43], v235, s[66:67] offset:-1024 sc1\n"
    "v_mfma_f32_16x16x32_bf16 v[208:211], v[76:79], v[172:175], v[208:211]\n"
    "global_load_dwordx4 v[20:23], v235, s[60:61] offset:0 sc1\n"
    "global_load_dwordx4 v[44:47], v235, s[66:67] offset:0 sc1\n"
    "v_mfma_f32_16x16x32_bf16 v[212:215], v[100:103], v[172:175], v[212:215]\n"
    "global_load_dwordx4 v[24:27], v235, s[60:61] offset:1024 sc1\n"
    "global_load_dwordx4 v[48:51], v235, s[66:67] offset:1024 sc1\n"
    "v_mfma_f32_16x16x32_bf16 v[216:219], v[104:107], v[152:155], 0\n"
    "global_load_dwordx4 v[28:31], v235, s[60:61] offset:2048 sc1\n"
    "global_load_dwordx4 v[52:55], v235, s[66:67] offset:2048 sc1\n"
    "v_mfma_f32_16x16x32_bf16 v[220:223], v[128:131], v[152:155], 0\n"
    "v_cvt_pk_bf16_f32 v208, v208, v209\n"
    "v_cvt_pk_bf16_f32 v209, v210, v211\n"
    "v_mfma_f32_16x16x32_bf16 v[216:219], v[108:111], v[156:159], v[216:219]\n"
    "v_cvt_pk_bf16_f32 v210, v212, v213\n"
    "v_cvt_pk_bf16_f32 v211, v214, v215\n"
    "v_mfma_f32_16x16x32_bf16 v[220:223], v[132:135], v[156:159], v[220:223]\n"
    "ds_write_b128 v7, v[208:211] offset:1024\n"
    "v_mfma_f32_16x16x32_bf16 v[216:219], v[112:115], v[160:163], v[216:219]\n"
    "s_add_u32 s60, s40, 146432\n"
    "s_addc_u32 s61, s41, 0\n"
    "v_mfma_f32_16x16x32_bf16 v[220:223], v[136:139], v[160:163], v[220:223]\n"
    "s_add_u32 s66, s60, 6144\n"
    "s_addc_u32 s67, s61, 0\n"
    "v_mfma_f32_16x16x32_bf16 v[216:219], v[116:119], v[164:167], v[216:219]\n"
    "global_load_dwordx4 v[56:59], v235, s[60:61] offset:-3072 sc1\n"
    "global_load_dwordx4 v[80:83], v235, s[66:67] offset:-3072 sc1\n"
    "v_mfma_f32_16x16x32_bf16 v[220:223], v[140:143], v[164:167], v[220:223]\n"
    "global_load_dwordx4 v[60:63], v235, s[60:61] offset:-2048 sc1\n"
    "global_load_dwordx4 v[84:87], v235, s[66:67] offset:-2048 sc1\n"
    "v_mfma_f32_16x16x32_bf16 v[216:219], v[120:123], v[168:171], v[216:219]\n"
    "global_load_dwordx4 v[64:67], v235, s[60:61] offset:-1024 sc1\n"
    "global_load_dwordx4 v[88:91], v235, s[66:67] offset:-1024 sc1\n"
    "v_mfma_f32_16x16x32_bf16 v[220:223], v[144:147], v[168:171], v[220:223]\n"
    "global_load_dwordx4 v[68:71], v235, s[60:61] offset:0 sc1\n"
    "global_load_dwordx4 v[92:95], v235, s[66:67] offset:0 sc1\n"
    "v_mfma_f32_16x16x32_bf16 v[216:219], v[124:127], v[172:175], v[216:219]\n"
    "global_load_dwordx4 v[72:75], v235, s[60:61] offset:1024 sc1\n"
    "global_load_dwordx4 v[96:99], v235, s[66:67] offset:1024 sc1\n"
    "v_mfma_f32_16x16x32_bf16 v[220:223], v[148:151], v[172:175], v[220:223]\n"
    "global_load_dwordx4 v[76:79], v235, s[60:61] offset:2048 sc1\n"
    "global_load_dwordx4 v[100:103], v235, s[66:67] offset:2048 sc1\n"
    "s_nop 2\n"
    "v_cvt_pk_bf16_f32 v216, v216, v217\n"
    "v_cvt_pk_bf16_f32 v217, v218, v219\n"
    "s_nop 0\n"
    "v_cvt_pk_bf16_f32 v218, v220, v221\n"
    "v_cvt_pk_bf16_f32 v219, v222, v223\n"
    "ds_write_b128 v7, v[216:219] offset:2048\n"
    "v_mov_b32 v227, 13\n"
    "ds_write_b32 v225, v227 offset:16\n"
    ".Lg_to:\n"
    "s_waitcnt vmcnt(12)\n"
    "v_sub_u32 v8, v8, v240\n"
    "v_sub_u32 v12, v12, v240\n"
    "v_sub_u32 v16, v16, v240\n"
    "v_sub_u32 v20, v20, v240\n"
    "v_sub_u32 v24, v24, v240\n"
    "v_sub_u32 v28, v28, v240\n"
    "v_sub_u32 v32, v32, v240\n"
    "v_sub_u32 v36, v36, v240\n"
    "v_sub_u32 v40, v40, v240\n"
    "v_sub_u32 v44, v44, v240\n"
    "v_sub_u32 v48, v48, v240\n"
    "v_sub_u32 v52, v52, v240\n"
    "s_waitcnt vmcnt(0)\n"
    "v_sub_u32 v56, v56, v240\n"
    "v_sub_u32 v60, v60, v240\n"
    "v_sub_u32 v64, v64, v240\n"
    "v_sub_u32 v68, v68, v240\n"
    "v_sub_u32 v72, v72, v240\n"
    "v_sub_u32 v76, v76, v240\n"
    "v_sub_u32 v80, v80, v240\n"
    "v_sub_u32 v84, v84, v240\n"
    "v_sub_u32 v88, v88, v240\n"
    "v_sub_u32 v92, v92, v240\n"
    "v_sub_u32 v96, v96, v240\n"
    "v_sub_u32 v100, v100, v240\n"
    "ds_read_b32 v228, v224 offset:128\n"
    "s_waitcnt lgkmcnt(0)\n"
    "v_cmp_gt_u32 vcc, 3, v228\n"
    "s_cbranch_vccnz .Lg28\n"
    ".Lg28b:\n"
    "ds_read_b64_tr_b16 v[152:153], v0 offset:30720\n"
    "ds_read_b64_tr_b16 v[154:155], v1 offset:30720\n"
    "ds_read_b64_tr_b16 v[156:157], v0 offset:31744\n"
    "ds_read_b64_tr_b16 v[158:159], v1 offset:31744\n"
    "ds_read_b64_tr_b16 v[160:161], v0 offset:32768\n"
    "ds_read_b64_tr_b16 v[162:163], v1 offset:32768\n"
    "ds_read_b64_tr_b16 v[164:165], v0 offset:33792\n"
    "ds_read_b64_tr_b16 v[166:167], v1 offset:33792\n"
    "ds_read_b64_tr_b16 v[168:169], v0 offset:34816\n"
    "ds_read_b64_tr_b16 v[170:171], v1 offset:34816\n"
    "ds_read_b64_tr_b16 v[172:173], v0 offset:35840\n"
    "ds_read_b64_tr_b16 v[174:175], v1 offset:35840\n"
    "ds_read_b32 v252, v238 offset:128\n"
    "s_waitcnt lgkmcnt(9)\n"
    "v_mfma_f32_16x16x32_bf16 v[200:203], v[8:11], v[152:155], 0\n"
    "v_mfma_f32_16x16x32_bf16 v[204:207], v[32:35], v[152:155], 0\n"
    "ds_read_b32 v228, v224 offset:144\n"
    "v_mfma_f32_16x16x32_bf16 v[200:203], v[12:15], v[156:159], v[200:203]\n"
    "v_mfma_f32_16x16x32_bf16 v[204:207], v[36:39], v[156:159], v[204:207]\n"
    "s_waitcnt lgkmcnt(8)\n"
    "v_mfma_f32_16x16x32_bf16 v[200:203], v[16:19], v[160:163], v[200:203]\n"
    "v_mfma_f32_16x16x32_bf16 v[204:207], v[40:43], v[160:163], v[204:207]\n"
    "s_waitcnt lgkmcnt(6)\n"
    "v_mfma_f32_16x16x32_bf16 v[200:203], v[20:23], v[164:167], v[200:203]\n"
    "v_mfma_f32_16x16x32_bf16 v[204:207], v[44:47], v[164:167], v[204:207]\n"
    "s_waitcnt lgkmcnt(4)\n"
    "v_mfma_f32_16x16x32_bf16 v[200:203], v[24:27], v[168:171], v[200:203]\n"
    "v_mfma_f32_16x16x32_bf16 v[204:207], v[48:51], v[168:171], v[204:207]\n"
    "s_waitcnt lgkmcnt(2)\n"
    "v_mfma_f32_16x16x32_bf16 v[200:203], v[28:31], v[172:175], v[200:203]\n"
    "s_waitcnt lgkmcnt(0)\n"
    "v_cmp_gt_u32 vcc, 3, v228\n"
    "v_mfma_f32_16x16x32_bf16 v[204:207], v[52:55], v[172:175], v[204:207]\n"
    "s_cbranch_vccnz .Lg29\n"
    "v_mfma_f32_16x16x32_bf16 v[208:211], v[56:59], v[152:155], 0\n"
    "ds_read_b64_tr_b16 v[176:177], v0 offset:18432\n"
    "ds_read_b64_tr_b16 v[178:179], v1 offset:18432\n"
    "v_mfma_f32_16x16x32_bf16 v[212:215], v[80:83], v[152:155], 0\n"
    "ds_read_b64_tr_b16 v[180:181], v0 offset:19456\n"
    "ds_read_b64_tr_b16 v[182:183], v1 offset:19456\n"
    "v_mfma_f32_16x16x32_bf16 v[208:211], v[60:63], v[156:159], v[208:211]\n"
    "ds_read_b64_tr_b16 v[184:185], v0 offset:20480\n"
    "ds_read_b64_tr_b16 v[186:187], v1 offset:20480\n"
    "v_mfma_f32_16x16x32_bf16 v[212:215], v[84:87], v[156:159], v[212:215]\n"
    "ds_read_b64_tr_b16 v[188:189], v0 offset:21504\n"
    "ds_read_b64_tr_b16 v[190:191], v1 offset:21504\n"
    "v_mfma_f32_16x16x32_bf16 v[208:211], v[64:67], v[160:163], v[208:211]\n"
    "v_mul_f32 v200, v200, v252\n"
    "v_mul_f32 v201, v201, v252\n"
    "v_mfma_f32_16x16x32_bf16 v[212:215], v[88:91], v[160:163], v[212:215]\n"
    "ds_read_b64_tr_b16 v[192:193], v0 offset:22528\n"
    "ds_read_b64_tr_b16 v[194:195], v1 offset:22528\n"
    "v_mfma_f32_16x16x32_bf16 v[208:211], v[68:71], v[164:167], v[208:211]\n"
    "v_mul_f32 v202, v202, v252\n"
    "v_mul_f32 v203, v203, v252\n"
    "v_mfma_f32_16x16x32_bf16 v[212:215], v[92:95], v[164:167], v[212:215]\n"
    "ds_read_b64_tr_b16 v[196:197], v0 offset:23552\n"
    "ds_read_b64_tr_b16 v[198:199], v1 offset:23552\n"
    "v_mfma_f32_16x16x32_bf16 v[208:211], v[72:75], v[168:171], v[208:211]\n"
    "v_mul_f32 v204, v204, v252\n"
    "v_mul_f32 v205, v205, v252\n"
    "v_mfma_f32_16x16x32_bf16 v[212:215], v[96:99], v[168:171], v[212:215]\n"
    "ds_read_b32 v253, v238 offset:0\n"
    "v_mul_f32 v206, v206, v252\n"
    "v_mfma_f32_16x16x32_bf16 v[208:211], v[76:79], v[172:175], v[208:211]\n"
    "v_mul_f32 v207, v207, v252\n"
    "v_cvt_pk_bf16_f32 v200, v200, v201\n"
    "v_mfma_f32_16x16x32_bf16 v[212:215], v[100:103], v[172:175], v[212:215]\n"
    "v_cvt_pk_bf16_f32 v201, v202, v203\n"
    "v_cvt_pk_bf16_f32 v202, v204, v205\n"
    "v_cvt_pk_bf16_f32 v203, v206, v207\n"
    "buffer_store_dwordx4 v[200:203], v248, s[80:83], 0 offen offset:128 sc1\n"
    ".Lg29j:\n"
    "s_waitcnt lgkmcnt(1)\n"
    "s_nop 0\n"
    "v_mfma_f32_16x16x32_bf16 v[200:203], v[8:11], v[176:179], 0\n"
    "v_mul_f32 v208, v208, v252\n"
    "v_mul_f32 v209, v209, v252\n"
    "v_mfma_f32_16x16x32_bf16 v[204:207], v[32:35], v[176:179], 0\n"
    "ds_read_b32 v228, v224 offset:160\n"
    "v_mul_f32 v210, v210, v252\n"
    "v_mfma_f32_16x16x32_bf16 v[200:203], v[12:15], v[180:183], v[200:203]\n"
    "v_mul_f32 v211, v211, v252\n"
    "v_mul_f32 v212, v212, v252\n"
    "v_mfma_f32_16x16x32_bf16 v[204:207], v[36:39], v[180:183], v[204:207]\n"
    "v_mul_f32 v213, v213, v252\n"
    "v_mul_f32 v214, v214, v252\n"
    "v_mfma_f32_16x16x32_bf16 v[200:203], v[16:19], v[184:187], v[200:203]\n"
    "v_mul_f32 v215, v215, v252\n"
    "v_cvt_pk_bf16_f32 v208, v208, v209\n"
    "v_mfma_f32_16x16x32_bf16 v[204:207], v[40:43], v[184:187], v[204:207]\n"
    "v_cvt_pk_bf16_f32 v209, v210, v211\n"
    "v_cvt_pk_bf16_f32 v210, v212, v213\n"
    "v_mfma_f32_16x16x32_bf16 v[200:203], v[20:23], v[188:191], v[200:203]\n"
    "v_cvt_pk_bf16_f32 v211, v214, v215\n"
    "v_mfma_f32_16x16x32_bf16 v[204:207], v[44:47], v[188:191], v[204:207]\n"
    "buffer_store_dwordx4 v[208:211], v248, s[80:83], 0 offen offset:320 sc1\n"
    "v_mfma_f32_16x16x32_bf16 v[200:203], v[24:27], v[192:195], v[200:203]\n"
    "v_mfma_f32_16x16x32_bf16 v[204:207], v[48:51], v[192:195], v[204:207]\n"
    "v_add_u32 v248, v251, v248\n"
    "v_mfma_f32_16x16x32_bf16 v[200:203], v[28:31], v[196:199], v[200:203]\n"
    "s_waitcnt lgkmcnt(0)\n"
    "v_cmp_gt_u32 vcc, 3, v228\n"
    "v_mfma_f32_16x16x32_bf16 v[204:207], v[52:55], v[196:199], v[204:207]\n"
    "s_cbranch_vccnz .Lg30\n"
    "v_mfma_f32_16x16x32_bf16 v[208:211], v[56:59], v[176:179], 0\n"
    "ds_read_b64_tr_b16 v[152:153], v0 offset:6144\n"
    "ds_read_b64_tr_b16 v[154:155], v1 offset:6144\n"
    "v_mfma_f32_16x16x32_bf16 v[212:215], v[80:83], v[176:179], 0\n"
    "ds_read_b64_tr_b16 v[156:157], v0 offset:7168\n"
    "ds_read_b64_tr_b16 v[158:159], v1 offset:7168\n"
    "v_mfma_f32_16x16x32_bf16 v[208:211], v[60:63], v[180:183], v[208:211]\n"
    "ds_read_b64_tr_b16 v[160:161], v0 offset:8192\n"
    "ds_read_b64_tr_b16 v[162:163], v1 offset:8192\n"
    "v_mfma_f32_16x16x32_bf16 v[212:215], v[84:87], v[180:183], v[212:215]\n"
    "ds_read_b64_tr_b16 v[164:165], v0 offset:9216\n"
    "ds_read_b64_tr_b16 v[166:167], v1 offset:9216\n"
    "v_mfma_f32_16x16x32_bf16 v[208:211], v[64:67], v[184:187], v[208:211]\n"
    "v_mul_f32 v200, v200, v253\n"
    "v_mul_f32 v201, v201, v253\n"
    "v_mfma_f32_16x16x32_bf16 v[212:215], v[88:91], v[184:187], v[212:215]\n"
    "ds_read_b64_tr_b16 v[168:169], v0 offset:10240\n"
    "ds_read_b64_tr_b16 v[170:171], v1 offset:10240\n"
    "v_mfma_f32_16x16x32_bf16 v[208:211], v[68:71], v[188:191], v[208:211]\n"
    "v_mul_f32 v202, v202, v253\n"
    "v_mul_f32 v203, v203, v253\n"
    "v_mfma_f32_16x16x32_bf16 v[212:215], v[92:95], v[188:191], v[212:215]\n"
    "ds_read_b64_tr_b16 v[172:173], v0 offset:11264\n"
    "ds_read_b64_tr_b16 v[174:175], v1 offset:11264\n"
    "v_mfma_f32_16x16x32_bf16 v[208:211], v[72:75], v[192:195], v[208:211]\n"
    "v_mul_f32 v204, v204, v253\n"
    "v_mul_f32 v205, v205, v253\n"
    "v_mfma_f32_16x16x32_bf16 v[212:215], v[96:99], v[192:195], v[212:215]\n"
    "ds_read_b32 v252, v238 offset:256\n"
    "v_mul_f32 v206, v206, v253\n"
    "v_mfma_f32_16x16x32_bf16 v[208:211], v[76:79], v[196:199], v[208:211]\n"
    "v_mul_f32 v207, v207, v253\n"
    "v_cvt_pk_bf16_f32 v200, v200, v201\n"
    "v_mfma_f32_16x16x32_bf16 v[212:215], v[100:103], v[196:199], v[212:215]\n"
    "v_cvt_pk_bf16_f32 v201, v202, v203\n"
    "v_cvt_pk_bf16_f32 v202, v204, v205\n"
    "v_cvt_pk_bf16_f32 v203, v206, v207\n"
    "buffer_store_dwordx4 v[200:203], v249, s[80:83], 0 offen offset:128 sc1\n"
    ".Lg30j:\n"
    "s_waitcnt lgkmcnt(1)\n"
    "s_nop 0\n"
    "v_mfma_f32_16x16x32_bf16 v[200:203], v[8:11], v[152:155], 0\n"
    "v_mul_f32 v208, v208, v253\n"
    "v_mul_f32 v209, v209, v253\n"
    "v_mfma_f32_16x16x32_bf16 v[204:207], v[32:35], v[152:155], 0\n"
    "ds_read_b32 v228, v224 offset:176\n"
    "v_mul_f32 v210, v210, v253\n"
    "v_mfma_f32_16x16x32_bf16 v[200:203], v[12:15], v[156:159], v[200:203]\n"
    "v_mul_f32 v211, v211, v253\n"
    "v_mul_f32 v212, v212, v253\n"
    "v_mfma_f32_16x16x32_bf16 v[204:207], v[36:39], v[156:159], v[204:207]\n"
    "v_mul_f32 v213, v213, v253\n"
    "v_mul_f32 v214, v214, v253\n"
    "v_mfma_f32_16x16x32_bf16 v[200:203], v[16:19], v[160:163], v[200:203]\n"
    "v_mul_f32 v215, v215, v253\n"
    "v_cvt_pk_bf16_f32 v208, v208, v209\n"
    "v_mfma_f32_16x16x32_bf16 v[204:207], v[40:43], v[160:163], v[204:207]\n"
    "v_cvt_pk_bf16_f32 v209, v210, v211\n"
    "v_cvt_pk_bf16_f32 v210, v212, v213\n"
    "v_mfma_f32_16x16x32_bf16 v[200:203], v[20:23], v[164:167], v[200:203]\n"
    "v_cvt_pk_bf16_f32 v211, v214, v215\n"
    "v_mfma_f32_16x16x32_bf16 v[204:207], v[44:47], v[164:167], v[204:207]\n"
    "buffer_store_dwordx4 v[208:211], v249, s[80:83], 0 offen offset:320 sc1\n"
    "v_mfma_f32_16x16x32_bf16 v[200:203], v[24:27], v[168:171], v[200:203]\n"
    "v_mfma_f32_16x16x32_bf16 v[204:207], v[48:51], v[168:171], v[204:207]\n"
    "v_add_u32 v249, v251, v249\n"
    "v_mfma_f32_16x16x32_bf16 v[200:203], v[28:31], v[172:175], v[200:203]\n"
    "s_waitcnt lgkmcnt(0)\n"
    "v_cmp_gt_u32 vcc, 3, v228\n"
    "v_mfma_f32_16x16x32_bf16 v[204:207], v[52:55], v[172:175], v[204:207]\n"
    "s_cbranch_vccnz .Lg31\n"
    "v_mfma_f32_16x16x32_bf16 v[208:211], v[56:59], v[152:155], 0\n"
    "ds_read_b64_tr_b16 v[176:177], v0 offset:12288\n"
    "ds_read_b64_tr_b16 v[178:179], v1 offset:12288\n"
    "v_mfma_f32_16x16x32_bf16 v[212:215], v[80:83], v[152:155], 0\n"
    "ds_read_b64_tr_b16 v[180:181], v0 offset:13312\n"
    "ds_read_b64_tr_b16 v[182:183], v1 offset:13312\n"
    "v_mfma_f32_16x16x32_bf16 v[208:211], v[60:63], v[156:159], v[208:211]\n"
    "ds_read_b64_tr_b16 v[184:185], v0 offset:14336\n"
    "ds_read_b64_tr_b16 v[186:187], v1 offset:14336\n"
    "v_mfma_f32_16x16x32_bf16 v[212:215], v[84:87], v[156:159], v[212:215]\n"
    "ds_read_b64_tr_b16 v[188:189], v0 offset:15360\n"
    "ds_read_b64_tr_b16 v[190:191], v1 offset:15360\n"
    "v_mfma_f32_16x16x32_bf16 v[208:211], v[64:67], v[160:163], v[208:211]\n"
    "v_mul_f32 v200, v200, v252\n"
    "v_mul_f32 v201, v201, v252\n"
    "v_mfma_f32_16x16x32_bf16 v[212:215], v[88:91], v[160:163], v[212:215]\n"
    "ds_read_b64_tr_b16 v[192:193], v0 offset:16384\n"
    "ds_read_b64_tr_b16 v[194:195], v1 offset:16384\n"
    "v_mfma_f32_16x16x32_bf16 v[208:211], v[68:71], v[164:167], v[208:211]\n"
    "v_mul_f32 v202, v202, v252\n"
    "v_mul_f32 v203, v203, v252\n"
    "v_mfma_f32_16x16x32_bf16 v[212:215], v[92:95], v[164:167], v[212:215]\n"
    "ds_read_b64_tr_b16 v[196:197], v0 offset:17408\n"
    "ds_read_b64_tr_b16 v[198:199], v1 offset:17408\n"
    "v_mfma_f32_16x16x32_bf16 v[208:211], v[72:75], v[168:171], v[208:211]\n"
    "v_mul_f32 v204, v204, v252\n"
    "v_mul_f32 v205, v205, v252\n"
    "v_mfma_f32_16x16x32_bf16 v[212:215], v[96:99], v[168:171], v[212:215]\n"
    "ds_read_b32 v253, v238 offset:320\n"
    "v_mul_f32 v206, v206, v252\n"
    "v_mfma_f32_16x16x32_bf16 v[208:211], v[76:79], v[172:175], v[208:211]\n"
    "v_mul_f32 v207, v207, v252\n"
    "v_cvt_pk_bf16_f32 v200, v200, v201\n"
    "v_mfma_f32_16x16x32_bf16 v[212:215], v[100:103], v[172:175], v[212:215]\n"
    "v_cvt_pk_bf16_f32 v201, v202, v203\n"
    "v_cvt_pk_bf16_f32 v202, v204, v205\n"
    "v_cvt_pk_bf16_f32 v203, v206, v207\n"
    "buffer_store_dwordx4 v[200:203], v248, s[80:83], 0 offen offset:128 sc1\n"
    ".Lg31j:\n"
    "s_cmp_lg_u32 s58, 0\n"
    "s_cbranch_scc1 .Lg_t12\n"
    "s_waitcnt lgkmcnt(1)\n"
    "v_mfma_f32_16x16x32_bf16 v[200:203], v[8:11], v[176:179], 0\n"
    "v_mul_f32 v208, v208, v252\n"
    "v_mul_f32 v209, v209, v252\n"
    "v_mfma_f32_16x16x32_bf16 v[204:207], v[32:35], v[176:179], 0\n"
    "ds_read_b32 v228, v224 offset:832\n"
    "v_mul_f32 v210, v210, v252\n"
    "v_mfma_f32_16x16x32_bf16 v[200:203], v[12:15], v[180:183], v[200:203]\n"
    "v_mul_f32 v211, v211, v252\n"
    "v_mul_f32 v212, v212, v252\n"
    "v_mfma_f32_16x16x32_bf16 v[204:207], v[36:39], v[180:183], v[204:207]\n"
    "v_mul_f32 v213, v213, v252\n"
    "v_mul_f32 v214, v214, v252\n"
    "v_mfma_f32_16x16x32_bf16 v[200:203], v[16:19], v[184:187], v[200:203]\n"
    "v_mul_f32 v215, v215, v252\n"
    "v_cvt_pk_bf16_f32 v208, v208, v209\n"
    "v_mfma_f32_16x16x32_bf16 v[204:207], v[40:43], v[184:187], v[204:207]\n"
    "v_cvt_pk_bf16_f32 v209, v210, v211\n"
    "v_cvt_pk_bf16_f32 v210, v212, v213\n"
    "v_mfma_f32_16x16x32_bf16 v[200:203], v[20:23], v[188:191], v[200:203]\n"
    "v_cvt_pk_bf16_f32 v211, v214, v215\n"
    "v_mfma_f32_16x16x32_bf16 v[204:207], v[44:47], v[188:191], v[204:207]\n"
    "buffer_store_dwordx4 v[208:211], v248, s[80:83], 0 offen offset:320 sc1\n"
    "v_mfma_f32_16x16x32_bf16 v[200:203], v[24:27], v[192:195], v[200:203]\n"
    "v_mfma_f32_16x16x32_bf16 v[204:207], v[48:51], v[192:195], v[204:207]\n"
    "v_add_u32 v248, v255, v248\n"
    "v_mfma_f32_16x16x32_bf16 v[200:203], v[28:31], v[196:199], v[200:203]\n"
    "s_waitcnt lgkmcnt(0)\n"
    "v_cmp_gt_u32 vcc, 4, v228\n"
    "v_mfma_f32_16x16x32_bf16 v[204:207], v[52:55], v[196:199], v[204:207]\n"
    "s_cbranch_vccnz .Lg32\n"
    "v_mfma_f32_16x16x32_bf16 v[208:211], v[56:59], v[176:179], 0\n"
    "ds_read_b64_tr_b16 v[152:153], v0 offset:0\n"
    "ds_read_b64_tr_b16 v[154:155], v1 offset:0\n"
    "v_mfma_f32_16x16x32_bf16 v[212:215], v[80:83], v[176:179], 0\n"
    "ds_read_b64_tr_b16 v[156:157], v0 offset:1024\n"
    "ds_read_b64_tr_b16 v[158:159], v1 offset:1024\n"
    "v_mfma_f32_16x16x32_bf16 v[208:211], v[60:63], v[180:183], v[208:211]\n"
    "ds_read_b64_tr_b16 v[160:161], v0 offset:2048\n"
    "ds_read_b64_tr_b16 v[162:163], v1 offset:2048\n"
    "v_mfma_f32_16x16x32_bf16 v[212:215], v[84:87], v[180:183], v[212:215]\n"
    "ds_read_b64_tr_b16 v[164:165], v0 offset:3072\n"
    "ds_read_b64_tr_b16 v[166:167], v1 offset:3072\n"
    "v_mfma_f32_16x16x32_bf16 v[208:211], v[64:67], v[184:187], v[208:211]\n"
    "v_mul_f32 v200, v200, v253\n"
    "v_mul_f32 v201, v201, v253\n"
    "v_mfma_f32_16x16x32_bf16 v[212:215], v[88:91], v[184:187], v[212:215]\n"
    "ds_read_b64_tr_b16 v[168:169], v0 offset:4096\n"
    "ds_read_b64_tr_b16 v[170:171], v1 offset:4096\n"
    "v_mfma_f32_16x16x32_bf16 v[208:211], v[68:71], v[188:191], v[208:211]\n"
    "v_mul_f32 v202, v202, v253\n"
    "v_mul_f32 v203, v203, v253\n"
    "v_mfma_f32_16x16x32_bf16 v[212:215], v[92:95], v[188:191], v[212:215]\n"
    "ds_read_b64_tr_b16 v[172:173], v0 offset:5120\n"
    "ds_read_b64_tr_b16 v[174:175], v1 offset:5120\n"
    "v_mfma_f32_16x16x32_bf16 v[208:211], v[72:75], v[192:195], v[208:211]\n"
    "v_mul_f32 v204, v204, v253\n"
    "v_mul_f32 v205, v205, v253\n"
    "v_mfma_f32_16x16x32_bf16 v[212:215], v[96:99], v[192:195], v[212:215]\n"
    "ds_read_b32 v252, v238 offset:192\n"
    "v_mul_f32 v206, v206, v253\n"
    "v_mfma_f32_16x16x32_bf16 v[208:211], v[76:79], v[196:199], v[208:211]\n"
    "v_mul_f32 v207, v207, v253\n"
    "v_cvt_pk_bf16_f32 v200, v200, v201\n"
    "v_mfma_f32_16x16x32_bf16 v[212:215], v[100:103], v[196:199], v[212:215]\n"
    "v_cvt_pk_bf16_f32 v201, v202, v203\n"
    "v_cvt_pk_bf16_f32 v202, v204, v205\n"
    "v_cvt_pk_bf16_f32 v203, v206, v207\n"
    "buffer_store_dwordx4 v[200:203], v249, s[80:83], 0 offen offset:128 sc1\n"
    ".Lg32j:\n"
    "s_waitcnt lgkmcnt(1)\n"
    "s_nop 0\n"
    "v_mfma_f32_16x16x32_bf16 v[200:203], v[8:11], v[152:155], 0\n"
    "v_mul_f32 v208, v208, v253\n"
    "v_mul_f32 v209, v209, v253\n"
    "v_mfma_f32_16x16x32_bf16 v[204:207], v[32:35], v[152:155], 0\n"
    "v_mul_f32 v210, v210, v253\n"
    "v_mul_f32 v211, v211, v253\n"
    "v_mfma_f32_16x16x32_bf16 v[200:203], v[12:15], v[156:159], v[200:203]\n"
    "v_mul_f32 v212, v212, v253\n"
    "v_mul_f32 v213, v213, v253\n"
    "v_mfma_f32_16x16x32_bf16 v[204:207], v[36:39], v[156:159], v[204:207]\n"
    "v_mul_f32 v214, v214, v253\n"
    "v_mul_f32 v215, v215, v253\n"
    "v_mfma_f32_16x16x32_bf16 v[200:203], v[16:19], v[160:163], v[200:203]\n"
    "v_cvt_pk_bf16_f32 v208, v208, v209\n"
    "v_cvt_pk_bf16_f32 v209, v210, v211\n"
    "v_mfma_f32_16x16x32_bf16 v[204:207], v[40:43], v[160:163], v[204:207]\n"
    "v_cvt_pk_bf16_f32 v210, v212, v213\n"
    "v_cvt_pk_bf16_f32 v211, v214, v215\n"
    "v_mfma_f32_16x16x32_bf16 v[200:203], v[20:23], v[164:167], v[200:203]\n"
    "buffer_store_dwordx4 v[208:211], v249, s[80:83], 0 offen offset:320 sc1\n"
    "v_mfma_f32_16x16x32_bf16 v[204:207], v[44:47], v[164:167], v[204:207]\n"
    "v_mfma_f32_16x16x32_bf16 v[200:203], v[24:27], v[168:171], v[200:203]\n"
    "v_mfma_f32_16x16x32_bf16 v[204:207], v[48:51], v[168:171], v[204:207]\n"
    "v_mfma_f32_16x16x32_bf16 v[200:203], v[28:31], v[172:175], v[200:203]\n"
    "v_mfma_f32_16x16x32_bf16 v[204:207], v[52:55], v[172:175], v[204:207]\n"
    "v_mfma_f32_16x16x32_bf16 v[208:211], v[56:59], v[152:155], 0\n"
    "v_mfma_f32_16x16x32_bf16 v[212:215], v[80:83], v[152:155], 0\n"
    "v_mfma_f32_16x16x32_bf16 v[208:211], v[60:63], v[156:159], v[208:211]\n"
    "v_mfma_f32_16x16x32_bf16 v[212:215], v[84:87], v[156:159], v[212:215]\n"
    "v_mfma_f32_16x16x32_bf16 v[208:211], v[64:67], v[160:163], v[208:211]\n"
    "v_mfma_f32_16x16x32_bf16 v[212:215], v[88:91], v[160:163], v[212:215]\n"
    "s_waitcnt lgkmcnt(0)\n"
    "v_mul_f32 v200, v200, v252\n"
    "v_mfma_f32_16x16x32_bf16 v[208:211], v[68:71], v[164:167], v[208:211]\n"
    "v_mul_f32 v201, v201, v252\n"
    "v_mul_f32 v202, v202, v252\n"
    "v_mfma_f32_16x16x32_bf16 v[212:215], v[92:95], v[164:167], v[212:215]\n"
    "v_mul_f32 v203, v203, v252\n"
    "v_mul_f32 v204, v204, v252\n"
    "v_mfma_f32_16x16x32_bf16 v[208:211], v[72:75], v[168:171], v[208:211]\n"
    "v_mul_f32 v205, v205, v252\n"
    "v_mul_f32 v206, v206, v252\n"
    "v_mfma_f32_16x16x32_bf16 v[212:215], v[96:99], v[168:171], v[212:215]\n"
    "v_mul_f32 v207, v207, v252\n"
    "v_cvt_pk_bf16_f32 v200, v200, v201\n"
    "v_mfma_f32_16x16x32_bf16 v[208:211], v[76:79], v[172:175], v[208:211]\n"
    "v_cvt_pk_bf16_f32 v201, v202, v203\n"
    "v_cvt_pk_bf16_f32 v202, v204, v205\n"
    "v_mfma_f32_16x16x32_bf16 v[212:215], v[100:103], v[172:175], v[212:215]\n"
    "v_cvt_pk_bf16_f32 v203, v206, v207\n"
    "buffer_store_dwordx4 v[200:203], v248, s[80:83], 0 offen offset:128 sc1\n"
    "s_nop 2\n"
    "v_mul_f32 v208, v208, v252\n"
    "v_mul_f32 v209, v209, v252\n"
    "v_mul_f32 v210, v210, v252\n"
    "v_mul_f32 v211, v211, v252\n"
    "v_mul_f32 v212, v212, v252\n"
    "v_mul_f32 v213, v213, v252\n"
    "v_mul_f32 v214, v214, v252\n"
    "v_mul_f32 v215, v215, v252\n"
    "v_cvt_pk_bf16_f32 v208, v208, v209\n"
    "v_cvt_pk_bf16_f32 v209, v210, v211\n"
    "v_cvt_pk_bf16_f32 v210, v212, v213\n"
    "v_cvt_pk_bf16_f32 v211, v214, v215\n"
    "buffer_store_dwordx4 v[208:211], v248, s[80:83], 0 offen offset:320 sc1\n"
    ".Lg_end:\n"
    "s_waitcnt lgkmcnt(0)\n"
    "s_endpgm\n"
    ".Lg1:\n"
    "s_sub_u32 s52, s52, 1\n"
    "s_cmp_lt_i32 s52, 0\n"
    "s_cbranch_scc1 .Lg1b\n"
    "s_sleep 1\n"
    "ds_read_b32 v228, v224 offset:48\n"
    "s_waitcnt lgkmcnt(0)\n"
    "v_cmp_gt_u32 vcc, 1, v228\n"
    "s_cbranch_vccnz .Lg1\n"
    "s_branch .Lg1b\n"
    ".Lg_fail:\n"
    "s_sub_u32 s51, s51, 1\n"
    "s_sleep 8\n"
    "s_cmp_gt_i32 s51, 0\n"
    "s_cbranch_scc1 .Lg_retry\n"
    "s_branch .Lg_ok\n"
    ".Lg2:\n"
    "s_sub_u32 s52, s52, 1\n"
    "s_cmp_lt_i32 s52, 0\n"
    "s_cbranch_scc1 .Lg2b\n"
    "s_sleep 1\n"
    "ds_read_b32 v228, v224 offset:0\n"
    "s_waitcnt lgkmcnt(0)\n"
    "v_cmp_gt_u32 vcc, 1, v228\n"
    "s_cbranch_vccnz .Lg2\n"
    "s_branch .Lg2b\n"
    ".Lg3:\n"
    "s_sub_u32 s52, s52, 1\n"
    "s_cmp_lt_i32 s52, 0\n"
    "s_cbranch_scc1 .Lg3b\n"
    "s_sleep 1\n"
    "ds_read_b32 v229, v224 offset:0\n"
    "s_waitcnt lgkmcnt(0)\n"
    "v_cmp_gt_u32 vcc, 2, v229\n"
    "s_cbranch_vccnz .Lg3\n"
    "s_branch .Lg3b\n"
    ".Lg4:\n"
    "s_sub_u32 s52, s52, 1\n"
    "s_cmp_lt_i32 s52, 0\n"
    "s_cbranch_scc1 .Lg4b\n"
    "s_sleep 1\n"
    "ds_read_b32 v228, v224 offset:192\n"
    "s_waitcnt lgkmcnt(0)\n"
    "v_cmp_gt_u32 vcc, 1, v228\n"
    "s_cbranch_vccnz .Lg4\n"
    "s_branch .Lg4b\n"
    ".Lg5:\n"
    "s_sub_u32 s52, s52, 1\n"
    "s_cmp_lt_i32 s52, 0\n"
    "s_cbranch_scc1 .Lg5b\n"
    "s_sleep 1\n"
    "ds_read_b32 v229, v224 offset:0\n"
    "s_waitcnt lgkmcnt(0)\n"
    "v_cmp_gt_u32 vcc, 3, v229\n"
    "s_cbranch_vccnz .Lg5\n"
    "s_branch .Lg5b\n"
    ".Lg6:\n"
    "s_sub_u32 s52, s52, 1\n"
    "s_cmp_lt_i32 s52, 0\n"
    "s_cbranch_scc1 .Lg6b\n"
    "s_sleep 1\n"
    "ds_read_b32 v228, v224 offset:208\n"
    "s_waitcnt lgkmcnt(0)\n"
    "v_cmp_gt_u32 vcc, 1, v228\n"
    "s_cbranch_vccnz .Lg6\n"
    "s_branch .Lg6b\n"
    ".Lg7:\n"
    "s_sub_u32 s52, s52, 1\n"
    "s_cmp_lt_i32 s52, 0\n"
    "s_cbranch_scc1 .Lg7b\n"
    "s_sleep 1\n"
    "ds_read_b32 v239, v224 offset:64\n"
    "s_waitcnt lgkmcnt(0)\n"
    "v_cmp_gt_u32 vcc, 1, v239\n"
    "s_cbranch_vccnz .Lg7\n"
    "s_branch .Lg7b\n"
    ".Lg8:\n"
    "s_sub_u32 s52, s52, 1\n"
    "s_cmp_lt_i32 s52, 0\n"
    "s_cbranch_scc1 .Lg8b\n"
    "s_sleep 1\n"
    "ds_read_b32 v228, v224 offset:224\n"
    "s_waitcnt lgkmcnt(0)\n"
    "v_cmp_gt_u32 vcc, 1, v228\n"
    "s_cbranch_vccnz .Lg8\n"
    "s_branch .Lg8b\n"
    ".Lg9:\n"
    "s_sub_u32 s52, s52, 1\n"
    "s_cmp_lt_i32 s52, 0\n"
    "s_cbranch_scc1 .Lg9b\n"
    "s_sleep 1\n"
    "ds_read_b32 v229, v224 offset:0\n"
    "s_waitcnt lgkmcnt(0)\n"
    "v_cmp_gt_u32 vcc, 6, v229\n"
    "s_cbranch_vccnz .Lg9\n"
    "s_branch .Lg9b\n"
    ".Lg10:\n"
    "s_sub_u32 s52, s52, 1\n"
    "s_cmp_lt_i32 s52, 0\n"
    "s_cbranch_scc1 .Lg10b\n"
    "s_sleep 1\n"
    "ds_read_b32 v239, v224 offset:80\n"
    "s_waitcnt lgkmcnt(0)\n"
    "v_cmp_gt_u32 vcc, 1, v239\n"
    "s_cbranch_vccnz .Lg10\n"
    "s_branch .Lg10b\n"
    ".Lg11:\n"
    "s_sub_u32 s52, s52, 1\n"
    "s_cmp_lt_i32 s52, 0\n"
    "s_cbranch_scc1 .Lg11b\n"
    "s_sleep 1\n"
    "ds_read_b32 v228, v224 offset:240\n"
    "s_waitcnt lgkmcnt(0)\n"
    "v_cmp_gt_u32 vcc, 1, v228\n"
    "s_cbranch_vccnz .Lg11\n"
    "s_branch .Lg11b\n"
    ".Lg12:\n"
    "s_sub_u32 s52, s52, 1\n"
    "s_cmp_lt_i32 s52, 0\n"
    "s_cbranch_scc1 .Lg12b\n"
    "s_sleep 1\n"
    "ds_read_b32 v239, v224 offset:96\n"
    "s_waitcnt lgkmcnt(0)\n"
    "v_cmp_gt_u32 vcc, 1, v239\n"
    "s_cbranch_vccnz .Lg12\n"
    "s_branch .Lg12b\n"
    ".Lg13:\n"
    "s_sub_u32 s52, s52, 1\n"
    "s_cmp_lt_i32 s52, 0\n"
    "s_cbranch_scc1 .Lg13b\n"
    "s_sleep 1\n"
    "ds_read_b32 v228, v224 offset:192\n"
    "s_waitcnt lgkmcnt(0)\n"
    "v_cmp_gt_u32 vcc, 2, v228\n"
    "s_cbranch_vccnz .Lg13\n"
    "s_branch .Lg13b\n"
    ".Lg14:\n"
    "s_sub_u32 s52, s52, 1\n"
    "s_cmp_lt_i32 s52, 0\n"
    "s_cbranch_scc1 .Lg14b\n"
    "s_sleep 1\n"
    "ds_read_b32 v229, v224 offset:0\n"
    "s_waitcnt lgkmcnt(0)\n"
    "v_cmp_gt_u32 vcc, 7, v229\n"
    "s_cbranch_vccnz .Lg14\n"
    "s_branch .Lg14b\n"
    ".Lg15:\n"
    "s_sub_u32 s52, s52, 1\n"
    "s_cmp_lt_i32 s52, 0\n"
    "s_cbranch_scc1 .Lg15b\n"
    "s_sleep 1\n"
    "ds_read_b32 v239, v224 offset:112\n"
    "s_waitcnt lgkmcnt(0)\n"
    "v_cmp_gt_u32 vcc, 1, v239\n"
    "s_cbranch_vccnz .Lg15\n"
    "s_branch .Lg15b\n"
    ".Lg16:\n"
    "s_sub_u32 s52, s52, 1\n"
    "s_cmp_lt_i32 s52, 0\n"
    "s_cbranch_scc1 .Lg16b\n"
    "s_sleep 1\n"
    "ds_read_b32 v228, v224 offset:208\n"
    "s_waitcnt lgkmcnt(0)\n"
    "v_cmp_gt_u32 vcc, 2, v228\n"
    "s_cbranch_vccnz .Lg16\n"
    "s_branch .Lg16b\n"
    ".Lg17:\n"
    "s_sub_u32 s52, s52, 1\n"
    "s_cmp_lt_i32 s52, 0\n"
    "s_cbranch_scc1 .Lg17b\n"
    "s_sleep 1\n"
    "ds_read_b32 v239, v224 offset:64\n"
    "s_waitcnt lgkmcnt(0)\n"
    "v_cmp_gt_u32 vcc, 2, v239\n"
    "s_cbranch_vccnz .Lg17\n"
    "s_branch .Lg17b\n"
    ".Lg18:\n"
    "s_sub_u32 s52, s52, 1\n"
    "s_cmp_lt_i32 s52, 0\n"
    "s_cbranch_scc1 .Lg18b\n"
    "s_sleep 1\n"
    "ds_read_b32 v228, v224 offset:224\n"
    "s_waitcnt lgkmcnt(0)\n"
    "v_cmp_gt_u32 vcc, 2, v228\n"
    "s_cbranch_vccnz .Lg18\n"
    "s_branch .Lg18b\n"
    ".Lg19:\n"
    "s_sub_u32 s52, s52, 1\n"
    "s_cmp_lt_i32 s52, 0\n"
    "s_cbranch_scc1 .Lg19b\n"
    "s_sleep 1\n"
    "ds_read_b32 v229, v224 offset:0\n"
    "s_waitcnt lgkmcnt(0)\n"
    "v_cmp_gt_u32 vcc, 8, v229\n"
    "s_cbranch_vccnz .Lg19\n"
    "s_branch .Lg19b\n"
    ".Lg20:\n"
    "s_sub_u32 s52, s52, 1\n"
    "s_cmp_lt_i32 s52, 0\n"
    "s_cbranch_scc1 .Lg20b\n"
    "s_sleep 1\n"
    "ds_read_b32 v239, v224 offset:80\n"
    "s_waitcnt lgkmcnt(0)\n"
    "v_cmp_gt_u32 vcc, 2, v239\n"
    "s_cbranch_vccnz .Lg20\n"
    "s_branch .Lg20b\n"
    ".Lg21:\n"
    "s_sub_u32 s52, s52, 1\n"
    "s_cmp_lt_i32 s52, 0\n"
    "s_cbranch_scc1 .Lg21b\n"
    "s_sleep 1\n"
    "ds_read_b32 v228, v224 offset:240\n"
    "s_waitcnt lgkmcnt(0)\n"
    "v_cmp_gt_u32 vcc, 2, v228\n"
    "s_cbranch_vccnz .Lg21\n"
    "s_branch .Lg21b\n"
    ".Lg22:\n"
    "s_sub_u32 s52, s52, 1\n"
    "s_cmp_lt_i32 s52, 0\n"
    "s_cbranch_scc1 .Lg22b\n"
    "s_sleep 1\n"
    "ds_read_b32 v228, v224 offset:192\n"
    "s_waitcnt lgkmcnt(0)\n"
    "v_cmp_gt_u32 vcc, 3, v228\n"
    "s_cbranch_vccnz .Lg22\n"
    "s_branch .Lg22b\n"
    ".Lg_e12a:\n"
    "v_mfma_f32_16x16x32_bf16 v[200:203], v[8:11], v[176:179], 0\n"
    "v_cvt_pk_bf16_f32 v216, v216, v217\n"
    "v_cvt_pk_bf16_f32 v217, v218, v219\n"
    "v_mfma_f32_16x16x32_bf16 v[204:207], v[32:35], v[176:179], 0\n"
    "v_cvt_pk_bf16_f32 v218, v220, v221\n"
    "v_cvt_pk_bf16_f32 v219, v222, v223\n"
    "v_mfma_f32_16x16x32_bf16 v[200:203], v[12:15], v[180:183], v[200:203]\n"
    "ds_write_b128 v7, v[216:219] offset:8192\n"
    "v_mfma_f32_16x16x32_bf16 v[204:207], v[36:39], v[180:183], v[204:207]\n"
    "v_mov_b32 v227, 11\n"
    "v_mfma_f32_16x16x32_bf16 v[200:203], v[16:19], v[184:187], v[200:203]\n"
    "ds_write_b32 v225, v227 offset:16\n"
    "ds_read_b32 v228, v224 offset:192\n"
    "v_mfma_f32_16x16x32_bf16 v[204:207], v[40:43], v[184:187], v[204:207]\n"
    "v_mfma_f32_16x16x32_bf16 v[200:203], v[20:23], v[188:191], v[200:203]\n"
    "v_mfma_f32_16x16x32_bf16 v[204:207], v[44:47], v[188:191], v[204:207]\n"
    "v_mfma_f32_16x16x32_bf16 v[200:203], v[24:27], v[192:195], v[200:203]\n"
    "v_mfma_f32_16x16x32_bf16 v[204:207], v[48:51], v[192:195], v[204:207]\n"
    "v_mfma_f32_16x16x32_bf16 v[200:203], v[28:31], v[196:199], v[200:203]\n"
    "v_mfma_f32_16x16x32_bf16 v[204:207], v[52:55], v[196:199], v[204:207]\n"
    "v_mfma_f32_16x16x32_bf16 v[208:211], v[56:59], v[176:179], 0\n"
    "s_waitcnt lgkmcnt(0)\n"
    "v_cmp_gt_u32 vcc, 3, v228\n"
    "v_mfma_f32_16x16x32_bf16 v[212:215], v[80:83], v[176:179], 0\n"
    "v_mfma_f32_16x16x32_bf16 v[208:211], v[60:63], v[180:183], v[208:211]\n"
    "s_cbranch_vccnz .Lg22\n"
    ".Lg22b:\n"
    "v_mfma_f32_16x16x32_bf16 v[212:215], v[84:87], v[180:183], v[212:215]\n"
    "v_cvt_pk_bf16_f32 v200, v200, v201\n"
    "v_cvt_pk_bf16_f32 v201, v202, v203\n"
    "v_mfma_f32_16x16x32_bf16 v[208:211], v[64:67], v[184:187], v[208:211]\n"
    "v_cvt_pk_bf16_f32 v202, v204, v205\n"
    "v_cvt_pk_bf16_f32 v203, v206, v207\n"
    "v_mfma_f32_16x16x32_bf16 v[212:215], v[88:91], v[184:187], v[212:215]\n"
    "ds_write_b128 v7, v[200:203] offset:12288\n"
    "v_mfma_f32_16x16x32_bf16 v[208:211], v[68:71], v[188:191], v[208:211]\n"
    "v_mfma_f32_16x16x32_bf16 v[212:215], v[92:95], v[188:191], v[212:215]\n"
    "v_mfma_f32_16x16x32_bf16 v[208:211], v[72:75], v[192:195], v[208:211]\n"
    "v_mfma_f32_16x16x32_bf16 v[212:215], v[96:99], v[192:195], v[212:215]\n"
    "v_mfma_f32_16x16x32_bf16 v[208:211], v[76:79], v[196:199], v[208:211]\n"
    "v_mfma_f32_16x16x32_bf16 v[212:215], v[100:103], v[196:199], v[212:215]\n"
    "v_mfma_f32_16x16x32_bf16 v[216:219], v[104:107], v[176:179], 0\n"
    "v_mfma_f32_16x16x32_bf16 v[220:223], v[128:131], v[176:179], 0\n"
    "v_mfma_f32_16x16x32_bf16 v[216:219], v[108:111], v[180:183], v[216:219]\n"
    "v_mfma_f32_16x16x32_bf16 v[220:223], v[132:135], v[180:183], v[220:223]\n"
    "v_mfma_f32_16x16x32_bf16 v[216:219], v[112:115], v[184:187], v[216:219]\n"
    "v_mfma_f32_16x16x32_bf16 v[220:223], v[136:139], v[184:187], v[220:223]\n"
    "v_mfma_f32_16x16x32_bf16 v[216:219], v[116:119], v[188:191], v[216:219]\n"
    "v_cvt_pk_bf16_f32 v208, v208, v209\n"
    "v_cvt_pk_bf16_f32 v209, v210, v211\n"
    "v_mfma_f32_16x16x32_bf16 v[220:223], v[140:143], v[188:191], v[220:223]\n"
    "v_cvt_pk_bf16_f32 v210, v212, v213\n"
    "v_cvt_pk_bf16_f32 v211, v214, v215\n"
    "v_mfma_f32_16x16x32_bf16 v[216:219], v[120:123], v[192:195], v[216:219]\n"
    "ds_write_b128 v7, v[208:211] offset:13312\n"
    "v_mfma_f32_16x16x32_bf16 v[220:223], v[144:147], v[192:195], v[220:223]\n"
    "v_mfma_f32_16x16x32_bf16 v[216:219], v[124:127], v[196:199], v[216:219]\n"
    "v_mfma_f32_16x16x32_bf16 v[220:223], v[148:151], v[196:199], v[220:223]\n"
    "s_nop 6\n"
    "v_cvt_pk_bf16_f32 v216, v216, v217\n"
    "v_cvt_pk_bf16_f32 v217, v218, v219\n"
    "v_cvt_pk_bf16_f32 v218, v220, v221\n"
    "v_cvt_pk_bf16_f32 v219, v222, v223\n"
    "ds_write_b128 v7, v[216:219] offset:14336\n"
    "v_mov_b32 v227, 12\n"
    "ds_write_b32 v225, v227 offset:16\n"
    "s_branch .Lg_end\n"
    ".Lg23:\n"
    "s_sub_u32 s52, s52, 1\n"
    "s_cmp_lt_i32 s52, 0\n"
    "s_cbranch_scc1 .Lg23b\n"
    "s_sleep 1\n"
    "ds_read_b32 v228, v224 offset:192\n"
    "s_waitcnt lgkmcnt(0)\n"
    "v_cmp_gt_u32 vcc, 3, v228\n"
    "s_cbranch_vccnz .Lg23\n"
    "s_branch .Lg23b\n"
    ".Lg_e12b:\n"
    "v_mfma_f32_16x16x32_bf16 v[200:203], v[8:11], v[176:179], 0\n"
    "v_cvt_pk_bf16_f32 v216, v216, v217\n"
    "v_cvt_pk_bf16_f32 v217, v218, v219\n"
    "v_mfma_f32_16x16x32_bf16 v[204:207], v[32:35], v[176:179], 0\n"
    "v_cvt_pk_bf16_f32 v218, v220, v221\n"
    "v_cvt_pk_bf16_f32 v219, v222, v223\n"
    "v_mfma_f32_16x16x32_bf16 v[200:203], v[12:15], v[180:183], v[200:203]\n"
    "ds_write_b128 v7, v[216:219] offset:8192\n"
    "v_mfma_f32_16x16x32_bf16 v[204:207], v[36:39], v[180:183], v[204:207]\n"
    "v_mov_b32 v227, 11\n"
    "v_mfma_f32_16x16x32_bf16 v[200:203], v[16:19], v[184:187], v[200:203]\n"
    "ds_write_b32 v225, v227 offset:16\n"
    "ds_read_b32 v228, v224 offset:192\n"
    "v_mfma_f32_16x16x32_bf16 v[204:207], v[40:43], v[184:187], v[204:207]\n"
    "v_mfma_f32_16x16x32_bf16 v[200:203], v[20:23], v[188:191], v[200:203]\n"
    "v_mfma_f32_16x16x32_bf16 v[204:207], v[44:47], v[188:191], v[204:207]\n"
    "v_mfma_f32_16x16x32_bf16 v[200:203], v[24:27], v[192:195], v[200:203]\n"
    "v_mfma_f32_16x16x32_bf16 v[204:207], v[48:51], v[192:195], v[204:207]\n"
    "v_mfma_f32_16x16x32_bf16 v[200:203], v[28:31], v[196:199], v[200:203]\n"
    "v_mfma_f32_16x16x32_bf16 v[204:207], v[52:55], v[196:199], v[204:207]\n"
    "v_mfma_f32_16x16x32_bf16 v[208:211], v[56:59], v[176:179], 0\n"
    "s_waitcnt lgkmcnt(0)\n"
    "v_cmp_gt_u32 vcc, 3, v228\n"
    "v_mfma_f32_16x16x32_bf16 v[212:215], v[80:83], v[176:179], 0\n"
    "s_add_u32 s60, s40, 109568\n"
    "s_addc_u32 s61, s41, 0\n"
    "v_mfma_f32_16x16x32_bf16 v[208:211], v[60:63], v[180:183], v[208:211]\n"
    "s_cbranch_vccnz .Lg23\n"
    ".Lg23b:\n"
    "v_cvt_pk_bf16_f32 v200, v200, v201\n"
    "v_mfma_f32_16x16x32_bf16 v[212:215], v[84:87], v[180:183], v[212:215]\n"
    "v_cvt_pk_bf16_f32 v201, v202, v203\n"
    "v_cvt_pk_bf16_f32 v202, v204, v205\n"
    "v_mfma_f32_16x16x32_bf16 v[208:211], v[64:67], v[184:187], v[208:211]\n"
    "v_cvt_pk_bf16_f32 v203, v206, v207\n"
    "v_mfma_f32_16x16x32_bf16 v[212:215], v[88:91], v[184:187], v[212:215]\n"
    "ds_write_b128 v7, v[200:203] offset:12288\n"
    "v_mfma_f32_16x16x32_bf16 v[208:211], v[68:71], v[188:191], v[208:211]\n"
    "s_add_u32 s66, s60, 6144\n"
    "s_addc_u32 s67, s61, 0\n"
    "v_mfma_f32_16x16x32_bf16 v[212:215], v[92:95], v[188:191], v[212:215]\n"
    "global_load_dwordx4 v[8:11], v235, s[60:61] offset:-3072 sc1\n"
    "global_load_dwordx4 v[32:35], v235, s[66:67] offset:-3072 sc1\n"
    "v_mfma_f32_16x16x32_bf16 v[208:211], v[72:75], v[192:195], v[208:211]\n"
    "global_load_dwordx4 v[12:15], v235, s[60:61] offset:-2048 sc1\n"
    "global_load_dwordx4 v[36:39], v235, s[66:67] offset:-2048 sc1\n"
    "v_mfma_f32_16x16x32_bf16 v[212:215], v[96:99], v[192:195], v[212:215]\n"
    "global_load_dwordx4 v[16:19], v235, s[60:61] offset:-1024 sc1\n"
    "global_load_dwordx4 v[40:43], v235, s[66:67] offset:-1024 sc1\n"
    "v_mfma_f32_16x16x32_bf16 v[208:211], v[76:79], v[196:199], v[208:211]\n"
    "global_load_dwordx4 v[20:23], v235, s[60:61] offset:0 sc1\n"
    "global_load_dwordx4 v[44:47], v235, s[66:67] offset:0 sc1\n"
    "v_mfma_f32_16x16x32_bf16 v[212:215], v[100:103], v[196:199], v[212:215]\n"
    "global_load_dwordx4 v[24:27], v235, s[60:61] offset:1024 sc1\n"
    "global_load_dwordx4 v[48:51], v235, s[66:67] offset:1024 sc1\n"
    "v_mfma_f32_16x16x32_bf16 v[216:219], v[104:107], v[176:179], 0\n"
    "global_load_dwordx4 v[28:31], v235, s[60:61] offset:2048 sc1\n"
    "global_load_dwordx4 v[52:55], v235, s[66:67] offset:2048 sc1\n"
    "v_mfma_f32_16x16x32_bf16 v[220:223], v[128:131], v[176:179], 0\n"
    "v_cvt_pk_bf16_f32 v208, v208, v209\n"
    "v_cvt_pk_bf16_f32 v209, v210, v211\n"
    "v_mfma_f32_16x16x32_bf16 v[216:219], v[108:111], v[180:183], v[216:219]\n"
    "v_cvt_pk_bf16_f32 v210, v212, v213\n"
    "v_cvt_pk_bf16_f32 v211, v214, v215\n"
    "v_mfma_f32_16x16x32_bf16 v[220:223], v[132:135], v[180:183], v[220:223]\n"
    "ds_write_b128 v7, v[208:211] offset:13312\n"
    "v_mfma_f32_16x16x32_bf16 v[216:219], v[112:115], v[184:187], v[216:219]\n"
    "s_add_u32 s60, s40, 146432\n"
    "s_addc_u32 s61, s41, 0\n"
    "v_mfma_f32_16x16x32_bf16 v[220:223], v[136:139], v[184:187], v[220:223]\n"
    "s_add_u32 s66, s60, 6144\n"
    "s_addc_u32 s67, s61, 0\n"
    "v_mfma_f32_16x16x32_bf16 v[216:219], v[116:119], v[188:191], v[216:219]\n"
    "global_load_dwordx4 v[56:59], v235, s[60:61] offset:-3072 sc1\n"
    "global_load_dwordx4 v[80:83], v235, s[66:67] offset:-3072 sc1\n"
    "v_mfma_f32_16x16x32_bf16 v[220:223], v[140:143], v[188:191], v[220:223]\n"
    "global_load_dwordx4 v[60:63], v235, s[60:61] offset:-2048 sc1\n"
    "global_load_dwordx4 v[84:87], v235, s[66:67] offset:-2048 sc1\n"
    "v_mfma_f32_16x16x32_bf16 v[216:219], v[120:123], v[192:195], v[216:219]\n"
    "global_load_dwordx4 v[64:67], v235, s[60:61] offset:-1024 sc1\n"
    "global_load_dwordx4 v[88:91], v235, s[66:67] offset:-1024 sc1\n"
    "v_mfma_f32_16x16x32_bf16 v[220:223], v[144:147], v[192:195], v[220:223]\n"
    "global_load_dwordx4 v[68:71], v235, s[60:61] offset:0 sc1\n"
    "global_load_dwordx4 v[92:95], v235, s[66:67] offset:0 sc1\n"
    "v_mfma_f32_16x16x32_bf16 v[216:219], v[124:127], v[196:199], v[216:219]\n"
    "global_load_dwordx4 v[72:75], v235, s[60:61] offset:1024 sc1\n"
    "global_load_dwordx4 v[96:99], v235, s[66:67] offset:1024 sc1\n"
    "v_mfma_f32_16x16x32_bf16 v[220:223], v[148:151], v[196:199], v[220:223]\n"
    "global_load_dwordx4 v[76:79], v235, s[60:61] offset:2048 sc1\n"
    "global_load_dwordx4 v[100:103], v235, s[66:67] offset:2048 sc1\n"
    "s_nop 2\n"
    "v_cvt_pk_bf16_f32 v216, v216, v217\n"
    "v_cvt_pk_bf16_f32 v217, v218, v219\n"
    "s_nop 0\n"
    "v_cvt_pk_bf16_f32 v218, v220, v221\n"
    "v_cvt_pk_bf16_f32 v219, v222, v223\n"
    "ds_write_b128 v7, v[216:219] offset:14336\n"
    "v_mov_b32 v227, 12\n"
    "ds_write_b32 v225, v227 offset:16\n"
    "s_branch .Lg_to\n"
    ".Lg24:\n"
    "s_sub_u32 s52, s52, 1\n"
    "s_cmp_lt_i32 s52, 0\n"
    "s_cbranch_scc1 .Lg24b\n"
    "s_sleep 1\n"
    "ds_read_b32 v228, v224 offset:192\n"
    "s_waitcnt lgkmcnt(0)\n"
    "v_cmp_gt_u32 vcc, 3, v228\n"
    "s_cbranch_vccnz .Lg24\n"
    "s_branch .Lg24b\n"
    ".Lg25:\n"
    "s_sub_u32 s52, s52, 1\n"
    "s_cmp_lt_i32 s52, 0\n"
    "s_cbranch_scc1 .Lg25b\n"
    "s_sleep 1\n"
    "ds_read_b32 v229, v224 offset:0\n"
    "s_waitcnt lgkmcnt(0)\n"
    "v_cmp_gt_u32 vcc, 9, v229\n"
    "s_cbranch_vccnz .Lg25\n"
    "s_branch .Lg25b\n"
    ".Lg26:\n"
    "s_sub_u32 s52, s52, 1\n"
    "s_cmp_lt_i32 s52, 0\n"
    "s_cbranch_scc1 .Lg26b\n"
    "s_sleep 1\n"
    "ds_read_b32 v228, v224 offset:208\n"
    "s_waitcnt lgkmcnt(0)\n"
    "v_cmp_gt_u32 vcc, 3, v228\n"
    "s_cbranch_vccnz .Lg26\n"
    "s_branch .Lg26b\n"
    ".Lg_e13a:\n"
    "s_setprio 2\n"
    "v_mfma_f32_16x16x32_bf16 v[200:203], v[8:11], v[152:155], 0\n"
    "v_mov_b32 v227, 12\n"
    "v_mfma_f32_16x16x32_bf16 v[204:207], v[32:35], v[152:155], 0\n"
    "v_cvt_pk_bf16_f32 v216, v216, v217\n"
    "v_cvt_pk_bf16_f32 v217, v218, v219\n"
    "v_mfma_f32_16x16x32_bf16 v[200:203], v[12:15], v[156:159], v[200:203]\n"
    "v_cvt_pk_bf16_f32 v218, v220, v221\n"
    "v_cvt_pk_bf16_f32 v219, v222, v223\n"
    "v_mfma_f32_16x16x32_bf16 v[204:207], v[36:39], v[156:159], v[204:207]\n"
    "ds_write_b128 v7, v[216:219] offset:14336\n"
    "v_mfma_f32_16x16x32_bf16 v[200:203], v[16:19], v[160:163], v[200:203]\n"
    "ds_write_b32 v225, v227 offset:16\n"
    "ds_read_b32 v228, v224 offset:208\n"
    "v_mfma_f32_16x16x32_bf16 v[204:207], v[40:43], v[160:163], v[204:207]\n"
    "s_waitcnt vmcnt(0)\n"
    "v_mov_b32 v227, 10\n"
    "v_mfma_f32_16x16x32_bf16 v[200:203], v[20:23], v[164:167], v[200:203]\n"
    "ds_write_b32 v226, v227 offset:0\n"
    "v_mfma_f32_16x16x32_bf16 v[204:207], v[44:47], v[164:167], v[204:207]\n"
    "v_mfma_f32_16x16x32_bf16 v[200:203], v[24:27], v[168:171], v[200:203]\n"
    "v_mfma_f32_16x16x32_bf16 v[204:207], v[48:51], v[168:171], v[204:207]\n"
    "v_mfma_f32_16x16x32_bf16 v[200:203], v[28:31], v[172:175], v[200:203]\n"
    "v_mfma_f32_16x16x32_bf16 v[204:207], v[52:55], v[172:175], v[204:207]\n"
    "v_mfma_f32_16x16x32_bf16 v[208:211], v[56:59], v[152:155], 0\n"
    "s_waitcnt lgkmcnt(1)\n"
    "v_cmp_gt_u32 vcc, 3, v228\n"
    "v_mfma_f32_16x16x32_bf16 v[212:215], v[80:83], v[152:155], 0\n"
    "v_mfma_f32_16x16x32_bf16 v[208:211], v[60:63], v[156:159], v[208:211]\n"
    "s_cbranch_vccnz .Lg26\n"
    ".Lg26b:\n"
    "v_mfma_f32_16x16x32_bf16 v[212:215], v[84:87], v[156:159], v[212:215]\n"
    "v_cvt_pk_bf16_f32 v200, v200, v201\n"
    "v_cvt_pk_bf16_f32 v201, v202, v203\n"
    "v_mfma_f32_16x16x32_bf16 v[208:211], v[64:67], v[160:163], v[208:211]\n"
    "v_cvt_pk_bf16_f32 v202, v204, v205\n"
    "v_cvt_pk_bf16_f32 v203, v206, v207\n"
    "v_mfma_f32_16x16x32_bf16 v[212:215], v[88:91], v[160:163], v[212:215]\n"
    "ds_write_b128 v7, v[200:203] offset:0\n"
    "v_mfma_f32_16x16x32_bf16 v[208:211], v[68:71], v[164:167], v[208:211]\n"
    "v_mfma_f32_16x16x32_bf16 v[212:215], v[92:95], v[164:167], v[212:215]\n"
    "v_mfma_f32_16x16x32_bf16 v[208:211], v[72:75], v[168:171], v[208:211]\n"
    "v_mfma_f32_16x16x32_bf16 v[212:215], v[96:99], v[168:171], v[212:215]\n"
    "v_mfma_f32_16x16x32_bf16 v[208:211], v[76:79], v[172:175], v[208:211]\n"
    "v_mfma_f32_16x16x32_bf16 v[212:215], v[100:103], v[172:175], v[212:215]\n"
    "v_mfma_f32_16x16x32_bf16 v[216:219], v[104:107], v[152:155], 0\n"
    "v_mfma_f32_16x16x32_bf16 v[220:223], v[128:131], v[152:155], 0\n"
    "v_mfma_f32_16x16x32_bf16 v[216:219], v[108:111], v[156:159], v[216:219]\n"
    "v_mfma_f32_16x16x32_bf16 v[220:223], v[132:135], v[156:159], v[220:223]\n"
    "v_mfma_f32_16x16x32_bf16 v[216:219], v[112:115], v[160:163], v[216:219]\n"
    "v_mfma_f32_16x16x32_bf16 v[220:223], v[136:139], v[160:163], v[220:223]\n"
    "v_mfma_f32_16x16x32_bf16 v[216:219], v[116:119], v[164:167], v[216:219]\n"
    "v_cvt_pk_bf16_f32 v208, v208, v209\n"
    "v_cvt_pk_bf16_f32 v209, v210, v211\n"
    "v_mfma_f32_16x16x32_bf16 v[220:223], v[140:143], v[164:167], v[220:223]\n"
    "v_cvt_pk_bf16_f32 v210, v212, v213\n"
    "v_cvt_pk_bf16_f32 v211, v214, v215\n"
    "v_mfma_f32_16x16x32_bf16 v[216:219], v[120:123], v[168:171], v[216:219]\n"
    "ds_write_b128 v7, v[208:211] offset:1024\n"
    "v_mfma_f32_16x16x32_bf16 v[220:223], v[144:147], v[168:171], v[220:223]\n"
    "v_mfma_f32_16x16x32_bf16 v[216:219], v[124:127], v[172:175], v[216:219]\n"
    "v_mfma_f32_16x16x32_bf16 v[220:223], v[148:151], v[172:175], v[220:223]\n"
    "s_nop 6\n"
    "v_cvt_pk_bf16_f32 v216, v216, v217\n"
    "v_cvt_pk_bf16_f32 v217, v218, v219\n"
    "v_cvt_pk_bf16_f32 v218, v220, v221\n"
    "v_cvt_pk_bf16_f32 v219, v222, v223\n"
    "ds_write_b128 v7, v[216:219] offset:2048\n"
    "v_mov_b32 v227, 13\n"
    "ds_write_b32 v225, v227 offset:16\n"
    "s_setprio 0\n"
    "s_branch .Lg_att\n"
    ".Lg27:\n"
    "s_sub_u32 s52, s52, 1\n"
    "s_cmp_lt_i32 s52, 0\n"
    "s_cbranch_scc1 .Lg27b\n"
    "s_sleep 1\n"
    "ds_read_b32 v228, v224 offset:208\n"
    "s_waitcnt lgkmcnt(0)\n"
    "v_cmp_gt_u32 vcc, 3, v228\n"
    "s_cbranch_vccnz .Lg27\n"
    "s_branch .Lg27b\n"
    ".Lg_att:\n"
    "s_waitcnt vmcnt(0)\n"
    "v_mov_b32 v8, 0xff800000\n"
    "v_add_u32 v3, 124544, v235\n"
    "v_mov_b32 v6, v224\n"
    "v_lshrrev_b32 v9, 6, v235\n"
    "v_and_b32 v9, 12, v9\n"
    "v_lshlrev_b32 v9, 4, v9\n"
    "v_xor_b32 v4, v9, v235\n"
    "v_add_u32 v4, 124544, v4\n"
    "v_cmp_gt_u32 vcc, 64, v235\n"
    "v_add_u32 v7, 704, v224\n"
    "v_mov_b32 v9, 0x100000\n"
    "v_mov_b32 v0, v248\n"
    "v_cndmask_b32 v7, v9, v7, vcc\n"
    "v_mov_b32 v1, v249\n"
    "v_mov_b32 v2, v250\n"
    "v_and_b32 v5, 15, v251\n"
    "v_mul_u32_u24 v5, 0x1111, v5\n"
    "v_bfe_i32 v10, v251, 8, 1\n"
    "v_bfe_i32 v9, v251, 4, 1\n"
    "v_bfi_b32 v232, v9, 0, v8\n"
    "v_bfi_b32 v236, v10, v8, v232\n"
    "v_bfi_b32 v240, v10, v232, v8\n"
    "v_bfe_i32 v9, v251, 5, 1\n"
    "v_bfi_b32 v233, v9, 0, v8\n"
    "v_bfi_b32 v237, v10, v8, v233\n"
    "v_bfi_b32 v241, v10, v233, v8\n"
    "v_bfe_i32 v9, v251, 6, 1\n"
    "v_bfi_b32 v234, v9, 0, v8\n"
    "v_bfi_b32 v238, v10, v8, v234\n"
    "v_bfi_b32 v242, v10, v234, v8\n"
    "v_bfe_i32 v9, v251, 7, 1\n"
    "v_bfi_b32 v235, v9, 0, v8\n"
    "v_bfi_b32 v239, v10, v8, v235\n"
    "v_bfi_b32 v243, v10, v235, v8\n"
    "s_mov_b32 s45, 12\n"
    "s_mov_b32 s54, 10\n"
    "s_movk_i32 s60, 0x420c\n"
    "s_mov_b32 s61, 0\n"
    "s_mov_b32 s46, 0xbdd53b94\n"
    "s_mov_b32 s47, 0x3dd53b94\n"
    "s_mov_b32 s63, 161920\n"
    "s_mov_b32 s66, 0\n"
    "s_mov_b32 s56, -16\n"
    "s_branch .La_join\n"
    ".Lg28:\n"
    "s_sub_u32 s52, s52, 1\n"
    "s_cmp_lt_i32 s52, 0\n"
    "s_cbranch_scc1 .Lg28b\n"
    "s_sleep 1\n"
    "ds_read_b32 v228, v224 offset:128\n"
    "s_waitcnt lgkmcnt(0)\n"
    "v_cmp_gt_u32 vcc, 3, v228\n"
    "s_cbranch_vccnz .Lg28\n"
    "s_branch .Lg28b\n"
    ".Lg29:\n"
    "v_mfma_f32_16x16x32_bf16 v[208:211], v[56:59], v[152:155], 0\n"
    "v_mfma_f32_16x16x32_bf16 v[212:215], v[80:83], v[152:155], 0\n"
    "v_mfma_f32_16x16x32_bf16 v[208:211], v[60:63], v[156:159], v[208:211]\n"
    "v_mfma_f32_16x16x32_bf16 v[212:215], v[84:87], v[156:159], v[212:215]\n"
    "v_mul_f32 v200, v200, v252\n"
    "v_mul_f32 v201, v201, v252\n"
    "v_mfma_f32_16x16x32_bf16 v[208:211], v[64:67], v[160:163], v[208:211]\n"
    "v_mul_f32 v202, v202, v252\n"
    "v_mul_f32 v203, v203, v252\n"
    "v_mfma_f32_16x16x32_bf16 v[212:215], v[88:91], v[160:163], v[212:215]\n"
    "v_mul_f32 v204, v204, v252\n"
    "v_mul_f32 v205, v205, v252\n"
    "v_mfma_f32_16x16x32_bf16 v[208:211], v[68:71], v[164:167], v[208:211]\n"
    "v_mul_f32 v206, v206, v252\n"
    "v_mul_f32 v207, v207, v252\n"
    "v_mfma_f32_16x16x32_bf16 v[212:215], v[92:95], v[164:167], v[212:215]\n"
    "v_cvt_pk_bf16_f32 v200, v200, v201\n"
    "v_cvt_pk_bf16_f32 v201, v202, v203\n"
    "v_mfma_f32_16x16x32_bf16 v[208:211], v[72:75], v[168:171], v[208:211]\n"
    "v_cvt_pk_bf16_f32 v202, v204, v205\n"
    "v_cvt_pk_bf16_f32 v203, v206, v207\n"
    "v_mfma_f32_16x16x32_bf16 v[212:215], v[96:99], v[168:171], v[212:215]\n"
    "buffer_store_dwordx4 v[200:203], v248, s[80:83], 0 offen offset:128 sc1\n"
    "v_mfma_f32_16x16x32_bf16 v[208:211], v[76:79], v[172:175], v[208:211]\n"
    "v_mfma_f32_16x16x32_bf16 v[212:215], v[100:103], v[172:175], v[212:215]\n"
    ".Lg29r:\n"
    "s_sub_u32 s52, s52, 1\n"
    "s_cmp_lt_i32 s52, 0\n"
    "s_cbranch_scc1 .Lg29g\n"
    "s_sleep 1\n"
    "ds_read_b32 v228, v224 offset:144\n"
    "s_waitcnt lgkmcnt(0)\n"
    "v_cmp_gt_u32 vcc, 3, v228\n"
    "s_cbranch_vccnz .Lg29r\n"
    "s_branch .Lg29g\n"
    ".Lg29g:\n"
    "ds_read_b64_tr_b16 v[176:177], v0 offset:18432\n"
    "ds_read_b64_tr_b16 v[178:179], v1 offset:18432\n"
    "ds_read_b64_tr_b16 v[180:181], v0 offset:19456\n"
    "ds_read_b64_tr_b16 v[182:183], v1 offset:19456\n"
    "ds_read_b64_tr_b16 v[184:185], v0 offset:20480\n"
    "ds_read_b64_tr_b16 v[186:187], v1 offset:20480\n"
    "ds_read_b64_tr_b16 v[188:189], v0 offset:21504\n"
    "ds_read_b64_tr_b16 v[190:191], v1 offset:21504\n"
    "ds_read_b64_tr_b16 v[192:193], v0 offset:22528\n"
    "ds_read_b64_tr_b16 v[194:195], v1 offset:22528\n"
    "ds_read_b64_tr_b16 v[196:197], v0 offset:23552\n"
    "ds_read_b64_tr_b16 v[198:199], v1 offset:23552\n"
    "ds_read_b32 v253, v238 offset:0\n"
    "s_branch .Lg29j\n"
    ".Lg30:\n"
    "v_mfma_f32_16x16x32_bf16 v[208:211], v[56:59], v[176:179], 0\n"
    "v_mfma_f32_16x16x32_bf16 v[212:215], v[80:83], v[176:179], 0\n"
    "v_mfma_f32_16x16x32_bf16 v[208:211], v[60:63], v[180:183], v[208:211]\n"
    "v_mfma_f32_16x16x32_bf16 v[212:215], v[84:87], v[180:183], v[212:215]\n"
    "v_mul_f32 v200, v200, v253\n"
    "v_mul_f32 v201, v201, v253\n"
    "v_mfma_f32_16x16x32_bf16 v[208:211], v[64:67], v[184:187], v[208:211]\n"
    "v_mul_f32 v202, v202, v253\n"
    "v_mul_f32 v203, v203, v253\n"
    "v_mfma_f32_16x16x32_bf16 v[212:215], v[88:91], v[184:187], v[212:215]\n"
    "v_mul_f32 v204, v204, v253\n"
    "v_mul_f32 v205, v205, v253\n"
    "v_mfma_f32_16x16x32_bf16 v[208:211], v[68:71], v[188:191], v[208:211]\n"
    "v_mul_f32 v206, v206, v253\n"
    "v_mul_f32 v207, v207, v253\n"
    "v_mfma_f32_16x16x32_bf16 v[212:215], v[92:95], v[188:191], v[212:215]\n"
    "v_cvt_pk_bf16_f32 v200, v200, v201\n"
    "v_cvt_pk_bf16_f32 v201, v202, v203\n"
    "v_mfma_f32_16x16x32_bf16 v[208:211], v[72:75], v[192:195], v[208:211]\n"
    "v_cvt_pk_bf16_f32 v202, v204, v205\n"
    "v_cvt_pk_bf16_f32 v203, v206, v207\n"
    "v_mfma_f32_16x16x32_bf16 v[212:215], v[96:99], v[192:195], v[212:215]\n"
    "buffer_store_dwordx4 v[200:203], v249, s[80:83], 0 offen offset:128 sc1\n"
    "v_mfma_f32_16x16x32_bf16 v[208:211], v[76:79], v[196:199], v[208:211]\n"
    "v_mfma_f32_16x16x32_bf16 v[212:215], v[100:103], v[196:199], v[212:215]\n"
    ".Lg30r:\n"
    "s_sub_u32 s52, s52, 1\n"
    "s_cmp_lt_i32 s52, 0\n"
    "s_cbranch_scc1 .Lg30g\n"
    "s_sleep 1\n"
    "ds_read_b32 v228, v224 offset:160\n"
    "s_waitcnt lgkmcnt(0)\n"
    "v_cmp_gt_u32 vcc, 3, v228\n"
    "s_cbranch_vccnz .Lg30r\n"
    "s_branch .Lg30g\n"
    ".Lg30g:\n"
    "ds_read_b64_tr_b16 v[152:153], v0 offset:6144\n"
    "ds_read_b64_tr_b16 v[154:155], v1 offset:6144\n"
    "ds_read_b64_tr_b16 v[156:157], v0 offset:7168\n"
    "ds_read_b64_tr_b16 v[158:159], v1 offset:7168\n"
    "ds_read_b64_tr_b16 v[160:161], v0 offset:8192\n"
    "ds_read_b64_tr_b16 v[162:163], v1 offset:8192\n"
    "ds_read_b64_tr_b16 v[164:165], v0 offset:9216\n"
    "ds_read_b64_tr_b16 v[166:167], v1 offset:9216\n"
    "ds_read_b64_tr_b16 v[168:169], v0 offset:10240\n"
    "ds_read_b64_tr_b16 v[170:171], v1 offset:10240\n"
    "ds_read_b64_tr_b16 v[172:173], v0 offset:11264\n"
    "ds_read_b64_tr_b16 v[174:175], v1 offset:11264\n"
    "ds_read_b32 v252, v238 offset:256\n"
    "s_branch .Lg30j\n"
    ".Lg31:\n"
    "v_mfma_f32_16x16x32_bf16 v[208:211], v[56:59], v[152:155], 0\n"
    "v_mfma_f32_16x16x32_bf16 v[212:215], v[80:83], v[152:155], 0\n"
    "v_mfma_f32_16x16x32_bf16 v[208:211], v[60:63], v[156:159], v[208:211]\n"
    "v_mfma_f32_16x16x32_bf16 v[212:215], v[84:87], v[156:159], v[212:215]\n"
    "v_mul_f32 v200, v200, v252\n"
    "v_mul_f32 v201, v201, v252\n"
    "v_mfma_f32_16x16x32_bf16 v[208:211], v[64:67], v[160:163], v[208:211]\n"
    "v_mul_f32 v202, v202, v252\n"
    "v_mul_f32 v203, v203, v252\n"
    "v_mfma_f32_16x16x32_bf16 v[212:215], v[88:91], v[160:163], v[212:215]\n"
    "v_mul_f32 v204, v204, v252\n"
    "v_mul_f32 v205, v205, v252\n"
    "v_mfma_f32_16x16x32_bf16 v[208:211], v[68:71], v[164:167], v[208:211]\n"
    "v_mul_f32 v206, v206, v252\n"
    "v_mul_f32 v207, v207, v252\n"
    "v_mfma_f32_16x16x32_bf16 v[212:215], v[92:95], v[164:167], v[212:215]\n"
    "v_cvt_pk_bf16_f32 v200, v200, v201\n"
    "v_cvt_pk_bf16_f32 v201, v202, v203\n"
    "v_mfma_f32_16x16x32_bf16 v[208:211], v[72:75], v[168:171], v[208:211]\n"
    "v_cvt_pk_bf16_f32 v202, v204, v205\n"
    "v_cvt_pk_bf16_f32 v203, v206, v207\n"
    "v_mfma_f32_16x16x32_bf16 v[212:215], v[96:99], v[168:171], v[212:215]\n"
    "buffer_store_dwordx4 v[200:203], v248, s[80:83], 0 offen offset:128 sc1\n"
    "v_mfma_f32_16x16x32_bf16 v[208:211], v[76:79], v[172:175], v[208:211]\n"
    "v_mfma_f32_16x16x32_bf16 v[212:215], v[100:103], v[172:175], v[212:215]\n"
    ".Lg31r:\n"
    "s_sub_u32 s52, s52, 1\n"
    "s_cmp_lt_i32 s52, 0\n"
    "s_cbranch_scc1 .Lg31g\n"
    "s_sleep 1\n"
    "ds_read_b32 v228, v224 offset:176\n"
    "s_waitcnt lgkmcnt(0)\n"
    "v_cmp_gt_u32 vcc, 3, v228\n"
    "s_cbranch_vccnz .Lg31r\n"
    "s_branch .Lg31g\n"
    ".Lg31g:\n"
    "ds_read_b64_tr_b16 v[176:177], v0 offset:12288\n"
    "ds_read_b64_tr_b16 v[178:179], v1 offset:12288\n"
    "ds_read_b64_tr_b16 v[180:181], v0 offset:13312\n"
    "ds_read_b64_tr_b16 v[182:183], v1 offset:13312\n"
    "ds_read_b64_tr_b16 v[184:185], v0 offset:14336\n"
    "ds_read_b64_tr_b16 v[186:187], v1 offset:14336\n"
    "ds_read_b64_tr_b16 v[188:189], v0 offset:15360\n"
    "ds_read_b64_tr_b16 v[190:191], v1 offset:15360\n"
    "ds_read_b64_tr_b16 v[192:193], v0 offset:16384\n"
    "ds_read_b64_tr_b16 v[194:195], v1 offset:16384\n"
    "ds_read_b64_tr_b16 v[196:197], v0 offset:17408\n"
    "ds_read_b64_tr_b16 v[198:199], v1 offset:17408\n"
    "ds_read_b32 v253, v238 offset:320\n"
    "s_branch .Lg31j\n"
    ".Lg_t12:\n"
    "s_waitcnt lgkmcnt(1)\n"
    "v_mfma_f32_16x16x32_bf16 v[200:203], v[8:11], v[176:179], 0\n"
    "v_mul_f32 v208, v208, v252\n"
    "v_mul_f32 v209, v209, v252\n"
    "v_mfma_f32_16x16x32_bf16 v[204:207], v[32:35], v[176:179], 0\n"
    "v_mul_f32 v210, v210, v252\n"
    "v_mul_f32 v211, v211, v252\n"
    "v_mfma_f32_16x16x32_bf16 v[200:203], v[12:15], v[180:183], v[200:203]\n"
    "v_mul_f32 v212, v212, v252\n"
    "v_mul_f32 v213, v213, v252\n"
    "v_mfma_f32_16x16x32_bf16 v[204:207], v[36:39], v[180:183], v[204:207]\n"
    "v_mul_f32 v214, v214, v252\n"
    "v_mul_f32 v215, v215, v252\n"
    "v_mfma_f32_16x16x32_bf16 v[200:203], v[16:19], v[184:187], v[200:203]\n"
    "v_cvt_pk_bf16_f32 v208, v208, v209\n"
    "v_cvt_pk_bf16_f32 v209, v210, v211\n"
    "v_mfma_f32_16x16x32_bf16 v[204:207], v[40:43], v[184:187], v[204:207]\n"
    "v_cvt_pk_bf16_f32 v210, v212, v213\n"
    "v_cvt_pk_bf16_f32 v211, v214, v215\n"
    "v_mfma_f32_16x16x32_bf16 v[200:203], v[20:23], v[188:191], v[200:203]\n"
    "buffer_store_dwordx4 v[208:211], v248, s[80:83], 0 offen offset:320 sc1\n"
    "v_mfma_f32_16x16x32_bf16 v[204:207], v[44:47], v[188:191], v[204:207]\n"
    "v_mfma_f32_16x16x32_bf16 v[200:203], v[24:27], v[192:195], v[200:203]\n"
    "v_add_u32 v248, v255, v248\n"
    "v_mfma_f32_16x16x32_bf16 v[204:207], v[48:51], v[192:195], v[204:207]\n"
    "v_mfma_f32_16x16x32_bf16 v[200:203], v[28:31], v[196:199], v[200:203]\n"
    "v_mfma_f32_16x16x32_bf16 v[204:207], v[52:55], v[196:199], v[204:207]\n"
    "v_mfma_f32_16x16x32_bf16 v[208:211], v[56:59], v[176:179], 0\n"
    "v_mfma_f32_16x16x32_bf16 v[212:215], v[80:83], v[176:179], 0\n"
    "v_mfma_f32_16x16x32_bf16 v[208:211], v[60:63], v[180:183], v[208:211]\n"
    "v_mfma_f32_16x16x32_bf16 v[212:215], v[84:87], v[180:183], v[212:215]\n"
    "v_mfma_f32_16x16x32_bf16 v[208:211], v[64:67], v[184:187], v[208:211]\n"
    "v_mfma_f32_16x16x32_bf16 v[212:215], v[88:91], v[184:187], v[212:215]\n"
    "s_waitcnt lgkmcnt(0)\n"
    "v_mul_f32 v200, v200, v253\n"
    "v_mfma_f32_16x16x32_bf16 v[208:211], v[68:71], v[188:191], v[208:211]\n"
    "v_mul_f32 v201, v201, v253\n"
    "v_mul_f32 v202, v202, v253\n"
    "v_mfma_f32_16x16x32_bf16 v[212:215], v[92:95], v[188:191], v[212:215]\n"
    "v_mul_f32 v203, v203, v253\n"
    "v_mul_f32 v204, v204, v253\n"
    "v_mfma_f32_16x16x32_bf16 v[208:211], v[72:75], v[192:195], v[208:211]\n"
    "v_mul_f32 v205, v205, v253\n"
    "v_mul_f32 v206, v206, v253\n"
    "v_mfma_f32_16x16x32_bf16 v[212:215], v[96:99], v[192:195], v[212:215]\n"
    "v_mul_f32 v207, v207, v253\n"
    "v_cvt_pk_bf16_f32 v200, v200, v201\n"
    "v_mfma_f32_16x16x32_bf16 v[208:211], v[76:79], v[196:199], v[208:211]\n"
    "v_cvt_pk_bf16_f32 v201, v202, v203\n"
    "v_cvt_pk_bf16_f32 v202, v204, v205\n"
    "v_mfma_f32_16x16x32_bf16 v[212:215], v[100:103], v[196:199], v[212:215]\n"
    "v_cvt_pk_bf16_f32 v203, v206, v207\n"
    "buffer_store_dwordx4 v[200:203], v249, s[80:83], 0 offen offset:128 sc1\n"
    "s_nop 2\n"
    "v_mul_f32 v208, v208, v253\n"
    "v_mul_f32 v209, v209, v253\n"
    "v_mul_f32 v210, v210, v253\n"
    "v_mul_f32 v211, v211, v253\n"
    "v_mul_f32 v212, v212, v253\n"
    "v_mul_f32 v213, v213, v253\n"
    "v_mul_f32 v214, v214, v253\n"
    "v_mul_f32 v215, v215, v253\n"
    "v_cvt_pk_bf16_f32 v208, v208, v209\n"
    "v_cvt_pk_bf16_f32 v209, v210, v211\n"
    "v_cvt_pk_bf16_f32 v210, v212, v213\n"
    "v_cvt_pk_bf16_f32 v211, v214, v215\n"
    "buffer_store_dwordx4 v[208:211], v249, s[80:83], 0 offen offset:320 sc1\n"
    "s_branch .Lg_end\n"
    ".Lg32:\n"
    "v_mfma_f32_16x16x32_bf16 v[208:211], v[56:59], v[176:179], 0\n"
    "v_mfma_f32_16x16x32_bf16 v[212:215], v[80:83], v[176:179], 0\n"
    "v_mfma_f32_16x16x32_bf16 v[208:211], v[60:63], v[180:183], v[208:211]\n"
    "v_mfma_f32_16x16x32_bf16 v[212:215], v[84:87], v[180:183], v[212:215]\n"
    "v_mul_f32 v200, v200, v253\n"
    "v_mul_f32 v201, v201, v253\n"
    "v_mfma_f32_16x16x32_bf16 v[208:211], v[64:67], v[184:187], v[208:211]\n"
    "v_mul_f32 v202, v202, v253\n"
    "v_mul_f32 v203, v203, v253\n"
    "v_mfma_f32_16x16x32_bf16 v[212:215], v[88:91], v[184:187], v[212:215]\n"
    "v_mul_f32 v204, v204, v253\n"
    "v_mul_f32 v205, v205, v253\n"
    "v_mfma_f32_16x16x32_bf16 v[208:211], v[68:71], v[188:191], v[208:211]\n"
    "v_mul_f32 v206, v206, v253\n"
    "v_mul_f32 v207, v207, v253\n"
    "v_mfma_f32_16x16x32_bf16 v[212:215], v[92:95], v[188:191], v[212:215]\n"
    "v_cvt_pk_bf16_f32 v200, v200, v201\n"
    "v_cvt_pk_bf16_f32 v201, v202, v203\n"
    "v_mfma_f32_16x16x32_bf16 v[208:211], v[72:75], v[192:195], v[208:211]\n"
    "v_cvt_pk_bf16_f32 v202, v204, v205\n"
    "v_cvt_pk_bf16_f32 v203, v206, v207\n"
    "v_mfma_f32_16x16x32_bf16 v[212:215], v[96:99], v[192:195], v[212:215]\n"
    "buffer_store_dwordx4 v[200:203], v249, s[80:83], 0 offen offset:128 sc1\n"
    "v_mfma_f32_16x16x32_bf16 v[208:211], v[76:79], v[196:199], v[208:211]\n"
    "v_mfma_f32_16x16x32_bf16 v[212:215], v[100:103], v[196:199], v[212:215]\n"
    ".Lg32r:\n"
    "s_sub_u32 s52, s52, 1\n"
    "s_cmp_lt_i32 s52, 0\n"
    "s_cbranch_scc1 .Lg32g\n"
    "s_sleep 1\n"
    "ds_read_b32 v228, v224 offset:832\n"
    "s_waitcnt lgkmcnt(0)\n"
    "v_cmp_gt_u32 vcc, 4, v228\n"
    "s_cbranch_vccnz .Lg32r\n"
    "s_branch .Lg32g\n"
    ".Lg32g:\n"
    "ds_read_b64_tr_b16 v[152:153], v0 offset:0\n"
    "ds_read_b64_tr_b16 v[154:155], v1 offset:0\n"
    "ds_read_b64_tr_b16 v[156:157], v0 offset:1024\n"
    "ds_read_b64_tr_b16 v[158:159], v1 offset:1024\n"
    "ds_read_b64_tr_b16 v[160:161], v0 offset:2048\n"
    "ds_read_b64_tr_b16 v[162:163], v1 offset:2048\n"
    "ds_read_b64_tr_b16 v[164:165], v0 offset:3072\n"
    "ds_read_b64_tr_b16 v[166:167], v1 offset:3072\n"
    "ds_read_b64_tr_b16 v[168:169], v0 offset:4096\n"
    "ds_read_b64_tr_b16 v[170:171], v1 offset:4096\n"
    "ds_read_b64_tr_b16 v[172:173], v0 offset:5120\n"
    "ds_read_b64_tr_b16 v[174:175], v1 offset:5120\n"
    "ds_read_b32 v252, v238 offset:192\n"
    "s_branch .Lg32j\n"
    ".Lg_last:\n"
                :: "{v[0:1]}"(vz), "{v238}"(vi), "{v[248:251]}"(to), "{s[80:83]}"(srd), "{v[2:3]}"(va), "{v[6:7]}"(vb), "{v[224:227]}"(fl), "{v[230:233]}"(xo), "{v234}"(xo4), "{v235}"(lf[0]), "{v236}"(lf[1]),
                   "{s[40:41]}"(ws), "{s[42:43]}"(xb), "{s44}"(UNI(axis_at(rm, 14) * IROWB)), "{s45}"(UNI(axis_at(rm, 15) * IROWB)),
                   "{s46}"(UNI(axis_at(rm, 16) * IROWB)), "{s47}"(UNI(axis_at(rm, 17) * IROWB)), "{s48}"(UNI(axis_at(rm, t12 ? 4 : 18) * IROWB)),
                   "{s49}"(UNI(axis_at(rm, t12 ? 5 : 19) * IROWB)), "{s84}"(UNI(3200 * hs)), "{s85}"(UNI(2 * rm.d * IROWB)), "{v255}"(d56), "{s50}"(h), "{s58}"(UNI(t12)), "{s59}"(UNI(t0 + (bid & 4 ? 5000 : 5300)))
                : "memory", "v253", "s51", "s52", "s53", "s54", "s55", "s56", "s57", "s60", "s61", "s79", "s90", "s91", "s92", "s93", "s94", "s95", "vcc", "scc");
        } else {
            const unsigned r3 = axis_at(rm, 3 + e) * IROWB + 192 * h;
            const u32x4 vo = {r3 + c0, r3 + c1, dd[0], dd[1]};
            u32x2 xp;
#pragma unroll
            for (int k = 0; k < 2; ++k) {
                const int i = 72 * h + min(lane + 64 * k, 71), r = i / 48, c = (i - 48 * r) / 3;
                xp[k] = axis_at(rm, 14 + r) * IROWB + axis_at(cm, 3 + c) * PIXB + 128 * (i - 48 * r - 3 * c);
            }
            u32x2 lf = {l16, (unsigned)(WS_FL + ((bid & (NREP - 1)) * 144 + 72 + nf) * 128)};
            asm volatile("s_nop 7\n"
    "s_mov_b32 s52, 0x40000\n"
    "s_load_dwordx2 s[78:79], s[40:41], 0x0\n"
    "s_mul_i32 s53, s50, 36864\n"
    "s_add_u32 s57, s53, 84992\n"
    "s_add_u32 s64, s40, s57\n"
    "s_addc_u32 s65, s41, 0\n"
    "s_add_u32 s57, s53, 91136\n"
    "s_add_u32 s66, s40, s57\n"
    "s_addc_u32 s67, s41, 0\n"
    "s_add_u32 s57, s53, 97280\n"
    "s_add_u32 s68, s40, s57\n"
    "s_addc_u32 s69, s41, 0\n"
    "s_add_u32 s57, s53, 103424\n"
    "s_add_u32 s70, s40, s57\n"
    "s_addc_u32 s71, s41, 0\n"
    "s_add_u32 s57, s53, 109568\n"
    "s_add_u32 s72, s40, s57\n"
    "s_addc_u32 s73, s41, 0\n"
    "s_add_u32 s57, s53, 115712\n"
    "s_add_u32 s74, s40, s57\n"
    "s_addc_u32 s75, s41, 0\n"
    "s_cmp_eq_u32 s50, 1\n"
    "s_cbranch_scc1 .Lh_s1\n"
    "ds_read_b32 v228, v224 offset:256\n"
    "s_waitcnt lgkmcnt(0)\n"
    "v_cmp_gt_u32 vcc, 1, v228\n"
    "s_cbranch_vccnz .Lh2\n"
    ".Lh2b:\n"
    "s_mul_i32 s56, s45, 10\n"
    "s_add_u32 s56, s56, s44\n"
    "s_add_u32 s60, s42, s56\n"
    "s_addc_u32 s61, s43, 0\n"
    "s_mov_b32 m0, 89984\n"
    "s_nop 0\n"
    "global_load_lds_dwordx4 v230, s[60:61]\n"
    "s_mov_b32 m0, 92032\n"
    "s_nop 0\n"
    "global_load_lds_dwordx4 v231, s[60:61]\n"
    "s_mov_b32 m0, 94080\n"
    "s_nop 0\n"
    "global_load_lds_dwordx4 v232, s[60:61]\n"
    "s_mov_b32 m0, 96128\n"
    "s_nop 0\n"
    "global_load_lds_dwordx4 v233, s[60:61]\n"
    "s_mov_b32 m0, 88960\n"
    "s_nop 0\n"
    "global_load_lds_dwordx4 v234, s[60:61]\n"
    "s_mul_i32 s56, s45, 11\n"
    "s_add_u32 s56, s56, s44\n"
    "s_add_u32 s60, s42, s56\n"
    "s_addc_u32 s61, s43, 0\n"
    "s_mov_b32 m0, 98880\n"
    "s_nop 0\n"
    "global_load_lds_dwordx4 v230, s[60:61]\n"
    "s_mov_b32 m0, 100928\n"
    "s_nop 0\n"
    "global_load_lds_dwordx4 v231, s[60:61]\n"
    "s_mov_b32 m0, 102976\n"
    "s_nop 0\n"
    "global_load_lds_dwordx4 v232, s[60:61]\n"
    "s_mov_b32 m0, 105024\n"
    "s_nop 0\n"
    "global_load_lds_dwordx4 v233, s[60:61]\n"
    "s_mov_b32 m0, 97856\n"
    "s_nop 0\n"
    "global_load_lds_dwordx4 v234, s[60:61]\n"
    "s_waitcnt lgkmcnt(0)\n"
    "s_add_u32 s78, s78, 0x7f4a7c15\n"
    "s_add_u32 s79, s79, 0x9e3779b9\n"
    "v_mov_b64 v[240:241], s[78:79]\n"
    "s_mov_b32 s51, 0x4000\n"
    "v_mov_b64 v[244:245], v[240:241]\n"
    "v_mov_b64 v[246:247], 0\n"
    "v_mov_b64 v[242:243], 0\n"
    "s_mov_b32 exec_hi, 15\n"
    "global_load_dwordx4 v[244:247], v236, s[40:41] sc1\n"
    "s_mov_b32 exec_hi, -1\n"
    "global_load_dwordx4 v[8:11], v235, s[64:65] offset:-3072 sc1\n"
    "global_load_dwordx4 v[32:35], v235, s[66:67] offset:-3072 sc1\n"
    "global_load_dwordx4 v[12:15], v235, s[64:65] offset:-2048 sc1\n"
    "global_load_dwordx4 v[36:39], v235, s[66:67] offset:-2048 sc1\n"
    "global_load_dwordx4 v[16:19], v235, s[64:65] offset:-1024 sc1\n"
    "global_load_dwordx4 v[40:43], v235, s[66:67] offset:-1024 sc1\n"
    "global_load_dwordx4 v[20:23], v235, s[64:65] offset:0 sc1\n"
    "global_load_dwordx4 v[44:47], v235, s[66:67] offset:0 sc1\n"
    "global_load_dwordx4 v[24:27], v235, s[64:65] offset:1024 sc1\n"
    "global_load_dwordx4 v[48:51], v235, s[66:67] offset:1024 sc1\n"
    "global_load_dwordx4 v[28:31], v235, s[64:65] offset:2048 sc1\n"
    "global_load_dwordx4 v[52:55], v235, s[66:67] offset:2048 sc1\n"
    "s_waitcnt vmcnt(13)\n"
    "v_mov_b32 v227, 6\n"
    "ds_write_b32 v226, v227 offset:0\n"
    "global_load_dwordx4 v[56:59], v235, s[68:69] offset:-3072 sc1\n"
    "global_load_dwordx4 v[80:83], v235, s[70:71] offset:-3072 sc1\n"
    "global_load_dwordx4 v[60:63], v235, s[68:69] offset:-2048 sc1\n"
    "global_load_dwordx4 v[84:87], v235, s[70:71] offset:-2048 sc1\n"
    "global_load_dwordx4 v[64:67], v235, s[68:69] offset:-1024 sc1\n"
    "global_load_dwordx4 v[88:91], v235, s[70:71] offset:-1024 sc1\n"
    "global_load_dwordx4 v[68:71], v235, s[68:69] offset:0 sc1\n"
    "global_load_dwordx4 v[92:95], v235, s[70:71] offset:0 sc1\n"
    "global_load_dwordx4 v[72:75], v235, s[68:69] offset:1024 sc1\n"
    "global_load_dwordx4 v[96:99], v235, s[70:71] offset:1024 sc1\n"
    "global_load_dwordx4 v[76:79], v235, s[68:69] offset:2048 sc1\n"
    "global_load_dwordx4 v[100:103], v235, s[70:71] offset:2048 sc1\n"
    "global_load_dwordx4 v[104:107], v235, s[72:73] offset:-3072 sc1\n"
    "global_load_dwordx4 v[128:131], v235, s[74:75] offset:-3072 sc1\n"
    "global_load_dwordx4 v[108:111], v235, s[72:73] offset:-2048 sc1\n"
    "global_load_dwordx4 v[132:135], v235, s[74:75] offset:-2048 sc1\n"
    "global_load_dwordx4 v[112:115], v235, s[72:73] offset:-1024 sc1\n"
    "global_load_dwordx4 v[136:139], v235, s[74:75] offset:-1024 sc1\n"
    "global_load_dwordx4 v[116:119], v235, s[72:73] offset:0 sc1\n"
    "global_load_dwordx4 v[140:143], v235, s[74:75] offset:0 sc1\n"
    "global_load_dwordx4 v[120:123], v235, s[72:73] offset:1024 sc1\n"
    "global_load_dwordx4 v[144:147], v235, s[74:75] offset:1024 sc1\n"
    "global_load_dwordx4 v[124:127], v235, s[72:73] offset:2048 sc1\n"
    "global_load_dwordx4 v[148:151], v235, s[74:75] offset:2048 sc1\n"
    "s_add_u32 s60, s42, s84\n"
    "s_addc_u32 s61, s43, 0\n"
    "s_mov_b32 m0, 107776\n"
    "s_nop 0\n"
    "global_load_lds_dwordx4 v230, s[60:61]\n"
    "s_mov_b32 m0, 109824\n"
    "s_nop 0\n"
    "global_load_lds_dwordx4 v231, s[60:61]\n"
    "s_mov_b32 m0, 111872\n"
    "s_nop 0\n"
    "global_load_lds_dwordx4 v232, s[60:61]\n"
    "s_mov_b32 m0, 113920\n"
    "s_nop 0\n"
    "global_load_lds_dwordx4 v233, s[60:61]\n"
    "s_mov_b32 m0, 106752\n"
    "s_nop 0\n"
    "global_load_lds_dwordx4 v234, s[60:61]\n"
    "s_add_u32 s60, s42, s85\n"
    "s_addc_u32 s61, s43, 0\n"
    "s_mov_b32 m0, 116672\n"
    "s_nop 0\n"
    "global_load_lds_dwordx4 v230, s[60:61]\n"
    "s_mov_b32 m0, 118720\n"
    "s_nop 0\n"
    "global_load_lds_dwordx4 v231, s[60:61]\n"
    "s_mov_b32 m0, 120768\n"
    "s_nop 0\n"
    "global_load_lds_dwordx4 v232, s[60:61]\n"
    "s_mov_b32 m0, 122816\n"
    "s_nop 0\n"
    "global_load_lds_dwordx4 v233, s[60:61]\n"
    "s_mov_b32 m0, 115648\n"
    "s_nop 0\n"
    "global_load_lds_dwordx4 v234, s[60:61]\n"
    "global_load_dword v250, v238, s[42:43]\n"
    "global_load_dword v250, v239, s[42:43]\n"
    ".Lh0_ck:\n"
    "s_waitcnt vmcnt(44)\n"
    "v_add3_u32 v242, v242, v8, v9\n"
    "v_sub_u32 v8, v8, v240\n"
    "v_add3_u32 v243, v243, v10, v11\n"
    "v_add3_u32 v242, v242, v32, v33\n"
    "v_sub_u32 v32, v32, v240\n"
    "v_add3_u32 v243, v243, v34, v35\n"
    "v_add3_u32 v242, v242, v12, v13\n"
    "v_sub_u32 v12, v12, v240\n"
    "v_add3_u32 v243, v243, v14, v15\n"
    "v_add3_u32 v242, v242, v36, v37\n"
    "v_sub_u32 v36, v36, v240\n"
    "v_add3_u32 v243, v243, v38, v39\n"
    "s_waitcnt vmcnt(40)\n"
    "v_add3_u32 v242, v242, v16, v17\n"
    "v_sub_u32 v16, v16, v240\n"
    "v_add3_u32 v243, v243, v18, v19\n"
    "v_add3_u32 v242, v242, v40, v41\n"
    "v_sub_u32 v40, v40, v240\n"
    "v_add3_u32 v243, v243, v42, v43\n"
    "v_add3_u32 v242, v242, v20, v21\n"
    "v_sub_u32 v20, v20, v240\n"
    "v_add3_u32 v243, v243, v22, v23\n"
    "v_add3_u32 v242, v242, v44, v45\n"
    "v_sub_u32 v44, v44, v240\n"
    "v_add3_u32 v243, v243, v46, v47\n"
    "s_waitcnt vmcnt(36)\n"
    "v_add3_u32 v242, v242, v24, v25\n"
    "v_sub_u32 v24, v24, v240\n"
    "v_add3_u32 v243, v243, v26, v27\n"
    "v_add3_u32 v242, v242, v48, v49\n"
    "v_sub_u32 v48, v48, v240\n"
    "v_add3_u32 v243, v243, v50, v51\n"
    "v_add3_u32 v242, v242, v28, v29\n"
    "v_sub_u32 v28, v28, v240\n"
    "v_add3_u32 v243, v243, v30, v31\n"
    "v_add3_u32 v242, v242, v52, v53\n"
    "v_sub_u32 v52, v52, v240\n"
    "v_add3_u32 v243, v243, v54, v55\n"
    "s_waitcnt vmcnt(32)\n"
    "v_add3_u32 v242, v242, v56, v57\n"
    "v_sub_u32 v56, v56, v240\n"
    "v_add3_u32 v243, v243, v58, v59\n"
    "v_add3_u32 v242, v242, v80, v81\n"
    "v_sub_u32 v80, v80, v240\n"
    "v_add3_u32 v243, v243, v82, v83\n"
    "v_add3_u32 v242, v242, v60, v61\n"
    "v_sub_u32 v60, v60, v240\n"
    "v_add3_u32 v243, v243, v62, v63\n"
    "v_add3_u32 v242, v242, v84, v85\n"
    "v_sub_u32 v84, v84, v240\n"
    "v_add3_u32 v243, v243, v86, v87\n"
    "s_waitcnt vmcnt(28)\n"
    "v_add3_u32 v242, v242, v64, v65\n"
    "v_sub_u32 v64, v64, v240\n"
    "v_add3_u32 v243, v243, v66, v67\n"
    "v_add3_u32 v242, v242, v88, v89\n"
    "v_sub_u32 v88, v88, v240\n"
    "v_add3_u32 v243, v243, v90, v91\n"
    "v_add3_u32 v242, v242, v68, v69\n"
    "v_sub_u32 v68, v68, v240\n"
    "v_add3_u32 v243, v243, v70, v71\n"
    "v_add3_u32 v242, v242, v92, v93\n"
    "v_sub_u32 v92, v92, v240\n"
    "v_add3_u32 v243, v243, v94, v95\n"
    "s_waitcnt vmcnt(24)\n"
    "v_add3_u32 v242, v242, v72, v73\n"
    "v_sub_u32 v72, v72, v240\n"
    "v_add3_u32 v243, v243, v74, v75\n"
    "v_add3_u32 v242, v242, v96, v97\n"
    "v_sub_u32 v96, v96, v240\n"
    "v_add3_u32 v243, v243, v98, v99\n"
    "v_add3_u32 v242, v242, v76, v77\n"
    "v_sub_u32 v76, v76, v240\n"
    "v_add3_u32 v243, v243, v78, v79\n"
    "v_add3_u32 v242, v242, v100, v101\n"
    "v_sub_u32 v100, v100, v240\n"
    "v_add3_u32 v243, v243, v102, v103\n"
    "s_waitcnt vmcnt(20)\n"
    "v_add3_u32 v242, v242, v104, v105\n"
    "v_sub_u32 v104, v104, v240\n"
    "v_add3_u32 v243, v243, v106, v107\n"
    "v_add3_u32 v242, v242, v128, v129\n"
    "v_sub_u32 v128, v128, v240\n"
    "v_add3_u32 v243, v243, v130, v131\n"
    "v_add3_u32 v242, v242, v108, v109\n"
    "v_sub_u32 v108, v108, v240\n"
    "v_add3_u32 v243, v243, v110, v111\n"
    "v_add3_u32 v242, v242, v132, v133\n"
    "v_sub_u32 v132, v132, v240\n"
    "v_add3_u32 v243, v243, v134, v135\n"
    "s_waitcnt vmcnt(16)\n"
    "v_add3_u32 v242, v242, v112, v113\n"
    "v_sub_u32 v112, v112, v240\n"
    "v_add3_u32 v243, v243, v114, v115\n"
    "v_add3_u32 v242, v242, v136, v137\n"
    "v_sub_u32 v136, v136, v240\n"
    "v_add3_u32 v243, v243, v138, v139\n"
    "v_add3_u32 v242, v242, v116, v117\n"
    "v_sub_u32 v116, v116, v240\n"
    "v_add3_u32 v243, v243, v118, v119\n"
    "v_add3_u32 v242, v242, v140, v141\n"
    "v_sub_u32 v140, v140, v240\n"
    "v_add3_u32 v243, v243, v142, v143\n"
    "s_waitcnt vmcnt(12)\n"
    "v_add3_u32 v242, v242, v120, v121\n"
    "v_sub_u32 v120, v120, v240\n"
    "v_add3_u32 v243, v243, v122, v123\n"
    "v_add3_u32 v242, v242, v144, v145\n"
    "v_sub_u32 v144, v144, v240\n"
    "v_add3_u32 v243, v243, v146, v147\n"
    "v_add3_u32 v242, v242, v124, v125\n"
    "v_sub_u32 v124, v124, v240\n"
    "v_add3_u32 v243, v243, v126, v127\n"
    "v_add3_u32 v242, v242, v148, v149\n"
    "v_sub_u32 v148, v148, v240\n"
    "v_add3_u32 v243, v243, v150, v151\n"
    "v_cmp_ne_u64 s[76:77], v[240:241], v[244:245]\n"
    "v_sub_u32 v242, v242, v246\n"
    "v_sub_u32 v243, v243, v247\n"
    "s_nop 1\n"
    "v_permlane32_swap_b32 v242, v243\n"
    "v_add_u32 v242, v242, v243\n"
    "s_nop 1\n"
    "v_add_u32_dpp v242, v242, v242 row_ror:8 row_mask:0xf bank_mask:0xf\n"
    "s_nop 1\n"
    "v_add_u32_dpp v242, v242, v242 row_ror:4 row_mask:0xf bank_mask:0xf\n"
    "s_nop 1\n"
    "v_add_u32_dpp v242, v242, v242 row_ror:2 row_mask:0xf bank_mask:0xf\n"
    "s_nop 1\n"
    "v_add_u32_dpp v242, v242, v242 row_ror:1 row_mask:0xf bank_mask:0xf\n"
    "v_mov_b32 v243, v242\n"
    "s_nop 1\n"
    "v_permlane16_swap_b32 v242, v243\n"
    "v_add_u32 v242, v242, v243\n"
    "v_cmp_ne_u32 vcc, 0, v242\n"
    "s_nop 1\n"
    "s_or_b64 vcc, vcc, s[76:77]\n"
    "s_cbranch_scc1 .Lh0_re\n"
    ".Lh0_ok:\n"
    "s_waitcnt vmcnt(2)\n"
    "v_mov_b32 v227, 99\n"
    "ds_write_b32 v226, v227 offset:0\n"
    ".Lh_sd:\n"
    "s_setprio 0\n"
    "ds_read_b32 v228, v224 offset:128\n"
    "s_waitcnt lgkmcnt(0)\n"
    "v_cmp_gt_u32 vcc, 1, v228\n"
    "s_cbranch_vccnz .Lh3\n"
    ".Lh3b:\n"
    "ds_read_b64_tr_b16 v[152:153], v2 offset:18432\n"
    "ds_read_b64_tr_b16 v[154:155], v3 offset:18432\n"
    "ds_read_b64_tr_b16 v[156:157], v2 offset:19456\n"
    "ds_read_b64_tr_b16 v[158:159], v3 offset:19456\n"
    "ds_read_b64_tr_b16 v[160:161], v2 offset:20480\n"
    "ds_read_b64_tr_b16 v[162:163], v3 offset:20480\n"
    "ds_read_b64_tr_b16 v[164:165], v2 offset:21504\n"
    "ds_read_b64_tr_b16 v[166:167], v3 offset:21504\n"
    "ds_read_b64_tr_b16 v[168:169], v2 offset:22528\n"
    "ds_read_b64_tr_b16 v[170:171], v3 offset:22528\n"
    "ds_read_b64_tr_b16 v[172:173], v2 offset:23552\n"
    "ds_read_b64_tr_b16 v[174:175], v3 offset:23552\n"
    "ds_read_b32 v252, v1 offset:0\n"
    "v_mov_b32 v227, 1\n"
    "ds_write_b32 v226, v227 offset:32\n"
    "v_mov_b32 v242, v240\n"
    "v_mov_b32 v243, v241\n"
    "v_and_b32 v229, 0x70, v235\n"
    "v_add_u32 v229, s48, v229\n"
    "global_store_dwordx4 v229, v[240:243], s[40:41] sc1\n"
    "s_waitcnt lgkmcnt(10)\n"
    "v_mfma_f32_16x16x32_bf16 v[200:203], v[8:11], v[152:155], 0\n"
    "v_mfma_f32_16x16x32_bf16 v[204:207], v[32:35], v[152:155], 0\n"
    "v_mfma_f32_16x16x32_bf16 v[200:203], v[12:15], v[156:159], v[200:203]\n"
    "v_mfma_f32_16x16x32_bf16 v[204:207], v[36:39], v[156:159], v[204:207]\n"
    "s_waitcnt lgkmcnt(8)\n"
    "v_mfma_f32_16x16x32_bf16 v[200:203], v[16:19], v[160:163], v[200:203]\n"
    "v_mfma_f32_16x16x32_bf16 v[204:207], v[40:43], v[160:163], v[204:207]\n"
    "s_waitcnt lgkmcnt(6)\n"
    "v_mfma_f32_16x16x32_bf16 v[200:203], v[20:23], v[164:167], v[200:203]\n"
    "v_mfma_f32_16x16x32_bf16 v[204:207], v[44:47], v[164:167], v[204:207]\n"
    "s_waitcnt lgkmcnt(4)\n"
    "v_mfma_f32_16x16x32_bf16 v[200:203], v[24:27], v[168:171], v[200:203]\n"
    "v_mfma_f32_16x16x32_bf16 v[204:207], v[48:51], v[168:171], v[204:207]\n"
    "s_waitcnt lgkmcnt(2)\n"
    "v_mfma_f32_16x16x32_bf16 v[200:203], v[28:31], v[172:175], v[200:203]\n"
    "v_mfma_f32_16x16x32_bf16 v[204:207], v[52:55], v[172:175], v[204:207]\n"
    "v_mfma_f32_16x16x32_bf16 v[208:211], v[56:59], v[152:155], 0\n"
    "v_mfma_f32_16x16x32_bf16 v[212:215], v[80:83], v[152:155], 0\n"
    "ds_read_b32 v228, v224 offset:144\n"
    "v_mfma_f32_16x16x32_bf16 v[208:211], v[60:63], v[156:159], v[208:211]\n"
    "v_mfma_f32_16x16x32_bf16 v[212:215], v[84:87], v[156:159], v[212:215]\n"
    "v_mfma_f32_16x16x32_bf16 v[208:211], v[64:67], v[160:163], v[208:211]\n"
    "s_waitcnt lgkmcnt(2)\n"
    "v_mul_f32 v200, v200, v252\n"
    "v_mfma_f32_16x16x32_bf16 v[212:215], v[88:91], v[160:163], v[212:215]\n"
    "v_mul_f32 v201, v201, v252\n"
    "v_mul_f32 v202, v202, v252\n"
    "v_mfma_f32_16x16x32_bf16 v[208:211], v[68:71], v[164:167], v[208:211]\n"
    "v_mul_f32 v203, v203, v252\n"
    "v_mul_f32 v204, v204, v252\n"
    "v_mfma_f32_16x16x32_bf16 v[212:215], v[92:95], v[164:167], v[212:215]\n"
    "v_mul_f32 v205, v205, v252\n"
    "v_mul_f32 v206, v206, v252\n"
    "v_mfma_f32_16x16x32_bf16 v[208:211], v[72:75], v[168:171], v[208:211]\n"
    "v_mul_f32 v207, v207, v252\n"
    "v_cvt_pk_bf16_f32 v200, v200, v201\n"
    "v_mfma_f32_16x16x32_bf16 v[212:215], v[96:99], v[168:171], v[212:215]\n"
    "v_cvt_pk_bf16_f32 v201, v202, v203\n"
    "v_cvt_pk_bf16_f32 v202, v204, v205\n"
    "v_mfma_f32_16x16x32_bf16 v[208:211], v[76:79], v[172:175], v[208:211]\n"
    "s_waitcnt lgkmcnt(0)\n"
    "v_cmp_gt_u32 vcc, 1, v228\n"
    "v_mfma_f32_16x16x32_bf16 v[212:215], v[100:103], v[172:175], v[212:215]\n"
    "v_cvt_pk_bf16_f32 v203, v206, v207\n"
    "s_cbranch_vccnz .Lh4\n"
    "v_mfma_f32_16x16x32_bf16 v[216:219], v[104:107], v[152:155], 0\n"
    "ds_read_b64_tr_b16 v[176:177], v2 offset:24576\n"
    "ds_read_b64_tr_b16 v[178:179], v3 offset:24576\n"
    "v_mfma_f32_16x16x32_bf16 v[220:223], v[128:131], v[152:155], 0\n"
    "ds_read_b64_tr_b16 v[180:181], v2 offset:25600\n"
    "ds_read_b64_tr_b16 v[182:183], v3 offset:25600\n"
    "v_mfma_f32_16x16x32_bf16 v[216:219], v[108:111], v[156:159], v[216:219]\n"
    "ds_read_b64_tr_b16 v[184:185], v2 offset:26624\n"
    "ds_read_b64_tr_b16 v[186:187], v3 offset:26624\n"
    "v_mfma_f32_16x16x32_bf16 v[220:223], v[132:135], v[156:159], v[220:223]\n"
    "ds_read_b64_tr_b16 v[188:189], v2 offset:27648\n"
    "ds_read_b64_tr_b16 v[190:191], v3 offset:27648\n"
    "v_mfma_f32_16x16x32_bf16 v[216:219], v[112:115], v[160:163], v[216:219]\n"
    "buffer_store_dwordx4 v[200:203], v4, s[80:83], 0 offen offset:0 sc1\n"
    "v_mfma_f32_16x16x32_bf16 v[220:223], v[136:139], v[160:163], v[220:223]\n"
    "v_mul_f32 v208, v208, v252\n"
    "v_mul_f32 v209, v209, v252\n"
    "v_mfma_f32_16x16x32_bf16 v[216:219], v[116:119], v[164:167], v[216:219]\n"
    "ds_read_b64_tr_b16 v[192:193], v2 offset:28672\n"
    "ds_read_b64_tr_b16 v[194:195], v3 offset:28672\n"
    "v_mfma_f32_16x16x32_bf16 v[220:223], v[140:143], v[164:167], v[220:223]\n"
    "v_mul_f32 v210, v210, v252\n"
    "v_mul_f32 v211, v211, v252\n"
    "v_mfma_f32_16x16x32_bf16 v[216:219], v[120:123], v[168:171], v[216:219]\n"
    "ds_read_b64_tr_b16 v[196:197], v2 offset:29696\n"
    "ds_read_b64_tr_b16 v[198:199], v3 offset:29696\n"
    "v_mfma_f32_16x16x32_bf16 v[220:223], v[144:147], v[168:171], v[220:223]\n"
    "v_mul_f32 v212, v212, v252\n"
    "v_mov_b32 v227, 2\n"
    "v_mfma_f32_16x16x32_bf16 v[216:219], v[124:127], v[172:175], v[216:219]\n"
    "ds_read_b32 v253, v1 offset:64\n"
    "v_mul_f32 v213, v213, v252\n"
    "v_mfma_f32_16x16x32_bf16 v[220:223], v[148:151], v[172:175], v[220:223]\n"
    "v_mul_f32 v214, v214, v252\n"
    "v_mul_f32 v215, v215, v252\n"
    "ds_write_b32 v226, v227 offset:32\n"
    "v_cvt_pk_bf16_f32 v208, v208, v209\n"
    "v_cvt_pk_bf16_f32 v209, v210, v211\n"
    "v_cvt_pk_bf16_f32 v210, v212, v213\n"
    "v_cvt_pk_bf16_f32 v211, v214, v215\n"
    "buffer_store_dwordx4 v[208:211], v4, s[80:83], 0 offen offset:64 sc1\n"
    ".Lh4j:\n"
    "s_waitcnt lgkmcnt(2)\n"
    "v_mfma_f32_16x16x32_bf16 v[200:203], v[8:11], v[176:179], 0\n"
    "v_mul_f32 v216, v216, v252\n"
    "v_mul_f32 v217, v217, v252\n"
    "v_mfma_f32_16x16x32_bf16 v[204:207], v[32:35], v[176:179], 0\n"
    "v_mul_f32 v218, v218, v252\n"
    "v_mul_f32 v219, v219, v252\n"
    "v_mfma_f32_16x16x32_bf16 v[200:203], v[12:15], v[180:183], v[200:203]\n"
    "v_mul_f32 v220, v220, v252\n"
    "v_mul_f32 v221, v221, v252\n"
    "v_mfma_f32_16x16x32_bf16 v[204:207], v[36:39], v[180:183], v[204:207]\n"
    "v_mul_f32 v222, v222, v252\n"
    "v_mul_f32 v223, v223, v252\n"
    "v_mfma_f32_16x16x32_bf16 v[200:203], v[16:19], v[184:187], v[200:203]\n"
    "v_cvt_pk_bf16_f32 v216, v216, v217\n"
    "v_cvt_pk_bf16_f32 v217, v218, v219\n"
    "v_mfma_f32_16x16x32_bf16 v[204:207], v[40:43], v[184:187], v[204:207]\n"
    "v_cvt_pk_bf16_f32 v218, v220, v221\n"
    "v_cvt_pk_bf16_f32 v219, v222, v223\n"
    "v_mfma_f32_16x16x32_bf16 v[200:203], v[20:23], v[188:191], v[200:203]\n"
    "buffer_store_dwordx4 v[216:219], v4, s[80:83], 0 offen offset:128 sc1\n"
    "v_mfma_f32_16x16x32_bf16 v[204:207], v[44:47], v[188:191], v[204:207]\n"
    "v_mfma_f32_16x16x32_bf16 v[200:203], v[24:27], v[192:195], v[200:203]\n"
    "v_add_u32 v4, s46, v4\n"
    "v_mfma_f32_16x16x32_bf16 v[204:207], v[48:51], v[192:195], v[204:207]\n"
    "v_mfma_f32_16x16x32_bf16 v[200:203], v[28:31], v[196:199], v[200:203]\n"
    "v_mfma_f32_16x16x32_bf16 v[204:207], v[52:55], v[196:199], v[204:207]\n"
    "v_mfma_f32_16x16x32_bf16 v[208:211], v[56:59], v[176:179], 0\n"
    "v_mfma_f32_16x16x32_bf16 v[212:215], v[80:83], v[176:179], 0\n"
    "ds_read_b32 v228, v224 offset:160\n"
    "v_mfma_f32_16x16x32_bf16 v[208:211], v[60:63], v[180:183], v[208:211]\n"
    "v_mfma_f32_16x16x32_bf16 v[212:215], v[84:87], v[180:183], v[212:215]\n"
    "v_mfma_f32_16x16x32_bf16 v[208:211], v[64:67], v[184:187], v[208:211]\n"
    "s_waitcnt lgkmcnt(2)\n"
    "v_mul_f32 v200, v200, v253\n"
    "v_mfma_f32_16x16x32_bf16 v[212:215], v[88:91], v[184:187], v[212:215]\n"
    "v_mul_f32 v201, v201, v253\n"
    "v_mul_f32 v202, v202, v253\n"
    "v_mfma_f32_16x16x32_bf16 v[208:211], v[68:71], v[188:191], v[208:211]\n"
    "v_mul_f32 v203, v203, v253\n"
    "v_mul_f32 v204, v204, v253\n"
    "v_mfma_f32_16x16x32_bf16 v[212:215], v[92:95], v[188:191], v[212:215]\n"
    "v_mul_f32 v205, v205, v253\n"
    "v_mul_f32 v206, v206, v253\n"
    "v_mfma_f32_16x16x32_bf16 v[208:211], v[72:75], v[192:195], v[208:211]\n"
    "v_mul_f32 v207, v207, v253\n"
    "v_cvt_pk_bf16_f32 v200, v200, v201\n"
    "v_mfma_f32_16x16x32_bf16 v[212:215], v[96:99], v[192:195], v[212:215]\n"
    "v_cvt_pk_bf16_f32 v201, v202, v203\n"
    "v_cvt_pk_bf16_f32 v202, v204, v205\n"
    "v_mfma_f32_16x16x32_bf16 v[208:211], v[76:79], v[196:199], v[208:211]\n"
    "s_waitcnt lgkmcnt(0)\n"
    "v_cmp_gt_u32 vcc, 1, v228\n"
    "v_mfma_f32_16x16x32_bf16 v[212:215], v[100:103], v[196:199], v[212:215]\n"
    "v_cvt_pk_bf16_f32 v203, v206, v207\n"
    "s_cbranch_vccnz .Lh5\n"
    "v_mfma_f32_16x16x32_bf16 v[216:219], v[104:107], v[176:179], 0\n"
    "ds_read_b64_tr_b16 v[152:153], v2 offset:30720\n"
    "ds_read_b64_tr_b16 v[154:155], v3 offset:30720\n"
    "v_mfma_f32_16x16x32_bf16 v[220:223], v[128:131], v[176:179], 0\n"
    "ds_read_b64_tr_b16 v[156:157], v2 offset:31744\n"
    "ds_read_b64_tr_b16 v[158:159], v3 offset:31744\n"
    "v_mfma_f32_16x16x32_bf16 v[216:219], v[108:111], v[180:183], v[216:219]\n"
    "ds_read_b64_tr_b16 v[160:161], v2 offset:32768\n"
    "ds_read_b64_tr_b16 v[162:163], v3 offset:32768\n"
    "v_mfma_f32_16x16x32_bf16 v[220:223], v[132:135], v[180:183], v[220:223]\n"
    "ds_read_b64_tr_b16 v[164:165], v2 offset:33792\n"
    "ds_read_b64_tr_b16 v[166:167], v3 offset:33792\n"
    "v_mfma_f32_16x16x32_bf16 v[216:219], v[112:115], v[184:187], v[216:219]\n"
    "buffer_store_dwordx4 v[200:203], v5, s[80:83], 0 offen offset:0 sc1\n"
    "v_mfma_f32_16x16x32_bf16 v[220:223], v[136:139], v[184:187], v[220:223]\n"
    "v_mul_f32 v208, v208, v253\n"
    "v_mul_f32 v209, v209, v253\n"
    "v_mfma_f32_16x16x32_bf16 v[216:219], v[116:119], v[188:191], v[216:219]\n"
    "ds_read_b64_tr_b16 v[168:169], v2 offset:34816\n"
    "ds_read_b64_tr_b16 v[170:171], v3 offset:34816\n"
    "v_mfma_f32_16x16x32_bf16 v[220:223], v[140:143], v[188:191], v[220:223]\n"
    "v_mul_f32 v210, v210, v253\n"
    "v_mul_f32 v211, v211, v253\n"
    "v_mfma_f32_16x16x32_bf16 v[216:219], v[120:123], v[192:195], v[216:219]\n"
    "ds_read_b64_tr_b16 v[172:173], v2 offset:35840\n"
    "ds_read_b64_tr_b16 v[174:175], v3 offset:35840\n"
    "v_mfma_f32_16x16x32_bf16 v[220:223], v[144:147], v[192:195], v[220:223]\n"
    "v_mul_f32 v212, v212, v253\n"
    "v_mov_b32 v227, 3\n"
    "v_mfma_f32_16x16x32_bf16 v[216:219], v[124:127], v[196:199], v[216:219]\n"
    "ds_read_b32 v252, v1 offset:128\n"
    "v_mul_f32 v213, v213, v253\n"
    "v_mfma_f32_16x16x32_bf16 v[220:223], v[148:151], v[196:199], v[220:223]\n"
    "v_mul_f32 v214, v214, v253\n"
    "v_mul_f32 v215, v215, v253\n"
    "ds_write_b32 v226, v227 offset:32\n"
    "v_cvt_pk_bf16_f32 v208, v208, v209\n"
    "v_cvt_pk_bf16_f32 v209, v210, v211\n"
    "v_cvt_pk_bf16_f32 v210, v212, v213\n"
    "v_cvt_pk_bf16_f32 v211, v214, v215\n"
    "buffer_store_dwordx4 v[208:211], v5, s[80:83], 0 offen offset:64 sc1\n"
    ".Lh5j:\n"
    "s_waitcnt lgkmcnt(2)\n"
    "v_mfma_f32_16x16x32_bf16 v[200:203], v[8:11], v[152:155], 0\n"
    "v_mul_f32 v216, v216, v253\n"
    "v_mul_f32 v217, v217, v253\n"
    "v_mfma_f32_16x16x32_bf16 v[204:207], v[32:35], v[152:155], 0\n"
    "v_mul_f32 v218, v218, v253\n"
    "v_mul_f32 v219, v219, v253\n"
    "v_mfma_f32_16x16x32_bf16 v[200:203], v[12:15], v[156:159], v[200:203]\n"
    "v_mul_f32 v220, v220, v253\n"
    "v_mul_f32 v221, v221, v253\n"
    "v_mfma_f32_16x16x32_bf16 v[204:207], v[36:39], v[156:159], v[204:207]\n"
    "v_mul_f32 v222, v222, v253\n"
    "v_mul_f32 v223, v223, v253\n"
    "v_mfma_f32_16x16x32_bf16 v[200:203], v[16:19], v[160:163], v[200:203]\n"
    "v_cvt_pk_bf16_f32 v216, v216, v217\n"
    "v_cvt_pk_bf16_f32 v217, v218, v219\n"
    "v_mfma_f32_16x16x32_bf16 v[204:207], v[40:43], v[160:163], v[204:207]\n"
    "v_cvt_pk_bf16_f32 v218, v220, v221\n"
    "v_cvt_pk_bf16_f32 v219, v222, v223\n"
    "v_mfma_f32_16x16x32_bf16 v[200:203], v[20:23], v[164:167], v[200:203]\n"
    "buffer_store_dwordx4 v[216:219], v5, s[80:83], 0 offen offset:128 sc1\n"
    "v_mfma_f32_16x16x32_bf16 v[204:207], v[44:47], v[164:167], v[204:207]\n"
    "v_mfma_f32_16x16x32_bf16 v[200:203], v[24:27], v[168:171], v[200:203]\n"
    "v_add_u32 v5, s46, v5\n"
    "v_mfma_f32_16x16x32_bf16 v[204:207], v[48:51], v[168:171], v[204:207]\n"
    "v_mfma_f32_16x16x32_bf16 v[200:203], v[28:31], v[172:175], v[200:203]\n"
    "v_mfma_f32_16x16x32_bf16 v[204:207], v[52:55], v[172:175], v[204:207]\n"
    "v_mfma_f32_16x16x32_bf16 v[208:211], v[56:59], v[152:155], 0\n"
    "v_mfma_f32_16x16x32_bf16 v[212:215], v[80:83], v[152:155], 0\n"
    "ds_read_b32 v228, v224 offset:176\n"
    "v_mfma_f32_16x16x32_bf16 v[208:211], v[60:63], v[156:159], v[208:211]\n"
    "v_mfma_f32_16x16x32_bf16 v[212:215], v[84:87], v[156:159], v[212:215]\n"
    "v_mfma_f32_16x16x32_bf16 v[208:211], v[64:67], v[160:163], v[208:211]\n"
    "s_waitcnt lgkmcnt(2)\n"
    "v_mul_f32 v200, v200, v252\n"
    "v_mfma_f32_16x16x32_bf16 v[212:215], v[88:91], v[160:163], v[212:215]\n"
    "v_mul_f32 v201, v201, v252\n"
    "v_mul_f32 v202, v202, v252\n"
    "v_mfma_f32_16x16x32_bf16 v[208:211], v[68:71], v[164:167], v[208:211]\n"
    "v_mul_f32 v203, v203, v252\n"
    "v_mul_f32 v204, v204, v252\n"
    "v_mfma_f32_16x16x32_bf16 v[212:215], v[92:95], v[164:167], v[212:215]\n"
    "v_mul_f32 v205, v205, v252\n"
    "v_mul_f32 v206, v206, v252\n"
    "v_mfma_f32_16x16x32_bf16 v[208:211], v[72:75], v[168:171], v[208:211]\n"
    "v_mul_f32 v207, v207, v252\n"
    "v_cvt_pk_bf16_f32 v200, v200, v201\n"
    "v_mfma_f32_16x16x32_bf16 v[212:215], v[96:99], v[168:171], v[212:215]\n"
    "v_cvt_pk_bf16_f32 v201, v202, v203\n"
    "v_cvt_pk_bf16_f32 v202, v204, v205\n"
    "v_mfma_f32_16x16x32_bf16 v[208:211], v[76:79], v[172:175], v[208:211]\n"
    "s_waitcnt lgkmcnt(0)\n"
    "v_cmp_gt_u32 vcc, 1, v228\n"
    "v_mfma_f32_16x16x32_bf16 v[212:215], v[100:103], v[172:175], v[212:215]\n"
    "v_cvt_pk_bf16_f32 v203, v206, v207\n"
    "s_cbranch_vccnz .Lh6\n"
    "v_mfma_f32_16x16x32_bf16 v[216:219], v[104:107], v[152:155], 0\n"
    "ds_read_b64_tr_b16 v[176:177], v2 offset:18432\n"
    "ds_read_b64_tr_b16 v[178:179], v3 offset:18432\n"
    "v_mfma_f32_16x16x32_bf16 v[220:223], v[128:131], v[152:155], 0\n"
    "ds_read_b64_tr_b16 v[180:181], v2 offset:19456\n"
    "ds_read_b64_tr_b16 v[182:183], v3 offset:19456\n"
    "v_mfma_f32_16x16x32_bf16 v[216:219], v[108:111], v[156:159], v[216:219]\n"
    "ds_read_b64_tr_b16 v[184:185], v2 offset:20480\n"
    "ds_read_b64_tr_b16 v[186:187], v3 offset:20480\n"
    "v_mfma_f32_16x16x32_bf16 v[220:223], v[132:135], v[156:159], v[220:223]\n"
    "ds_read_b64_tr_b16 v[188:189], v2 offset:21504\n"
    "ds_read_b64_tr_b16 v[190:191], v3 offset:21504\n"
    "v_mfma_f32_16x16x32_bf16 v[216:219], v[112:115], v[160:163], v[216:219]\n"
    "buffer_store_dwordx4 v[200:203], v4, s[80:83], 0 offen offset:0 sc1\n"
    "v_mfma_f32_16x16x32_bf16 v[220:223], v[136:139], v[160:163], v[220:223]\n"
    "v_mul_f32 v208, v208, v252\n"
    "v_mul_f32 v209, v209, v252\n"
    "v_mfma_f32_16x16x32_bf16 v[216:219], v[116:119], v[164:167], v[216:219]\n"
    "ds_read_b64_tr_b16 v[192:193], v2 offset:22528\n"
    "ds_read_b64_tr_b16 v[194:195], v3 offset:22528\n"
    "v_mfma_f32_16x16x32_bf16 v[220:223], v[140:143], v[164:167], v[220:223]\n"
    "v_mul_f32 v210, v210, v252\n"
    "v_mul_f32 v211, v211, v252\n"
    "v_mfma_f32_16x16x32_bf16 v[216:219], v[120:123], v[168:171], v[216:219]\n"
    "ds_read_b64_tr_b16 v[196:197], v2 offset:23552\n"
    "ds_read_b64_tr_b16 v[198:199], v3 offset:23552\n"
    "v_mfma_f32_16x16x32_bf16 v[220:223], v[144:147], v[168:171], v[220:223]\n"
    "v_mul_f32 v212, v212, v252\n"
    "v_mov_b32 v227, 4\n"
    "v_mfma_f32_16x16x32_bf16 v[216:219], v[124:127], v[172:175], v[216:219]\n"
    "ds_read_b32 v253, v1 offset:0\n"
    "v_mul_f32 v213, v213, v252\n"
    "v_mfma_f32_16x16x32_bf16 v[220:223], v[148:151], v[172:175], v[220:223]\n"
    "v_mul_f32 v214, v214, v252\n"
    "v_mul_f32 v215, v215, v252\n"
    "ds_write_b32 v226, v227 offset:32\n"
    "v_cvt_pk_bf16_f32 v208, v208, v209\n"
    "v_cvt_pk_bf16_f32 v209, v210, v211\n"
    "v_cvt_pk_bf16_f32 v210, v212, v213\n"
    "v_cvt_pk_bf16_f32 v211, v214, v215\n"
    "buffer_store_dwordx4 v[208:211], v4, s[80:83], 0 offen offset:64 sc1\n"
    ".Lh6j:\n"
    "s_waitcnt lgkmcnt(2)\n"
    "v_mfma_f32_16x16x32_bf16 v[200:203], v[8:11], v[176:179], 0\n"
    "v_mul_f32 v216, v216, v252\n"
    "v_mul_f32 v217, v217, v252\n"
    "v_mfma_f32_16x16x32_bf16 v[204:207], v[32:35], v[176:179], 0\n"
    "v_mul_f32 v218, v218, v252\n"
    "v_mul_f32 v219, v219, v252\n"
    "v_mfma_f32_16x16x32_bf16 v[200:203], v[12:15], v[180:183], v[200:203]\n"
    "v_mul_f32 v220, v220, v252\n"
    "v_mul_f32 v221, v221, v252\n"
    "v_mfma_f32_16x16x32_bf16 v[204:207], v[36:39], v[180:183], v[204:207]\n"
    "v_mul_f32 v222, v222, v252\n"
    "v_mul_f32 v223, v223, v252\n"
    "v_mfma_f32_16x16x32_bf16 v[200:203], v[16:19], v[184:187], v[200:203]\n"
    "v_cvt_pk_bf16_f32 v216, v216, v217\n"
    "v_cvt_pk_bf16_f32 v217, v218, v219\n"
    "v_mfma_f32_16x16x32_bf16 v[204:207], v[40:43], v[184:187], v[204:207]\n"
    "v_cvt_pk_bf16_f32 v218, v220, v221\n"
    "v_cvt_pk_bf16_f32 v219, v222, v223\n"
    "v_mfma_f32_16x16x32_bf16 v[200:203], v[20:23], v[188:191], v[200:203]\n"
    "buffer_store_dwordx4 v[216:219], v4, s[80:83], 0 offen offset:128 sc1\n"
    "v_mfma_f32_16x16x32_bf16 v[204:207], v[44:47], v[188:191], v[204:207]\n"
    "v_mfma_f32_16x16x32_bf16 v[200:203], v[24:27], v[192:195], v[200:203]\n"
    "v_add_u32 v4, s46, v4\n"
    "v_mfma_f32_16x16x32_bf16 v[204:207], v[48:51], v[192:195], v[204:207]\n"
    "v_mfma_f32_16x16x32_bf16 v[200:203], v[28:31], v[196:199], v[200:203]\n"
    "v_mfma_f32_16x16x32_bf16 v[204:207], v[52:55], v[196:199], v[204:207]\n"
    "v_mfma_f32_16x16x32_bf16 v[208:211], v[56:59], v[176:179], 0\n"
    "v_mfma_f32_16x16x32_bf16 v[212:215], v[80:83], v[176:179], 0\n"
    "ds_read_b32 v228, v224 offset:128\n"
    "v_mfma_f32_16x16x32_bf16 v[208:211], v[60:63], v[180:183], v[208:211]\n"
    "v_mfma_f32_16x16x32_bf16 v[212:215], v[84:87], v[180:183], v[212:215]\n"
    "v_mfma_f32_16x16x32_bf16 v[208:211], v[64:67], v[184:187], v[208:211]\n"
    "s_waitcnt lgkmcnt(2)\n"
    "v_mul_f32 v200, v200, v253\n"
    "v_mfma_f32_16x16x32_bf16 v[212:215], v[88:91], v[184:187], v[212:215]\n"
    "v_mul_f32 v201, v201, v253\n"
    "v_mul_f32 v202, v202, v253\n"
    "v_mfma_f32_16x16x32_bf16 v[208:211], v[68:71], v[188:191], v[208:211]\n"
    "v_mul_f32 v203, v203, v253\n"
    "v_mul_f32 v204, v204, v253\n"
    "v_mfma_f32_16x16x32_bf16 v[212:215], v[92:95], v[188:191], v[212:215]\n"
    "v_mul_f32 v205, v205, v253\n"
    "v_mul_f32 v206, v206, v253\n"
    "v_mfma_f32_16x16x32_bf16 v[208:211], v[72:75], v[192:195], v[208:211]\n"
    "v_mul_f32 v207, v207, v253\n"
    "v_cvt_pk_bf16_f32 v200, v200, v201\n"
    "v_mfma_f32_16x16x32_bf16 v[212:215], v[96:99], v[192:195], v[212:215]\n"
    "v_cvt_pk_bf16_f32 v201, v202, v203\n"
    "v_cvt_pk_bf16_f32 v202, v204, v205\n"
    "v_mfma_f32_16x16x32_bf16 v[208:211], v[76:79], v[196:199], v[208:211]\n"
    "s_waitcnt lgkmcnt(0)\n"
    "v_cmp_gt_u32 vcc, 2, v228\n"
    "v_mfma_f32_16x16x32_bf16 v[212:215], v[100:103], v[196:199], v[212:215]\n"
    "v_cvt_pk_bf16_f32 v203, v206, v207\n"
    "s_cbranch_vccnz .Lh7\n"
    "v_mfma_f32_16x16x32_bf16 v[216:219], v[104:107], v[176:179], 0\n"
    "ds_read_b64_tr_b16 v[152:153], v2 offset:24576\n"
    "ds_read_b64_tr_b16 v[154:155], v3 offset:24576\n"
    "v_mfma_f32_16x16x32_bf16 v[220:223], v[128:131], v[176:179], 0\n"
    "ds_read_b64_tr_b16 v[156:157], v2 offset:25600\n"
    "ds_read_b64_tr_b16 v[158:159], v3 offset:25600\n"
    "v_mfma_f32_16x16x32_bf16 v[216:219], v[108:111], v[180:183], v[216:219]\n"
    "ds_read_b64_tr_b16 v[160:161], v2 offset:26624\n"
    "ds_read_b64_tr_b16 v[162:163], v3 offset:26624\n"
    "v_mfma_f32_16x16x32_bf16 v[220:223], v[132:135], v[180:183], v[220:223]\n"
    "ds_read_b64_tr_b16 v[164:165], v2 offset:27648\n"
    "ds_read_b64_tr_b16 v[166:167], v3 offset:27648\n"
    "v_mfma_f32_16x16x32_bf16 v[216:219], v[112:115], v[184:187], v[216:219]\n"
    "buffer_store_dwordx4 v[200:203], v5, s[80:83], 0 offen offset:0 sc1\n"
    "v_mfma_f32_16x16x32_bf16 v[220:223], v[136:139], v[184:187], v[220:223]\n"
    "v_mul_f32 v208, v208, v253\n"
    "v_mul_f32 v209, v209, v253\n"
    "v_mfma_f32_16x16x32_bf16 v[216:219], v[116:119], v[188:191], v[216:219]\n"
    "ds_read_b64_tr_b16 v[168:169], v2 offset:28672\n"
    "ds_read_b64_tr_b16 v[170:171], v3 offset:28672\n"
    "v_mfma_f32_16x16x32_bf16 v[220:223], v[140:143], v[188:191], v[220:223]\n"
    "v_mul_f32 v210, v210, v253\n"
    "v_mul_f32 v211, v211, v253\n"
    "v_mfma_f32_16x16x32_bf16 v[216:219], v[120:123], v[192:195], v[216:219]\n"
    "ds_read_b64_tr_b16 v[172:173], v2 offset:29696\n"
    "ds_read_b64_tr_b16 v[174:175], v3 offset:29696\n"
    "v_mfma_f32_16x16x32_bf16 v[220:223], v[144:147], v[192:195], v[220:223]\n"
    "v_mul_f32 v212, v212, v253\n"
    "v_mov_b32 v227, 5\n"
    "v_mfma_f32_16x16x32_bf16 v[216:219], v[124:127], v[196:199], v[216:219]\n"
    "ds_read_b32 v252, v1 offset:64\n"
    "v_mul_f32 v213, v213, v253\n"
    "v_mfma_f32_16x16x32_bf16 v[220:223], v[148:151], v[196:199], v[220:223]\n"
    "v_mul_f32 v214, v214, v253\n"
    "v_mul_f32 v215, v215, v253\n"
    "ds_write_b32 v226, v227 offset:32\n"
    "v_cvt_pk_bf16_f32 v208, v208, v209\n"
    "v_cvt_pk_bf16_f32 v209, v210, v211\n"
    "v_cvt_pk_bf16_f32 v210, v212, v213\n"
    "v_cvt_pk_bf16_f32 v211, v214, v215\n"
    "buffer_store_dwordx4 v[208:211], v5, s[80:83], 0 offen offset:64 sc1\n"
    ".Lh7j:\n"
    "s_waitcnt lgkmcnt(2)\n"
    "v_mfma_f32_16x16x32_bf16 v[200:203], v[8:11], v[152:155], 0\n"
    "v_mul_f32 v216, v216, v253\n"
    "v_mul_f32 v217, v217, v253\n"
    "v_mfma_f32_16x16x32_bf16 v[204:207], v[32:35], v[152:155], 0\n"
    "v_mul_f32 v218, v218, v253\n"
    "v_mul_f32 v219, v219, v253\n"
    "v_mfma_f32_16x16x32_bf16 v[200:203], v[12:15], v[156:159], v[200:203]\n"
    "v_mul_f32 v220, v220, v253\n"
    "v_mul_f32 v221, v221, v253\n"
    "v_mfma_f32_16x16x32_bf16 v[204:207], v[36:39], v[156:159], v[204:207]\n"
    "v_mul_f32 v222, v222, v253\n"
    "v_mul_f32 v223, v223, v253\n"
    "v_mfma_f32_16x16x32_bf16 v[200:203], v[16:19], v[160:163], v[200:203]\n"
    "v_cvt_pk_bf16_f32 v216, v216, v217\n"
    "v_cvt_pk_bf16_f32 v217, v218, v219\n"
    "v_mfma_f32_16x16x32_bf16 v[204:207], v[40:43], v[160:163], v[204:207]\n"
    "v_cvt_pk_bf16_f32 v218, v220, v221\n"
    "v_cvt_pk_bf16_f32 v219, v222, v223\n"
    "v_mfma_f32_16x16x32_bf16 v[200:203], v[20:23], v[164:167], v[200:203]\n"
    "buffer_store_dwordx4 v[216:219], v5, s[80:83], 0 offen offset:128 sc1\n"
    "v_mfma_f32_16x16x32_bf16 v[204:207], v[44:47], v[164:167], v[204:207]\n"
    "v_mfma_f32_16x16x32_bf16 v[200:203], v[24:27], v[168:171], v[200:203]\n"
    "v_add_u32 v5, s46, v5\n"
    "v_mfma_f32_16x16x32_bf16 v[204:207], v[48:51], v[168:171], v[204:207]\n"
    "v_mfma_f32_16x16x32_bf16 v[200:203], v[28:31], v[172:175], v[200:203]\n"
    "v_mfma_f32_16x16x32_bf16 v[204:207], v[52:55], v[172:175], v[204:207]\n"
    "v_mfma_f32_16x16x32_bf16 v[208:211], v[56:59], v[152:155], 0\n"
    "v_mfma_f32_16x16x32_bf16 v[212:215], v[80:83], v[152:155], 0\n"
    "ds_read_b32 v228, v224 offset:144\n"
    "v_mfma_f32_16x16x32_bf16 v[208:211], v[60:63], v[156:159], v[208:211]\n"
    "v_mfma_f32_16x16x32_bf16 v[212:215], v[84:87], v[156:159], v[212:215]\n"
    "v_mfma_f32_16x16x32_bf16 v[208:211], v[64:67], v[160:163], v[208:211]\n"
    "s_waitcnt lgkmcnt(2)\n"
    "v_mul_f32 v200, v200, v252\n"
    "v_mfma_f32_16x16x32_bf16 v[212:215], v[88:91], v[160:163], v[212:215]\n"
    "v_mul_f32 v201, v201, v252\n"
    "v_mul_f32 v202, v202, v252\n"
    "v_mfma_f32_16x16x32_bf16 v[208:211], v[68:71], v[164:167], v[208:211]\n"
    "v_mul_f32 v203, v203, v252\n"
    "v_mul_f32 v204, v204, v252\n"
    "v_mfma_f32_16x16x32_bf16 v[212:215], v[92:95], v[164:167], v[212:215]\n"
    "v_mul_f32 v205, v205, v252\n"
    "v_mul_f32 v206, v206, v252\n"
    "v_mfma_f32_16x16x32_bf16 v[208:211], v[72:75], v[168:171], v[208:211]\n"
    "v_mul_f32 v207, v207, v252\n"
    "v_cvt_pk_bf16_f32 v200, v200, v201\n"
    "v_mfma_f32_16x16x32_bf16 v[212:215], v[96:99], v[168:171], v[212:215]\n"
    "v_cvt_pk_bf16_f32 v201, v202, v203\n"
    "v_cvt_pk_bf16_f32 v202, v204, v205\n"
    "v_mfma_f32_16x16x32_bf16 v[208:211], v[76:79], v[172:175], v[208:211]\n"
    "s_waitcnt lgkmcnt(0)\n"
    "v_cmp_gt_u32 vcc, 2, v228\n"
    "v_mfma_f32_16x16x32_bf16 v[212:215], v[100:103], v[172:175], v[212:215]\n"
    "v_cvt_pk_bf16_f32 v203, v206, v207\n"
    "s_cbranch_vccnz .Lh8\n"
    "v_mfma_f32_16x16x32_bf16 v[216:219], v[104:107], v[152:155], 0\n"
    "ds_read_b64_tr_b16 v[176:177], v2 offset:30720\n"
    "ds_read_b64_tr_b16 v[178:179], v3 offset:30720\n"
    "v_mfma_f32_16x16x32_bf16 v[220:223], v[128:131], v[152:155], 0\n"
    "ds_read_b64_tr_b16 v[180:181], v2 offset:31744\n"
    "ds_read_b64_tr_b16 v[182:183], v3 offset:31744\n"
    "v_mfma_f32_16x16x32_bf16 v[216:219], v[108:111], v[156:159], v[216:219]\n"
    "ds_read_b64_tr_b16 v[184:185], v2 offset:32768\n"
    "ds_read_b64_tr_b16 v[186:187], v3 offset:32768\n"
    "v_mfma_f32_16x16x32_bf16 v[220:223], v[132:135], v[156:159], v[220:223]\n"
    "ds_read_b64_tr_b16 v[188:189], v2 offset:33792\n"
    "ds_read_b64_tr_b16 v[190:191], v3 offset:33792\n"
    "v_mfma_f32_16x16x32_bf16 v[216:219], v[112:115], v[160:163], v[216:219]\n"
    "buffer_store_dwordx4 v[200:203], v4, s[80:83], 0 offen offset:0 sc1\n"
    "v_mfma_f32_16x16x32_bf16 v[220:223], v[136:139], v[160:163], v[220:223]\n"
    "v_mul_f32 v208, v208, v252\n"
    "v_mul_f32 v209, v209, v252\n"
    "v_mfma_f32_16x16x32_bf16 v[216:219], v[116:119], v[164:167], v[216:219]\n"
    "ds_read_b64_tr_b16 v[192:193], v2 offset:34816\n"
    "ds_read_b64_tr_b16 v[194:195], v3 offset:34816\n"
    "v_mfma_f32_16x16x32_bf16 v[220:223], v[140:143], v[164:167], v[220:223]\n"
    "v_mul_f32 v210, v210, v252\n"
    "v_mul_f32 v211, v211, v252\n"
    "v_mfma_f32_16x16x32_bf16 v[216:219], v[120:123], v[168:171], v[216:219]\n"
    "ds_read_b64_tr_b16 v[196:197], v2 offset:35840\n"
    "ds_read_b64_tr_b16 v[198:199], v3 offset:35840\n"
    "v_mfma_f32_16x16x32_bf16 v[220:223], v[144:147], v[168:171], v[220:223]\n"
    "v_mul_f32 v212, v212, v252\n"
    "v_mov_b32 v227, 6\n"
    "v_mfma_f32_16x16x32_bf16 v[216:219], v[124:127], v[172:175], v[216:219]\n"
    "ds_read_b32 v253, v1 offset:128\n"
    "v_mul_f32 v213, v213, v252\n"
    "v_mfma_f32_16x16x32_bf16 v[220:223], v[148:151], v[172:175], v[220:223]\n"
    "v_mul_f32 v214, v214, v252\n"
    "v_mul_f32 v215, v215, v252\n"
    "ds_write_b32 v226, v227 offset:32\n"
    "v_cvt_pk_bf16_f32 v208, v208, v209\n"
    "v_cvt_pk_bf16_f32 v209, v210, v211\n"
    "v_cvt_pk_bf16_f32 v210, v212, v213\n"
    "v_cvt_pk_bf16_f32 v211, v214, v215\n"
    "buffer_store_dwordx4 v[208:211], v4, s[80:83], 0 offen offset:64 sc1\n"
    ".Lh8j:\n"
    "s_waitcnt lgkmcnt(2)\n"
    "v_mfma_f32_16x16x32_bf16 v[200:203], v[8:11], v[176:179], 0\n"
    "v_mul_f32 v216, v216, v252\n"
    "v_mul_f32 v217, v217, v252\n"
    "v_mfma_f32_16x16x32_bf16 v[204:207], v[32:35], v[176:179], 0\n"
    "v_mul_f32 v218, v218, v252\n"
    "v_mul_f32 v219, v219, v252\n"
    "v_mfma_f32_16x16x32_bf16 v[200:203], v[12:15], v[180:183], v[200:203]\n"
    "v_mul_f32 v220, v220, v252\n"
    "v_mul_f32 v221, v221, v252\n"
    "v_mfma_f32_16x16x32_bf16 v[204:207], v[36:39], v[180:183], v[204:207]\n"
    "v_mul_f32 v222, v222, v252\n"
    "v_mul_f32 v223, v223, v252\n"
    "v_mfma_f32_16x16x32_bf16 v[200:203], v[16:19], v[184:187], v[200:203]\n"
    "v_cvt_pk_bf16_f32 v216, v216, v217\n"
    "v_cvt_pk_bf16_f32 v217, v218, v219\n"
    "v_mfma_f32_16x16x32_bf16 v[204:207], v[40:43], v[184:187], v[204:207]\n"
    "v_cvt_pk_bf16_f32 v218, v220, v221\n"
    "v_cvt_pk_bf16_f32 v219, v222, v223\n"
    "v_mfma_f32_16x16x32_bf16 v[200:203], v[20:23], v[188:191], v[200:203]\n"
    "buffer_store_dwordx4 v[216:219], v4, s[80:83], 0 offen offset:128 sc1\n"
    "v_mfma_f32_16x16x32_bf16 v[204:207], v[44:47], v[188:191], v[204:207]\n"
    "v_mfma_f32_16x16x32_bf16 v[200:203], v[24:27], v[192:195], v[200:203]\n"
    "v_add_u32 v4, s46, v4\n"
    "v_mfma_f32_16x16x32_bf16 v[204:207], v[48:51], v[192:195], v[204:207]\n"
    "v_mfma_f32_16x16x32_bf16 v[200:203], v[28:31], v[196:199], v[200:203]\n"
    "v_mfma_f32_16x16x32_bf16 v[204:207], v[52:55], v[196:199], v[204:207]\n"
    "v_mfma_f32_16x16x32_bf16 v[208:211], v[56:59], v[176:179], 0\n"
    "v_mfma_f32_16x16x32_bf16 v[212:215], v[80:83], v[176:179], 0\n"
    "ds_read_b32 v228, v224 offset:160\n"
    "v_mfma_f32_16x16x32_bf16 v[208:211], v[60:63], v[180:183], v[208:211]\n"
    "v_mfma_f32_16x16x32_bf16 v[212:215], v[84:87], v[180:183], v[212:215]\n"
    "v_mfma_f32_16x16x32_bf16 v[208:211], v[64:67], v[184:187], v[208:211]\n"
    "s_waitcnt lgkmcnt(2)\n"
    "v_mul_f32 v200, v200, v253\n"
    "v_mfma_f32_16x16x32_bf16 v[212:215], v[88:91], v[184:187], v[212:215]\n"
    "v_mul_f32 v201, v201, v253\n"
    "v_mul_f32 v202, v202, v253\n"
    "v_mfma_f32_16x16x32_bf16 v[208:211], v[68:71], v[188:191], v[208:211]\n"
    "v_mul_f32 v203, v203, v253\n"
    "v_mul_f32 v204, v204, v253\n"
    "v_mfma_f32_16x16x32_bf16 v[212:215], v[92:95], v[188:191], v[212:215]\n"
    "v_mul_f32 v205, v205, v253\n"
    "v_mul_f32 v206, v206, v253\n"
    "v_mfma_f32_16x16x32_bf16 v[208:211], v[72:75], v[192:195], v[208:211]\n"
    "v_mul_f32 v207, v207, v253\n"
    "v_cvt_pk_bf16_f32 v200, v200, v201\n"
    "v_mfma_f32_16x16x32_bf16 v[212:215], v[96:99], v[192:195], v[212:215]\n"
    "v_cvt_pk_bf16_f32 v201, v202, v203\n"
    "v_cvt_pk_bf16_f32 v202, v204, v205\n"
    "v_mfma_f32_16x16x32_bf16 v[208:211], v[76:79], v[196:199], v[208:211]\n"
    "s_waitcnt lgkmcnt(0)\n"
    "v_cmp_gt_u32 vcc, 2, v228\n"
    "v_mfma_f32_16x16x32_bf16 v[212:215], v[100:103], v[196:199], v[212:215]\n"
    "v_cvt_pk_bf16_f32 v203, v206, v207\n"
    "s_cbranch_vccnz .Lh9\n"
    "v_mfma_f32_16x16x32_bf16 v[216:219], v[104:107], v[176:179], 0\n"
    "ds_read_b64_tr_b16 v[152:153], v2 offset:18432\n"
    "ds_read_b64_tr_b16 v[154:155], v3 offset:18432\n"
    "v_mfma_f32_16x16x32_bf16 v[220:223], v[128:131], v[176:179], 0\n"
    "ds_read_b64_tr_b16 v[156:157], v2 offset:19456\n"
    "ds_read_b64_tr_b16 v[158:159], v3 offset:19456\n"
    "v_mfma_f32_16x16x32_bf16 v[216:219], v[108:111], v[180:183], v[216:219]\n"
    "ds_read_b64_tr_b16 v[160:161], v2 offset:20480\n"
    "ds_read_b64_tr_b16 v[162:163], v3 offset:20480\n"
    "v_mfma_f32_16x16x32_bf16 v[220:223], v[132:135], v[180:183], v[220:223]\n"
    "ds_read_b64_tr_b16 v[164:165], v2 offset:21504\n"
    "ds_read_b64_tr_b16 v[166:167], v3 offset:21504\n"
    "v_mfma_f32_16x16x32_bf16 v[216:219], v[112:115], v[184:187], v[216:219]\n"
    "buffer_store_dwordx4 v[200:203], v5, s[80:83], 0 offen offset:0 sc1\n"
    "v_mfma_f32_16x16x32_bf16 v[220:223], v[136:139], v[184:187], v[220:223]\n"
    "v_mul_f32 v208, v208, v253\n"
    "v_mul_f32 v209, v209, v253\n"
    "v_mfma_f32_16x16x32_bf16 v[216:219], v[116:119], v[188:191], v[216:219]\n"
    "ds_read_b64_tr_b16 v[168:169], v2 offset:22528\n"
    "ds_read_b64_tr_b16 v[170:171], v3 offset:22528\n"
    "v_mfma_f32_16x16x32_bf16 v[220:223], v[140:143], v[188:191], v[220:223]\n"
    "v_mul_f32 v210, v210, v253\n"
    "v_mul_f32 v211, v211, v253\n"
    "v_mfma_f32_16x16x32_bf16 v[216:219], v[120:123], v[192:195], v[216:219]\n"
    "ds_read_b64_tr_b16 v[172:173], v2 offset:23552\n"
    "ds_read_b64_tr_b16 v[174:175], v3 offset:23552\n"
    "v_mfma_f32_16x16x32_bf16 v[220:223], v[144:147], v[192:195], v[220:223]\n"
    "v_mul_f32 v212, v212, v253\n"
    "v_mov_b32 v227, 7\n"
    "v_mfma_f32_16x16x32_bf16 v[216:219], v[124:127], v[196:199], v[216:219]\n"
    "ds_read_b32 v252, v1 offset:0\n"
    "v_mul_f32 v213, v213, v253\n"
    "v_mfma_f32_16x16x32_bf16 v[220:223], v[148:151], v[196:199], v[220:223]\n"
    "v_mul_f32 v214, v214, v253\n"
    "v_mul_f32 v215, v215, v253\n"
    "ds_write_b32 v226, v227 offset:32\n"
    "v_cvt_pk_bf16_f32 v208, v208, v209\n"
    "v_cvt_pk_bf16_f32 v209, v210, v211\n"
    "v_cvt_pk_bf16_f32 v210, v212, v213\n"
    "v_cvt_pk_bf16_f32 v211, v214, v215\n"
    "buffer_store_dwordx4 v[208:211], v5, s[80:83], 0 offen offset:64 sc1\n"
    ".Lh9j:\n"
    "s_waitcnt lgkmcnt(2)\n"
    "v_mfma_f32_16x16x32_bf16 v[200:203], v[8:11], v[152:155], 0\n"
    "v_mul_f32 v216, v216, v253\n"
    "v_mul_f32 v217, v217, v253\n"
    "v_mfma_f32_16x16x32_bf16 v[204:207], v[32:35], v[152:155], 0\n"
    "v_mul_f32 v218, v218, v253\n"
    "v_mul_f32 v219, v219, v253\n"
    "v_mfma_f32_16x16x32_bf16 v[200:203], v[12:15], v[156:159], v[200:203]\n"
    "v_mul_f32 v220, v220, v253\n"
    "v_mul_f32 v221, v221, v253\n"
    "v_mfma_f32_16x16x32_bf16 v[204:207], v[36:39], v[156:159], v[204:207]\n"
    "v_mul_f32 v222, v222, v253\n"
    "v_mul_f32 v223, v223, v253\n"
    "v_mfma_f32_16x16x32_bf16 v[200:203], v[16:19], v[160:163], v[200:203]\n"
    "v_cvt_pk_bf16_f32 v216, v216, v217\n"
    "v_cvt_pk_bf16_f32 v217, v218, v219\n"
    "v_mfma_f32_16x16x32_bf16 v[204:207], v[40:43], v[160:163], v[204:207]\n"
    "v_cvt_pk_bf16_f32 v218, v220, v221\n"
    "v_cvt_pk_bf16_f32 v219, v222, v223\n"
    "v_mfma_f32_16x16x32_bf16 v[200:203], v[20:23], v[164:167], v[200:203]\n"
    "buffer_store_dwordx4 v[216:219], v5, s[80:83], 0 offen offset:128 sc1\n"
    "v_mfma_f32_16x16x32_bf16 v[204:207], v[44:47], v[164:167], v[204:207]\n"
    "v_mfma_f32_16x16x32_bf16 v[200:203], v[24:27], v[168:171], v[200:203]\n"
    "v_add_u32 v5, s46, v5\n"
    "v_mfma_f32_16x16x32_bf16 v[204:207], v[48:51], v[168:171], v[204:207]\n"
    "v_mfma_f32_16x16x32_bf16 v[200:203], v[28:31], v[172:175], v[200:203]\n"
    "v_mfma_f32_16x16x32_bf16 v[204:207], v[52:55], v[172:175], v[204:207]\n"
    "v_mfma_f32_16x16x32_bf16 v[208:211], v[56:59], v[152:155], 0\n"
    "v_mfma_f32_16x16x32_bf16 v[212:215], v[80:83], v[152:155], 0\n"
    "ds_read_b32 v228, v224 offset:176\n"
    "v_mfma_f32_16x16x32_bf16 v[208:211], v[60:63], v[156:159], v[208:211]\n"
    "v_mfma_f32_16x16x32_bf16 v[212:215], v[84:87], v[156:159], v[212:215]\n"
    "v_mfma_f32_16x16x32_bf16 v[208:211], v[64:67], v[160:163], v[208:211]\n"
    "s_waitcnt lgkmcnt(2)\n"
    "v_mul_f32 v200, v200, v252\n"
    "v_mfma_f32_16x16x32_bf16 v[212:215], v[88:91], v[160:163], v[212:215]\n"
    "v_mul_f32 v201, v201, v252\n"
    "v_mul_f32 v202, v202, v252\n"
    "v_mfma_f32_16x16x32_bf16 v[208:211], v[68:71], v[164:167], v[208:211]\n"
    "v_mul_f32 v203, v203, v252\n"
    "v_mul_f32 v204, v204, v252\n"
    "v_mfma_f32_16x16x32_bf16 v[212:215], v[92:95], v[164:167], v[212:215]\n"
    "v_mul_f32 v205, v205, v252\n"
    "v_mul_f32 v206, v206, v252\n"
    "v_mfma_f32_16x16x32_bf16 v[208:211], v[72:75], v[168:171], v[208:211]\n"
    "v_mul_f32 v207, v207, v252\n"
    "v_cvt_pk_bf16_f32 v200, v200, v201\n"
    "v_mfma_f32_16x16x32_bf16 v[212:215], v[96:99], v[168:171], v[212:215]\n"
    "v_cvt_pk_bf16_f32 v201, v202, v203\n"
    "v_cvt_pk_bf16_f32 v202, v204, v205\n"
    "v_mfma_f32_16x16x32_bf16 v[208:211], v[76:79], v[172:175], v[208:211]\n"
    "s_waitcnt lgkmcnt(0)\n"
    "v_cmp_gt_u32 vcc, 2, v228\n"
    "v_mfma_f32_16x16x32_bf16 v[212:215], v[100:103], v[172:175], v[212:215]\n"
    "v_cvt_pk_bf16_f32 v203, v206, v207\n"
    "s_cbranch_vccnz .Lh10\n"
    "v_mfma_f32_16x16x32_bf16 v[216:219], v[104:107], v[152:155], 0\n"
    "ds_read_b64_tr_b16 v[176:177], v2 offset:24576\n"
    "ds_read_b64_tr_b16 v[178:179], v3 offset:24576\n"
    "v_mfma_f32_16x16x32_bf16 v[220:223], v[128:131], v[152:155], 0\n"
    "ds_read_b64_tr_b16 v[180:181], v2 offset:25600\n"
    "ds_read_b64_tr_b16 v[182:183], v3 offset:25600\n"
    "v_mfma_f32_16x16x32_bf16 v[216:219], v[108:111], v[156:159], v[216:219]\n"
    "ds_read_b64_tr_b16 v[184:185], v2 offset:26624\n"
    "ds_read_b64_tr_b16 v[186:187], v3 offset:26624\n"
    "v_mfma_f32_16x16x32_bf16 v[220:223], v[132:135], v[156:159], v[220:223]\n"
    "ds_read_b64_tr_b16 v[188:189], v2 offset:27648\n"
    "ds_read_b64_tr_b16 v[190:191], v3 offset:27648\n"
    "v_mfma_f32_16x16x32_bf16 v[216:219], v[112:115], v[160:163], v[216:219]\n"
    "buffer_store_dwordx4 v[200:203], v4, s[80:83], 0 offen offset:0 sc1\n"
    "v_mfma_f32_16x16x32_bf16 v[220:223], v[136:139], v[160:163], v[220:223]\n"
    "v_mul_f32 v208, v208, v252\n"
    "v_mul_f32 v209, v209, v252\n"
    "v_mfma_f32_16x16x32_bf16 v[216:219], v[116:119], v[164:167], v[216:219]\n"
    "ds_read_b64_tr_b16 v[192:193], v2 offset:28672\n"
    "ds_read_b64_tr_b16 v[194:195], v3 offset:28672\n"
    "v_mfma_f32_16x16x32_bf16 v[220:223], v[140:143], v[164:167], v[220:223]\n"
    "v_mul_f32 v210, v210, v252\n"
    "v_mul_f32 v211, v211, v252\n"
    "v_mfma_f32_16x16x32_bf16 v[216:219], v[120:123], v[168:171], v[216:219]\n"
    "ds_read_b64_tr_b16 v[196:197], v2 offset:29696\n"
    "ds_read_b64_tr_b16 v[198:199], v3 offset:29696\n"
    "v_mfma_f32_16x16x32_bf16 v[220:223], v[144:147], v[168:171], v[220:223]\n"
    "v_mul_f32 v212, v212, v252\n"
    "v_mul_f32 v213, v213, v252\n"
    "v_mfma_f32_16x16x32_bf16 v[216:219], v[124:127], v[172:175], v[216:219]\n"
    "ds_read_b32 v253, v1 offset:64\n"
    "v_mul_f32 v214, v214, v252\n"
    "v_mfma_f32_16x16x32_bf16 v[220:223], v[148:151], v[172:175], v[220:223]\n"
    "v_mul_f32 v215, v215, v252\n"
    "v_cvt_pk_bf16_f32 v208, v208, v209\n"
    "v_cvt_pk_bf16_f32 v209, v210, v211\n"
    "v_cvt_pk_bf16_f32 v210, v212, v213\n"
    "v_cvt_pk_bf16_f32 v211, v214, v215\n"
    "buffer_store_dwordx4 v[208:211], v4, s[80:83], 0 offen offset:64 sc1\n"
    ".Lh10j:\n"
    "s_waitcnt lgkmcnt(1)\n"
    "v_mfma_f32_16x16x32_bf16 v[200:203], v[8:11], v[176:179], 0\n"
    "v_mul_f32 v216, v216, v252\n"
    "v_mul_f32 v217, v217, v252\n"
    "v_mfma_f32_16x16x32_bf16 v[204:207], v[32:35], v[176:179], 0\n"
    "v_mul_f32 v218, v218, v252\n"
    "v_mul_f32 v219, v219, v252\n"
    "v_mfma_f32_16x16x32_bf16 v[200:203], v[12:15], v[180:183], v[200:203]\n"
    "v_mul_f32 v220, v220, v252\n"
    "v_mul_f32 v221, v221, v252\n"
    "v_mfma_f32_16x16x32_bf16 v[204:207], v[36:39], v[180:183], v[204:207]\n"
    "v_mul_f32 v222, v222, v252\n"
    "v_mul_f32 v223, v223, v252\n"
    "v_mfma_f32_16x16x32_bf16 v[200:203], v[16:19], v[184:187], v[200:203]\n"
    "v_cvt_pk_bf16_f32 v216, v216, v217\n"
    "v_cvt_pk_bf16_f32 v217, v218, v219\n"
    "v_mfma_f32_16x16x32_bf16 v[204:207], v[40:43], v[184:187], v[204:207]\n"
    "v_cvt_pk_bf16_f32 v218, v220, v221\n"
    "v_cvt_pk_bf16_f32 v219, v222, v223\n"
    "v_mfma_f32_16x16x32_bf16 v[200:203], v[20:23], v[188:191], v[200:203]\n"
    "buffer_store_dwordx4 v[216:219], v4, s[80:83], 0 offen offset:128 sc1\n"
    "v_mfma_f32_16x16x32_bf16 v[204:207], v[44:47], v[188:191], v[204:207]\n"
    "v_mfma_f32_16x16x32_bf16 v[200:203], v[24:27], v[192:195], v[200:203]\n"
    "v_add_u32 v4, v6, v4\n"
    "v_mfma_f32_16x16x32_bf16 v[204:207], v[48:51], v[192:195], v[204:207]\n"
    "v_mfma_f32_16x16x32_bf16 v[200:203], v[28:31], v[196:199], v[200:203]\n"
    "v_mfma_f32_16x16x32_bf16 v[204:207], v[52:55], v[196:199], v[204:207]\n"
    "v_mfma_f32_16x16x32_bf16 v[208:211], v[56:59], v[176:179], 0\n"
    "v_mfma_f32_16x16x32_bf16 v[212:215], v[80:83], v[176:179], 0\n"
    "ds_read_b32 v228, v224 offset:128\n"
    "v_mfma_f32_16x16x32_bf16 v[208:211], v[60:63], v[180:183], v[208:211]\n"
    "v_mfma_f32_16x16x32_bf16 v[212:215], v[84:87], v[180:183], v[212:215]\n"
    "v_mfma_f32_16x16x32_bf16 v[208:211], v[64:67], v[184:187], v[208:211]\n"
    "s_waitcnt lgkmcnt(1)\n"
    "v_mul_f32 v200, v200, v253\n"
    "v_mfma_f32_16x16x32_bf16 v[212:215], v[88:91], v[184:187], v[212:215]\n"
    "v_mul_f32 v201, v201, v253\n"
    "v_mul_f32 v202, v202, v253\n"
    "v_mfma_f32_16x16x32_bf16 v[208:211], v[68:71], v[188:191], v[208:211]\n"
    "v_mul_f32 v203, v203, v253\n"
    "v_mul_f32 v204, v204, v253\n"
    "v_mfma_f32_16x16x32_bf16 v[212:215], v[92:95], v[188:191], v[212:215]\n"
    "v_mul_f32 v205, v205, v253\n"
    "v_mul_f32 v206, v206, v253\n"
    "v_mfma_f32_16x16x32_bf16 v[208:211], v[72:75], v[192:195], v[208:211]\n"
    "v_mul_f32 v207, v207, v253\n"
    "v_cvt_pk_bf16_f32 v200, v200, v201\n"
    "v_mfma_f32_16x16x32_bf16 v[212:215], v[96:99], v[192:195], v[212:215]\n"
    "v_cvt_pk_bf16_f32 v201, v202, v203\n"
    "v_cvt_pk_bf16_f32 v202, v204, v205\n"
    "v_mfma_f32_16x16x32_bf16 v[208:211], v[76:79], v[196:199], v[208:211]\n"
    "s_waitcnt lgkmcnt(0)\n"
    "v_cmp_gt_u32 vcc, 3, v228\n"
    "v_mfma_f32_16x16x32_bf16 v[212:215], v[100:103], v[196:199], v[212:215]\n"
    "v_cvt_pk_bf16_f32 v203, v206, v207\n"
    "s_cbranch_vccnz .Lh11\n"
    "v_mfma_f32_16x16x32_bf16 v[216:219], v[104:107], v[176:179], 0\n"
    "ds_read_b64_tr_b16 v[152:153], v2 offset:30720\n"
    "ds_read_b64_tr_b16 v[154:155], v3 offset:30720\n"
    "v_mfma_f32_16x16x32_bf16 v[220:223], v[128:131], v[176:179], 0\n"
    "ds_read_b64_tr_b16 v[156:157], v2 offset:31744\n"
    "ds_read_b64_tr_b16 v[158:159], v3 offset:31744\n"
    "v_mfma_f32_16x16x32_bf16 v[216:219], v[108:111], v[180:183], v[216:219]\n"
    "ds_read_b64_tr_b16 v[160:161], v2 offset:32768\n"
    "ds_read_b64_tr_b16 v[162:163], v3 offset:32768\n"
    "v_mfma_f32_16x16x32_bf16 v[220:223], v[132:135], v[180:183], v[220:223]\n"
    "ds_read_b64_tr_b16 v[164:165], v2 offset:33792\n"
    "ds_read_b64_tr_b16 v[166:167], v3 offset:33792\n"
    "v_mfma_f32_16x16x32_bf16 v[216:219], v[112:115], v[184:187], v[216:219]\n"
    "buffer_store_dwordx4 v[200:203], v5, s[80:83], 0 offen offset:0 sc1\n"
    "v_mfma_f32_16x16x32_bf16 v[220:223], v[136:139], v[184:187], v[220:223]\n"
    "v_mul_f32 v208, v208, v253\n"
    "v_mul_f32 v209, v209, v253\n"
    "v_mfma_f32_16x16x32_bf16 v[216:219], v[116:119], v[188:191], v[216:219]\n"
    "ds_read_b64_tr_b16 v[168:169], v2 offset:34816\n"
    "ds_read_b64_tr_b16 v[170:171], v3 offset:34816\n"
    "v_mfma_f32_16x16x32_bf16 v[220:223], v[140:143], v[188:191], v[220:223]\n"
    "v_mul_f32 v210, v210, v253\n"
    "v_mul_f32 v211, v211, v253\n"
    "v_mfma_f32_16x16x32_bf16 v[216:219], v[120:123], v[192:195], v[216:219]\n"
    "ds_read_b64_tr_b16 v[172:173], v2 offset:35840\n"
    "ds_read_b64_tr_b16 v[174:175], v3 offset:35840\n"
    "v_mfma_f32_16x16x32_bf16 v[220:223], v[144:147], v[192:195], v[220:223]\n"
    "v_mul_f32 v212, v212, v253\n"
    "v_mul_f32 v213, v213, v253\n"
    "v_mfma_f32_16x16x32_bf16 v[216:219], v[124:127], v[196:199], v[216:219]\n"
    "ds_read_b32 v252, v1 offset:128\n"
    "v_mul_f32 v214, v214, v253\n"
    "v_mfma_f32_16x16x32_bf16 v[220:223], v[148:151], v[196:199], v[220:223]\n"
    "v_mul_f32 v215, v215, v253\n"
    "v_cvt_pk_bf16_f32 v208, v208, v209\n"
    "v_cvt_pk_bf16_f32 v209, v210, v211\n"
    "v_cvt_pk_bf16_f32 v210, v212, v213\n"
    "v_cvt_pk_bf16_f32 v211, v214, v215\n"
    "buffer_store_dwordx4 v[208:211], v5, s[80:83], 0 offen offset:64 sc1\n"
    ".Lh11j:\n"
    "s_waitcnt lgkmcnt(1)\n"
    "v_mfma_f32_16x16x32_bf16 v[200:203], v[8:11], v[152:155], 0\n"
    "v_mul_f32 v216, v216, v253\n"
    "v_mul_f32 v217, v217, v253\n"
    "v_mfma_f32_16x16x32_bf16 v[204:207], v[32:35], v[152:155], 0\n"
    "ds_read_b32 v228, v224 offset:144\n"
    "v_mul_f32 v218, v218, v253\n"
    "v_mfma_f32_16x16x32_bf16 v[200:203], v[12:15], v[156:159], v[200:203]\n"
    "v_mul_f32 v219, v219, v253\n"
    "v_mul_f32 v220, v220, v253\n"
    "v_mfma_f32_16x16x32_bf16 v[204:207], v[36:39], v[156:159], v[204:207]\n"
    "v_mul_f32 v221, v221, v253\n"
    "v_mul_f32 v222, v222, v253\n"
    "v_mfma_f32_16x16x32_bf16 v[200:203], v[16:19], v[160:163], v[200:203]\n"
    "v_mul_f32 v223, v223, v253\n"
    "v_cvt_pk_bf16_f32 v216, v216, v217\n"
    "v_mfma_f32_16x16x32_bf16 v[204:207], v[40:43], v[160:163], v[204:207]\n"
    "v_cvt_pk_bf16_f32 v217, v218, v219\n"
    "v_cvt_pk_bf16_f32 v218, v220, v221\n"
    "v_mfma_f32_16x16x32_bf16 v[200:203], v[20:23], v[164:167], v[200:203]\n"
    "v_cvt_pk_bf16_f32 v219, v222, v223\n"
    "v_mfma_f32_16x16x32_bf16 v[204:207], v[44:47], v[164:167], v[204:207]\n"
    "buffer_store_dwordx4 v[216:219], v5, s[80:83], 0 offen offset:128 sc1\n"
    "v_mfma_f32_16x16x32_bf16 v[200:203], v[24:27], v[168:171], v[200:203]\n"
    "v_mfma_f32_16x16x32_bf16 v[204:207], v[48:51], v[168:171], v[204:207]\n"
    "v_add_u32 v5, v6, v5\n"
    "v_mfma_f32_16x16x32_bf16 v[200:203], v[28:31], v[172:175], v[200:203]\n"
    "s_waitcnt lgkmcnt(0)\n"
    "v_cmp_gt_u32 vcc, 3, v228\n"
    "v_mfma_f32_16x16x32_bf16 v[204:207], v[52:55], v[172:175], v[204:207]\n"
    "s_cbranch_vccnz .Lh12\n"
    "v_mfma_f32_16x16x32_bf16 v[208:211], v[56:59], v[152:155], 0\n"
    "ds_read_b64_tr_b16 v[176:177], v2 offset:18432\n"
    "ds_read_b64_tr_b16 v[178:179], v3 offset:18432\n"
    "v_mfma_f32_16x16x32_bf16 v[212:215], v[80:83], v[152:155], 0\n"
    "ds_read_b64_tr_b16 v[180:181], v2 offset:19456\n"
    "ds_read_b64_tr_b16 v[182:183], v3 offset:19456\n"
    "v_mfma_f32_16x16x32_bf16 v[208:211], v[60:63], v[156:159], v[208:211]\n"
    "ds_read_b64_tr_b16 v[184:185], v2 offset:20480\n"
    "ds_read_b64_tr_b16 v[186:187], v3 offset:20480\n"
    "v_mfma_f32_16x16x32_bf16 v[212:215], v[84:87], v[156:159], v[212:215]\n"
    "ds_read_b64_tr_b16 v[188:189], v2 offset:21504\n"
    "ds_read_b64_tr_b16 v[190:191], v3 offset:21504\n"
    "v_mfma_f32_16x16x32_bf16 v[208:211], v[64:67], v[160:163], v[208:211]\n"
    "v_mul_f32 v200, v200, v252\n"
    "v_mul_f32 v201, v201, v252\n"
    "v_mfma_f32_16x16x32_bf16 v[212:215], v[88:91], v[160:163], v[212:215]\n"
    "ds_read_b64_tr_b16 v[192:193], v2 offset:22528\n"
    "ds_read_b64_tr_b16 v[194:195], v3 offset:22528\n"
    "v_mfma_f32_16x16x32_bf16 v[208:211], v[68:71], v[164:167], v[208:211]\n"
    "v_mul_f32 v202, v202, v252\n"
    "v_mul_f32 v203, v203, v252\n"
    "v_mfma_f32_16x16x32_bf16 v[212:215], v[92:95], v[164:167], v[212:215]\n"
    "ds_read_b64_tr_b16 v[196:197], v2 offset:23552\n"
    "ds_read_b64_tr_b16 v[198:199], v3 offset:23552\n"
    "v_mfma_f32_16x16x32_bf16 v[208:211], v[72:75], v[168:171], v[208:211]\n"
    "v_mul_f32 v204, v204, v252\n"
    "v_mul_f32 v205, v205, v252\n"
    "v_mfma_f32_16x16x32_bf16 v[212:215], v[96:99], v[168:171], v[212:215]\n"
    "ds_read_b32 v253, v1 offset:0\n"
    "v_mul_f32 v206, v206, v252\n"
    "v_mfma_f32_16x16x32_bf16 v[208:211], v[76:79], v[172:175], v[208:211]\n"
    "v_mul_f32 v207, v207, v252\n"
    "v_cvt_pk_bf16_f32 v200, v200, v201\n"
    "v_mfma_f32_16x16x32_bf16 v[212:215], v[100:103], v[172:175], v[212:215]\n"
    "v_cvt_pk_bf16_f32 v201, v202, v203\n"
    "v_cvt_pk_bf16_f32 v202, v204, v205\n"
    "v_cvt_pk_bf16_f32 v203, v206, v207\n"
    "buffer_store_dwordx4 v[200:203], v4, s[80:83], 0 offen offset:0 sc1\n"
    ".Lh12j:\n"
    "s_waitcnt lgkmcnt(1)\n"
    "s_nop 0\n"
    "v_mfma_f32_16x16x32_bf16 v[200:203], v[8:11], v[176:179], 0\n"
    "v_mul_f32 v208, v208, v252\n"
    "v_mul_f32 v209, v209, v252\n"
    "v_mfma_f32_16x16x32_bf16 v[204:207], v[32:35], v[176:179], 0\n"
    "ds_read_b32 v228, v224 offset:160\n"
    "v_mul_f32 v210, v210, v252\n"
    "v_mfma_f32_16x16x32_bf16 v[200:203], v[12:15], v[180:183], v[200:203]\n"
    "v_mul_f32 v211, v211, v252\n"
    "v_mul_f32 v212, v212, v252\n"
    "v_mfma_f32_16x16x32_bf16 v[204:207], v[36:39], v[180:183], v[204:207]\n"
    "v_mul_f32 v213, v213, v252\n"
    "v_mul_f32 v214, v214, v252\n"
    "v_mfma_f32_16x16x32_bf16 v[200:203], v[16:19], v[184:187], v[200:203]\n"
    "v_mul_f32 v215, v215, v252\n"
    "v_cvt_pk_bf16_f32 v208, v208, v209\n"
    "v_mfma_f32_16x16x32_bf16 v[204:207], v[40:43], v[184:187], v[204:207]\n"
    "v_cvt_pk_bf16_f32 v209, v210, v211\n"
    "v_cvt_pk_bf16_f32 v210, v212, v213\n"
    "v_mfma_f32_16x16x32_bf16 v[200:203], v[20:23], v[188:191], v[200:203]\n"
    "v_cvt_pk_bf16_f32 v211, v214, v215\n"
    "v_mfma_f32_16x16x32_bf16 v[204:207], v[44:47], v[188:191], v[204:207]\n"
    "buffer_store_dwordx4 v[208:211], v4, s[80:83], 0 offen offset:64 sc1\n"
    "v_mfma_f32_16x16x32_bf16 v[200:203], v[24:27], v[192:195], v[200:203]\n"
    "v_mfma_f32_16x16x32_bf16 v[204:207], v[48:51], v[192:195], v[204:207]\n"
    "v_add_u32 v4, v7, v4\n"
    "v_mfma_f32_16x16x32_bf16 v[200:203], v[28:31], v[196:199], v[200:203]\n"
    "s_waitcnt lgkmcnt(0)\n"
    "v_cmp_gt_u32 vcc, 3, v228\n"
    "v_mfma_f32_16x16x32_bf16 v[204:207], v[52:55], v[196:199], v[204:207]\n"
    "s_cbranch_vccnz .Lh13\n"
    "v_mfma_f32_16x16x32_bf16 v[208:211], v[56:59], v[176:179], 0\n"
    "ds_read_b64_tr_b16 v[152:153], v2 offset:6144\n"
    "ds_read_b64_tr_b16 v[154:155], v3 offset:6144\n"
    "v_mfma_f32_16x16x32_bf16 v[212:215], v[80:83], v[176:179], 0\n"
    "ds_read_b64_tr_b16 v[156:157], v2 offset:7168\n"
    "ds_read_b64_tr_b16 v[158:159], v3 offset:7168\n"
    "v_mfma_f32_16x16x32_bf16 v[208:211], v[60:63], v[180:183], v[208:211]\n"
    "ds_read_b64_tr_b16 v[160:161], v2 offset:8192\n"
    "ds_read_b64_tr_b16 v[162:163], v3 offset:8192\n"
    "v_mfma_f32_16x16x32_bf16 v[212:215], v[84:87], v[180:183], v[212:215]\n"
    "ds_read_b64_tr_b16 v[164:165], v2 offset:9216\n"
    "ds_read_b64_tr_b16 v[166:167], v3 offset:9216\n"
    "v_mfma_f32_16x16x32_bf16 v[208:211], v[64:67], v[184:187], v[208:211]\n"
    "v_mul_f32 v200, v200, v253\n"
    "v_mul_f32 v201, v201, v253\n"
    "v_mfma_f32_16x16x32_bf16 v[212:215], v[88:91], v[184:187], v[212:215]\n"
    "ds_read_b64_tr_b16 v[168:169], v2 offset:10240\n"
    "ds_read_b64_tr_b16 v[170:171], v3 offset:10240\n"
    "v_mfma_f32_16x16x32_bf16 v[208:211], v[68:71], v[188:191], v[208:211]\n"
    "v_mul_f32 v202, v202, v253\n"
    "v_mul_f32 v203, v203, v253\n"
    "v_mfma_f32_16x16x32_bf16 v[212:215], v[92:95], v[188:191], v[212:215]\n"
    "ds_read_b64_tr_b16 v[172:173], v2 offset:11264\n"
    "ds_read_b64_tr_b16 v[174:175], v3 offset:11264\n"
    "v_mfma_f32_16x16x32_bf16 v[208:211], v[72:75], v[192:195], v[208:211]\n"
    "v_mul_f32 v204, v204, v253\n"
    "v_mul_f32 v205, v205, v253\n"
    "v_mfma_f32_16x16x32_bf16 v[212:215], v[96:99], v[192:195], v[212:215]\n"
    "ds_read_b32 v252, v1 offset:256\n"
    "v_mul_f32 v206, v206, v253\n"
    "v_mfma_f32_16x16x32_bf16 v[208:211], v[76:79], v[196:199], v[208:211]\n"
    "v_mul_f32 v207, v207, v253\n"
    "v_cvt_pk_bf16_f32 v200, v200, v201\n"
    "v_mfma_f32_16x16x32_bf16 v[212:215], v[100:103], v[196:199], v[212:215]\n"
    "v_cvt_pk_bf16_f32 v201, v202, v203\n"
    "v_cvt_pk_bf16_f32 v202, v204, v205\n"
    "v_cvt_pk_bf16_f32 v203, v206, v207\n"
    "buffer_store_dwordx4 v[200:203], v5, s[80:83], 0 offen offset:0 sc1\n"
    ".Lh13j:\n"
    "s_waitcnt lgkmcnt(1)\n"
    "s_nop 0\n"
    "v_mfma_f32_16x16x32_bf16 v[200:203], v[8:11], v[152:155], 0\n"
    "v_mul_f32 v208, v208, v253\n"
    "v_mul_f32 v209, v209, v253\n"
    "v_mfma_f32_16x16x32_bf16 v[204:207], v[32:35], v[152:155], 0\n"
    "ds_read_b32 v228, v224 offset:176\n"
    "v_mul_f32 v210, v210, v253\n"
    "v_mfma_f32_16x16x32_bf16 v[200:203], v[12:15], v[156:159], v[200:203]\n"
    "v_mul_f32 v211, v211, v253\n"
    "v_mul_f32 v212, v212, v253\n"
    "v_mfma_f32_16x16x32_bf16 v[204:207], v[36:39], v[156:159], v[204:207]\n"
    "v_mul_f32 v213, v213, v253\n"
    "v_mul_f32 v214, v214, v253\n"
    "v_mfma_f32_16x16x32_bf16 v[200:203], v[16:19], v[160:163], v[200:203]\n"
    "v_mul_f32 v215, v215, v253\n"
    "v_cvt_pk_bf16_f32 v208, v208, v209\n"
    "v_mfma_f32_16x16x32_bf16 v[204:207], v[40:43], v[160:163], v[204:207]\n"
    "v_cvt_pk_bf16_f32 v209, v210, v211\n"
    "v_cvt_pk_bf16_f32 v210, v212, v213\n"
    "v_mfma_f32_16x16x32_bf16 v[200:203], v[20:23], v[164:167], v[200:203]\n"
    "v_cvt_pk_bf16_f32 v211, v214, v215\n"
    "v_mfma_f32_16x16x32_bf16 v[204:207], v[44:47], v[164:167], v[204:207]\n"
    "buffer_store_dwordx4 v[208:211], v5, s[80:83], 0 offen offset:64 sc1\n"
    "v_mfma_f32_16x16x32_bf16 v[200:203], v[24:27], v[168:171], v[200:203]\n"
    "v_mfma_f32_16x16x32_bf16 v[204:207], v[48:51], v[168:171], v[204:207]\n"
    "v_add_u32 v5, v7, v5\n"
    "v_mfma_f32_16x16x32_bf16 v[200:203], v[28:31], v[172:175], v[200:203]\n"
    "s_waitcnt lgkmcnt(0)\n"
    "v_cmp_gt_u32 vcc, 3, v228\n"
    "v_mfma_f32_16x16x32_bf16 v[204:207], v[52:55], v[172:175], v[204:207]\n"
    "s_cbranch_vccnz .Lh14\n"
    "v_mfma_f32_16x16x32_bf16 v[208:211], v[56:59], v[152:155], 0\n"
    "ds_read_b64_tr_b16 v[176:177], v2 offset:12288\n"
    "ds_read_b64_tr_b16 v[178:179], v3 offset:12288\n"
    "v_mfma_f32_16x16x32_bf16 v[212:215], v[80:83], v[152:155], 0\n"
    "ds_read_b64_tr_b16 v[180:181], v2 offset:13312\n"
    "ds_read_b64_tr_b16 v[182:183], v3 offset:13312\n"
    "v_mfma_f32_16x16x32_bf16 v[208:211], v[60:63], v[156:159], v[208:211]\n"
    "ds_read_b64_tr_b16 v[184:185], v2 offset:14336\n"
    "ds_read_b64_tr_b16 v[186:187], v3 offset:14336\n"
    "v_mfma_f32_16x16x32_bf16 v[212:215], v[84:87], v[156:159], v[212:215]\n"
    "ds_read_b64_tr_b16 v[188:189], v2 offset:15360\n"
    "ds_read_b64_tr_b16 v[190:191], v3 offset:15360\n"
    "v_mfma_f32_16x16x32_bf16 v[208:211], v[64:67], v[160:163], v[208:211]\n"
    "v_mul_f32 v200, v200, v252\n"
    "v_mul_f32 v201, v201, v252\n"
    "v_mfma_f32_16x16x32_bf16 v[212:215], v[88:91], v[160:163], v[212:215]\n"
    "ds_read_b64_tr_b16 v[192:193], v2 offset:16384\n"
    "ds_read_b64_tr_b16 v[194:195], v3 offset:16384\n"
    "v_mfma_f32_16x16x32_bf16 v[208:211], v[68:71], v[164:167], v[208:211]\n"
    "v_mul_f32 v202, v202, v252\n"
    "v_mul_f32 v203, v203, v252\n"
    "v_mfma_f32_16x16x32_bf16 v[212:215], v[92:95], v[164:167], v[212:215]\n"
    "ds_read_b64_tr_b16 v[196:197], v2 offset:17408\n"
    "ds_read_b64_tr_b16 v[198:199], v3 offset:17408\n"
    "v_mfma_f32_16x16x32_bf16 v[208:211], v[72:75], v[168:171], v[208:211]\n"
    "v_mul_f32 v204, v204, v252\n"
    "v_mul_f32 v205, v205, v252\n"
    "v_mfma_f32_16x16x32_bf16 v[212:215], v[96:99], v[168:171], v[212:215]\n"
    "ds_read_b32 v253, v1 offset:320\n"
    "v_mul_f32 v206, v206, v252\n"
    "v_mfma_f32_16x16x32_bf16 v[208:211], v[76:79], v[172:175], v[208:211]\n"
    "v_mul_f32 v207, v207, v252\n"
    "v_cvt_pk_bf16_f32 v200, v200, v201\n"
    "v_mfma_f32_16x16x32_bf16 v[212:215], v[100:103], v[172:175], v[212:215]\n"
    "v_cvt_pk_bf16_f32 v201, v202, v203\n"
    "v_cvt_pk_bf16_f32 v202, v204, v205\n"
    "v_cvt_pk_bf16_f32 v203, v206, v207\n"
    "buffer_store_dwordx4 v[200:203], v4, s[80:83], 0 offen offset:0 sc1\n"
    ".Lh14j:\n"
    "s_cmp_lg_u32 s58, 0\n"
    "s_cbranch_scc1 .Lh_e12\n"
    "s_waitcnt lgkmcnt(1)\n"
    "v_mfma_f32_16x16x32_bf16 v[200:203], v[8:11], v[176:179], 0\n"
    "v_mul_f32 v208, v208, v252\n"
    "v_mul_f32 v209, v209, v252\n"
    "v_mfma_f32_16x16x32_bf16 v[204:207], v[32:35], v[176:179], 0\n"
    "ds_read_b32 v228, v224 offset:832\n"
    "v_mul_f32 v210, v210, v252\n"
    "v_mfma_f32_16x16x32_bf16 v[200:203], v[12:15], v[180:183], v[200:203]\n"
    "v_mul_f32 v211, v211, v252\n"
    "v_mul_f32 v212, v212, v252\n"
    "v_mfma_f32_16x16x32_bf16 v[204:207], v[36:39], v[180:183], v[204:207]\n"
    "v_mul_f32 v213, v213, v252\n"
    "v_mul_f32 v214, v214, v252\n"
    "v_mfma_f32_16x16x32_bf16 v[200:203], v[16:19], v[184:187], v[200:203]\n"
    "v_mul_f32 v215, v215, v252\n"
    "v_cvt_pk_bf16_f32 v208, v208, v209\n"
    "v_mfma_f32_16x16x32_bf16 v[204:207], v[40:43], v[184:187], v[204:207]\n"
    "v_cvt_pk_bf16_f32 v209, v210, v211\n"
    "v_cvt_pk_bf16_f32 v210, v212, v213\n"
    "v_mfma_f32_16x16x32_bf16 v[200:203], v[20:23], v[188:191], v[200:203]\n"
    "v_cvt_pk_bf16_f32 v211, v214, v215\n"
    "v_mfma_f32_16x16x32_bf16 v[204:207], v[44:47], v[188:191], v[204:207]\n"
    "buffer_store_dwordx4 v[208:211], v4, s[80:83], 0 offen offset:64 sc1\n"
    "v_mfma_f32_16x16x32_bf16 v[200:203], v[24:27], v[192:195], v[200:203]\n"
    "v_mfma_f32_16x16x32_bf16 v[204:207], v[48:51], v[192:195], v[204:207]\n"
    "v_add_u32 v4, v0, v4\n"
    "v_mfma_f32_16x16x32_bf16 v[200:203], v[28:31], v[196:199], v[200:203]\n"
    "s_waitcnt lgkmcnt(0)\n"
    "v_cmp_gt_u32 vcc, 4, v228\n"
    "v_mfma_f32_16x16x32_bf16 v[204:207], v[52:55], v[196:199], v[204:207]\n"
    "s_cbranch_vccnz .Lh15\n"
    "v_mfma_f32_16x16x32_bf16 v[208:211], v[56:59], v[176:179], 0\n"
    "ds_read_b64_tr_b16 v[152:153], v2 offset:0\n"
    "ds_read_b64_tr_b16 v[154:155], v3 offset:0\n"
    "v_mfma_f32_16x16x32_bf16 v[212:215], v[80:83], v[176:179], 0\n"
    "ds_read_b64_tr_b16 v[156:157], v2 offset:1024\n"
    "ds_read_b64_tr_b16 v[158:159], v3 offset:1024\n"
    "v_mfma_f32_16x16x32_bf16 v[208:211], v[60:63], v[180:183], v[208:211]\n"
    "ds_read_b64_tr_b16 v[160:161], v2 offset:2048\n"
    "ds_read_b64_tr_b16 v[162:163], v3 offset:2048\n"
    "v_mfma_f32_16x16x32_bf16 v[212:215], v[84:87], v[180:183], v[212:215]\n"
    "ds_read_b64_tr_b16 v[164:165], v2 offset:3072\n"
    "ds_read_b64_tr_b16 v[166:167], v3 offset:3072\n"
    "v_mfma_f32_16x16x32_bf16 v[208:211], v[64:67], v[184:187], v[208:211]\n"
    "v_mul_f32 v200, v200, v253\n"
    "v_mul_f32 v201, v201, v253\n"
    "v_mfma_f32_16x16x32_bf16 v[212:215], v[88:91], v[184:187], v[212:215]\n"
    "ds_read_b64_tr_b16 v[168:169], v2 offset:4096\n"
    "ds_read_b64_tr_b16 v[170:171], v3 offset:4096\n"
    "v_mfma_f32_16x16x32_bf16 v[208:211], v[68:71], v[188:191], v[208:211]\n"
    "v_mul_f32 v202, v202, v253\n"
    "v_mul_f32 v203, v203, v253\n"
    "v_mfma_f32_16x16x32_bf16 v[212:215], v[92:95], v[188:191], v[212:215]\n"
    "ds_read_b64_tr_b16 v[172:173], v2 offset:5120\n"
    "ds_read_b64_tr_b16 v[174:175], v3 offset:5120\n"
    "v_mfma_f32_16x16x32_bf16 v[208:211], v[72:75], v[192:195], v[208:211]\n"
    "v_mul_f32 v204, v204, v253\n"
    "v_mul_f32 v205, v205, v253\n"
    "v_mfma_f32_16x16x32_bf16 v[212:215], v[96:99], v[192:195], v[212:215]\n"
    "ds_read_b32 v252, v1 offset:192\n"
    "v_mul_f32 v206, v206, v253\n"
    "v_mfma_f32_16x16x32_bf16 v[208:211], v[76:79], v[196:199], v[208:211]\n"
    "v_mul_f32 v207, v207, v253\n"
    "v_cvt_pk_bf16_f32 v200, v200, v201\n"
    "v_mfma_f32_16x16x32_bf16 v[212:215], v[100:103], v[196:199], v[212:215]\n"
    "v_cvt_pk_bf16_f32 v201, v202, v203\n"
    "v_cvt_pk_bf16_f32 v202, v204, v205\n"
    "v_cvt_pk_bf16_f32 v203, v206, v207\n"
    "buffer_store_dwordx4 v[200:203], v5, s[80:83], 0 offen offset:0 sc1\n"
    ".Lh15j:\n"
    "s_waitcnt lgkmcnt(1)\n"
    "s_nop 0\n"
    "v_mfma_f32_16x16x32_bf16 v[200:203], v[8:11], v[152:155], 0\n"
    "v_mul_f32 v208, v208, v253\n"
    "v_mul_f32 v209, v209, v253\n"
    "v_mfma_f32_16x16x32_bf16 v[204:207], v[32:35], v[152:155], 0\n"
    "v_mul_f32 v210, v210, v253\n"
    "v_mul_f32 v211, v211, v253\n"
    "v_mfma_f32_16x16x32_bf16 v[200:203], v[12:15], v[156:159], v[200:203]\n"
    "v_mul_f32 v212, v212, v253\n"
    "v_mul_f32 v213, v213, v253\n"
    "v_mfma_f32_16x16x32_bf16 v[204:207], v[36:39], v[156:159], v[204:207]\n"
    "v_mul_f32 v214, v214, v253\n"
    "v_mul_f32 v215, v215, v253\n"
    "v_mfma_f32_16x16x32_bf16 v[200:203], v[16:19], v[160:163], v[200:203]\n"
    "v_cvt_pk_bf16_f32 v208, v208, v209\n"
    "v_cvt_pk_bf16_f32 v209, v210, v211\n"
    "v_mfma_f32_16x16x32_bf16 v[204:207], v[40:43], v[160:163], v[204:207]\n"
    "v_cvt_pk_bf16_f32 v210, v212, v213\n"
    "v_cvt_pk_bf16_f32 v211, v214, v215\n"
    "v_mfma_f32_16x16x32_bf16 v[200:203], v[20:23], v[164:167], v[200:203]\n"
    "buffer_store_dwordx4 v[208:211], v5, s[80:83], 0 offen offset:64 sc1\n"
    "v_mfma_f32_16x16x32_bf16 v[204:207], v[44:47], v[164:167], v[204:207]\n"
    "v_mfma_f32_16x16x32_bf16 v[200:203], v[24:27], v[168:171], v[200:203]\n"
    "v_mfma_f32_16x16x32_bf16 v[204:207], v[48:51], v[168:171], v[204:207]\n"
    "v_mfma_f32_16x16x32_bf16 v[200:203], v[28:31], v[172:175], v[200:203]\n"
    "v_mfma_f32_16x16x32_bf16 v[204:207], v[52:55], v[172:175], v[204:207]\n"
    "v_mfma_f32_16x16x32_bf16 v[208:211], v[56:59], v[152:155], 0\n"
    "v_mfma_f32_16x16x32_bf16 v[212:215], v[80:83], v[152:155], 0\n"
    "v_mfma_f32_16x16x32_bf16 v[208:211], v[60:63], v[156:159], v[208:211]\n"
    "v_mfma_f32_16x16x32_bf16 v[212:215], v[84:87], v[156:159], v[212:215]\n"
    "v_mfma_f32_16x16x32_bf16 v[208:211], v[64:67], v[160:163], v[208:211]\n"
    "v_mfma_f32_16x16x32_bf16 v[212:215], v[88:91], v[160:163], v[212:215]\n"
    "s_waitcnt lgkmcnt(0)\n"
    "v_mul_f32 v200, v200, v252\n"
    "v_mfma_f32_16x16x32_bf16 v[208:211], v[68:71], v[164:167], v[208:211]\n"
    "v_mul_f32 v201, v201, v252\n"
    "v_mul_f32 v202, v202, v252\n"
    "v_mfma_f32_16x16x32_bf16 v[212:215], v[92:95], v[164:167], v[212:215]\n"
    "v_mul_f32 v203, v203, v252\n"
    "v_mul_f32 v204, v204, v252\n"
    "v_mfma_f32_16x16x32_bf16 v[208:211], v[72:75], v[168:171], v[208:211]\n"
    "v_mul_f32 v205, v205, v252\n"
    "v_mul_f32 v206, v206, v252\n"
    "v_mfma_f32_16x16x32_bf16 v[212:215], v[96:99], v[168:171], v[212:215]\n"
    "v_mul_f32 v207, v207, v252\n"
    "v_cvt_pk_bf16_f32 v200, v200, v201\n"
    "v_mfma_f32_16x16x32_bf16 v[208:211], v[76:79], v[172:175], v[208:211]\n"
    "v_cvt_pk_bf16_f32 v201, v202, v203\n"
    "v_cvt_pk_bf16_f32 v202, v204, v205\n"
    "v_mfma_f32_16x16x32_bf16 v[212:215], v[100:103], v[172:175], v[212:215]\n"
    "v_cvt_pk_bf16_f32 v203, v206, v207\n"
    "buffer_store_dwordx4 v[200:203], v4, s[80:83], 0 offen offset:0 sc1\n"
    "s_nop 2\n"
    "v_mul_f32 v208, v208, v252\n"
    "v_mul_f32 v209, v209, v252\n"
    "v_mul_f32 v210, v210, v252\n"
    "v_mul_f32 v211, v211, v252\n"
    "v_mul_f32 v212, v212, v252\n"
    "v_mul_f32 v213, v213, v252\n"
    "v_mul_f32 v214, v214, v252\n"
    "v_mul_f32 v215, v215, v252\n"
    "v_cvt_pk_bf16_f32 v208, v208, v209\n"
    "v_cvt_pk_bf16_f32 v209, v210, v211\n"
    "v_cvt_pk_bf16_f32 v210, v212, v213\n"
    "v_cvt_pk_bf16_f32 v211, v214, v215\n"
    "buffer_store_dwordx4 v[208:211], v4, s[80:83], 0 offen offset:64 sc1\n"
    ".Lh_end:\n"
    "s_endpgm\n"
    ".Lh1:\n"
    "s_sub_u32 s52, s52, 1\n"
    "s_cmp_lt_i32 s52, 0\n"
    "s_cbranch_scc1 .Lh1b\n"
    "s_sleep 1\n"
    "ds_read_b32 v228, v224 offset:256\n"
    "s_waitcnt lgkmcnt(0)\n"
    "v_cmp_gt_u32 vcc, 1, v228\n"
    "s_cbranch_vccnz .Lh1\n"
    "s_branch .Lh1b\n"
    ".Lh1_re:\n"
    "s_sub_u32 s51, s51, 1\n"
    "s_cmp_le_i32 s51, 0\n"
    "s_cbranch_scc1 .Lh1_ok\n"
    "s_sleep 8\n"
    "v_mov_b64 v[244:245], v[240:241]\n"
    "v_mov_b64 v[246:247], 0\n"
    "v_mov_b64 v[242:243], 0\n"
    "s_mov_b32 exec_hi, 15\n"
    "global_load_dwordx4 v[244:247], v236, s[40:41] sc1\n"
    "s_mov_b32 exec_hi, -1\n"
    "global_load_dwordx4 v[8:11], v235, s[64:65] offset:-3072 sc1\n"
    "global_load_dwordx4 v[32:35], v235, s[66:67] offset:-3072 sc1\n"
    "global_load_dwordx4 v[12:15], v235, s[64:65] offset:-2048 sc1\n"
    "global_load_dwordx4 v[36:39], v235, s[66:67] offset:-2048 sc1\n"
    "global_load_dwordx4 v[16:19], v235, s[64:65] offset:-1024 sc1\n"
    "global_load_dwordx4 v[40:43], v235, s[66:67] offset:-1024 sc1\n"
    "global_load_dwordx4 v[20:23], v235, s[64:65] offset:0 sc1\n"
    "global_load_dwordx4 v[44:47], v235, s[66:67] offset:0 sc1\n"
    "global_load_dwordx4 v[24:27], v235, s[64:65] offset:1024 sc1\n"
    "global_load_dwordx4 v[48:51], v235, s[66:67] offset:1024 sc1\n"
    "global_load_dwordx4 v[28:31], v235, s[64:65] offset:2048 sc1\n"
    "global_load_dwordx4 v[52:55], v235, s[66:67] offset:2048 sc1\n"
    "global_load_dwordx4 v[56:59], v235, s[68:69] offset:-3072 sc1\n"
    "global_load_dwordx4 v[80:83], v235, s[70:71] offset:-3072 sc1\n"
    "global_load_dwordx4 v[60:63], v235, s[68:69] offset:-2048 sc1\n"
    "global_load_dwordx4 v[84:87], v235, s[70:71] offset:-2048 sc1\n"
    "global_load_dwordx4 v[64:67], v235, s[68:69] offset:-1024 sc1\n"
    "global_load_dwordx4 v[88:91], v235, s[70:71] offset:-1024 sc1\n"
    "global_load_dwordx4 v[68:71], v235, s[68:69] offset:0 sc1\n"
    "global_load_dwordx4 v[92:95], v235, s[70:71] offset:0 sc1\n"
    "global_load_dwordx4 v[72:75], v235, s[68:69] offset:1024 sc1\n"
    "global_load_dwordx4 v[96:99], v235, s[70:71] offset:1024 sc1\n"
    "global_load_dwordx4 v[76:79], v235, s[68:69] offset:2048 sc1\n"
    "global_load_dwordx4 v[100:103], v235, s[70:71] offset:2048 sc1\n"
    "global_load_dwordx4 v[104:107], v235, s[72:73] offset:-3072 sc1\n"
    "global_load_dwordx4 v[128:131], v235, s[74:75] offset:-3072 sc1\n"
    "global_load_dwordx4 v[108:111], v235, s[72:73] offset:-2048 sc1\n"
    "global_load_dwordx4 v[132:135], v235, s[74:75] offset:-2048 sc1\n"
    "global_load_dwordx4 v[112:115], v235, s[72:73] offset:-1024 sc1\n"
    "global_load_dwordx4 v[136:139], v235, s[74:75] offset:-1024 sc1\n"
    "global_load_dwordx4 v[116:119], v235, s[72:73] offset:0 sc1\n"
    "global_load_dwordx4 v[140:143], v235, s[74:75] offset:0 sc1\n"
    "global_load_dwordx4 v[120:123], v235, s[72:73] offset:1024 sc1\n"
    "global_load_dwordx4 v[144:147], v235, s[74:75] offset:1024 sc1\n"
    "global_load_dwordx4 v[124:127], v235, s[72:73] offset:2048 sc1\n"
    "global_load_dwordx4 v[148:151], v235, s[74:75] offset:2048 sc1\n"
    "s_waitcnt vmcnt(0)\n"
    "s_branch .Lh1_ck\n"
    ".Lh_s1:\n"
    "ds_read_b32 v228, v224 offset:256\n"
    "s_waitcnt lgkmcnt(0)\n"
    "v_cmp_gt_u32 vcc, 1, v228\n"
    "s_cbranch_vccnz .Lh1\n"
    ".Lh1b:\n"
    "s_mul_i32 s56, s45, 10\n"
    "s_add_u32 s56, s56, s44\n"
    "s_add_u32 s60, s42, s56\n"
    "s_addc_u32 s61, s43, 0\n"
    "s_mov_b32 m0, 91008\n"
    "s_nop 0\n"
    "global_load_lds_dwordx4 v230, s[60:61]\n"
    "s_mov_b32 m0, 93056\n"
    "s_nop 0\n"
    "global_load_lds_dwordx4 v231, s[60:61]\n"
    "s_mov_b32 m0, 95104\n"
    "s_nop 0\n"
    "global_load_lds_dwordx4 v232, s[60:61]\n"
    "s_mov_b32 m0, 96736\n"
    "s_nop 0\n"
    "global_load_lds_dwordx4 v234, s[60:61]\n"
    "s_mul_i32 s56, s45, 11\n"
    "s_add_u32 s56, s56, s44\n"
    "s_add_u32 s60, s42, s56\n"
    "s_addc_u32 s61, s43, 0\n"
    "s_mov_b32 m0, 99904\n"
    "s_nop 0\n"
    "global_load_lds_dwordx4 v230, s[60:61]\n"
    "s_mov_b32 m0, 101952\n"
    "s_nop 0\n"
    "global_load_lds_dwordx4 v231, s[60:61]\n"
    "s_mov_b32 m0, 104000\n"
    "s_nop 0\n"
    "global_load_lds_dwordx4 v232, s[60:61]\n"
    "s_mov_b32 m0, 105632\n"
    "s_nop 0\n"
    "global_load_lds_dwordx4 v234, s[60:61]\n"
    "s_waitcnt lgkmcnt(0)\n"
    "s_add_u32 s78, s78, 0x7f4a7c15\n"
    "s_add_u32 s79, s79, 0x9e3779b9\n"
    "v_mov_b64 v[240:241], s[78:79]\n"
    "s_mov_b32 s51, 0x4000\n"
    "v_mov_b64 v[244:245], v[240:241]\n"
    "v_mov_b64 v[246:247], 0\n"
    "v_mov_b64 v[242:243], 0\n"
    "s_mov_b32 exec_hi, 15\n"
    "global_load_dwordx4 v[244:247], v236, s[40:41] sc1\n"
    "s_mov_b32 exec_hi, -1\n"
    "global_load_dwordx4 v[8:11], v235, s[64:65] offset:-3072 sc1\n"
    "global_load_dwordx4 v[32:35], v235, s[66:67] offset:-3072 sc1\n"
    "global_load_dwordx4 v[12:15], v235, s[64:65] offset:-2048 sc1\n"
    "global_load_dwordx4 v[36:39], v235, s[66:67] offset:-2048 sc1\n"
    "global_load_dwordx4 v[16:19], v235, s[64:65] offset:-1024 sc1\n"
    "global_load_dwordx4 v[40:43], v235, s[66:67] offset:-1024 sc1\n"
    "global_load_dwordx4 v[20:23], v235, s[64:65] offset:0 sc1\n"
    "global_load_dwordx4 v[44:47], v235, s[66:67] offset:0 sc1\n"
    "global_load_dwordx4 v[24:27], v235, s[64:65] offset:1024 sc1\n"
    "global_load_dwordx4 v[48:51], v235, s[66:67] offset:1024 sc1\n"
    "global_load_dwordx4 v[28:31], v235, s[64:65] offset:2048 sc1\n"
    "global_load_dwordx4 v[52:55], v235, s[66:67] offset:2048 sc1\n"
    "s_waitcnt vmcnt(13)\n"
    "v_mov_b32 v227, 6\n"
    "ds_write_b32 v226, v227 offset:0\n"
    "global_load_dwordx4 v[56:59], v235, s[68:69] offset:-3072 sc1\n"
    "global_load_dwordx4 v[80:83], v235, s[70:71] offset:-3072 sc1\n"
    "global_load_dwordx4 v[60:63], v235, s[68:69] offset:-2048 sc1\n"
    "global_load_dwordx4 v[84:87], v235, s[70:71] offset:-2048 sc1\n"
    "global_load_dwordx4 v[64:67], v235, s[68:69] offset:-1024 sc1\n"
    "global_load_dwordx4 v[88:91], v235, s[70:71] offset:-1024 sc1\n"
    "global_load_dwordx4 v[68:71], v235, s[68:69] offset:0 sc1\n"
    "global_load_dwordx4 v[92:95], v235, s[70:71] offset:0 sc1\n"
    "global_load_dwordx4 v[72:75], v235, s[68:69] offset:1024 sc1\n"
    "global_load_dwordx4 v[96:99], v235, s[70:71] offset:1024 sc1\n"
    "global_load_dwordx4 v[76:79], v235, s[68:69] offset:2048 sc1\n"
    "global_load_dwordx4 v[100:103], v235, s[70:71] offset:2048 sc1\n"
    "global_load_dwordx4 v[104:107], v235, s[72:73] offset:-3072 sc1\n"
    "global_load_dwordx4 v[128:131], v235, s[74:75] offset:-3072 sc1\n"
    "global_load_dwordx4 v[108:111], v235, s[72:73] offset:-2048 sc1\n"
    "global_load_dwordx4 v[132:135], v235, s[74:75] offset:-2048 sc1\n"
    "global_load_dwordx4 v[112:115], v235, s[72:73] offset:-1024 sc1\n"
    "global_load_dwordx4 v[136:139], v235, s[74:75] offset:-1024 sc1\n"
    "global_load_dwordx4 v[116:119], v235, s[72:73] offset:0 sc1\n"
    "global_load_dwordx4 v[140:143], v235, s[74:75] offset:0 sc1\n"
    "global_load_dwordx4 v[120:123], v235, s[72:73] offset:1024 sc1\n"
    "global_load_dwordx4 v[144:147], v235, s[74:75] offset:1024 sc1\n"
    "global_load_dwordx4 v[124:127], v235, s[72:73] offset:2048 sc1\n"
    "global_load_dwordx4 v[148:151], v235, s[74:75] offset:2048 sc1\n"
    "s_add_u32 s60, s42, s84\n"
    "s_addc_u32 s61, s43, 0\n"
    "s_mov_b32 m0, 108800\n"
    "s_nop 0\n"
    "global_load_lds_dwordx4 v230, s[60:61]\n"
    "s_mov_b32 m0, 110848\n"
    "s_nop 0\n"
    "global_load_lds_dwordx4 v231, s[60:61]\n"
    "s_mov_b32 m0, 112896\n"
    "s_nop 0\n"
    "global_load_lds_dwordx4 v232, s[60:61]\n"
    "s_mov_b32 m0, 114528\n"
    "s_nop 0\n"
    "global_load_lds_dwordx4 v234, s[60:61]\n"
    "s_add_u32 s60, s42, s85\n"
    "s_addc_u32 s61, s43, 0\n"
    "s_mov_b32 m0, 117696\n"
    "s_nop 0\n"
    "global_load_lds_dwordx4 v230, s[60:61]\n"
    "s_mov_b32 m0, 119744\n"
    "s_nop 0\n"
    "global_load_lds_dwordx4 v231, s[60:61]\n"
    "s_mov_b32 m0, 121792\n"
    "s_nop 0\n"
    "global_load_lds_dwordx4 v232, s[60:61]\n"
    "s_mov_b32 m0, 123424\n"
    "s_nop 0\n"
    "global_load_lds_dwordx4 v234, s[60:61]\n"
    "global_load_dword v250, v238, s[42:43]\n"
    "global_load_dword v250, v239, s[42:43]\n"
    ".Lh1_ck:\n"
    "s_waitcnt vmcnt(42)\n"
    "v_add3_u32 v242, v242, v8, v9\n"
    "v_sub_u32 v8, v8, v240\n"
    "v_add3_u32 v243, v243, v10, v11\n"
    "v_add3_u32 v242, v242, v32, v33\n"
    "v_sub_u32 v32, v32, v240\n"
    "v_add3_u32 v243, v243, v34, v35\n"
    "v_add3_u32 v242, v242, v12, v13\n"
    "v_sub_u32 v12, v12, v240\n"
    "v_add3_u32 v243, v243, v14, v15\n"
    "v_add3_u32 v242, v242, v36, v37\n"
    "v_sub_u32 v36, v36, v240\n"
    "v_add3_u32 v243, v243, v38, v39\n"
    "s_waitcnt vmcnt(38)\n"
    "v_add3_u32 v242, v242, v16, v17\n"
    "v_sub_u32 v16, v16, v240\n"
    "v_add3_u32 v243, v243, v18, v19\n"
    "v_add3_u32 v242, v242, v40, v41\n"
    "v_sub_u32 v40, v40, v240\n"
    "v_add3_u32 v243, v243, v42, v43\n"
    "v_add3_u32 v242, v242, v20, v21\n"
    "v_sub_u32 v20, v20, v240\n"
    "v_add3_u32 v243, v243, v22, v23\n"
    "v_add3_u32 v242, v242, v44, v45\n"
    "v_sub_u32 v44, v44, v240\n"
    "v_add3_u32 v243, v243, v46, v47\n"
    "s_waitcnt vmcnt(34)\n"
    "v_add3_u32 v242, v242, v24, v25\n"
    "v_sub_u32 v24, v24, v240\n"
    "v_add3_u32 v243, v243, v26, v27\n"
    "v_add3_u32 v242, v242, v48, v49\n"
    "v_sub_u32 v48, v48, v240\n"
    "v_add3_u32 v243, v243, v50, v51\n"
    "v_add3_u32 v242, v242, v28, v29\n"
    "v_sub_u32 v28, v28, v240\n"
    "v_add3_u32 v243, v243, v30, v31\n"
    "v_add3_u32 v242, v242, v52, v53\n"
    "v_sub_u32 v52, v52, v240\n"
    "v_add3_u32 v243, v243, v54, v55\n"
    "s_waitcnt vmcnt(30)\n"
    "v_add3_u32 v242, v242, v56, v57\n"
    "v_sub_u32 v56, v56, v240\n"
    "v_add3_u32 v243, v243, v58, v59\n"
    "v_add3_u32 v242, v242, v80, v81\n"
    "v_sub_u32 v80, v80, v240\n"
    "v_add3_u32 v243, v243, v82, v83\n"
    "v_add3_u32 v242, v242, v60, v61\n"
    "v_sub_u32 v60, v60, v240\n"
    "v_add3_u32 v243, v243, v62, v63\n"
    "v_add3_u32 v242, v242, v84, v85\n"
    "v_sub_u32 v84, v84, v240\n"
    "v_add3_u32 v243, v243, v86, v87\n"
    "s_waitcnt vmcnt(26)\n"
    "v_add3_u32 v242, v242, v64, v65\n"
    "v_sub_u32 v64, v64, v240\n"
    "v_add3_u32 v243, v243, v66, v67\n"
    "v_add3_u32 v242, v242, v88, v89\n"
    "v_sub_u32 v88, v88, v240\n"
    "v_add3_u32 v243, v243, v90, v91\n"
    "v_add3_u32 v242, v242, v68, v69\n"
    "v_sub_u32 v68, v68, v240\n"
    "v_add3_u32 v243, v243, v70, v71\n"
    "v_add3_u32 v242, v242, v92, v93\n"
    "v_sub_u32 v92, v92, v240\n"
    "v_add3_u32 v243, v243, v94, v95\n"
    "s_waitcnt vmcnt(22)\n"
    "v_add3_u32 v242, v242, v72, v73\n"
    "v_sub_u32 v72, v72, v240\n"
    "v_add3_u32 v243, v243, v74, v75\n"
    "v_add3_u32 v242, v242, v96, v97\n"
    "v_sub_u32 v96, v96, v240\n"
    "v_add3_u32 v243, v243, v98, v99\n"
    "v_add3_u32 v242, v242, v76, v77\n"
    "v_sub_u32 v76, v76, v240\n"
    "v_add3_u32 v243, v243, v78, v79\n"
    "v_add3_u32 v242, v242, v100, v101\n"
    "v_sub_u32 v100, v100, v240\n"
    "v_add3_u32 v243, v243, v102, v103\n"
    "s_waitcnt vmcnt(18)\n"
    "v_add3_u32 v242, v242, v104, v105\n"
    "v_sub_u32 v104, v104, v240\n"
    "v_add3_u32 v243, v243, v106, v107\n"
    "v_add3_u32 v242, v242, v128, v129\n"
    "v_sub_u32 v128, v128, v240\n"
    "v_add3_u32 v243, v243, v130, v131\n"
    "v_add3_u32 v242, v242, v108, v109\n"
    "v_sub_u32 v108, v108, v240\n"
    "v_add3_u32 v243, v243, v110, v111\n"
    "v_add3_u32 v242, v242, v132, v133\n"
    "v_sub_u32 v132, v132, v240\n"
    "v_add3_u32 v243, v243, v134, v135\n"
    "s_waitcnt vmcnt(14)\n"
    "v_add3_u32 v242, v242, v112, v113\n"
    "v_sub_u32 v112, v112, v240\n"
    "v_add3_u32 v243, v243, v114, v115\n"
    "v_add3_u32 v242, v242, v136, v137\n"
    "v_sub_u32 v136, v136, v240\n"
    "v_add3_u32 v243, v243, v138, v139\n"
    "v_add3_u32 v242, v242, v116, v117\n"
    "v_sub_u32 v116, v116, v240\n"
    "v_add3_u32 v243, v243, v118, v119\n"
    "v_add3_u32 v242, v242, v140, v141\n"
    "v_sub_u32 v140, v140, v240\n"
    "v_add3_u32 v243, v243, v142, v143\n"
    "s_waitcnt vmcnt(10)\n"
    "v_add3_u32 v242, v242, v120, v121\n"
    "v_sub_u32 v120, v120, v240\n"
    "v_add3_u32 v243, v243, v122, v123\n"
    "v_add3_u32 v242, v242, v144, v145\n"
    "v_sub_u32 v144, v144, v240\n"
    "v_add3_u32 v243, v243, v146, v147\n"
    "v_add3_u32 v242, v242, v124, v125\n"
    "v_sub_u32 v124, v124, v240\n"
    "v_add3_u32 v243, v243, v126, v127\n"
    "v_add3_u32 v242, v242, v148, v149\n"
    "v_sub_u32 v148, v148, v240\n"
    "v_add3_u32 v243, v243, v150, v151\n"
    "v_cmp_ne_u64 s[76:77], v[240:241], v[244:245]\n"
    "v_sub_u32 v242, v242, v246\n"
    "v_sub_u32 v243, v243, v247\n"
    "s_nop 1\n"
    "v_permlane32_swap_b32 v242, v243\n"
    "v_add_u32 v242, v242, v243\n"
    "s_nop 1\n"
    "v_add_u32_dpp v242, v242, v242 row_ror:8 row_mask:0xf bank_mask:0xf\n"
    "s_nop 1\n"
    "v_add_u32_dpp v242, v242, v242 row_ror:4 row_mask:0xf bank_mask:0xf\n"
    "s_nop 1\n"
    "v_add_u32_dpp v242, v242, v242 row_ror:2 row_mask:0xf bank_mask:0xf\n"
    "s_nop 1\n"
    "v_add_u32_dpp v242, v242, v242 row_ror:1 row_mask:0xf bank_mask:0xf\n"
    "v_mov_b32 v243, v242\n"
    "s_nop 1\n"
    "v_permlane16_swap_b32 v242, v243\n"
    "v_add_u32 v242, v242, v243\n"
    "v_cmp_ne_u32 vcc, 0, v242\n"
    "s_nop 1\n"
    "s_or_b64 vcc, vcc, s[76:77]\n"
    "s_cbranch_scc1 .Lh1_re\n"
    ".Lh1_ok:\n"
    "s_waitcnt vmcnt(2)\n"
    "v_mov_b32 v227, 99\n"
    "ds_write_b32 v226, v227 offset:0\n"
    "s_branch .Lh_sd\n"
    ".Lh2:\n"
    "s_sub_u32 s52, s52, 1\n"
    "s_cmp_lt_i32 s52, 0\n"
    "s_cbranch_scc1 .Lh2b\n"
    "s_sleep 1\n"
    "ds_read_b32 v228, v224 offset:256\n"
    "s_waitcnt lgkmcnt(0)\n"
    "v_cmp_gt_u32 vcc, 1, v228\n"
    "s_cbranch_vccnz .Lh2\n"
    "s_branch .Lh2b\n"
    ".Lh0_re:\n"
    "s_sub_u32 s51, s51, 1\n"
    "s_cmp_le_i32 s51, 0\n"
    "s_cbranch_scc1 .Lh0_ok\n"
    "s_sleep 8\n"
    "v_mov_b64 v[244:245], v[240:241]\n"
    "v_mov_b64 v[246:247], 0\n"
    "v_mov_b64 v[242:243], 0\n"
    "s_mov_b32 exec_hi, 15\n"
    "global_load_dwordx4 v[244:247], v236, s[40:41] sc1\n"
    "s_mov_b32 exec_hi, -1\n"
    "global_load_dwordx4 v[8:11], v235, s[64:65] offset:-3072 sc1\n"
    "global_load_dwordx4 v[32:35], v235, s[66:67] offset:-3072 sc1\n"
    "global_load_dwordx4 v[12:15], v235, s[64:65] offset:-2048 sc1\n"
    "global_load_dwordx4 v[36:39], v235, s[66:67] offset:-2048 sc1\n"
    "global_load_dwordx4 v[16:19], v235, s[64:65] offset:-1024 sc1\n"
    "global_load_dwordx4 v[40:43], v235, s[66:67] offset:-1024 sc1\n"
    "global_load_dwordx4 v[20:23], v235, s[64:65] offset:0 sc1\n"
    "global_load_dwordx4 v[44:47], v235, s[66:67] offset:0 sc1\n"
    "global_load_dwordx4 v[24:27], v235, s[64:65] offset:1024 sc1\n"
    "global_load_dwordx4 v[48:51], v235, s[66:67] offset:1024 sc1\n"
    "global_load_dwordx4 v[28:31], v235, s[64:65] offset:2048 sc1\n"
    "global_load_dwordx4 v[52:55], v235, s[66:67] offset:2048 sc1\n"
    "global_load_dwordx4 v[56:59], v235, s[68:69] offset:-3072 sc1\n"
    "global_load_dwordx4 v[80:83], v235, s[70:71] offset:-3072 sc1\n"
    "global_load_dwordx4 v[60:63], v235, s[68:69] offset:-2048 sc1\n"
    "global_load_dwordx4 v[84:87], v235, s[70:71] offset:-2048 sc1\n"
    "global_load_dwordx4 v[64:67], v235, s[68:69] offset:-1024 sc1\n"
    "global_load_dwordx4 v[88:91], v235, s[70:71] offset:-1024 sc1\n"
    "global_load_dwordx4 v[68:71], v235, s[68:69] offset:0 sc1\n"
    "global_load_dwordx4 v[92:95], v235, s[70:71] offset:0 sc1\n"
    "global_load_dwordx4 v[72:75], v235, s[68:69] offset:1024 sc1\n"
    "global_load_dwordx4 v[96:99], v235, s[70:71] offset:1024 sc1\n"
    "global_load_dwordx4 v[76:79], v235, s[68:69] offset:2048 sc1\n"
    "global_load_dwordx4 v[100:103], v235, s[70:71] offset:2048 sc1\n"
    "global_load_dwordx4 v[104:107], v235, s[72:73] offset:-3072 sc1\n"
    "global_load_dwordx4 v[128:131], v235, s[74:75] offset:-3072 sc1\n"
    "global_load_dwordx4 v[108:111], v235, s[72:73] offset:-2048 sc1\n"
    "global_load_dwordx4 v[132:135], v235, s[74:75] offset:-2048 sc1\n"
    "global_load_dwordx4 v[112:115], v235, s[72:73] offset:-1024 sc1\n"
    "global_load_dwordx4 v[136:139], v235, s[74:75] offset:-1024 sc1\n"
    "global_load_dwordx4 v[116:119], v235, s[72:73] offset:0 sc1\n"
    "global_load_dwordx4 v[140:143], v235, s[74:75] offset:0 sc1\n"
    "global_load_dwordx4 v[120:123], v235, s[72:73] offset:1024 sc1\n"
    "global_load_dwordx4 v[144:147], v235, s[74:75] offset:1024 sc1\n"
    "global_load_dwordx4 v[124:127], v235, s[72:73] offset:2048 sc1\n"
    "global_load_dwordx4 v[148:151], v235, s[74:75] offset:2048 sc1\n"
    "s_waitcnt vmcnt(0)\n"
    "s_branch .Lh0_ck\n"
    ".Lh3:\n"
    "s_sub_u32 s52, s52, 1\n"
    "s_cmp_lt_i32 s52, 0\n"
    "s_cbranch_scc1 .Lh3b\n"
    "s_sleep 1\n"
    "ds_read_b32 v228, v224 offset:128\n"
    "s_waitcnt lgkmcnt(0)\n"
    "v_cmp_gt_u32 vcc, 1, v228\n"
    "s_cbranch_vccnz .Lh3\n"
    "s_branch .Lh3b\n"
    ".Lh4:\n"
    "v_mfma_f32_16x16x32_bf16 v[216:219], v[104:107], v[152:155], 0\n"
    "buffer_store_dwordx4 v[200:203], v4, s[80:83], 0 offen offset:0 sc1\n"
    "v_mfma_f32_16x16x32_bf16 v[220:223], v[128:131], v[152:155], 0\n"
    "v_mul_f32 v208, v208, v252\n"
    "v_mul_f32 v209, v209, v252\n"
    "v_mfma_f32_16x16x32_bf16 v[216:219], v[108:111], v[156:159], v[216:219]\n"
    "v_mul_f32 v210, v210, v252\n"
    "v_mul_f32 v211, v211, v252\n"
    "v_mfma_f32_16x16x32_bf16 v[220:223], v[132:135], v[156:159], v[220:223]\n"
    "v_mul_f32 v212, v212, v252\n"
    "v_mul_f32 v213, v213, v252\n"
    "v_mfma_f32_16x16x32_bf16 v[216:219], v[112:115], v[160:163], v[216:219]\n"
    "v_mul_f32 v214, v214, v252\n"
    "v_mul_f32 v215, v215, v252\n"
    "v_mfma_f32_16x16x32_bf16 v[220:223], v[136:139], v[160:163], v[220:223]\n"
    "v_cvt_pk_bf16_f32 v208, v208, v209\n"
    "v_cvt_pk_bf16_f32 v209, v210, v211\n"
    "v_mfma_f32_16x16x32_bf16 v[216:219], v[116:119], v[164:167], v[216:219]\n"
    "v_cvt_pk_bf16_f32 v210, v212, v213\n"
    "v_cvt_pk_bf16_f32 v211, v214, v215\n"
    "v_mfma_f32_16x16x32_bf16 v[220:223], v[140:143], v[164:167], v[220:223]\n"
    "buffer_store_dwordx4 v[208:211], v4, s[80:83], 0 offen offset:64 sc1\n"
    "v_mfma_f32_16x16x32_bf16 v[216:219], v[120:123], v[168:171], v[216:219]\n"
    "v_mfma_f32_16x16x32_bf16 v[220:223], v[144:147], v[168:171], v[220:223]\n"
    "v_mfma_f32_16x16x32_bf16 v[216:219], v[124:127], v[172:175], v[216:219]\n"
    "v_mfma_f32_16x16x32_bf16 v[220:223], v[148:151], v[172:175], v[220:223]\n"
    ".Lh4r:\n"
    "s_sub_u32 s52, s52, 1\n"
    "s_cmp_lt_i32 s52, 0\n"
    "s_cbranch_scc1 .Lh4g\n"
    "s_sleep 1\n"
    "ds_read_b32 v228, v224 offset:144\n"
    "s_waitcnt lgkmcnt(0)\n"
    "v_cmp_gt_u32 vcc, 1, v228\n"
    "s_cbranch_vccnz .Lh4r\n"
    "s_branch .Lh4g\n"
    ".Lh4g:\n"
    "ds_read_b64_tr_b16 v[176:177], v2 offset:24576\n"
    "ds_read_b64_tr_b16 v[178:179], v3 offset:24576\n"
    "ds_read_b64_tr_b16 v[180:181], v2 offset:25600\n"
    "ds_read_b64_tr_b16 v[182:183], v3 offset:25600\n"
    "ds_read_b64_tr_b16 v[184:185], v2 offset:26624\n"
    "ds_read_b64_tr_b16 v[186:187], v3 offset:26624\n"
    "ds_read_b64_tr_b16 v[188:189], v2 offset:27648\n"
    "ds_read_b64_tr_b16 v[190:191], v3 offset:27648\n"
    "ds_read_b64_tr_b16 v[192:193], v2 offset:28672\n"
    "ds_read_b64_tr_b16 v[194:195], v3 offset:28672\n"
    "ds_read_b64_tr_b16 v[196:197], v2 offset:29696\n"
    "ds_read_b64_tr_b16 v[198:199], v3 offset:29696\n"
    "ds_read_b32 v253, v1 offset:64\n"
    "v_mov_b32 v227, 2\n"
    "ds_write_b32 v226, v227 offset:32\n"
    "s_branch .Lh4j\n"
    ".Lh5:\n"
    "v_mfma_f32_16x16x32_bf16 v[216:219], v[104:107], v[176:179], 0\n"
    "buffer_store_dwordx4 v[200:203], v5, s[80:83], 0 offen offset:0 sc1\n"
    "v_mfma_f32_16x16x32_bf16 v[220:223], v[128:131], v[176:179], 0\n"
    "v_mul_f32 v208, v208, v253\n"
    "v_mul_f32 v209, v209, v253\n"
    "v_mfma_f32_16x16x32_bf16 v[216:219], v[108:111], v[180:183], v[216:219]\n"
    "v_mul_f32 v210, v210, v253\n"
    "v_mul_f32 v211, v211, v253\n"
    "v_mfma_f32_16x16x32_bf16 v[220:223], v[132:135], v[180:183], v[220:223]\n"
    "v_mul_f32 v212, v212, v253\n"
    "v_mul_f32 v213, v213, v253\n"
    "v_mfma_f32_16x16x32_bf16 v[216:219], v[112:115], v[184:187], v[216:219]\n"
    "v_mul_f32 v214, v214, v253\n"
    "v_mul_f32 v215, v215, v253\n"
    "v_mfma_f32_16x16x32_bf16 v[220:223], v[136:139], v[184:187], v[220:223]\n"
    "v_cvt_pk_bf16_f32 v208, v208, v209\n"
    "v_cvt_pk_bf16_f32 v209, v210, v211\n"
    "v_mfma_f32_16x16x32_bf16 v[216:219], v[116:119], v[188:191], v[216:219]\n"
    "v_cvt_pk_bf16_f32 v210, v212, v213\n"
    "v_cvt_pk_bf16_f32 v211, v214, v215\n"
    "v_mfma_f32_16x16x32_bf16 v[220:223], v[140:143], v[188:191], v[220:223]\n"
    "buffer_store_dwordx4 v[208:211], v5, s[80:83], 0 offen offset:64 sc1\n"
    "v_mfma_f32_16x16x32_bf16 v[216:219], v[120:123], v[192:195], v[216:219]\n"
    "v_mfma_f32_16x16x32_bf16 v[220:223], v[144:147], v[192:195], v[220:223]\n"
    "v_mfma_f32_16x16x32_bf16 v[216:219], v[124:127], v[196:199], v[216:219]\n"
    "v_mfma_f32_16x16x32_bf16 v[220:223], v[148:151], v[196:199], v[220:223]\n"
    ".Lh5r:\n"
    "s_sub_u32 s52, s52, 1\n"
    "s_cmp_lt_i32 s52, 0\n"
    "s_cbranch_scc1 .Lh5g\n"
    "s_sleep 1\n"
    "ds_read_b32 v228, v224 offset:160\n"
    "s_waitcnt lgkmcnt(0)\n"
    "v_cmp_gt_u32 vcc, 1, v228\n"
    "s_cbranch_vccnz .Lh5r\n"
    "s_branch .Lh5g\n"
    ".Lh5g:\n"
    "ds_read_b64_tr_b16 v[152:153], v2 offset:30720\n"
    "ds_read_b64_tr_b16 v[154:155], v3 offset:30720\n"
    "ds_read_b64_tr_b16 v[156:157], v2 offset:31744\n"
    "ds_read_b64_tr_b16 v[158:159], v3 offset:31744\n"
    "ds_read_b64_tr_b16 v[160:161], v2 offset:32768\n"
    "ds_read_b64_tr_b16 v[162:163], v3 offset:32768\n"
    "ds_read_b64_tr_b16 v[164:165], v2 offset:33792\n"
    "ds_read_b64_tr_b16 v[166:167], v3 offset:33792\n"
    "ds_read_b64_tr_b16 v[168:169], v2 offset:34816\n"
    "ds_read_b64_tr_b16 v[170:171], v3 offset:34816\n"
    "ds_read_b64_tr_b16 v[172:173], v2 offset:35840\n"
    "ds_read_b64_tr_b16 v[174:175], v3 offset:35840\n"
    "ds_read_b32 v252, v1 offset:128\n"
    "v_mov_b32 v227, 3\n"
    "ds_write_b32 v226, v227 offset:32\n"
    "s_branch .Lh5j\n"
    ".Lh6:\n"
    "v_mfma_f32_16x16x32_bf16 v[216:219], v[104:107], v[152:155], 0\n"
    "buffer_store_dwordx4 v[200:203], v4, s[80:83], 0 offen offset:0 sc1\n"
    "v_mfma_f32_16x16x32_bf16 v[220:223], v[128:131], v[152:155], 0\n"
    "v_mul_f32 v208, v208, v252\n"
    "v_mul_f32 v209, v209, v252\n"
    "v_mfma_f32_16x16x32_bf16 v[216:219], v[108:111], v[156:159], v[216:219]\n"
    "v_mul_f32 v210, v210, v252\n"
    "v_mul_f32 v211, v211, v252\n"
    "v_mfma_f32_16x16x32_bf16 v[220:223], v[132:135], v[156:159], v[220:223]\n"
    "v_mul_f32 v212, v212, v252\n"
    "v_mul_f32 v213, v213, v252\n"
    "v_mfma_f32_16x16x32_bf16 v[216:219], v[112:115], v[160:163], v[216:219]\n"
    "v_mul_f32 v214, v214, v252\n"
    "v_mul_f32 v215, v215, v252\n"
    "v_mfma_f32_16x16x32_bf16 v[220:223], v[136:139], v[160:163], v[220:223]\n"
    "v_cvt_pk_bf16_f32 v208, v208, v209\n"
    "v_cvt_pk_bf16_f32 v209, v210, v211\n"
    "v_mfma_f32_16x16x32_bf16 v[216:219], v[116:119], v[164:167], v[216:219]\n"
    "v_cvt_pk_bf16_f32 v210, v212, v213\n"
    "v_cvt_pk_bf16_f32 v211, v214, v215\n"
    "v_mfma_f32_16x16x32_bf16 v[220:223], v[140:143], v[164:167], v[220:223]\n"
    "buffer_store_dwordx4 v[208:211], v4, s[80:83], 0 offen offset:64 sc1\n"
    "v_mfma_f32_16x16x32_bf16 v[216:219], v[120:123], v[168:171], v[216:219]\n"
    "v_mfma_f32_16x16x32_bf16 v[220:223], v[144:147], v[168:171], v[220:223]\n"
    "v_mfma_f32_16x16x32_bf16 v[216:219], v[124:127], v[172:175], v[216:219]\n"
    "v_mfma_f32_16x16x32_bf16 v[220:223], v[148:151], v[172:175], v[220:223]\n"
    ".Lh6r:\n"
    "s_sub_u32 s52, s52, 1\n"
    "s_cmp_lt_i32 s52, 0\n"
    "s_cbranch_scc1 .Lh6g\n"
    "s_sleep 1\n"
    "ds_read_b32 v228, v224 offset:176\n"
    "s_waitcnt lgkmcnt(0)\n"
    "v_cmp_gt_u32 vcc, 1, v228\n"
    "s_cbranch_vccnz .Lh6r\n"
    "s_branch .Lh6g\n"
    ".Lh6g:\n"
    "ds_read_b64_tr_b16 v[176:177], v2 offset:18432\n"
    "ds_read_b64_tr_b16 v[178:179], v3 offset:18432\n"
    "ds_read_b64_tr_b16 v[180:181], v2 offset:19456\n"
    "ds_read_b64_tr_b16 v[182:183], v3 offset:19456\n"
    "ds_read_b64_tr_b16 v[184:185], v2 offset:20480\n"
    "ds_read_b64_tr_b16 v[186:187], v3 offset:20480\n"
    "ds_read_b64_tr_b16 v[188:189], v2 offset:21504\n"
    "ds_read_b64_tr_b16 v[190:191], v3 offset:21504\n"
    "ds_read_b64_tr_b16 v[192:193], v2 offset:22528\n"
    "ds_read_b64_tr_b16 v[194:195], v3 offset:22528\n"
    "ds_read_b64_tr_b16 v[196:197], v2 offset:23552\n"
    "ds_read_b64_tr_b16 v[198:199], v3 offset:23552\n"
    "ds_read_b32 v253, v1 offset:0\n"
    "v_mov_b32 v227, 4\n"
    "ds_write_b32 v226, v227 offset:32\n"
    "s_branch .Lh6j\n"
    ".Lh7:\n"
    "v_mfma_f32_16x16x32_bf16 v[216:219], v[104:107], v[176:179], 0\n"
    "buffer_store_dwordx4 v[200:203], v5, s[80:83], 0 offen offset:0 sc1\n"
    "v_mfma_f32_16x16x32_bf16 v[220:223], v[128:131], v[176:179], 0\n"
    "v_mul_f32 v208, v208, v253\n"
    "v_mul_f32 v209, v209, v253\n"
    "v_mfma_f32_16x16x32_bf16 v[216:219], v[108:111], v[180:183], v[216:219]\n"
    "v_mul_f32 v210, v210, v253\n"
    "v_mul_f32 v211, v211, v253\n"
    "v_mfma_f32_16x16x32_bf16 v[220:223], v[132:135], v[180:183], v[220:223]\n"
    "v_mul_f32 v212, v212, v253\n"
    "v_mul_f32 v213, v213, v253\n"
    "v_mfma_f32_16x16x32_bf16 v[216:219], v[112:115], v[184:187], v[216:219]\n"
    "v_mul_f32 v214, v214, v253\n"
    "v_mul_f32 v215, v215, v253\n"
    "v_mfma_f32_16x16x32_bf16 v[220:223], v[136:139], v[184:187], v[220:223]\n"
    "v_cvt_pk_bf16_f32 v208, v208, v209\n"
    "v_cvt_pk_bf16_f32 v209, v210, v211\n"
    "v_mfma_f32_16x16x32_bf16 v[216:219], v[116:119], v[188:191], v[216:219]\n"
    "v_cvt_pk_bf16_f32 v210, v212, v213\n"
    "v_cvt_pk_bf16_f32 v211, v214, v215\n"
    "v_mfma_f32_16x16x32_bf16 v[220:223], v[140:143], v[188:191], v[220:223]\n"
    "buffer_store_dwordx4 v[208:211], v5, s[80:83], 0 offen offset:64 sc1\n"
    "v_mfma_f32_16x16x32_bf16 v[216:219], v[120:123], v[192:195], v[216:219]\n"
    "v_mfma_f32_16x16x32_bf16 v[220:223], v[144:147], v[192:195], v[220:223]\n"
    "v_mfma_f32_16x16x32_bf16 v[216:219], v[124:127], v[196:199], v[216:219]\n"
    "v_mfma_f32_16x16x32_bf16 v[220:223], v[148:151], v[196:199], v[220:223]\n"
    ".Lh7r:\n"
    "s_sub_u32 s52, s52, 1\n"
    "s_cmp_lt_i32 s52, 0\n"
    "s_cbranch_scc1 .Lh7g\n"
    "s_sleep 1\n"
    "ds_read_b32 v228, v224 offset:128\n"
    "s_waitcnt lgkmcnt(0)\n"
    "v_cmp_gt_u32 vcc, 2, v228\n"
    "s_cbranch_vccnz .Lh7r\n"
    "s_branch .Lh7g\n"
    ".Lh7g:\n"
    "ds_read_b64_tr_b16 v[152:153], v2 offset:24576\n"
    "ds_read_b64_tr_b16 v[154:155], v3 offset:24576\n"
    "ds_read_b64_tr_b16 v[156:157], v2 offset:25600\n"
    "ds_read_b64_tr_b16 v[158:159], v3 offset:25600\n"
    "ds_read_b64_tr_b16 v[160:161], v2 offset:26624\n"
    "ds_read_b64_tr_b16 v[162:163], v3 offset:26624\n"
    "ds_read_b64_tr_b16 v[164:165], v2 offset:27648\n"
    "ds_read_b64_tr_b16 v[166:167], v3 offset:27648\n"
    "ds_read_b64_tr_b16 v[168:169], v2 offset:28672\n"
    "ds_read_b64_tr_b16 v[170:171], v3 offset:28672\n"
    "ds_read_b64_tr_b16 v[172:173], v2 offset:29696\n"
    "ds_read_b64_tr_b16 v[174:175], v3 offset:29696\n"
    "ds_read_b32 v252, v1 offset:64\n"
    "v_mov_b32 v227, 5\n"
    "ds_write_b32 v226, v227 offset:32\n"
    "s_branch .Lh7j\n"
    ".Lh8:\n"
    "v_mfma_f32_16x16x32_bf16 v[216:219], v[104:107], v[152:155], 0\n"
    "buffer_store_dwordx4 v[200:203], v4, s[80:83], 0 offen offset:0 sc1\n"
    "v_mfma_f32_16x16x32_bf16 v[220:223], v[128:131], v[152:155], 0\n"
    "v_mul_f32 v208, v208, v252\n"
    "v_mul_f32 v209, v209, v252\n"
    "v_mfma_f32_16x16x32_bf16 v[216:219], v[108:111], v[156:159], v[216:219]\n"
    "v_mul_f32 v210, v210, v252\n"
    "v_mul_f32 v211, v211, v252\n"
    "v_mfma_f32_16x16x32_bf16 v[220:223], v[132:135], v[156:159], v[220:223]\n"
    "v_mul_f32 v212, v212, v252\n"
    "v_mul_f32 v213, v213, v252\n"
    "v_mfma_f32_16x16x32_bf16 v[216:219], v[112:115], v[160:163], v[216:219]\n"
    "v_mul_f32 v214, v214, v252\n"
    "v_mul_f32 v215, v215, v252\n"
    "v_mfma_f32_16x16x32_bf16 v[220:223], v[136:139], v[160:163], v[220:223]\n"
    "v_cvt_pk_bf16_f32 v208, v208, v209\n"
    "v_cvt_pk_bf16_f32 v209, v210, v211\n"
    "v_mfma_f32_16x16x32_bf16 v[216:219], v[116:119], v[164:167], v[216:219]\n"
    "v_cvt_pk_bf16_f32 v210, v212, v213\n"
    "v_cvt_pk_bf16_f32 v211, v214, v215\n"
    "v_mfma_f32_16x16x32_bf16 v[220:223], v[140:143], v[164:167], v[220:223]\n"
    "buffer_store_dwordx4 v[208:211], v4, s[80:83], 0 offen offset:64 sc1\n"
    "v_mfma_f32_16x16x32_bf16 v[216:219], v[120:123], v[168:171], v[216:219]\n"
    "v_mfma_f32_16x16x32_bf16 v[220:223], v[144:147], v[168:171], v[220:223]\n"
    "v_mfma_f32_16x16x32_bf16 v[216:219], v[124:127], v[172:175], v[216:219]\n"
    "v_mfma_f32_16x16x32_bf16 v[220:223], v[148:151], v[172:175], v[220:223]\n"
    ".Lh8r:\n"
    "s_sub_u32 s52, s52, 1\n"
    "s_cmp_lt_i32 s52, 0\n"
    "s_cbranch_scc1 .Lh8g\n"
    "s_sleep 1\n"
    "ds_read_b32 v228, v224 offset:144\n"
    "s_waitcnt lgkmcnt(0)\n"
    "v_cmp_gt_u32 vcc, 2, v228\n"
    "s_cbranch_vccnz .Lh8r\n"
    "s_branch .Lh8g\n"
    ".Lh8g:\n"
    "ds_read_b64_tr_b16 v[176:177], v2 offset:30720\n"
    "ds_read_b64_tr_b16 v[178:179], v3 offset:30720\n"
    "ds_read_b64_tr_b16 v[180:181], v2 offset:31744\n"
    "ds_read_b64_tr_b16 v[182:183], v3 offset:31744\n"
    "ds_read_b64_tr_b16 v[184:185], v2 offset:32768\n"
    "ds_read_b64_tr_b16 v[186:187], v3 offset:32768\n"
    "ds_read_b64_tr_b16 v[188:189], v2 offset:33792\n"
    "ds_read_b64_tr_b16 v[190:191], v3 offset:33792\n"
    "ds_read_b64_tr_b16 v[192:193], v2 offset:34816\n"
    "ds_read_b64_tr_b16 v[194:195], v3 offset:34816\n"
    "ds_read_b64_tr_b16 v[196:197], v2 offset:35840\n"
    "ds_read_b64_tr_b16 v[198:199], v3 offset:35840\n"
    "ds_read_b32 v253, v1 offset:128\n"
    "v_mov_b32 v227, 6\n"
    "ds_write_b32 v226, v227 offset:32\n"
    "s_branch .Lh8j\n"
    ".Lh9:\n"
    "v_mfma_f32_16x16x32_bf16 v[216:219], v[104:107], v[176:179], 0\n"
    "buffer_store_dwordx4 v[200:203], v5, s[80:83], 0 offen offset:0 sc1\n"
    "v_mfma_f32_16x16x32_bf16 v[220:223], v[128:131], v[176:179], 0\n"
    "v_mul_f32 v208, v208, v253\n"
    "v_mul_f32 v209, v209, v253\n"
    "v_mfma_f32_16x16x32_bf16 v[216:219], v[108:111], v[180:183], v[216:219]\n"
    "v_mul_f32 v210, v210, v253\n"
    "v_mul_f32 v211, v211, v253\n"
    "v_mfma_f32_16x16x32_bf16 v[220:223], v[132:135], v[180:183], v[220:223]\n"
    "v_mul_f32 v212, v212, v253\n"
    "v_mul_f32 v213, v213, v253\n"
    "v_mfma_f32_16x16x32_bf16 v[216:219], v[112:115], v[184:187], v[216:219]\n"
    "v_mul_f32 v214, v214, v253\n"
    "v_mul_f32 v215, v215, v253\n"
    "v_mfma_f32_16x16x32_bf16 v[220:223], v[136:139], v[184:187], v[220:223]\n"
    "v_cvt_pk_bf16_f32 v208, v208, v209\n"
    "v_cvt_pk_bf16_f32 v209, v210, v211\n"
    "v_mfma_f32_16x16x32_bf16 v[216:219], v[116:119], v[188:191], v[216:219]\n"
    "v_cvt_pk_bf16_f32 v210, v212, v213\n"
    "v_cvt_pk_bf16_f32 v211, v214, v215\n"
    "v_mfma_f32_16x16x32_bf16 v[220:223], v[140:143], v[188:191], v[220:223]\n"
    "buffer_store_dwordx4 v[208:211], v5, s[80:83], 0 offen offset:64 sc1\n"
    "v_mfma_f32_16x16x32_bf16 v[216:219], v[120:123], v[192:195], v[216:219]\n"
    "v_mfma_f32_16x16x32_bf16 v[220:223], v[144:147], v[192:195], v[220:223]\n"
    "v_mfma_f32_16x16x32_bf16 v[216:219], v[124:127], v[196:199], v[216:219]\n"
    "v_mfma_f32_16x16x32_bf16 v[220:223], v[148:151], v[196:199], v[220:223]\n"
    ".Lh9r:\n"
    "s_sub_u32 s52, s52, 1\n"
    "s_cmp_lt_i32 s52, 0\n"
    "s_cbranch_scc1 .Lh9g\n"
    "s_sleep 1\n"
    "ds_read_b32 v228, v224 offset:160\n"
    "s_waitcnt lgkmcnt(0)\n"
    "v_cmp_gt_u32 vcc, 2, v228\n"
    "s_cbranch_vccnz .Lh9r\n"
    "s_branch .Lh9g\n"
    ".Lh9g:\n"
    "ds_read_b64_tr_b16 v[152:153], v2 offset:18432\n"
    "ds_read_b64_tr_b16 v[154:155], v3 offset:18432\n"
    "ds_read_b64_tr_b16 v[156:157], v2 offset:19456\n"
    "ds_read_b64_tr_b16 v[158:159], v3 offset:19456\n"
    "ds_read_b64_tr_b16 v[160:161], v2 offset:20480\n"
    "ds_read_b64_tr_b16 v[162:163], v3 offset:20480\n"
    "ds_read_b64_tr_b16 v[164:165], v2 offset:21504\n"
    "ds_read_b64_tr_b16 v[166:167], v3 offset:21504\n"
    "ds_read_b64_tr_b16 v[168:169], v2 offset:22528\n"
    "ds_read_b64_tr_b16 v[170:171], v3 offset:22528\n"
    "ds_read_b64_tr_b16 v[172:173], v2 offset:23552\n"
    "ds_read_b64_tr_b16 v[174:175], v3 offset:23552\n"
    "ds_read_b32 v252, v1 offset:0\n"
    "v_mov_b32 v227, 7\n"
    "ds_write_b32 v226, v227 offset:32\n"
    "s_branch .Lh9j\n"
    ".Lh10:\n"
    "v_mfma_f32_16x16x32_bf16 v[216:219], v[104:107], v[152:155], 0\n"
    "buffer_store_dwordx4 v[200:203], v4, s[80:83], 0 offen offset:0 sc1\n"
    "v_mfma_f32_16x16x32_bf16 v[220:223], v[128:131], v[152:155], 0\n"
    "v_mul_f32 v208, v208, v252\n"
    "v_mul_f32 v209, v209, v252\n"
    "v_mfma_f32_16x16x32_bf16 v[216:219], v[108:111], v[156:159], v[216:219]\n"
    "v_mul_f32 v210, v210, v252\n"
    "v_mul_f32 v211, v211, v252\n"
    "v_mfma_f32_16x16x32_bf16 v[220:223], v[132:135], v[156:159], v[220:223]\n"
    "v_mul_f32 v212, v212, v252\n"
    "v_mul_f32 v213, v213, v252\n"
    "v_mfma_f32_16x16x32_bf16 v[216:219], v[112:115], v[160:163], v[216:219]\n"
    "v_mul_f32 v214, v214, v252\n"
    "v_mul_f32 v215, v215, v252\n"
    "v_mfma_f32_16x16x32_bf16 v[220:223], v[136:139], v[160:163], v[220:223]\n"
    "v_cvt_pk_bf16_f32 v208, v208, v209\n"
    "v_cvt_pk_bf16_f32 v209, v210, v211\n"
    "v_mfma_f32_16x16x32_bf16 v[216:219], v[116:119], v[164:167], v[216:219]\n"
    "v_cvt_pk_bf16_f32 v210, v212, v213\n"
    "v_cvt_pk_bf16_f32 v211, v214, v215\n"
    "v_mfma_f32_16x16x32_bf16 v[220:223], v[140:143], v[164:167], v[220:223]\n"
    "buffer_store_dwordx4 v[208:211], v4, s[80:83], 0 offen offset:64 sc1\n"
    "v_mfma_f32_16x16x32_bf16 v[216:219], v[120:123], v[168:171], v[216:219]\n"
    "v_mfma_f32_16x16x32_bf16 v[220:223], v[144:147], v[168:171], v[220:223]\n"
    "v_mfma_f32_16x16x32_bf16 v[216:219], v[124:127], v[172:175], v[216:219]\n"
    "v_mfma_f32_16x16x32_bf16 v[220:223], v[148:151], v[172:175], v[220:223]\n"
    ".Lh10r:\n"
    "s_sub_u32 s52, s52, 1\n"
    "s_cmp_lt_i32 s52, 0\n"
    "s_cbranch_scc1 .Lh10g\n"
    "s_sleep 1\n"
    "ds_read_b32 v228, v224 offset:176\n"
    "s_waitcnt lgkmcnt(0)\n"
    "v_cmp_gt_u32 vcc, 2, v228\n"
    "s_cbranch_vccnz .Lh10r\n"
    "s_branch .Lh10g\n"
    ".Lh10g:\n"
    "ds_read_b64_tr_b16 v[176:177], v2 offset:24576\n"
    "ds_read_b64_tr_b16 v[178:179], v3 offset:24576\n"
    "ds_read_b64_tr_b16 v[180:181], v2 offset:25600\n"
    "ds_read_b64_tr_b16 v[182:183], v3 offset:25600\n"
    "ds_read_b64_tr_b16 v[184:185], v2 offset:26624\n"
    "ds_read_b64_tr_b16 v[186:187], v3 offset:26624\n"
    "ds_read_b64_tr_b16 v[188:189], v2 offset:27648\n"
    "ds_read_b64_tr_b16 v[190:191], v3 offset:27648\n"
    "ds_read_b64_tr_b16 v[192:193], v2 offset:28672\n"
    "ds_read_b64_tr_b16 v[194:195], v3 offset:28672\n"
    "ds_read_b64_tr_b16 v[196:197], v2 offset:29696\n"
    "ds_read_b64_tr_b16 v[198:199], v3 offset:29696\n"
    "ds_read_b32 v253, v1 offset:64\n"
    "s_branch .Lh10j\n"
    ".Lh11:\n"
    "v_mfma_f32_16x16x32_bf16 v[216:219], v[104:107], v[176:179], 0\n"
    "buffer_store_dwordx4 v[200:203], v5, s[80:83], 0 offen offset:0 sc1\n"
    "v_mfma_f32_16x16x32_bf16 v[220:223], v[128:131], v[176:179], 0\n"
    "v_mul_f32 v208, v208, v253\n"
    "v_mul_f32 v209, v209, v253\n"
    "v_mfma_f32_16x16x32_bf16 v[216:219], v[108:111], v[180:183], v[216:219]\n"
    "v_mul_f32 v210, v210, v253\n"
    "v_mul_f32 v211, v211, v253\n"
    "v_mfma_f32_16x16x32_bf16 v[220:223], v[132:135], v[180:183], v[220:223]\n"
    "v_mul_f32 v212, v212, v253\n"
    "v_mul_f32 v213, v213, v253\n"
    "v_mfma_f32_16x16x32_bf16 v[216:219], v[112:115], v[184:187], v[216:219]\n"
    "v_mul_f32 v214, v214, v253\n"
    "v_mul_f32 v215, v215, v253\n"
    "v_mfma_f32_16x16x32_bf16 v[220:223], v[136:139], v[184:187], v[220:223]\n"
    "v_cvt_pk_bf16_f32 v208, v208, v209\n"
    "v_cvt_pk_bf16_f32 v209, v210, v211\n"
    "v_mfma_f32_16x16x32_bf16 v[216:219], v[116:119], v[188:191], v[216:219]\n"
    "v_cvt_pk_bf16_f32 v210, v212, v213\n"
    "v_cvt_pk_bf16_f32 v211, v214, v215\n"
    "v_mfma_f32_16x16x32_bf16 v[220:223], v[140:143], v[188:191], v[220:223]\n"
    "buffer_store_dwordx4 v[208:211], v5, s[80:83], 0 offen offset:64 sc1\n"
    "v_mfma_f32_16x16x32_bf16 v[216:219], v[120:123], v[192:195], v[216:219]\n"
    "v_mfma_f32_16x16x32_bf16 v[220:223], v[144:147], v[192:195], v[220:223]\n"
    "v_mfma_f32_16x16x32_bf16 v[216:219], v[124:127], v[196:199], v[216:219]\n"
    "v_mfma_f32_16x16x32_bf16 v[220:223], v[148:151], v[196:199], v[220:223]\n"
    ".Lh11r:\n"
    "s_sub_u32 s52, s52, 1\n"
    "s_cmp_lt_i32 s52, 0\n"
    "s_cbranch_scc1 .Lh11g\n"
    "s_sleep 1\n"
    "ds_read_b32 v228, v224 offset:128\n"
    "s_waitcnt lgkmcnt(0)\n"
    "v_cmp_gt_u32 vcc, 3, v228\n"
    "s_cbranch_vccnz .Lh11r\n"
    "s_branch .Lh11g\n"
    ".Lh11g:\n"
    "ds_read_b64_tr_b16 v[152:153], v2 offset:30720\n"
    "ds_read_b64_tr_b16 v[154:155], v3 offset:30720\n"
    "ds_read_b64_tr_b16 v[156:157], v2 offset:31744\n"
    "ds_read_b64_tr_b16 v[158:159], v3 offset:31744\n"
    "ds_read_b64_tr_b16 v[160:161], v2 offset:32768\n"
    "ds_read_b64_tr_b16 v[162:163], v3 offset:32768\n"
    "ds_read_b64_tr_b16 v[164:165], v2 offset:33792\n"
    "ds_read_b64_tr_b16 v[166:167], v3 offset:33792\n"
    "ds_read_b64_tr_b16 v[168:169], v2 offset:34816\n"
    "ds_read_b64_tr_b16 v[170:171], v3 offset:34816\n"
    "ds_read_b64_tr_b16 v[172:173], v2 offset:35840\n"
    "ds_read_b64_tr_b16 v[174:175], v3 offset:35840\n"
    "ds_read_b32 v252, v1 offset:128\n"
    "s_branch .Lh11j\n"
    ".Lh12:\n"
    "v_mfma_f32_16x16x32_bf16 v[208:211], v[56:59], v[152:155], 0\n"
    "v_mfma_f32_16x16x32_bf16 v[212:215], v[80:83], v[152:155], 0\n"
    "v_mfma_f32_16x16x32_bf16 v[208:211], v[60:63], v[156:159], v[208:211]\n"
    "v_mfma_f32_16x16x32_bf16 v[212:215], v[84:87], v[156:159], v[212:215]\n"
    "v_mul_f32 v200, v200, v252\n"
    "v_mul_f32 v201, v201, v252\n"
    "v_mfma_f32_16x16x32_bf16 v[208:211], v[64:67], v[160:163], v[208:211]\n"
    "v_mul_f32 v202, v202, v252\n"
    "v_mul_f32 v203, v203, v252\n"
    "v_mfma_f32_16x16x32_bf16 v[212:215], v[88:91], v[160:163], v[212:215]\n"
    "v_mul_f32 v204, v204, v252\n"
    "v_mul_f32 v205, v205, v252\n"
    "v_mfma_f32_16x16x32_bf16 v[208:211], v[68:71], v[164:167], v[208:211]\n"
    "v_mul_f32 v206, v206, v252\n"
    "v_mul_f32 v207, v207, v252\n"
    "v_mfma_f32_16x16x32_bf16 v[212:215], v[92:95], v[164:167], v[212:215]\n"
    "v_cvt_pk_bf16_f32 v200, v200, v201\n"
    "v_cvt_pk_bf16_f32 v201, v202, v203\n"
    "v_mfma_f32_16x16x32_bf16 v[208:211], v[72:75], v[168:171], v[208:211]\n"
    "v_cvt_pk_bf16_f32 v202, v204, v205\n"
    "v_cvt_pk_bf16_f32 v203, v206, v207\n"
    "v_mfma_f32_16x16x32_bf16 v[212:215], v[96:99], v[168:171], v[212:215]\n"
    "buffer_store_dwordx4 v[200:203], v4, s[80:83], 0 offen offset:0 sc1\n"
    "v_mfma_f32_16x16x32_bf16 v[208:211], v[76:79], v[172:175], v[208:211]\n"
    "v_mfma_f32_16x16x32_bf16 v[212:215], v[100:103], v[172:175], v[212:215]\n"
    ".Lh12r:\n"
    "s_sub_u32 s52, s52, 1\n"
    "s_cmp_lt_i32 s52, 0\n"
    "s_cbranch_scc1 .Lh12g\n"
    "s_sleep 1\n"
    "ds_read_b32 v228, v224 offset:144\n"
    "s_waitcnt lgkmcnt(0)\n"
    "v_cmp_gt_u32 vcc, 3, v228\n"
    "s_cbranch_vccnz .Lh12r\n"
    "s_branch .Lh12g\n"
    ".Lh12g:\n"
    "ds_read_b64_tr_b16 v[176:177], v2 offset:18432\n"
    "ds_read_b64_tr_b16 v[178:179], v3 offset:18432\n"
    "ds_read_b64_tr_b16 v[180:181], v2 offset:19456\n"
    "ds_read_b64_tr_b16 v[182:183], v3 offset:19456\n"
    "ds_read_b64_tr_b16 v[184:185], v2 offset:20480\n"
    "ds_read_b64_tr_b16 v[186:187], v3 offset:20480\n"
    "ds_read_b64_tr_b16 v[188:189], v2 offset:21504\n"
    "ds_read_b64_tr_b16 v[190:191], v3 offset:21504\n"
    "ds_read_b64_tr_b16 v[192:193], v2 offset:22528\n"
    "ds_read_b64_tr_b16 v[194:195], v3 offset:22528\n"
    "ds_read_b64_tr_b16 v[196:197], v2 offset:23552\n"
    "ds_read_b64_tr_b16 v[198:199], v3 offset:23552\n"
    "ds_read_b32 v253, v1 offset:0\n"
    "s_branch .Lh12j\n"
    ".Lh13:\n"
    "v_mfma_f32_16x16x32_bf16 v[208:211], v[56:59], v[176:179], 0\n"
    "v_mfma_f32_16x16x32_bf16 v[212:215], v[80:83], v[176:179], 0\n"
    "v_mfma_f32_16x16x32_bf16 v[208:211], v[60:63], v[180:183], v[208:211]\n"
    "v_mfma_f32_16x16x32_bf16 v[212:215], v[84:87], v[180:183], v[212:215]\n"
    "v_mul_f32 v200, v200, v253\n"
    "v_mul_f32 v201, v201, v253\n"
    "v_mfma_f32_16x16x32_bf16 v[208:211], v[64:67], v[184:187], v[208:211]\n"
    "v_mul_f32 v202, v202, v253\n"
    "v_mul_f32 v203, v203, v253\n"
    "v_mfma_f32_16x16x32_bf16 v[212:215], v[88:91], v[184:187], v[212:215]\n"
    "v_mul_f32 v204, v204, v253\n"
    "v_mul_f32 v205, v205, v253\n"
    "v_mfma_f32_16x16x32_bf16 v[208:211], v[68:71], v[188:191], v[208:211]\n"
    "v_mul_f32 v206, v206, v253\n"
    "v_mul_f32 v207, v207, v253\n"
    "v_mfma_f32_16x16x32_bf16 v[212:215], v[92:95], v[188:191], v[212:215]\n"
    "v_cvt_pk_bf16_f32 v200, v200, v201\n"
    "v_cvt_pk_bf16_f32 v201, v202, v203\n"
    "v_mfma_f32_16x16x32_bf16 v[208:211], v[72:75], v[192:195], v[208:211]\n"
    "v_cvt_pk_bf16_f32 v202, v204, v205\n"
    "v_cvt_pk_bf16_f32 v203, v206, v207\n"
    "v_mfma_f32_16x16x32_bf16 v[212:215], v[96:99], v[192:195], v[212:215]\n"
    "buffer_store_dwordx4 v[200:203], v5, s[80:83], 0 offen offset:0 sc1\n"
    "v_mfma_f32_16x16x32_bf16 v[208:211], v[76:79], v[196:199], v[208:211]\n"
    "v_mfma_f32_16x16x32_bf16 v[212:215], v[100:103], v[196:199], v[212:215]\n"
    ".Lh13r:\n"
    "s_sub_u32 s52, s52, 1\n"
    "s_cmp_lt_i32 s52, 0\n"
    "s_cbranch_scc1 .Lh13g\n"
    "s_sleep 1\n"
    "ds_read_b32 v228, v224 offset:160\n"
    "s_waitcnt lgkmcnt(0)\n"
    "v_cmp_gt_u32 vcc, 3, v228\n"
    "s_cbranch_vccnz .Lh13r\n"
    "s_branch .Lh13g\n"
    ".Lh13g:\n"
    "ds_read_b64_tr_b16 v[152:153], v2 offset:6144\n"
    "ds_read_b64_tr_b16 v[154:155], v3 offset:6144\n"
    "ds_read_b64_tr_b16 v[156:157], v2 offset:7168\n"
    "ds_read_b64_tr_b16 v[158:159], v3 offset:7168\n"
    "ds_read_b64_tr_b16 v[160:161], v2 offset:8192\n"
    "ds_read_b64_tr_b16 v[162:163], v3 offset:8192\n"
    "ds_read_b64_tr_b16 v[164:165], v2 offset:9216\n"
    "ds_read_b64_tr_b16 v[166:167], v3 offset:9216\n"
    "ds_read_b64_tr_b16 v[168:169], v2 offset:10240\n"
    "ds_read_b64_tr_b16 v[170:171], v3 offset:10240\n"
    "ds_read_b64_tr_b16 v[172:173], v2 offset:11264\n"
    "ds_read_b64_tr_b16 v[174:175], v3 offset:11264\n"
    "ds_read_b32 v252, v1 offset:256\n"
    "s_branch .Lh13j\n"
    ".Lh14:\n"
    "v_mfma_f32_16x16x32_bf16 v[208:211], v[56:59], v[152:155], 0\n"
    "v_mfma_f32_16x16x32_bf16 v[212:215], v[80:83], v[152:155], 0\n"
    "v_mfma_f32_16x16x32_bf16 v[208:211], v[60:63], v[156:159], v[208:211]\n"
    "v_mfma_f32_16x16x32_bf16 v[212:215], v[84:87], v[156:159], v[212:215]\n"
    "v_mul_f32 v200, v200, v252\n"
    "v_mul_f32 v201, v201, v252\n"
    "v_mfma_f32_16x16x32_bf16 v[208:211], v[64:67], v[160:163], v[208:211]\n"
    "v_mul_f32 v202, v202, v252\n"
    "v_mul_f32 v203, v203, v252\n"
    "v_mfma_f32_16x16x32_bf16 v[212:215], v[88:91], v[160:163], v[212:215]\n"
    "v_mul_f32 v204, v204, v252\n"
    "v_mul_f32 v205, v205, v252\n"
    "v_mfma_f32_16x16x32_bf16 v[208:211], v[68:71], v[164:167], v[208:211]\n"
    "v_mul_f32 v206, v206, v252\n"
    "v_mul_f32 v207, v207, v252\n"
    "v_mfma_f32_16x16x32_bf16 v[212:215], v[92:95], v[164:167], v[212:215]\n"
    "v_cvt_pk_bf16_f32 v200, v200, v201\n"
    "v_cvt_pk_bf16_f32 v201, v202, v203\n"
    "v_mfma_f32_16x16x32_bf16 v[208:211], v[72:75], v[168:171], v[208:211]\n"
    "v_cvt_pk_bf16_f32 v202, v204, v205\n"
    "v_cvt_pk_bf16_f32 v203, v206, v207\n"
    "v_mfma_f32_16x16x32_bf16 v[212:215], v[96:99], v[168:171], v[212:215]\n"
    "buffer_store_dwordx4 v[200:203], v4, s[80:83], 0 offen offset:0 sc1\n"
    "v_mfma_f32_16x16x32_bf16 v[208:211], v[76:79], v[172:175], v[208:211]\n"
    "v_mfma_f32_16x16x32_bf16 v[212:215], v[100:103], v[172:175], v[212:215]\n"
    ".Lh14r:\n"
    "s_sub_u32 s52, s52, 1\n"
    "s_cmp_lt_i32 s52, 0\n"
    "s_cbranch_scc1 .Lh14g\n"
    "s_sleep 1\n"
    "ds_read_b32 v228, v224 offset:176\n"
    "s_waitcnt lgkmcnt(0)\n"
    "v_cmp_gt_u32 vcc, 3, v228\n"
    "s_cbranch_vccnz .Lh14r\n"
    "s_branch .Lh14g\n"
    ".Lh14g:\n"
    "ds_read_b64_tr_b16 v[176:177], v2 offset:12288\n"
    "ds_read_b64_tr_b16 v[178:179], v3 offset:12288\n"
    "ds_read_b64_tr_b16 v[180:181], v2 offset:13312\n"
    "ds_read_b64_tr_b16 v[182:183], v3 offset:13312\n"
    "ds_read_b64_tr_b16 v[184:185], v2 offset:14336\n"
    "ds_read_b64_tr_b16 v[186:187], v3 offset:14336\n"
    "ds_read_b64_tr_b16 v[188:189], v2 offset:15360\n"
    "ds_read_b64_tr_b16 v[190:191], v3 offset:15360\n"
    "ds_read_b64_tr_b16 v[192:193], v2 offset:16384\n"
    "ds_read_b64_tr_b16 v[194:195], v3 offset:16384\n"
    "ds_read_b64_tr_b16 v[196:197], v2 offset:17408\n"
    "ds_read_b64_tr_b16 v[198:199], v3 offset:17408\n"
    "ds_read_b32 v253, v1 offset:320\n"
    "s_branch .Lh14j\n"
    ".Lh_e12:\n"
    "s_waitcnt lgkmcnt(1)\n"
    "v_mfma_f32_16x16x32_bf16 v[200:203], v[8:11], v[176:179], 0\n"
    "v_mul_f32 v208, v208, v252\n"
    "v_mul_f32 v209, v209, v252\n"
    "v_mfma_f32_16x16x32_bf16 v[204:207], v[32:35], v[176:179], 0\n"
    "v_mul_f32 v210, v210, v252\n"
    "v_mul_f32 v211, v211, v252\n"
    "v_mfma_f32_16x16x32_bf16 v[200:203], v[12:15], v[180:183], v[200:203]\n"
    "v_mul_f32 v212, v212, v252\n"
    "v_mul_f32 v213, v213, v252\n"
    "v_mfma_f32_16x16x32_bf16 v[204:207], v[36:39], v[180:183], v[204:207]\n"
    "v_mul_f32 v214, v214, v252\n"
    "v_mul_f32 v215, v215, v252\n"
    "v_mfma_f32_16x16x32_bf16 v[200:203], v[16:19], v[184:187], v[200:203]\n"
    "v_cvt_pk_bf16_f32 v208, v208, v209\n"
    "v_cvt_pk_bf16_f32 v209, v210, v211\n"
    "v_mfma_f32_16x16x32_bf16 v[204:207], v[40:43], v[184:187], v[204:207]\n"
    "v_cvt_pk_bf16_f32 v210, v212, v213\n"
    "v_cvt_pk_bf16_f32 v211, v214, v215\n"
    "v_mfma_f32_16x16x32_bf16 v[200:203], v[20:23], v[188:191], v[200:203]\n"
    "buffer_store_dwordx4 v[208:211], v4, s[80:83], 0 offen offset:64 sc1\n"
    "v_mfma_f32_16x16x32_bf16 v[204:207], v[44:47], v[188:191], v[204:207]\n"
    "v_mfma_f32_16x16x32_bf16 v[200:203], v[24:27], v[192:195], v[200:203]\n"
    "v_add_u32 v4, v0, v4\n"
    "v_mfma_f32_16x16x32_bf16 v[204:207], v[48:51], v[192:195], v[204:207]\n"
    "v_mfma_f32_16x16x32_bf16 v[200:203], v[28:31], v[196:199], v[200:203]\n"
    "v_mfma_f32_16x16x32_bf16 v[204:207], v[52:55], v[196:199], v[204:207]\n"
    "v_mfma_f32_16x16x32_bf16 v[208:211], v[56:59], v[176:179], 0\n"
    "v_mfma_f32_16x16x32_bf16 v[212:215], v[80:83], v[176:179], 0\n"
    "v_mfma_f32_16x16x32_bf16 v[208:211], v[60:63], v[180:183], v[208:211]\n"
    "v_mfma_f32_16x16x32_bf16 v[212:215], v[84:87], v[180:183], v[212:215]\n"
    "v_mfma_f32_16x16x32_bf16 v[208:211], v[64:67], v[184:187], v[208:211]\n"
    "v_mfma_f32_16x16x32_bf16 v[212:215], v[88:91], v[184:187], v[212:215]\n"
    "s_waitcnt lgkmcnt(0)\n"
    "v_mul_f32 v200, v200, v253\n"
    "v_mfma_f32_16x16x32_bf16 v[208:211], v[68:71], v[188:191], v[208:211]\n"
    "v_mul_f32 v201, v201, v253\n"
    "v_mul_f32 v202, v202, v253\n"
    "v_mfma_f32_16x16x32_bf16 v[212:215], v[92:95], v[188:191], v[212:215]\n"
    "v_mul_f32 v203, v203, v253\n"
    "v_mul_f32 v204, v204, v253\n"
    "v_mfma_f32_16x16x32_bf16 v[208:211], v[72:75], v[192:195], v[208:211]\n"
    "v_mul_f32 v205, v205, v253\n"
    "v_mul_f32 v206, v206, v253\n"
    "v_mfma_f32_16x16x32_bf16 v[212:215], v[96:99], v[192:195], v[212:215]\n"
    "v_mul_f32 v207, v207, v253\n"
    "v_cvt_pk_bf16_f32 v200, v200, v201\n"
    "v_mfma_f32_16x16x32_bf16 v[208:211], v[76:79], v[196:199], v[208:211]\n"
    "v_cvt_pk_bf16_f32 v201, v202, v203\n"
    "v_cvt_pk_bf16_f32 v202, v204, v205\n"
    "v_mfma_f32_16x16x32_bf16 v[212:215], v[100:103], v[196:199], v[212:215]\n"
    "v_cvt_pk_bf16_f32 v203, v206, v207\n"
    "buffer_store_dwordx4 v[200:203], v5, s[80:83], 0 offen offset:0 sc1\n"
    "s_nop 2\n"
    "v_mul_f32 v208, v208, v253\n"
    "v_mul_f32 v209, v209, v253\n"
    "v_mul_f32 v210, v210, v253\n"
    "v_mul_f32 v211, v211, v253\n"
    "v_mul_f32 v212, v212, v253\n"
    "v_mul_f32 v213, v213, v253\n"
    "v_mul_f32 v214, v214, v253\n"
    "v_mul_f32 v215, v215, v253\n"
    "v_cvt_pk_bf16_f32 v208, v208, v209\n"
    "v_cvt_pk_bf16_f32 v209, v210, v211\n"
    "v_cvt_pk_bf16_f32 v210, v212, v213\n"
    "v_cvt_pk_bf16_f32 v211, v214, v215\n"
    "buffer_store_dwordx4 v[208:211], v5, s[80:83], 0 offen offset:64 sc1\n"
    "s_branch .Lh_end\n"
    ".Lh15:\n"
    "v_mfma_f32_16x16x32_bf16 v[208:211], v[56:59], v[176:179], 0\n"
    "v_mfma_f32_16x16x32_bf16 v[212:215], v[80:83], v[176:179], 0\n"
    "v_mfma_f32_16x16x32_bf16 v[208:211], v[60:63], v[180:183], v[208:211]\n"
    "v_mfma_f32_16x16x32_bf16 v[212:215], v[84:87], v[180:183], v[212:215]\n"
    "v_mul_f32 v200, v200, v253\n"
    "v_mul_f32 v201, v201, v253\n"
    "v_mfma_f32_16x16x32_bf16 v[208:211], v[64:67], v[184:187], v[208:211]\n"
    "v_mul_f32 v202, v202, v253\n"
    "v_mul_f32 v203, v203, v253\n"
    "v_mfma_f32_16x16x32_bf16 v[212:215], v[88:91], v[184:187], v[212:215]\n"
    "v_mul_f32 v204, v204, v253\n"
    "v_mul_f32 v205, v205, v253\n"
    "v_mfma_f32_16x16x32_bf16 v[208:211], v[68:71], v[188:191], v[208:211]\n"
    "v_mul_f32 v206, v206, v253\n"
    "v_mul_f32 v207, v207, v253\n"
    "v_mfma_f32_16x16x32_bf16 v[212:215], v[92:95], v[188:191], v[212:215]\n"
    "v_cvt_pk_bf16_f32 v200, v200, v201\n"
    "v_cvt_pk_bf16_f32 v201, v202, v203\n"
    "v_mfma_f32_16x16x32_bf16 v[208:211], v[72:75], v[192:195], v[208:211]\n"
    "v_cvt_pk_bf16_f32 v202, v204, v205\n"
    "v_cvt_pk_bf16_f32 v203, v206, v207\n"
    "v_mfma_f32_16x16x32_bf16 v[212:215], v[96:99], v[192:195], v[212:215]\n"
    "buffer_store_dwordx4 v[200:203], v5, s[80:83], 0 offen offset:0 sc1\n"
    "v_mfma_f32_16x16x32_bf16 v[208:211], v[76:79], v[196:199], v[208:211]\n"
    "v_mfma_f32_16x16x32_bf16 v[212:215], v[100:103], v[196:199], v[212:215]\n"
    ".Lh15r:\n"
    "s_sub_u32 s52, s52, 1\n"
    "s_cmp_lt_i32 s52, 0\n"
    "s_cbranch_scc1 .Lh15g\n"
    "s_sleep 1\n"
    "ds_read_b32 v228, v224 offset:832\n"
    "s_waitcnt lgkmcnt(0)\n"
    "v_cmp_gt_u32 vcc, 4, v228\n"
    "s_cbranch_vccnz .Lh15r\n"
    "s_branch .Lh15g\n"
    ".Lh15g:\n"
    "ds_read_b64_tr_b16 v[152:153], v2 offset:0\n"
    "ds_read_b64_tr_b16 v[154:155], v3 offset:0\n"
    "ds_read_b64_tr_b16 v[156:157], v2 offset:1024\n"
    "ds_read_b64_tr_b16 v[158:159], v3 offset:1024\n"
    "ds_read_b64_tr_b16 v[160:161], v2 offset:2048\n"
    "ds_read_b64_tr_b16 v[162:163], v3 offset:2048\n"
    "ds_read_b64_tr_b16 v[164:165], v2 offset:3072\n"
    "ds_read_b64_tr_b16 v[166:167], v3 offset:3072\n"
    "ds_read_b64_tr_b16 v[168:169], v2 offset:4096\n"
    "ds_read_b64_tr_b16 v[170:171], v3 offset:4096\n"
    "ds_read_b64_tr_b16 v[172:173], v2 offset:5120\n"
    "ds_read_b64_tr_b16 v[174:175], v3 offset:5120\n"
    "ds_read_b32 v252, v1 offset:192\n"
    "s_branch .Lh15j\n"
    ".Lh_last:\n"
                :: "{v1}"(vi), "{v[2:3]}"(vz), "{v[4:7]}"(vo), "{v[224:227]}"(fl), "{v[230:233]}"(xo), "{v234}"(xo4), "{v235}"(lf[0]), "{v236}"(lf[1]),
                   "{v[238:239]}"(xp), "{s[40:41]}"(ws), "{s[42:43]}"(xb), "{s44}"(UNI(rm.o * IROWB)), "{s45}"(UNI(rm.d * IROWB)),
                   "{s46}"(UNI(2 * rm.d * IROWB)), "{v0}"(d56), "{s84}"(UNI(axis_at(rm, 12) * IROWB)), "{s85}"(UNI(axis_at(rm, 13) * IROWB)), "{s48}"(UNI(WS_ACK + (2 * bid + h) * 128)),
                   "{s50}"(h), "{s58}"(UNI(t12)), "{s[80:83]}"(srd)
                : "memory", "v255", "s51", "s52", "s53", "s54", "s55", "s56", "s57", "s59", "s60", "s61", "s76", "s77", "s79", "s90", "s91", "s92", "s93", "s94", "s95", "vcc", "scc");
        }
    } else {
        const int a = (0x3210 >> (4 * wave - 16)) & 3, H = a & 1, n0 = a, u0 = a >> 1;
        const AttC c = att_const(lane, H, cedge);
        f32x4 cbM, cbF, cbL;
        const float NEG = -__builtin_inff();
#pragma unroll
        for (int r = 0; r < 4; ++r) {
            const bool ok = (c.pk >> (4 + r)) & 1;
            cbM[r] = ok ? 0.f : NEG;
            cbF[r] = ok && e == 0 ? 0.f : NEG;
            cbL[r] = ok && e == 1 ? 0.f : NEG;
        }
        const int sl = n0 >= 3 ? n0 - 3 : n0;
        const u32x4 va = {c.sb, c.v0, c.v1, (unsigned)(LQ + sl * 6144 + lane * 16)};
        const u32x4 vb = {(unsigned)(LZ + sl * 6144 + (16 * g + (i16 ^ (4 * g))) * 16), (c.pk & 15) * 0x1111u, vf, lane < 4 ? vf + 16 * a : OOB};
        const u64 slots = (redge ? 0x975A86420ull : 0x420CA86420ull) >> (4 * u0);
        asm volatile("s_nop 7\n"
    "s_sub_u32 s56, s45, 2\n"
    ".La_join:\n"
    "s_mov_b32 s52, 0x40000\n"
    "s_bfe_u32 s48, s60, 0x40000\n"
    "s_bfe_u32 s49, s60, 0x40004\n"
    "s_bfe_u32 s50, s60, 0x40008\n"
    "s_bfe_u32 s51, s60, 0x4000c\n"
    "s_mul_i32 s48, s48, 8896\n"
    "s_mul_i32 s49, s49, 8896\n"
    "s_mul_i32 s50, s50, 8896\n"
    "s_mul_i32 s51, s51, 8896\n"
    "s_lshr_b64 s[60:61], s[60:61], 8\n"
    "v_add_u32 v244, s48, v0\n"
    "v_add_u32 v245, s49, v0\n"
    "v_add_u32 v246, s50, v0\n"
    "v_add_u32 v247, s51, v0\n"
    "v_add_u32 v200, s48, v1\n"
    "v_add_u32 v201, s49, v1\n"
    "v_add_u32 v202, s48, v2\n"
    "v_add_u32 v203, s49, v2\n"
    "s_mov_b32 s69, 1\n"
    "s_mov_b32 s53, 0\n"
    "s_add_u32 s55, s45, 1\n"
    "s_lshr_b32 s57, s45, 2\n"
    "v_mov_b32 v248, 0x3f803f80\n"
    "v_mov_b32 v249, 0x3f803f80\n"
    "v_mov_b32 v250, 0x3f803f80\n"
    "v_mov_b32 v251, 0x3f803f80\n"
    "s_mul_i32 s62, s57, 6\n"
    "ds_read_b32 v253, v6 offset:0\n"
    "s_waitcnt lgkmcnt(0)\n"
    "v_cmp_gt_u32 vcc, s54, v253\n"
    "s_cbranch_vccnz .La1\n"
    ".La1b:\n"
    "ds_read_b32 v254, v6 offset:16\n"
    "s_waitcnt lgkmcnt(0)\n"
    "v_cmp_gt_u32 vcc, s55, v254\n"
    "s_cbranch_vccnz .La2\n"
    ".La2b:\n"
    "ds_read_b128 v[8:11], v3 offset:0\n"
    "ds_read_b128 v[56:59], v244 offset:0\n"
    "ds_read_b128 v[12:15], v3 offset:1024\n"
    "ds_read_b128 v[60:63], v244 offset:64\n"
    "ds_read_b128 v[16:19], v3 offset:2048\n"
    "ds_read_b128 v[64:67], v244 offset:128\n"
    "ds_read_b128 v[20:23], v3 offset:3072\n"
    "ds_read_b128 v[68:71], v244 offset:192\n"
    "ds_read_b128 v[24:27], v3 offset:4096\n"
    "ds_read_b128 v[28:31], v3 offset:5120\n"
    "ds_read_b128 v[32:35], v244 offset:256\n"
    "ds_read_b128 v[36:39], v244 offset:320\n"
    "ds_read_b128 v[40:43], v245 offset:0\n"
    "ds_read_b128 v[44:47], v245 offset:64\n"
    ".La_loop:\n"
    "s_setprio 1\n"
    "s_add_u32 s57, s57, 1\n"
    "s_waitcnt lgkmcnt(8)\n"
    "v_mfma_f32_16x16x32_bf16 v[72:75], v[56:59], v[8:11], v[236:239]\n"
    "ds_read_b128 v[48:51], v245 offset:128\n"
    "v_mov_b32 v252, s57\n"
    "v_mfma_f32_16x16x32_bf16 v[72:75], v[60:63], v[12:15], v[72:75]\n"
    "ds_read_b128 v[52:55], v245 offset:192\n"
    "v_add_u32 v4, s53, v4\n"
    "v_mfma_f32_16x16x32_bf16 v[72:75], v[64:67], v[16:19], v[72:75]\n"
    "ds_read_b128 v[56:59], v245 offset:256\n"
    "s_waitcnt lgkmcnt(5)\n"
    "v_mfma_f32_16x16x32_bf16 v[72:75], v[68:71], v[20:23], v[72:75]\n"
    "ds_read_b128 v[60:63], v245 offset:320\n"
    "ds_write_b32 v7, v252 offset:192\n"
    "v_mfma_f32_16x16x32_bf16 v[72:75], v[32:35], v[24:27], v[72:75]\n"
    "ds_read_b128 v[64:67], v244 offset:8896\n"
    "ds_read_b128 v[208:211], v7 offset:32768\n"
    "v_mfma_f32_16x16x32_bf16 v[72:75], v[36:39], v[28:31], v[72:75]\n"
    "ds_read_b128 v[68:71], v244 offset:8960\n"
    "s_waitcnt lgkmcnt(7)\n"
    "v_mfma_f32_16x16x32_bf16 v[76:79], v[40:43], v[8:11], v[232:235]\n"
    "ds_read_b128 v[32:35], v244 offset:9024\n"
    "ds_read_b128 v[212:215], v7 offset:32768\n"
    "v_mfma_f32_16x16x32_bf16 v[76:79], v[44:47], v[12:15], v[76:79]\n"
    "ds_read_b128 v[36:39], v244 offset:9088\n"
    "ds_read_b128 v[216:219], v7 offset:32768\n"
    "v_mfma_f32_16x16x32_bf16 v[76:79], v[48:51], v[16:19], v[76:79]\n"
    "ds_read_b128 v[40:43], v244 offset:9152\n"
    "s_waitcnt lgkmcnt(9)\n"
    "v_mfma_f32_16x16x32_bf16 v[76:79], v[52:55], v[20:23], v[76:79]\n"
    "ds_read_b128 v[44:47], v244 offset:9216\n"
    "v_max_f32 v72, v72, v73\n"
    "v_mfma_f32_16x16x32_bf16 v[76:79], v[56:59], v[24:27], v[76:79]\n"
    "ds_read_b128 v[48:51], v245 offset:8896\n"
    "v_max_f32 v74, v74, v75\n"
    "v_mfma_f32_16x16x32_bf16 v[76:79], v[60:63], v[28:31], v[76:79]\n"
    "ds_read_b128 v[52:55], v245 offset:8960\n"
    "s_waitcnt lgkmcnt(7)\n"
    "v_mfma_f32_16x16x32_bf16 v[80:83], v[64:67], v[8:11], v[232:235]\n"
    "ds_read_b128 v[56:59], v245 offset:9024\n"
    "v_max_f32 v206, v72, v74\n"
    "v_mfma_f32_16x16x32_bf16 v[80:83], v[68:71], v[12:15], v[80:83]\n"
    "ds_read_b128 v[60:63], v245 offset:9088\n"
    "ds_read_b128 v[220:223], v7 offset:32768\n"
    "v_mfma_f32_16x16x32_bf16 v[80:83], v[32:35], v[16:19], v[80:83]\n"
    "ds_read_b128 v[64:67], v245 offset:9152\n"
    "s_waitcnt lgkmcnt(6)\n"
    "v_mfma_f32_16x16x32_bf16 v[80:83], v[36:39], v[20:23], v[80:83]\n"
    "ds_read_b128 v[68:71], v245 offset:9216\n"
    "v_max_f32 v76, v76, v77\n"
    "v_mfma_f32_16x16x32_bf16 v[80:83], v[40:43], v[24:27], v[80:83]\n"
    "ds_read_b128 v[32:35], v246 offset:0\n"
    "v_max_f32 v78, v78, v79\n"
    "v_mfma_f32_16x16x32_bf16 v[80:83], v[44:47], v[28:31], v[80:83]\n"
    "ds_read_b128 v[36:39], v246 offset:64\n"
    "s_waitcnt lgkmcnt(6)\n"
    "v_mfma_f32_16x16x32_bf16 v[84:87], v[48:51], v[8:11], v[232:235]\n"
    "ds_read_b128 v[40:43], v246 offset:128\n"
    "v_max3_f32 v206, v206, v76, v78\n"
    "v_mfma_f32_16x16x32_bf16 v[84:87], v[52:55], v[12:15], v[84:87]\n"
    "ds_read_b128 v[44:47], v246 offset:192\n"
    "ds_read_b128 v[224:227], v7 offset:32768\n"
    "v_mfma_f32_16x16x32_bf16 v[84:87], v[56:59], v[16:19], v[84:87]\n"
    "ds_read_b128 v[48:51], v246 offset:256\n"
    "s_waitcnt lgkmcnt(6)\n"
    "v_mfma_f32_16x16x32_bf16 v[84:87], v[60:63], v[20:23], v[84:87]\n"
    "ds_read_b128 v[52:55], v246 offset:320\n"
    "v_max_f32 v80, v80, v81\n"
    "v_mfma_f32_16x16x32_bf16 v[84:87], v[64:67], v[24:27], v[84:87]\n"
    "ds_read_b128 v[56:59], v247 offset:0\n"
    "v_max_f32 v82, v82, v83\n"
    "v_mfma_f32_16x16x32_bf16 v[84:87], v[68:71], v[28:31], v[84:87]\n"
    "ds_read_b128 v[60:63], v247 offset:64\n"
    "s_waitcnt lgkmcnt(6)\n"
    "v_mfma_f32_16x16x32_bf16 v[88:91], v[32:35], v[8:11], v[232:235]\n"
    "ds_read_b128 v[64:67], v247 offset:128\n"
    "v_max3_f32 v206, v206, v80, v82\n"
    "v_mfma_f32_16x16x32_bf16 v[88:91], v[36:39], v[12:15], v[88:91]\n"
    "ds_read_b128 v[68:71], v247 offset:192\n"
    "ds_read_b128 v[228:231], v7 offset:32768\n"
    "v_mfma_f32_16x16x32_bf16 v[88:91], v[40:43], v[16:19], v[88:91]\n"
    "ds_read_b128 v[32:35], v247 offset:256\n"
    "s_waitcnt lgkmcnt(6)\n"
    "v_mfma_f32_16x16x32_bf16 v[88:91], v[44:47], v[20:23], v[88:91]\n"
    "ds_read_b128 v[36:39], v247 offset:320\n"
    "v_max_f32 v84, v84, v85\n"
    "v_mfma_f32_16x16x32_bf16 v[88:91], v[48:51], v[24:27], v[88:91]\n"
    "ds_read_b128 v[40:43], v246 offset:8896\n"
    "v_max_f32 v86, v86, v87\n"
    "v_mfma_f32_16x16x32_bf16 v[88:91], v[52:55], v[28:31], v[88:91]\n"
    "ds_read_b128 v[44:47], v246 offset:8960\n"
    "s_waitcnt lgkmcnt(6)\n"
    "v_mfma_f32_16x16x32_bf16 v[92:95], v[56:59], v[8:11], v[232:235]\n"
    "ds_read_b128 v[48:51], v246 offset:9024\n"
    "v_max3_f32 v206, v206, v84, v86\n"
    "v_mfma_f32_16x16x32_bf16 v[92:95], v[60:63], v[12:15], v[92:95]\n"
    "ds_read_b128 v[52:55], v246 offset:9088\n"
    "v_mfma_f32_16x16x32_bf16 v[92:95], v[64:67], v[16:19], v[92:95]\n"
    "ds_read_b128 v[56:59], v246 offset:9152\n"
    "s_waitcnt lgkmcnt(5)\n"
    "v_mfma_f32_16x16x32_bf16 v[92:95], v[68:71], v[20:23], v[92:95]\n"
    "ds_read_b128 v[60:63], v246 offset:9216\n"
    "v_max_f32 v88, v88, v89\n"
    "v_mfma_f32_16x16x32_bf16 v[92:95], v[32:35], v[24:27], v[92:95]\n"
    "ds_read_b128 v[64:67], v247 offset:8896\n"
    "v_max_f32 v90, v90, v91\n"
    "v_mfma_f32_16x16x32_bf16 v[92:95], v[36:39], v[28:31], v[92:95]\n"
    "ds_read_b128 v[68:71], v247 offset:8960\n"
    "s_waitcnt lgkmcnt(5)\n"
    "v_mfma_f32_16x16x32_bf16 v[96:99], v[40:43], v[8:11], v[232:235]\n"
    "ds_read_b128 v[32:35], v247 offset:9024\n"
    "v_max3_f32 v206, v206, v88, v90\n"
    "v_mfma_f32_16x16x32_bf16 v[96:99], v[44:47], v[12:15], v[96:99]\n"
    "ds_read_b128 v[36:39], v247 offset:9088\n"
    "v_mfma_f32_16x16x32_bf16 v[96:99], v[48:51], v[16:19], v[96:99]\n"
    "ds_read_b128 v[40:43], v247 offset:9152\n"
    "s_waitcnt lgkmcnt(5)\n"
    "v_mfma_f32_16x16x32_bf16 v[96:99], v[52:55], v[20:23], v[96:99]\n"
    "ds_read_b128 v[44:47], v247 offset:9216\n"
    "v_max_f32 v92, v92, v93\n"
    "v_mfma_f32_16x16x32_bf16 v[96:99], v[56:59], v[24:27], v[96:99]\n"
    "v_add_u32 v244, s50, v1\n"
    "v_add_u32 v245, s51, v1\n"
    "v_mfma_f32_16x16x32_bf16 v[96:99], v[60:63], v[28:31], v[96:99]\n"
    "v_add_u32 v246, s50, v2\n"
    "s_waitcnt lgkmcnt(3)\n"
    "v_mfma_f32_16x16x32_bf16 v[100:103], v[64:67], v[8:11], v[240:243]\n"
    "v_add_u32 v247, s51, v2\n"
    "v_max_f32 v94, v94, v95\n"
    "v_mfma_f32_16x16x32_bf16 v[100:103], v[68:71], v[12:15], v[100:103]\n"
    "ds_read_b64_tr_b16 v[48:49], v244 offset:160\n"
    "v_max3_f32 v206, v206, v92, v94\n"
    "v_mfma_f32_16x16x32_bf16 v[100:103], v[32:35], v[16:19], v[100:103]\n"
    "ds_read_b64_tr_b16 v[50:51], v245 offset:160\n"
    "s_waitcnt lgkmcnt(2)\n"
    "v_mfma_f32_16x16x32_bf16 v[100:103], v[36:39], v[20:23], v[100:103]\n"
    "ds_read_b64_tr_b16 v[52:53], v246 offset:160\n"
    "v_max_f32 v96, v96, v97\n"
    "v_mfma_f32_16x16x32_bf16 v[100:103], v[40:43], v[24:27], v[100:103]\n"
    "ds_read_b64_tr_b16 v[54:55], v247 offset:160\n"
    "v_max_f32 v98, v98, v99\n"
    "v_mfma_f32_16x16x32_bf16 v[100:103], v[44:47], v[28:31], v[100:103]\n"
    "ds_read_b64_tr_b16 v[56:57], v244 offset:192\n"
    "ds_read_b64_tr_b16 v[58:59], v245 offset:192\n"
    "s_cmp_lg_u32 s69, 0\n"
    "s_cbranch_scc1 .La_fill\n"
    ".La_filled:\n"
    "ds_write_b32 v7, v252 offset:64\n"
    "s_setprio 0\n"
    "v_max3_f32 v206, v206, v96, v98\n"
    "ds_read_b64_tr_b16 v[60:61], v246 offset:192\n"
    "v_max_f32 v100, v100, v101\n"
    "v_max_f32 v102, v102, v103\n"
    "v_max3_f32 v206, v206, v100, v102\n"
    "v_mov_b32 v207, v206\n"
    "ds_read_b64_tr_b16 v[62:63], v247 offset:192\n"
    "ds_read_b64_tr_b16 v[64:65], v244 offset:224\n"
    "v_permlane16_swap_b32 v206, v207\n"
    "v_max_f32 v206, v206, v207\n"
    "v_mov_b32 v207, v206\n"
    "ds_read_b64_tr_b16 v[66:67], v245 offset:224\n"
    "ds_read_b64_tr_b16 v[68:69], v246 offset:224\n"
    "v_permlane32_swap_b32 v206, v207\n"
    "v_max_f32 v206, v206, v207\n"
    "v_mul_f32 v207, s46, v206\n"
    "v_fma_f32 v72, v72, s47, v207\n"
    "v_fma_f32 v74, v74, s47, v207\n"
    "v_fma_f32 v76, v76, s47, v207\n"
    "v_fma_f32 v78, v78, s47, v207\n"
    "v_fma_f32 v80, v80, s47, v207\n"
    "v_fma_f32 v82, v82, s47, v207\n"
    "v_fma_f32 v84, v84, s47, v207\n"
    "v_fma_f32 v86, v86, s47, v207\n"
    "v_fma_f32 v88, v88, s47, v207\n"
    "v_fma_f32 v90, v90, s47, v207\n"
    "v_fma_f32 v92, v92, s47, v207\n"
    "v_fma_f32 v94, v94, s47, v207\n"
    "v_fma_f32 v96, v96, s47, v207\n"
    "v_fma_f32 v98, v98, s47, v207\n"
    "v_fma_f32 v100, v100, s47, v207\n"
    "v_fma_f32 v102, v102, s47, v207\n"
    "ds_read_b64_tr_b16 v[70:71], v247 offset:224\n"
    "ds_read_b64_tr_b16 v[32:33], v244 offset:96\n"
    "ds_read_b64_tr_b16 v[34:35], v245 offset:96\n"
    "ds_read_b64_tr_b16 v[36:37], v246 offset:96\n"
    "ds_read_b64_tr_b16 v[38:39], v247 offset:96\n"
    "ds_read_b64_tr_b16 v[40:41], v244 offset:128\n"
    "ds_read_b64_tr_b16 v[42:43], v245 offset:128\n"
    "ds_read_b64_tr_b16 v[44:45], v246 offset:128\n"
    "ds_read_b64_tr_b16 v[46:47], v247 offset:128\n"
    "ds_read_b64_tr_b16 v[8:9], v244 offset:0\n"
    "ds_read_b64_tr_b16 v[10:11], v245 offset:0\n"
    "ds_read_b64_tr_b16 v[12:13], v246 offset:0\n"
    "ds_read_b64_tr_b16 v[14:15], v247 offset:0\n"
    "ds_read_b64_tr_b16 v[16:17], v244 offset:32\n"
    "ds_read_b64_tr_b16 v[18:19], v245 offset:32\n"
    "ds_read_b64_tr_b16 v[20:21], v246 offset:32\n"
    "ds_read_b64_tr_b16 v[22:23], v247 offset:32\n"
    "ds_read_b64_tr_b16 v[24:25], v244 offset:64\n"
    "ds_read_b64_tr_b16 v[26:27], v245 offset:64\n"
    "ds_read_b64_tr_b16 v[28:29], v246 offset:64\n"
    "ds_read_b64_tr_b16 v[30:31], v247 offset:64\n"
    "ds_read_b32 v253, v6 offset:32\n"
    "v_exp_f32 v72, v72\n"
    "s_bfe_u32 s48, s60, 0x40000\n"
    "v_exp_f32 v74, v74\n"
    "s_bfe_u32 s49, s60, 0x40004\n"
    "v_exp_f32 v76, v76\n"
    "s_bfe_u32 s50, s60, 0x40008\n"
    "v_exp_f32 v78, v78\n"
    "s_bfe_u32 s51, s60, 0x4000c\n"
    "v_exp_f32 v80, v80\n"
    "s_mul_i32 s48, s48, 8896\n"
    "v_exp_f32 v82, v82\n"
    "s_mul_i32 s49, s49, 8896\n"
    "v_exp_f32 v84, v84\n"
    "s_mul_i32 s50, s50, 8896\n"
    "v_exp_f32 v86, v86\n"
    "s_mul_i32 s51, s51, 8896\n"
    "v_exp_f32 v88, v88\n"
    "s_lshr_b64 s[60:61], s[60:61], 8\n"
    "v_exp_f32 v90, v90\n"
    "s_add_u32 s45, s45, 4\n"
    "v_exp_f32 v92, v92\n"
    "s_add_u32 s54, s54, 2\n"
    "v_exp_f32 v94, v94\n"
    "s_add_u32 s55, s55, 4\n"
    "v_exp_f32 v96, v96\n"
    "s_cmp_eq_u32 s66, 2\n"
    "s_cselect_b32 s67, -2, 1\n"
    "v_exp_f32 v98, v98\n"
    "s_add_u32 s66, s66, s67\n"
    "v_exp_f32 v100, v100\n"
    "s_mul_i32 s68, s67, 6144\n"
    "v_exp_f32 v102, v102\n"
    "v_cvt_pk_bf16_f32 v200, v72, v74\n"
    "v_cvt_pk_bf16_f32 v201, v76, v78\n"
    "v_cvt_pk_bf16_f32 v202, v80, v82\n"
    "v_cvt_pk_bf16_f32 v203, v84, v86\n"
    "v_cvt_pk_bf16_f32 v204, v88, v90\n"
    "v_cvt_pk_bf16_f32 v205, v92, v94\n"
    "v_cvt_pk_bf16_f32 v206, v96, v98\n"
    "v_cvt_pk_bf16_f32 v207, v100, v102\n"
    "ds_read_b64_tr_b16 v[72:73], v244 offset:256\n"
    "ds_read_b64_tr_b16 v[74:75], v245 offset:256\n"
    "ds_read_b64_tr_b16 v[76:77], v246 offset:256\n"
    "ds_read_b64_tr_b16 v[78:79], v247 offset:256\n"
    "ds_read_b64_tr_b16 v[80:81], v244 offset:288\n"
    "ds_read_b64_tr_b16 v[82:83], v245 offset:288\n"
    "ds_read_b64_tr_b16 v[84:85], v246 offset:288\n"
    "ds_read_b64_tr_b16 v[86:87], v247 offset:288\n"
    "s_waitcnt lgkmcnt(8)\n"
    "s_setprio 1\n"
    "v_mfma_f32_16x16x32_bf16 v[100:103], v[248:251], v[200:203], 0\n"
    "v_cmp_gt_i32 vcc, s56, v253\n"
    "v_mfma_f32_16x16x32_bf16 v[100:103], v[248:251], v[204:207], v[100:103]\n"
    "v_smfmac_f32_16x16x64_bf16 v[220:223], v[200:203], v[128:135], v5\n"
    "ds_read_b128 v[128:131], v7 offset:32768\n"
    "v_smfmac_f32_16x16x64_bf16 v[224:227], v[200:203], v[136:143], v5\n"
    "ds_read_b128 v[132:135], v7 offset:32768\n"
    "v_smfmac_f32_16x16x64_bf16 v[228:231], v[200:203], v[144:151], v5\n"
    "ds_read_b128 v[136:139], v7 offset:32768\n"
    "v_smfmac_f32_16x16x64_bf16 v[208:211], v[200:203], v[104:111], v5\n"
    "ds_read_b128 v[140:143], v7 offset:32768\n"
    "v_smfmac_f32_16x16x64_bf16 v[212:215], v[200:203], v[112:119], v5\n"
    "ds_read_b128 v[144:147], v7 offset:32768\n"
    "v_smfmac_f32_16x16x64_bf16 v[216:219], v[200:203], v[120:127], v5\n"
    "ds_read_b128 v[148:151], v7 offset:32768\n"
    "v_smfmac_f32_16x16x64_bf16 v[208:211], v[204:207], v[8:15], v5\n"
    "ds_read_b64_tr_b16 v[88:89], v244 offset:320\n"
    "v_rcp_f32 v100, v100\n"
    "v_smfmac_f32_16x16x64_bf16 v[212:215], v[204:207], v[16:23], v5\n"
    "ds_read_b64_tr_b16 v[90:91], v245 offset:320\n"
    "v_lshrrev_b32 v101, 2, v3\n"
    "v_smfmac_f32_16x16x64_bf16 v[216:219], v[204:207], v[24:31], v5\n"
    "ds_read_b64_tr_b16 v[92:93], v246 offset:320\n"
    "v_and_or_b32 v101, v101, 60, s63\n"
    "v_smfmac_f32_16x16x64_bf16 v[220:223], v[204:207], v[32:39], v5\n"
    "ds_read_b64_tr_b16 v[94:95], v247 offset:320\n"
    "s_cbranch_vccnz .La3\n"
    ".La3b:\n"
    "v_smfmac_f32_16x16x64_bf16 v[224:227], v[204:207], v[40:47], v5\n"
    "ds_read_b64_tr_b16 v[96:97], v244 offset:352\n"
    "ds_read_b64_tr_b16 v[98:99], v245 offset:352\n"
    "v_smfmac_f32_16x16x64_bf16 v[228:231], v[204:207], v[48:55], v5\n"
    "s_waitcnt lgkmcnt(6)\n"
    "v_smfmac_f32_16x16x64_bf16 v[128:131], v[200:203], v[152:159], v5\n"
    "ds_write_b32 v101, v100 offset:0\n"
    "ds_read_b64_tr_b16 v[100:101], v246 offset:352\n"
    "v_smfmac_f32_16x16x64_bf16 v[132:135], v[200:203], v[160:167], v5\n"
    "ds_read_b64_tr_b16 v[102:103], v247 offset:352\n"
    "v_smfmac_f32_16x16x64_bf16 v[136:139], v[200:203], v[168:175], v5\n"
    "v_cvt_pk_bf16_f32 v208, v208, v209\n"
    "v_cvt_pk_bf16_f32 v209, v210, v211\n"
    "v_smfmac_f32_16x16x64_bf16 v[140:143], v[200:203], v[176:183], v5\n"
    "v_cvt_pk_bf16_f32 v210, v212, v213\n"
    "v_cvt_pk_bf16_f32 v211, v214, v215\n"
    "v_smfmac_f32_16x16x64_bf16 v[144:147], v[200:203], v[184:191], v5\n"
    "ds_write_b128 v4, v[208:211] offset:0\n"
    "v_smfmac_f32_16x16x64_bf16 v[148:151], v[200:203], v[192:199], v5\n"
    "v_cvt_pk_bf16_f32 v216, v216, v217\n"
    "v_cvt_pk_bf16_f32 v217, v218, v219\n"
    "v_smfmac_f32_16x16x64_bf16 v[128:131], v[204:207], v[56:63], v5\n"
    "ds_read_b32 v254, v6 offset:16\n"
    "ds_read_b32 v200, v6 offset:0\n"
    "v_smfmac_f32_16x16x64_bf16 v[132:135], v[204:207], v[64:71], v5\n"
    "v_cvt_pk_bf16_f32 v218, v220, v221\n"
    "s_waitcnt lgkmcnt(8)\n"
    "v_smfmac_f32_16x16x64_bf16 v[136:139], v[204:207], v[72:79], v5\n"
    "v_cvt_pk_bf16_f32 v219, v222, v223\n"
    "v_smfmac_f32_16x16x64_bf16 v[140:143], v[204:207], v[80:87], v5\n"
    "ds_write_b128 v4, v[216:219] offset:1024\n"
    "v_smfmac_f32_16x16x64_bf16 v[144:147], v[204:207], v[88:95], v5\n"
    "v_cvt_pk_bf16_f32 v224, v224, v225\n"
    "s_waitcnt lgkmcnt(4)\n"
    "v_smfmac_f32_16x16x64_bf16 v[148:151], v[204:207], v[96:103], v5\n"
    "v_cvt_pk_bf16_f32 v225, v226, v227\n"
    "v_cvt_pk_bf16_f32 v226, v228, v229\n"
    "s_setprio 0\n"
    "v_add_u32 v244, s48, v0\n"
    "v_add_u32 v245, s49, v0\n"
    "v_add_u32 v246, s50, v0\n"
    "v_add_u32 v247, s51, v0\n"
    "v_add_u32 v3, s68, v3\n"
    "v_cvt_pk_bf16_f32 v227, v230, v231\n"
    "ds_write_b128 v4, v[224:227] offset:2048\n"
    "s_waitcnt lgkmcnt(3)\n"
    "v_cmp_gt_u32 vcc, s55, v254\n"
    "s_waitcnt lgkmcnt(2)\n"
    "v_cmp_gt_u32 s[58:59], s54, v200\n"
    "s_add_u32 s56, s56, 4\n"
    "s_lshl_b32 s67, s67, 6\n"
    "s_add_u32 s63, s63, s67\n"
    "s_sub_u32 s53, s68, 18432\n"
    "s_add_u32 s67, s63, 192\n"
    "s_cmp_ge_u32 s45, 10\n"
    "s_cselect_b32 s53, s53, s68\n"
    "s_cselect_b32 s63, s67, s63\n"
    "s_cselect_b32 s56, -16, s56\n"
    "s_cmp_ge_u32 s45, 12\n"
    "s_cbranch_scc1 .La4\n"
    "s_or_b64 vcc, vcc, s[58:59]\n"
    "s_cbranch_scc1 .La4\n"
    "ds_read_b128 v[104:107], v3 offset:0\n"
    "ds_read_b128 v[152:155], v244 offset:0\n"
    "ds_read_b128 v[108:111], v3 offset:1024\n"
    "ds_read_b128 v[156:159], v244 offset:64\n"
    "ds_read_b128 v[112:115], v3 offset:2048\n"
    "ds_read_b128 v[160:163], v244 offset:128\n"
    "ds_read_b128 v[116:119], v3 offset:3072\n"
    "ds_read_b128 v[164:167], v244 offset:192\n"
    "ds_read_b128 v[120:123], v3 offset:4096\n"
    "ds_read_b128 v[124:127], v3 offset:5120\n"
    "v_cvt_pk_bf16_f32 v128, v128, v129\n"
    "v_cvt_pk_bf16_f32 v129, v130, v131\n"
    "v_cvt_pk_bf16_f32 v130, v132, v133\n"
    "v_cvt_pk_bf16_f32 v131, v134, v135\n"
    "ds_write_b128 v4, v[128:131] offset:3072\n"
    "v_cvt_pk_bf16_f32 v136, v136, v137\n"
    "v_cvt_pk_bf16_f32 v137, v138, v139\n"
    "v_cvt_pk_bf16_f32 v138, v140, v141\n"
    "v_cvt_pk_bf16_f32 v139, v142, v143\n"
    "ds_write_b128 v4, v[136:139] offset:4096\n"
    "v_cvt_pk_bf16_f32 v144, v144, v145\n"
    "v_cvt_pk_bf16_f32 v145, v146, v147\n"
    "v_cvt_pk_bf16_f32 v146, v148, v149\n"
    "v_cvt_pk_bf16_f32 v147, v150, v151\n"
    "ds_write_b128 v4, v[144:147] offset:5120\n"
    "ds_write_b32 v7, v252 offset:128\n"
    "ds_read_b128 v[128:131], v244 offset:256\n"
    "ds_read_b128 v[132:135], v244 offset:320\n"
    "ds_read_b128 v[136:139], v245 offset:0\n"
    "ds_read_b128 v[140:143], v245 offset:64\n"
    ".La4j:\n"
    "s_setprio 1\n"
    "s_add_u32 s57, s57, 1\n"
    "s_waitcnt lgkmcnt(8)\n"
    "v_mfma_f32_16x16x32_bf16 v[168:171], v[152:155], v[104:107], v[236:239]\n"
    "ds_read_b128 v[144:147], v245 offset:128\n"
    "v_mov_b32 v252, s57\n"
    "v_mfma_f32_16x16x32_bf16 v[168:171], v[156:159], v[108:111], v[168:171]\n"
    "ds_read_b128 v[148:151], v245 offset:192\n"
    "v_add_u32 v4, s53, v4\n"
    "v_mfma_f32_16x16x32_bf16 v[168:171], v[160:163], v[112:115], v[168:171]\n"
    "ds_read_b128 v[152:155], v245 offset:256\n"
    "s_waitcnt lgkmcnt(5)\n"
    "v_mfma_f32_16x16x32_bf16 v[168:171], v[164:167], v[116:119], v[168:171]\n"
    "ds_read_b128 v[156:159], v245 offset:320\n"
    "ds_write_b32 v7, v252 offset:192\n"
    "v_mfma_f32_16x16x32_bf16 v[168:171], v[128:131], v[120:123], v[168:171]\n"
    "ds_read_b128 v[160:163], v244 offset:8896\n"
    "ds_read_b128 v[208:211], v7 offset:32768\n"
    "v_mfma_f32_16x16x32_bf16 v[168:171], v[132:135], v[124:127], v[168:171]\n"
    "ds_read_b128 v[164:167], v244 offset:8960\n"
    "s_waitcnt lgkmcnt(7)\n"
    "v_mfma_f32_16x16x32_bf16 v[172:175], v[136:139], v[104:107], v[232:235]\n"
    "ds_read_b128 v[128:131], v244 offset:9024\n"
    "ds_read_b128 v[212:215], v7 offset:32768\n"
    "v_mfma_f32_16x16x32_bf16 v[172:175], v[140:143], v[108:111], v[172:175]\n"
    "ds_read_b128 v[132:135], v244 offset:9088\n"
    "ds_read_b128 v[216:219], v7 offset:32768\n"
    "v_mfma_f32_16x16x32_bf16 v[172:175], v[144:147], v[112:115], v[172:175]\n"
    "ds_read_b128 v[136:139], v244 offset:9152\n"
    "s_waitcnt lgkmcnt(9)\n"
    "v_mfma_f32_16x16x32_bf16 v[172:175], v[148:151], v[116:119], v[172:175]\n"
    "ds_read_b128 v[140:143], v244 offset:9216\n"
    "v_max_f32 v168, v168, v169\n"
    "v_mfma_f32_16x16x32_bf16 v[172:175], v[152:155], v[120:123], v[172:175]\n"
    "ds_read_b128 v[144:147], v245 offset:8896\n"
    "v_max_f32 v170, v170, v171\n"
    "v_mfma_f32_16x16x32_bf16 v[172:175], v[156:159], v[124:127], v[172:175]\n"
    "ds_read_b128 v[148:151], v245 offset:8960\n"
    "s_waitcnt lgkmcnt(7)\n"
    "v_mfma_f32_16x16x32_bf16 v[176:179], v[160:163], v[104:107], v[232:235]\n"
    "ds_read_b128 v[152:155], v245 offset:9024\n"
    "v_max_f32 v206, v168, v170\n"
    "v_mfma_f32_16x16x32_bf16 v[176:179], v[164:167], v[108:111], v[176:179]\n"
    "ds_read_b128 v[156:159], v245 offset:9088\n"
    "ds_read_b128 v[220:223], v7 offset:32768\n"
    "v_mfma_f32_16x16x32_bf16 v[176:179], v[128:131], v[112:115], v[176:179]\n"
    "ds_read_b128 v[160:163], v245 offset:9152\n"
    "s_waitcnt lgkmcnt(6)\n"
    "v_mfma_f32_16x16x32_bf16 v[176:179], v[132:135], v[116:119], v[176:179]\n"
    "ds_read_b128 v[164:167], v245 offset:9216\n"
    "v_max_f32 v172, v172, v173\n"
    "v_mfma_f32_16x16x32_bf16 v[176:179], v[136:139], v[120:123], v[176:179]\n"
    "ds_read_b128 v[128:131], v246 offset:0\n"
    "v_max_f32 v174, v174, v175\n"
    "v_mfma_f32_16x16x32_bf16 v[176:179], v[140:143], v[124:127], v[176:179]\n"
    "ds_read_b128 v[132:135], v246 offset:64\n"
    "s_waitcnt lgkmcnt(6)\n"
    "v_mfma_f32_16x16x32_bf16 v[180:183], v[144:147], v[104:107], v[232:235]\n"
    "ds_read_b128 v[136:139], v246 offset:128\n"
    "v_max3_f32 v206, v206, v172, v174\n"
    "v_mfma_f32_16x16x32_bf16 v[180:183], v[148:151], v[108:111], v[180:183]\n"
    "ds_read_b128 v[140:143], v246 offset:192\n"
    "ds_read_b128 v[224:227], v7 offset:32768\n"
    "v_mfma_f32_16x16x32_bf16 v[180:183], v[152:155], v[112:115], v[180:183]\n"
    "ds_read_b128 v[144:147], v246 offset:256\n"
    "s_waitcnt lgkmcnt(6)\n"
    "v_mfma_f32_16x16x32_bf16 v[180:183], v[156:159], v[116:119], v[180:183]\n"
    "ds_read_b128 v[148:151], v246 offset:320\n"
    "v_max_f32 v176, v176, v177\n"
    "v_mfma_f32_16x16x32_bf16 v[180:183], v[160:163], v[120:123], v[180:183]\n"
    "ds_read_b128 v[152:155], v247 offset:0\n"
    "v_max_f32 v178, v178, v179\n"
    "v_mfma_f32_16x16x32_bf16 v[180:183], v[164:167], v[124:127], v[180:183]\n"
    "ds_read_b128 v[156:159], v247 offset:64\n"
    "s_waitcnt lgkmcnt(6)\n"
    "v_mfma_f32_16x16x32_bf16 v[184:187], v[128:131], v[104:107], v[232:235]\n"
    "ds_read_b128 v[160:163], v247 offset:128\n"
    "v_max3_f32 v206, v206, v176, v178\n"
    "v_mfma_f32_16x16x32_bf16 v[184:187], v[132:135], v[108:111], v[184:187]\n"
    "ds_read_b128 v[164:167], v247 offset:192\n"
    "ds_read_b128 v[228:231], v7 offset:32768\n"
    "v_mfma_f32_16x16x32_bf16 v[184:187], v[136:139], v[112:115], v[184:187]\n"
    "ds_read_b128 v[128:131], v247 offset:256\n"
    "s_waitcnt lgkmcnt(6)\n"
    "v_mfma_f32_16x16x32_bf16 v[184:187], v[140:143], v[116:119], v[184:187]\n"
    "ds_read_b128 v[132:135], v247 offset:320\n"
    "v_max_f32 v180, v180, v181\n"
    "v_mfma_f32_16x16x32_bf16 v[184:187], v[144:147], v[120:123], v[184:187]\n"
    "ds_read_b128 v[136:139], v246 offset:8896\n"
    "v_max_f32 v182, v182, v183\n"
    "v_mfma_f32_16x16x32_bf16 v[184:187], v[148:151], v[124:127], v[184:187]\n"
    "ds_read_b128 v[140:143], v246 offset:8960\n"
    "s_waitcnt lgkmcnt(6)\n"
    "v_mfma_f32_16x16x32_bf16 v[188:191], v[152:155], v[104:107], v[232:235]\n"
    "ds_read_b128 v[144:147], v246 offset:9024\n"
    "v_max3_f32 v206, v206, v180, v182\n"
    "v_mfma_f32_16x16x32_bf16 v[188:191], v[156:159], v[108:111], v[188:191]\n"
    "ds_read_b128 v[148:151], v246 offset:9088\n"
    "v_mfma_f32_16x16x32_bf16 v[188:191], v[160:163], v[112:115], v[188:191]\n"
    "ds_read_b128 v[152:155], v246 offset:9152\n"
    "s_waitcnt lgkmcnt(5)\n"
    "v_mfma_f32_16x16x32_bf16 v[188:191], v[164:167], v[116:119], v[188:191]\n"
    "ds_read_b128 v[156:159], v246 offset:9216\n"
    "v_max_f32 v184, v184, v185\n"
    "v_mfma_f32_16x16x32_bf16 v[188:191], v[128:131], v[120:123], v[188:191]\n"
    "ds_read_b128 v[160:163], v247 offset:8896\n"
    "v_max_f32 v186, v186, v187\n"
    "v_mfma_f32_16x16x32_bf16 v[188:191], v[132:135], v[124:127], v[188:191]\n"
    "ds_read_b128 v[164:167], v247 offset:8960\n"
    "s_waitcnt lgkmcnt(5)\n"
    "v_mfma_f32_16x16x32_bf16 v[192:195], v[136:139], v[104:107], v[232:235]\n"
    "ds_read_b128 v[128:131], v247 offset:9024\n"
    "v_max3_f32 v206, v206, v184, v186\n"
    "v_mfma_f32_16x16x32_bf16 v[192:195], v[140:143], v[108:111], v[192:195]\n"
    "ds_read_b128 v[132:135], v247 offset:9088\n"
    "v_mfma_f32_16x16x32_bf16 v[192:195], v[144:147], v[112:115], v[192:195]\n"
    "ds_read_b128 v[136:139], v247 offset:9152\n"
    "s_waitcnt lgkmcnt(5)\n"
    "v_mfma_f32_16x16x32_bf16 v[192:195], v[148:151], v[116:119], v[192:195]\n"
    "ds_read_b128 v[140:143], v247 offset:9216\n"
    "v_max_f32 v188, v188, v189\n"
    "v_mfma_f32_16x16x32_bf16 v[192:195], v[152:155], v[120:123], v[192:195]\n"
    "v_add_u32 v244, s50, v1\n"
    "v_add_u32 v245, s51, v1\n"
    "v_mfma_f32_16x16x32_bf16 v[192:195], v[156:159], v[124:127], v[192:195]\n"
    "v_add_u32 v246, s50, v2\n"
    "s_waitcnt lgkmcnt(3)\n"
    "v_mfma_f32_16x16x32_bf16 v[196:199], v[160:163], v[104:107], v[240:243]\n"
    "v_add_u32 v247, s51, v2\n"
    "v_max_f32 v190, v190, v191\n"
    "v_mfma_f32_16x16x32_bf16 v[196:199], v[164:167], v[108:111], v[196:199]\n"
    "ds_read_b64_tr_b16 v[144:145], v244 offset:160\n"
    "v_max3_f32 v206, v206, v188, v190\n"
    "v_mfma_f32_16x16x32_bf16 v[196:199], v[128:131], v[112:115], v[196:199]\n"
    "ds_read_b64_tr_b16 v[146:147], v245 offset:160\n"
    "s_waitcnt lgkmcnt(2)\n"
    "v_mfma_f32_16x16x32_bf16 v[196:199], v[132:135], v[116:119], v[196:199]\n"
    "ds_read_b64_tr_b16 v[148:149], v246 offset:160\n"
    "v_max_f32 v192, v192, v193\n"
    "v_mfma_f32_16x16x32_bf16 v[196:199], v[136:139], v[120:123], v[196:199]\n"
    "ds_read_b64_tr_b16 v[150:151], v247 offset:160\n"
    "v_max_f32 v194, v194, v195\n"
    "v_mfma_f32_16x16x32_bf16 v[196:199], v[140:143], v[124:127], v[196:199]\n"
    "ds_read_b64_tr_b16 v[152:153], v244 offset:192\n"
    "ds_read_b64_tr_b16 v[154:155], v245 offset:192\n"
    "ds_write_b32 v7, v252 offset:64\n"
    "s_setprio 0\n"
    "v_max3_f32 v206, v206, v192, v194\n"
    "ds_read_b64_tr_b16 v[156:157], v246 offset:192\n"
    "ds_read_b64_tr_b16 v[158:159], v247 offset:192\n"
    "ds_read_b64_tr_b16 v[160:161], v244 offset:224\n"
    "v_max_f32 v196, v196, v197\n"
    "v_max_f32 v198, v198, v199\n"
    "v_max3_f32 v206, v206, v196, v198\n"
    "v_mov_b32 v207, v206\n"
    "ds_read_b64_tr_b16 v[162:163], v245 offset:224\n"
    "ds_read_b64_tr_b16 v[164:165], v246 offset:224\n"
    "v_permlane16_swap_b32 v206, v207\n"
    "v_max_f32 v206, v206, v207\n"
    "v_mov_b32 v207, v206\n"
    "ds_read_b64_tr_b16 v[166:167], v247 offset:224\n"
    "ds_read_b64_tr_b16 v[128:129], v244 offset:96\n"
    "v_permlane32_swap_b32 v206, v207\n"
    "v_max_f32 v206, v206, v207\n"
    "v_mul_f32 v207, s46, v206\n"
    "v_fma_f32 v168, v168, s47, v207\n"
    "v_fma_f32 v170, v170, s47, v207\n"
    "v_fma_f32 v172, v172, s47, v207\n"
    "v_fma_f32 v174, v174, s47, v207\n"
    "v_fma_f32 v176, v176, s47, v207\n"
    "v_fma_f32 v178, v178, s47, v207\n"
    "v_fma_f32 v180, v180, s47, v207\n"
    "v_fma_f32 v182, v182, s47, v207\n"
    "v_fma_f32 v184, v184, s47, v207\n"
    "v_fma_f32 v186, v186, s47, v207\n"
    "v_fma_f32 v188, v188, s47, v207\n"
    "v_fma_f32 v190, v190, s47, v207\n"
    "v_fma_f32 v192, v192, s47, v207\n"
    "v_fma_f32 v194, v194, s47, v207\n"
    "v_fma_f32 v196, v196, s47, v207\n"
    "v_fma_f32 v198, v198, s47, v207\n"
    "ds_read_b64_tr_b16 v[130:131], v245 offset:96\n"
    "ds_read_b64_tr_b16 v[132:133], v246 offset:96\n"
    "ds_read_b64_tr_b16 v[134:135], v247 offset:96\n"
    "ds_read_b64_tr_b16 v[136:137], v244 offset:128\n"
    "ds_read_b64_tr_b16 v[138:139], v245 offset:128\n"
    "ds_read_b64_tr_b16 v[140:141], v246 offset:128\n"
    "ds_read_b64_tr_b16 v[142:143], v247 offset:128\n"
    "ds_read_b64_tr_b16 v[104:105], v244 offset:0\n"
    "ds_read_b64_tr_b16 v[106:107], v245 offset:0\n"
    "ds_read_b64_tr_b16 v[108:109], v246 offset:0\n"
    "ds_read_b64_tr_b16 v[110:111], v247 offset:0\n"
    "ds_read_b64_tr_b16 v[112:113], v244 offset:32\n"
    "ds_read_b64_tr_b16 v[114:115], v245 offset:32\n"
    "ds_read_b64_tr_b16 v[116:117], v246 offset:32\n"
    "ds_read_b64_tr_b16 v[118:119], v247 offset:32\n"
    "ds_read_b64_tr_b16 v[120:121], v244 offset:64\n"
    "ds_read_b64_tr_b16 v[122:123], v245 offset:64\n"
    "ds_read_b64_tr_b16 v[124:125], v246 offset:64\n"
    "ds_read_b64_tr_b16 v[126:127], v247 offset:64\n"
    "ds_read_b32 v253, v6 offset:32\n"
    "v_exp_f32 v168, v168\n"
    "s_bfe_u32 s48, s60, 0x40000\n"
    "v_exp_f32 v170, v170\n"
    "s_bfe_u32 s49, s60, 0x40004\n"
    "v_exp_f32 v172, v172\n"
    "s_bfe_u32 s50, s60, 0x40008\n"
    "v_exp_f32 v174, v174\n"
    "s_bfe_u32 s51, s60, 0x4000c\n"
    "v_exp_f32 v176, v176\n"
    "s_mul_i32 s48, s48, 8896\n"
    "v_exp_f32 v178, v178\n"
    "s_mul_i32 s49, s49, 8896\n"
    "v_exp_f32 v180, v180\n"
    "s_mul_i32 s50, s50, 8896\n"
    "v_exp_f32 v182, v182\n"
    "s_mul_i32 s51, s51, 8896\n"
    "v_exp_f32 v184, v184\n"
    "s_lshr_b64 s[60:61], s[60:61], 8\n"
    "v_exp_f32 v186, v186\n"
    "s_add_u32 s45, s45, 4\n"
    "v_exp_f32 v188, v188\n"
    "s_add_u32 s54, s54, 2\n"
    "v_exp_f32 v190, v190\n"
    "s_add_u32 s55, s55, 4\n"
    "v_exp_f32 v192, v192\n"
    "s_cmp_eq_u32 s66, 2\n"
    "s_cselect_b32 s67, -2, 1\n"
    "v_exp_f32 v194, v194\n"
    "s_add_u32 s66, s66, s67\n"
    "v_exp_f32 v196, v196\n"
    "s_mul_i32 s68, s67, 6144\n"
    "v_exp_f32 v198, v198\n"
    "v_cvt_pk_bf16_f32 v200, v168, v170\n"
    "v_cvt_pk_bf16_f32 v201, v172, v174\n"
    "v_cvt_pk_bf16_f32 v202, v176, v178\n"
    "v_cvt_pk_bf16_f32 v203, v180, v182\n"
    "v_cvt_pk_bf16_f32 v204, v184, v186\n"
    "v_cvt_pk_bf16_f32 v205, v188, v190\n"
    "v_cvt_pk_bf16_f32 v206, v192, v194\n"
    "v_cvt_pk_bf16_f32 v207, v196, v198\n"
    "ds_read_b64_tr_b16 v[168:169], v244 offset:256\n"
    "ds_read_b64_tr_b16 v[170:171], v245 offset:256\n"
    "ds_read_b64_tr_b16 v[172:173], v246 offset:256\n"
    "ds_read_b64_tr_b16 v[174:175], v247 offset:256\n"
    "ds_read_b64_tr_b16 v[176:177], v244 offset:288\n"
    "ds_read_b64_tr_b16 v[178:179], v245 offset:288\n"
    "ds_read_b64_tr_b16 v[180:181], v246 offset:288\n"
    "ds_read_b64_tr_b16 v[182:183], v247 offset:288\n"
    "s_waitcnt lgkmcnt(8)\n"
    "s_setprio 1\n"
    "v_mfma_f32_16x16x32_bf16 v[196:199], v[248:251], v[200:203], 0\n"
    "v_cmp_gt_i32 vcc, s56, v253\n"
    "v_mfma_f32_16x16x32_bf16 v[196:199], v[248:251], v[204:207], v[196:199]\n"
    "v_smfmac_f32_16x16x64_bf16 v[220:223], v[200:203], v[32:39], v5\n"
    "ds_read_b128 v[32:35], v7 offset:32768\n"
    "v_smfmac_f32_16x16x64_bf16 v[224:227], v[200:203], v[40:47], v5\n"
    "ds_read_b128 v[36:39], v7 offset:32768\n"
    "v_smfmac_f32_16x16x64_bf16 v[228:231], v[200:203], v[48:55], v5\n"
    "ds_read_b128 v[40:43], v7 offset:32768\n"
    "v_smfmac_f32_16x16x64_bf16 v[208:211], v[200:203], v[8:15], v5\n"
    "ds_read_b128 v[44:47], v7 offset:32768\n"
    "v_smfmac_f32_16x16x64_bf16 v[212:215], v[200:203], v[16:23], v5\n"
    "ds_read_b128 v[48:51], v7 offset:32768\n"
    "v_smfmac_f32_16x16x64_bf16 v[216:219], v[200:203], v[24:31], v5\n"
    "ds_read_b128 v[52:55], v7 offset:32768\n"
    "v_smfmac_f32_16x16x64_bf16 v[208:211], v[204:207], v[104:111], v5\n"
    "ds_read_b64_tr_b16 v[184:185], v244 offset:320\n"
    "v_rcp_f32 v196, v196\n"
    "v_smfmac_f32_16x16x64_bf16 v[212:215], v[204:207], v[112:119], v5\n"
    "ds_read_b64_tr_b16 v[186:187], v245 offset:320\n"
    "v_lshrrev_b32 v197, 2, v3\n"
    "v_smfmac_f32_16x16x64_bf16 v[216:219], v[204:207], v[120:127], v5\n"
    "ds_read_b64_tr_b16 v[188:189], v246 offset:320\n"
    "v_and_or_b32 v197, v197, 60, s63\n"
    "v_smfmac_f32_16x16x64_bf16 v[220:223], v[204:207], v[128:135], v5\n"
    "ds_read_b64_tr_b16 v[190:191], v247 offset:320\n"
    "s_cbranch_vccnz .La7\n"
    ".La7b:\n"
    "v_smfmac_f32_16x16x64_bf16 v[224:227], v[204:207], v[136:143], v5\n"
    "ds_read_b64_tr_b16 v[192:193], v244 offset:352\n"
    "ds_read_b64_tr_b16 v[194:195], v245 offset:352\n"
    "v_smfmac_f32_16x16x64_bf16 v[228:231], v[204:207], v[144:151], v5\n"
    "s_waitcnt lgkmcnt(6)\n"
    "v_smfmac_f32_16x16x64_bf16 v[32:35], v[200:203], v[56:63], v5\n"
    "ds_write_b32 v197, v196 offset:0\n"
    "ds_read_b64_tr_b16 v[196:197], v246 offset:352\n"
    "v_smfmac_f32_16x16x64_bf16 v[36:39], v[200:203], v[64:71], v5\n"
    "ds_read_b64_tr_b16 v[198:199], v247 offset:352\n"
    "v_smfmac_f32_16x16x64_bf16 v[40:43], v[200:203], v[72:79], v5\n"
    "v_cvt_pk_bf16_f32 v208, v208, v209\n"
    "v_cvt_pk_bf16_f32 v209, v210, v211\n"
    "v_smfmac_f32_16x16x64_bf16 v[44:47], v[200:203], v[80:87], v5\n"
    "v_cvt_pk_bf16_f32 v210, v212, v213\n"
    "v_cvt_pk_bf16_f32 v211, v214, v215\n"
    "v_smfmac_f32_16x16x64_bf16 v[48:51], v[200:203], v[88:95], v5\n"
    "ds_write_b128 v4, v[208:211] offset:0\n"
    "v_smfmac_f32_16x16x64_bf16 v[52:55], v[200:203], v[96:103], v5\n"
    "v_cvt_pk_bf16_f32 v216, v216, v217\n"
    "v_cvt_pk_bf16_f32 v217, v218, v219\n"
    "v_smfmac_f32_16x16x64_bf16 v[32:35], v[204:207], v[152:159], v5\n"
    "ds_read_b32 v254, v6 offset:16\n"
    "ds_read_b32 v200, v6 offset:0\n"
    "v_smfmac_f32_16x16x64_bf16 v[36:39], v[204:207], v[160:167], v5\n"
    "v_cvt_pk_bf16_f32 v218, v220, v221\n"
    "s_waitcnt lgkmcnt(8)\n"
    "v_smfmac_f32_16x16x64_bf16 v[40:43], v[204:207], v[168:175], v5\n"
    "v_cvt_pk_bf16_f32 v219, v222, v223\n"
    "v_smfmac_f32_16x16x64_bf16 v[44:47], v[204:207], v[176:183], v5\n"
    "ds_write_b128 v4, v[216:219] offset:1024\n"
    "v_smfmac_f32_16x16x64_bf16 v[48:51], v[204:207], v[184:191], v5\n"
    "v_cvt_pk_bf16_f32 v224, v224, v225\n"
    "s_waitcnt lgkmcnt(4)\n"
    "v_smfmac_f32_16x16x64_bf16 v[52:55], v[204:207], v[192:199], v5\n"
    "v_cvt_pk_bf16_f32 v225, v226, v227\n"
    "v_cvt_pk_bf16_f32 v226, v228, v229\n"
    "s_setprio 0\n"
    "v_add_u32 v244, s48, v0\n"
    "v_add_u32 v245, s49, v0\n"
    "v_add_u32 v246, s50, v0\n"
    "v_add_u32 v247, s51, v0\n"
    "v_add_u32 v3, s68, v3\n"
    "v_cvt_pk_bf16_f32 v227, v230, v231\n"
    "ds_write_b128 v4, v[224:227] offset:2048\n"
    "s_waitcnt lgkmcnt(3)\n"
    "v_cmp_gt_u32 vcc, s55, v254\n"
    "s_waitcnt lgkmcnt(2)\n"
    "v_cmp_gt_u32 s[58:59], s54, v200\n"
    "s_add_u32 s56, s56, 4\n"
    "s_lshl_b32 s67, s67, 6\n"
    "s_add_u32 s63, s63, s67\n"
    "s_sub_u32 s53, s68, 18432\n"
    "s_add_u32 s67, s63, 192\n"
    "s_cmp_ge_u32 s45, 10\n"
    "s_cselect_b32 s53, s53, s68\n"
    "s_cselect_b32 s63, s67, s63\n"
    "s_cselect_b32 s56, -16, s56\n"
    "s_cmp_ge_u32 s45, 12\n"
    "s_cbranch_scc1 .La8\n"
    "s_or_b64 vcc, vcc, s[58:59]\n"
    "s_cbranch_scc1 .La8\n"
    "ds_read_b128 v[8:11], v3 offset:0\n"
    "ds_read_b128 v[56:59], v244 offset:0\n"
    "ds_read_b128 v[12:15], v3 offset:1024\n"
    "ds_read_b128 v[60:63], v244 offset:64\n"
    "ds_read_b128 v[16:19], v3 offset:2048\n"
    "ds_read_b128 v[64:67], v244 offset:128\n"
    "ds_read_b128 v[20:23], v3 offset:3072\n"
    "ds_read_b128 v[68:71], v244 offset:192\n"
    "ds_read_b128 v[24:27], v3 offset:4096\n"
    "ds_read_b128 v[28:31], v3 offset:5120\n"
    "v_cvt_pk_bf16_f32 v32, v32, v33\n"
    "v_cvt_pk_bf16_f32 v33, v34, v35\n"
    "v_cvt_pk_bf16_f32 v34, v36, v37\n"
    "v_cvt_pk_bf16_f32 v35, v38, v39\n"
    "ds_write_b128 v4, v[32:35] offset:3072\n"
    "v_cvt_pk_bf16_f32 v40, v40, v41\n"
    "v_cvt_pk_bf16_f32 v41, v42, v43\n"
    "v_cvt_pk_bf16_f32 v42, v44, v45\n"
    "v_cvt_pk_bf16_f32 v43, v46, v47\n"
    "ds_write_b128 v4, v[40:43] offset:4096\n"
    "v_cvt_pk_bf16_f32 v48, v48, v49\n"
    "v_cvt_pk_bf16_f32 v49, v50, v51\n"
    "v_cvt_pk_bf16_f32 v50, v52, v53\n"
    "v_cvt_pk_bf16_f32 v51, v54, v55\n"
    "ds_write_b128 v4, v[48:51] offset:5120\n"
    "ds_write_b32 v7, v252 offset:128\n"
    "ds_read_b128 v[32:35], v244 offset:256\n"
    "ds_read_b128 v[36:39], v244 offset:320\n"
    "ds_read_b128 v[40:43], v245 offset:0\n"
    "ds_read_b128 v[44:47], v245 offset:64\n"
    ".La8j:\n"
    "s_branch .La_loop\n"
    ".La_end:\n"
    "s_waitcnt lgkmcnt(0)\n"
    "s_endpgm\n"
    ".La1:\n"
    "s_sub_u32 s52, s52, 1\n"
    "s_cmp_lt_i32 s52, 0\n"
    "s_cbranch_scc1 .La1b\n"
    "s_sleep 1\n"
    "ds_read_b32 v253, v6 offset:0\n"
    "s_waitcnt lgkmcnt(0)\n"
    "v_cmp_gt_u32 vcc, s54, v253\n"
    "s_cbranch_vccnz .La1\n"
    "s_branch .La1b\n"
    ".La_fill:\n"
    "s_mov_b32 s69, 0\n"
    "ds_read_b64_tr_b16 v[104:105], v200 offset:0\n"
    "ds_read_b64_tr_b16 v[106:107], v201 offset:0\n"
    "ds_read_b64_tr_b16 v[108:109], v202 offset:0\n"
    "ds_read_b64_tr_b16 v[110:111], v203 offset:0\n"
    "ds_read_b64_tr_b16 v[112:113], v200 offset:32\n"
    "ds_read_b64_tr_b16 v[114:115], v201 offset:32\n"
    "ds_read_b64_tr_b16 v[116:117], v202 offset:32\n"
    "ds_read_b64_tr_b16 v[118:119], v203 offset:32\n"
    "ds_read_b64_tr_b16 v[120:121], v200 offset:64\n"
    "ds_read_b64_tr_b16 v[122:123], v201 offset:64\n"
    "ds_read_b64_tr_b16 v[124:125], v202 offset:64\n"
    "ds_read_b64_tr_b16 v[126:127], v203 offset:64\n"
    "ds_read_b64_tr_b16 v[128:129], v200 offset:96\n"
    "ds_read_b64_tr_b16 v[130:131], v201 offset:96\n"
    "ds_read_b64_tr_b16 v[132:133], v202 offset:96\n"
    "ds_read_b64_tr_b16 v[134:135], v203 offset:96\n"
    "ds_read_b64_tr_b16 v[136:137], v200 offset:128\n"
    "ds_read_b64_tr_b16 v[138:139], v201 offset:128\n"
    "ds_read_b64_tr_b16 v[140:141], v202 offset:128\n"
    "ds_read_b64_tr_b16 v[142:143], v203 offset:128\n"
    "ds_read_b64_tr_b16 v[144:145], v200 offset:160\n"
    "ds_read_b64_tr_b16 v[146:147], v201 offset:160\n"
    "ds_read_b64_tr_b16 v[148:149], v202 offset:160\n"
    "ds_read_b64_tr_b16 v[150:151], v203 offset:160\n"
    "ds_read_b64_tr_b16 v[152:153], v200 offset:192\n"
    "ds_read_b64_tr_b16 v[154:155], v201 offset:192\n"
    "ds_read_b64_tr_b16 v[156:157], v202 offset:192\n"
    "ds_read_b64_tr_b16 v[158:159], v203 offset:192\n"
    "ds_read_b64_tr_b16 v[160:161], v200 offset:224\n"
    "ds_read_b64_tr_b16 v[162:163], v201 offset:224\n"
    "ds_read_b64_tr_b16 v[164:165], v202 offset:224\n"
    "ds_read_b64_tr_b16 v[166:167], v203 offset:224\n"
    "ds_read_b64_tr_b16 v[168:169], v200 offset:256\n"
    "ds_read_b64_tr_b16 v[170:171], v201 offset:256\n"
    "ds_read_b64_tr_b16 v[172:173], v202 offset:256\n"
    "ds_read_b64_tr_b16 v[174:175], v203 offset:256\n"
    "ds_read_b64_tr_b16 v[176:177], v200 offset:288\n"
    "ds_read_b64_tr_b16 v[178:179], v201 offset:288\n"
    "ds_read_b64_tr_b16 v[180:181], v202 offset:288\n"
    "ds_read_b64_tr_b16 v[182:183], v203 offset:288\n"
    "ds_read_b64_tr_b16 v[184:185], v200 offset:320\n"
    "ds_read_b64_tr_b16 v[186:187], v201 offset:320\n"
    "ds_read_b64_tr_b16 v[188:189], v202 offset:320\n"
    "ds_read_b64_tr_b16 v[190:191], v203 offset:320\n"
    "ds_read_b64_tr_b16 v[192:193], v200 offset:352\n"
    "ds_read_b64_tr_b16 v[194:195], v201 offset:352\n"
    "ds_read_b64_tr_b16 v[196:197], v202 offset:352\n"
    "ds_read_b64_tr_b16 v[198:199], v203 offset:352\n"
    "s_branch .La_filled\n"
    ".La2:\n"
    "s_sub_u32 s52, s52, 1\n"
    "s_cmp_lt_i32 s52, 0\n"
    "s_cbranch_scc1 .La2b\n"
    "s_sleep 1\n"
    "ds_read_b32 v254, v6 offset:16\n"
    "s_waitcnt lgkmcnt(0)\n"
    "v_cmp_gt_u32 vcc, s55, v254\n"
    "s_cbranch_vccnz .La2\n"
    "s_branch .La2b\n"
    ".La3:\n"
    "s_sub_u32 s52, s52, 1\n"
    "s_cmp_lt_i32 s52, 0\n"
    "s_cbranch_scc1 .La3b\n"
    "s_sleep 1\n"
    "ds_read_b32 v253, v6 offset:32\n"
    "s_waitcnt lgkmcnt(0)\n"
    "v_cmp_gt_i32 vcc, s56, v253\n"
    "s_cbranch_vccnz .La3\n"
    "s_branch .La3b\n"
    ".La5:\n"
    "s_sub_u32 s52, s52, 1\n"
    "s_cmp_lt_i32 s52, 0\n"
    "s_cbranch_scc1 .La5b\n"
    "s_sleep 1\n"
    "ds_read_b32 v200, v6 offset:0\n"
    "s_waitcnt lgkmcnt(0)\n"
    "v_cmp_gt_u32 vcc, s54, v200\n"
    "s_cbranch_vccnz .La5\n"
    "s_branch .La5b\n"
    ".La6:\n"
    "s_sub_u32 s52, s52, 1\n"
    "s_cmp_lt_i32 s52, 0\n"
    "s_cbranch_scc1 .La6b\n"
    "s_sleep 1\n"
    "ds_read_b32 v254, v6 offset:16\n"
    "s_waitcnt lgkmcnt(0)\n"
    "v_cmp_gt_u32 vcc, s55, v254\n"
    "s_cbranch_vccnz .La6\n"
    "s_branch .La6b\n"
    ".La4:\n"
    "v_cvt_pk_bf16_f32 v128, v128, v129\n"
    "v_cvt_pk_bf16_f32 v129, v130, v131\n"
    "v_cvt_pk_bf16_f32 v130, v132, v133\n"
    "v_cvt_pk_bf16_f32 v131, v134, v135\n"
    "ds_write_b128 v4, v[128:131] offset:3072\n"
    "v_cvt_pk_bf16_f32 v136, v136, v137\n"
    "v_cvt_pk_bf16_f32 v137, v138, v139\n"
    "v_cvt_pk_bf16_f32 v138, v140, v141\n"
    "v_cvt_pk_bf16_f32 v139, v142, v143\n"
    "ds_write_b128 v4, v[136:139] offset:4096\n"
    "v_cvt_pk_bf16_f32 v144, v144, v145\n"
    "v_cvt_pk_bf16_f32 v145, v146, v147\n"
    "v_cvt_pk_bf16_f32 v146, v148, v149\n"
    "v_cvt_pk_bf16_f32 v147, v150, v151\n"
    "ds_write_b128 v4, v[144:147] offset:5120\n"
    "ds_write_b32 v7, v252 offset:128\n"
    "s_cmp_ge_u32 s45, 12\n"
    "s_cbranch_scc1 .La_end\n"
    "ds_read_b32 v200, v6 offset:0\n"
    "s_waitcnt lgkmcnt(0)\n"
    "v_cmp_gt_u32 vcc, s54, v200\n"
    "s_cbranch_vccnz .La5\n"
    ".La5b:\n"
    "ds_read_b32 v254, v6 offset:16\n"
    "s_waitcnt lgkmcnt(0)\n"
    "v_cmp_gt_u32 vcc, s55, v254\n"
    "s_cbranch_vccnz .La6\n"
    ".La6b:\n"
    "ds_read_b128 v[104:107], v3 offset:0\n"
    "ds_read_b128 v[152:155], v244 offset:0\n"
    "ds_read_b128 v[108:111], v3 offset:1024\n"
    "ds_read_b128 v[156:159], v244 offset:64\n"
    "ds_read_b128 v[112:115], v3 offset:2048\n"
    "ds_read_b128 v[160:163], v244 offset:128\n"
    "ds_read_b128 v[116:119], v3 offset:3072\n"
    "ds_read_b128 v[164:167], v244 offset:192\n"
    "ds_read_b128 v[120:123], v3 offset:4096\n"
    "ds_read_b128 v[124:127], v3 offset:5120\n"
    "ds_read_b128 v[128:131], v244 offset:256\n"
    "ds_read_b128 v[132:135], v244 offset:320\n"
    "ds_read_b128 v[136:139], v245 offset:0\n"
    "ds_read_b128 v[140:143], v245 offset:64\n"
    "s_branch .La4j\n"
    ".La7:\n"
    "s_sub_u32 s52, s52, 1\n"
    "s_cmp_lt_i32 s52, 0\n"
    "s_cbranch_scc1 .La7b\n"
    "s_sleep 1\n"
    "ds_read_b32 v253, v6 offset:32\n"
    "s_waitcnt lgkmcnt(0)\n"
    "v_cmp_gt_i32 vcc, s56, v253\n"
    "s_cbranch_vccnz .La7\n"
    "s_branch .La7b\n"
    ".La9:\n"
    "s_sub_u32 s52, s52, 1\n"
    "s_cmp_lt_i32 s52, 0\n"
    "s_cbranch_scc1 .La9b\n"
    "s_sleep 1\n"
    "ds_read_b32 v200, v6 offset:0\n"
    "s_waitcnt lgkmcnt(0)\n"
    "v_cmp_gt_u32 vcc, s54, v200\n"
    "s_cbranch_vccnz .La9\n"
    "s_branch .La9b\n"
    ".La10:\n"
    "s_sub_u32 s52, s52, 1\n"
    "s_cmp_lt_i32 s52, 0\n"
    "s_cbranch_scc1 .La10b\n"
    "s_sleep 1\n"
    "ds_read_b32 v254, v6 offset:16\n"
    "s_waitcnt lgkmcnt(0)\n"
    "v_cmp_gt_u32 vcc, s55, v254\n"
    "s_cbranch_vccnz .La10\n"
    "s_branch .La10b\n"
    ".La8:\n"
    "v_cvt_pk_bf16_f32 v32, v32, v33\n"
    "v_cvt_pk_bf16_f32 v33, v34, v35\n"
    "v_cvt_pk_bf16_f32 v34, v36, v37\n"
    "v_cvt_pk_bf16_f32 v35, v38, v39\n"
    "ds_write_b128 v4, v[32:35] offset:3072\n"
    "v_cvt_pk_bf16_f32 v40, v40, v41\n"
    "v_cvt_pk_bf16_f32 v41, v42, v43\n"
    "v_cvt_pk_bf16_f32 v42, v44, v45\n"
    "v_cvt_pk_bf16_f32 v43, v46, v47\n"
    "ds_write_b128 v4, v[40:43] offset:4096\n"
    "v_cvt_pk_bf16_f32 v48, v48, v49\n"
    "v_cvt_pk_bf16_f32 v49, v50, v51\n"
    "v_cvt_pk_bf16_f32 v50, v52, v53\n"
    "v_cvt_pk_bf16_f32 v51, v54, v55\n"
    "ds_write_b128 v4, v[48:51] offset:5120\n"
    "ds_write_b32 v7, v252 offset:128\n"
    "s_cmp_ge_u32 s45, 12\n"
    "s_cbranch_scc1 .La_end\n"
    "ds_read_b32 v200, v6 offset:0\n"
    "s_waitcnt lgkmcnt(0)\n"
    "v_cmp_gt_u32 vcc, s54, v200\n"
    "s_cbranch_vccnz .La9\n"
    ".La9b:\n"
    "ds_read_b32 v254, v6 offset:16\n"
    "s_waitcnt lgkmcnt(0)\n"
    "v_cmp_gt_u32 vcc, s55, v254\n"
    "s_cbranch_vccnz .La10\n"
    ".La10b:\n"
    "ds_read_b128 v[8:11], v3 offset:0\n"
    "ds_read_b128 v[56:59], v244 offset:0\n"
    "ds_read_b128 v[12:15], v3 offset:1024\n"
    "ds_read_b128 v[60:63], v244 offset:64\n"
    "ds_read_b128 v[16:19], v3 offset:2048\n"
    "ds_read_b128 v[64:67], v244 offset:128\n"
    "ds_read_b128 v[20:23], v3 offset:3072\n"
    "ds_read_b128 v[68:71], v244 offset:192\n"
    "ds_read_b128 v[24:27], v3 offset:4096\n"
    "ds_read_b128 v[28:31], v3 offset:5120\n"
    "ds_read_b128 v[32:35], v244 offset:256\n"
    "ds_read_b128 v[36:39], v244 offset:320\n"
    "ds_read_b128 v[40:43], v245 offset:0\n"
    "ds_read_b128 v[44:47], v245 offset:64\n"
    "s_branch .La8j\n"
    ".La_last:\n"
            :: "{v[0:3]}"(va), "{v[4:7]}"(vb), "{v[232:235]}"(cbM), "{v[236:239]}"(cbF), "{v[240:243]}"(cbL),
               "{s60}"(UNI(slots)), "{s61}"(UNI(slots >> 32)), "{s45}"(UNI(n0)), "{s54}"(UNI(u0 + 4)), "{s63}"(UNI((unsigned)(size_t)smem + LF + 320 + 64 * sl)), "{s66}"(UNI(sl)),
               "{s46}"(-0.10411754112863712f), "{s47}"(0.10411754112863712f)
            : "memory", "v254", "s48", "s49", "s50", "s51", "s52", "s53", "s55", "s56", "s57", "s58", "s59", "s62", "s67", "s68", "s69", "s90", "s91", "s92", "s93", "s94", "s95", "vcc", "scc");
    }
}

extern "C" void kernel_launch(void* const* d_in, const int* in_sizes, int n_in,
                              void* d_out, int out_size, void* d_ws, size_t ws_size,
                              hipStream_t stream) {
    na_main<<<256, 512, 0, stream>>>((const u16*)d_in[0], (const u16*)d_in[1], (const u16*)d_in[2], (u16*)d_out, (char*)d_ws + 0);
}
```

```cpp
#include <hip/hip_runtime.h>
#include <stdint.h>

typedef unsigned short u16;
typedef unsigned long long u64;
typedef __attribute__((ext_vector_type(4))) float f32x4;
typedef __attribute__((ext_vector_type(4))) unsigned u32x4;
typedef __attribute__((ext_vector_type(2))) unsigned u32x2;
typedef __attribute__((ext_vector_type(8))) unsigned u32x8;

#define DEVINL __device__ __forceinline__
#define LAMBDA_INL __attribute__((always_inline))
#define UNI(x) __builtin_amdgcn_readfirstlane((int)(x))

static constexpr int IMW = 224;
static constexpr int PIXB = 384;
static constexpr int IROWB = IMW * PIXB;
static constexpr int RP = 8896;
static constexpr int LQ = 14 * RP, LZ = LQ + 3 * 6144, LF = LZ + 3 * 6144;
static constexpr int LDS_TOT = 163840;
static constexpr unsigned OOB = 0x100000u;
static constexpr int WS_W = 8192, WS_FL = WS_W + 144 * 1024, NREP = 8, WS_ACK = 393216;

DEVINL int pixad(int c) { return 800 * ((c + 1) >> 1) + 384 * ((c + 1) & 1) - 384; }
struct AxisMap { int o, d, x, last; };
DEVINL int axis_at(const AxisMap& m, int v) {
    const int reg = m.o + m.d * v, ext = m.x + 2 * min(v - m.last - 1, 2), sel = (m.last - v) >> 31;
    return (ext & sel) | (reg & ~sel);
}
DEVINL int scol(int i) { return (int)((0xD5B3E6C4A2F79180ull >> (4 * i)) & 15); }

struct AttC { unsigned sb, v0, v1, pk; };
DEVINL AttC att_const(int lane, int H, int cedge) {
    const int i16 = lane & 15, g = lane >> 4, e = (i16 + 4) >> 3 & 1, qj = i16 - 4 * ((i16 + 4) >> 3), sc = scol(i16);
    const int ws0 = (cedge ? min(8 * H + qj, 9) : 8 * H + qj) - 8 * H;
    AttC c;
    c.pk = 8 | e << 8;
#pragma unroll
    for (int r = 0; r < 4; ++r) {
        const unsigned ok = (unsigned)(scol(4 * g + r) - ws0) <= 6u;
        c.pk |= ok << (4 + r) | (r & 1 ? ok << (r - 1) : 0u);
    }
    c.sb = pixad(8 * H + (sc < 14 ? sc : sc - 8)) + 16 * g;
    c.v0 = pixad(8 * H + scol(4 * (g >> 1) + ((lane >> 2) & 3))) + (g & 1) * RP + 8 * (lane & 3);
    c.v1 = pixad(8 * H + scol(8 + 4 * (g >> 1) + ((lane >> 2) & 3))) + (g & 1) * RP + 8 * (lane & 3);
    return c;
}

__global__ __launch_bounds__(512, 2) void na_main(const u16* __restrict__ x_, const u16* __restrict__ wqkv_,
                                                  const u16* __restrict__ wo_, u16* __restrict__ out_, char* ws_) {
    __shared__ __attribute__((aligned(1024))) char smem[LDS_TOT];
    int tid = threadIdx.x, bid = blockIdx.x;
    u32x8 ka;
    u32x2 ka4;
    u32x4 ax, xo, xq;
    unsigned xo4, xq4, t0;
    asm volatile(
    "s_load_dwordx8 s[8:15], s[0:1], 0x0\n"
    "s_load_dwordx2 s[16:17], s[0:1], 0x20\n"
    "s_memtime s[4:5]\n"
    "v_readfirstlane_b32 s7, v0\n"
    "s_lshr_b32 s24, s2, 3\n"
    "v_and_b32 v6, 63, v0\n"
    "s_getpc_b64 s[28:29]\n"
    ".Le_pc:\n"
    "s_lshl_b32 s25, s24, 10\n"
    "s_mov_b32 m0, 162816\n"
    "s_cmp_lg_u32 s7, 128\n"
    "s_cbranch_scc1 .Le_ncp\n"
    "v_lshl_add_u32 v7, v6, 4, s25\n"
    "global_load_lds_dwordx4 v7, s[28:29]\n"
    ".Le_ncp:\n"
    "s_lshr_b32 s27, s2, 6\n"
    "s_and_b32 s26, s24, 7\n"
    "s_lshr_b32 s7, s7, 6\n"
    "s_add_u32 s3, s27, 14\n"
    "s_cmp_eq_u32 s26, 7\n"
    "s_cselect_b32 s3, s3, 99\n"
    "s_cmp_lt_u32 s24, 16\n"
    "s_cselect_b32 s3, s24, s3\n"
    "s_cmp_lg_u32 s7, 7\n"
    "s_cbranch_scc1 .Le_np\n"
    "s_cmp_ge_u32 s3, 18\n"
    "s_cbranch_scc1 .Le_7\n"
    "s_and_b32 s23, s2, 3\n"
    "s_mul_i32 s23, s23, 18\n"
    "s_add_u32 s3, s3, s23\n"
    "s_mul_i32 s44, s3, 43\n"
    "s_lshr_b32 s44, s44, 8\n"
    "s_mul_i32 s25, s44, 6\n"
    "s_sub_u32 s25, s3, s25\n"
    "v_lshrrev_b32 v4, 2, v6\n"
    "v_lshrrev_b32 v5, 4, v6\n"
    "v_sub_u32 v3, 0, v5\n"
    "v_xor_b32 v3, v3, v6\n"
    "v_and_b32 v3, 3, v3\n"
    "v_lshlrev_b32 v3, 4, v3\n"
    "s_mul_i32 s25, s25, 36864\n"
    "s_movk_i32 s23, 1152\n"
    "s_lshr_b32 s42, s44, 1\n"
    "s_and_b32 s43, s44, 1\n"
    "v_mov_b32 v9, 0\n"
    "s_mov_b32 m0, 124544\n"
    "s_bitcmp1_b32 s2, 2\n"
    "s_cbranch_scc1 .Le_r2\n"
    "v_lshl_add_u32 v7, v5, 2, v4\n"
    "v_mad_u32_u24 v7, v7, s23, v3\n"
    "s_lshl3_add_u32 s44, s42, s43\n"
    "s_mul_i32 s44, s44, 4608\n"
    "s_addk_i32 s44, 384\n"
    "s_mov_b32 s23, 6\n"
    "s_waitcnt lgkmcnt(0)\n"
    "s_add_u32 s26, s10, s25\n"
    "s_addc_u32 s27, s11, 0\n"
    "global_load_dwordx2 v[12:13], v9, s[16:17] sc1\n"
    "s_add_u32 s28, s26, 4608\n"
    "s_addc_u32 s29, s27, 0\n"
    "s_add_u32 s30, s10, s44\n"
    "s_addc_u32 s31, s11, 0\n"
    ".Le_l1:\n"
    "global_load_lds_dwordx4 v7, s[26:27]\n"
    "s_addk_i32 m0, 1024\n"
    "s_sub_u32 s23, s23, 1\n"
    "global_load_lds_dwordx4 v7, s[28:29]\n"
    "s_addk_i32 m0, 1024\n"
    "s_nop 0\n"
    "global_load_lds_dwordx4 v7, s[30:31]\n"
    "s_addk_i32 m0, 1024\n"
    "s_cmp_lg_u32 s23, 0\n"
    "v_add_u32 v7, 64, v7\n"
    "s_cbranch_scc1 .Le_l1\n"
    "s_branch .Le_p1\n"
    ".Le_r2:\n"
    "v_bfe_u32 v100, v6, 2, 2\n"
    "v_and_b32 v7, 1, v5\n"
    "v_lshl_add_u32 v7, v7, 4, v100\n"
    "v_lshrrev_b32 v16, 5, v6\n"
    "v_lshl_add_u32 v7, v16, 3, v7\n"
    "v_mad_u32_u24 v7, v7, s23, v3\n"
    "v_and_b32 v19, 3, v6\n"
    "v_mul_u32_u24 v16, 24, v4\n"
    "v_add_lshl_u32 v16, v16, v19, 4\n"
    "s_lshl_b32 s31, s42, 6\n"
    "s_addk_i32 s25, 768\n"
    "s_mov_b32 s23, 6\n"
    "s_waitcnt lgkmcnt(0)\n"
    "s_add_u32 s26, s10, s25\n"
    "s_addc_u32 s27, s11, 0\n"
    "global_load_dwordx2 v[12:13], v9, s[16:17] sc1\n"
    "s_add_u32 s28, s26, 4608\n"
    "s_addc_u32 s29, s27, 0\n"
    "s_add_u32 s30, s12, s31\n"
    "s_addc_u32 s31, s13, 0\n"
    ".Le_l2:\n"
    "global_load_lds_dwordx4 v7, s[26:27]\n"
    "s_addk_i32 m0, 1024\n"
    "s_sub_u32 s23, s23, 1\n"
    "global_load_lds_dwordx4 v7, s[28:29]\n"
    "s_addk_i32 m0, 1024\n"
    "v_add_u32 v7, 64, v7\n"
    "global_load_lds_dwordx4 v16, s[30:31]\n"
    "s_addk_i32 m0, 1024\n"
    "v_add_u32 v16, 6144, v16\n"
    "global_load_lds_dwordx4 v16, s[30:31]\n"
    "s_addk_i32 m0, 1024\n"
    "v_add_u32 v16, 6144, v16\n"
    "s_cmp_lg_u32 s23, 0\n"
    "s_cbranch_scc1 .Le_l2\n"
    "s_branch .Le_p2\n"
    ".Le_np:\n"
    "s_cmp_eq_u32 s7, 4\n"
    "s_cbranch_scc1 .Le_a\n"
    ".Le_xo:\n"
    "s_and_b32 s25, s2, 1\n"
    "s_lshr_b32 s23, 0x14a, s7\n"
    "s_and_b32 s23, s23, 1\n"
    "s_lshl2_add_u32 s29, s25, s27\n"
    "s_mul_i32 s22, s23, 486\n"
    "s_cmp_eq_u32 s26, 7\n"
    "s_cselect_b32 s26, s29, s26\n"
    "s_cselect_b32 s31, 1, s27\n"
    "s_cselect_b32 s18, 1, 0\n"
    "s_cmp_eq_u32 s26, 7\n"
    "s_cbranch_scc1 .Le_sp\n"
    "v_lshl_add_u32 v1, s23, 6, v6\n"
    "v_add_u32 v5, s22, v6\n"
    "s_movk_i32 s22, 1311\n"
    "v_add_u32 v2, 64 + 24 + 128, v1\n"
    "v_add_u32 v3, 64 + 24 + 256, v1\n"
    "v_add_u32 v4, 64 + 24 + 384, v1\n"
    "v_add_u32 v1, 64 + 24, v1\n"
    "v_add_u32 v5, 24, v5\n"
    "v_mul_u32_u24 v7, s22, v1\n"
    "s_lshl_b32 s29, s26, 5\n"
    "v_mul_u32_u24 v8, s22, v2\n"
    "s_sub_u32 s20, 222, s29\n"
    "v_mul_u32_u24 v9, s22, v3\n"
    "s_add_u32 s29, s29, 1\n"
    "v_mul_u32_u24 v10, s22, v4\n"
    "s_bitcmp1_b32 s2, 1\n"
    "v_mul_u32_u24 v11, s22, v5\n"
    "s_cselect_b32 s20, s29, s20\n"
    "v_not_b32 v12, 49\n"
    "v_lshrrev_b32 v7, 16, v7\n"
    "s_cselect_b32 s21, 2, -2\n"
    "v_lshrrev_b32 v8, 16, v8\n"
    "s_cselect_b32 s30, 218, 1\n"
    "v_lshrrev_b32 v9, 16, v9\n"
    "v_lshrrev_b32 v10, 16, v10\n"
    "v_lshrrev_b32 v11, 16, v11\n"
    "s_sub_u32 s29, s20, s21\n"
    "s_mul_i32 s29, s29, 384\n"
    "s_mul_i32 s22, s21, 768\n"
    "v_mad_i32_i24 v1, v7, v12, v1\n"
    "s_lshl_b32 s28, s27, 3\n"
    "v_mad_i32_i24 v2, v8, v12, v2\n"
    "s_lshr_b32 s28, 0x1837321d, s28\n"
    "v_mad_i32_i24 v3, v9, v12, v3\n"
    "s_and_b32 s28, s28, 0xff\n"
    "v_mad_i32_i24 v4, v10, v12, v4\n"
    "v_mad_i32_i24 v5, v11, v12, v5\n"
    "v_mov_b32 v18, s29\n"
    "s_add_u32 s29, s21, -1\n"
    "s_mul_i32 s29, s29, 384\n"
    "v_add_u32 v19, s29, v18\n"
    "v_min_u32 v1, 47, v1\n"
    "s_mul_i32 s29, s25, 50\n"
    "v_min_u32 v2, 47, v2\n"
    "s_add_u32 s28, s28, s29\n"
    "v_min_u32 v3, 47, v3\n"
    "s_cmp_lg_u32 s18, 0\n"
    "v_min_u32 v4, 47, v4\n"
    "s_cselect_b32 s28, 0, s28\n"
    "v_min_u32 v5, 47, v5\n"
    "v_cmp_lt_u32 s[42:43], 23, v1\n"
    "v_cmp_lt_u32 s[44:45], 23, v2\n"
    "v_cmp_lt_u32 s[46:47], 23, v3\n"
    "v_cmp_lt_u32 s[48:49], 23, v4\n"
    "v_cmp_lt_u32 s[50:51], 23, v5\n"
    "s_lshl_b32 s28, s28, 1\n"
    "s_sub_u32 s18, 222, s28\n"
    "s_add_u32 s28, s28, 1\n"
    "s_lshr_b32 s29, s2, 2\n"
    "v_cndmask_b32 v13, v18, v19, s[42:43]\n"
    "v_cndmask_b32 v14, v18, v19, s[44:45]\n"
    "v_cndmask_b32 v15, v18, v19, s[46:47]\n"
    "v_cndmask_b32 v16, v18, v19, s[48:49]\n"
    "v_cndmask_b32 v17, v18, v19, s[50:51]\n"
    "s_xor_b32 s31, s29, s31\n"
    "s_bitcmp1_b32 s2, 2\n"
    "s_cselect_b32 s18, s28, s18\n"
    "v_mad_i32_i24 v13, v7, s22, v13\n"
    "v_mad_i32_i24 v14, v8, s22, v14\n"
    "v_mad_i32_i24 v15, v9, s22, v15\n"
    "v_mad_i32_i24 v16, v10, s22, v16\n"
    "v_mad_i32_i24 v17, v11, s22, v17\n"
    "s_bitcmp1_b32 s31, 0\n"
    "s_cselect_b32 s19, -2, 2\n"
    "s_cmp_eq_u32 s26, 6\n"
    "s_cbranch_scc1 .Le_ce\n"
    ".Le_cd:\n"
    "v_lshl_add_u32 v1, v1, 4, v13\n"
    "v_lshl_add_u32 v2, v2, 4, v14\n"
    "v_lshl_add_u32 v3, v3, 4, v15\n"
    "v_lshl_add_u32 v4, v4, 4, v16\n"
    "v_lshl_add_u32 v5, v5, 4, v17\n"
    "s_cmp_lt_u32 s7, 4\n"
    "s_cbranch_scc1 .Le_b\n"
    "s_cmp_ge_u32 s7, 7\n"
    "s_cbranch_scc1 .Le_72\n"
    "s_mul_i32 s25, s19, 3\n"
    "s_add_u32 s25, s25, s18\n"
    "s_mul_i32 s25, s25, 86016\n"
    "s_mul_i32 s28, s19, 86016\n"
    "s_lshl_b32 s24, s23, 10\n"
    "s_addk_i32 s24, 27712\n"
    "s_mov_b32 s29, 6\n"
    "s_cmp_lt_u32 s3, 18\n"
    "s_cselect_b32 s31, 3, -1\n"
    "s_cselect_b32 s30, 2, 0\n"
    "s_bitcmp1_b32 s2, 2\n"
    "s_cselect_b32 s31, -1, s31\n"
    "s_nop 0\n"
    "s_or_b32 s30, s30, s23\n"
    "s_barrier\n"
    "s_waitcnt lgkmcnt(0)\n"
    ".Le_xl:\n"
    "s_cmp_eq_u32 s29, s31\n"
    "s_cbranch_scc1 .Le_h\n"
    ".Le_go:\n"
    "s_mov_b32 m0, s24\n"
    "s_add_u32 s26, s8, s25\n"
    "s_addc_u32 s27, s9, 0\n"
    "global_load_lds_dwordx4 v1, s[26:27]\n"
    "s_addk_i32 m0, 2048\n"
    "s_add_u32 s25, s25, s28\n"
    "global_load_lds_dwordx4 v2, s[26:27]\n"
    "s_addk_i32 m0, 2048\n"
    "s_add_u32 s24, s24, 8896\n"
    "global_load_lds_dwordx4 v3, s[26:27]\n"
    "s_addk_i32 m0, 2048\n"
    "s_cmp_lg_u32 s23, 0\n"
    "s_cbranch_scc1 .Le_x3\n"
    "global_load_lds_dwordx4 v4, s[26:27]\n"
    ".Le_x3:\n"
    "s_sub_u32 s29, s29, 1\n"
    "s_cmp_lg_u32 s29, 0\n"
    "s_cbranch_scc1 .Le_xl\n"
    "s_branch .Le_g2s\n"
    ".Le_h:\n"
    "v_mov_b32 v16, 161456\n"
    "ds_read_b32 v7, v16\n"
    "s_waitcnt lgkmcnt(0)\n"
    "v_cmp_gt_u32 vcc, 1, v7\n"
    "s_cbranch_vccnz .Le_h\n"
    "s_branch .Le_go\n"
    ".Le_a:\n"
    "s_add_u32 s25, s25, 32768\n"
    "s_mov_b32 s23, .Lg_last-.Le_pc\n"
    "s_max_u32 s23, s23, .Lh_last-.Le_pc\n"
    "s_max_u32 s23, s23, .La_last-.Le_pc\n"
    "s_max_u32 s23, s23, .Ls_last-.Le_pc\n"
    "s_min_u32 s25, s25, s23\n"
    "v_lshl_add_u32 v7, v6, 4, s25\n"
    "global_load_lds_dwordx4 v7, s[28:29]\n"
    "s_cmp_lt_u32 s3, 18\n"
    "s_cselect_b32 s23, 0, 8\n"
    "v_lshrrev_b32 v10, 1, v6\n"
    "v_cmp_eq_u32 vcc, 1, v10\n"
    "v_lshrrev_b32 v7, 2, v6\n"
    "v_lshrrev_b32 v8, v7, s23\n"
    "v_mov_b32 v9, 0\n"
    "v_and_b32 v8, 1, v8\n"
    "v_lshlrev_b32 v8, 1, v8\n"
    "v_cndmask_b32 v8, v8, 7, vcc\n"
    "v_cmp_eq_u32 vcc, 5, v10\n"
    "v_lshlrev_b32 v7, 2, v6\n"
    "v_add_u32 v7, 161408, v7\n"
    "v_cndmask_b32 v8, v8, 15, vcc\n"
    "ds_write_b32 v7, v9 offset:64\n"
    "ds_write_b32 v7, v9 offset:768\n"
    "ds_write_b32 v7, v8\n"
    "s_waitcnt lgkmcnt(0)\n"
    "s_branch .Le_b\n"
    ".Le_ce:\n"
    "s_movk_i32 s29, 384\n"
    "v_cndmask_b32 v20, 0, 1, s[42:43]\n"
    "v_lshl_add_u32 v21, v7, 1, v20\n"
    "v_cmp_lt_u32 vcc, 16, v21\n"
    "v_add_u32 v22, -17, v21\n"
    "v_min_u32 v22, 2, v22\n"
    "v_lshl_add_u32 v22, v22, 1, s30\n"
    "v_sub_u32 v22, v22, v20\n"
    "v_mul_i32_i24 v22, s29, v22\n"
    "v_cndmask_b32 v13, v13, v22, vcc\n"
    "v_cndmask_b32 v20, 0, 1, s[44:45]\n"
    "v_lshl_add_u32 v21, v8, 1, v20\n"
    "v_cmp_lt_u32 vcc, 16, v21\n"
    "v_add_u32 v22, -17, v21\n"
    "v_min_u32 v22, 2, v22\n"
    "v_lshl_add_u32 v22, v22, 1, s30\n"
    "v_sub_u32 v22, v22, v20\n"
    "v_mul_i32_i24 v22, s29, v22\n"
    "v_cndmask_b32 v14, v14, v22, vcc\n"
    "v_cndmask_b32 v20, 0, 1, s[46:47]\n"
    "v_lshl_add_u32 v21, v9, 1, v20\n"
    "v_cmp_lt_u32 vcc, 16, v21\n"
    "v_add_u32 v22, -17, v21\n"
    "v_min_u32 v22, 2, v22\n"
    "v_lshl_add_u32 v22, v22, 1, s30\n"
    "v_sub_u32 v22, v22, v20\n"
    "v_mul_i32_i24 v22, s29, v22\n"
    "v_cndmask_b32 v15, v15, v22, vcc\n"
    "v_cndmask_b32 v20, 0, 1, s[48:49]\n"
    "v_lshl_add_u32 v21, v10, 1, v20\n"
    "v_cmp_lt_u32 vcc, 16, v21\n"
    "v_add_u32 v22, -17, v21\n"
    "v_min_u32 v22, 2, v22\n"
    "v_lshl_add_u32 v22, v22, 1, s30\n"
    "v_sub_u32 v22, v22, v20\n"
    "v_mul_i32_i24 v22, s29, v22\n"
    "v_cndmask_b32 v16, v16, v22, vcc\n"
    "v_cndmask_b32 v20, 0, 1, s[50:51]\n"
    "v_lshl_add_u32 v21, v11, 1, v20\n"
    "v_cmp_lt_u32 vcc, 16, v21\n"
    "v_add_u32 v22, -17, v21\n"
    "v_min_u32 v22, 2, v22\n"
    "v_lshl_add_u32 v22, v22, 1, s30\n"
    "v_sub_u32 v22, v22, v20\n"
    "v_mul_i32_i24 v22, s29, v22\n"
    "v_cndmask_b32 v17, v17, v22, vcc\n"
    "s_branch .Le_cd\n"
    ".Le_p1:\n"
    "s_barrier\n"
    "v_bfe_u32 v8, v6, 2, 2\n"
    "v_sub_u32 v8, 0, v8\n"
    "v_xor_b32 v8, v8, v5\n"
    "v_and_b32 v8, 3, v8\n"
    "v_lshlrev_b32 v8, 4, v8\n"
    "v_and_b32 v10, 15, v6\n"
    "v_lshl_add_u32 v8, v10, 6, v8\n"
    "v_add_u32 v8, 124544, v8\n"
    "s_waitcnt vmcnt(9)\n"
    "v_add_u32 v12, 0x7f4a7c15, v12\n"
    "v_add_u32 v13, 0x9e3779b9, v13\n"
    "ds_read_b128 v[20:23], v8 offset:0\n"
    "ds_read_b128 v[24:27], v8 offset:1024\n"
    "ds_read_b128 v[28:31], v8 offset:2048\n"
    "ds_read_b128 v[32:35], v8 offset:3072\n"
    "ds_read_b128 v[36:39], v8 offset:4096\n"
    "ds_read_b128 v[40:43], v8 offset:5120\n"
    "ds_read_b128 v[44:47], v8 offset:6144\n"
    "ds_read_b128 v[48:51], v8 offset:7168\n"
    "ds_read_b128 v[52:55], v8 offset:8192\n"
    "s_waitcnt vmcnt(0)\n"
    "ds_read_b128 v[56:59], v8 offset:9216\n"
    "ds_read_b128 v[60:63], v8 offset:10240\n"
    "ds_read_b128 v[64:67], v8 offset:11264\n"
    "ds_read_b128 v[68:71], v8 offset:12288\n"
    "ds_read_b128 v[72:75], v8 offset:13312\n"
    "ds_read_b128 v[76:79], v8 offset:14336\n"
    "ds_read_b128 v[80:83], v8 offset:15360\n"
    "ds_read_b128 v[84:87], v8 offset:16384\n"
    "ds_read_b128 v[88:91], v8 offset:17408\n"
    "s_waitcnt lgkmcnt(14)\n"
    "v_mfma_f32_16x16x32_bf16 v[92:95], v[20:23], v[28:31], 0\n"
    "v_mfma_f32_16x16x32_bf16 v[96:99], v[24:27], v[28:31], 0\n"
    "s_waitcnt lgkmcnt(12)\n"
    "v_mfma_f32_16x16x32_bf16 v[92:95], v[32:35], v[40:43], v[92:95]\n"
    "v_mfma_f32_16x16x32_bf16 v[96:99], v[36:39], v[40:43], v[96:99]\n"
    "s_waitcnt lgkmcnt(9)\n"
    "v_mfma_f32_16x16x32_bf16 v[92:95], v[44:47], v[52:55], v[92:95]\n"
    "v_mfma_f32_16x16x32_bf16 v[96:99], v[48:51], v[52:55], v[96:99]\n"
    "s_waitcnt lgkmcnt(6)\n"
    "v_mfma_f32_16x16x32_bf16 v[92:95], v[56:59], v[64:67], v[92:95]\n"
    "v_mfma_f32_16x16x32_bf16 v[96:99], v[60:63], v[64:67], v[96:99]\n"
    "s_waitcnt lgkmcnt(3)\n"
    "v_mfma_f32_16x16x32_bf16 v[92:95], v[68:71], v[76:79], v[92:95]\n"
    "v_mfma_f32_16x16x32_bf16 v[96:99], v[72:75], v[76:79], v[96:99]\n"
    "s_waitcnt lgkmcnt(0)\n"
    "v_mfma_f32_16x16x32_bf16 v[92:95], v[80:83], v[88:91], v[92:95]\n"
    "v_mfma_f32_16x16x32_bf16 v[96:99], v[84:87], v[88:91], v[96:99]\n"
    ".Le_pub:\n"
    "s_lshl_b32 s23, s3, 10\n"
    "s_add_u32 s23, s23, 8192\n"
    "v_lshl_add_u32 v9, v6, 4, s23\n"
    "s_lshl_b32 s23, s3, 7\n"
    "s_add_u32 s23, s23, 155648\n"
    "v_and_b32 v11, 7, v6\n"
    "v_mov_b32 v17, 2\n"
    "s_nop 1\n"
    "v_cvt_pk_bf16_f32 v92, v92, v93\n"
    "v_cvt_pk_bf16_f32 v93, v94, v95\n"
    "v_cvt_pk_bf16_f32 v94, v96, v97\n"
    "v_cvt_pk_bf16_f32 v95, v98, v99\n"
    "v_add_u32 v92, v92, v12\n"
    "global_store_dwordx4 v9, v[92:95], s[16:17] sc1\n"
    "v_add_u32 v14, v92, v95\n"
    "v_add_u32 v15, v92, v95\n"
    "v_lshlrev_b32 v18, 2, v11\n"
    "v_and_b32 v18, 12, v18\n"
    "v_add_u32 v18, 161456, v18\n"
    "v_lshrrev_b32 v10, 3, v6\n"
    "ds_write_b32 v18, v17\n"
    "v_mul_u32_u24 v10, 18432, v10\n"
    "v_lshl_add_u32 v10, v11, 4, v10\n"
    "v_add_u32 v10, s23, v10\n"
    "v_permlane32_swap_b32 v14, v15\n"
    "v_add_u32 v14, v14, v15\n"
    "s_nop 1\n"
    "v_add_u32_dpp v14, v14, v14 row_ror:8 row_mask:0xf bank_mask:0xf\n"
    "s_nop 1\n"
    "v_add_u32_dpp v14, v14, v14 row_ror:4 row_mask:0xf bank_mask:0xf\n"
    "s_nop 1\n"
    "v_add_u32_dpp v14, v14, v14 row_ror:2 row_mask:0xf bank_mask:0xf\n"
    "s_nop 1\n"
    "v_add_u32_dpp v14, v14, v14 row_ror:1 row_mask:0xf bank_mask:0xf\n"
    "v_mov_b32 v15, v14\n"
    "s_nop 1\n"
    "v_permlane16_swap_b32 v14, v15\n"
    "v_add_u32 v14, v14, v15\n"
    "global_store_dwordx4 v10, v[12:15], s[16:17] sc1\n"
    "s_branch .Le_71\n"
    ".Le_p2:\n"
    "s_barrier\n"
    "v_bfe_u32 v8, v6, 2, 2\n"
    "v_sub_u32 v8, 0, v8\n"
    "v_xor_b32 v8, v8, v5\n"
    "v_and_b32 v8, 3, v8\n"
    "v_lshlrev_b32 v8, 4, v8\n"
    "v_and_b32 v10, 15, v6\n"
    "v_lshl_add_u32 v8, v10, 6, v8\n"
    "v_add_u32 v8, 124544, v8\n"
    "v_lshl_add_u32 v7, v5, 3, v100\n"
    "v_lshlrev_b32 v7, 6, v7\n"
    "v_lshl_add_u32 v7, v19, 4, v7\n"
    "s_lshl_b32 s23, s43, 3\n"
    "s_add_u32 s23, s23, 126592\n"
    "v_add_u32 v7, s23, v7\n"
    "s_add_u32 s3, s3, 72\n"
    "s_waitcnt vmcnt(0)\n"
    "v_add_u32 v12, 0x7f4a7c15, v12\n"
    "v_add_u32 v13, 0x9e3779b9, v13\n"
    "ds_read_b128 v[20:23], v8 offset:0\n"
    "ds_read_b128 v[24:27], v8 offset:1024\n"
    "ds_read_b64_tr_b16 v[28:29], v7 offset:0\n"
    "ds_read_b64_tr_b16 v[30:31], v7 offset:256\n"
    "ds_read_b128 v[32:35], v8 offset:4096\n"
    "ds_read_b128 v[36:39], v8 offset:5120\n"
    "ds_read_b64_tr_b16 v[40:41], v7 offset:4096\n"
    "ds_read_b64_tr_b16 v[42:43], v7 offset:4352\n"
    "ds_read_b128 v[44:47], v8 offset:8192\n"
    "ds_read_b128 v[48:51], v8 offset:9216\n"
    "ds_read_b64_tr_b16 v[52:53], v7 offset:8192\n"
    "ds_read_b64_tr_b16 v[54:55], v7 offset:8448\n"
    "ds_read_b128 v[56:59], v8 offset:12288\n"
    "ds_read_b128 v[60:63], v8 offset:13312\n"
    "ds_read_b64_tr_b16 v[64:65], v7 offset:12288\n"
    "ds_read_b64_tr_b16 v[66:67], v7 offset:12544\n"
    "ds_read_b128 v[68:71], v8 offset:16384\n"
    "ds_read_b128 v[72:75], v8 offset:17408\n"
    "ds_read_b64_tr_b16 v[76:77], v7 offset:16384\n"
    "ds_read_b64_tr_b16 v[78:79], v7 offset:16640\n"
    "ds_read_b128 v[80:83], v8 offset:20480\n"
    "ds_read_b128 v[84:87], v8 offset:21504\n"
    "ds_read_b64_tr_b16 v[88:89], v7 offset:20480\n"
    "ds_read_b64_tr_b16 v[90:91], v7 offset:20736\n"
    "s_waitcnt lgkmcnt(14)\n"
    "v_mfma_f32_16x16x32_bf16 v[92:95], v[20:23], v[28:31], 0\n"
    "v_mfma_f32_16x16x32_bf16 v[96:99], v[24:27], v[28:31], 0\n"
    "v_mfma_f32_16x16x32_bf16 v[92:95], v[32:35], v[40:43], v[92:95]\n"
    "v_mfma_f32_16x16x32_bf16 v[96:99], v[36:39], v[40:43], v[96:99]\n"
    "s_waitcnt lgkmcnt(12)\n"
    "v_mfma_f32_16x16x32_bf16 v[92:95], v[44:47], v[52:55], v[92:95]\n"
    "v_mfma_f32_16x16x32_bf16 v[96:99], v[48:51], v[52:55], v[96:99]\n"
    "s_waitcnt lgkmcnt(8)\n"
    "v_mfma_f32_16x16x32_bf16 v[92:95], v[56:59], v[64:67], v[92:95]\n"
    "v_mfma_f32_16x16x32_bf16 v[96:99], v[60:63], v[64:67], v[96:99]\n"
    "s_waitcnt lgkmcnt(4)\n"
    "v_mfma_f32_16x16x32_bf16 v[92:95], v[68:71], v[76:79], v[92:95]\n"
    "v_mfma_f32_16x16x32_bf16 v[96:99], v[72:75], v[76:79], v[96:99]\n"
    "s_waitcnt lgkmcnt(0)\n"
    "v_mfma_f32_16x16x32_bf16 v[92:95], v[80:83], v[88:91], v[92:95]\n"
    "v_mfma_f32_16x16x32_bf16 v[96:99], v[84:87], v[88:91], v[96:99]\n"
    "s_branch .Le_pub\n"
    ".Le_7:\n"
    "s_barrier\n"
    ".Le_71:\n"
    "s_lshr_b32 s27, s2, 6\n"
    "s_bfe_u32 s26, s2, 0x30003\n"
    "s_branch .Le_xo\n"
    ".Le_72:\n"
    "s_cmp_eq_u32 s7, 8\n"
    "s_cbranch_scc1 .Le_w\n"
    "s_mov_b32 s7, 8\n"
    "v_mov_b32 v101, v1\n"
    "v_mov_b32 v102, v2\n"
    "v_mov_b32 v103, v3\n"
    "v_mov_b32 v104, v4\n"
    "v_mov_b32 v105, v5\n"
    "s_branch .Le_71\n"
    ".Le_sp:\n"
    "s_cmp_lt_u32 s7, 7\n"
    "s_cbranch_scc1 .Le_b\n"
    "s_branch .Le_w\n"
    ".Le_g2s:\n"
    "s_mul_i32 s44, s18, 86016\n"
    "s_mul_i32 s45, s19, 86016\n"
    "s_lshl_b32 s56, s23, 2\n"
    "s_add_u32 s56, s56, 161408\n"
    "v_lshlrev_b32 v16, 20, v6\n"
    "v_add_u32 v16, s56, v16\n"
    "s_cmp_eq_u32 s30, 1\n"
    "s_cbranch_scc1 .Li_s1\n"
    "s_cmp_eq_u32 s30, 2\n"
    "s_cbranch_scc1 .Li_s2\n"
    "s_cmp_eq_u32 s30, 3\n"
    "s_cbranch_scc1 .Li_s3\n"
    "s_waitcnt vmcnt(16)\n"
    "v_mov_b32 v17, 1\n"
    "ds_write_b32 v16, v17 offset:0\n"
    "s_waitcnt vmcnt(8)\n"
    "v_mov_b32 v17, 2\n"
    "ds_write_b32 v16, v17 offset:0\n"
    "s_mul_i32 s56, s45, 2\n"
    "s_add_u32 s56, s56, s44\n"
    "s_add_u32 s60, s8, s56\n"
    "s_addc_u32 s61, s9, 0\n"
    "s_mov_b32 m0, 18816\n"
    "s_nop 0\n"
    "global_load_lds_dwordx4 v1, s[60:61]\n"
    "s_mov_b32 m0, 20864\n"
    "s_nop 0\n"
    "global_load_lds_dwordx4 v2, s[60:61]\n"
    "s_mov_b32 m0, 22912\n"
    "s_nop 0\n"
    "global_load_lds_dwordx4 v3, s[60:61]\n"
    "s_mov_b32 m0, 24960\n"
    "s_nop 0\n"
    "global_load_lds_dwordx4 v4, s[60:61]\n"
    "s_mov_b32 m0, 17792\n"
    "s_nop 0\n"
    "global_load_lds_dwordx4 v5, s[60:61]\n"
    "s_mul_i32 s56, s45, 1\n"
    "s_add_u32 s56, s56, s44\n"
    "s_add_u32 s60, s8, s56\n"
    "s_addc_u32 s61, s9, 0\n"
    "s_mov_b32 m0, 9920\n"
    "s_nop 0\n"
    "global_load_lds_dwordx4 v1, s[60:61]\n"
    "s_mov_b32 m0, 11968\n"
    "s_nop 0\n"
    "global_load_lds_dwordx4 v2, s[60:61]\n"
    "s_mov_b32 m0, 14016\n"
    "s_nop 0\n"
    "global_load_lds_dwordx4 v3, s[60:61]\n"
    "s_mov_b32 m0, 16064\n"
    "s_nop 0\n"
    "global_load_lds_dwordx4 v4, s[60:61]\n"
    "s_mov_b32 m0, 8896\n"
    "s_nop 0\n"
    "global_load_lds_dwordx4 v5, s[60:61]\n"
    "s_mul_i32 s56, s45, 0\n"
    "s_add_u32 s56, s56, s44\n"
    "s_add_u32 s60, s8, s56\n"
    "s_addc_u32 s61, s9, 0\n"
    "s_mov_b32 m0, 1024\n"
    "s_nop 0\n"
    "global_load_lds_dwordx4 v1, s[60:61]\n"
    "s_mov_b32 m0, 3072\n"
    "s_nop 0\n"
    "global_load_lds_dwordx4 v2, s[60:61]\n"
    "s_mov_b32 m0, 5120\n"
    "s_nop 0\n"
    "global_load_lds_dwordx4 v3, s[60:61]\n"
    "s_mov_b32 m0, 7168\n"
    "s_nop 0\n"
    "global_load_lds_dwordx4 v4, s[60:61]\n"
    "s_mov_b32 m0, 0\n"
    "s_nop 0\n"
    "global_load_lds_dwordx4 v5, s[60:61]\n"
    "s_mul_i32 s56, s45, 3\n"
    "s_add_u32 s56, s56, s44\n"
    "s_add_u32 s60, s8, s56\n"
    "s_addc_u32 s61, s9, 0\n"
    "s_mov_b32 m0, 26688\n"
    "s_nop 0\n"
    "global_load_lds_dwordx4 v5, s[60:61]\n"
    "s_mul_i32 s56, s45, 4\n"
    "s_add_u32 s56, s56, s44\n"
    "s_add_u32 s60, s8, s56\n"
    "s_addc_u32 s61, s9, 0\n"
    "s_mov_b32 m0, 35584\n"
    "s_nop 0\n"
    "global_load_lds_dwordx4 v5, s[60:61]\n"
    "s_mul_i32 s56, s45, 5\n"
    "s_add_u32 s56, s56, s44\n"
    "s_add_u32 s60, s8, s56\n"
    "s_addc_u32 s61, s9, 0\n"
    "s_mov_b32 m0, 44480\n"
    "s_nop 0\n"
    "global_load_lds_dwordx4 v5, s[60:61]\n"
    "s_mul_i32 s56, s45, 6\n"
    "s_add_u32 s56, s56, s44\n"
    "s_add_u32 s60, s8, s56\n"
    "s_addc_u32 s61, s9, 0\n"
    "s_mov_b32 m0, 53376\n"
    "s_nop 0\n"
    "global_load_lds_dwordx4 v5, s[60:61]\n"
    "s_mul_i32 s56, s45, 7\n"
    "s_add_u32 s56, s56, s44\n"
    "s_add_u32 s60, s8, s56\n"
    "s_addc_u32 s61, s9, 0\n"
    "s_mov_b32 m0, 62272\n"
    "s_nop 0\n"
    "global_load_lds_dwordx4 v5, s[60:61]\n"
    "s_mul_i32 s56, s45, 8\n"
    "s_add_u32 s56, s56, s44\n"
    "s_add_u32 s60, s8, s56\n"
    "s_addc_u32 s61, s9, 0\n"
    "s_mov_b32 m0, 71168\n"
    "s_nop 0\n"
    "global_load_lds_dwordx4 v5, s[60:61]\n"
    "s_waitcnt vmcnt(21)\n"
    "v_mov_b32 v17, 3\n"
    "ds_write_b32 v16, v17 offset:0\n"
    "s_waitcnt vmcnt(0)\n"
    "v_mov_b32 v17, 4\n"
    "ds_write_b32 v16, v17 offset:0\n"
    "s_branch .Li_sd\n"
    ".Li_s1:\n"
    "s_waitcnt vmcnt(12)\n"
    "v_mov_b32 v17, 1\n"
    "ds_write_b32 v16, v17 offset:0\n"
    "s_waitcnt vmcnt(6)\n"
    "v_mov_b32 v17, 2\n"
    "ds_write_b32 v16, v17 offset:0\n"
    "s_mul_i32 s56, s45, 2\n"
    "s_add_u32 s56, s56, s44\n"
    "s_add_u32 s60, s8, s56\n"
    "s_addc_u32 s61, s9, 0\n"
    "s_mov_b32 m0, 19840\n"
    "s_nop 0\n"
    "global_load_lds_dwordx4 v1, s[60:61]\n"
    "s_mov_b32 m0, 21888\n"
    "s_nop 0\n"
    "global_load_lds_dwordx4 v2, s[60:61]\n"
    "s_mov_b32 m0, 23936\n"
    "s_nop 0\n"
    "global_load_lds_dwordx4 v3, s[60:61]\n"
    "s_mov_b32 m0, 25568\n"
    "s_nop 0\n"
    "global_load_lds_dwordx4 v5, s[60:61]\n"
    "s_mul_i32 s56, s45, 1\n"
    "s_add_u32 s56, s56, s44\n"
    "s_add_u32 s60, s8, s56\n"
    "s_addc_u32 s61, s9, 0\n"
    "s_mov_b32 m0, 10944\n"
    "s_nop 0\n"
    "global_load_lds_dwordx4 v1, s[60:61]\n"
    "s_mov_b32 m0, 12992\n"
    "s_nop 0\n"
    "global_load_lds_dwordx4 v2, s[60:61]\n"
    "s_mov_b32 m0, 15040\n"
    "s_nop 0\n"
    "global_load_lds_dwordx4 v3, s[60:61]\n"
    "s_mov_b32 m0, 16672\n"
    "s_nop 0\n"
    "global_load_lds_dwordx4 v5, s[60:61]\n"
    "s_mul_i32 s56, s45, 0\n"
    "s_add_u32 s56, s56, s44\n"
    "s_add_u32 s60, s8, s56\n"
    "s_addc_u32 s61, s9, 0\n"
    "s_mov_b32 m0, 2048\n"
    "s_nop 0\n"
    "global_load_lds_dwordx4 v1, s[60:61]\n"
    "s_mov_b32 m0, 4096\n"
    "s_nop 0\n"
    "global_load_lds_dwordx4 v2, s[60:61]\n"
    "s_mov_b32 m0, 6144\n"
    "s_nop 0\n"
    "global_load_lds_dwordx4 v3, s[60:61]\n"
    "s_mov_b32 m0, 7776\n"
    "s_nop 0\n"
    "global_load_lds_dwordx4 v5, s[60:61]\n"
    "s_mul_i32 s56, s45, 3\n"
    "s_add_u32 s56, s56, s44\n"
    "s_add_u32 s60, s8, s56\n"
    "s_addc_u32 s61, s9, 0\n"
    "s_mov_b32 m0, 34464\n"
    "s_nop 0\n"
    "global_load_lds_dwordx4 v5, s[60:61]\n"
    "s_mul_i32 s56, s45, 4\n"
    "s_add_u32 s56, s56, s44\n"
    "s_add_u32 s60, s8, s56\n"
    "s_addc_u32 s61, s9, 0\n"
    "s_mov_b32 m0, 43360\n"
    "s_nop 0\n"
    "global_load_lds_dwordx4 v5, s[60:61]\n"
    "s_mul_i32 s56, s45, 5\n"
    "s_add_u32 s56, s56, s44\n"
    "s_add_u32 s60, s8, s56\n"
    "s_addc_u32 s61, s9, 0\n"
    "s_mov_b32 m0, 52256\n"
    "s_nop 0\n"
    "global_load_lds_dwordx4 v5, s[60:61]\n"
    "s_mul_i32 s56, s45, 6\n"
    "s_add_u32 s56, s56, s44\n"
    "s_add_u32 s60, s8, s56\n"
    "s_addc_u32 s61, s9, 0\n"
    "s_mov_b32 m0, 61152\n"
    "s_nop 0\n"
    "global_load_lds_dwordx4 v5, s[60:61]\n"
    "s_mul_i32 s56, s45, 7\n"
    "s_add_u32 s56, s56, s44\n"
    "s_add_u32 s60, s8, s56\n"
    "s_addc_u32 s61, s9, 0\n"
    "s_mov_b32 m0, 70048\n"
    "s_nop 0\n"
    "global_load_lds_dwordx4 v5, s[60:61]\n"
    "s_mul_i32 s56, s45, 8\n"
    "s_add_u32 s56, s56, s44\n"
    "s_add_u32 s60, s8, s56\n"
    "s_addc_u32 s61, s9, 0\n"
    "s_mov_b32 m0, 78944\n"
    "s_nop 0\n"
    "global_load_lds_dwordx4 v5, s[60:61]\n"
    "s_waitcnt vmcnt(18)\n"
    "v_mov_b32 v17, 3\n"
    "ds_write_b32 v16, v17 offset:0\n"
    "s_waitcnt vmcnt(0)\n"
    "v_mov_b32 v17, 4\n"
    "ds_write_b32 v16, v17 offset:0\n"
    "s_branch .Li_sd\n"
    ".Li_s2:\n"
    "s_waitcnt vmcnt(16)\n"
    "v_mov_b32 v17, 1\n"
    "ds_write_b32 v16, v17 offset:0\n"
    "s_waitcnt vmcnt(8)\n"
    "v_mov_b32 v17, 2\n"
    "ds_write_b32 v16, v17 offset:0\n"
    "s_waitcnt vmcnt(0)\n"
    "v_mov_b32 v17, 3\n"
    "ds_write_b32 v16, v17 offset:0\n"
    "v_add_u32 v18, 256, v16\n"
    ".Li_w01:\n"
    "s_sleep 1\n"
    "ds_read_b32 v19, v18\n"
    "s_waitcnt lgkmcnt(0)\n"
    "v_readfirstlane_b32 s56, v19\n"
    "s_cmp_eq_u32 s56, 0\n"
    "s_cbranch_scc1 .Li_w01\n"
    "s_mul_i32 s56, s45, 2\n"
    "s_add_u32 s56, s56, s44\n"
    "s_add_u32 s60, s8, s56\n"
    "s_addc_u32 s61, s9, 0\n"
    "s_mov_b32 m0, 18816\n"
    "s_nop 0\n"
    "global_load_lds_dwordx4 v1, s[60:61]\n"
    "s_mov_b32 m0, 20864\n"
    "s_nop 0\n"
    "global_load_lds_dwordx4 v2, s[60:61]\n"
    "s_mov_b32 m0, 22912\n"
    "s_nop 0\n"
    "global_load_lds_dwordx4 v3, s[60:61]\n"
    "s_mov_b32 m0, 24960\n"
    "s_nop 0\n"
    "global_load_lds_dwordx4 v4, s[60:61]\n"
    "s_mov_b32 m0, 17792\n"
    "s_nop 0\n"
    "global_load_lds_dwordx4 v5, s[60:61]\n"
    "s_mul_i32 s56, s45, 1\n"
    "s_add_u32 s56, s56, s44\n"
    "s_add_u32 s60, s8, s56\n"
    "s_addc_u32 s61, s9, 0\n"
    "s_mov_b32 m0, 9920\n"
    "s_nop 0\n"
    "global_load_lds_dwordx4 v1, s[60:61]\n"
    "s_mov_b32 m0, 11968\n"
    "s_nop 0\n"
    "global_load_lds_dwordx4 v2, s[60:61]\n"
    "s_mov_b32 m0, 14016\n"
    "s_nop 0\n"
    "global_load_lds_dwordx4 v3, s[60:61]\n"
    "s_mov_b32 m0, 16064\n"
    "s_nop 0\n"
    "global_load_lds_dwordx4 v4, s[60:61]\n"
    "s_mov_b32 m0, 8896\n"
    "s_nop 0\n"
    "global_load_lds_dwordx4 v5, s[60:61]\n"
    "s_mul_i32 s56, s45, 0\n"
    "s_add_u32 s56, s56, s44\n"
    "s_add_u32 s60, s8, s56\n"
    "s_addc_u32 s61, s9, 0\n"
    "s_mov_b32 m0, 1024\n"
    "s_nop 0\n"
    "global_load_lds_dwordx4 v1, s[60:61]\n"
    "s_mov_b32 m0, 3072\n"
    "s_nop 0\n"
    "global_load_lds_dwordx4 v2, s[60:61]\n"
    "s_mov_b32 m0, 5120\n"
    "s_nop 0\n"
    "global_load_lds_dwordx4 v3, s[60:61]\n"
    "s_mov_b32 m0, 7168\n"
    "s_nop 0\n"
    "global_load_lds_dwordx4 v4, s[60:61]\n"
    "s_mov_b32 m0, 0\n"
    "s_nop 0\n"
    "global_load_lds_dwordx4 v5, s[60:61]\n"
    "s_mul_i32 s56, s45, 3\n"
    "s_add_u32 s56, s56, s44\n"
    "s_add_u32 s60, s8, s56\n"
    "s_addc_u32 s61, s9, 0\n"
    "s_mov_b32 m0, 26688\n"
    "s_nop 0\n"
    "global_load_lds_dwordx4 v5, s[60:61]\n"
    "s_mul_i32 s56, s45, 4\n"
    "s_add_u32 s56, s56, s44\n"
    "s_add_u32 s60, s8, s56\n"
    "s_addc_u32 s61, s9, 0\n"
    "s_mov_b32 m0, 35584\n"
    "s_nop 0\n"
    "global_load_lds_dwordx4 v5, s[60:61]\n"
    "s_mul_i32 s56, s45, 5\n"
    "s_add_u32 s56, s56, s44\n"
    "s_add_u32 s60, s8, s56\n"
    "s_addc_u32 s61, s9, 0\n"
    "s_mov_b32 m0, 44480\n"
    "s_nop 0\n"
    "global_load_lds_dwordx4 v5, s[60:61]\n"
    "s_mul_i32 s56, s45, 6\n"
    "s_add_u32 s56, s56, s44\n"
    "s_add_u32 s60, s8, s56\n"
    "s_addc_u32 s61, s9, 0\n"
    "s_mov_b32 m0, 53376\n"
    "s_nop 0\n"
    "global_load_lds_dwordx4 v5, s[60:61]\n"
    "s_mul_i32 s56, s45, 7\n"
    "s_add_u32 s56, s56, s44\n"
    "s_add_u32 s60, s8, s56\n"
    "s_addc_u32 s61, s9, 0\n"
    "s_mov_b32 m0, 62272\n"
    "s_nop 0\n"
    "global_load_lds_dwordx4 v5, s[60:61]\n"
    "s_mul_i32 s56, s45, 8\n"
    "s_add_u32 s56, s56, s44\n"
    "s_add_u32 s60, s8, s56\n"
    "s_addc_u32 s61, s9, 0\n"
    "s_mov_b32 m0, 71168\n"
    "s_nop 0\n"
    "global_load_lds_dwordx4 v5, s[60:61]\n"
    "s_waitcnt vmcnt(0)\n"
    "v_mov_b32 v17, 4\n"
    "ds_write_b32 v16, v17 offset:0\n"
    "s_branch .Li_sd\n"
    ".Li_s3:\n"
    "s_waitcnt vmcnt(12)\n"
    "v_mov_b32 v17, 1\n"
    "ds_write_b32 v16, v17 offset:0\n"
    "s_waitcnt vmcnt(6)\n"
    "v_mov_b32 v17, 2\n"
    "ds_write_b32 v16, v17 offset:0\n"
    "s_waitcnt vmcnt(0)\n"
    "v_mov_b32 v17, 3\n"
    "ds_write_b32 v16, v17 offset:0\n"
    "v_add_u32 v18, 256, v16\n"
    ".Li_w11:\n"
    "s_sleep 1\n"
    "ds_read_b32 v19, v18\n"
    "s_waitcnt lgkmcnt(0)\n"
    "v_readfirstlane_b32 s56, v19\n"
    "s_cmp_eq_u32 s56, 0\n"
    "s_cbranch_scc1 .Li_w11\n"
    "s_mul_i32 s56, s45, 2\n"
    "s_add_u32 s56, s56, s44\n"
    "s_add_u32 s60, s8, s56\n"
    "s_addc_u32 s61, s9, 0\n"
    "s_mov_b32 m0, 19840\n"
    "s_nop 0\n"
    "global_load_lds_dwordx4 v1, s[60:61]\n"
    "s_mov_b32 m0, 21888\n"
    "s_nop 0\n"
    "global_load_lds_dwordx4 v2, s[60:61]\n"
    "s_mov_b32 m0, 23936\n"
    "s_nop 0\n"
    "global_load_lds_dwordx4 v3, s[60:61]\n"
    "s_mov_b32 m0, 25568\n"
    "s_nop 0\n"
    "global_load_lds_dwordx4 v5, s[60:61]\n"
    "s_mul_i32 s56, s45, 1\n"
    "s_add_u32 s56, s56, s44\n"
    "s_add_u32 s60, s8, s56\n"
    "s_addc_u32 s61, s9, 0\n"
    "s_mov_b32 m0, 10944\n"
    "s_nop 0\n"
    "global_load_lds_dwordx4 v1, s[60:61]\n"
    "s_mov_b32 m0, 12992\n"
    "s_nop 0\n"
    "global_load_lds_dwordx4 v2, s[60:61]\n"
    "s_mov_b32 m0, 15040\n"
    "s_nop 0\n"
    "global_load_lds_dwordx4 v3, s[60:61]\n"
    "s_mov_b32 m0, 16672\n"
    "s_nop 0\n"
    "global_load_lds_dwordx4 v5, s[60:61]\n"
    "s_mul_i32 s56, s45, 0\n"
    "s_add_u32 s56, s56, s44\n"
    "s_add_u32 s60, s8, s56\n"
    "s_addc_u32 s61, s9, 0\n"
    "s_mov_b32 m0, 2048\n"
    "s_nop 0\n"
    "global_load_lds_dwordx4 v1, s[60:61]\n"
    "s_mov_b32 m0, 4096\n"
    "s_nop 0\n"
    "global_load_lds_dwordx4 v2, s[60:61]\n"
    "s_mov_b32 m0, 6144\n"
    "s_nop 0\n"
    "global_load_lds_dwordx4 v3, s[60:61]\n"
    "s_mov_b32 m0, 7776\n"
    "s_nop 0\n"
    "global_load_lds_dwordx4 v5, s[60:61]\n"
    "s_mul_i32 s56, s45, 3\n"
    "s_add_u32 s56, s56, s44\n"
    "s_add_u32 s60, s8, s56\n"
    "s_addc_u32 s61, s9, 0\n"
    "s_mov_b32 m0, 34464\n"
    "s_nop 0\n"
    "global_load_lds_dwordx4 v5, s[60:61]\n"
    "s_mul_i32 s56, s45, 4\n"
    "s_add_u32 s56, s56, s44\n"
    "s_add_u32 s60, s8, s56\n"
    "s_addc_u32 s61, s9, 0\n"
    "s_mov_b32 m0, 43360\n"
    "s_nop 0\n"
    "global_load_lds_dwordx4 v5, s[60:61]\n"
    "s_mul_i32 s56, s45, 5\n"
    "s_add_u32 s56, s56, s44\n"
    "s_add_u32 s60, s8, s56\n"
    "s_addc_u32 s61, s9, 0\n"
    "s_mov_b32 m0, 52256\n"
    "s_nop 0\n"
    "global_load_lds_dwordx4 v5, s[60:61]\n"
    "s_mul_i32 s56, s45, 6\n"
    "s_add_u32 s56, s56, s44\n"
    "s_add_u32 s60, s8, s56\n"
    "s_addc_u32 s61, s9, 0\n"
    "s_mov_b32 m0, 61152\n"
    "s_nop 0\n"
    "global_load_lds_dwordx4 v5, s[60:61]\n"
    "s_mul_i32 s56, s45, 7\n"
    "s_add_u32 s56, s56, s44\n"
    "s_add_u32 s60, s8, s56\n"
    "s_addc_u32 s61, s9, 0\n"
    "s_mov_b32 m0, 70048\n"
    "s_nop 0\n"
    "global_load_lds_dwordx4 v5, s[60:61]\n"
    "s_mul_i32 s56, s45, 8\n"
    "s_add_u32 s56, s56, s44\n"
    "s_add_u32 s60, s8, s56\n"
    "s_addc_u32 s61, s9, 0\n"
    "s_mov_b32 m0, 78944\n"
    "s_nop 0\n"
    "global_load_lds_dwordx4 v5, s[60:61]\n"
    "s_waitcnt vmcnt(0)\n"
    "v_mov_b32 v17, 4\n"
    "ds_write_b32 v16, v17 offset:0\n"
    "s_branch .Li_sd\n"
    ".Li_sd:\n"
    "s_setprio 3\n"
    "s_branch .Le_w\n"
    ".Le_b:\n"
    "s_barrier\n"
    ".Le_w:\n"
    "s_waitcnt lgkmcnt(0)\n"
        :
          "={s[8:15]}"(ka), "={s[16:17]}"(ka4), "={s4}"(t0), "={s18}"(ax[0]), "={s19}"(ax[1]), "={s20}"(ax[2]), "={s21}"(ax[3]),
          "={v1}"(xo[0]), "={v2}"(xo[1]), "={v3}"(xo[2]), "={v4}"(xo[3]), "={v5}"(xo4),
          "={v101}"(xq[0]), "={v102}"(xq[1]), "={v103}"(xq[2]), "={v104}"(xq[3]), "={v105}"(xq4), "+{v0}"(tid), "+{s2}"(bid)
        : "{s[0:1]}"(__builtin_amdgcn_kernarg_segment_ptr())
        : "memory", "scc", "vcc", "s3", "s5", "s7", "s22", "s42", "s43", "s44", "s45", "s46", "s47", "s48", "s49", "s50", "s51", "s87", "s56", "s60", "s61", "s23", "s24", "s25", "s26", "s27", "s28", "s29", "s30", "s31",
          "v6", "v7", "v8", "v9", "v10", "v11", "v12", "v13", "v14", "v15", "v16", "v17", "v18", "v19", "v20", "v21", "v22", "v23", "v24", "v25", "v26", "v27", "v28", "v29", "v30", "v31", "v32", "v33", "v34", "v35", "v36", "v37", "v38", "v39", "v40", "v41", "v42", "v43", "v44", "v45", "v46", "v47", "v48", "v49", "v50", "v51", "v52", "v53", "v54", "v55", "v56", "v57", "v58", "v59", "v60", "v61", "v62", "v63", "v64", "v65", "v66", "v67", "v68", "v69", "v70", "v71", "v72", "v73", "v74", "v75", "v76", "v77", "v78", "v79", "v80", "v81", "v82", "v83", "v84", "v85", "v86", "v87", "v88", "v89", "v90", "v91", "v92", "v93", "v94", "v95", "v96", "v97", "v98", "v99", "v100");
    auto mkptr = [](unsigned lo, unsigned hi) LAMBDA_INL { return (char*)(((u64)(unsigned)UNI(hi) << 32) | (unsigned)UNI(lo)); };
    const char* xb = mkptr(ka[0], ka[1]);
    char* ws = mkptr(ka4[0], ka4[1]);
    bid = UNI(bid);
    int lane = tid & 63;
    const int wave = __builtin_amdgcn_readfirstlane(tid >> 6);
    const int tq = bid >> 6, ts = (bid >> 3) & 7, strip = ts == 7 ? tq + 4 * (bid & 1) : ts;
    const int t12 = tq < 2 || ts == 7, hs = tq == 3 && ts != 7, redge = tq == 1 && ts != 7 && (bid & 1), cedge = strip == 6;
    const AxisMap rm = {UNI(ax[0]), UNI(ax[1]), bid & 4 ? 218 : 1, redge ? 11 : 99};
    const AxisMap cm = {UNI(ax[2]), UNI(ax[3]), bid & 2 ? 218 : 1, cedge ? 15 : 99};
    const unsigned vf = LF + 4 * (lane & 3);
    const u32x4 fl = {vf, lane < 2 ? LF + 4 * (wave & 1) + 8 * lane : OOB, lane < 1 ? LF + 4 * wave : OOB, 0};
    const int i16 = lane & 15, g = lane >> 4;
    const int e = (i16 + 4) >> 3 & 1, qj = i16 - 4 * ((i16 + 4) >> 3);

    if (strip == 7) {
        asm volatile("s_nop 7\n"
    "s_cmp_lg_u32 s46, 6\n"
    "s_cbranch_scc1 .Ls_end\n"
    "v_mov_b32 v1, 0\n"
    "global_load_dwordx2 v[12:13], v1, s[40:41] sc1\n"
    "v_and_b32 v2, 15, v0\n"
    "v_lshlrev_b32 v2, 4, v2\n"
    "s_waitcnt vmcnt(0)\n"
    "v_add_u32 v6, 0x7f4a7c15, v12\n"
    "v_add_u32 v7, 0x9e3779b9, v13\n"
    "v_add_u32 v4, 0x7f4a7c15, v12\n"
    "v_add_u32 v5, 0x9e3779b9, v13\n"
    "v_add_u32 v3, s48, v2\n"
    "global_store_dwordx4 v3, v[4:7], s[40:41] sc1\n"
    "s_cmp_eq_u32 s49, 0\n"
    "s_cbranch_scc1 .Ls_end\n"
    "v_and_b32 v2, 0x70, v2\n"
    "v_lshlrev_b32 v3, 10, v0\n"
    "v_add_u32 v3, 393216, v3\n"
    "s_mov_b32 s52, 0x10000\n"
    "s_sleep 110\n"
    ".Ls_p:\n"
    "s_sleep 8\n"
    "global_load_dwordx2 v[8:9], v3, s[40:41] offset:0 sc1\n"
    "global_load_dwordx2 v[10:11], v3, s[40:41] offset:128 sc1\n"
    "global_load_dwordx2 v[12:13], v3, s[40:41] offset:256 sc1\n"
    "global_load_dwordx2 v[14:15], v3, s[40:41] offset:384 sc1\n"
    "global_load_dwordx2 v[16:17], v3, s[40:41] offset:512 sc1\n"
    "global_load_dwordx2 v[18:19], v3, s[40:41] offset:640 sc1\n"
    "global_load_dwordx2 v[20:21], v3, s[40:41] offset:768 sc1\n"
    "global_load_dwordx2 v[22:23], v3, s[40:41] offset:896 sc1\n"
    "s_mov_b64 s[46:47], 0\n"
    "s_waitcnt vmcnt(0)\n"
    "v_cmp_ne_u64 vcc, v[4:5], v[8:9]\n"
    "s_or_b64 s[46:47], s[46:47], vcc\n"
    "v_cmp_ne_u64 vcc, v[4:5], v[10:11]\n"
    "s_or_b64 s[46:47], s[46:47], vcc\n"
    "v_cmp_ne_u64 vcc, v[4:5], v[12:13]\n"
    "s_or_b64 s[46:47], s[46:47], vcc\n"
    "v_cmp_ne_u64 vcc, v[4:5], v[14:15]\n"
    "s_or_b64 s[46:47], s[46:47], vcc\n"
    "v_cmp_ne_u64 vcc, v[4:5], v[16:17]\n"
    "s_or_b64 s[46:47], s[46:47], vcc\n"
    "v_cmp_ne_u64 vcc, v[4:5], v[18:19]\n"
    "s_or_b64 s[46:47], s[46:47], vcc\n"
    "v_cmp_ne_u64 vcc, v[4:5], v[20:21]\n"
    "s_or_b64 s[46:47], s[46:47], vcc\n"
    "v_cmp_ne_u64 vcc, v[4:5], v[22:23]\n"
    "s_or_b64 s[46:47], s[46:47], vcc\n"
    "s_cbranch_scc0 .Ls_c\n"
    "s_sub_u32 s52, s52, 1\n"
    "s_cmp_lg_u32 s52, 0\n"
    "s_cbranch_scc1 .Ls_p\n"
    ".Ls_c:\n"
    "global_store_dwordx4 v2, v[4:7], s[40:41] sc1\n"
    ".Ls_end:\n"
    "s_endpgm\n"
    ".Ls_last:\n"
            :: "{v0}"(lane), "{s[40:41]}"(ws), "{s46}"(wave), "{s48}"(UNI(WS_ACK + 2 * bid * 128)), "{s49}"(UNI(bid == 253))
            : "memory", "v23", "s47", "s52", "vcc", "scc");
    } else if (wave < 4) {
        const int h = wave & 1;
        const unsigned l16 = lane * 16;
        const int nf = min(lane, 35) + 36 * h;
        const unsigned z0 = LQ + (lane & 3) * 256 + (4 * ((2 * (g >> 1)) ^ (lane & 3)) + ((lane >> 2) & 3)) * 16 + (g & 1) * 8;
        const u32x2 vz = {z0, z0 ^ 64};
        const unsigned vi = LF + 320 + 4 * ((i16 + 8) & 15);
        const unsigned c0 = axis_at(cm, 3 + qj) * PIXB + 16 * g, c1 = axis_at(cm, 11 + qj) * PIXB + 16 * g;
        const u32x2 dd = {(unsigned)((axis_at(rm, 11 + e) - axis_at(rm, 9 + e)) * IROWB), (unsigned)((axis_at(rm, 13 + e) - axis_at(rm, 11 + e)) * IROWB)};
        const unsigned d56 = 2 * rm.d * IROWB + (hs ? c1 - c0 : 0);
        const u32x4 srd = {(unsigned)UNI(ka[6]), (unsigned)UNI(ka[7]) & 0xffffu, (unsigned)(IMW * IROWB), 0x00020000u};
        if (wave >= 2) {
            const unsigned b = pixad(3 + qj) + e * RP + 16 * g;
            const u32x2 va = {b, b + 3200};
            const u32x2 vb = {(unsigned)(e ? -9 * RP : 5 * RP), LQ + l16 + h * 3072};
            const u32x2 lf = {l16, (unsigned)(WS_FL + ((bid & (NREP - 1)) * 144 + nf) * 128)};
            constexpr int N0 = 8, UE = (N0 + 1) / 2, UO = N0 / 2;
            u32x4 to;
            if (h != 0) {
                to = (u32x4){axis_at(rm, 3 + 2 * UE + e) * IROWB + c0, axis_at(rm, 3 + 2 * UO + e) * IROWB + c1, dd[0], dd[1]};
            } else {
                const AttC c = att_const(lane, hs, cedge);
                to = (u32x4){c.sb, c.v0, c.v1, c.pk};
            }
            asm volatile("s_nop 7\n"
    "s_mov_b32 s52, 0x40000\n"
    "s_load_dwordx2 s[78:79], s[40:41], 0x0\n"
    "s_mul_i32 s53, s50, 36864\n"
    "s_add_u32 s57, s53, 11264\n"
    "s_add_u32 s64, s40, s57\n"
    "s_addc_u32 s65, s41, 0\n"
    "s_add_u32 s57, s53, 17408\n"
    "s_add_u32 s66, s40, s57\n"
    "s_addc_u32 s67, s41, 0\n"
    "s_add_u32 s57, s53, 23552\n"
    "s_add_u32 s68, s40, s57\n"
    "s_addc_u32 s69, s41, 0\n"
    "s_add_u32 s57, s53, 29696\n"
    "s_add_u32 s70, s40, s57\n"
    "s_addc_u32 s71, s41, 0\n"
    "s_add_u32 s57, s53, 35840\n"
    "s_add_u32 s72, s40, s57\n"
    "s_addc_u32 s73, s41, 0\n"
    "s_add_u32 s57, s53, 41984\n"
    "s_add_u32 s74, s40, s57\n"
    "s_addc_u32 s75, s41, 0\n"
    "v_add_u32 v4, 71168, v2\n"
    "v_add_u32 v5, 71168, v3\n"
    "s_lshl_b32 s54, s50, 10\n"
    "s_add_u32 s54, s54, 1024\n"
    "s_mul_i32 s56, s50, 7776\n"
    "s_cmp_eq_u32 s50, 0\n"
    "s_cbranch_scc1 .Lg_x0\n"
    "v_mov_b32 v233, v234\n"
    ".Lg_x0:\n"
    "s_max_u32 s55, s56, 7168\n"
    "v_add_u32 v254, -8, v226\n"
    ".Lg_dl:\n"
    "s_sleep 1\n"
    "s_memtime s[76:77]\n"
    "s_waitcnt lgkmcnt(0)\n"
    "s_sub_u32 s76, s76, s59\n"
    "s_cmp_lt_i32 s76, 0\n"
    "s_cbranch_scc1 .Lg_dl\n"
    "s_waitcnt lgkmcnt(0)\n"
    "s_add_u32 s78, s78, 0x7f4a7c15\n"
    "s_add_u32 s79, s79, 0x9e3779b9\n"
    "v_mov_b64 v[240:241], s[78:79]\n"
    "s_mov_b32 s51, 0x4000\n"
    ".Lg_retry:\n"
    "v_mov_b64 v[244:245], v[240:241]\n"
    "v_mov_b64 v[246:247], 0\n"
    "v_mov_b64 v[242:243], 0\n"
    "s_mov_b32 exec_hi, 15\n"
    "global_load_dwordx4 v[244:247], v236, s[40:41] sc1\n"
    "s_mov_b64 exec, -1\n"
    "ds_read_b32 v228, v224 offset:48\n"
    "s_waitcnt lgkmcnt(0)\n"
    "v_cmp_gt_u32 vcc, 1, v228\n"
    "s_cbranch_vccnz .Lg1\n"
    ".Lg1b:\n"
    "global_load_dwordx4 v[8:11], v235, s[64:65] offset:-3072 sc1\n"
    "global_load_dwordx4 v[32:35], v235, s[66:67] offset:-3072 sc1\n"
    "global_load_dwordx4 v[12:15], v235, s[64:65] offset:-2048 sc1\n"
    "global_load_dwordx4 v[36:39], v235, s[66:67] offset:-2048 sc1\n"
    "global_load_dwordx4 v[16:19], v235, s[64:65] offset:-1024 sc1\n"
    "global_load_dwordx4 v[40:43], v235, s[66:67] offset:-1024 sc1\n"
    "global_load_dwordx4 v[20:23], v235, s[64:65] offset:0 sc1\n"
    "global_load_dwordx4 v[44:47], v235, s[66:67] offset:0 sc1\n"
    "global_load_dwordx4 v[24:27], v235, s[64:65] offset:1024 sc1\n"
    "global_load_dwordx4 v[48:51], v235, s[66:67] offset:1024 sc1\n"
    "global_load_dwordx4 v[28:31], v235, s[64:65] offset:2048 sc1\n"
    "global_load_dwordx4 v[52:55], v235, s[66:67] offset:2048 sc1\n"
    "global_load_dwordx4 v[56:59], v235, s[68:69] offset:-3072 sc1\n"
    "global_load_dwordx4 v[80:83], v235, s[70:71] offset:-3072 sc1\n"
    "global_load_dwordx4 v[60:63], v235, s[68:69] offset:-2048 sc1\n"
    "global_load_dwordx4 v[84:87], v235, s[70:71] offset:-2048 sc1\n"
    "global_load_dwordx4 v[64:67], v235, s[68:69] offset:-1024 sc1\n"
    "global_load_dwordx4 v[88:91], v235, s[70:71] offset:-1024 sc1\n"
    "global_load_dwordx4 v[68:71], v235, s[68:69] offset:0 sc1\n"
    "global_load_dwordx4 v[92:95], v235, s[70:71] offset:0 sc1\n"
    "s_waitcnt vmcnt(20)\n"
    "v_cmp_ne_u64 vcc, v[240:241], v[244:245]\n"
    "s_cmp_gt_i32 s51, 0\n"
    "s_cbranch_scc0 .Lg_pd\n"
    "s_cbranch_vccnz .Lg_poll\n"
    ".Lg_pd:\n"
    "ds_read_b32 v228, v224 offset:0\n"
    "s_waitcnt lgkmcnt(0)\n"
    "v_cmp_gt_u32 vcc, 1, v228\n"
    "s_cbranch_vccnz .Lg2\n"
    ".Lg2b:\n"
    "ds_read_b128 v[152:155], v2 offset:26688\n"
    "ds_read_b128 v[156:159], v2 offset:26752\n"
    "ds_read_b128 v[160:163], v2 offset:26816\n"
    "ds_read_b128 v[164:167], v2 offset:26880\n"
    "ds_read_b128 v[168:171], v2 offset:26944\n"
    "ds_read_b128 v[172:175], v2 offset:27008\n"
    "s_waitcnt vmcnt(18)\n"
    "v_add3_u32 v242, v242, v8, v11\n"
    "v_sub_u32 v8, v8, v240\n"
    "v_add3_u32 v242, v242, v32, v35\n"
    "v_sub_u32 v32, v32, v240\n"
    "s_waitcnt vmcnt(16)\n"
    "v_add3_u32 v242, v242, v12, v15\n"
    "v_sub_u32 v12, v12, v240\n"
    "s_waitcnt lgkmcnt(5)\n"
    "v_mfma_f32_16x16x32_bf16 v[200:203], v[8:11], v[152:155], 0\n"
    "global_load_dwordx4 v[72:75], v235, s[68:69] offset:1024 sc1\n"
    "v_add3_u32 v242, v242, v36, v39\n"
    "v_sub_u32 v36, v36, v240\n"
    "v_mfma_f32_16x16x32_bf16 v[204:207], v[32:35], v[152:155], 0\n"
    "global_load_dwordx4 v[96:99], v235, s[70:71] offset:1024 sc1\n"
    "s_waitcnt vmcnt(16)\n"
    "v_add3_u32 v242, v242, v16, v19\n"
    "v_sub_u32 v16, v16, v240\n"
    "s_waitcnt lgkmcnt(4)\n"
    "v_mfma_f32_16x16x32_bf16 v[200:203], v[12:15], v[156:159], v[200:203]\n"
    "global_load_dwordx4 v[76:79], v235, s[68:69] offset:2048 sc1\n"
    "v_add3_u32 v242, v242, v40, v43\n"
    "v_sub_u32 v40, v40, v240\n"
    "v_mfma_f32_16x16x32_bf16 v[204:207], v[36:39], v[156:159], v[204:207]\n"
    "global_load_dwordx4 v[100:103], v235, s[70:71] offset:2048 sc1\n"
    "s_waitcnt vmcnt(16)\n"
    "v_add3_u32 v242, v242, v20, v23\n"
    "v_sub_u32 v20, v20, v240\n"
    "s_waitcnt lgkmcnt(3)\n"
    "v_mfma_f32_16x16x32_bf16 v[200:203], v[16:19], v[160:163], v[200:203]\n"
    "global_load_dwordx4 v[104:107], v235, s[72:73] offset:-3072 sc1\n"
    "v_add3_u32 v242, v242, v44, v47\n"
    "v_sub_u32 v44, v44, v240\n"
    "v_mfma_f32_16x16x32_bf16 v[204:207], v[40:43], v[160:163], v[204:207]\n"
    "global_load_dwordx4 v[128:131], v235, s[74:75] offset:-3072 sc1\n"
    "s_waitcnt vmcnt(16)\n"
    "v_add3_u32 v242, v242, v24, v27\n"
    "v_sub_u32 v24, v24, v240\n"
    "s_waitcnt lgkmcnt(2)\n"
    "v_mfma_f32_16x16x32_bf16 v[200:203], v[20:23], v[164:167], v[200:203]\n"
    "global_load_dwordx4 v[108:111], v235, s[72:73] offset:-2048 sc1\n"
    "v_add3_u32 v242, v242, v48, v51\n"
    "v_sub_u32 v48, v48, v240\n"
    "v_mfma_f32_16x16x32_bf16 v[204:207], v[44:47], v[164:167], v[204:207]\n"
    "global_load_dwordx4 v[132:135], v235, s[74:75] offset:-2048 sc1\n"
    "s_waitcnt vmcnt(16)\n"
    "v_add3_u32 v242, v242, v28, v31\n"
    "v_sub_u32 v28, v28, v240\n"
    "s_waitcnt lgkmcnt(1)\n"
    "v_mfma_f32_16x16x32_bf16 v[200:203], v[24:27], v[168:171], v[200:203]\n"
    "global_load_dwordx4 v[112:115], v235, s[72:73] offset:-1024 sc1\n"
    "v_add3_u32 v242, v242, v52, v55\n"
    "v_sub_u32 v52, v52, v240\n"
    "v_mfma_f32_16x16x32_bf16 v[204:207], v[48:51], v[168:171], v[204:207]\n"
    "global_load_dwordx4 v[136:139], v235, s[74:75] offset:-1024 sc1\n"
    "s_waitcnt lgkmcnt(0)\n"
    "v_mfma_f32_16x16x32_bf16 v[200:203], v[28:31], v[172:175], v[200:203]\n"
    "global_load_dwordx4 v[116:119], v235, s[72:73] offset:0 sc1\n"
    "v_mfma_f32_16x16x32_bf16 v[204:207], v[52:55], v[172:175], v[204:207]\n"
    "global_load_dwordx4 v[140:143], v235, s[74:75] offset:0 sc1\n"
    "s_waitcnt vmcnt(18)\n"
    "v_add3_u32 v242, v242, v56, v59\n"
    "v_sub_u32 v56, v56, v240\n"
    "v_add3_u32 v242, v242, v80, v83\n"
    "v_sub_u32 v80, v80, v240\n"
    "s_waitcnt vmcnt(16)\n"
    "v_add3_u32 v242, v242, v60, v63\n"
    "v_sub_u32 v60, v60, v240\n"
    "v_mfma_f32_16x16x32_bf16 v[208:211], v[56:59], v[152:155], 0\n"
    "global_load_dwordx4 v[120:123], v235, s[72:73] offset:1024 sc1\n"
    "v_add3_u32 v242, v242, v84, v87\n"
    "v_sub_u32 v84, v84, v240\n"
    "v_mfma_f32_16x16x32_bf16 v[212:215], v[80:83], v[152:155], 0\n"
    "global_load_dwordx4 v[144:147], v235, s[74:75] offset:1024 sc1\n"
    "s_waitcnt vmcnt(16)\n"
    "v_add3_u32 v242, v242, v64, v67\n"
    "v_sub_u32 v64, v64, v240\n"
    "v_mfma_f32_16x16x32_bf16 v[208:211], v[60:63], v[156:159], v[208:211]\n"
    "global_load_dwordx4 v[124:127], v235, s[72:73] offset:2048 sc1\n"
    "v_add3_u32 v242, v242, v88, v91\n"
    "v_sub_u32 v88, v88, v240\n"
    "v_mfma_f32_16x16x32_bf16 v[212:215], v[84:87], v[156:159], v[212:215]\n"
    "global_load_dwordx4 v[148:151], v235, s[74:75] offset:2048 sc1\n"
    "s_waitcnt vmcnt(16)\n"
    "v_add3_u32 v242, v242, v68, v71\n"
    "v_sub_u32 v68, v68, v240\n"
    "v_mfma_f32_16x16x32_bf16 v[208:211], v[64:67], v[160:163], v[208:211]\n"
    "v_mov_b32 v227, 1\n"
    "v_add3_u32 v242, v242, v92, v95\n"
    "v_sub_u32 v92, v92, v240\n"
    "v_mfma_f32_16x16x32_bf16 v[212:215], v[88:91], v[160:163], v[212:215]\n"
    "ds_write_b32 v225, v227 offset:256\n"
    "s_waitcnt vmcnt(14)\n"
    "v_add3_u32 v242, v242, v72, v75\n"
    "v_sub_u32 v72, v72, v240\n"
    "v_mfma_f32_16x16x32_bf16 v[208:211], v[68:71], v[164:167], v[208:211]\n"
    "v_cvt_pk_bf16_f32 v200, v200, v201\n"
    "v_add3_u32 v242, v242, v96, v99\n"
    "v_sub_u32 v96, v96, v240\n"
    "v_mfma_f32_16x16x32_bf16 v[212:215], v[92:95], v[164:167], v[212:215]\n"
    "v_cvt_pk_bf16_f32 v201, v202, v203\n"
    "s_waitcnt vmcnt(12)\n"
    "v_add3_u32 v242, v242, v76, v79\n"
    "v_sub_u32 v76, v76, v240\n"
    "v_mfma_f32_16x16x32_bf16 v[208:211], v[72:75], v[168:171], v[208:211]\n"
    "v_cvt_pk_bf16_f32 v202, v204, v205\n"
    "v_add3_u32 v242, v242, v100, v103\n"
    "v_sub_u32 v100, v100, v240\n"
    "v_mfma_f32_16x16x32_bf16 v[212:215], v[96:99], v[168:171], v[212:215]\n"
    "v_cvt_pk_bf16_f32 v203, v206, v207\n"
    "v_mfma_f32_16x16x32_bf16 v[208:211], v[76:79], v[172:175], v[208:211]\n"
    "ds_write_b128 v7, v[200:203] offset:0\n"
    "v_mfma_f32_16x16x32_bf16 v[212:215], v[100:103], v[172:175], v[212:215]\n"
    "s_waitcnt vmcnt(10)\n"
    "v_add3_u32 v242, v242, v104, v107\n"
    "v_sub_u32 v104, v104, v240\n"
    "v_add3_u32 v242, v242, v128, v131\n"
    "v_sub_u32 v128, v128, v240\n"
    "s_waitcnt vmcnt(8)\n"
    "v_add3_u32 v242, v242, v108, v111\n"
    "v_sub_u32 v108, v108, v240\n"
    "v_mfma_f32_16x16x32_bf16 v[216:219], v[104:107], v[152:155], 0\n"
    "ds_read_b128 v[176:179], v3 offset:26688\n"
    "v_add3_u32 v242, v242, v132, v135\n"
    "v_sub_u32 v132, v132, v240\n"
    "v_mfma_f32_16x16x32_bf16 v[220:223], v[128:131], v[152:155], 0\n"
    "ds_read_b128 v[180:183], v3 offset:26752\n"
    "s_waitcnt vmcnt(6)\n"
    "v_add3_u32 v242, v242, v112, v115\n"
    "v_sub_u32 v112, v112, v240\n"
    "v_mfma_f32_16x16x32_bf16 v[216:219], v[108:111], v[156:159], v[216:219]\n"
    "ds_read_b128 v[184:187], v3 offset:26816\n"
    "v_add3_u32 v242, v242, v136, v139\n"
    "v_sub_u32 v136, v136, v240\n"
    "v_mfma_f32_16x16x32_bf16 v[220:223], v[132:135], v[156:159], v[220:223]\n"
    "ds_read_b128 v[188:191], v3 offset:26880\n"
    "s_waitcnt vmcnt(4)\n"
    "v_add3_u32 v242, v242, v116, v119\n"
    "v_sub_u32 v116, v116, v240\n"
    "v_mfma_f32_16x16x32_bf16 v[216:219], v[112:115], v[160:163], v[216:219]\n"
    "ds_read_b128 v[192:195], v3 offset:26944\n"
    "v_add3_u32 v242, v242, v140, v143\n"
    "v_sub_u32 v140, v140, v240\n"
    "v_mfma_f32_16x16x32_bf16 v[220:223], v[136:139], v[160:163], v[220:223]\n"
    "ds_read_b128 v[196:199], v3 offset:27008\n"
    "s_waitcnt vmcnt(2)\n"
    "v_add3_u32 v242, v242, v120, v123\n"
    "v_sub_u32 v120, v120, v240\n"
    "v_mfma_f32_16x16x32_bf16 v[216:219], v[116:119], v[164:167], v[216:219]\n"
    "v_cvt_pk_bf16_f32 v208, v208, v209\n"
    "v_add3_u32 v242, v242, v144, v147\n"
    "v_sub_u32 v144, v144, v240\n"
    "v_mfma_f32_16x16x32_bf16 v[220:223], v[140:143], v[164:167], v[220:223]\n"
    "v_cvt_pk_bf16_f32 v209, v210, v211\n"
    "s_waitcnt vmcnt(0)\n"
    "v_add3_u32 v242, v242, v124, v127\n"
    "v_sub_u32 v124, v124, v240\n"
    "v_mfma_f32_16x16x32_bf16 v[216:219], v[120:123], v[168:171], v[216:219]\n"
    "v_cvt_pk_bf16_f32 v210, v212, v213\n"
    "v_add3_u32 v242, v242, v148, v151\n"
    "v_sub_u32 v148, v148, v240\n"
    "v_mfma_f32_16x16x32_bf16 v[220:223], v[144:147], v[168:171], v[220:223]\n"
    "v_cvt_pk_bf16_f32 v211, v214, v215\n"
    "v_mfma_f32_16x16x32_bf16 v[216:219], v[124:127], v[172:175], v[216:219]\n"
    "ds_write_b128 v7, v[208:211] offset:1024\n"
    "v_mfma_f32_16x16x32_bf16 v[220:223], v[148:151], v[172:175], v[220:223]\n"
    "v_cmp_ne_u64 s[76:77], v[240:241], v[244:245]\n"
    "v_sub_u32 v242, v242, v246\n"
    "v_mov_b32 v243, v242\n"
    "s_nop 1\n"
    "v_permlane32_swap_b32 v242, v243\n"
    "v_add_u32 v242, v242, v243\n"
    "v_cvt_pk_bf16_f32 v216, v216, v217\n"
    "v_cvt_pk_bf16_f32 v217, v218, v219\n"
    "v_add_u32_dpp v242, v242, v242 row_ror:8 row_mask:0xf bank_mask:0xf\n"
    "v_cvt_pk_bf16_f32 v218, v220, v221\n"
    "v_cvt_pk_bf16_f32 v219, v222, v223\n"
    "v_add_u32_dpp v242, v242, v242 row_ror:4 row_mask:0xf bank_mask:0xf\n"
    "ds_write_b128 v7, v[216:219] offset:2048\n"
    "s_nop 0\n"
    "v_add_u32_dpp v242, v242, v242 row_ror:2 row_mask:0xf bank_mask:0xf\n"
    "s_nop 1\n"
    "v_add_u32_dpp v242, v242, v242 row_ror:1 row_mask:0xf bank_mask:0xf\n"
    "v_mov_b32 v243, v242\n"
    "s_nop 1\n"
    "v_permlane16_swap_b32 v242, v243\n"
    "v_add_u32 v242, v242, v243\n"
    "v_cmp_ne_u32 vcc, 0, v242\n"
    "v_mov_b32 v227, 1\n"
    "s_cmp_lg_u64 vcc, 0\n"
    "s_cbranch_scc1 .Lg_fail\n"
    "s_cmp_lg_u64 s[76:77], 0\n"
    "s_cbranch_scc1 .Lg_fail\n"
    ".Lg_ok:\n"
    "ds_write_b32 v225, v227 offset:16\n"
    "v_mov_b32 v242, 0\n"
    "s_waitcnt lgkmcnt(3)\n"
    "v_mfma_f32_16x16x32_bf16 v[200:203], v[8:11], v[176:179], 0\n"
    "v_mfma_f32_16x16x32_bf16 v[204:207], v[32:35], v[176:179], 0\n"
    "v_mfma_f32_16x16x32_bf16 v[200:203], v[12:15], v[180:183], v[200:203]\n"
    "v_mfma_f32_16x16x32_bf16 v[204:207], v[36:39], v[180:183], v[204:207]\n"
    "v_mfma_f32_16x16x32_bf16 v[200:203], v[16:19], v[184:187], v[200:203]\n"
    "v_mfma_f32_16x16x32_bf16 v[204:207], v[40:43], v[184:187], v[204:207]\n"
    "v_mfma_f32_16x16x32_bf16 v[200:203], v[20:23], v[188:191], v[200:203]\n"
    "v_mfma_f32_16x16x32_bf16 v[204:207], v[44:47], v[188:191], v[204:207]\n"
    "v_mfma_f32_16x16x32_bf16 v[200:203], v[24:27], v[192:195], v[200:203]\n"
    "v_mfma_f32_16x16x32_bf16 v[204:207], v[48:51], v[192:195], v[204:207]\n"
    "v_mfma_f32_16x16x32_bf16 v[200:203], v[28:31], v[196:199], v[200:203]\n"
    "v_mfma_f32_16x16x32_bf16 v[204:207], v[52:55], v[196:199], v[204:207]\n"
    "v_mfma_f32_16x16x32_bf16 v[208:211], v[56:59], v[176:179], 0\n"
    "v_mfma_f32_16x16x32_bf16 v[212:215], v[80:83], v[176:179], 0\n"
    "ds_read_b32 v229, v224 offset:0\n"
    "v_mfma_f32_16x16x32_bf16 v[208:211], v[60:63], v[180:183], v[208:211]\n"
    "v_mfma_f32_16x16x32_bf16 v[212:215], v[84:87], v[180:183], v[212:215]\n"
    "v_mfma_f32_16x16x32_bf16 v[208:211], v[64:67], v[184:187], v[208:211]\n"
    "v_mfma_f32_16x16x32_bf16 v[212:215], v[88:91], v[184:187], v[212:215]\n"
    "v_cvt_pk_bf16_f32 v200, v200, v201\n"
    "v_cvt_pk_bf16_f32 v201, v202, v203\n"
    "v_mfma_f32_16x16x32_bf16 v[208:211], v[68:71], v[188:191], v[208:211]\n"
    "v_cvt_pk_bf16_f32 v202, v204, v205\n"
    "v_cvt_pk_bf16_f32 v203, v206, v207\n"
    "v_mfma_f32_16x16x32_bf16 v[212:215], v[92:95], v[188:191], v[212:215]\n"
    "ds_write_b128 v7, v[200:203] offset:6144\n"
    "v_mfma_f32_16x16x32_bf16 v[208:211], v[72:75], v[192:195], v[208:211]\n"
    "v_mfma_f32_16x16x32_bf16 v[212:215], v[96:99], v[192:195], v[212:215]\n"
    "s_waitcnt lgkmcnt(1)\n"
    "v_cmp_gt_u32 vcc, 2, v229\n"
    "v_mfma_f32_16x16x32_bf16 v[208:211], v[76:79], v[196:199], v[208:211]\n"
    "v_mfma_f32_16x16x32_bf16 v[212:215], v[100:103], v[196:199], v[212:215]\n"
    "s_cbranch_vccnz .Lg3\n"
    ".Lg3b:\n"
    "v_mfma_f32_16x16x32_bf16 v[216:219], v[104:107], v[176:179], 0\n"
    "ds_read_b128 v[152:155], v2 offset:44480\n"
    "ds_read_b128 v[156:159], v2 offset:44544\n"
    "v_mfma_f32_16x16x32_bf16 v[220:223], v[128:131], v[176:179], 0\n"
    "ds_read_b128 v[160:163], v2 offset:44608\n"
    "ds_read_b128 v[164:167], v2 offset:44672\n"
    "v_mfma_f32_16x16x32_bf16 v[216:219], v[108:111], v[180:183], v[216:219]\n"
    "ds_read_b128 v[168:171], v2 offset:44736\n"
    "ds_read_b128 v[172:175], v2 offset:44800\n"
    "v_mfma_f32_16x16x32_bf16 v[220:223], v[132:135], v[180:183], v[220:223]\n"
    "v_cvt_pk_bf16_f32 v208, v208, v209\n"
    "v_cvt_pk_bf16_f32 v209, v210, v211\n"
    "v_mfma_f32_16x16x32_bf16 v[216:219], v[112:115], v[184:187], v[216:219]\n"
    "v_cvt_pk_bf16_f32 v210, v212, v213\n"
    "v_cvt_pk_bf16_f32 v211, v214, v215\n"
    "v_mfma_f32_16x16x32_bf16 v[220:223], v[136:139], v[184:187], v[220:223]\n"
    "ds_write_b128 v7, v[208:211] offset:7168\n"
    "v_mfma_f32_16x16x32_bf16 v[216:219], v[116:119], v[188:191], v[216:219]\n"
    "v_mfma_f32_16x16x32_bf16 v[220:223], v[140:143], v[188:191], v[220:223]\n"
    "v_mfma_f32_16x16x32_bf16 v[216:219], v[120:123], v[192:195], v[216:219]\n"
    "v_mfma_f32_16x16x32_bf16 v[220:223], v[144:147], v[192:195], v[220:223]\n"
    "v_mfma_f32_16x16x32_bf16 v[216:219], v[124:127], v[196:199], v[216:219]\n"
    "v_mfma_f32_16x16x32_bf16 v[220:223], v[148:151], v[196:199], v[220:223]\n"
    "s_waitcnt lgkmcnt(1)\n"
    "v_mfma_f32_16x16x32_bf16 v[200:203], v[8:11], v[152:155], 0\n"
    "v_mov_b32 v227, 2\n"
    "v_mfma_f32_16x16x32_bf16 v[204:207], v[32:35], v[152:155], 0\n"
    "v_mfma_f32_16x16x32_bf16 v[200:203], v[12:15], v[156:159], v[200:203]\n"
    "v_mfma_f32_16x16x32_bf16 v[204:207], v[36:39], v[156:159], v[204:207]\n"
    "v_mfma_f32_16x16x32_bf16 v[200:203], v[16:19], v[160:163], v[200:203]\n"
    "v_cvt_pk_bf16_f32 v216, v216, v217\n"
    "v_cvt_pk_bf16_f32 v217, v218, v219\n"
    "v_mfma_f32_16x16x32_bf16 v[204:207], v[40:43], v[160:163], v[204:207]\n"
    "v_cvt_pk_bf16_f32 v218, v220, v221\n"
    "v_cvt_pk_bf16_f32 v219, v222, v223\n"
    "v_mfma_f32_16x16x32_bf16 v[200:203], v[20:23], v[164:167], v[200:203]\n"
    "ds_write_b128 v7, v[216:219] offset:8192\n"
    "v_mfma_f32_16x16x32_bf16 v[204:207], v[44:47], v[164:167], v[204:207]\n"
    "ds_write_b32 v225, v227 offset:16\n"
    "v_mfma_f32_16x16x32_bf16 v[200:203], v[24:27], v[168:171], v[200:203]\n"
    "v_mfma_f32_16x16x32_bf16 v[204:207], v[48:51], v[168:171], v[204:207]\n"
    "v_mfma_f32_16x16x32_bf16 v[200:203], v[28:31], v[172:175], v[200:203]\n"
    "v_mfma_f32_16x16x32_bf16 v[204:207], v[52:55], v[172:175], v[204:207]\n"
    "v_mfma_f32_16x16x32_bf16 v[208:211], v[56:59], v[152:155], 0\n"
    "v_mfma_f32_16x16x32_bf16 v[212:215], v[80:83], v[152:155], 0\n"
    "v_mfma_f32_16x16x32_bf16 v[208:211], v[60:63], v[156:159], v[208:211]\n"
    "v_mfma_f32_16x16x32_bf16 v[212:215], v[84:87], v[156:159], v[212:215]\n"
    "v_mfma_f32_16x16x32_bf16 v[208:211], v[64:67], v[160:163], v[208:211]\n"
    "v_mfma_f32_16x16x32_bf16 v[212:215], v[88:91], v[160:163], v[212:215]\n"
    "v_mfma_f32_16x16x32_bf16 v[208:211], v[68:71], v[164:167], v[208:211]\n"
    "v_cvt_pk_bf16_f32 v200, v200, v201\n"
    "v_cvt_pk_bf16_f32 v201, v202, v203\n"
    "v_mfma_f32_16x16x32_bf16 v[212:215], v[92:95], v[164:167], v[212:215]\n"
    "v_cvt_pk_bf16_f32 v202, v204, v205\n"
    "v_cvt_pk_bf16_f32 v203, v206, v207\n"
    "v_mfma_f32_16x16x32_bf16 v[208:211], v[72:75], v[168:171], v[208:211]\n"
    "ds_write_b128 v7, v[200:203] offset:12288\n"
    "v_mfma_f32_16x16x32_bf16 v[212:215], v[96:99], v[168:171], v[212:215]\n"
    "v_mfma_f32_16x16x32_bf16 v[208:211], v[76:79], v[172:175], v[208:211]\n"
    "v_mfma_f32_16x16x32_bf16 v[212:215], v[100:103], v[172:175], v[212:215]\n"
    "v_mfma_f32_16x16x32_bf16 v[216:219], v[104:107], v[152:155], 0\n"
    "ds_read_b128 v[176:179], v3 offset:44480\n"
    "ds_read_b128 v[180:183], v3 offset:44544\n"
    "v_mfma_f32_16x16x32_bf16 v[220:223], v[128:131], v[152:155], 0\n"
    "ds_read_b128 v[184:187], v3 offset:44608\n"
    "ds_read_b128 v[188:191], v3 offset:44672\n"
    "v_mfma_f32_16x16x32_bf16 v[216:219], v[108:111], v[156:159], v[216:219]\n"
    "ds_read_b128 v[192:195], v3 offset:44736\n"
    "ds_read_b128 v[196:199], v3 offset:44800\n"
    "v_mfma_f32_16x16x32_bf16 v[220:223], v[132:135], v[156:159], v[220:223]\n"
    "v_cvt_pk_bf16_f32 v208, v208, v209\n"
    "v_cvt_pk_bf16_f32 v209, v210, v211\n"
    "v_mfma_f32_16x16x32_bf16 v[216:219], v[112:115], v[160:163], v[216:219]\n"
    "v_cvt_pk_bf16_f32 v210, v212, v213\n"
    "v_cvt_pk_bf16_f32 v211, v214, v215\n"
    "v_mfma_f32_16x16x32_bf16 v[220:223], v[136:139], v[160:163], v[220:223]\n"
    "ds_write_b128 v7, v[208:211] offset:13312\n"
    "v_mfma_f32_16x16x32_bf16 v[216:219], v[116:119], v[164:167], v[216:219]\n"
    "v_mfma_f32_16x16x32_bf16 v[220:223], v[140:143], v[164:167], v[220:223]\n"
    "v_mfma_f32_16x16x32_bf16 v[216:219], v[120:123], v[168:171], v[216:219]\n"
    "v_mfma_f32_16x16x32_bf16 v[220:223], v[144:147], v[168:171], v[220:223]\n"
    "v_mfma_f32_16x16x32_bf16 v[216:219], v[124:127], v[172:175], v[216:219]\n"
    "v_mfma_f32_16x16x32_bf16 v[220:223], v[148:151], v[172:175], v[220:223]\n"
    "s_waitcnt lgkmcnt(1)\n"
    "v_mfma_f32_16x16x32_bf16 v[200:203], v[8:11], v[176:179], 0\n"
    "v_mov_b32 v227, 3\n"
    "v_mfma_f32_16x16x32_bf16 v[204:207], v[32:35], v[176:179], 0\n"
    "v_mfma_f32_16x16x32_bf16 v[200:203], v[12:15], v[180:183], v[200:203]\n"
    "v_mfma_f32_16x16x32_bf16 v[204:207], v[36:39], v[180:183], v[204:207]\n"
    "v_mfma_f32_16x16x32_bf16 v[200:203], v[16:19], v[184:187], v[200:203]\n"
    "v_cvt_pk_bf16_f32 v216, v216, v217\n"
    "v_cvt_pk_bf16_f32 v217, v218, v219\n"
    "v_mfma_f32_16x16x32_bf16 v[204:207], v[40:43], v[184:187], v[204:207]\n"
    "v_cvt_pk_bf16_f32 v218, v220, v221\n"
    "v_cvt_pk_bf16_f32 v219, v222, v223\n"
    "v_mfma_f32_16x16x32_bf16 v[200:203], v[20:23], v[188:191], v[200:203]\n"
    "ds_write_b128 v7, v[216:219] offset:14336\n"
    "v_mfma_f32_16x16x32_bf16 v[204:207], v[44:47], v[188:191], v[204:207]\n"
    "ds_write_b32 v225, v227 offset:16\n"
    "ds_read_b32 v228, v224 offset:192\n"
    "v_mfma_f32_16x16x32_bf16 v[200:203], v[24:27], v[192:195], v[200:203]\n"
    "v_mfma_f32_16x16x32_bf16 v[204:207], v[48:51], v[192:195], v[204:207]\n"
    "v_mfma_f32_16x16x32_bf16 v[200:203], v[28:31], v[196:199], v[200:203]\n"
    "v_mfma_f32_16x16x32_bf16 v[204:207], v[52:55], v[196:199], v[204:207]\n"
    "v_mfma_f32_16x16x32_bf16 v[208:211], v[56:59], v[176:179], 0\n"
    "v_mfma_f32_16x16x32_bf16 v[212:215], v[80:83], v[176:179], 0\n"
    "ds_read_b32 v229, v224 offset:0\n"
    "v_mfma_f32_16x16x32_bf16 v[208:211], v[60:63], v[180:183], v[208:211]\n"
    "v_mfma_f32_16x16x32_bf16 v[212:215], v[84:87], v[180:183], v[212:215]\n"
    "s_waitcnt lgkmcnt(1)\n"
    "v_cmp_gt_u32 vcc, 1, v228\n"
    "v_mfma_f32_16x16x32_bf16 v[208:211], v[64:67], v[184:187], v[208:211]\n"
    "v_cvt_pk_bf16_f32 v200, v200, v201\n"
    "s_cbranch_vccnz .Lg5\n"
    ".Lg5b:\n"
    "v_mfma_f32_16x16x32_bf16 v[212:215], v[88:91], v[184:187], v[212:215]\n"
    "v_cvt_pk_bf16_f32 v201, v202, v203\n"
    "v_cvt_pk_bf16_f32 v202, v204, v205\n"
    "v_mfma_f32_16x16x32_bf16 v[208:211], v[68:71], v[188:191], v[208:211]\n"
    "v_cvt_pk_bf16_f32 v203, v206, v207\n"
    "v_mfma_f32_16x16x32_bf16 v[212:215], v[92:95], v[188:191], v[212:215]\n"
    "ds_write_b128 v7, v[200:203] offset:0\n"
    "v_mfma_f32_16x16x32_bf16 v[208:211], v[72:75], v[192:195], v[208:211]\n"
    "v_mfma_f32_16x16x32_bf16 v[212:215], v[96:99], v[192:195], v[212:215]\n"
    "s_waitcnt lgkmcnt(1)\n"
    "v_cmp_gt_u32 vcc, 3, v229\n"
    "v_mfma_f32_16x16x32_bf16 v[208:211], v[76:79], v[196:199], v[208:211]\n"
    "v_mfma_f32_16x16x32_bf16 v[212:215], v[100:103], v[196:199], v[212:215]\n"
    "s_cbranch_vccnz .Lg4\n"
    ".Lg4b:\n"
    "v_mfma_f32_16x16x32_bf16 v[216:219], v[104:107], v[176:179], 0\n"
    "ds_read_b128 v[152:155], v2 offset:62272\n"
    "ds_read_b128 v[156:159], v2 offset:62336\n"
    "v_mfma_f32_16x16x32_bf16 v[220:223], v[128:131], v[176:179], 0\n"
    "ds_read_b128 v[160:163], v2 offset:62400\n"
    "ds_read_b128 v[164:167], v2 offset:62464\n"
    "v_mfma_f32_16x16x32_bf16 v[216:219], v[108:111], v[180:183], v[216:219]\n"
    "ds_read_b128 v[168:171], v2 offset:62528\n"
    "ds_read_b128 v[172:175], v2 offset:62592\n"
    "v_mfma_f32_16x16x32_bf16 v[220:223], v[132:135], v[180:183], v[220:223]\n"
    "v_cvt_pk_bf16_f32 v208, v208, v209\n"
    "v_cvt_pk_bf16_f32 v209, v210, v211\n"
    "v_mfma_f32_16x16x32_bf16 v[216:219], v[112:115], v[184:187], v[216:219]\n"
    "v_cvt_pk_bf16_f32 v210, v212, v213\n"
    "v_cvt_pk_bf16_f32 v211, v214, v215\n"
    "v_mfma_f32_16x16x32_bf16 v[220:223], v[136:139], v[184:187], v[220:223]\n"
    "ds_write_b128 v7, v[208:211] offset:1024\n"
    "v_mfma_f32_16x16x32_bf16 v[216:219], v[116:119], v[188:191], v[216:219]\n"
    "v_mfma_f32_16x16x32_bf16 v[220:223], v[140:143], v[188:191], v[220:223]\n"
    "v_mfma_f32_16x16x32_bf16 v[216:219], v[120:123], v[192:195], v[216:219]\n"
    "v_mfma_f32_16x16x32_bf16 v[220:223], v[144:147], v[192:195], v[220:223]\n"
    "v_mfma_f32_16x16x32_bf16 v[216:219], v[124:127], v[196:199], v[216:219]\n"
    "v_mfma_f32_16x16x32_bf16 v[220:223], v[148:151], v[196:199], v[220:223]\n"
    "s_waitcnt lgkmcnt(1)\n"
    "v_mfma_f32_16x16x32_bf16 v[200:203], v[8:11], v[152:155], 0\n"
    "v_mov_b32 v227, 4\n"
    "v_mfma_f32_16x16x32_bf16 v[204:207], v[32:35], v[152:155], 0\n"
    "v_mfma_f32_16x16x32_bf16 v[200:203], v[12:15], v[156:159], v[200:203]\n"
    "v_mfma_f32_16x16x32_bf16 v[204:207], v[36:39], v[156:159], v[204:207]\n"
    "v_mfma_f32_16x16x32_bf16 v[200:203], v[16:19], v[160:163], v[200:203]\n"
    "v_cvt_pk_bf16_f32 v216, v216, v217\n"
    "v_cvt_pk_bf16_f32 v217, v218, v219\n"
    "v_mfma_f32_16x16x32_bf16 v[204:207], v[40:43], v[160:163], v[204:207]\n"
    "v_cvt_pk_bf16_f32 v218, v220, v221\n"
    "v_cvt_pk_bf16_f32 v219, v222, v223\n"
    "v_mfma_f32_16x16x32_bf16 v[200:203], v[20:23], v[164:167], v[200:203]\n"
    "ds_write_b128 v7, v[216:219] offset:2048\n"
    "v_mfma_f32_16x16x32_bf16 v[204:207], v[44:47], v[164:167], v[204:207]\n"
    "ds_write_b32 v225, v227 offset:16\n"
    "ds_read_b32 v228, v224 offset:208\n"
    "v_mfma_f32_16x16x32_bf16 v[200:203], v[24:27], v[168:171], v[200:203]\n"
    "ds_read_b32 v239, v224 offset:64\n"
    "v_mfma_f32_16x16x32_bf16 v[204:207], v[48:51], v[168:171], v[204:207]\n"
    "v_mfma_f32_16x16x32_bf16 v[200:203], v[28:31], v[172:175], v[200:203]\n"
    "v_mfma_f32_16x16x32_bf16 v[204:207], v[52:55], v[172:175], v[204:207]\n"
    "v_mfma_f32_16x16x32_bf16 v[208:211], v[56:59], v[152:155], 0\n"
    "v_mfma_f32_16x16x32_bf16 v[212:215], v[80:83], v[152:155], 0\n"
    "v_mfma_f32_16x16x32_bf16 v[208:211], v[60:63], v[156:159], v[208:211]\n"
    "v_mfma_f32_16x16x32_bf16 v[212:215], v[84:87], v[156:159], v[212:215]\n"
    "s_waitcnt lgkmcnt(1)\n"
    "v_cmp_gt_u32 vcc, 1, v228\n"
    "v_mfma_f32_16x16x32_bf16 v[208:211], v[64:67], v[160:163], v[208:211]\n"
    "v_cvt_pk_bf16_f32 v200, v200, v201\n"
    "s_cbranch_vccnz .Lg6\n"
    ".Lg6b:\n"
    "v_mfma_f32_16x16x32_bf16 v[212:215], v[88:91], v[160:163], v[212:215]\n"
    "v_cvt_pk_bf16_f32 v201, v202, v203\n"
    "v_cvt_pk_bf16_f32 v202, v204, v205\n"
    "v_mfma_f32_16x16x32_bf16 v[208:211], v[68:71], v[164:167], v[208:211]\n"
    "v_cvt_pk_bf16_f32 v203, v206, v207\n"
    "v_mfma_f32_16x16x32_bf16 v[212:215], v[92:95], v[164:167], v[212:215]\n"
    "ds_write_b128 v7, v[200:203] offset:6144\n"
    "v_mfma_f32_16x16x32_bf16 v[208:211], v[72:75], v[168:171], v[208:211]\n"
    "v_mfma_f32_16x16x32_bf16 v[212:215], v[96:99], v[168:171], v[212:215]\n"
    "v_mfma_f32_16x16x32_bf16 v[208:211], v[76:79], v[172:175], v[208:211]\n"
    "v_mfma_f32_16x16x32_bf16 v[212:215], v[100:103], v[172:175], v[212:215]\n"
    "v_mfma_f32_16x16x32_bf16 v[216:219], v[104:107], v[152:155], 0\n"
    "ds_read_b128 v[176:179], v3 offset:62272\n"
    "ds_read_b128 v[180:183], v3 offset:62336\n"
    "v_mfma_f32_16x16x32_bf16 v[220:223], v[128:131], v[152:155], 0\n"
    "ds_read_b128 v[184:187], v3 offset:62400\n"
    "ds_read_b128 v[188:191], v3 offset:62464\n"
    "v_mfma_f32_16x16x32_bf16 v[216:219], v[108:111], v[156:159], v[216:219]\n"
    "ds_read_b128 v[192:195], v3 offset:62528\n"
    "ds_read_b128 v[196:199], v3 offset:62592\n"
    "v_mfma_f32_16x16x32_bf16 v[220:223], v[132:135], v[156:159], v[220:223]\n"
    "v_cvt_pk_bf16_f32 v208, v208, v209\n"
    "v_cvt_pk_bf16_f32 v209, v210, v211\n"
    "v_mfma_f32_16x16x32_bf16 v[216:219], v[112:115], v[160:163], v[216:219]\n"
    "v_cvt_pk_bf16_f32 v210, v212, v213\n"
    "v_cvt_pk_bf16_f32 v211, v214, v215\n"
    "v_mfma_f32_16x16x32_bf16 v[220:223], v[136:139], v[160:163], v[220:223]\n"
    "ds_write_b128 v7, v[208:211] offset:7168\n"
    "v_mfma_f32_16x16x32_bf16 v[216:219], v[116:119], v[164:167], v[216:219]\n"
    "s_waitcnt lgkmcnt(8)\n"
    "v_cmp_gt_u32 vcc, 1, v239\n"
    "v_mfma_f32_16x16x32_bf16 v[220:223], v[140:143], v[164:167], v[220:223]\n"
    "v_mfma_f32_16x16x32_bf16 v[216:219], v[120:123], v[168:171], v[216:219]\n"
    "s_cbranch_vccnz .Lg7\n"
    ".Lg7b:\n"
    "v_mfma_f32_16x16x32_bf16 v[220:223], v[144:147], v[168:171], v[220:223]\n"
    "v_mfma_f32_16x16x32_bf16 v[216:219], v[124:127], v[172:175], v[216:219]\n"
    "v_mfma_f32_16x16x32_bf16 v[220:223], v[148:151], v[172:175], v[220:223]\n"
    "s_waitcnt lgkmcnt(1)\n"
    "v_mfma_f32_16x16x32_bf16 v[200:203], v[8:11], v[176:179], 0\n"
    "v_mov_b32 v227, 5\n"
    "v_mfma_f32_16x16x32_bf16 v[204:207], v[32:35], v[176:179], 0\n"
    "v_mfma_f32_16x16x32_bf16 v[200:203], v[12:15], v[180:183], v[200:203]\n"
    "v_mfma_f32_16x16x32_bf16 v[204:207], v[36:39], v[180:183], v[204:207]\n"
    "v_mfma_f32_16x16x32_bf16 v[200:203], v[16:19], v[184:187], v[200:203]\n"
    "v_cvt_pk_bf16_f32 v216, v216, v217\n"
    "v_cvt_pk_bf16_f32 v217, v218, v219\n"
    "v_mfma_f32_16x16x32_bf16 v[204:207], v[40:43], v[184:187], v[204:207]\n"
    "v_cvt_pk_bf16_f32 v218, v220, v221\n"
    "v_cvt_pk_bf16_f32 v219, v222, v223\n"
    "v_mfma_f32_16x16x32_bf16 v[200:203], v[20:23], v[188:191], v[200:203]\n"
    "ds_write_b128 v7, v[216:219] offset:8192\n"
    "v_mfma_f32_16x16x32_bf16 v[204:207], v[44:47], v[188:191], v[204:207]\n"
    "ds_write_b32 v225, v227 offset:16\n"
    "ds_read_b32 v228, v224 offset:224\n"
    "v_mfma_f32_16x16x32_bf16 v[200:203], v[24:27], v[192:195], v[200:203]\n"
    "ds_read_b32 v239, v224 offset:80\n"
    "v_mfma_f32_16x16x32_bf16 v[204:207], v[48:51], v[192:195], v[204:207]\n"
    "v_mfma_f32_16x16x32_bf16 v[200:203], v[28:31], v[196:199], v[200:203]\n"
    "v_mfma_f32_16x16x32_bf16 v[204:207], v[52:55], v[196:199], v[204:207]\n"
    "v_mfma_f32_16x16x32_bf16 v[208:211], v[56:59], v[176:179], 0\n"
    "v_mfma_f32_16x16x32_bf16 v[212:215], v[80:83], v[176:179], 0\n"
    "ds_read_b32 v229, v224 offset:0\n"
    "v_mfma_f32_16x16x32_bf16 v[208:211], v[60:63], v[180:183], v[208:211]\n"
    "v_mfma_f32_16x16x32_bf16 v[212:215], v[84:87], v[180:183], v[212:215]\n"
    "s_waitcnt lgkmcnt(2)\n"
    "v_cmp_gt_u32 vcc, 1, v228\n"
    "v_mfma_f32_16x16x32_bf16 v[208:211], v[64:67], v[184:187], v[208:211]\n"
    "v_cvt_pk_bf16_f32 v200, v200, v201\n"
    "s_cbranch_vccnz .Lg9\n"
    ".Lg9b:\n"
    "v_mfma_f32_16x16x32_bf16 v[212:215], v[88:91], v[184:187], v[212:215]\n"
    "v_cvt_pk_bf16_f32 v201, v202, v203\n"
    "v_cvt_pk_bf16_f32 v202, v204, v205\n"
    "v_mfma_f32_16x16x32_bf16 v[208:211], v[68:71], v[188:191], v[208:211]\n"
    "v_cvt_pk_bf16_f32 v203, v206, v207\n"
    "v_mfma_f32_16x16x32_bf16 v[212:215], v[92:95], v[188:191], v[212:215]\n"
    "ds_write_b128 v7, v[200:203] offset:12288\n"
    "v_mfma_f32_16x16x32_bf16 v[208:211], v[72:75], v[192:195], v[208:211]\n"
    "v_mfma_f32_16x16x32_bf16 v[212:215], v[96:99], v[192:195], v[212:215]\n"
    "s_waitcnt lgkmcnt(1)\n"
    "v_cmp_gt_u32 vcc, 6, v229\n"
    "v_mfma_f32_16x16x32_bf16 v[208:211], v[76:79], v[196:199], v[208:211]\n"
    "v_mfma_f32_16x16x32_bf16 v[212:215], v[100:103], v[196:199], v[212:215]\n"
    "s_cbranch_vccnz .Lg8\n"
    ".Lg8b:\n"
    "v_mfma_f32_16x16x32_bf16 v[216:219], v[104:107], v[176:179], 0\n"
    "ds_read_b128 v[152:155], v4 offset:8896\n"
    "ds_read_b128 v[156:159], v4 offset:8960\n"
    "v_mfma_f32_16x16x32_bf16 v[220:223], v[128:131], v[176:179], 0\n"
    "ds_read_b128 v[160:163], v4 offset:9024\n"
    "ds_read_b128 v[164:167], v4 offset:9088\n"
    "v_mfma_f32_16x16x32_bf16 v[216:219], v[108:111], v[180:183], v[216:219]\n"
    "ds_read_b128 v[168:171], v4 offset:9152\n"
    "ds_read_b128 v[172:175], v4 offset:9216\n"
    "v_mfma_f32_16x16x32_bf16 v[220:223], v[132:135], v[180:183], v[220:223]\n"
    "v_cvt_pk_bf16_f32 v208, v208, v209\n"
    "v_cvt_pk_bf16_f32 v209, v210, v211\n"
    "v_mfma_f32_16x16x32_bf16 v[216:219], v[112:115], v[184:187], v[216:219]\n"
    "v_cvt_pk_bf16_f32 v210, v212, v213\n"
    "v_cvt_pk_bf16_f32 v211, v214, v215\n"
    "v_mfma_f32_16x16x32_bf16 v[220:223], v[136:139], v[184:187], v[220:223]\n"
    "ds_write_b128 v7, v[208:211] offset:13312\n"
    "v_mfma_f32_16x16x32_bf16 v[216:219], v[116:119], v[188:191], v[216:219]\n"
    "v_cmp_gt_u32 vcc, 1, v239\n"
    "v_mfma_f32_16x16x32_bf16 v[220:223], v[140:143], v[188:191], v[220:223]\n"
    "s_cbranch_vccnz .Lg10\n"
    ".Lg10b:\n"
    "v_mfma_f32_16x16x32_bf16 v[216:219], v[120:123], v[192:195], v[216:219]\n"
    "s_add_u32 s60, s42, s44\n"
    "s_addc_u32 s61, s43, 0\n"
    "v_mfma_f32_16x16x32_bf16 v[220:223], v[144:147], v[192:195], v[220:223]\n"
    "s_add_u32 m0, s54, 0\n"
    "v_mfma_f32_16x16x32_bf16 v[216:219], v[124:127], v[196:199], v[216:219]\n"
    "global_load_lds_dwordx4 v230, s[60:61]\n"
    "s_add_u32 m0, s54, 2048\n"
    "v_mfma_f32_16x16x32_bf16 v[220:223], v[148:151], v[196:199], v[220:223]\n"
    "global_load_lds_dwordx4 v231, s[60:61]\n"
    "s_add_u32 m0, s54, 4096\n"
    "s_waitcnt lgkmcnt(1)\n"
    "v_mfma_f32_16x16x32_bf16 v[200:203], v[8:11], v[152:155], 0\n"
    "v_mov_b32 v227, 6\n"
    "v_cvt_pk_bf16_f32 v216, v216, v217\n"
    "v_mfma_f32_16x16x32_bf16 v[204:207], v[32:35], v[152:155], 0\n"
    "v_cvt_pk_bf16_f32 v217, v218, v219\n"
    "v_cvt_pk_bf16_f32 v218, v220, v221\n"
    "v_mfma_f32_16x16x32_bf16 v[200:203], v[12:15], v[156:159], v[200:203]\n"
    "v_cvt_pk_bf16_f32 v219, v222, v223\n"
    "v_mfma_f32_16x16x32_bf16 v[204:207], v[36:39], v[156:159], v[204:207]\n"
    "ds_write_b128 v7, v[216:219] offset:14336\n"
    "v_mfma_f32_16x16x32_bf16 v[200:203], v[16:19], v[160:163], v[200:203]\n"
    "ds_write_b32 v225, v227 offset:16\n"
    "ds_read_b32 v228, v224 offset:240\n"
    "v_mfma_f32_16x16x32_bf16 v[204:207], v[40:43], v[160:163], v[204:207]\n"
    "ds_read_b32 v239, v224 offset:96\n"
    "global_load_lds_dwordx4 v232, s[60:61]\n"
    "v_mfma_f32_16x16x32_bf16 v[200:203], v[20:23], v[164:167], v[200:203]\n"
    "s_add_u32 m0, s55, 0\n"
    "v_mfma_f32_16x16x32_bf16 v[204:207], v[44:47], v[164:167], v[204:207]\n"
    "global_load_lds_dwordx4 v233, s[60:61]\n"
    "s_add_u32 m0, s56, 0\n"
    "v_mfma_f32_16x16x32_bf16 v[200:203], v[24:27], v[168:171], v[200:203]\n"
    "global_load_lds_dwordx4 v234, s[60:61]\n"
    "v_mfma_f32_16x16x32_bf16 v[204:207], v[48:51], v[168:171], v[204:207]\n"
    "s_add_u32 s60, s42, s45\n"
    "s_addc_u32 s61, s43, 0\n"
    "v_mfma_f32_16x16x32_bf16 v[200:203], v[28:31], v[172:175], v[200:203]\n"
    "s_add_u32 m0, s54, 8896\n"
    "v_mfma_f32_16x16x32_bf16 v[204:207], v[52:55], v[172:175], v[204:207]\n"
    "global_load_lds_dwordx4 v230, s[60:61]\n"
    "s_add_u32 m0, s54, 10944\n"
    "v_mfma_f32_16x16x32_bf16 v[208:211], v[56:59], v[152:155], 0\n"
    "s_waitcnt lgkmcnt(1)\n"
    "v_cmp_gt_u32 vcc, 1, v228\n"
    "v_mfma_f32_16x16x32_bf16 v[212:215], v[80:83], v[152:155], 0\n"
    "v_cvt_pk_bf16_f32 v200, v200, v201\n"
    "s_cbranch_vccnz .Lg11\n"
    ".Lg11b:\n"
    "v_mfma_f32_16x16x32_bf16 v[208:211], v[60:63], v[156:159], v[208:211]\n"
    "v_cvt_pk_bf16_f32 v201, v202, v203\n"
    "v_cvt_pk_bf16_f32 v202, v204, v205\n"
    "v_mfma_f32_16x16x32_bf16 v[212:215], v[84:87], v[156:159], v[212:215]\n"
    "v_cvt_pk_bf16_f32 v203, v206, v207\n"
    "v_mfma_f32_16x16x32_bf16 v[208:211], v[64:67], v[160:163], v[208:211]\n"
    "ds_write_b128 v7, v[200:203] offset:0\n"
    "v_mfma_f32_16x16x32_bf16 v[212:215], v[88:91], v[160:163], v[212:215]\n"
    "global_load_lds_dwordx4 v231, s[60:61]\n"
    "s_add_u32 m0, s54, 12992\n"
    "v_mfma_f32_16x16x32_bf16 v[208:211], v[68:71], v[164:167], v[208:211]\n"
    "global_load_lds_dwordx4 v232, s[60:61]\n"
    "s_add_u32 m0, s55, 8896\n"
    "v_mfma_f32_16x16x32_bf16 v[212:215], v[92:95], v[164:167], v[212:215]\n"
    "global_load_lds_dwordx4 v233, s[60:61]\n"
    "s_add_u32 m0, s56, 8896\n"
    "v_mfma_f32_16x16x32_bf16 v[208:211], v[72:75], v[168:171], v[208:211]\n"
    "global_load_lds_dwordx4 v234, s[60:61]\n"
    "v_mfma_f32_16x16x32_bf16 v[212:215], v[96:99], v[168:171], v[212:215]\n"
    "v_mfma_f32_16x16x32_bf16 v[208:211], v[76:79], v[172:175], v[208:211]\n"
    "v_mfma_f32_16x16x32_bf16 v[212:215], v[100:103], v[172:175], v[212:215]\n"
    "v_mfma_f32_16x16x32_bf16 v[216:219], v[104:107], v[152:155], 0\n"
    "ds_read_b128 v[176:179], v5 offset:8896\n"
    "ds_read_b128 v[180:183], v5 offset:8960\n"
    "v_mfma_f32_16x16x32_bf16 v[220:223], v[128:131], v[152:155], 0\n"
    "ds_read_b128 v[184:187], v5 offset:9024\n"
    "ds_read_b128 v[188:191], v5 offset:9088\n"
    "v_mfma_f32_16x16x32_bf16 v[216:219], v[108:111], v[156:159], v[216:219]\n"
    "ds_read_b128 v[192:195], v5 offset:9152\n"
    "ds_read_b128 v[196:199], v5 offset:9216\n"
    "v_mfma_f32_16x16x32_bf16 v[220:223], v[132:135], v[156:159], v[220:223]\n"
    "v_cvt_pk_bf16_f32 v208, v208, v209\n"
    "v_cvt_pk_bf16_f32 v209, v210, v211\n"
    "v_mfma_f32_16x16x32_bf16 v[216:219], v[112:115], v[160:163], v[216:219]\n"
    "v_cvt_pk_bf16_f32 v210, v212, v213\n"
    "v_cvt_pk_bf16_f32 v211, v214, v215\n"
    "v_mfma_f32_16x16x32_bf16 v[220:223], v[136:139], v[160:163], v[220:223]\n"
    "ds_write_b128 v7, v[208:211] offset:1024\n"
    "v_mfma_f32_16x16x32_bf16 v[216:219], v[116:119], v[164:167], v[216:219]\n"
    "s_waitcnt lgkmcnt(8)\n"
    "v_cmp_gt_u32 vcc, 1, v239\n"
    "v_mfma_f32_16x16x32_bf16 v[220:223], v[140:143], v[164:167], v[220:223]\n"
    "v_mfma_f32_16x16x32_bf16 v[216:219], v[120:123], v[168:171], v[216:219]\n"
    "s_cbranch_vccnz .Lg12\n"
    ".Lg12b:\n"
    "v_mfma_f32_16x16x32_bf16 v[220:223], v[144:147], v[168:171], v[220:223]\n"
    "v_mfma_f32_16x16x32_bf16 v[216:219], v[124:127], v[172:175], v[216:219]\n"
    "v_mfma_f32_16x16x32_bf16 v[220:223], v[148:151], v[172:175], v[220:223]\n"
    "s_waitcnt lgkmcnt(1)\n"
    "v_mfma_f32_16x16x32_bf16 v[200:203], v[8:11], v[176:179], 0\n"
    "v_mov_b32 v227, 7\n"
    "v_mfma_f32_16x16x32_bf16 v[204:207], v[32:35], v[176:179], 0\n"
    "v_mfma_f32_16x16x32_bf16 v[200:203], v[12:15], v[180:183], v[200:203]\n"
    "v_mfma_f32_16x16x32_bf16 v[204:207], v[36:39], v[180:183], v[204:207]\n"
    "v_mfma_f32_16x16x32_bf16 v[200:203], v[16:19], v[184:187], v[200:203]\n"
    "v_cvt_pk_bf16_f32 v216, v216, v217\n"
    "v_cvt_pk_bf16_f32 v217, v218, v219\n"
    "v_mfma_f32_16x16x32_bf16 v[204:207], v[40:43], v[184:187], v[204:207]\n"
    "v_cvt_pk_bf16_f32 v218, v220, v221\n"
    "v_cvt_pk_bf16_f32 v219, v222, v223\n"
    "v_mfma_f32_16x16x32_bf16 v[200:203], v[20:23], v[188:191], v[200:203]\n"
    "ds_write_b128 v7, v[216:219] offset:2048\n"
    "v_mfma_f32_16x16x32_bf16 v[204:207], v[44:47], v[188:191], v[204:207]\n"
    "ds_write_b32 v225, v227 offset:16\n"
    "ds_read_b32 v228, v224 offset:192\n"
    "v_mfma_f32_16x16x32_bf16 v[200:203], v[24:27], v[192:195], v[200:203]\n"
    "ds_read_b32 v239, v224 offset:112\n"
    "v_mfma_f32_16x16x32_bf16 v[204:207], v[48:51], v[192:195], v[204:207]\n"
    "v_mfma_f32_16x16x32_bf16 v[200:203], v[28:31], v[196:199], v[200:203]\n"
    "v_mfma_f32_16x16x32_bf16 v[204:207], v[52:55], v[196:199], v[204:207]\n"
    "v_mfma_f32_16x16x32_bf16 v[208:211], v[56:59], v[176:179], 0\n"
    "v_mfma_f32_16x16x32_bf16 v[212:215], v[80:83], v[176:179], 0\n"
    "ds_read_b32 v229, v224 offset:0\n"
    "v_mfma_f32_16x16x32_bf16 v[208:211], v[60:63], v[180:183], v[208:211]\n"
    "v_mfma_f32_16x16x32_bf16 v[212:215], v[84:87], v[180:183], v[212:215]\n"
    "s_waitcnt lgkmcnt(2)\n"
    "v_cmp_gt_u32 vcc, 2, v228\n"
    "v_mfma_f32_16x16x32_bf16 v[208:211], v[64:67], v[184:187], v[208:211]\n"
    "v_cvt_pk_bf16_f32 v200, v200, v201\n"
    "s_cbranch_vccnz .Lg14\n"
    ".Lg14b:\n"
    "v_mfma_f32_16x16x32_bf16 v[212:215], v[88:91], v[184:187], v[212:215]\n"
    "v_cvt_pk_bf16_f32 v201, v202, v203\n"
    "v_cvt_pk_bf16_f32 v202, v204, v205\n"
    "v_mfma_f32_16x16x32_bf16 v[208:211], v[68:71], v[188:191], v[208:211]\n"
    "v_cvt_pk_bf16_f32 v203, v206, v207\n"
    "v_mfma_f32_16x16x32_bf16 v[212:215], v[92:95], v[188:191], v[212:215]\n"
    "ds_write_b128 v7, v[200:203] offset:6144\n"
    "v_mfma_f32_16x16x32_bf16 v[208:211], v[72:75], v[192:195], v[208:211]\n"
    "v_mfma_f32_16x16x32_bf16 v[212:215], v[96:99], v[192:195], v[212:215]\n"
    "s_waitcnt lgkmcnt(1)\n"
    "v_cmp_gt_u32 vcc, 7, v229\n"
    "v_mfma_f32_16x16x32_bf16 v[208:211], v[76:79], v[196:199], v[208:211]\n"
    "v_mfma_f32_16x16x32_bf16 v[212:215], v[100:103], v[196:199], v[212:215]\n"
    "s_cbranch_vccnz .Lg13\n"
    ".Lg13b:\n"
    "v_mfma_f32_16x16x32_bf16 v[216:219], v[104:107], v[176:179], 0\n"
    "ds_read_b128 v[152:155], v4 offset:26688\n"
    "ds_read_b128 v[156:159], v4 offset:26752\n"
    "v_mfma_f32_16x16x32_bf16 v[220:223], v[128:131], v[176:179], 0\n"
    "ds_read_b128 v[160:163], v4 offset:26816\n"
    "ds_read_b128 v[164:167], v4 offset:26880\n"
    "v_mfma_f32_16x16x32_bf16 v[216:219], v[108:111], v[180:183], v[216:219]\n"
    "ds_read_b128 v[168:171], v4 offset:26944\n"
    "ds_read_b128 v[172:175], v4 offset:27008\n"
    "v_mfma_f32_16x16x32_bf16 v[220:223], v[132:135], v[180:183], v[220:223]\n"
    "v_cvt_pk_bf16_f32 v208, v208, v209\n"
    "v_cvt_pk_bf16_f32 v209, v210, v211\n"
    "v_mfma_f32_16x16x32_bf16 v[216:219], v[112:115], v[184:187], v[216:219]\n"
    "v_cvt_pk_bf16_f32 v210, v212, v213\n"
    "v_cvt_pk_bf16_f32 v211, v214, v215\n"
    "v_mfma_f32_16x16x32_bf16 v[220:223], v[136:139], v[184:187], v[220:223]\n"
    "ds_write_b128 v7, v[208:211] offset:7168\n"
    "v_mfma_f32_16x16x32_bf16 v[216:219], v[116:119], v[188:191], v[216:219]\n"
    "v_cmp_gt_u32 vcc, 1, v239\n"
    "v_mfma_f32_16x16x32_bf16 v[220:223], v[140:143], v[188:191], v[220:223]\n"
    "s_cbranch_vccnz .Lg15\n"
    ".Lg15b:\n"
    "v_mfma_f32_16x16x32_bf16 v[216:219], v[120:123], v[192:195], v[216:219]\n"
    "s_add_u32 s60, s42, s46\n"
    "s_addc_u32 s61, s43, 0\n"
    "v_mfma_f32_16x16x32_bf16 v[220:223], v[144:147], v[192:195], v[220:223]\n"
    "s_add_u32 m0, s54, 17792\n"
    "v_mfma_f32_16x16x32_bf16 v[216:219], v[124:127], v[196:199], v[216:219]\n"
    "global_load_lds_dwordx4 v230, s[60:61]\n"
    "s_add_u32 m0, s54, 19840\n"
    "v_mfma_f32_16x16x32_bf16 v[220:223], v[148:151], v[196:199], v[220:223]\n"
    "global_load_lds_dwordx4 v231, s[60:61]\n"
    "s_add_u32 m0, s54, 21888\n"
    "s_waitcnt lgkmcnt(1)\n"
    "global_load_lds_dwordx4 v232, s[60:61]\n"
    "s_add_u32 m0, s55, 17792\n"
    "s_nop 0\n"
    "global_load_lds_dwordx4 v233, s[60:61]\n"
    "s_add_u32 m0, s56, 17792\n"
    "s_nop 0\n"
    "global_load_lds_dwordx4 v234, s[60:61]\n"
    "s_add_u32 s60, s42, s47\n"
    "s_addc_u32 s61, s43, 0\n"
    "s_add_u32 m0, s54, 26688\n"
    "s_nop 0\n"
    "global_load_lds_dwordx4 v230, s[60:61]\n"
    "s_add_u32 m0, s54, 28736\n"
    "s_nop 0\n"
    "global_load_lds_dwordx4 v231, s[60:61]\n"
    "s_add_u32 m0, s54, 30784\n"
    "s_nop 0\n"
    "global_load_lds_dwordx4 v232, s[60:61]\n"
    "s_add_u32 m0, s55, 26688\n"
    "s_nop 0\n"
    "global_load_lds_dwordx4 v233, s[60:61]\n"
    "s_add_u32 m0, s56, 26688\n"
    "s_nop 0\n"
    "global_load_lds_dwordx4 v234, s[60:61]\n"
    "v_mfma_f32_16x16x32_bf16 v[200:203], v[8:11], v[152:155], 0\n"
    "v_cvt_pk_bf16_f32 v216, v216, v217\n"
    "v_cvt_pk_bf16_f32 v217, v218, v219\n"
    "v_mfma_f32_16x16x32_bf16 v[204:207], v[32:35], v[152:155], 0\n"
    "v_cvt_pk_bf16_f32 v218, v220, v221\n"
    "v_cvt_pk_bf16_f32 v219, v222, v223\n"
    "v_mfma_f32_16x16x32_bf16 v[200:203], v[12:15], v[156:159], v[200:203]\n"
    "ds_write_b128 v7, v[216:219] offset:8192\n"
    "v_mfma_f32_16x16x32_bf16 v[204:207], v[36:39], v[156:159], v[204:207]\n"
    "v_mov_b32 v227, 8\n"
    "v_mfma_f32_16x16x32_bf16 v[200:203], v[16:19], v[160:163], v[200:203]\n"
    "ds_write_b32 v225, v227 offset:16\n"
    "ds_read_b32 v228, v224 offset:208\n"
    "v_mfma_f32_16x16x32_bf16 v[204:207], v[40:43], v[160:163], v[204:207]\n"
    "ds_read_b32 v239, v224 offset:64\n"
    "s_waitcnt vmcnt(10)\n"
    "v_mfma_f32_16x16x32_bf16 v[200:203], v[20:23], v[164:167], v[200:203]\n"
    "v_mov_b32 v227, 8\n"
    "v_mfma_f32_16x16x32_bf16 v[204:207], v[44:47], v[164:167], v[204:207]\n"
    "ds_write_b32 v226, v227 offset:0\n"
    "v_mfma_f32_16x16x32_bf16 v[200:203], v[24:27], v[168:171], v[200:203]\n"
    "v_mfma_f32_16x16x32_bf16 v[204:207], v[48:51], v[168:171], v[204:207]\n"
    "v_mfma_f32_16x16x32_bf16 v[200:203], v[28:31], v[172:175], v[200:203]\n"
    "v_mfma_f32_16x16x32_bf16 v[204:207], v[52:55], v[172:175], v[204:207]\n"
    "v_mfma_f32_16x16x32_bf16 v[208:211], v[56:59], v[152:155], 0\n"
    "s_waitcnt lgkmcnt(2)\n"
    "v_cmp_gt_u32 vcc, 2, v228\n"
    "v_mfma_f32_16x16x32_bf16 v[212:215], v[80:83], v[152:155], 0\n"
    "v_mfma_f32_16x16x32_bf16 v[208:211], v[60:63], v[156:159], v[208:211]\n"
    "s_cbranch_vccnz .Lg16\n"
    ".Lg16b:\n"
    "v_mfma_f32_16x16x32_bf16 v[212:215], v[84:87], v[156:159], v[212:215]\n"
    "v_cvt_pk_bf16_f32 v200, v200, v201\n"
    "v_cvt_pk_bf16_f32 v201, v202, v203\n"
    "v_mfma_f32_16x16x32_bf16 v[208:211], v[64:67], v[160:163], v[208:211]\n"
    "v_cvt_pk_bf16_f32 v202, v204, v205\n"
    "v_cvt_pk_bf16_f32 v203, v206, v207\n"
    "v_mfma_f32_16x16x32_bf16 v[212:215], v[88:91], v[160:163], v[212:215]\n"
    "ds_write_b128 v7, v[200:203] offset:12288\n"
    "v_mfma_f32_16x16x32_bf16 v[208:211], v[68:71], v[164:167], v[208:211]\n"
    "v_mfma_f32_16x16x32_bf16 v[212:215], v[92:95], v[164:167], v[212:215]\n"
    "v_mfma_f32_16x16x32_bf16 v[208:211], v[72:75], v[168:171], v[208:211]\n"
    "v_mfma_f32_16x16x32_bf16 v[212:215], v[96:99], v[168:171], v[212:215]\n"
    "v_mfma_f32_16x16x32_bf16 v[208:211], v[76:79], v[172:175], v[208:211]\n"
    "v_mfma_f32_16x16x32_bf16 v[212:215], v[100:103], v[172:175], v[212:215]\n"
    "v_mfma_f32_16x16x32_bf16 v[216:219], v[104:107], v[152:155], 0\n"
    "ds_read_b128 v[176:179], v5 offset:26688\n"
    "ds_read_b128 v[180:183], v5 offset:26752\n"
    "v_mfma_f32_16x16x32_bf16 v[220:223], v[128:131], v[152:155], 0\n"
    "ds_read_b128 v[184:187], v5 offset:26816\n"
    "ds_read_b128 v[188:191], v5 offset:26880\n"
    "v_mfma_f32_16x16x32_bf16 v[216:219], v[108:111], v[156:159], v[216:219]\n"
    "ds_read_b128 v[192:195], v5 offset:26944\n"
    "ds_read_b128 v[196:199], v5 offset:27008\n"
    "v_mfma_f32_16x16x32_bf16 v[220:223], v[132:135], v[156:159], v[220:223]\n"
    "v_cvt_pk_bf16_f32 v208, v208, v209\n"
    "v_cvt_pk_bf16_f32 v209, v210, v211\n"
    "v_mfma_f32_16x16x32_bf16 v[216:219], v[112:115], v[160:163], v[216:219]\n"
    "v_cvt_pk_bf16_f32 v210, v212, v213\n"
    "v_cvt_pk_bf16_f32 v211, v214, v215\n"
    "v_mfma_f32_16x16x32_bf16 v[220:223], v[136:139], v[160:163], v[220:223]\n"
    "ds_write_b128 v7, v[208:211] offset:13312\n"
    "v_mfma_f32_16x16x32_bf16 v[216:219], v[116:119], v[164:167], v[216:219]\n"
    "s_waitcnt lgkmcnt(9)\n"
    "v_cmp_gt_u32 vcc, 2, v239\n"
    "v_mfma_f32_16x16x32_bf16 v[220:223], v[140:143], v[164:167], v[220:223]\n"
    "v_mfma_f32_16x16x32_bf16 v[216:219], v[120:123], v[168:171], v[216:219]\n"
    "s_cbranch_vccnz .Lg17\n"
    ".Lg17b:\n"
    "v_mfma_f32_16x16x32_bf16 v[220:223], v[144:147], v[168:171], v[220:223]\n"
    "v_mfma_f32_16x16x32_bf16 v[216:219], v[124:127], v[172:175], v[216:219]\n"
    "v_mfma_f32_16x16x32_bf16 v[220:223], v[148:151], v[172:175], v[220:223]\n"
    "s_waitcnt lgkmcnt(1)\n"
    "v_mfma_f32_16x16x32_bf16 v[200:203], v[8:11], v[176:179], 0\n"
    "v_mov_b32 v227, 9\n"
    "v_mfma_f32_16x16x32_bf16 v[204:207], v[32:35], v[176:179], 0\n"
    "v_mfma_f32_16x16x32_bf16 v[200:203], v[12:15], v[180:183], v[200:203]\n"
    "v_mfma_f32_16x16x32_bf16 v[204:207], v[36:39], v[180:183], v[204:207]\n"
    "v_mfma_f32_16x16x32_bf16 v[200:203], v[16:19], v[184:187], v[200:203]\n"
    "v_cvt_pk_bf16_f32 v216, v216, v217\n"
    "v_cvt_pk_bf16_f32 v217, v218, v219\n"
    "v_mfma_f32_16x16x32_bf16 v[204:207], v[40:43], v[184:187], v[204:207]\n"
    "v_cvt_pk_bf16_f32 v218, v220, v221\n"
    "v_cvt_pk_bf16_f32 v219, v222, v223\n"
    "v_mfma_f32_16x16x32_bf16 v[200:203], v[20:23], v[188:191], v[200:203]\n"
    "ds_write_b128 v7, v[216:219] offset:14336\n"
    "v_mfma_f32_16x16x32_bf16 v[204:207], v[44:47], v[188:191], v[204:207]\n"
    "ds_write_b32 v225, v227 offset:16\n"
    "ds_read_b32 v228, v224 offset:224\n"
    "v_mfma_f32_16x16x32_bf16 v[200:203], v[24:27], v[192:195], v[200:203]\n"
    "ds_read_b32 v239, v224 offset:80\n"
    "v_mfma_f32_16x16x32_bf16 v[204:207], v[48:51], v[192:195], v[204:207]\n"
    "v_mfma_f32_16x16x32_bf16 v[200:203], v[28:31], v[196:199], v[200:203]\n"
    "v_mfma_f32_16x16x32_bf16 v[204:207], v[52:55], v[196:199], v[204:207]\n"
    "v_mfma_f32_16x16x32_bf16 v[208:211], v[56:59], v[176:179], 0\n"
    "v_mfma_f32_16x16x32_bf16 v[212:215], v[80:83], v[176:179], 0\n"
    "ds_read_b32 v229, v224 offset:0\n"
    "v_mfma_f32_16x16x32_bf16 v[208:211], v[60:63], v[180:183], v[208:211]\n"
    "v_mfma_f32_16x16x32_bf16 v[212:215], v[84:87], v[180:183], v[212:215]\n"
    "s_waitcnt lgkmcnt(2)\n"
    "v_cmp_gt_u32 vcc, 2, v228\n"
    "v_mfma_f32_16x16x32_bf16 v[208:211], v[64:67], v[184:187], v[208:211]\n"
    "v_cvt_pk_bf16_f32 v200, v200, v201\n"
    "s_cbranch_vccnz .Lg19\n"
    ".Lg19b:\n"
    "v_mfma_f32_16x16x32_bf16 v[212:215], v[88:91], v[184:187], v[212:215]\n"
    "v_cvt_pk_bf16_f32 v201, v202, v203\n"
    "v_cvt_pk_bf16_f32 v202, v204, v205\n"
    "v_mfma_f32_16x16x32_bf16 v[208:211], v[68:71], v[188:191], v[208:211]\n"
    "v_cvt_pk_bf16_f32 v203, v206, v207\n"
    "v_mfma_f32_16x16x32_bf16 v[212:215], v[92:95], v[188:191], v[212:215]\n"
    "ds_write_b128 v7, v[200:203] offset:0\n"
    "v_mfma_f32_16x16x32_bf16 v[208:211], v[72:75], v[192:195], v[208:211]\n"
    "v_mfma_f32_16x16x32_bf16 v[212:215], v[96:99], v[192:195], v[212:215]\n"
    "s_waitcnt lgkmcnt(1)\n"
    "v_cmp_gt_u32 vcc, 8, v229\n"
    "v_mfma_f32_16x16x32_bf16 v[208:211], v[76:79], v[196:199], v[208:211]\n"
    "v_mfma_f32_16x16x32_bf16 v[212:215], v[100:103], v[196:199], v[212:215]\n"
    "s_cbranch_vccnz .Lg18\n"
    ".Lg18b:\n"
    "v_mfma_f32_16x16x32_bf16 v[216:219], v[104:107], v[176:179], 0\n"
    "v_add_u32 v4, v6, v4\n"
    "v_cmp_gt_u32 vcc, 2, v239\n"
    "v_mfma_f32_16x16x32_bf16 v[220:223], v[128:131], v[176:179], 0\n"
    "ds_read_b128 v[152:155], v4 offset:0\n"
    "ds_read_b128 v[156:159], v4 offset:64\n"
    "v_mfma_f32_16x16x32_bf16 v[216:219], v[108:111], v[180:183], v[216:219]\n"
    "ds_read_b128 v[160:163], v4 offset:128\n"
    "ds_read_b128 v[164:167], v4 offset:192\n"
    "v_mfma_f32_16x16x32_bf16 v[220:223], v[132:135], v[180:183], v[220:223]\n"
    "ds_read_b128 v[168:171], v4 offset:256\n"
    "ds_read_b128 v[172:175], v4 offset:320\n"
    "v_mfma_f32_16x16x32_bf16 v[216:219], v[112:115], v[184:187], v[216:219]\n"
    "v_cvt_pk_bf16_f32 v208, v208, v209\n"
    "v_cvt_pk_bf16_f32 v209, v210, v211\n"
    "v_mfma_f32_16x16x32_bf16 v[220:223], v[136:139], v[184:187], v[220:223]\n"
    "v_cvt_pk_bf16_f32 v210, v212, v213\n"
    "v_cvt_pk_bf16_f32 v211, v214, v215\n"
    "v_mfma_f32_16x16x32_bf16 v[216:219], v[116:119], v[188:191], v[216:219]\n"
    "ds_write_b128 v7, v[208:211] offset:1024\n"
    "v_mfma_f32_16x16x32_bf16 v[220:223], v[140:143], v[188:191], v[220:223]\n"
    "s_cbranch_vccnz .Lg20\n"
    ".Lg20b:\n"
    "v_mfma_f32_16x16x32_bf16 v[216:219], v[120:123], v[192:195], v[216:219]\n"
    "s_add_u32 s60, s42, s48\n"
    "s_addc_u32 s61, s43, 0\n"
    "v_mfma_f32_16x16x32_bf16 v[220:223], v[144:147], v[192:195], v[220:223]\n"
    "s_add_u32 m0, s54, 35584\n"
    "v_mfma_f32_16x16x32_bf16 v[216:219], v[124:127], v[196:199], v[216:219]\n"
    "global_load_lds_dwordx4 v230, s[60:61]\n"
    "s_add_u32 m0, s54, 37632\n"
    "v_mfma_f32_16x16x32_bf16 v[220:223], v[148:151], v[196:199], v[220:223]\n"
    "global_load_lds_dwordx4 v231, s[60:61]\n"
    "s_add_u32 m0, s54, 39680\n"
    "s_waitcnt lgkmcnt(1)\n"
    "global_load_lds_dwordx4 v232, s[60:61]\n"
    "s_add_u32 m0, s55, 35584\n"
    "s_nop 0\n"
    "global_load_lds_dwordx4 v233, s[60:61]\n"
    "s_add_u32 m0, s56, 35584\n"
    "s_nop 0\n"
    "global_load_lds_dwordx4 v234, s[60:61]\n"
    "s_add_u32 s60, s42, s49\n"
    "s_addc_u32 s61, s43, 0\n"
    "s_add_u32 m0, s54, 44480\n"
    "s_nop 0\n"
    "global_load_lds_dwordx4 v230, s[60:61]\n"
    "s_add_u32 m0, s54, 46528\n"
    "s_nop 0\n"
    "global_load_lds_dwordx4 v231, s[60:61]\n"
    "s_add_u32 m0, s54, 48576\n"
    "s_nop 0\n"
    "global_load_lds_dwordx4 v232, s[60:61]\n"
    "s_add_u32 m0, s55, 44480\n"
    "s_nop 0\n"
    "global_load_lds_dwordx4 v233, s[60:61]\n"
    "s_add_u32 m0, s56, 44480\n"
    "s_nop 0\n"
    "global_load_lds_dwordx4 v234, s[60:61]\n"
    "v_mfma_f32_16x16x32_bf16 v[200:203], v[8:11], v[152:155], 0\n"
    "v_cvt_pk_bf16_f32 v216, v216, v217\n"
    "v_cvt_pk_bf16_f32 v217, v218, v219\n"
    "v_mfma_f32_16x16x32_bf16 v[204:207], v[32:35], v[152:155], 0\n"
    "v_cvt_pk_bf16_f32 v218, v220, v221\n"
    "v_cvt_pk_bf16_f32 v219, v222, v223\n"
    "v_mfma_f32_16x16x32_bf16 v[200:203], v[12:15], v[156:159], v[200:203]\n"
    "ds_write_b128 v7, v[216:219] offset:2048\n"
    "v_mfma_f32_16x16x32_bf16 v[204:207], v[36:39], v[156:159], v[204:207]\n"
    "v_mov_b32 v227, 10\n"
    "v_mfma_f32_16x16x32_bf16 v[200:203], v[16:19], v[160:163], v[200:203]\n"
    "ds_write_b32 v225, v227 offset:16\n"
    "ds_read_b32 v228, v224 offset:240\n"
    "v_mfma_f32_16x16x32_bf16 v[204:207], v[40:43], v[160:163], v[204:207]\n"
    "s_waitcnt vmcnt(10)\n"
    "v_mov_b32 v227, 9\n"
    "v_mfma_f32_16x16x32_bf16 v[200:203], v[20:23], v[164:167], v[200:203]\n"
    "ds_write_b32 v226, v227 offset:0\n"
    "v_mfma_f32_16x16x32_bf16 v[204:207], v[44:47], v[164:167], v[204:207]\n"
    "v_mfma_f32_16x16x32_bf16 v[200:203], v[24:27], v[168:171], v[200:203]\n"
    "v_mfma_f32_16x16x32_bf16 v[204:207], v[48:51], v[168:171], v[204:207]\n"
    "v_mfma_f32_16x16x32_bf16 v[200:203], v[28:31], v[172:175], v[200:203]\n"
    "v_mfma_f32_16x16x32_bf16 v[204:207], v[52:55], v[172:175], v[204:207]\n"
    "v_mfma_f32_16x16x32_bf16 v[208:211], v[56:59], v[152:155], 0\n"
    "s_waitcnt lgkmcnt(1)\n"
    "v_cmp_gt_u32 vcc, 2, v228\n"
    "v_mfma_f32_16x16x32_bf16 v[212:215], v[80:83], v[152:155], 0\n"
    "v_mfma_f32_16x16x32_bf16 v[208:211], v[60:63], v[156:159], v[208:211]\n"
    "s_cbranch_vccnz .Lg21\n"
    ".Lg21b:\n"
    "v_mfma_f32_16x16x32_bf16 v[212:215], v[84:87], v[156:159], v[212:215]\n"
    "v_cvt_pk_bf16_f32 v200, v200, v201\n"
    "v_cvt_pk_bf16_f32 v201, v202, v203\n"
    "v_mfma_f32_16x16x32_bf16 v[208:211], v[64:67], v[160:163], v[208:211]\n"
    "v_cvt_pk_bf16_f32 v202, v204, v205\n"
    "v_cvt_pk_bf16_f32 v203, v206, v207\n"
    "v_mfma_f32_16x16x32_bf16 v[212:215], v[88:91], v[160:163], v[212:215]\n"
    "ds_write_b128 v7, v[200:203] offset:6144\n"
    "v_mfma_f32_16x16x32_bf16 v[208:211], v[68:71], v[164:167], v[208:211]\n"
    "v_mfma_f32_16x16x32_bf16 v[212:215], v[92:95], v[164:167], v[212:215]\n"
    "v_mfma_f32_16x16x32_bf16 v[208:211], v[72:75], v[168:171], v[208:211]\n"
    "v_mfma_f32_16x16x32_bf16 v[212:215], v[96:99], v[168:171], v[212:215]\n"
    "v_mfma_f32_16x16x32_bf16 v[208:211], v[76:79], v[172:175], v[208:211]\n"
    "v_mfma_f32_16x16x32_bf16 v[212:215], v[100:103], v[172:175], v[212:215]\n"
    "v_mfma_f32_16x16x32_bf16 v[216:219], v[104:107], v[152:155], 0\n"
    "v_add_u32 v5, v6, v5\n"
    "v_mfma_f32_16x16x32_bf16 v[220:223], v[128:131], v[152:155], 0\n"
    "ds_read_b128 v[176:179], v5 offset:0\n"
    "ds_read_b128 v[180:183], v5 offset:64\n"
    "v_mfma_f32_16x16x32_bf16 v[216:219], v[108:111], v[156:159], v[216:219]\n"
    "ds_read_b128 v[184:187], v5 offset:128\n"
    "ds_read_b128 v[188:191], v5 offset:192\n"
    "v_mfma_f32_16x16x32_bf16 v[220:223], v[132:135], v[156:159], v[220:223]\n"
    "ds_read_b128 v[192:195], v5 offset:256\n"
    "ds_read_b128 v[196:199], v5 offset:320\n"
    "v_mfma_f32_16x16x32_bf16 v[216:219], v[112:115], v[160:163], v[216:219]\n"
    "v_cvt_pk_bf16_f32 v208, v208, v209\n"
    "v_cvt_pk_bf16_f32 v209, v210, v211\n"
    "v_mfma_f32_16x16x32_bf16 v[220:223], v[136:139], v[160:163], v[220:223]\n"
    "v_cvt_pk_bf16_f32 v210, v212, v213\n"
    "v_cvt_pk_bf16_f32 v211, v214, v215\n"
    "v_mfma_f32_16x16x32_bf16 v[216:219], v[116:119], v[164:167], v[216:219]\n"
    "ds_write_b128 v7, v[208:211] offset:7168\n"
    "v_mfma_f32_16x16x32_bf16 v[220:223], v[140:143], v[164:167], v[220:223]\n"
    "v_mfma_f32_16x16x32_bf16 v[216:219], v[120:123], v[168:171], v[216:219]\n"
    "v_mfma_f32_16x16x32_bf16 v[220:223], v[144:147], v[168:171], v[220:223]\n"
    "v_mfma_f32_16x16x32_bf16 v[216:219], v[124:127], v[172:175], v[216:219]\n"
    "v_mfma_f32_16x16x32_bf16 v[220:223], v[148:151], v[172:175], v[220:223]\n"
    "s_waitcnt lgkmcnt(1)\n"
    "s_lshl1_add_u32 s53, s58, s50\n"
    "s_cmp_eq_u32 s53, 2\n"
    "s_cbranch_scc1 .Lg_e12a\n"
    "s_cmp_eq_u32 s53, 3\n"
    "s_cbranch_scc1 .Lg_e12b\n"
    "v_mfma_f32_16x16x32_bf16 v[200:203], v[8:11], v[176:179], 0\n"
    "v_cvt_pk_bf16_f32 v216, v216, v217\n"
    "v_cvt_pk_bf16_f32 v217, v218, v219\n"
    "v_mfma_f32_16x16x32_bf16 v[204:207], v[32:35], v[176:179], 0\n"
    "v_cvt_pk_bf16_f32 v218, v220, v221\n"
    "v_cvt_pk_bf16_f32 v219, v222, v223\n"
    "v_mfma_f32_16x16x32_bf16 v[200:203], v[12:15], v[180:183], v[200:203]\n"
    "ds_write_b128 v7, v[216:219] offset:8192\n"
    "v_mfma_f32_16x16x32_bf16 v[204:207], v[36:39], v[180:183], v[204:207]\n"
    "v_mov_b32 v227, 11\n"
    "v_mfma_f32_16x16x32_bf16 v[200:203], v[16:19], v[184:187], v[200:203]\n"
    "ds_write_b32 v225, v227 offset:16\n"
    "ds_read_b32 v228, v224 offset:192\n"
    "v_mfma_f32_16x16x32_bf16 v[204:207], v[40:43], v[184:187], v[204:207]\n"
    "v_mfma_f32_16x16x32_bf16 v[200:203], v[20:23], v[188:191], v[200:203]\n"
    "v_mfma_f32_16x16x32_bf16 v[204:207], v[44:47], v[188:191], v[204:207]\n"
    "v_mfma_f32_16x16x32_bf16 v[200:203], v[24:27], v[192:195], v[200:203]\n"
    "v_mfma_f32_16x16x32_bf16 v[204:207], v[48:51], v[192:195], v[204:207]\n"
    "v_mfma_f32_16x16x32_bf16 v[200:203], v[28:31], v[196:199], v[200:203]\n"
    "v_mfma_f32_16x16x32_bf16 v[204:207], v[52:55], v[196:199], v[204:207]\n"
    "v_mfma_f32_16x16x32_bf16 v[208:211], v[56:59], v[176:179], 0\n"
    "s_waitcnt lgkmcnt(0)\n"
    "v_cmp_gt_u32 vcc, 3, v228\n"
    "v_mfma_f32_16x16x32_bf16 v[212:215], v[80:83], v[176:179], 0\n"
    "ds_read_b32 v229, v224 offset:0\n"
    "s_cbranch_vccnz .Lg26\n"
    ".Lg26b:\n"
    "v_mfma_f32_16x16x32_bf16 v[208:211], v[60:63], v[180:183], v[208:211]\n"
    "v_cvt_pk_bf16_f32 v200, v200, v201\n"
    "v_cvt_pk_bf16_f32 v201, v202, v203\n"
    "v_mfma_f32_16x16x32_bf16 v[212:215], v[84:87], v[180:183], v[212:215]\n"
    "v_cvt_pk_bf16_f32 v202, v204, v205\n"
    "v_cvt_pk_bf16_f32 v203, v206, v207\n"
    "v_mfma_f32_16x16x32_bf16 v[208:211], v[64:67], v[184:187], v[208:211]\n"
    "ds_write_b128 v7, v[200:203] offset:12288\n"
    "v_mfma_f32_16x16x32_bf16 v[212:215], v[88:91], v[184:187], v[212:215]\n"
    "v_mfma_f32_16x16x32_bf16 v[208:211], v[68:71], v[188:191], v[208:211]\n"
    "v_mfma_f32_16x16x32_bf16 v[212:215], v[92:95], v[188:191], v[212:215]\n"
    "v_mfma_f32_16x16x32_bf16 v[208:211], v[72:75], v[192:195], v[208:211]\n"
    "v_mfma_f32_16x16x32_bf16 v[212:215], v[96:99], v[192:195], v[212:215]\n"
    "s_waitcnt lgkmcnt(1)\n"
    "v_cmp_gt_u32 vcc, 9, v229\n"
    "v_mfma_f32_16x16x32_bf16 v[208:211], v[76:79], v[196:199], v[208:211]\n"
    "v_mfma_f32_16x16x32_bf16 v[212:215], v[100:103], v[196:199], v[212:215]\n"
    "s_cbranch_vccnz .Lg25\n"
    ".Lg25b:\n"
    "v_mfma_f32_16x16x32_bf16 v[216:219], v[104:107], v[176:179], 0\n"
    "v_add_u32 v2, s84, v2\n"
    "v_mfma_f32_16x16x32_bf16 v[220:223], v[128:131], v[176:179], 0\n"
    "ds_read_b128 v[152:155], v2 offset:8896\n"
    "ds_read_b128 v[156:159], v2 offset:8960\n"
    "v_mfma_f32_16x16x32_bf16 v[216:219], v[108:111], v[180:183], v[216:219]\n"
    "ds_read_b128 v[160:163], v2 offset:9024\n"
    "ds_read_b128 v[164:167], v2 offset:9088\n"
    "v_mfma_f32_16x16x32_bf16 v[220:223], v[132:135], v[180:183], v[220:223]\n"
    "ds_read_b128 v[168:171], v2 offset:9152\n"
    "ds_read_b128 v[172:175], v2 offset:9216\n"
    "v_mfma_f32_16x16x32_bf16 v[216:219], v[112:115], v[184:187], v[216:219]\n"
    "v_cvt_pk_bf16_f32 v208, v208, v209\n"
    "v_cvt_pk_bf16_f32 v209, v210, v211\n"
    "v_mfma_f32_16x16x32_bf16 v[220:223], v[136:139], v[184:187], v[220:223]\n"
    "v_cvt_pk_bf16_f32 v210, v212, v213\n"
    "v_cvt_pk_bf16_f32 v211, v214, v215\n"
    "v_mfma_f32_16x16x32_bf16 v[216:219], v[116:119], v[188:191], v[216:219]\n"
    "ds_write_b128 v7, v[208:211] offset:13312\n"
    "v_mfma_f32_16x16x32_bf16 v[220:223], v[140:143], v[188:191], v[220:223]\n"
    "v_mfma_f32_16x16x32_bf16 v[216:219], v[120:123], v[192:195], v[216:219]\n"
    "v_mfma_f32_16x16x32_bf16 v[220:223], v[144:147], v[192:195], v[220:223]\n"
    "v_mfma_f32_16x16x32_bf16 v[216:219], v[124:127], v[196:199], v[216:219]\n"
    "v_mfma_f32_16x16x32_bf16 v[220:223], v[148:151], v[196:199], v[220:223]\n"
    "s_waitcnt lgkmcnt(1)\n"
    "s_cmp_eq_u32 s50, 0\n"
    "s_cbranch_scc1 .Lg_e13a\n"
    "v_mfma_f32_16x16x32_bf16 v[200:203], v[8:11], v[152:155], 0\n"
    "v_mov_b32 v227, 12\n"
    "v_mfma_f32_16x16x32_bf16 v[204:207], v[32:35], v[152:155], 0\n"
    "v_mfma_f32_16x16x32_bf16 v[200:203], v[12:15], v[156:159], v[200:203]\n"
    "v_cvt_pk_bf16_f32 v216, v216, v217\n"
    "v_cvt_pk_bf16_f32 v217, v218, v219\n"
    "v_mfma_f32_16x16x32_bf16 v[204:207], v[36:39], v[156:159], v[204:207]\n"
    "v_cvt_pk_bf16_f32 v218, v220, v221\n"
    "v_cvt_pk_bf16_f32 v219, v222, v223\n"
    "v_mfma_f32_16x16x32_bf16 v[200:203], v[16:19], v[160:163], v[200:203]\n"
    "ds_write_b128 v7, v[216:219] offset:14336\n"
    "v_mfma_f32_16x16x32_bf16 v[204:207], v[40:43], v[160:163], v[204:207]\n"
    "ds_write_b32 v225, v227 offset:16\n"
    "ds_read_b32 v228, v224 offset:208\n"
    "v_mfma_f32_16x16x32_bf16 v[200:203], v[20:23], v[164:167], v[200:203]\n"
    "s_waitcnt vmcnt(0)\n"
    "v_mov_b32 v227, 10\n"
    "v_mfma_f32_16x16x32_bf16 v[204:207], v[44:47], v[164:167], v[204:207]\n"
    "ds_write_b32 v226, v227 offset:0\n"
    "v_mfma_f32_16x16x32_bf16 v[200:203], v[24:27], v[168:171], v[200:203]\n"
    "v_mfma_f32_16x16x32_bf16 v[204:207], v[48:51], v[168:171], v[204:207]\n"
    "v_mfma_f32_16x16x32_bf16 v[200:203], v[28:31], v[172:175], v[200:203]\n"
    "v_mfma_f32_16x16x32_bf16 v[204:207], v[52:55], v[172:175], v[204:207]\n"
    "ds_read_b32 v239, v224 offset:272\n"
    "s_waitcnt lgkmcnt(0)\n"
    "v_cmp_gt_u32 vcc, 1, v239\n"
    "s_cbranch_vccnz .Lg29\n"
    ".Lg29b:\n"
    "v_mfma_f32_16x16x32_bf16 v[208:211], v[56:59], v[152:155], 0\n"
    "v_cmp_gt_u32 vcc, 3, v228\n"
    "v_mfma_f32_16x16x32_bf16 v[212:215], v[80:83], v[152:155], 0\n"
    "s_cbranch_vccnz .Lg28\n"
    ".Lg28b:\n"
    "v_cvt_pk_bf16_f32 v200, v200, v201\n"
    "v_mfma_f32_16x16x32_bf16 v[208:211], v[60:63], v[156:159], v[208:211]\n"
    "v_cvt_pk_bf16_f32 v201, v202, v203\n"
    "v_cvt_pk_bf16_f32 v202, v204, v205\n"
    "v_mfma_f32_16x16x32_bf16 v[212:215], v[84:87], v[156:159], v[212:215]\n"
    "v_cvt_pk_bf16_f32 v203, v206, v207\n"
    "v_mfma_f32_16x16x32_bf16 v[208:211], v[64:67], v[160:163], v[208:211]\n"
    "ds_write_b128 v7, v[200:203] offset:0\n"
    "v_mfma_f32_16x16x32_bf16 v[212:215], v[88:91], v[160:163], v[212:215]\n"
    "s_add_u32 s60, s40, 109568\n"
    "s_addc_u32 s61, s41, 0\n"
    "v_mfma_f32_16x16x32_bf16 v[208:211], v[68:71], v[164:167], v[208:211]\n"
    "s_add_u32 s66, s60, 6144\n"
    "s_addc_u32 s67, s61, 0\n"
    "v_mfma_f32_16x16x32_bf16 v[212:215], v[92:95], v[164:167], v[212:215]\n"
    "global_load_dwordx4 v[8:11], v235, s[60:61] offset:-3072 sc1\n"
    "global_load_dwordx4 v[32:35], v235, s[66:67] offset:-3072 sc1\n"
    "v_mfma_f32_16x16x32_bf16 v[208:211], v[72:75], v[168:171], v[208:211]\n"
    "global_load_dwordx4 v[12:15], v235, s[60:61] offset:-2048 sc1\n"
    "global_load_dwordx4 v[36:39], v235, s[66:67] offset:-2048 sc1\n"
    "v_mfma_f32_16x16x32_bf16 v[212:215], v[96:99], v[168:171], v[212:215]\n"
    "global_load_dwordx4 v[16:19], v235, s[60:61] offset:-1024 sc1\n"
    "global_load_dwordx4 v[40:43], v235, s[66:67] offset:-1024 sc1\n"
    "v_mfma_f32_16x16x32_bf16 v[208:211], v[76:79], v[172:175], v[208:211]\n"
    "global_load_dwordx4 v[20:23], v235, s[60:61] offset:0 sc1\n"
    "global_load_dwordx4 v[44:47], v235, s[66:67] offset:0 sc1\n"
    "v_mfma_f32_16x16x32_bf16 v[212:215], v[100:103], v[172:175], v[212:215]\n"
    "global_load_dwordx4 v[24:27], v235, s[60:61] offset:1024 sc1\n"
    "global_load_dwordx4 v[48:51], v235, s[66:67] offset:1024 sc1\n"
    "v_mfma_f32_16x16x32_bf16 v[216:219], v[104:107], v[152:155], 0\n"
    "global_load_dwordx4 v[28:31], v235, s[60:61] offset:2048 sc1\n"
    "global_load_dwordx4 v[52:55], v235, s[66:67] offset:2048 sc1\n"
    "v_mfma_f32_16x16x32_bf16 v[220:223], v[128:131], v[152:155], 0\n"
    "v_cvt_pk_bf16_f32 v208, v208, v209\n"
    "v_cvt_pk_bf16_f32 v209, v210, v211\n"
    "v_mfma_f32_16x16x32_bf16 v[216:219], v[108:111], v[156:159], v[216:219]\n"
    "v_cvt_pk_bf16_f32 v210, v212, v213\n"
    "v_cvt_pk_bf16_f32 v211, v214, v215\n"
    "v_mfma_f32_16x16x32_bf16 v[220:223], v[132:135], v[156:159], v[220:223]\n"
    "ds_write_b128 v7, v[208:211] offset:1024\n"
    "v_mfma_f32_16x16x32_bf16 v[216:219], v[112:115], v[160:163], v[216:219]\n"
    "s_add_u32 s60, s40, 146432\n"
    "s_addc_u32 s61, s41, 0\n"
    "v_mfma_f32_16x16x32_bf16 v[220:223], v[136:139], v[160:163], v[220:223]\n"
    "s_add_u32 s66, s60, 6144\n"
    "s_addc_u32 s67, s61, 0\n"
    "v_mfma_f32_16x16x32_bf16 v[216:219], v[116:119], v[164:167], v[216:219]\n"
    "global_load_dwordx4 v[56:59], v235, s[60:61] offset:-3072 sc1\n"
    "global_load_dwordx4 v[80:83], v235, s[66:67] offset:-3072 sc1\n"
    "v_mfma_f32_16x16x32_bf16 v[220:223], v[140:143], v[164:167], v[220:223]\n"
    "global_load_dwordx4 v[60:63], v235, s[60:61] offset:-2048 sc1\n"
    "global_load_dwordx4 v[84:87], v235, s[66:67] offset:-2048 sc1\n"
    "v_mfma_f32_16x16x32_bf16 v[216:219], v[120:123], v[168:171], v[216:219]\n"
    "global_load_dwordx4 v[64:67], v235, s[60:61] offset:-1024 sc1\n"
    "global_load_dwordx4 v[88:91], v235, s[66:67] offset:-1024 sc1\n"
    "v_mfma_f32_16x16x32_bf16 v[220:223], v[144:147], v[168:171], v[220:223]\n"
    "global_load_dwordx4 v[68:71], v235, s[60:61] offset:0 sc1\n"
    "global_load_dwordx4 v[92:95], v235, s[66:67] offset:0 sc1\n"
    "v_mfma_f32_16x16x32_bf16 v[216:219], v[124:127], v[172:175], v[216:219]\n"
    "global_load_dwordx4 v[72:75], v235, s[60:61] offset:1024 sc1\n"
    "global_load_dwordx4 v[96:99], v235, s[66:67] offset:1024 sc1\n"
    "v_mfma_f32_16x16x32_bf16 v[220:223], v[148:151], v[172:175], v[220:223]\n"
    "global_load_dwordx4 v[76:79], v235, s[60:61] offset:2048 sc1\n"
    "global_load_dwordx4 v[100:103], v235, s[66:67] offset:2048 sc1\n"
    "s_nop 2\n"
    "v_cvt_pk_bf16_f32 v216, v216, v217\n"
    "v_cvt_pk_bf16_f32 v217, v218, v219\n"
    "s_nop 0\n"
    "v_cvt_pk_bf16_f32 v218, v220, v221\n"
    "v_cvt_pk_bf16_f32 v219, v222, v223\n"
    "ds_write_b128 v7, v[216:219] offset:2048\n"
    "v_mov_b32 v227, 13\n"
    "ds_write_b32 v225, v227 offset:16\n"
    ".Lg_to:\n"
    "s_waitcnt vmcnt(12)\n"
    "v_sub_u32 v8, v8, v240\n"
    "v_sub_u32 v12, v12, v240\n"
    "v_sub_u32 v16, v16, v240\n"
    "v_sub_u32 v20, v20, v240\n"
    "v_sub_u32 v24, v24, v240\n"
    "v_sub_u32 v28, v28, v240\n"
    "v_sub_u32 v32, v32, v240\n"
    "v_sub_u32 v36, v36, v240\n"
    "v_sub_u32 v40, v40, v240\n"
    "v_sub_u32 v44, v44, v240\n"
    "v_sub_u32 v48, v48, v240\n"
    "v_sub_u32 v52, v52, v240\n"
    "s_waitcnt vmcnt(0)\n"
    "v_sub_u32 v56, v56, v240\n"
    "v_sub_u32 v60, v60, v240\n"
    "v_sub_u32 v64, v64, v240\n"
    "v_sub_u32 v68, v68, v240\n"
    "v_sub_u32 v72, v72, v240\n"
    "v_sub_u32 v76, v76, v240\n"
    "v_sub_u32 v80, v80, v240\n"
    "v_sub_u32 v84, v84, v240\n"
    "v_sub_u32 v88, v88, v240\n"
    "v_sub_u32 v92, v92, v240\n"
    "v_sub_u32 v96, v96, v240\n"
    "v_sub_u32 v100, v100, v240\n"
    "ds_read_b32 v228, v224 offset:128\n"
    "s_waitcnt lgkmcnt(0)\n"
    "v_cmp_gt_u32 vcc, 3, v228\n"
    "s_cbranch_vccnz .Lg30\n"
    ".Lg30b:\n"
    "ds_read_b64_tr_b16 v[152:153], v0 offset:30720\n"
    "ds_read_b64_tr_b16 v[154:155], v1 offset:30720\n"
    "ds_read_b64_tr_b16 v[156:157], v0 offset:31744\n"
    "ds_read_b64_tr_b16 v[158:159], v1 offset:31744\n"
    "ds_read_b64_tr_b16 v[160:161], v0 offset:32768\n"
    "ds_read_b64_tr_b16 v[162:163], v1 offset:32768\n"
    "ds_read_b64_tr_b16 v[164:165], v0 offset:33792\n"
    "ds_read_b64_tr_b16 v[166:167], v1 offset:33792\n"
    "ds_read_b64_tr_b16 v[168:169], v0 offset:34816\n"
    "ds_read_b64_tr_b16 v[170:171], v1 offset:34816\n"
    "ds_read_b64_tr_b16 v[172:173], v0 offset:35840\n"
    "ds_read_b64_tr_b16 v[174:175], v1 offset:35840\n"
    "ds_read_b32 v252, v238 offset:128\n"
    "s_waitcnt lgkmcnt(9)\n"
    "v_mfma_f32_16x16x32_bf16 v[200:203], v[8:11], v[152:155], 0\n"
    "v_mfma_f32_16x16x32_bf16 v[204:207], v[32:35], v[152:155], 0\n"
    "ds_read_b32 v228, v224 offset:144\n"
    "v_mfma_f32_16x16x32_bf16 v[200:203], v[12:15], v[156:159], v[200:203]\n"
    "v_mfma_f32_16x16x32_bf16 v[204:207], v[36:39], v[156:159], v[204:207]\n"
    "s_waitcnt lgkmcnt(8)\n"
    "v_mfma_f32_16x16x32_bf16 v[200:203], v[16:19], v[160:163], v[200:203]\n"
    "v_mfma_f32_16x16x32_bf16 v[204:207], v[40:43], v[160:163], v[204:207]\n"
    "s_waitcnt lgkmcnt(6)\n"
    "v_mfma_f32_16x16x32_bf16 v[200:203], v[20:23], v[164:167], v[200:203]\n"
    "v_mfma_f32_16x16x32_bf16 v[204:207], v[44:47], v[164:167], v[204:207]\n"
    "s_waitcnt lgkmcnt(4)\n"
    "v_mfma_f32_16x16x32_bf16 v[200:203], v[24:27], v[168:171], v[200:203]\n"
    "v_mfma_f32_16x16x32_bf16 v[204:207], v[48:51], v[168:171], v[204:207]\n"
    "s_waitcnt lgkmcnt(2)\n"
    "v_mfma_f32_16x16x32_bf16 v[200:203], v[28:31], v[172:175], v[200:203]\n"
    "s_waitcnt lgkmcnt(0)\n"
    "v_cmp_gt_u32 vcc, 3, v228\n"
    "v_mfma_f32_16x16x32_bf16 v[204:207], v[52:55], v[172:175], v[204:207]\n"
    "s_cbranch_vccnz .Lg31\n"
    "v_mfma_f32_16x16x32_bf16 v[208:211], v[56:59], v[152:155], 0\n"
    "ds_read_b64_tr_b16 v[176:177], v0 offset:18432\n"
    "ds_read_b64_tr_b16 v[178:179], v1 offset:18432\n"
    "v_mfma_f32_16x16x32_bf16 v[212:215], v[80:83], v[152:155], 0\n"
    "ds_read_b64_tr_b16 v[180:181], v0 offset:19456\n"
    "ds_read_b64_tr_b16 v[182:183], v1 offset:19456\n"
    "v_mfma_f32_16x16x32_bf16 v[208:211], v[60:63], v[156:159], v[208:211]\n"
    "ds_read_b64_tr_b16 v[184:185], v0 offset:20480\n"
    "ds_read_b64_tr_b16 v[186:187], v1 offset:20480\n"
    "v_mfma_f32_16x16x32_bf16 v[212:215], v[84:87], v[156:159], v[212:215]\n"
    "ds_read_b64_tr_b16 v[188:189], v0 offset:21504\n"
    "ds_read_b64_tr_b16 v[190:191], v1 offset:21504\n"
    "v_mfma_f32_16x16x32_bf16 v[208:211], v[64:67], v[160:163], v[208:211]\n"
    "v_mul_f32 v200, v200, v252\n"
    "v_mul_f32 v201, v201, v252\n"
    "v_mfma_f32_16x16x32_bf16 v[212:215], v[88:91], v[160:163], v[212:215]\n"
    "ds_read_b64_tr_b16 v[192:193], v0 offset:22528\n"
    "ds_read_b64_tr_b16 v[194:195], v1 offset:22528\n"
    "v_mfma_f32_16x16x32_bf16 v[208:211], v[68:71], v[164:167], v[208:211]\n"
    "v_mul_f32 v202, v202, v252\n"
    "v_mul_f32 v203, v203, v252\n"
    "v_mfma_f32_16x16x32_bf16 v[212:215], v[92:95], v[164:167], v[212:215]\n"
    "ds_read_b64_tr_b16 v[196:197], v0 offset:23552\n"
    "ds_read_b64_tr_b16 v[198:199], v1 offset:23552\n"
    "v_mfma_f32_16x16x32_bf16 v[208:211], v[72:75], v[168:171], v[208:211]\n"
    "v_mul_f32 v204, v204, v252\n"
    "v_mul_f32 v205, v205, v252\n"
    "v_mfma_f32_16x16x32_bf16 v[212:215], v[96:99], v[168:171], v[212:215]\n"
    "ds_read_b32 v253, v238 offset:0\n"
    "v_mul_f32 v206, v206, v252\n"
    "v_mfma_f32_16x16x32_bf16 v[208:211], v[76:79], v[172:175], v[208:211]\n"
    "v_mul_f32 v207, v207, v252\n"
    "v_cvt_pk_bf16_f32 v200, v200, v201\n"
    "v_mfma_f32_16x16x32_bf16 v[212:215], v[100:103], v[172:175], v[212:215]\n"
    "v_cvt_pk_bf16_f32 v201, v202, v203\n"
    "v_cvt_pk_bf16_f32 v202, v204, v205\n"
    "v_cvt_pk_bf16_f32 v203, v206, v207\n"
    "buffer_store_dwordx4 v[200:203], v248, s[80:83], 0 offen offset:128 sc1\n"
    ".Lg31j:\n"
    "s_waitcnt lgkmcnt(1)\n"
    "s_nop 0\n"
    "v_mfma_f32_16x16x32_bf16 v[200:203], v[8:11], v[176:179], 0\n"
    "v_mul_f32 v208, v208, v252\n"
    "v_mul_f32 v209, v209, v252\n"
    "v_mfma_f32_16x16x32_bf16 v[204:207], v[32:35], v[176:179], 0\n"
    "ds_read_b32 v228, v224 offset:160\n"
    "v_mul_f32 v210, v210, v252\n"
    "v_mfma_f32_16x16x32_bf16 v[200:203], v[12:15], v[180:183], v[200:203]\n"
    "v_mul_f32 v211, v211, v252\n"
    "v_mul_f32 v212, v212, v252\n"
    "v_mfma_f32_16x16x32_bf16 v[204:207], v[36:39], v[180:183], v[204:207]\n"
    "v_mul_f32 v213, v213, v252\n"
    "v_mul_f32 v214, v214, v252\n"
    "v_mfma_f32_16x16x32_bf16 v[200:203], v[16:19], v[184:187], v[200:203]\n"
    "v_mul_f32 v215, v215, v252\n"
    "v_cvt_pk_bf16_f32 v208, v208, v209\n"
    "v_mfma_f32_16x16x32_bf16 v[204:207], v[40:43], v[184:187], v[204:207]\n"
    "v_cvt_pk_bf16_f32 v209, v210, v211\n"
    "v_cvt_pk_bf16_f32 v210, v212, v213\n"
    "v_mfma_f32_16x16x32_bf16 v[200:203], v[20:23], v[188:191], v[200:203]\n"
    "v_cvt_pk_bf16_f32 v211, v214, v215\n"
    "v_mfma_f32_16x16x32_bf16 v[204:207], v[44:47], v[188:191], v[204:207]\n"
    "buffer_store_dwordx4 v[208:211], v248, s[80:83], 0 offen offset:320 sc1\n"
    "v_mfma_f32_16x16x32_bf16 v[200:203], v[24:27], v[192:195], v[200:203]\n"
    "v_mfma_f32_16x16x32_bf16 v[204:207], v[48:51], v[192:195], v[204:207]\n"
    "v_add_u32 v248, v251, v248\n"
    "v_mfma_f32_16x16x32_bf16 v[200:203], v[28:31], v[196:199], v[200:203]\n"
    "s_waitcnt lgkmcnt(0)\n"
    "v_cmp_gt_u32 vcc, 3, v228\n"
    "v_mfma_f32_16x16x32_bf16 v[204:207], v[52:55], v[196:199], v[204:207]\n"
    "s_cbranch_vccnz .Lg32\n"
    "v_mfma_f32_16x16x32_bf16 v[208:211], v[56:59], v[176:179], 0\n"
    "ds_read_b64_tr_b16 v[152:153], v0 offset:6144\n"
    "ds_read_b64_tr_b16 v[154:155], v1 offset:6144\n"
    "v_mfma_f32_16x16x32_bf16 v[212:215], v[80:83], v[176:179], 0\n"
    "ds_read_b64_tr_b16 v[156:157], v0 offset:7168\n"
    "ds_read_b64_tr_b16 v[158:159], v1 offset:7168\n"
    "v_mfma_f32_16x16x32_bf16 v[208:211], v[60:63], v[180:183], v[208:211]\n"
    "ds_read_b64_tr_b16 v[160:161], v0 offset:8192\n"
    "ds_read_b64_tr_b16 v[162:163], v1 offset:8192\n"
    "v_mfma_f32_16x16x32_bf16 v[212:215], v[84:87], v[180:183], v[212:215]\n"
    "ds_read_b64_tr_b16 v[164:165], v0 offset:9216\n"
    "ds_read_b64_tr_b16 v[166:167], v1 offset:9216\n"
    "v_mfma_f32_16x16x32_bf16 v[208:211], v[64:67], v[184:187], v[208:211]\n"
    "v_mul_f32 v200, v200, v253\n"
    "v_mul_f32 v201, v201, v253\n"
    "v_mfma_f32_16x16x32_bf16 v[212:215], v[88:91], v[184:187], v[212:215]\n"
    "ds_read_b64_tr_b16 v[168:169], v0 offset:10240\n"
    "ds_read_b64_tr_b16 v[170:171], v1 offset:10240\n"
    "v_mfma_f32_16x16x32_bf16 v[208:211], v[68:71], v[188:191], v[208:211]\n"
    "v_mul_f32 v202, v202, v253\n"
    "v_mul_f32 v203, v203, v253\n"
    "v_mfma_f32_16x16x32_bf16 v[212:215], v[92:95], v[188:191], v[212:215]\n"
    "ds_read_b64_tr_b16 v[172:173], v0 offset:11264\n"
    "ds_read_b64_tr_b16 v[174:175], v1 offset:11264\n"
    "v_mfma_f32_16x16x32_bf16 v[208:211], v[72:75], v[192:195], v[208:211]\n"
    "v_mul_f32 v204, v204, v253\n"
    "v_mul_f32 v205, v205, v253\n"
    "v_mfma_f32_16x16x32_bf16 v[212:215], v[96:99], v[192:195], v[212:215]\n"
    "ds_read_b32 v252, v238 offset:256\n"
    "v_mul_f32 v206, v206, v253\n"
    "v_mfma_f32_16x16x32_bf16 v[208:211], v[76:79], v[196:199], v[208:211]\n"
    "v_mul_f32 v207, v207, v253\n"
    "v_cvt_pk_bf16_f32 v200, v200, v201\n"
    "v_mfma_f32_16x16x32_bf16 v[212:215], v[100:103], v[196:199], v[212:215]\n"
    "v_cvt_pk_bf16_f32 v201, v202, v203\n"
    "v_cvt_pk_bf16_f32 v202, v204, v205\n"
    "v_cvt_pk_bf16_f32 v203, v206, v207\n"
    "buffer_store_dwordx4 v[200:203], v249, s[80:83], 0 offen offset:128 sc1\n"
    ".Lg32j:\n"
    "s_waitcnt lgkmcnt(1)\n"
    "s_nop 0\n"
    "v_mfma_f32_16x16x32_bf16 v[200:203], v[8:11], v[152:155], 0\n"
    "v_mul_f32 v208, v208, v253\n"
    "v_mul_f32 v209, v209, v253\n"
    "v_mfma_f32_16x16x32_bf16 v[204:207], v[32:35], v[152:155], 0\n"
    "ds_read_b32 v228, v224 offset:176\n"
    "v_mul_f32 v210, v210, v253\n"
    "v_mfma_f32_16x16x32_bf16 v[200:203], v[12:15], v[156:159], v[200:203]\n"
    "v_mul_f32 v211, v211, v253\n"
    "v_mul_f32 v212, v212, v253\n"
    "v_mfma_f32_16x16x32_bf16 v[204:207], v[36:39], v[156:159], v[204:207]\n"
    "v_mul_f32 v213, v213, v253\n"
    "v_mul_f32 v214, v214, v253\n"
    "v_mfma_f32_16x16x32_bf16 v[200:203], v[16:19], v[160:163], v[200:203]\n"
    "v_mul_f32 v215, v215, v253\n"
    "v_cvt_pk_bf16_f32 v208, v208, v209\n"
    "v_mfma_f32_16x16x32_bf16 v[204:207], v[40:43], v[160:163], v[204:207]\n"
    "v_cvt_pk_bf16_f32 v209, v210, v211\n"
    "v_cvt_pk_bf16_f32 v210, v212, v213\n"
    "v_mfma_f32_16x16x32_bf16 v[200:203], v[20:23], v[164:167], v[200:203]\n"
    "v_cvt_pk_bf16_f32 v211, v214, v215\n"
    "v_mfma_f32_16x16x32_bf16 v[204:207], v[44:47], v[164:167], v[204:207]\n"
    "buffer_store_dwordx4 v[208:211], v249, s[80:83], 0 offen offset:320 sc1\n"
    "v_mfma_f32_16x16x32_bf16 v[200:203], v[24:27], v[168:171], v[200:203]\n"
    "v_mfma_f32_16x16x32_bf16 v[204:207], v[48:51], v[168:171], v[204:207]\n"
    "v_add_u32 v249, v251, v249\n"
    "v_mfma_f32_16x16x32_bf16 v[200:203], v[28:31], v[172:175], v[200:203]\n"
    "s_waitcnt lgkmcnt(0)\n"
    "v_cmp_gt_u32 vcc, 3, v228\n"
    "v_mfma_f32_16x16x32_bf16 v[204:207], v[52:55], v[172:175], v[204:207]\n"
    "s_cbranch_vccnz .Lg33\n"
    "v_mfma_f32_16x16x32_bf16 v[208:211], v[56:59], v[152:155], 0\n"
    "ds_read_b64_tr_b16 v[176:177], v0 offset:12288\n"
    "ds_read_b64_tr_b16 v[178:179], v1 offset:12288\n"
    "v_mfma_f32_16x16x32_bf16 v[212:215], v[80:83], v[152:155], 0\n"
    "ds_read_b64_tr_b16 v[180:181], v0 offset:13312\n"
    "ds_read_b64_tr_b16 v[182:183], v1 offset:13312\n"
    "v_mfma_f32_16x16x32_bf16 v[208:211], v[60:63], v[156:159], v[208:211]\n"
    "ds_read_b64_tr_b16 v[184:185], v0 offset:14336\n"
    "ds_read_b64_tr_b16 v[186:187], v1 offset:14336\n"
    "v_mfma_f32_16x16x32_bf16 v[212:215], v[84:87], v[156:159], v[212:215]\n"
    "ds_read_b64_tr_b16 v[188:189], v0 offset:15360\n"
    "ds_read_b64_tr_b16 v[190:191], v1 offset:15360\n"
    "v_mfma_f32_16x16x32_bf16 v[208:211], v[64:67], v[160:163], v[208:211]\n"
    "v_mul_f32 v200, v200, v252\n"
    "v_mul_f32 v201, v201, v252\n"
    "v_mfma_f32_16x16x32_bf16 v[212:215], v[88:91], v[160:163], v[212:215]\n"
    "ds_read_b64_tr_b16 v[192:193], v0 offset:16384\n"
    "ds_read_b64_tr_b16 v[194:195], v1 offset:16384\n"
    "v_mfma_f32_16x16x32_bf16 v[208:211], v[68:71], v[164:167], v[208:211]\n"
    "v_mul_f32 v202, v202, v252\n"
    "v_mul_f32 v203, v203, v252\n"
    "v_mfma_f32_16x16x32_bf16 v[212:215], v[92:95], v[164:167], v[212:215]\n"
    "ds_read_b64_tr_b16 v[196:197], v0 offset:17408\n"
    "ds_read_b64_tr_b16 v[198:199], v1 offset:17408\n"
    "v_mfma_f32_16x16x32_bf16 v[208:211], v[72:75], v[168:171], v[208:211]\n"
    "v_mul_f32 v204, v204, v252\n"
    "v_mul_f32 v205, v205, v252\n"
    "v_mfma_f32_16x16x32_bf16 v[212:215], v[96:99], v[168:171], v[212:215]\n"
    "ds_read_b32 v253, v238 offset:320\n"
    "v_mul_f32 v206, v206, v252\n"
    "v_mfma_f32_16x16x32_bf16 v[208:211], v[76:79], v[172:175], v[208:211]\n"
    "v_mul_f32 v207, v207, v252\n"
    "v_cvt_pk_bf16_f32 v200, v200, v201\n"
    "v_mfma_f32_16x16x32_bf16 v[212:215], v[100:103], v[172:175], v[212:215]\n"
    "v_cvt_pk_bf16_f32 v201, v202, v203\n"
    "v_cvt_pk_bf16_f32 v202, v204, v205\n"
    "v_cvt_pk_bf16_f32 v203, v206, v207\n"
    "buffer_store_dwordx4 v[200:203], v248, s[80:83], 0 offen offset:128 sc1\n"
    ".Lg33j:\n"
    "s_cmp_lg_u32 s58, 0\n"
    "s_cbranch_scc1 .Lg_t12\n"
    "s_waitcnt lgkmcnt(1)\n"
    "v_mfma_f32_16x16x32_bf16 v[200:203], v[8:11], v[176:179], 0\n"
    "v_mul_f32 v208, v208, v252\n"
    "v_mul_f32 v209, v209, v252\n"
    "v_mfma_f32_16x16x32_bf16 v[204:207], v[32:35], v[176:179], 0\n"
    "ds_read_b32 v228, v224 offset:832\n"
    "v_mul_f32 v210, v210, v252\n"
    "v_mfma_f32_16x16x32_bf16 v[200:203], v[12:15], v[180:183], v[200:203]\n"
    "v_mul_f32 v211, v211, v252\n"
    "v_mul_f32 v212, v212, v252\n"
    "v_mfma_f32_16x16x32_bf16 v[204:207], v[36:39], v[180:183], v[204:207]\n"
    "v_mul_f32 v213, v213, v252\n"
    "v_mul_f32 v214, v214, v252\n"
    "v_mfma_f32_16x16x32_bf16 v[200:203], v[16:19], v[184:187], v[200:203]\n"
    "v_mul_f32 v215, v215, v252\n"
    "v_cvt_pk_bf16_f32 v208, v208, v209\n"
    "v_mfma_f32_16x16x32_bf16 v[204:207], v[40:43], v[184:187], v[204:207]\n"
    "v_cvt_pk_bf16_f32 v209, v210, v211\n"
    "v_cvt_pk_bf16_f32 v210, v212, v213\n"
    "v_mfma_f32_16x16x32_bf16 v[200:203], v[20:23], v[188:191], v[200:203]\n"
    "v_cvt_pk_bf16_f32 v211, v214, v215\n"
    "v_mfma_f32_16x16x32_bf16 v[204:207], v[44:47], v[188:191], v[204:207]\n"
    "buffer_store_dwordx4 v[208:211], v248, s[80:83], 0 offen offset:320 sc1\n"
    "v_mfma_f32_16x16x32_bf16 v[200:203], v[24:27], v[192:195], v[200:203]\n"
    "v_mfma_f32_16x16x32_bf16 v[204:207], v[48:51], v[192:195], v[204:207]\n"
    "v_add_u32 v248, v255, v248\n"
    "v_mfma_f32_16x16x32_bf16 v[200:203], v[28:31], v[196:199], v[200:203]\n"
    "s_waitcnt lgkmcnt(0)\n"
    "v_cmp_gt_u32 vcc, 4, v228\n"
    "v_mfma_f32_16x16x32_bf16 v[204:207], v[52:55], v[196:199], v[204:207]\n"
    "s_cbranch_vccnz .Lg34\n"
    "v_mfma_f32_16x16x32_bf16 v[208:211], v[56:59], v[176:179], 0\n"
    "ds_read_b64_tr_b16 v[152:153], v0 offset:0\n"
    "ds_read_b64_tr_b16 v[154:155], v1 offset:0\n"
    "v_mfma_f32_16x16x32_bf16 v[212:215], v[80:83], v[176:179], 0\n"
    "ds_read_b64_tr_b16 v[156:157], v0 offset:1024\n"
    "ds_read_b64_tr_b16 v[158:159], v1 offset:1024\n"
    "v_mfma_f32_16x16x32_bf16 v[208:211], v[60:63], v[180:183], v[208:211]\n"
    "ds_read_b64_tr_b16 v[160:161], v0 offset:2048\n"
    "ds_read_b64_tr_b16 v[162:163], v1 offset:2048\n"
    "v_mfma_f32_16x16x32_bf16 v[212:215], v[84:87], v[180:183], v[212:215]\n"
    "ds_read_b64_tr_b16 v[164:165], v0 offset:3072\n"
    "ds_read_b64_tr_b16 v[166:167], v1 offset:3072\n"
    "v_mfma_f32_16x16x32_bf16 v[208:211], v[64:67], v[184:187], v[208:211]\n"
    "v_mul_f32 v200, v200, v253\n"
    "v_mul_f32 v201, v201, v253\n"
    "v_mfma_f32_16x16x32_bf16 v[212:215], v[88:91], v[184:187], v[212:215]\n"
    "ds_read_b64_tr_b16 v[168:169], v0 offset:4096\n"
    "ds_read_b64_tr_b16 v[170:171], v1 offset:4096\n"
    "v_mfma_f32_16x16x32_bf16 v[208:211], v[68:71], v[188:191], v[208:211]\n"
    "v_mul_f32 v202, v202, v253\n"
    "v_mul_f32 v203, v203, v253\n"
    "v_mfma_f32_16x16x32_bf16 v[212:215], v[92:95], v[188:191], v[212:215]\n"
    "ds_read_b64_tr_b16 v[172:173], v0 offset:5120\n"
    "ds_read_b64_tr_b16 v[174:175], v1 offset:5120\n"
    "v_mfma_f32_16x16x32_bf16 v[208:211], v[72:75], v[192:195], v[208:211]\n"
    "v_mul_f32 v204, v204, v253\n"
    "v_mul_f32 v205, v205, v253\n"
    "v_mfma_f32_16x16x32_bf16 v[212:215], v[96:99], v[192:195], v[212:215]\n"
    "ds_read_b32 v252, v238 offset:192\n"
    "v_mul_f32 v206, v206, v253\n"
    "v_mfma_f32_16x16x32_bf16 v[208:211], v[76:79], v[196:199], v[208:211]\n"
    "v_mul_f32 v207, v207, v253\n"
    "v_cvt_pk_bf16_f32 v200, v200, v201\n"
    "v_mfma_f32_16x16x32_bf16 v[212:215], v[100:103], v[196:199], v[212:215]\n"
    "v_cvt_pk_bf16_f32 v201, v202, v203\n"
    "v_cvt_pk_bf16_f32 v202, v204, v205\n"
    "v_cvt_pk_bf16_f32 v203, v206, v207\n"
    "buffer_store_dwordx4 v[200:203], v249, s[80:83], 0 offen offset:128 sc1\n"
    ".Lg34j:\n"
    "s_waitcnt lgkmcnt(1)\n"
    "s_nop 0\n"
    "v_mfma_f32_16x16x32_bf16 v[200:203], v[8:11], v[152:155], 0\n"
    "v_mul_f32 v208, v208, v253\n"
    "v_mul_f32 v209, v209, v253\n"
    "v_mfma_f32_16x16x32_bf16 v[204:207], v[32:35], v[152:155], 0\n"
    "v_mul_f32 v210, v210, v253\n"
    "v_mul_f32 v211, v211, v253\n"
    "v_mfma_f32_16x16x32_bf16 v[200:203], v[12:15], v[156:159], v[200:203]\n"
    "v_mul_f32 v212, v212, v253\n"
    "v_mul_f32 v213, v213, v253\n"
    "v_mfma_f32_16x16x32_bf16 v[204:207], v[36:39], v[156:159], v[204:207]\n"
    "v_mul_f32 v214, v214, v253\n"
    "v_mul_f32 v215, v215, v253\n"
    "v_mfma_f32_16x16x32_bf16 v[200:203], v[16:19], v[160:163], v[200:203]\n"
    "v_cvt_pk_bf16_f32 v208, v208, v209\n"
    "v_cvt_pk_bf16_f32 v209, v210, v211\n"
    "v_mfma_f32_16x16x32_bf16 v[204:207], v[40:43], v[160:163], v[204:207]\n"
    "v_cvt_pk_bf16_f32 v210, v212, v213\n"
    "v_cvt_pk_bf16_f32 v211, v214, v215\n"
    "v_mfma_f32_16x16x32_bf16 v[200:203], v[20:23], v[164:167], v[200:203]\n"
    "buffer_store_dwordx4 v[208:211], v249, s[80:83], 0 offen offset:320 sc1\n"
    "v_mfma_f32_16x16x32_bf16 v[204:207], v[44:47], v[164:167], v[204:207]\n"
    "v_mfma_f32_16x16x32_bf16 v[200:203], v[24:27], v[168:171], v[200:203]\n"
    "v_mfma_f32_16x16x32_bf16 v[204:207], v[48:51], v[168:171], v[204:207]\n"
    "v_mfma_f32_16x16x32_bf16 v[200:203], v[28:31], v[172:175], v[200:203]\n"
    "v_mfma_f32_16x16x32_bf16 v[204:207], v[52:55], v[172:175], v[204:207]\n"
    "v_mfma_f32_16x16x32_bf16 v[208:211], v[56:59], v[152:155], 0\n"
    "v_mfma_f32_16x16x32_bf16 v[212:215], v[80:83], v[152:155], 0\n"
    "v_mfma_f32_16x16x32_bf16 v[208:211], v[60:63], v[156:159], v[208:211]\n"
    "v_mfma_f32_16x16x32_bf16 v[212:215], v[84:87], v[156:159], v[212:215]\n"
    "v_mfma_f32_16x16x32_bf16 v[208:211], v[64:67], v[160:163], v[208:211]\n"
    "v_mfma_f32_16x16x32_bf16 v[212:215], v[88:91], v[160:163], v[212:215]\n"
    "s_waitcnt lgkmcnt(0)\n"
    "v_mul_f32 v200, v200, v252\n"
    "v_mfma_f32_16x16x32_bf16 v[208:211], v[68:71], v[164:167], v[208:211]\n"
    "v_mul_f32 v201, v201, v252\n"
    "v_mul_f32 v202, v202, v252\n"
    "v_mfma_f32_16x16x32_bf16 v[212:215], v[92:95], v[164:167], v[212:215]\n"
    "v_mul_f32 v203, v203, v252\n"
    "v_mul_f32 v204, v204, v252\n"
    "v_mfma_f32_16x16x32_bf16 v[208:211], v[72:75], v[168:171], v[208:211]\n"
    "v_mul_f32 v205, v205, v252\n"
    "v_mul_f32 v206, v206, v252\n"
    "v_mfma_f32_16x16x32_bf16 v[212:215], v[96:99], v[168:171], v[212:215]\n"
    "v_mul_f32 v207, v207, v252\n"
    "v_cvt_pk_bf16_f32 v200, v200, v201\n"
    "v_mfma_f32_16x16x32_bf16 v[208:211], v[76:79], v[172:175], v[208:211]\n"
    "v_cvt_pk_bf16_f32 v201, v202, v203\n"
    "v_cvt_pk_bf16_f32 v202, v204, v205\n"
    "v_mfma_f32_16x16x32_bf16 v[212:215], v[100:103], v[172:175], v[212:215]\n"
    "v_cvt_pk_bf16_f32 v203, v206, v207\n"
    "buffer_store_dwordx4 v[200:203], v248, s[80:83], 0 offen offset:128 sc1\n"
    "s_nop 2\n"
    "v_mul_f32 v208, v208, v252\n"
    "v_mul_f32 v209, v209, v252\n"
    "v_mul_f32 v210, v210, v252\n"
    "v_mul_f32 v211, v211, v252\n"
    "v_mul_f32 v212, v212, v252\n"
    "v_mul_f32 v213, v213, v252\n"
    "v_mul_f32 v214, v214, v252\n"
    "v_mul_f32 v215, v215, v252\n"
    "v_cvt_pk_bf16_f32 v208, v208, v209\n"
    "v_cvt_pk_bf16_f32 v209, v210, v211\n"
    "v_cvt_pk_bf16_f32 v210, v212, v213\n"
    "v_cvt_pk_bf16_f32 v211, v214, v215\n"
    "buffer_store_dwordx4 v[208:211], v248, s[80:83], 0 offen offset:320 sc1\n"
    ".Lg_end:\n"
    "s_waitcnt lgkmcnt(0)\n"
    "s_endpgm\n"
    ".Lg1:\n"
    "s_sub_u32 s52, s52, 1\n"
    "s_cmp_lt_i32 s52, 0\n"
    "s_cbranch_scc1 .Lg1b\n"
    "s_sleep 1\n"
    "ds_read_b32 v228, v224 offset:48\n"
    "s_waitcnt lgkmcnt(0)\n"
    "v_cmp_gt_u32 vcc, 1, v228\n"
    "s_cbranch_vccnz .Lg1\n"
    "s_branch .Lg1b\n"
    ".Lg_poll:\n"
    "s_sub_u32 s51, s51, 1\n"
    "s_cmp_le_i32 s51, 0\n"
    "s_cbranch_scc1 .Lg_retry\n"
    "s_sleep 4\n"
    "s_mov_b32 exec_hi, 15\n"
    "global_load_dwordx2 v[244:245], v236, s[40:41] sc1\n"
    "s_mov_b64 exec, -1\n"
    "s_waitcnt vmcnt(0)\n"
    "v_cmp_ne_u64 vcc, v[240:241], v[244:245]\n"
    "s_cbranch_vccnz .Lg_poll\n"
    "s_branch .Lg_retry\n"
    ".Lg_fail:\n"
    "s_sub_u32 s51, s51, 1\n"
    "s_sleep 2\n"
    "s_cmp_gt_i32 s51, 0\n"
    "s_cbranch_scc1 .Lg_retry\n"
    "s_branch .Lg_ok\n"
    ".Lg2:\n"
    "s_sub_u32 s52, s52, 1\n"
    "s_cmp_lt_i32 s52, 0\n"
    "s_cbranch_scc1 .Lg2b\n"
    "s_sleep 1\n"
    "ds_read_b32 v228, v224 offset:0\n"
    "s_waitcnt lgkmcnt(0)\n"
    "v_cmp_gt_u32 vcc, 1, v228\n"
    "s_cbranch_vccnz .Lg2\n"
    "s_branch .Lg2b\n"
    ".Lg3:\n"
    "s_sub_u32 s52, s52, 1\n"
    "s_cmp_lt_i32 s52, 0\n"
    "s_cbranch_scc1 .Lg3b\n"
    "s_sleep 1\n"
    "ds_read_b32 v229, v224 offset:0\n"
    "s_waitcnt lgkmcnt(0)\n"
    "v_cmp_gt_u32 vcc, 2, v229\n"
    "s_cbranch_vccnz .Lg3\n"
    "s_branch .Lg3b\n"
    ".Lg4:\n"
    "s_sub_u32 s52, s52, 1\n"
    "s_cmp_lt_i32 s52, 0\n"
    "s_cbranch_scc1 .Lg4b\n"
    "s_sleep 1\n"
    "ds_read_b32 v229, v224 offset:0\n"
    "s_waitcnt lgkmcnt(0)\n"
    "v_cmp_gt_u32 vcc, 3, v229\n"
    "s_cbranch_vccnz .Lg4\n"
    "s_branch .Lg4b\n"
    ".Lg5:\n"
    "s_sub_u32 s52, s52, 1\n"
    "s_cmp_lt_i32 s52, 0\n"
    "s_cbranch_scc1 .Lg5b\n"
    "s_sleep 1\n"
    "ds_read_b32 v228, v224 offset:192\n"
    "s_waitcnt lgkmcnt(0)\n"
    "v_cmp_gt_u32 vcc, 1, v228\n"
    "s_cbranch_vccnz .Lg5\n"
    "s_branch .Lg5b\n"
    ".Lg6:\n"
    "s_sub_u32 s52, s52, 1\n"
    "s_cmp_lt_i32 s52, 0\n"
    "s_cbranch_scc1 .Lg6b\n"
    "s_sleep 1\n"
    "ds_read_b32 v228, v224 offset:208\n"
    "s_waitcnt lgkmcnt(0)\n"
    "v_cmp_gt_u32 vcc, 1, v228\n"
    "s_cbranch_vccnz .Lg6\n"
    "s_branch .Lg6b\n"
    ".Lg7:\n"
    "s_sub_u32 s52, s52, 1\n"
    "s_cmp_lt_i32 s52, 0\n"
    "s_cbranch_scc1 .Lg7b\n"
    "s_sleep 1\n"
    "ds_read_b32 v239, v224 offset:64\n"
    "s_waitcnt lgkmcnt(0)\n"
    "v_cmp_gt_u32 vcc, 1, v239\n"
    "s_cbranch_vccnz .Lg7\n"
    "s_branch .Lg7b\n"
    ".Lg8:\n"
    "s_sub_u32 s52, s52, 1\n"
    "s_cmp_lt_i32 s52, 0\n"
    "s_cbranch_scc1 .Lg8b\n"
    "s_sleep 1\n"
    "ds_read_b32 v229, v224 offset:0\n"
    "s_waitcnt lgkmcnt(0)\n"
    "v_cmp_gt_u32 vcc, 6, v229\n"
    "s_cbranch_vccnz .Lg8\n"
    "s_branch .Lg8b\n"
    ".Lg9:\n"
    "s_sub_u32 s52, s52, 1\n"
    "s_cmp_lt_i32 s52, 0\n"
    "s_cbranch_scc1 .Lg9b\n"
    "s_sleep 1\n"
    "ds_read_b32 v228, v224 offset:224\n"
    "s_waitcnt lgkmcnt(0)\n"
    "v_cmp_gt_u32 vcc, 1, v228\n"
    "s_cbranch_vccnz .Lg9\n"
    "s_branch .Lg9b\n"
    ".Lg10:\n"
    "s_sub_u32 s52, s52, 1\n"
    "s_cmp_lt_i32 s52, 0\n"
    "s_cbranch_scc1 .Lg10b\n"
    "s_sleep 1\n"
    "ds_read_b32 v239, v224 offset:80\n"
    "s_waitcnt lgkmcnt(0)\n"
    "v_cmp_gt_u32 vcc, 1, v239\n"
    "s_cbranch_vccnz .Lg10\n"
    "s_branch .Lg10b\n"
    ".Lg11:\n"
    "s_sub_u32 s52, s52, 1\n"
    "s_cmp_lt_i32 s52, 0\n"
    "s_cbranch_scc1 .Lg11b\n"
    "s_sleep 1\n"
    "ds_read_b32 v228, v224 offset:240\n"
    "s_waitcnt lgkmcnt(0)\n"
    "v_cmp_gt_u32 vcc, 1, v228\n"
    "s_cbranch_vccnz .Lg11\n"
    "s_branch .Lg11b\n"
    ".Lg12:\n"
    "s_sub_u32 s52, s52, 1\n"
    "s_cmp_lt_i32 s52, 0\n"
    "s_cbranch_scc1 .Lg12b\n"
    "s_sleep 1\n"
    "ds_read_b32 v239, v224 offset:96\n"
    "s_waitcnt lgkmcnt(0)\n"
    "v_cmp_gt_u32 vcc, 1, v239\n"
    "s_cbranch_vccnz .Lg12\n"
    "s_branch .Lg12b\n"
    ".Lg13:\n"
    "s_sub_u32 s52, s52, 1\n"
    "s_cmp_lt_i32 s52, 0\n"
    "s_cbranch_scc1 .Lg13b\n"
    "s_sleep 1\n"
    "ds_read_b32 v229, v224 offset:0\n"
    "s_waitcnt lgkmcnt(0)\n"
    "v_cmp_gt_u32 vcc, 7, v229\n"
    "s_cbranch_vccnz .Lg13\n"
    "s_branch .Lg13b\n"
    ".Lg14:\n"
    "s_sub_u32 s52, s52, 1\n"
    "s_cmp_lt_i32 s52, 0\n"
    "s_cbranch_scc1 .Lg14b\n"
    "s_sleep 1\n"
    "ds_read_b32 v228, v224 offset:192\n"
    "s_waitcnt lgkmcnt(0)\n"
    "v_cmp_gt_u32 vcc, 2, v228\n"
    "s_cbranch_vccnz .Lg14\n"
    "s_branch .Lg14b\n"
    ".Lg15:\n"
    "s_sub_u32 s52, s52, 1\n"
    "s_cmp_lt_i32 s52, 0\n"
    "s_cbranch_scc1 .Lg15b\n"
    "s_sleep 1\n"
    "ds_read_b32 v239, v224 offset:112\n"
    "s_waitcnt lgkmcnt(0)\n"
    "v_cmp_gt_u32 vcc, 1, v239\n"
    "s_cbranch_vccnz .Lg15\n"
    "s_branch .Lg15b\n"
    ".Lg16:\n"
    "s_sub_u32 s52, s52, 1\n"
    "s_cmp_lt_i32 s52, 0\n"
    "s_cbranch_scc1 .Lg16b\n"
    "s_sleep 1\n"
    "ds_read_b32 v228, v224 offset:208\n"
    "s_waitcnt lgkmcnt(0)\n"
    "v_cmp_gt_u32 vcc, 2, v228\n"
    "s_cbranch_vccnz .Lg16\n"
    "s_branch .Lg16b\n"
    ".Lg17:\n"
    "s_sub_u32 s52, s52, 1\n"
    "s_cmp_lt_i32 s52, 0\n"
    "s_cbranch_scc1 .Lg17b\n"
    "s_sleep 1\n"
    "ds_read_b32 v239, v224 offset:64\n"
    "s_waitcnt lgkmcnt(0)\n"
    "v_cmp_gt_u32 vcc, 2, v239\n"
    "s_cbranch_vccnz .Lg17\n"
    "s_branch .Lg17b\n"
    ".Lg18:\n"
    "s_sub_u32 s52, s52, 1\n"
    "s_cmp_lt_i32 s52, 0\n"
    "s_cbranch_scc1 .Lg18b\n"
    "s_sleep 1\n"
    "ds_read_b32 v229, v224 offset:0\n"
    "s_waitcnt lgkmcnt(0)\n"
    "v_cmp_gt_u32 vcc, 8, v229\n"
    "s_cbranch_vccnz .Lg18\n"
    "s_branch .Lg18b\n"
    ".Lg19:\n"
    "s_sub_u32 s52, s52, 1\n"
    "s_cmp_lt_i32 s52, 0\n"
    "s_cbranch_scc1 .Lg19b\n"
    "s_sleep 1\n"
    "ds_read_b32 v228, v224 offset:224\n"
    "s_waitcnt lgkmcnt(0)\n"
    "v_cmp_gt_u32 vcc, 2, v228\n"
    "s_cbranch_vccnz .Lg19\n"
    "s_branch .Lg19b\n"
    ".Lg20:\n"
    "s_sub_u32 s52, s52, 1\n"
    "s_cmp_lt_i32 s52, 0\n"
    "s_cbranch_scc1 .Lg20b\n"
    "s_sleep 1\n"
    "ds_read_b32 v239, v224 offset:80\n"
    "s_waitcnt lgkmcnt(0)\n"
    "v_cmp_gt_u32 vcc, 2, v239\n"
    "s_cbranch_vccnz .Lg20\n"
    "s_branch .Lg20b\n"
    ".Lg21:\n"
    "s_sub_u32 s52, s52, 1\n"
    "s_cmp_lt_i32 s52, 0\n"
    "s_cbranch_scc1 .Lg21b\n"
    "s_sleep 1\n"
    "ds_read_b32 v228, v224 offset:240\n"
    "s_waitcnt lgkmcnt(0)\n"
    "v_cmp_gt_u32 vcc, 2, v228\n"
    "s_cbranch_vccnz .Lg21\n"
    "s_branch .Lg21b\n"
    ".Lg22:\n"
    "s_sub_u32 s52, s52, 1\n"
    "s_cmp_lt_i32 s52, 0\n"
    "s_cbranch_scc1 .Lg22b\n"
    "s_sleep 1\n"
    "ds_read_b32 v228, v224 offset:192\n"
    "s_waitcnt lgkmcnt(0)\n"
    "v_cmp_gt_u32 vcc, 3, v228\n"
    "s_cbranch_vccnz .Lg22\n"
    "s_branch .Lg22b\n"
    ".Lg_e12a:\n"
    "v_mfma_f32_16x16x32_bf16 v[200:203], v[8:11], v[176:179], 0\n"
    "v_cvt_pk_bf16_f32 v216, v216, v217\n"
    "v_cvt_pk_bf16_f32 v217, v218, v219\n"
    "v_mfma_f32_16x16x32_bf16 v[204:207], v[32:35], v[176:179], 0\n"
    "v_cvt_pk_bf16_f32 v218, v220, v221\n"
    "v_cvt_pk_bf16_f32 v219, v222, v223\n"
    "v_mfma_f32_16x16x32_bf16 v[200:203], v[12:15], v[180:183], v[200:203]\n"
    "ds_write_b128 v7, v[216:219] offset:8192\n"
    "v_mfma_f32_16x16x32_bf16 v[204:207], v[36:39], v[180:183], v[204:207]\n"
    "v_mov_b32 v227, 11\n"
    "v_mfma_f32_16x16x32_bf16 v[200:203], v[16:19], v[184:187], v[200:203]\n"
    "ds_write_b32 v225, v227 offset:16\n"
    "ds_read_b32 v228, v224 offset:192\n"
    "v_mfma_f32_16x16x32_bf16 v[204:207], v[40:43], v[184:187], v[204:207]\n"
    "v_mfma_f32_16x16x32_bf16 v[200:203], v[20:23], v[188:191], v[200:203]\n"
    "v_mfma_f32_16x16x32_bf16 v[204:207], v[44:47], v[188:191], v[204:207]\n"
    "v_mfma_f32_16x16x32_bf16 v[200:203], v[24:27], v[192:195], v[200:203]\n"
    "v_mfma_f32_16x16x32_bf16 v[204:207], v[48:51], v[192:195], v[204:207]\n"
    "v_mfma_f32_16x16x32_bf16 v[200:203], v[28:31], v[196:199], v[200:203]\n"
    "v_mfma_f32_16x16x32_bf16 v[204:207], v[52:55], v[196:199], v[204:207]\n"
    "v_mfma_f32_16x16x32_bf16 v[208:211], v[56:59], v[176:179], 0\n"
    "s_waitcnt lgkmcnt(0)\n"
    "v_cmp_gt_u32 vcc, 3, v228\n"
    "v_mfma_f32_16x16x32_bf16 v[212:215], v[80:83], v[176:179], 0\n"
    "v_mfma_f32_16x16x32_bf16 v[208:211], v[60:63], v[180:183], v[208:211]\n"
    "s_cbranch_vccnz .Lg22\n"
    ".Lg22b:\n"
    "v_mfma_f32_16x16x32_bf16 v[212:215], v[84:87], v[180:183], v[212:215]\n"
    "v_cvt_pk_bf16_f32 v200, v200, v201\n"
    "v_cvt_pk_bf16_f32 v201, v202, v203\n"
    "v_mfma_f32_16x16x32_bf16 v[208:211], v[64:67], v[184:187], v[208:211]\n"
    "v_cvt_pk_bf16_f32 v202, v204, v205\n"
    "v_cvt_pk_bf16_f32 v203, v206, v207\n"
    "v_mfma_f32_16x16x32_bf16 v[212:215], v[88:91], v[184:187], v[212:215]\n"
    "ds_write_b128 v7, v[200:203] offset:12288\n"
    "v_mfma_f32_16x16x32_bf16 v[208:211], v[68:71], v[188:191], v[208:211]\n"
    "v_mfma_f32_16x16x32_bf16 v[212:215], v[92:95], v[188:191], v[212:215]\n"
    "v_mfma_f32_16x16x32_bf16 v[208:211], v[72:75], v[192:195], v[208:211]\n"
    "v_mfma_f32_16x16x32_bf16 v[212:215], v[96:99], v[192:195], v[212:215]\n"
    "v_mfma_f32_16x16x32_bf16 v[208:211], v[76:79], v[196:199], v[208:211]\n"
    "v_mfma_f32_16x16x32_bf16 v[212:215], v[100:103], v[196:199], v[212:215]\n"
    "v_mfma_f32_16x16x32_bf16 v[216:219], v[104:107], v[176:179], 0\n"
    "v_mfma_f32_16x16x32_bf16 v[220:223], v[128:131], v[176:179], 0\n"
    "v_mfma_f32_16x16x32_bf16 v[216:219], v[108:111], v[180:183], v[216:219]\n"
    "v_mfma_f32_16x16x32_bf16 v[220:223], v[132:135], v[180:183], v[220:223]\n"
    "v_mfma_f32_16x16x32_bf16 v[216:219], v[112:115], v[184:187], v[216:219]\n"
    "v_mfma_f32_16x16x32_bf16 v[220:223], v[136:139], v[184:187], v[220:223]\n"
    "v_mfma_f32_16x16x32_bf16 v[216:219], v[116:119], v[188:191], v[216:219]\n"
    "v_cvt_pk_bf16_f32 v208, v208, v209\n"
    "v_cvt_pk_bf16_f32 v209, v210, v211\n"
    "v_mfma_f32_16x16x32_bf16 v[220:223], v[140:143], v[188:191], v[220:223]\n"
    "v_cvt_pk_bf16_f32 v210, v212, v213\n"
    "v_cvt_pk_bf16_f32 v211, v214, v215\n"
    "v_mfma_f32_16x16x32_bf16 v[216:219], v[120:123], v[192:195], v[216:219]\n"
    "ds_write_b128 v7, v[208:211] offset:13312\n"
    "v_mfma_f32_16x16x32_bf16 v[220:223], v[144:147], v[192:195], v[220:223]\n"
    "v_mfma_f32_16x16x32_bf16 v[216:219], v[124:127], v[196:199], v[216:219]\n"
    "v_mfma_f32_16x16x32_bf16 v[220:223], v[148:151], v[196:199], v[220:223]\n"
    "s_nop 6\n"
    "v_cvt_pk_bf16_f32 v216, v216, v217\n"
    "v_cvt_pk_bf16_f32 v217, v218, v219\n"
    "v_cvt_pk_bf16_f32 v218, v220, v221\n"
    "v_cvt_pk_bf16_f32 v219, v222, v223\n"
    "ds_write_b128 v7, v[216:219] offset:14336\n"
    "v_mov_b32 v227, 12\n"
    "ds_write_b32 v225, v227 offset:16\n"
    "s_branch .Lg_end\n"
    ".Lg23:\n"
    "s_sub_u32 s52, s52, 1\n"
    "s_cmp_lt_i32 s52, 0\n"
    "s_cbranch_scc1 .Lg23b\n"
    "s_sleep 1\n"
    "ds_read_b32 v228, v224 offset:192\n"
    "s_waitcnt lgkmcnt(0)\n"
    "v_cmp_gt_u32 vcc, 3, v228\n"
    "s_cbranch_vccnz .Lg23\n"
    "s_branch .Lg23b\n"
    ".Lg24:\n"
    "s_sub_u32 s52, s52, 1\n"
    "s_cmp_lt_i32 s52, 0\n"
    "s_cbranch_scc1 .Lg24b\n"
    "s_sleep 1\n"
    "ds_read_b32 v239, v224 offset:272\n"
    "s_waitcnt lgkmcnt(0)\n"
    "v_cmp_gt_u32 vcc, 1, v239\n"
    "s_cbranch_vccnz .Lg24\n"
    "s_branch .Lg24b\n"
    ".Lg_e12b:\n"
    "v_mfma_f32_16x16x32_bf16 v[200:203], v[8:11], v[176:179], 0\n"
    "v_cvt_pk_bf16_f32 v216, v216, v217\n"
    "v_cvt_pk_bf16_f32 v217, v218, v219\n"
    "v_mfma_f32_16x16x32_bf16 v[204:207], v[32:35], v[176:179], 0\n"
    "v_cvt_pk_bf16_f32 v218, v220, v221\n"
    "v_cvt_pk_bf16_f32 v219, v222, v223\n"
    "v_mfma_f32_16x16x32_bf16 v[200:203], v[12:15], v[180:183], v[200:203]\n"
    "ds_write_b128 v7, v[216:219] offset:8192\n"
    "v_mfma_f32_16x16x32_bf16 v[204:207], v[36:39], v[180:183], v[204:207]\n"
    "v_mov_b32 v227, 11\n"
    "v_mfma_f32_16x16x32_bf16 v[200:203], v[16:19], v[184:187], v[200:203]\n"
    "ds_write_b32 v225, v227 offset:16\n"
    "ds_read_b32 v228, v224 offset:192\n"
    "v_mfma_f32_16x16x32_bf16 v[204:207], v[40:43], v[184:187], v[204:207]\n"
    "v_mfma_f32_16x16x32_bf16 v[200:203], v[20:23], v[188:191], v[200:203]\n"
    "v_mfma_f32_16x16x32_bf16 v[204:207], v[44:47], v[188:191], v[204:207]\n"
    "v_mfma_f32_16x16x32_bf16 v[200:203], v[24:27], v[192:195], v[200:203]\n"
    "v_mfma_f32_16x16x32_bf16 v[204:207], v[48:51], v[192:195], v[204:207]\n"
    "v_mfma_f32_16x16x32_bf16 v[200:203], v[28:31], v[196:199], v[200:203]\n"
    "v_mfma_f32_16x16x32_bf16 v[204:207], v[52:55], v[196:199], v[204:207]\n"
    "ds_read_b32 v239, v224 offset:272\n"
    "s_waitcnt lgkmcnt(0)\n"
    "v_cmp_gt_u32 vcc, 1, v239\n"
    "s_cbranch_vccnz .Lg24\n"
    ".Lg24b:\n"
    "v_mfma_f32_16x16x32_bf16 v[208:211], v[56:59], v[176:179], 0\n"
    "v_cmp_gt_u32 vcc, 3, v228\n"
    "v_mfma_f32_16x16x32_bf16 v[212:215], v[80:83], v[176:179], 0\n"
    "s_cbranch_vccnz .Lg23\n"
    ".Lg23b:\n"
    "v_cvt_pk_bf16_f32 v200, v200, v201\n"
    "v_mfma_f32_16x16x32_bf16 v[208:211], v[60:63], v[180:183], v[208:211]\n"
    "v_cvt_pk_bf16_f32 v201, v202, v203\n"
    "v_cvt_pk_bf16_f32 v202, v204, v205\n"
    "v_mfma_f32_16x16x32_bf16 v[212:215], v[84:87], v[180:183], v[212:215]\n"
    "v_cvt_pk_bf16_f32 v203, v206, v207\n"
    "v_mfma_f32_16x16x32_bf16 v[208:211], v[64:67], v[184:187], v[208:211]\n"
    "ds_write_b128 v7, v[200:203] offset:12288\n"
    "v_mfma_f32_16x16x32_bf16 v[212:215], v[88:91], v[184:187], v[212:215]\n"
    "s_add_u32 s60, s40, 109568\n"
    "s_addc_u32 s61, s41, 0\n"
    "v_mfma_f32_16x16x32_bf16 v[208:211], v[68:71], v[188:191], v[208:211]\n"
    "s_add_u32 s66, s60, 6144\n"
    "s_addc_u32 s67, s61, 0\n"
    "v_mfma_f32_16x16x32_bf16 v[212:215], v[92:95], v[188:191], v[212:215]\n"
    "global_load_dwordx4 v[8:11], v235, s[60:61] offset:-3072 sc1\n"
    "global_load_dwordx4 v[32:35], v235, s[66:67] offset:-3072 sc1\n"
    "v_mfma_f32_16x16x32_bf16 v[208:211], v[72:75], v[192:195], v[208:211]\n"
    "global_load_dwordx4 v[12:15], v235, s[60:61] offset:-2048 sc1\n"
    "global_load_dwordx4 v[36:39], v235, s[66:67] offset:-2048 sc1\n"
    "v_mfma_f32_16x16x32_bf16 v[212:215], v[96:99], v[192:195], v[212:215]\n"
    "global_load_dwordx4 v[16:19], v235, s[60:61] offset:-1024 sc1\n"
    "global_load_dwordx4 v[40:43], v235, s[66:67] offset:-1024 sc1\n"
    "v_mfma_f32_16x16x32_bf16 v[208:211], v[76:79], v[196:199], v[208:211]\n"
    "global_load_dwordx4 v[20:23], v235, s[60:61] offset:0 sc1\n"
    "global_load_dwordx4 v[44:47], v235, s[66:67] offset:0 sc1\n"
    "v_mfma_f32_16x16x32_bf16 v[212:215], v[100:103], v[196:199], v[212:215]\n"
    "global_load_dwordx4 v[24:27], v235, s[60:61] offset:1024 sc1\n"
    "global_load_dwordx4 v[48:51], v235, s[66:67] offset:1024 sc1\n"
    "v_mfma_f32_16x16x32_bf16 v[216:219], v[104:107], v[176:179], 0\n"
    "global_load_dwordx4 v[28:31], v235, s[60:61] offset:2048 sc1\n"
    "global_load_dwordx4 v[52:55], v235, s[66:67] offset:2048 sc1\n"
    "v_mfma_f32_16x16x32_bf16 v[220:223], v[128:131], v[176:179], 0\n"
    "v_cvt_pk_bf16_f32 v208, v208, v209\n"
    "v_cvt_pk_bf16_f32 v209, v210, v211\n"
    "v_mfma_f32_16x16x32_bf16 v[216:219], v[108:111], v[180:183], v[216:219]\n"
    "v_cvt_pk_bf16_f32 v210, v212, v213\n"
    "v_cvt_pk_bf16_f32 v211, v214, v215\n"
    "v_mfma_f32_16x16x32_bf16 v[220:223], v[132:135], v[180:183], v[220:223]\n"
    "ds_write_b128 v7, v[208:211] offset:13312\n"
    "v_mfma_f32_16x16x32_bf16 v[216:219], v[112:115], v[184:187], v[216:219]\n"
    "s_add_u32 s60, s40, 146432\n"
    "s_addc_u32 s61, s41, 0\n"
    "v_mfma_f32_16x16x32_bf16 v[220:223], v[136:139], v[184:187], v[220:223]\n"
    "s_add_u32 s66, s60, 6144\n"
    "s_addc_u32 s67, s61, 0\n"
    "v_mfma_f32_16x16x32_bf16 v[216:219], v[116:119], v[188:191], v[216:219]\n"
    "global_load_dwordx4 v[56:59], v235, s[60:61] offset:-3072 sc1\n"
    "global_load_dwordx4 v[80:83], v235, s[66:67] offset:-3072 sc1\n"
    "v_mfma_f32_16x16x32_bf16 v[220:223], v[140:143], v[188:191], v[220:223]\n"
    "global_load_dwordx4 v[60:63], v235, s[60:61] offset:-2048 sc1\n"
    "global_load_dwordx4 v[84:87], v235, s[66:67] offset:-2048 sc1\n"
    "v_mfma_f32_16x16x32_bf16 v[216:219], v[120:123], v[192:195], v[216:219]\n"
    "global_load_dwordx4 v[64:67], v235, s[60:61] offset:-1024 sc1\n"
    "global_load_dwordx4 v[88:91], v235, s[66:67] offset:-1024 sc1\n"
    "v_mfma_f32_16x16x32_bf16 v[220:223], v[144:147], v[192:195], v[220:223]\n"
    "global_load_dwordx4 v[68:71], v235, s[60:61] offset:0 sc1\n"
    "global_load_dwordx4 v[92:95], v235, s[66:67] offset:0 sc1\n"
    "v_mfma_f32_16x16x32_bf16 v[216:219], v[124:127], v[196:199], v[216:219]\n"
    "global_load_dwordx4 v[72:75], v235, s[60:61] offset:1024 sc1\n"
    "global_load_dwordx4 v[96:99], v235, s[66:67] offset:1024 sc1\n"
    "v_mfma_f32_16x16x32_bf16 v[220:223], v[148:151], v[196:199], v[220:223]\n"
    "global_load_dwordx4 v[76:79], v235, s[60:61] offset:2048 sc1\n"
    "global_load_dwordx4 v[100:103], v235, s[66:67] offset:2048 sc1\n"
    "s_nop 2\n"
    "v_cvt_pk_bf16_f32 v216, v216, v217\n"
    "v_cvt_pk_bf16_f32 v217, v218, v219\n"
    "s_nop 0\n"
    "v_cvt_pk_bf16_f32 v218, v220, v221\n"
    "v_cvt_pk_bf16_f32 v219, v222, v223\n"
    "ds_write_b128 v7, v[216:219] offset:14336\n"
    "v_mov_b32 v227, 12\n"
    "ds_write_b32 v225, v227 offset:16\n"
    "s_branch .Lg_to\n"
    ".Lg25:\n"
    "s_sub_u32 s52, s52, 1\n"
    "s_cmp_lt_i32 s52, 0\n"
    "s_cbranch_scc1 .Lg25b\n"
    "s_sleep 1\n"
    "ds_read_b32 v229, v224 offset:0\n"
    "s_waitcnt lgkmcnt(0)\n"
    "v_cmp_gt_u32 vcc, 9, v229\n"
    "s_cbranch_vccnz .Lg25\n"
    "s_branch .Lg25b\n"
    ".Lg26:\n"
    "s_sub_u32 s52, s52, 1\n"
    "s_cmp_lt_i32 s52, 0\n"
    "s_cbranch_scc1 .Lg26b\n"
    "s_sleep 1\n"
    "ds_read_b32 v228, v224 offset:192\n"
    "s_waitcnt lgkmcnt(0)\n"
    "v_cmp_gt_u32 vcc, 3, v228\n"
    "s_cbranch_vccnz .Lg26\n"
    "s_branch .Lg26b\n"
    ".Lg27:\n"
    "s_sub_u32 s52, s52, 1\n"
    "s_cmp_lt_i32 s52, 0\n"
    "s_cbranch_scc1 .Lg27b\n"
    "s_sleep 1\n"
    "ds_read_b32 v228, v224 offset:208\n"
    "s_waitcnt lgkmcnt(0)\n"
    "v_cmp_gt_u32 vcc, 3, v228\n"
    "s_cbranch_vccnz .Lg27\n"
    "s_branch .Lg27b\n"
    ".Lg_e13a:\n"
    "s_setprio 2\n"
    "v_mfma_f32_16x16x32_bf16 v[200:203], v[8:11], v[152:155], 0\n"
    "v_mov_b32 v227, 12\n"
    "v_mfma_f32_16x16x32_bf16 v[204:207], v[32:35], v[152:155], 0\n"
    "v_cvt_pk_bf16_f32 v216, v216, v217\n"
    "v_cvt_pk_bf16_f32 v217, v218, v219\n"
    "v_mfma_f32_16x16x32_bf16 v[200:203], v[12:15], v[156:159], v[200:203]\n"
    "v_cvt_pk_bf16_f32 v218, v220, v221\n"
    "v_cvt_pk_bf16_f32 v219, v222, v223\n"
    "v_mfma_f32_16x16x32_bf16 v[204:207], v[36:39], v[156:159], v[204:207]\n"
    "ds_write_b128 v7, v[216:219] offset:14336\n"
    "v_mfma_f32_16x16x32_bf16 v[200:203], v[16:19], v[160:163], v[200:203]\n"
    "ds_write_b32 v225, v227 offset:16\n"
    "ds_read_b32 v228, v224 offset:208\n"
    "v_mfma_f32_16x16x32_bf16 v[204:207], v[40:43], v[160:163], v[204:207]\n"
    "s_waitcnt vmcnt(0)\n"
    "v_mov_b32 v227, 10\n"
    "v_mfma_f32_16x16x32_bf16 v[200:203], v[20:23], v[164:167], v[200:203]\n"
    "ds_write_b32 v226, v227 offset:0\n"
    "v_mfma_f32_16x16x32_bf16 v[204:207], v[44:47], v[164:167], v[204:207]\n"
    "v_mfma_f32_16x16x32_bf16 v[200:203], v[24:27], v[168:171], v[200:203]\n"
    "v_mfma_f32_16x16x32_bf16 v[204:207], v[48:51], v[168:171], v[204:207]\n"
    "v_mfma_f32_16x16x32_bf16 v[200:203], v[28:31], v[172:175], v[200:203]\n"
    "v_mfma_f32_16x16x32_bf16 v[204:207], v[52:55], v[172:175], v[204:207]\n"
    "v_mfma_f32_16x16x32_bf16 v[208:211], v[56:59], v[152:155], 0\n"
    "s_waitcnt lgkmcnt(1)\n"
    "v_cmp_gt_u32 vcc, 3, v228\n"
    "v_mfma_f32_16x16x32_bf16 v[212:215], v[80:83], v[152:155], 0\n"
    "v_mfma_f32_16x16x32_bf16 v[208:211], v[60:63], v[156:159], v[208:211]\n"
    "s_cbranch_vccnz .Lg27\n"
    ".Lg27b:\n"
    "v_mfma_f32_16x16x32_bf16 v[212:215], v[84:87], v[156:159], v[212:215]\n"
    "v_cvt_pk_bf16_f32 v200, v200, v201\n"
    "v_cvt_pk_bf16_f32 v201, v202, v203\n"
    "v_mfma_f32_16x16x32_bf16 v[208:211], v[64:67], v[160:163], v[208:211]\n"
    "v_cvt_pk_bf16_f32 v202, v204, v205\n"
    "v_cvt_pk_bf16_f32 v203, v206, v207\n"
    "v_mfma_f32_16x16x32_bf16 v[212:215], v[88:91], v[160:163], v[212:215]\n"
    "ds_write_b128 v7, v[200:203] offset:0\n"
    "v_mfma_f32_16x16x32_bf16 v[208:211], v[68:71], v[164:167], v[208:211]\n"
    "v_mfma_f32_16x16x32_bf16 v[212:215], v[92:95], v[164:167], v[212:215]\n"
    "v_mfma_f32_16x16x32_bf16 v[208:211], v[72:75], v[168:171], v[208:211]\n"
    "v_mfma_f32_16x16x32_bf16 v[212:215], v[96:99], v[168:171], v[212:215]\n"
    "v_mfma_f32_16x16x32_bf16 v[208:211], v[76:79], v[172:175], v[208:211]\n"
    "v_mfma_f32_16x16x32_bf16 v[212:215], v[100:103], v[172:175], v[212:215]\n"
    "v_mfma_f32_16x16x32_bf16 v[216:219], v[104:107], v[152:155], 0\n"
    "v_mfma_f32_16x16x32_bf16 v[220:223], v[128:131], v[152:155], 0\n"
    "v_mfma_f32_16x16x32_bf16 v[216:219], v[108:111], v[156:159], v[216:219]\n"
    "v_mfma_f32_16x16x32_bf16 v[220:223], v[132:135], v[156:159], v[220:223]\n"
    "v_mfma_f32_16x16x32_bf16 v[216:219], v[112:115], v[160:163], v[216:219]\n"
    "v_mfma_f32_16x16x32_bf16 v[220:223], v[136:139], v[160:163], v[220:223]\n"
    "v_mfma_f32_16x16x32_bf16 v[216:219], v[116:119], v[164:167], v[216:219]\n"
    "v_cvt_pk_bf16_f32 v208, v208, v209\n"
    "v_cvt_pk_bf16_f32 v209, v210, v211\n"
    "v_mfma_f32_16x16x32_bf16 v[220:223], v[140:143], v[164:167], v[220:223]\n"
    "v_cvt_pk_bf16_f32 v210, v212, v213\n"
    "v_cvt_pk_bf16_f32 v211, v214, v215\n"
    "v_mfma_f32_16x16x32_bf16 v[216:219], v[120:123], v[168:171], v[216:219]\n"
    "ds_write_b128 v7, v[208:211] offset:1024\n"
    "v_mfma_f32_16x16x32_bf16 v[220:223], v[144:147], v[168:171], v[220:223]\n"
    "v_mfma_f32_16x16x32_bf16 v[216:219], v[124:127], v[172:175], v[216:219]\n"
    "v_mfma_f32_16x16x32_bf16 v[220:223], v[148:151], v[172:175], v[220:223]\n"
    "s_nop 6\n"
    "v_cvt_pk_bf16_f32 v216, v216, v217\n"
    "v_cvt_pk_bf16_f32 v217, v218, v219\n"
    "v_cvt_pk_bf16_f32 v218, v220, v221\n"
    "v_cvt_pk_bf16_f32 v219, v222, v223\n"
    "ds_write_b128 v7, v[216:219] offset:2048\n"
    "v_mov_b32 v227, 13\n"
    "ds_write_b32 v225, v227 offset:16\n"
    "s_setprio 0\n"
    "s_branch .Lg_att\n"
    ".Lg28:\n"
    "s_sub_u32 s52, s52, 1\n"
    "s_cmp_lt_i32 s52, 0\n"
    "s_cbranch_scc1 .Lg28b\n"
    "s_sleep 1\n"
    "ds_read_b32 v228, v224 offset:208\n"
    "s_waitcnt lgkmcnt(0)\n"
    "v_cmp_gt_u32 vcc, 3, v228\n"
    "s_cbranch_vccnz .Lg28\n"
    "s_branch .Lg28b\n"
    ".Lg29:\n"
    "s_sub_u32 s52, s52, 1\n"
    "s_cmp_lt_i32 s52, 0\n"
    "s_cbranch_scc1 .Lg29b\n"
    "s_sleep 1\n"
    "ds_read_b32 v239, v224 offset:272\n"
    "s_waitcnt lgkmcnt(0)\n"
    "v_cmp_gt_u32 vcc, 1, v239\n"
    "s_cbranch_vccnz .Lg29\n"
    "s_branch .Lg29b\n"
    ".Lg_att:\n"
    "s_waitcnt vmcnt(0)\n"
    "v_mov_b32 v8, 0xff800000\n"
    "v_add_u32 v3, 124544, v235\n"
    "v_mov_b32 v6, v224\n"
    "v_lshrrev_b32 v9, 6, v235\n"
    "v_and_b32 v9, 12, v9\n"
    "v_lshlrev_b32 v9, 4, v9\n"
    "v_xor_b32 v4, v9, v235\n"
    "v_add_u32 v4, 124544, v4\n"
    "v_cmp_gt_u32 vcc, 64, v235\n"
    "v_add_u32 v7, 704, v224\n"
    "v_mov_b32 v9, 0x100000\n"
    "v_mov_b32 v0, v248\n"
    "v_cndmask_b32 v7, v9, v7, vcc\n"
    "v_mov_b32 v1, v249\n"
    "v_mov_b32 v2, v250\n"
    "v_and_b32 v5, 15, v251\n"
    "v_mul_u32_u24 v5, 0x1111, v5\n"
    "v_bfe_i32 v10, v251, 8, 1\n"
    "v_bfe_i32 v9, v251, 4, 1\n"
    "v_bfi_b32 v232, v9, 0, v8\n"
    "v_bfi_b32 v236, v10, v8, v232\n"
    "v_bfi_b32 v240, v10, v232, v8\n"
    "v_bfe_i32 v9, v251, 5, 1\n"
    "v_bfi_b32 v233, v9, 0, v8\n"
    "v_bfi_b32 v237, v10, v8, v233\n"
    "v_bfi_b32 v241, v10, v233, v8\n"
    "v_bfe_i32 v9, v251, 6, 1\n"
    "v_bfi_b32 v234, v9, 0, v8\n"
    "v_bfi_b32 v238, v10, v8, v234\n"
    "v_bfi_b32 v242, v10, v234, v8\n"
    "v_bfe_i32 v9, v251, 7, 1\n"
    "v_bfi_b32 v235, v9, 0, v8\n"
    "v_bfi_b32 v239, v10, v8, v235\n"
    "v_bfi_b32 v243, v10, v235, v8\n"
    "s_mov_b32 s45, 12\n"
    "s_mov_b32 s54, 10\n"
    "s_movk_i32 s60, 0x420c\n"
    "s_mov_b32 s61, 0\n"
    "s_mov_b32 s46, 0xbdd53b94\n"
    "s_mov_b32 s47, 0x3dd53b94\n"
    "s_mov_b32 s63, 161920\n"
    "s_mov_b32 s66, 0\n"
    "s_mov_b32 s56, -16\n"
    "s_branch .La_join\n"
    ".Lg30:\n"
    "s_sub_u32 s52, s52, 1\n"
    "s_cmp_lt_i32 s52, 0\n"
    "s_cbranch_scc1 .Lg30b\n"
    "s_sleep 1\n"
    "ds_read_b32 v228, v224 offset:128\n"
    "s_waitcnt lgkmcnt(0)\n"
    "v_cmp_gt_u32 vcc, 3, v228\n"
    "s_cbranch_vccnz .Lg30\n"
    "s_branch .Lg30b\n"
    ".Lg31:\n"
    "v_mfma_f32_16x16x32_bf16 v[208:211], v[56:59], v[152:155], 0\n"
    "v_mfma_f32_16x16x32_bf16 v[212:215], v[80:83], v[152:155], 0\n"
    "v_mfma_f32_16x16x32_bf16 v[208:211], v[60:63], v[156:159], v[208:211]\n"
    "v_mfma_f32_16x16x32_bf16 v[212:215], v[84:87], v[156:159], v[212:215]\n"
    "v_mul_f32 v200, v200, v252\n"
    "v_mul_f32 v201, v201, v252\n"
    "v_mfma_f32_16x16x32_bf16 v[208:211], v[64:67], v[160:163], v[208:211]\n"
    "v_mul_f32 v202, v202, v252\n"
    "v_mul_f32 v203, v203, v252\n"
    "v_mfma_f32_16x16x32_bf16 v[212:215], v[88:91], v[160:163], v[212:215]\n"
    "v_mul_f32 v204, v204, v252\n"
    "v_mul_f32 v205, v205, v252\n"
    "v_mfma_f32_16x16x32_bf16 v[208:211], v[68:71], v[164:167], v[208:211]\n"
    "v_mul_f32 v206, v206, v252\n"
    "v_mul_f32 v207, v207, v252\n"
    "v_mfma_f32_16x16x32_bf16 v[212:215], v[92:95], v[164:167], v[212:215]\n"
    "v_cvt_pk_bf16_f32 v200, v200, v201\n"
    "v_cvt_pk_bf16_f32 v201, v202, v203\n"
    "v_mfma_f32_16x16x32_bf16 v[208:211], v[72:75], v[168:171], v[208:211]\n"
    "v_cvt_pk_bf16_f32 v202, v204, v205\n"
    "v_cvt_pk_bf16_f32 v203, v206, v207\n"
    "v_mfma_f32_16x16x32_bf16 v[212:215], v[96:99], v[168:171], v[212:215]\n"
    "buffer_store_dwordx4 v[200:203], v248, s[80:83], 0 offen offset:128 sc1\n"
    "v_mfma_f32_16x16x32_bf16 v[208:211], v[76:79], v[172:175], v[208:211]\n"
    "v_mfma_f32_16x16x32_bf16 v[212:215], v[100:103], v[172:175], v[212:215]\n"
    ".Lg31r:\n"
    "s_sub_u32 s52, s52, 1\n"
    "s_cmp_lt_i32 s52, 0\n"
    "s_cbranch_scc1 .Lg31g\n"
    "s_sleep 1\n"
    "ds_read_b32 v228, v224 offset:144\n"
    "s_waitcnt lgkmcnt(0)\n"
    "v_cmp_gt_u32 vcc, 3, v228\n"
    "s_cbranch_vccnz .Lg31r\n"
    "s_branch .Lg31g\n"
    ".Lg31g:\n"
    "ds_read_b64_tr_b16 v[176:177], v0 offset:18432\n"
    "ds_read_b64_tr_b16 v[178:179], v1 offset:18432\n"
    "ds_read_b64_tr_b16 v[180:181], v0 offset:19456\n"
    "ds_read_b64_tr_b16 v[182:183], v1 offset:19456\n"
    "ds_read_b64_tr_b16 v[184:185], v0 offset:20480\n"
    "ds_read_b64_tr_b16 v[186:187], v1 offset:20480\n"
    "ds_read_b64_tr_b16 v[188:189], v0 offset:21504\n"
    "ds_read_b64_tr_b16 v[190:191], v1 offset:21504\n"
    "ds_read_b64_tr_b16 v[192:193], v0 offset:22528\n"
    "ds_read_b64_tr_b16 v[194:195], v1 offset:22528\n"
    "ds_read_b64_tr_b16 v[196:197], v0 offset:23552\n"
    "ds_read_b64_tr_b16 v[198:199], v1 offset:23552\n"
    "ds_read_b32 v253, v238 offset:0\n"
    "s_branch .Lg31j\n"
    ".Lg32:\n"
    "v_mfma_f32_16x16x32_bf16 v[208:211], v[56:59], v[176:179], 0\n"
    "v_mfma_f32_16x16x32_bf16 v[212:215], v[80:83], v[176:179], 0\n"
    "v_mfma_f32_16x16x32_bf16 v[208:211], v[60:63], v[180:183], v[208:211]\n"
    "v_mfma_f32_16x16x32_bf16 v[212:215], v[84:87], v[180:183], v[212:215]\n"
    "v_mul_f32 v200, v200, v253\n"
    "v_mul_f32 v201, v201, v253\n"
    "v_mfma_f32_16x16x32_bf16 v[208:211], v[64:67], v[184:187], v[208:211]\n"
    "v_mul_f32 v202, v202, v253\n"
    "v_mul_f32 v203, v203, v253\n"
    "v_mfma_f32_16x16x32_bf16 v[212:215], v[88:91], v[184:187], v[212:215]\n"
    "v_mul_f32 v204, v204, v253\n"
    "v_mul_f32 v205, v205, v253\n"
    "v_mfma_f32_16x16x32_bf16 v[208:211], v[68:71], v[188:191], v[208:211]\n"
    "v_mul_f32 v206, v206, v253\n"
    "v_mul_f32 v207, v207, v253\n"
    "v_mfma_f32_16x16x32_bf16 v[212:215], v[92:95], v[188:191], v[212:215]\n"
    "v_cvt_pk_bf16_f32 v200, v200, v201\n"
    "v_cvt_pk_bf16_f32 v201, v202, v203\n"
    "v_mfma_f32_16x16x32_bf16 v[208:211], v[72:75], v[192:195], v[208:211]\n"
    "v_cvt_pk_bf16_f32 v202, v204, v205\n"
    "v_cvt_pk_bf16_f32 v203, v206, v207\n"
    "v_mfma_f32_16x16x32_bf16 v[212:215], v[96:99], v[192:195], v[212:215]\n"
    "buffer_store_dwordx4 v[200:203], v249, s[80:83], 0 offen offset:128 sc1\n"
    "v_mfma_f32_16x16x32_bf16 v[208:211], v[76:79], v[196:199], v[208:211]\n"
    "v_mfma_f32_16x16x32_bf16 v[212:215], v[100:103], v[196:199], v[212:215]\n"
    ".Lg32r:\n"
    "s_sub_u32 s52, s52, 1\n"
    "s_cmp_lt_i32 s52, 0\n"
    "s_cbranch_scc1 .Lg32g\n"
    "s_sleep 1\n"
    "ds_read_b32 v228, v224 offset:160\n"
    "s_waitcnt lgkmcnt(0)\n"
    "v_cmp_gt_u32 vcc, 3, v228\n"
    "s_cbranch_vccnz .Lg32r\n"
    "s_branch .Lg32g\n"
    ".Lg32g:\n"
    "ds_read_b64_tr_b16 v[152:153], v0 offset:6144\n"
    "ds_read_b64_tr_b16 v[154:155], v1 offset:6144\n"
    "ds_read_b64_tr_b16 v[156:157], v0 offset:7168\n"
    "ds_read_b64_tr_b16 v[158:159], v1 offset:7168\n"
    "ds_read_b64_tr_b16 v[160:161], v0 offset:8192\n"
    "ds_read_b64_tr_b16 v[162:163], v1 offset:8192\n"
    "ds_read_b64_tr_b16 v[164:165], v0 offset:9216\n"
    "ds_read_b64_tr_b16 v[166:167], v1 offset:9216\n"
    "ds_read_b64_tr_b16 v[168:169], v0 offset:10240\n"
    "ds_read_b64_tr_b16 v[170:171], v1 offset:10240\n"
    "ds_read_b64_tr_b16 v[172:173], v0 offset:11264\n"
    "ds_read_b64_tr_b16 v[174:175], v1 offset:11264\n"
    "ds_read_b32 v252, v238 offset:256\n"
    "s_branch .Lg32j\n"
    ".Lg33:\n"
    "v_mfma_f32_16x16x32_bf16 v[208:211], v[56:59], v[152:155], 0\n"
    "v_mfma_f32_16x16x32_bf16 v[212:215], v[80:83], v[152:155], 0\n"
    "v_mfma_f32_16x16x32_bf16 v[208:211], v[60:63], v[156:159], v[208:211]\n"
    "v_mfma_f32_16x16x32_bf16 v[212:215], v[84:87], v[156:159], v[212:215]\n"
    "v_mul_f32 v200, v200, v252\n"
    "v_mul_f32 v201, v201, v252\n"
    "v_mfma_f32_16x16x32_bf16 v[208:211], v[64:67], v[160:163], v[208:211]\n"
    "v_mul_f32 v202, v202, v252\n"
    "v_mul_f32 v203, v203, v252\n"
    "v_mfma_f32_16x16x32_bf16 v[212:215], v[88:91], v[160:163], v[212:215]\n"
    "v_mul_f32 v204, v204, v252\n"
    "v_mul_f32 v205, v205, v252\n"
    "v_mfma_f32_16x16x32_bf16 v[208:211], v[68:71], v[164:167], v[208:211]\n"
    "v_mul_f32 v206, v206, v252\n"
    "v_mul_f32 v207, v207, v252\n"
    "v_mfma_f32_16x16x32_bf16 v[212:215], v[92:95], v[164:167], v[212:215]\n"
    "v_cvt_pk_bf16_f32 v200, v200, v201\n"
    "v_cvt_pk_bf16_f32 v201, v202, v203\n"
    "v_mfma_f32_16x16x32_bf16 v[208:211], v[72:75], v[168:171], v[208:211]\n"
    "v_cvt_pk_bf16_f32 v202, v204, v205\n"
    "v_cvt_pk_bf16_f32 v203, v206, v207\n"
    "v_mfma_f32_16x16x32_bf16 v[212:215], v[96:99], v[168:171], v[212:215]\n"
    "buffer_store_dwordx4 v[200:203], v248, s[80:83], 0 offen offset:128 sc1\n"
    "v_mfma_f32_16x16x32_bf16 v[208:211], v[76:79], v[172:175], v[208:211]\n"
    "v_mfma_f32_16x16x32_bf16 v[212:215], v[100:103], v[172:175], v[212:215]\n"
    ".Lg33r:\n"
    "s_sub_u32 s52, s52, 1\n"
    "s_cmp_lt_i32 s52, 0\n"
    "s_cbranch_scc1 .Lg33g\n"
    "s_sleep 1\n"
    "ds_read_b32 v228, v224 offset:176\n"
    "s_waitcnt lgkmcnt(0)\n"
    "v_cmp_gt_u32 vcc, 3, v228\n"
    "s_cbranch_vccnz .Lg33r\n"
    "s_branch .Lg33g\n"
    ".Lg33g:\n"
    "ds_read_b64_tr_b16 v[176:177], v0 offset:12288\n"
    "ds_read_b64_tr_b16 v[178:179], v1 offset:12288\n"
    "ds_read_b64_tr_b16 v[180:181], v0 offset:13312\n"
    "ds_read_b64_tr_b16 v[182:183], v1 offset:13312\n"
    "ds_read_b64_tr_b16 v[184:185], v0 offset:14336\n"
    "ds_read_b64_tr_b16 v[186:187], v1 offset:14336\n"
    "ds_read_b64_tr_b16 v[188:189], v0 offset:15360\n"
    "ds_read_b64_tr_b16 v[190:191], v1 offset:15360\n"
    "ds_read_b64_tr_b16 v[192:193], v0 offset:16384\n"
    "ds_read_b64_tr_b16 v[194:195], v1 offset:16384\n"
    "ds_read_b64_tr_b16 v[196:197], v0 offset:17408\n"
    "ds_read_b64_tr_b16 v[198:199], v1 offset:17408\n"
    "ds_read_b32 v253, v238 offset:320\n"
    "s_branch .Lg33j\n"
    ".Lg_t12:\n"
    "s_waitcnt lgkmcnt(1)\n"
    "v_mfma_f32_16x16x32_bf16 v[200:203], v[8:11], v[176:179], 0\n"
    "v_mul_f32 v208, v208, v252\n"
    "v_mul_f32 v209, v209, v252\n"
    "v_mfma_f32_16x16x32_bf16 v[204:207], v[32:35], v[176:179], 0\n"
    "v_mul_f32 v210, v210, v252\n"
    "v_mul_f32 v211, v211, v252\n"
    "v_mfma_f32_16x16x32_bf16 v[200:203], v[12:15], v[180:183], v[200:203]\n"
    "v_mul_f32 v212, v212, v252\n"
    "v_mul_f32 v213, v213, v252\n"
    "v_mfma_f32_16x16x32_bf16 v[204:207], v[36:39], v[180:183], v[204:207]\n"
    "v_mul_f32 v214, v214, v252\n"
    "v_mul_f32 v215, v215, v252\n"
    "v_mfma_f32_16x16x32_bf16 v[200:203], v[16:19], v[184:187], v[200:203]\n"
    "v_cvt_pk_bf16_f32 v208, v208, v209\n"
    "v_cvt_pk_bf16_f32 v209, v210, v211\n"
    "v_mfma_f32_16x16x32_bf16 v[204:207], v[40:43], v[184:187], v[204:207]\n"
    "v_cvt_pk_bf16_f32 v210, v212, v213\n"
    "v_cvt_pk_bf16_f32 v211, v214, v215\n"
    "v_mfma_f32_16x16x32_bf16 v[200:203], v[20:23], v[188:191], v[200:203]\n"
    "buffer_store_dwordx4 v[208:211], v248, s[80:83], 0 offen offset:320 sc1\n"
    "v_mfma_f32_16x16x32_bf16 v[204:207], v[44:47], v[188:191], v[204:207]\n"
    "v_mfma_f32_16x16x32_bf16 v[200:203], v[24:27], v[192:195], v[200:203]\n"
    "v_add_u32 v248, v255, v248\n"
    "v_mfma_f32_16x16x32_bf16 v[204:207], v[48:51], v[192:195], v[204:207]\n"
    "v_mfma_f32_16x16x32_bf16 v[200:203], v[28:31], v[196:199], v[200:203]\n"
    "v_mfma_f32_16x16x32_bf16 v[204:207], v[52:55], v[196:199], v[204:207]\n"
    "v_mfma_f32_16x16x32_bf16 v[208:211], v[56:59], v[176:179], 0\n"
    "v_mfma_f32_16x16x32_bf16 v[212:215], v[80:83], v[176:179], 0\n"
    "v_mfma_f32_16x16x32_bf16 v[208:211], v[60:63], v[180:183], v[208:211]\n"
    "v_mfma_f32_16x16x32_bf16 v[212:215], v[84:87], v[180:183], v[212:215]\n"
    "v_mfma_f32_16x16x32_bf16 v[208:211], v[64:67], v[184:187], v[208:211]\n"
    "v_mfma_f32_16x16x32_bf16 v[212:215], v[88:91], v[184:187], v[212:215]\n"
    "s_waitcnt lgkmcnt(0)\n"
    "v_mul_f32 v200, v200, v253\n"
    "v_mfma_f32_16x16x32_bf16 v[208:211], v[68:71], v[188:191], v[208:211]\n"
    "v_mul_f32 v201, v201, v253\n"
    "v_mul_f32 v202, v202, v253\n"
    "v_mfma_f32_16x16x32_bf16 v[212:215], v[92:95], v[188:191], v[212:215]\n"
    "v_mul_f32 v203, v203, v253\n"
    "v_mul_f32 v204, v204, v253\n"
    "v_mfma_f32_16x16x32_bf16 v[208:211], v[72:75], v[192:195], v[208:211]\n"
    "v_mul_f32 v205, v205, v253\n"
    "v_mul_f32 v206, v206, v253\n"
    "v_mfma_f32_16x16x32_bf16 v[212:215], v[96:99], v[192:195], v[212:215]\n"
    "v_mul_f32 v207, v207, v253\n"
    "v_cvt_pk_bf16_f32 v200, v200, v201\n"
    "v_mfma_f32_16x16x32_bf16 v[208:211], v[76:79], v[196:199], v[208:211]\n"
    "v_cvt_pk_bf16_f32 v201, v202, v203\n"
    "v_cvt_pk_bf16_f32 v202, v204, v205\n"
    "v_mfma_f32_16x16x32_bf16 v[212:215], v[100:103], v[196:199], v[212:215]\n"
    "v_cvt_pk_bf16_f32 v203, v206, v207\n"
    "buffer_store_dwordx4 v[200:203], v249, s[80:83], 0 offen offset:128 sc1\n"
    "s_nop 2\n"
    "v_mul_f32 v208, v208, v253\n"
    "v_mul_f32 v209, v209, v253\n"
    "v_mul_f32 v210, v210, v253\n"
    "v_mul_f32 v211, v211, v253\n"
    "v_mul_f32 v212, v212, v253\n"
    "v_mul_f32 v213, v213, v253\n"
    "v_mul_f32 v214, v214, v253\n"
    "v_mul_f32 v215, v215, v253\n"
    "v_cvt_pk_bf16_f32 v208, v208, v209\n"
    "v_cvt_pk_bf16_f32 v209, v210, v211\n"
    "v_cvt_pk_bf16_f32 v210, v212, v213\n"
    "v_cvt_pk_bf16_f32 v211, v214, v215\n"
    "buffer_store_dwordx4 v[208:211], v249, s[80:83], 0 offen offset:320 sc1\n"
    "s_branch .Lg_end\n"
    ".Lg34:\n"
    "v_mfma_f32_16x16x32_bf16 v[208:211], v[56:59], v[176:179], 0\n"
    "v_mfma_f32_16x16x32_bf16 v[212:215], v[80:83], v[176:179], 0\n"
    "v_mfma_f32_16x16x32_bf16 v[208:211], v[60:63], v[180:183], v[208:211]\n"
    "v_mfma_f32_16x16x32_bf16 v[212:215], v[84:87], v[180:183], v[212:215]\n"
    "v_mul_f32 v200, v200, v253\n"
    "v_mul_f32 v201, v201, v253\n"
    "v_mfma_f32_16x16x32_bf16 v[208:211], v[64:67], v[184:187], v[208:211]\n"
    "v_mul_f32 v202, v202, v253\n"
    "v_mul_f32 v203, v203, v253\n"
    "v_mfma_f32_16x16x32_bf16 v[212:215], v[88:91], v[184:187], v[212:215]\n"
    "v_mul_f32 v204, v204, v253\n"
    "v_mul_f32 v205, v205, v253\n"
    "v_mfma_f32_16x16x32_bf16 v[208:211], v[68:71], v[188:191], v[208:211]\n"
    "v_mul_f32 v206, v206, v253\n"
    "v_mul_f32 v207, v207, v253\n"
    "v_mfma_f32_16x16x32_bf16 v[212:215], v[92:95], v[188:191], v[212:215]\n"
    "v_cvt_pk_bf16_f32 v200, v200, v201\n"
    "v_cvt_pk_bf16_f32 v201, v202, v203\n"
    "v_mfma_f32_16x16x32_bf16 v[208:211], v[72:75], v[192:195], v[208:211]\n"
    "v_cvt_pk_bf16_f32 v202, v204, v205\n"
    "v_cvt_pk_bf16_f32 v203, v206, v207\n"
    "v_mfma_f32_16x16x32_bf16 v[212:215], v[96:99], v[192:195], v[212:215]\n"
    "buffer_store_dwordx4 v[200:203], v249, s[80:83], 0 offen offset:128 sc1\n"
    "v_mfma_f32_16x16x32_bf16 v[208:211], v[76:79], v[196:199], v[208:211]\n"
    "v_mfma_f32_16x16x32_bf16 v[212:215], v[100:103], v[196:199], v[212:215]\n"
    ".Lg34r:\n"
    "s_sub_u32 s52, s52, 1\n"
    "s_cmp_lt_i32 s52, 0\n"
    "s_cbranch_scc1 .Lg34g\n"
    "s_sleep 1\n"
    "ds_read_b32 v228, v224 offset:832\n"
    "s_waitcnt lgkmcnt(0)\n"
    "v_cmp_gt_u32 vcc, 4, v228\n"
    "s_cbranch_vccnz .Lg34r\n"
    "s_branch .Lg34g\n"
    ".Lg34g:\n"
    "ds_read_b64_tr_b16 v[152:153], v0 offset:0\n"
    "ds_read_b64_tr_b16 v[154:155], v1 offset:0\n"
    "ds_read_b64_tr_b16 v[156:157], v0 offset:1024\n"
    "ds_read_b64_tr_b16 v[158:159], v1 offset:1024\n"
    "ds_read_b64_tr_b16 v[160:161], v0 offset:2048\n"
    "ds_read_b64_tr_b16 v[162:163], v1 offset:2048\n"
    "ds_read_b64_tr_b16 v[164:165], v0 offset:3072\n"
    "ds_read_b64_tr_b16 v[166:167], v1 offset:3072\n"
    "ds_read_b64_tr_b16 v[168:169], v0 offset:4096\n"
    "ds_read_b64_tr_b16 v[170:171], v1 offset:4096\n"
    "ds_read_b64_tr_b16 v[172:173], v0 offset:5120\n"
    "ds_read_b64_tr_b16 v[174:175], v1 offset:5120\n"
    "ds_read_b32 v252, v238 offset:192\n"
    "s_branch .Lg34j\n"
    ".Lg_last:\n"
                :: "{v[0:1]}"(vz), "{v238}"(vi), "{v[248:251]}"(to), "{s[80:83]}"(srd), "{v[2:3]}"(va), "{v[6:7]}"(vb), "{v[224:227]}"(fl), "{v[230:233]}"(xo), "{v234}"(xo4), "{v235}"(lf[0]), "{v236}"(lf[1]),
                   "{s[40:41]}"(ws), "{s[42:43]}"(xb), "{s44}"(UNI(axis_at(rm, 14) * IROWB)), "{s45}"(UNI(axis_at(rm, 15) * IROWB)),
                   "{s46}"(UNI(axis_at(rm, 16) * IROWB)), "{s47}"(UNI(axis_at(rm, 17) * IROWB)), "{s48}"(UNI(axis_at(rm, t12 ? 4 : 18) * IROWB)),
                   "{s49}"(UNI(axis_at(rm, t12 ? 5 : 19) * IROWB)), "{s84}"(UNI(3200 * hs)), "{s85}"(UNI(2 * rm.d * IROWB)), "{v255}"(d56), "{s50}"(h), "{s58}"(UNI(t12)), "{s59}"(UNI(t0 + (bid & 4 ? 4500 : 5300)))
                : "memory", "v253", "s51", "s52", "s53", "s54", "s55", "s56", "s57", "s60", "s61", "s79", "s90", "s91", "s98", "s99", "s92", "s93", "s94", "s95", "vcc", "scc");
        } else {
            const unsigned r3 = axis_at(rm, 3 + e) * IROWB + 192 * h;
            const u32x4 vo = {r3 + c0, r3 + c1, dd[0], dd[1]};
            const u32x2 uu = {(unsigned)(pixad(3 + qj) + (3 + e) * RP + 16 * g), LQ + l16 + h * 3072 + 2048};
            u32x2 xp;
#pragma unroll
            for (int k = 0; k < 2; ++k) {
                const int i = 72 * h + min(lane + 64 * k, 71), r = i / 48, c = (i - 48 * r) / 3;
                xp[k] = axis_at(rm, 14 + r) * IROWB + axis_at(cm, 3 + c) * PIXB + 128 * (i - 48 * r - 3 * c);
            }
            u32x2 lf = {l16, (unsigned)(WS_FL + ((bid & (NREP - 1)) * 144 + 72 + nf) * 128)};
            asm volatile("s_nop 7\n"
    "s_mov_b32 s52, 0x40000\n"
    "s_load_dwordx2 s[78:79], s[40:41], 0x0\n"
    "s_mul_i32 s53, s50, 36864\n"
    "s_add_u32 s57, s53, 84992\n"
    "s_add_u32 s64, s40, s57\n"
    "s_addc_u32 s65, s41, 0\n"
    "s_add_u32 s57, s53, 91136\n"
    "s_add_u32 s66, s40, s57\n"
    "s_addc_u32 s67, s41, 0\n"
    "s_add_u32 s57, s53, 97280\n"
    "s_add_u32 s68, s40, s57\n"
    "s_addc_u32 s69, s41, 0\n"
    "s_add_u32 s57, s53, 103424\n"
    "s_add_u32 s70, s40, s57\n"
    "s_addc_u32 s71, s41, 0\n"
    "s_add_u32 s57, s53, 109568\n"
    "s_add_u32 s72, s40, s57\n"
    "s_addc_u32 s73, s41, 0\n"
    "s_add_u32 s57, s53, 115712\n"
    "s_add_u32 s74, s40, s57\n"
    "s_addc_u32 s75, s41, 0\n"
    "s_sub_u32 s88, s72, 73728\n"
    "s_subb_u32 s89, s73, 0\n"
    "s_sub_u32 s90, s74, 73728\n"
    "s_subb_u32 s91, s75, 0\n"
    "v_add_u32 v251, -6144, v236\n"
    "s_cmp_eq_u32 s50, 0\n"
    "s_cbranch_scc1 .Lh_x0\n"
    "v_mov_b32 v233, v234\n"
    ".Lh_x0:\n"
    "s_setprio 2\n"
    "s_waitcnt lgkmcnt(0)\n"
    "s_add_u32 s78, s78, 0x7f4a7c15\n"
    "s_add_u32 s79, s79, 0x9e3779b9\n"
    "v_mov_b64 v[240:241], s[78:79]\n"
    "s_mov_b32 s51, 0x4000\n"
    "ds_read_b32 v228, v224 offset:288\n"
    "s_waitcnt lgkmcnt(0)\n"
    "v_cmp_gt_u32 vcc, 1, v228\n"
    "s_cbranch_vccnz .Lh1\n"
    ".Lh1b:\n"
    ".Lh_d2:\n"
    "s_sleep 1\n"
    "s_memtime s[76:77]\n"
    "s_waitcnt lgkmcnt(0)\n"
    "s_sub_u32 s76, s76, s87\n"
    "s_cmp_lt_i32 s76, 0\n"
    "s_cbranch_scc1 .Lh_d2\n"
    "s_mov_b32 s51, 0x4000\n"
    "v_mov_b64 v[244:245], v[240:241]\n"
    "v_mov_b64 v[246:247], 0\n"
    "v_mov_b64 v[242:243], 0\n"
    "s_mov_b32 exec_hi, 15\n"
    "global_load_dwordx4 v[244:247], v236, s[40:41] sc1\n"
    "s_mov_b64 exec, -1\n"
    "global_load_dwordx4 v[8:11], v235, s[64:65] offset:-3072 sc1\n"
    "global_load_dwordx4 v[32:35], v235, s[66:67] offset:-3072 sc1\n"
    "global_load_dwordx4 v[12:15], v235, s[64:65] offset:-2048 sc1\n"
    "global_load_dwordx4 v[36:39], v235, s[66:67] offset:-2048 sc1\n"
    "global_load_dwordx4 v[16:19], v235, s[64:65] offset:-1024 sc1\n"
    "global_load_dwordx4 v[40:43], v235, s[66:67] offset:-1024 sc1\n"
    "global_load_dwordx4 v[20:23], v235, s[64:65] offset:0 sc1\n"
    "global_load_dwordx4 v[44:47], v235, s[66:67] offset:0 sc1\n"
    "global_load_dwordx4 v[24:27], v235, s[64:65] offset:1024 sc1\n"
    "global_load_dwordx4 v[48:51], v235, s[66:67] offset:1024 sc1\n"
    "global_load_dwordx4 v[28:31], v235, s[64:65] offset:2048 sc1\n"
    "global_load_dwordx4 v[52:55], v235, s[66:67] offset:2048 sc1\n"
    "s_waitcnt vmcnt(12)\n"
    "v_cmp_ne_u64 vcc, v[240:241], v[244:245]\n"
    "s_cbranch_vccnz .Lh_pl\n"
    "global_load_dwordx4 v[56:59], v235, s[68:69] offset:-3072 sc1\n"
    "global_load_dwordx4 v[80:83], v235, s[70:71] offset:-3072 sc1\n"
    "global_load_dwordx4 v[60:63], v235, s[68:69] offset:-2048 sc1\n"
    "global_load_dwordx4 v[84:87], v235, s[70:71] offset:-2048 sc1\n"
    "global_load_dwordx4 v[64:67], v235, s[68:69] offset:-1024 sc1\n"
    "global_load_dwordx4 v[88:91], v235, s[70:71] offset:-1024 sc1\n"
    "global_load_dwordx4 v[68:71], v235, s[68:69] offset:0 sc1\n"
    "global_load_dwordx4 v[92:95], v235, s[70:71] offset:0 sc1\n"
    "global_load_dwordx4 v[72:75], v235, s[68:69] offset:1024 sc1\n"
    "global_load_dwordx4 v[96:99], v235, s[70:71] offset:1024 sc1\n"
    "global_load_dwordx4 v[76:79], v235, s[68:69] offset:2048 sc1\n"
    "global_load_dwordx4 v[100:103], v235, s[70:71] offset:2048 sc1\n"
    "global_load_dwordx4 v[104:107], v235, s[72:73] offset:-3072 sc1\n"
    "global_load_dwordx4 v[128:131], v235, s[74:75] offset:-3072 sc1\n"
    "global_load_dwordx4 v[108:111], v235, s[72:73] offset:-2048 sc1\n"
    "global_load_dwordx4 v[132:135], v235, s[74:75] offset:-2048 sc1\n"
    "global_load_dwordx4 v[112:115], v235, s[72:73] offset:-1024 sc1\n"
    "global_load_dwordx4 v[136:139], v235, s[74:75] offset:-1024 sc1\n"
    "global_load_dwordx4 v[116:119], v235, s[72:73] offset:0 sc1\n"
    "global_load_dwordx4 v[140:143], v235, s[74:75] offset:0 sc1\n"
    "global_load_dwordx4 v[120:123], v235, s[72:73] offset:1024 sc1\n"
    "global_load_dwordx4 v[144:147], v235, s[74:75] offset:1024 sc1\n"
    "global_load_dwordx4 v[124:127], v235, s[72:73] offset:2048 sc1\n"
    "global_load_dwordx4 v[148:151], v235, s[74:75] offset:2048 sc1\n"
    ".Lh_rs:\n"
    "s_lshl_b32 s54, s50, 10\n"
    "s_add_u32 s54, s54, 1024\n"
    "s_mul_i32 s56, s50, 7776\n"
    "s_max_u32 s55, s56, 7168\n"
    "s_add_u32 s60, s42, s84\n"
    "s_addc_u32 s61, s43, 0\n"
    "s_add_u32 m0, s54, 106752\n"
    "s_nop 0\n"
    "global_load_lds_dwordx4 v230, s[60:61]\n"
    "s_add_u32 m0, s54, 108800\n"
    "s_nop 0\n"
    "global_load_lds_dwordx4 v231, s[60:61]\n"
    "s_add_u32 m0, s54, 110848\n"
    "s_nop 0\n"
    "global_load_lds_dwordx4 v232, s[60:61]\n"
    "s_add_u32 m0, s55, 106752\n"
    "s_nop 0\n"
    "global_load_lds_dwordx4 v233, s[60:61]\n"
    "s_add_u32 m0, s56, 106752\n"
    "s_nop 0\n"
    "global_load_lds_dwordx4 v234, s[60:61]\n"
    "s_add_u32 s60, s42, s85\n"
    "s_addc_u32 s61, s43, 0\n"
    "s_add_u32 m0, s54, 115648\n"
    "s_nop 0\n"
    "global_load_lds_dwordx4 v230, s[60:61]\n"
    "s_add_u32 m0, s54, 117696\n"
    "s_nop 0\n"
    "global_load_lds_dwordx4 v231, s[60:61]\n"
    "s_add_u32 m0, s54, 119744\n"
    "s_nop 0\n"
    "global_load_lds_dwordx4 v232, s[60:61]\n"
    "s_add_u32 m0, s55, 115648\n"
    "s_nop 0\n"
    "global_load_lds_dwordx4 v233, s[60:61]\n"
    "s_add_u32 m0, s56, 115648\n"
    "s_nop 0\n"
    "global_load_lds_dwordx4 v234, s[60:61]\n"
    "global_load_dword v250, v238, s[42:43]\n"
    "global_load_dword v250, v239, s[42:43]\n"
    "s_waitcnt vmcnt(44)\n"
    "v_add3_u32 v242, v242, v8, v11\n"
    "v_sub_u32 v8, v8, v240\n"
    "v_add3_u32 v242, v242, v32, v35\n"
    "v_sub_u32 v32, v32, v240\n"
    "v_add3_u32 v242, v242, v12, v15\n"
    "v_sub_u32 v12, v12, v240\n"
    "v_add3_u32 v242, v242, v36, v39\n"
    "v_sub_u32 v36, v36, v240\n"
    "s_waitcnt vmcnt(40)\n"
    "v_add3_u32 v242, v242, v16, v19\n"
    "v_sub_u32 v16, v16, v240\n"
    "v_add3_u32 v242, v242, v40, v43\n"
    "v_sub_u32 v40, v40, v240\n"
    "v_add3_u32 v242, v242, v20, v23\n"
    "v_sub_u32 v20, v20, v240\n"
    "v_add3_u32 v242, v242, v44, v47\n"
    "v_sub_u32 v44, v44, v240\n"
    "s_waitcnt vmcnt(36)\n"
    "v_add3_u32 v242, v242, v24, v27\n"
    "v_sub_u32 v24, v24, v240\n"
    "v_add3_u32 v242, v242, v48, v51\n"
    "v_sub_u32 v48, v48, v240\n"
    "v_add3_u32 v242, v242, v28, v31\n"
    "v_sub_u32 v28, v28, v240\n"
    "v_add3_u32 v242, v242, v52, v55\n"
    "v_sub_u32 v52, v52, v240\n"
    "s_waitcnt vmcnt(32)\n"
    "v_add3_u32 v242, v242, v56, v59\n"
    "v_sub_u32 v56, v56, v240\n"
    "v_add3_u32 v242, v242, v80, v83\n"
    "v_sub_u32 v80, v80, v240\n"
    "v_add3_u32 v242, v242, v60, v63\n"
    "v_sub_u32 v60, v60, v240\n"
    "v_add3_u32 v242, v242, v84, v87\n"
    "v_sub_u32 v84, v84, v240\n"
    "s_waitcnt vmcnt(28)\n"
    "v_add3_u32 v242, v242, v64, v67\n"
    "v_sub_u32 v64, v64, v240\n"
    "v_add3_u32 v242, v242, v88, v91\n"
    "v_sub_u32 v88, v88, v240\n"
    "v_add3_u32 v242, v242, v68, v71\n"
    "v_sub_u32 v68, v68, v240\n"
    "v_add3_u32 v242, v242, v92, v95\n"
    "v_sub_u32 v92, v92, v240\n"
    "s_waitcnt vmcnt(24)\n"
    "v_add3_u32 v242, v242, v72, v75\n"
    "v_sub_u32 v72, v72, v240\n"
    "v_add3_u32 v242, v242, v96, v99\n"
    "v_sub_u32 v96, v96, v240\n"
    "v_add3_u32 v242, v242, v76, v79\n"
    "v_sub_u32 v76, v76, v240\n"
    "v_add3_u32 v242, v242, v100, v103\n"
    "v_sub_u32 v100, v100, v240\n"
    "s_waitcnt vmcnt(20)\n"
    "v_add3_u32 v242, v242, v104, v107\n"
    "v_sub_u32 v104, v104, v240\n"
    "v_add3_u32 v242, v242, v128, v131\n"
    "v_sub_u32 v128, v128, v240\n"
    "v_add3_u32 v242, v242, v108, v111\n"
    "v_sub_u32 v108, v108, v240\n"
    "v_add3_u32 v242, v242, v132, v135\n"
    "v_sub_u32 v132, v132, v240\n"
    "s_waitcnt vmcnt(16)\n"
    "v_add3_u32 v242, v242, v112, v115\n"
    "v_sub_u32 v112, v112, v240\n"
    "v_add3_u32 v242, v242, v136, v139\n"
    "v_sub_u32 v136, v136, v240\n"
    "v_add3_u32 v242, v242, v116, v119\n"
    "v_sub_u32 v116, v116, v240\n"
    "v_add3_u32 v242, v242, v140, v143\n"
    "v_sub_u32 v140, v140, v240\n"
    "s_waitcnt vmcnt(12)\n"
    "v_add3_u32 v242, v242, v120, v123\n"
    "v_sub_u32 v120, v120, v240\n"
    "v_add3_u32 v242, v242, v144, v147\n"
    "v_sub_u32 v144, v144, v240\n"
    "v_add3_u32 v242, v242, v124, v127\n"
    "v_sub_u32 v124, v124, v240\n"
    "v_add3_u32 v242, v242, v148, v151\n"
    "v_sub_u32 v148, v148, v240\n"
    ".Lh_ce:\n"
    "v_cmp_ne_u64 s[76:77], v[240:241], v[244:245]\n"
    "v_sub_u32 v242, v242, v246\n"
    "v_mov_b32 v243, v242\n"
    "s_nop 1\n"
    "v_permlane32_swap_b32 v242, v243\n"
    "v_add_u32 v242, v242, v243\n"
    "s_nop 1\n"
    "v_add_u32_dpp v242, v242, v242 row_ror:8 row_mask:0xf bank_mask:0xf\n"
    "s_nop 1\n"
    "v_add_u32_dpp v242, v242, v242 row_ror:4 row_mask:0xf bank_mask:0xf\n"
    "s_nop 1\n"
    "v_add_u32_dpp v242, v242, v242 row_ror:2 row_mask:0xf bank_mask:0xf\n"
    "s_nop 1\n"
    "v_add_u32_dpp v242, v242, v242 row_ror:1 row_mask:0xf bank_mask:0xf\n"
    "v_mov_b32 v243, v242\n"
    "s_nop 1\n"
    "v_permlane16_swap_b32 v242, v243\n"
    "v_add_u32 v242, v242, v243\n"
    "v_cmp_ne_u32 vcc, 0, v242\n"
    "s_nop 1\n"
    "s_or_b64 vcc, vcc, s[76:77]\n"
    "s_cbranch_scc1 .Lh_re\n"
    ".Lh_ok:\n"
    "v_mov_b32 v227, 1\n"
    "ds_write_b32 v225, v227 offset:272\n"
    "ds_read_b32 v228, v224 offset:0\n"
    "s_waitcnt lgkmcnt(0)\n"
    "v_cmp_gt_u32 vcc, 6, v228\n"
    "s_cbranch_vccnz .Lh2\n"
    ".Lh2b:\n"
    "s_waitcnt vmcnt(2)\n"
    "v_mov_b32 v227, 99\n"
    "ds_write_b32 v226, v227 offset:0\n"
    "s_setprio 0\n"
    "ds_read_b32 v228, v224 offset:128\n"
    "s_waitcnt lgkmcnt(0)\n"
    "v_cmp_gt_u32 vcc, 1, v228\n"
    "s_cbranch_vccnz .Lh3\n"
    ".Lh3b:\n"
    "ds_read_b64_tr_b16 v[152:153], v2 offset:18432\n"
    "ds_read_b64_tr_b16 v[154:155], v3 offset:18432\n"
    "ds_read_b64_tr_b16 v[156:157], v2 offset:19456\n"
    "ds_read_b64_tr_b16 v[158:159], v3 offset:19456\n"
    "ds_read_b64_tr_b16 v[160:161], v2 offset:20480\n"
    "ds_read_b64_tr_b16 v[162:163], v3 offset:20480\n"
    "ds_read_b64_tr_b16 v[164:165], v2 offset:21504\n"
    "ds_read_b64_tr_b16 v[166:167], v3 offset:21504\n"
    "ds_read_b64_tr_b16 v[168:169], v2 offset:22528\n"
    "ds_read_b64_tr_b16 v[170:171], v3 offset:22528\n"
    "ds_read_b64_tr_b16 v[172:173], v2 offset:23552\n"
    "ds_read_b64_tr_b16 v[174:175], v3 offset:23552\n"
    "ds_read_b32 v252, v1 offset:0\n"
    "v_mov_b32 v227, 1\n"
    "ds_write_b32 v226, v227 offset:32\n"
    "v_mov_b32 v242, v240\n"
    "v_mov_b32 v243, v241\n"
    "v_and_b32 v229, 0x70, v235\n"
    "v_add_u32 v229, s48, v229\n"
    "global_store_dwordx4 v229, v[240:243], s[40:41] sc1\n"
    "s_waitcnt lgkmcnt(10)\n"
    "v_mfma_f32_16x16x32_bf16 v[200:203], v[8:11], v[152:155], 0\n"
    "v_mfma_f32_16x16x32_bf16 v[204:207], v[32:35], v[152:155], 0\n"
    "v_mfma_f32_16x16x32_bf16 v[200:203], v[12:15], v[156:159], v[200:203]\n"
    "v_mfma_f32_16x16x32_bf16 v[204:207], v[36:39], v[156:159], v[204:207]\n"
    "s_waitcnt lgkmcnt(8)\n"
    "v_mfma_f32_16x16x32_bf16 v[200:203], v[16:19], v[160:163], v[200:203]\n"
    "v_mfma_f32_16x16x32_bf16 v[204:207], v[40:43], v[160:163], v[204:207]\n"
    "s_waitcnt lgkmcnt(6)\n"
    "v_mfma_f32_16x16x32_bf16 v[200:203], v[20:23], v[164:167], v[200:203]\n"
    "v_mfma_f32_16x16x32_bf16 v[204:207], v[44:47], v[164:167], v[204:207]\n"
    "s_waitcnt lgkmcnt(4)\n"
    "v_mfma_f32_16x16x32_bf16 v[200:203], v[24:27], v[168:171], v[200:203]\n"
    "v_mfma_f32_16x16x32_bf16 v[204:207], v[48:51], v[168:171], v[204:207]\n"
    "s_waitcnt lgkmcnt(2)\n"
    "v_mfma_f32_16x16x32_bf16 v[200:203], v[28:31], v[172:175], v[200:203]\n"
    "v_mfma_f32_16x16x32_bf16 v[204:207], v[52:55], v[172:175], v[204:207]\n"
    "v_mfma_f32_16x16x32_bf16 v[208:211], v[56:59], v[152:155], 0\n"
    "v_mfma_f32_16x16x32_bf16 v[212:215], v[80:83], v[152:155], 0\n"
    "ds_read_b32 v228, v224 offset:144\n"
    "v_mfma_f32_16x16x32_bf16 v[208:211], v[60:63], v[156:159], v[208:211]\n"
    "v_mfma_f32_16x16x32_bf16 v[212:215], v[84:87], v[156:159], v[212:215]\n"
    "v_mfma_f32_16x16x32_bf16 v[208:211], v[64:67], v[160:163], v[208:211]\n"
    "s_waitcnt lgkmcnt(2)\n"
    "v_mul_f32 v200, v200, v252\n"
    "v_mfma_f32_16x16x32_bf16 v[212:215], v[88:91], v[160:163], v[212:215]\n"
    "v_mul_f32 v201, v201, v252\n"
    "v_mul_f32 v202, v202, v252\n"
    "v_mfma_f32_16x16x32_bf16 v[208:211], v[68:71], v[164:167], v[208:211]\n"
    "v_mul_f32 v203, v203, v252\n"
    "v_mul_f32 v204, v204, v252\n"
    "v_mfma_f32_16x16x32_bf16 v[212:215], v[92:95], v[164:167], v[212:215]\n"
    "v_mul_f32 v205, v205, v252\n"
    "v_mul_f32 v206, v206, v252\n"
    "v_mfma_f32_16x16x32_bf16 v[208:211], v[72:75], v[168:171], v[208:211]\n"
    "v_mul_f32 v207, v207, v252\n"
    "v_cvt_pk_bf16_f32 v200, v200, v201\n"
    "v_mfma_f32_16x16x32_bf16 v[212:215], v[96:99], v[168:171], v[212:215]\n"
    "v_cvt_pk_bf16_f32 v201, v202, v203\n"
    "v_cvt_pk_bf16_f32 v202, v204, v205\n"
    "v_mfma_f32_16x16x32_bf16 v[208:211], v[76:79], v[172:175], v[208:211]\n"
    "s_waitcnt lgkmcnt(0)\n"
    "v_cmp_gt_u32 vcc, 1, v228\n"
    "v_mfma_f32_16x16x32_bf16 v[212:215], v[100:103], v[172:175], v[212:215]\n"
    "v_cvt_pk_bf16_f32 v203, v206, v207\n"
    "s_cbranch_vccnz .Lh4\n"
    "v_mfma_f32_16x16x32_bf16 v[216:219], v[104:107], v[152:155], 0\n"
    "ds_read_b64_tr_b16 v[176:177], v2 offset:24576\n"
    "ds_read_b64_tr_b16 v[178:179], v3 offset:24576\n"
    "v_mfma_f32_16x16x32_bf16 v[220:223], v[128:131], v[152:155], 0\n"
    "ds_read_b64_tr_b16 v[180:181], v2 offset:25600\n"
    "ds_read_b64_tr_b16 v[182:183], v3 offset:25600\n"
    "v_mfma_f32_16x16x32_bf16 v[216:219], v[108:111], v[156:159], v[216:219]\n"
    "ds_read_b64_tr_b16 v[184:185], v2 offset:26624\n"
    "ds_read_b64_tr_b16 v[186:187], v3 offset:26624\n"
    "v_mfma_f32_16x16x32_bf16 v[220:223], v[132:135], v[156:159], v[220:223]\n"
    "ds_read_b64_tr_b16 v[188:189], v2 offset:27648\n"
    "ds_read_b64_tr_b16 v[190:191], v3 offset:27648\n"
    "v_mfma_f32_16x16x32_bf16 v[216:219], v[112:115], v[160:163], v[216:219]\n"
    "buffer_store_dwordx4 v[200:203], v4, s[80:83], 0 offen offset:0 sc1\n"
    "v_mfma_f32_16x16x32_bf16 v[220:223], v[136:139], v[160:163], v[220:223]\n"
    "v_mul_f32 v208, v208, v252\n"
    "v_mul_f32 v209, v209, v252\n"
    "v_mfma_f32_16x16x32_bf16 v[216:219], v[116:119], v[164:167], v[216:219]\n"
    "ds_read_b64_tr_b16 v[192:193], v2 offset:28672\n"
    "ds_read_b64_tr_b16 v[194:195], v3 offset:28672\n"
    "v_mfma_f32_16x16x32_bf16 v[220:223], v[140:143], v[164:167], v[220:223]\n"
    "v_mul_f32 v210, v210, v252\n"
    "v_mul_f32 v211, v211, v252\n"
    "v_mfma_f32_16x16x32_bf16 v[216:219], v[120:123], v[168:171], v[216:219]\n"
    "ds_read_b64_tr_b16 v[196:197], v2 offset:29696\n"
    "ds_read_b64_tr_b16 v[198:199], v3 offset:29696\n"
    "v_mfma_f32_16x16x32_bf16 v[220:223], v[144:147], v[168:171], v[220:223]\n"
    "v_mul_f32 v212, v212, v252\n"
    "v_mov_b32 v227, 2\n"
    "v_mfma_f32_16x16x32_bf16 v[216:219], v[124:127], v[172:175], v[216:219]\n"
    "ds_read_b32 v253, v1 offset:64\n"
    "v_mul_f32 v213, v213, v252\n"
    "v_mfma_f32_16x16x32_bf16 v[220:223], v[148:151], v[172:175], v[220:223]\n"
    "v_mul_f32 v214, v214, v252\n"
    "v_mul_f32 v215, v215, v252\n"
    "ds_write_b32 v226, v227 offset:32\n"
    "v_cvt_pk_bf16_f32 v208, v208, v209\n"
    "v_cvt_pk_bf16_f32 v209, v210, v211\n"
    "v_cvt_pk_bf16_f32 v210, v212, v213\n"
    "v_cvt_pk_bf16_f32 v211, v214, v215\n"
    "buffer_store_dwordx4 v[208:211], v4, s[80:83], 0 offen offset:64 sc1\n"
    ".Lh4j:\n"
    "s_waitcnt lgkmcnt(2)\n"
    "v_mfma_f32_16x16x32_bf16 v[200:203], v[8:11], v[176:179], 0\n"
    "v_mul_f32 v216, v216, v252\n"
    "v_mul_f32 v217, v217, v252\n"
    "v_mfma_f32_16x16x32_bf16 v[204:207], v[32:35], v[176:179], 0\n"
    "v_mul_f32 v218, v218, v252\n"
    "v_mul_f32 v219, v219, v252\n"
    "v_mfma_f32_16x16x32_bf16 v[200:203], v[12:15], v[180:183], v[200:203]\n"
    "v_mul_f32 v220, v220, v252\n"
    "v_mul_f32 v221, v221, v252\n"
    "v_mfma_f32_16x16x32_bf16 v[204:207], v[36:39], v[180:183], v[204:207]\n"
    "v_mul_f32 v222, v222, v252\n"
    "v_mul_f32 v223, v223, v252\n"
    "v_mfma_f32_16x16x32_bf16 v[200:203], v[16:19], v[184:187], v[200:203]\n"
    "v_cvt_pk_bf16_f32 v216, v216, v217\n"
    "v_cvt_pk_bf16_f32 v217, v218, v219\n"
    "v_mfma_f32_16x16x32_bf16 v[204:207], v[40:43], v[184:187], v[204:207]\n"
    "v_cvt_pk_bf16_f32 v218, v220, v221\n"
    "v_cvt_pk_bf16_f32 v219, v222, v223\n"
    "v_mfma_f32_16x16x32_bf16 v[200:203], v[20:23], v[188:191], v[200:203]\n"
    "buffer_store_dwordx4 v[216:219], v4, s[80:83], 0 offen offset:128 sc1\n"
    "v_mfma_f32_16x16x32_bf16 v[204:207], v[44:47], v[188:191], v[204:207]\n"
    "v_mfma_f32_16x16x32_bf16 v[200:203], v[24:27], v[192:195], v[200:203]\n"
    "v_add_u32 v4, s46, v4\n"
    "v_mfma_f32_16x16x32_bf16 v[204:207], v[48:51], v[192:195], v[204:207]\n"
    "v_mfma_f32_16x16x32_bf16 v[200:203], v[28:31], v[196:199], v[200:203]\n"
    "v_mfma_f32_16x16x32_bf16 v[204:207], v[52:55], v[196:199], v[204:207]\n"
    "v_mfma_f32_16x16x32_bf16 v[208:211], v[56:59], v[176:179], 0\n"
    "v_mfma_f32_16x16x32_bf16 v[212:215], v[80:83], v[176:179], 0\n"
    "ds_read_b32 v228, v224 offset:160\n"
    "v_mfma_f32_16x16x32_bf16 v[208:211], v[60:63], v[180:183], v[208:211]\n"
    "v_mfma_f32_16x16x32_bf16 v[212:215], v[84:87], v[180:183], v[212:215]\n"
    "v_mfma_f32_16x16x32_bf16 v[208:211], v[64:67], v[184:187], v[208:211]\n"
    "s_waitcnt lgkmcnt(2)\n"
    "v_mul_f32 v200, v200, v253\n"
    "v_mfma_f32_16x16x32_bf16 v[212:215], v[88:91], v[184:187], v[212:215]\n"
    "v_mul_f32 v201, v201, v253\n"
    "v_mul_f32 v202, v202, v253\n"
    "v_mfma_f32_16x16x32_bf16 v[208:211], v[68:71], v[188:191], v[208:211]\n"
    "v_mul_f32 v203, v203, v253\n"
    "v_mul_f32 v204, v204, v253\n"
    "v_mfma_f32_16x16x32_bf16 v[212:215], v[92:95], v[188:191], v[212:215]\n"
    "v_mul_f32 v205, v205, v253\n"
    "v_mul_f32 v206, v206, v253\n"
    "v_mfma_f32_16x16x32_bf16 v[208:211], v[72:75], v[192:195], v[208:211]\n"
    "v_mul_f32 v207, v207, v253\n"
    "v_cvt_pk_bf16_f32 v200, v200, v201\n"
    "v_mfma_f32_16x16x32_bf16 v[212:215], v[96:99], v[192:195], v[212:215]\n"
    "v_cvt_pk_bf16_f32 v201, v202, v203\n"
    "v_cvt_pk_bf16_f32 v202, v204, v205\n"
    "v_mfma_f32_16x16x32_bf16 v[208:211], v[76:79], v[196:199], v[208:211]\n"
    "s_waitcnt lgkmcnt(0)\n"
    "v_cmp_gt_u32 vcc, 1, v228\n"
    "v_mfma_f32_16x16x32_bf16 v[212:215], v[100:103], v[196:199], v[212:215]\n"
    "v_cvt_pk_bf16_f32 v203, v206, v207\n"
    "s_cbranch_vccnz .Lh5\n"
    "v_mfma_f32_16x16x32_bf16 v[216:219], v[104:107], v[176:179], 0\n"
    "ds_read_b64_tr_b16 v[152:153], v2 offset:30720\n"
    "ds_read_b64_tr_b16 v[154:155], v3 offset:30720\n"
    "v_mfma_f32_16x16x32_bf16 v[220:223], v[128:131], v[176:179], 0\n"
    "ds_read_b64_tr_b16 v[156:157], v2 offset:31744\n"
    "ds_read_b64_tr_b16 v[158:159], v3 offset:31744\n"
    "v_mfma_f32_16x16x32_bf16 v[216:219], v[108:111], v[180:183], v[216:219]\n"
    "ds_read_b64_tr_b16 v[160:161], v2 offset:32768\n"
    "ds_read_b64_tr_b16 v[162:163], v3 offset:32768\n"
    "v_mfma_f32_16x16x32_bf16 v[220:223], v[132:135], v[180:183], v[220:223]\n"
    "ds_read_b64_tr_b16 v[164:165], v2 offset:33792\n"
    "ds_read_b64_tr_b16 v[166:167], v3 offset:33792\n"
    "v_mfma_f32_16x16x32_bf16 v[216:219], v[112:115], v[184:187], v[216:219]\n"
    "buffer_store_dwordx4 v[200:203], v5, s[80:83], 0 offen offset:0 sc1\n"
    "v_mfma_f32_16x16x32_bf16 v[220:223], v[136:139], v[184:187], v[220:223]\n"
    "v_mul_f32 v208, v208, v253\n"
    "v_mul_f32 v209, v209, v253\n"
    "v_mfma_f32_16x16x32_bf16 v[216:219], v[116:119], v[188:191], v[216:219]\n"
    "ds_read_b64_tr_b16 v[168:169], v2 offset:34816\n"
    "ds_read_b64_tr_b16 v[170:171], v3 offset:34816\n"
    "v_mfma_f32_16x16x32_bf16 v[220:223], v[140:143], v[188:191], v[220:223]\n"
    "v_mul_f32 v210, v210, v253\n"
    "v_mul_f32 v211, v211, v253\n"
    "v_mfma_f32_16x16x32_bf16 v[216:219], v[120:123], v[192:195], v[216:219]\n"
    "ds_read_b64_tr_b16 v[172:173], v2 offset:35840\n"
    "ds_read_b64_tr_b16 v[174:175], v3 offset:35840\n"
    "v_mfma_f32_16x16x32_bf16 v[220:223], v[144:147], v[192:195], v[220:223]\n"
    "v_mul_f32 v212, v212, v253\n"
    "v_mov_b32 v227, 3\n"
    "v_mfma_f32_16x16x32_bf16 v[216:219], v[124:127], v[196:199], v[216:219]\n"
    "ds_read_b32 v252, v1 offset:128\n"
    "v_mul_f32 v213, v213, v253\n"
    "v_mfma_f32_16x16x32_bf16 v[220:223], v[148:151], v[196:199], v[220:223]\n"
    "v_mul_f32 v214, v214, v253\n"
    "v_mul_f32 v215, v215, v253\n"
    "ds_write_b32 v226, v227 offset:32\n"
    "v_cvt_pk_bf16_f32 v208, v208, v209\n"
    "v_cvt_pk_bf16_f32 v209, v210, v211\n"
    "v_cvt_pk_bf16_f32 v210, v212, v213\n"
    "v_cvt_pk_bf16_f32 v211, v214, v215\n"
    "buffer_store_dwordx4 v[208:211], v5, s[80:83], 0 offen offset:64 sc1\n"
    ".Lh5j:\n"
    "s_waitcnt lgkmcnt(2)\n"
    "v_mfma_f32_16x16x32_bf16 v[200:203], v[8:11], v[152:155], 0\n"
    "v_mul_f32 v216, v216, v253\n"
    "v_mul_f32 v217, v217, v253\n"
    "v_mfma_f32_16x16x32_bf16 v[204:207], v[32:35], v[152:155], 0\n"
    "v_mul_f32 v218, v218, v253\n"
    "v_mul_f32 v219, v219, v253\n"
    "v_mfma_f32_16x16x32_bf16 v[200:203], v[12:15], v[156:159], v[200:203]\n"
    "v_mul_f32 v220, v220, v253\n"
    "v_mul_f32 v221, v221, v253\n"
    "v_mfma_f32_16x16x32_bf16 v[204:207], v[36:39], v[156:159], v[204:207]\n"
    "v_mul_f32 v222, v222, v253\n"
    "v_mul_f32 v223, v223, v253\n"
    "v_mfma_f32_16x16x32_bf16 v[200:203], v[16:19], v[160:163], v[200:203]\n"
    "v_cvt_pk_bf16_f32 v216, v216, v217\n"
    "v_cvt_pk_bf16_f32 v217, v218, v219\n"
    "v_mfma_f32_16x16x32_bf16 v[204:207], v[40:43], v[160:163], v[204:207]\n"
    "v_cvt_pk_bf16_f32 v218, v220, v221\n"
    "v_cvt_pk_bf16_f32 v219, v222, v223\n"
    "v_mfma_f32_16x16x32_bf16 v[200:203], v[20:23], v[164:167], v[200:203]\n"
    "buffer_store_dwordx4 v[216:219], v5, s[80:83], 0 offen offset:128 sc1\n"
    "v_mfma_f32_16x16x32_bf16 v[204:207], v[44:47], v[164:167], v[204:207]\n"
    "v_mfma_f32_16x16x32_bf16 v[200:203], v[24:27], v[168:171], v[200:203]\n"
    "v_add_u32 v5, s46, v5\n"
    "v_mfma_f32_16x16x32_bf16 v[204:207], v[48:51], v[168:171], v[204:207]\n"
    "v_mfma_f32_16x16x32_bf16 v[200:203], v[28:31], v[172:175], v[200:203]\n"
    "v_mfma_f32_16x16x32_bf16 v[204:207], v[52:55], v[172:175], v[204:207]\n"
    "v_mfma_f32_16x16x32_bf16 v[208:211], v[56:59], v[152:155], 0\n"
    "v_mfma_f32_16x16x32_bf16 v[212:215], v[80:83], v[152:155], 0\n"
    "ds_read_b32 v228, v224 offset:176\n"
    "v_mfma_f32_16x16x32_bf16 v[208:211], v[60:63], v[156:159], v[208:211]\n"
    "v_mfma_f32_16x16x32_bf16 v[212:215], v[84:87], v[156:159], v[212:215]\n"
    "v_mfma_f32_16x16x32_bf16 v[208:211], v[64:67], v[160:163], v[208:211]\n"
    "s_waitcnt lgkmcnt(2)\n"
    "v_mul_f32 v200, v200, v252\n"
    "v_mfma_f32_16x16x32_bf16 v[212:215], v[88:91], v[160:163], v[212:215]\n"
    "v_mul_f32 v201, v201, v252\n"
    "v_mul_f32 v202, v202, v252\n"
    "v_mfma_f32_16x16x32_bf16 v[208:211], v[68:71], v[164:167], v[208:211]\n"
    "v_mul_f32 v203, v203, v252\n"
    "v_mul_f32 v204, v204, v252\n"
    "v_mfma_f32_16x16x32_bf16 v[212:215], v[92:95], v[164:167], v[212:215]\n"
    "v_mul_f32 v205, v205, v252\n"
    "v_mul_f32 v206, v206, v252\n"
    "v_mfma_f32_16x16x32_bf16 v[208:211], v[72:75], v[168:171], v[208:211]\n"
    "v_mul_f32 v207, v207, v252\n"
    "v_cvt_pk_bf16_f32 v200, v200, v201\n"
    "v_mfma_f32_16x16x32_bf16 v[212:215], v[96:99], v[168:171], v[212:215]\n"
    "v_cvt_pk_bf16_f32 v201, v202, v203\n"
    "v_cvt_pk_bf16_f32 v202, v204, v205\n"
    "v_mfma_f32_16x16x32_bf16 v[208:211], v[76:79], v[172:175], v[208:211]\n"
    "s_waitcnt lgkmcnt(0)\n"
    "v_cmp_gt_u32 vcc, 1, v228\n"
    "v_mfma_f32_16x16x32_bf16 v[212:215], v[100:103], v[172:175], v[212:215]\n"
    "v_cvt_pk_bf16_f32 v203, v206, v207\n"
    "s_cbranch_vccnz .Lh6\n"
    "v_mfma_f32_16x16x32_bf16 v[216:219], v[104:107], v[152:155], 0\n"
    "ds_read_b64_tr_b16 v[176:177], v2 offset:18432\n"
    "ds_read_b64_tr_b16 v[178:179], v3 offset:18432\n"
    "v_mfma_f32_16x16x32_bf16 v[220:223], v[128:131], v[152:155], 0\n"
    "ds_read_b64_tr_b16 v[180:181], v2 offset:19456\n"
    "ds_read_b64_tr_b16 v[182:183], v3 offset:19456\n"
    "v_mfma_f32_16x16x32_bf16 v[216:219], v[108:111], v[156:159], v[216:219]\n"
    "ds_read_b64_tr_b16 v[184:185], v2 offset:20480\n"
    "ds_read_b64_tr_b16 v[186:187], v3 offset:20480\n"
    "v_mfma_f32_16x16x32_bf16 v[220:223], v[132:135], v[156:159], v[220:223]\n"
    "ds_read_b64_tr_b16 v[188:189], v2 offset:21504\n"
    "ds_read_b64_tr_b16 v[190:191], v3 offset:21504\n"
    "v_mfma_f32_16x16x32_bf16 v[216:219], v[112:115], v[160:163], v[216:219]\n"
    "buffer_store_dwordx4 v[200:203], v4, s[80:83], 0 offen offset:0 sc1\n"
    "v_mfma_f32_16x16x32_bf16 v[220:223], v[136:139], v[160:163], v[220:223]\n"
    "v_mul_f32 v208, v208, v252\n"
    "v_mul_f32 v209, v209, v252\n"
    "v_mfma_f32_16x16x32_bf16 v[216:219], v[116:119], v[164:167], v[216:219]\n"
    "ds_read_b64_tr_b16 v[192:193], v2 offset:22528\n"
    "ds_read_b64_tr_b16 v[194:195], v3 offset:22528\n"
    "v_mfma_f32_16x16x32_bf16 v[220:223], v[140:143], v[164:167], v[220:223]\n"
    "v_mul_f32 v210, v210, v252\n"
    "v_mul_f32 v211, v211, v252\n"
    "v_mfma_f32_16x16x32_bf16 v[216:219], v[120:123], v[168:171], v[216:219]\n"
    "ds_read_b64_tr_b16 v[196:197], v2 offset:23552\n"
    "ds_read_b64_tr_b16 v[198:199], v3 offset:23552\n"
    "v_mfma_f32_16x16x32_bf16 v[220:223], v[144:147], v[168:171], v[220:223]\n"
    "v_mul_f32 v212, v212, v252\n"
    "v_mov_b32 v227, 4\n"
    "v_mfma_f32_16x16x32_bf16 v[216:219], v[124:127], v[172:175], v[216:219]\n"
    "ds_read_b32 v253, v1 offset:0\n"
    "v_mul_f32 v213, v213, v252\n"
    "v_mfma_f32_16x16x32_bf16 v[220:223], v[148:151], v[172:175], v[220:223]\n"
    "v_mul_f32 v214, v214, v252\n"
    "v_mul_f32 v215, v215, v252\n"
    "ds_write_b32 v226, v227 offset:32\n"
    "v_cvt_pk_bf16_f32 v208, v208, v209\n"
    "v_cvt_pk_bf16_f32 v209, v210, v211\n"
    "v_cvt_pk_bf16_f32 v210, v212, v213\n"
    "v_cvt_pk_bf16_f32 v211, v214, v215\n"
    "buffer_store_dwordx4 v[208:211], v4, s[80:83], 0 offen offset:64 sc1\n"
    ".Lh6j:\n"
    "s_waitcnt lgkmcnt(2)\n"
    "v_mfma_f32_16x16x32_bf16 v[200:203], v[8:11], v[176:179], 0\n"
    "v_mul_f32 v216, v216, v252\n"
    "v_mul_f32 v217, v217, v252\n"
    "v_mfma_f32_16x16x32_bf16 v[204:207], v[32:35], v[176:179], 0\n"
    "v_mul_f32 v218, v218, v252\n"
    "v_mul_f32 v219, v219, v252\n"
    "v_mfma_f32_16x16x32_bf16 v[200:203], v[12:15], v[180:183], v[200:203]\n"
    "v_mul_f32 v220, v220, v252\n"
    "v_mul_f32 v221, v221, v252\n"
    "v_mfma_f32_16x16x32_bf16 v[204:207], v[36:39], v[180:183], v[204:207]\n"
    "v_mul_f32 v222, v222, v252\n"
    "v_mul_f32 v223, v223, v252\n"
    "v_mfma_f32_16x16x32_bf16 v[200:203], v[16:19], v[184:187], v[200:203]\n"
    "v_cvt_pk_bf16_f32 v216, v216, v217\n"
    "v_cvt_pk_bf16_f32 v217, v218, v219\n"
    "v_mfma_f32_16x16x32_bf16 v[204:207], v[40:43], v[184:187], v[204:207]\n"
    "v_cvt_pk_bf16_f32 v218, v220, v221\n"
    "v_cvt_pk_bf16_f32 v219, v222, v223\n"
    "v_mfma_f32_16x16x32_bf16 v[200:203], v[20:23], v[188:191], v[200:203]\n"
    "buffer_store_dwordx4 v[216:219], v4, s[80:83], 0 offen offset:128 sc1\n"
    "v_mfma_f32_16x16x32_bf16 v[204:207], v[44:47], v[188:191], v[204:207]\n"
    "v_mfma_f32_16x16x32_bf16 v[200:203], v[24:27], v[192:195], v[200:203]\n"
    "v_add_u32 v4, s46, v4\n"
    "v_mfma_f32_16x16x32_bf16 v[204:207], v[48:51], v[192:195], v[204:207]\n"
    "v_mfma_f32_16x16x32_bf16 v[200:203], v[28:31], v[196:199], v[200:203]\n"
    "v_mfma_f32_16x16x32_bf16 v[204:207], v[52:55], v[196:199], v[204:207]\n"
    "v_mfma_f32_16x16x32_bf16 v[208:211], v[56:59], v[176:179], 0\n"
    "v_mfma_f32_16x16x32_bf16 v[212:215], v[80:83], v[176:179], 0\n"
    "ds_read_b32 v228, v224 offset:128\n"
    "v_mfma_f32_16x16x32_bf16 v[208:211], v[60:63], v[180:183], v[208:211]\n"
    "v_mfma_f32_16x16x32_bf16 v[212:215], v[84:87], v[180:183], v[212:215]\n"
    "v_mfma_f32_16x16x32_bf16 v[208:211], v[64:67], v[184:187], v[208:211]\n"
    "s_waitcnt lgkmcnt(2)\n"
    "v_mul_f32 v200, v200, v253\n"
    "v_mfma_f32_16x16x32_bf16 v[212:215], v[88:91], v[184:187], v[212:215]\n"
    "v_mul_f32 v201, v201, v253\n"
    "v_mul_f32 v202, v202, v253\n"
    "v_mfma_f32_16x16x32_bf16 v[208:211], v[68:71], v[188:191], v[208:211]\n"
    "v_mul_f32 v203, v203, v253\n"
    "v_mul_f32 v204, v204, v253\n"
    "v_mfma_f32_16x16x32_bf16 v[212:215], v[92:95], v[188:191], v[212:215]\n"
    "v_mul_f32 v205, v205, v253\n"
    "v_mul_f32 v206, v206, v253\n"
    "v_mfma_f32_16x16x32_bf16 v[208:211], v[72:75], v[192:195], v[208:211]\n"
    "v_mul_f32 v207, v207, v253\n"
    "v_cvt_pk_bf16_f32 v200, v200, v201\n"
    "v_mfma_f32_16x16x32_bf16 v[212:215], v[96:99], v[192:195], v[212:215]\n"
    "v_cvt_pk_bf16_f32 v201, v202, v203\n"
    "v_cvt_pk_bf16_f32 v202, v204, v205\n"
    "v_mfma_f32_16x16x32_bf16 v[208:211], v[76:79], v[196:199], v[208:211]\n"
    "s_waitcnt lgkmcnt(0)\n"
    "v_cmp_gt_u32 vcc, 2, v228\n"
    "v_mfma_f32_16x16x32_bf16 v[212:215], v[100:103], v[196:199], v[212:215]\n"
    "v_cvt_pk_bf16_f32 v203, v206, v207\n"
    "s_cbranch_vccnz .Lh7\n"
    "v_mfma_f32_16x16x32_bf16 v[216:219], v[104:107], v[176:179], 0\n"
    "ds_read_b64_tr_b16 v[152:153], v2 offset:24576\n"
    "ds_read_b64_tr_b16 v[154:155], v3 offset:24576\n"
    "v_mfma_f32_16x16x32_bf16 v[220:223], v[128:131], v[176:179], 0\n"
    "ds_read_b64_tr_b16 v[156:157], v2 offset:25600\n"
    "ds_read_b64_tr_b16 v[158:159], v3 offset:25600\n"
    "v_mfma_f32_16x16x32_bf16 v[216:219], v[108:111], v[180:183], v[216:219]\n"
    "ds_read_b64_tr_b16 v[160:161], v2 offset:26624\n"
    "ds_read_b64_tr_b16 v[162:163], v3 offset:26624\n"
    "v_mfma_f32_16x16x32_bf16 v[220:223], v[132:135], v[180:183], v[220:223]\n"
    "ds_read_b64_tr_b16 v[164:165], v2 offset:27648\n"
    "ds_read_b64_tr_b16 v[166:167], v3 offset:27648\n"
    "v_mfma_f32_16x16x32_bf16 v[216:219], v[112:115], v[184:187], v[216:219]\n"
    "buffer_store_dwordx4 v[200:203], v5, s[80:83], 0 offen offset:0 sc1\n"
    "v_mfma_f32_16x16x32_bf16 v[220:223], v[136:139], v[184:187], v[220:223]\n"
    "v_mul_f32 v208, v208, v253\n"
    "v_mul_f32 v209, v209, v253\n"
    "v_mfma_f32_16x16x32_bf16 v[216:219], v[116:119], v[188:191], v[216:219]\n"
    "ds_read_b64_tr_b16 v[168:169], v2 offset:28672\n"
    "ds_read_b64_tr_b16 v[170:171], v3 offset:28672\n"
    "v_mfma_f32_16x16x32_bf16 v[220:223], v[140:143], v[188:191], v[220:223]\n"
    "v_mul_f32 v210, v210, v253\n"
    "v_mul_f32 v211, v211, v253\n"
    "v_mfma_f32_16x16x32_bf16 v[216:219], v[120:123], v[192:195], v[216:219]\n"
    "ds_read_b64_tr_b16 v[172:173], v2 offset:29696\n"
    "ds_read_b64_tr_b16 v[174:175], v3 offset:29696\n"
    "v_mfma_f32_16x16x32_bf16 v[220:223], v[144:147], v[192:195], v[220:223]\n"
    "v_mul_f32 v212, v212, v253\n"
    "v_mov_b32 v227, 5\n"
    "v_mfma_f32_16x16x32_bf16 v[216:219], v[124:127], v[196:199], v[216:219]\n"
    "ds_read_b32 v252, v1 offset:64\n"
    "v_mul_f32 v213, v213, v253\n"
    "v_mfma_f32_16x16x32_bf16 v[220:223], v[148:151], v[196:199], v[220:223]\n"
    "v_mul_f32 v214, v214, v253\n"
    "v_mul_f32 v215, v215, v253\n"
    "ds_write_b32 v226, v227 offset:32\n"
    "v_cvt_pk_bf16_f32 v208, v208, v209\n"
    "v_cvt_pk_bf16_f32 v209, v210, v211\n"
    "v_cvt_pk_bf16_f32 v210, v212, v213\n"
    "v_cvt_pk_bf16_f32 v211, v214, v215\n"
    "buffer_store_dwordx4 v[208:211], v5, s[80:83], 0 offen offset:64 sc1\n"
    ".Lh7j:\n"
    "s_waitcnt lgkmcnt(2)\n"
    "v_mfma_f32_16x16x32_bf16 v[200:203], v[8:11], v[152:155], 0\n"
    "v_mul_f32 v216, v216, v253\n"
    "v_mul_f32 v217, v217, v253\n"
    "v_mfma_f32_16x16x32_bf16 v[204:207], v[32:35], v[152:155], 0\n"
    "v_mul_f32 v218, v218, v253\n"
    "v_mul_f32 v219, v219, v253\n"
    "v_mfma_f32_16x16x32_bf16 v[200:203], v[12:15], v[156:159], v[200:203]\n"
    "v_mul_f32 v220, v220, v253\n"
    "v_mul_f32 v221, v221, v253\n"
    "v_mfma_f32_16x16x32_bf16 v[204:207], v[36:39], v[156:159], v[204:207]\n"
    "v_mul_f32 v222, v222, v253\n"
    "v_mul_f32 v223, v223, v253\n"
    "v_mfma_f32_16x16x32_bf16 v[200:203], v[16:19], v[160:163], v[200:203]\n"
    "v_cvt_pk_bf16_f32 v216, v216, v217\n"
    "v_cvt_pk_bf16_f32 v217, v218, v219\n"
    "v_mfma_f32_16x16x32_bf16 v[204:207], v[40:43], v[160:163], v[204:207]\n"
    "v_cvt_pk_bf16_f32 v218, v220, v221\n"
    "v_cvt_pk_bf16_f32 v219, v222, v223\n"
    "v_mfma_f32_16x16x32_bf16 v[200:203], v[20:23], v[164:167], v[200:203]\n"
    "buffer_store_dwordx4 v[216:219], v5, s[80:83], 0 offen offset:128 sc1\n"
    "v_mfma_f32_16x16x32_bf16 v[204:207], v[44:47], v[164:167], v[204:207]\n"
    "v_mfma_f32_16x16x32_bf16 v[200:203], v[24:27], v[168:171], v[200:203]\n"
    "v_add_u32 v5, s46, v5\n"
    "v_mfma_f32_16x16x32_bf16 v[204:207], v[48:51], v[168:171], v[204:207]\n"
    "v_mfma_f32_16x16x32_bf16 v[200:203], v[28:31], v[172:175], v[200:203]\n"
    "v_mfma_f32_16x16x32_bf16 v[204:207], v[52:55], v[172:175], v[204:207]\n"
    "v_mfma_f32_16x16x32_bf16 v[208:211], v[56:59], v[152:155], 0\n"
    "v_mfma_f32_16x16x32_bf16 v[212:215], v[80:83], v[152:155], 0\n"
    "ds_read_b32 v228, v224 offset:144\n"
    "v_mfma_f32_16x16x32_bf16 v[208:211], v[60:63], v[156:159], v[208:211]\n"
    "v_mfma_f32_16x16x32_bf16 v[212:215], v[84:87], v[156:159], v[212:215]\n"
    "v_mfma_f32_16x16x32_bf16 v[208:211], v[64:67], v[160:163], v[208:211]\n"
    "s_waitcnt lgkmcnt(2)\n"
    "v_mul_f32 v200, v200, v252\n"
    "v_mfma_f32_16x16x32_bf16 v[212:215], v[88:91], v[160:163], v[212:215]\n"
    "v_mul_f32 v201, v201, v252\n"
    "v_mul_f32 v202, v202, v252\n"
    "v_mfma_f32_16x16x32_bf16 v[208:211], v[68:71], v[164:167], v[208:211]\n"
    "v_mul_f32 v203, v203, v252\n"
    "v_mul_f32 v204, v204, v252\n"
    "v_mfma_f32_16x16x32_bf16 v[212:215], v[92:95], v[164:167], v[212:215]\n"
    "v_mul_f32 v205, v205, v252\n"
    "v_mul_f32 v206, v206, v252\n"
    "v_mfma_f32_16x16x32_bf16 v[208:211], v[72:75], v[168:171], v[208:211]\n"
    "v_mul_f32 v207, v207, v252\n"
    "v_cvt_pk_bf16_f32 v200, v200, v201\n"
    "v_mfma_f32_16x16x32_bf16 v[212:215], v[96:99], v[168:171], v[212:215]\n"
    "v_cvt_pk_bf16_f32 v201, v202, v203\n"
    "v_cvt_pk_bf16_f32 v202, v204, v205\n"
    "v_mfma_f32_16x16x32_bf16 v[208:211], v[76:79], v[172:175], v[208:211]\n"
    "s_waitcnt lgkmcnt(0)\n"
    "v_cmp_gt_u32 vcc, 2, v228\n"
    "v_mfma_f32_16x16x32_bf16 v[212:215], v[100:103], v[172:175], v[212:215]\n"
    "v_cvt_pk_bf16_f32 v203, v206, v207\n"
    "s_cbranch_vccnz .Lh8\n"
    "v_mfma_f32_16x16x32_bf16 v[216:219], v[104:107], v[152:155], 0\n"
    "ds_read_b64_tr_b16 v[176:177], v2 offset:30720\n"
    "ds_read_b64_tr_b16 v[178:179], v3 offset:30720\n"
    "v_mfma_f32_16x16x32_bf16 v[220:223], v[128:131], v[152:155], 0\n"
    "ds_read_b64_tr_b16 v[180:181], v2 offset:31744\n"
    "ds_read_b64_tr_b16 v[182:183], v3 offset:31744\n"
    "v_mfma_f32_16x16x32_bf16 v[216:219], v[108:111], v[156:159], v[216:219]\n"
    "ds_read_b64_tr_b16 v[184:185], v2 offset:32768\n"
    "ds_read_b64_tr_b16 v[186:187], v3 offset:32768\n"
    "v_mfma_f32_16x16x32_bf16 v[220:223], v[132:135], v[156:159], v[220:223]\n"
    "ds_read_b64_tr_b16 v[188:189], v2 offset:33792\n"
    "ds_read_b64_tr_b16 v[190:191], v3 offset:33792\n"
    "v_mfma_f32_16x16x32_bf16 v[216:219], v[112:115], v[160:163], v[216:219]\n"
    "buffer_store_dwordx4 v[200:203], v4, s[80:83], 0 offen offset:0 sc1\n"
    "v_mfma_f32_16x16x32_bf16 v[220:223], v[136:139], v[160:163], v[220:223]\n"
    "v_mul_f32 v208, v208, v252\n"
    "v_mul_f32 v209, v209, v252\n"
    "v_mfma_f32_16x16x32_bf16 v[216:219], v[116:119], v[164:167], v[216:219]\n"
    "ds_read_b64_tr_b16 v[192:193], v2 offset:34816\n"
    "ds_read_b64_tr_b16 v[194:195], v3 offset:34816\n"
    "v_mfma_f32_16x16x32_bf16 v[220:223], v[140:143], v[164:167], v[220:223]\n"
    "v_mul_f32 v210, v210, v252\n"
    "v_mul_f32 v211, v211, v252\n"
    "v_mfma_f32_16x16x32_bf16 v[216:219], v[120:123], v[168:171], v[216:219]\n"
    "ds_read_b64_tr_b16 v[196:197], v2 offset:35840\n"
    "ds_read_b64_tr_b16 v[198:199], v3 offset:35840\n"
    "v_mfma_f32_16x16x32_bf16 v[220:223], v[144:147], v[168:171], v[220:223]\n"
    "v_mul_f32 v212, v212, v252\n"
    "v_mov_b32 v227, 6\n"
    "v_mfma_f32_16x16x32_bf16 v[216:219], v[124:127], v[172:175], v[216:219]\n"
    "ds_read_b32 v253, v1 offset:128\n"
    "v_mul_f32 v213, v213, v252\n"
    "v_mfma_f32_16x16x32_bf16 v[220:223], v[148:151], v[172:175], v[220:223]\n"
    "v_mul_f32 v214, v214, v252\n"
    "v_mul_f32 v215, v215, v252\n"
    "ds_write_b32 v226, v227 offset:32\n"
    "v_cvt_pk_bf16_f32 v208, v208, v209\n"
    "v_cvt_pk_bf16_f32 v209, v210, v211\n"
    "v_cvt_pk_bf16_f32 v210, v212, v213\n"
    "v_cvt_pk_bf16_f32 v211, v214, v215\n"
    "buffer_store_dwordx4 v[208:211], v4, s[80:83], 0 offen offset:64 sc1\n"
    ".Lh8j:\n"
    "s_waitcnt lgkmcnt(2)\n"
    "v_mfma_f32_16x16x32_bf16 v[200:203], v[8:11], v[176:179], 0\n"
    "v_mul_f32 v216, v216, v252\n"
    "v_mul_f32 v217, v217, v252\n"
    "v_mfma_f32_16x16x32_bf16 v[204:207], v[32:35], v[176:179], 0\n"
    "v_mul_f32 v218, v218, v252\n"
    "v_mul_f32 v219, v219, v252\n"
    "v_mfma_f32_16x16x32_bf16 v[200:203], v[12:15], v[180:183], v[200:203]\n"
    "v_mul_f32 v220, v220, v252\n"
    "v_mul_f32 v221, v221, v252\n"
    "v_mfma_f32_16x16x32_bf16 v[204:207], v[36:39], v[180:183], v[204:207]\n"
    "v_mul_f32 v222, v222, v252\n"
    "v_mul_f32 v223, v223, v252\n"
    "v_mfma_f32_16x16x32_bf16 v[200:203], v[16:19], v[184:187], v[200:203]\n"
    "v_cvt_pk_bf16_f32 v216, v216, v217\n"
    "v_cvt_pk_bf16_f32 v217, v218, v219\n"
    "v_mfma_f32_16x16x32_bf16 v[204:207], v[40:43], v[184:187], v[204:207]\n"
    "v_cvt_pk_bf16_f32 v218, v220, v221\n"
    "v_cvt_pk_bf16_f32 v219, v222, v223\n"
    "v_mfma_f32_16x16x32_bf16 v[200:203], v[20:23], v[188:191], v[200:203]\n"
    "buffer_store_dwordx4 v[216:219], v4, s[80:83], 0 offen offset:128 sc1\n"
    "v_mfma_f32_16x16x32_bf16 v[204:207], v[44:47], v[188:191], v[204:207]\n"
    "v_mfma_f32_16x16x32_bf16 v[200:203], v[24:27], v[192:195], v[200:203]\n"
    "v_add_u32 v4, s46, v4\n"
    "v_mfma_f32_16x16x32_bf16 v[204:207], v[48:51], v[192:195], v[204:207]\n"
    "v_mfma_f32_16x16x32_bf16 v[200:203], v[28:31], v[196:199], v[200:203]\n"
    "v_mfma_f32_16x16x32_bf16 v[204:207], v[52:55], v[196:199], v[204:207]\n"
    "v_mfma_f32_16x16x32_bf16 v[208:211], v[56:59], v[176:179], 0\n"
    "v_mfma_f32_16x16x32_bf16 v[212:215], v[80:83], v[176:179], 0\n"
    "ds_read_b32 v228, v224 offset:160\n"
    "v_mfma_f32_16x16x32_bf16 v[208:211], v[60:63], v[180:183], v[208:211]\n"
    "v_mfma_f32_16x16x32_bf16 v[212:215], v[84:87], v[180:183], v[212:215]\n"
    "v_mfma_f32_16x16x32_bf16 v[208:211], v[64:67], v[184:187], v[208:211]\n"
    "s_waitcnt lgkmcnt(2)\n"
    "v_mul_f32 v200, v200, v253\n"
    "v_mfma_f32_16x16x32_bf16 v[212:215], v[88:91], v[184:187], v[212:215]\n"
    "v_mul_f32 v201, v201, v253\n"
    "v_mul_f32 v202, v202, v253\n"
    "v_mfma_f32_16x16x32_bf16 v[208:211], v[68:71], v[188:191], v[208:211]\n"
    "v_mul_f32 v203, v203, v253\n"
    "v_mul_f32 v204, v204, v253\n"
    "v_mfma_f32_16x16x32_bf16 v[212:215], v[92:95], v[188:191], v[212:215]\n"
    "v_mul_f32 v205, v205, v253\n"
    "v_mul_f32 v206, v206, v253\n"
    "v_mfma_f32_16x16x32_bf16 v[208:211], v[72:75], v[192:195], v[208:211]\n"
    "v_mul_f32 v207, v207, v253\n"
    "v_cvt_pk_bf16_f32 v200, v200, v201\n"
    "v_mfma_f32_16x16x32_bf16 v[212:215], v[96:99], v[192:195], v[212:215]\n"
    "v_cvt_pk_bf16_f32 v201, v202, v203\n"
    "v_cvt_pk_bf16_f32 v202, v204, v205\n"
    "v_mfma_f32_16x16x32_bf16 v[208:211], v[76:79], v[196:199], v[208:211]\n"
    "s_waitcnt lgkmcnt(0)\n"
    "v_cmp_gt_u32 vcc, 2, v228\n"
    "v_mfma_f32_16x16x32_bf16 v[212:215], v[100:103], v[196:199], v[212:215]\n"
    "v_cvt_pk_bf16_f32 v203, v206, v207\n"
    "s_cbranch_vccnz .Lh9\n"
    "v_mfma_f32_16x16x32_bf16 v[216:219], v[104:107], v[176:179], 0\n"
    "ds_read_b64_tr_b16 v[152:153], v2 offset:18432\n"
    "ds_read_b64_tr_b16 v[154:155], v3 offset:18432\n"
    "v_mfma_f32_16x16x32_bf16 v[220:223], v[128:131], v[176:179], 0\n"
    "ds_read_b64_tr_b16 v[156:157], v2 offset:19456\n"
    "ds_read_b64_tr_b16 v[158:159], v3 offset:19456\n"
    "v_mfma_f32_16x16x32_bf16 v[216:219], v[108:111], v[180:183], v[216:219]\n"
    "ds_read_b64_tr_b16 v[160:161], v2 offset:20480\n"
    "ds_read_b64_tr_b16 v[162:163], v3 offset:20480\n"
    "v_mfma_f32_16x16x32_bf16 v[220:223], v[132:135], v[180:183], v[220:223]\n"
    "ds_read_b64_tr_b16 v[164:165], v2 offset:21504\n"
    "ds_read_b64_tr_b16 v[166:167], v3 offset:21504\n"
    "v_mfma_f32_16x16x32_bf16 v[216:219], v[112:115], v[184:187], v[216:219]\n"
    "buffer_store_dwordx4 v[200:203], v5, s[80:83], 0 offen offset:0 sc1\n"
    "v_mfma_f32_16x16x32_bf16 v[220:223], v[136:139], v[184:187], v[220:223]\n"
    "v_mul_f32 v208, v208, v253\n"
    "v_mul_f32 v209, v209, v253\n"
    "v_mfma_f32_16x16x32_bf16 v[216:219], v[116:119], v[188:191], v[216:219]\n"
    "ds_read_b64_tr_b16 v[168:169], v2 offset:22528\n"
    "ds_read_b64_tr_b16 v[170:171], v3 offset:22528\n"
    "v_mfma_f32_16x16x32_bf16 v[220:223], v[140:143], v[188:191], v[220:223]\n"
    "v_mul_f32 v210, v210, v253\n"
    "v_mul_f32 v211, v211, v253\n"
    "v_mfma_f32_16x16x32_bf16 v[216:219], v[120:123], v[192:195], v[216:219]\n"
    "ds_read_b64_tr_b16 v[172:173], v2 offset:23552\n"
    "ds_read_b64_tr_b16 v[174:175], v3 offset:23552\n"
    "v_mfma_f32_16x16x32_bf16 v[220:223], v[144:147], v[192:195], v[220:223]\n"
    "v_mul_f32 v212, v212, v253\n"
    "v_mov_b32 v227, 7\n"
    "v_mfma_f32_16x16x32_bf16 v[216:219], v[124:127], v[196:199], v[216:219]\n"
    "ds_read_b32 v252, v1 offset:0\n"
    "v_mul_f32 v213, v213, v253\n"
    "v_mfma_f32_16x16x32_bf16 v[220:223], v[148:151], v[196:199], v[220:223]\n"
    "v_mul_f32 v214, v214, v253\n"
    "v_mul_f32 v215, v215, v253\n"
    "ds_write_b32 v226, v227 offset:32\n"
    "v_cvt_pk_bf16_f32 v208, v208, v209\n"
    "v_cvt_pk_bf16_f32 v209, v210, v211\n"
    "v_cvt_pk_bf16_f32 v210, v212, v213\n"
    "v_cvt_pk_bf16_f32 v211, v214, v215\n"
    "buffer_store_dwordx4 v[208:211], v5, s[80:83], 0 offen offset:64 sc1\n"
    ".Lh9j:\n"
    "s_waitcnt lgkmcnt(2)\n"
    "v_mfma_f32_16x16x32_bf16 v[200:203], v[8:11], v[152:155], 0\n"
    "v_mul_f32 v216, v216, v253\n"
    "v_mul_f32 v217, v217, v253\n"
    "v_mfma_f32_16x16x32_bf16 v[204:207], v[32:35], v[152:155], 0\n"
    "v_mul_f32 v218, v218, v253\n"
    "v_mul_f32 v219, v219, v253\n"
    "v_mfma_f32_16x16x32_bf16 v[200:203], v[12:15], v[156:159], v[200:203]\n"
    "v_mul_f32 v220, v220, v253\n"
    "v_mul_f32 v221, v221, v253\n"
    "v_mfma_f32_16x16x32_bf16 v[204:207], v[36:39], v[156:159], v[204:207]\n"
    "v_mul_f32 v222, v222, v253\n"
    "v_mul_f32 v223, v223, v253\n"
    "v_mfma_f32_16x16x32_bf16 v[200:203], v[16:19], v[160:163], v[200:203]\n"
    "v_cvt_pk_bf16_f32 v216, v216, v217\n"
    "v_cvt_pk_bf16_f32 v217, v218, v219\n"
    "v_mfma_f32_16x16x32_bf16 v[204:207], v[40:43], v[160:163], v[204:207]\n"
    "v_cvt_pk_bf16_f32 v218, v220, v221\n"
    "v_cvt_pk_bf16_f32 v219, v222, v223\n"
    "v_mfma_f32_16x16x32_bf16 v[200:203], v[20:23], v[164:167], v[200:203]\n"
    "buffer_store_dwordx4 v[216:219], v5, s[80:83], 0 offen offset:128 sc1\n"
    "v_mfma_f32_16x16x32_bf16 v[204:207], v[44:47], v[164:167], v[204:207]\n"
    "v_mfma_f32_16x16x32_bf16 v[200:203], v[24:27], v[168:171], v[200:203]\n"
    "v_add_u32 v5, s46, v5\n"
    "v_mfma_f32_16x16x32_bf16 v[204:207], v[48:51], v[168:171], v[204:207]\n"
    "v_mfma_f32_16x16x32_bf16 v[200:203], v[28:31], v[172:175], v[200:203]\n"
    "v_mfma_f32_16x16x32_bf16 v[204:207], v[52:55], v[172:175], v[204:207]\n"
    "v_mfma_f32_16x16x32_bf16 v[208:211], v[56:59], v[152:155], 0\n"
    "v_mfma_f32_16x16x32_bf16 v[212:215], v[80:83], v[152:155], 0\n"
    "ds_read_b32 v228, v224 offset:176\n"
    "v_mfma_f32_16x16x32_bf16 v[208:211], v[60:63], v[156:159], v[208:211]\n"
    "v_mfma_f32_16x16x32_bf16 v[212:215], v[84:87], v[156:159], v[212:215]\n"
    "v_mfma_f32_16x16x32_bf16 v[208:211], v[64:67], v[160:163], v[208:211]\n"
    "s_waitcnt lgkmcnt(2)\n"
    "v_mul_f32 v200, v200, v252\n"
    "v_mfma_f32_16x16x32_bf16 v[212:215], v[88:91], v[160:163], v[212:215]\n"
    "v_mul_f32 v201, v201, v252\n"
    "v_mul_f32 v202, v202, v252\n"
    "v_mfma_f32_16x16x32_bf16 v[208:211], v[68:71], v[164:167], v[208:211]\n"
    "v_mul_f32 v203, v203, v252\n"
    "v_mul_f32 v204, v204, v252\n"
    "v_mfma_f32_16x16x32_bf16 v[212:215], v[92:95], v[164:167], v[212:215]\n"
    "v_mul_f32 v205, v205, v252\n"
    "v_mul_f32 v206, v206, v252\n"
    "v_mfma_f32_16x16x32_bf16 v[208:211], v[72:75], v[168:171], v[208:211]\n"
    "v_mul_f32 v207, v207, v252\n"
    "v_cvt_pk_bf16_f32 v200, v200, v201\n"
    "v_mfma_f32_16x16x32_bf16 v[212:215], v[96:99], v[168:171], v[212:215]\n"
    "v_cvt_pk_bf16_f32 v201, v202, v203\n"
    "v_cvt_pk_bf16_f32 v202, v204, v205\n"
    "v_mfma_f32_16x16x32_bf16 v[208:211], v[76:79], v[172:175], v[208:211]\n"
    "s_waitcnt lgkmcnt(0)\n"
    "v_cmp_gt_u32 vcc, 2, v228\n"
    "v_mfma_f32_16x16x32_bf16 v[212:215], v[100:103], v[172:175], v[212:215]\n"
    "v_cvt_pk_bf16_f32 v203, v206, v207\n"
    "s_cbranch_vccnz .Lh10\n"
    "v_mfma_f32_16x16x32_bf16 v[216:219], v[104:107], v[152:155], 0\n"
    "ds_read_b64_tr_b16 v[176:177], v2 offset:24576\n"
    "ds_read_b64_tr_b16 v[178:179], v3 offset:24576\n"
    "v_mfma_f32_16x16x32_bf16 v[220:223], v[128:131], v[152:155], 0\n"
    "ds_read_b64_tr_b16 v[180:181], v2 offset:25600\n"
    "ds_read_b64_tr_b16 v[182:183], v3 offset:25600\n"
    "v_mfma_f32_16x16x32_bf16 v[216:219], v[108:111], v[156:159], v[216:219]\n"
    "ds_read_b64_tr_b16 v[184:185], v2 offset:26624\n"
    "ds_read_b64_tr_b16 v[186:187], v3 offset:26624\n"
    "v_mfma_f32_16x16x32_bf16 v[220:223], v[132:135], v[156:159], v[220:223]\n"
    "ds_read_b64_tr_b16 v[188:189], v2 offset:27648\n"
    "ds_read_b64_tr_b16 v[190:191], v3 offset:27648\n"
    "v_mfma_f32_16x16x32_bf16 v[216:219], v[112:115], v[160:163], v[216:219]\n"
    "buffer_store_dwordx4 v[200:203], v4, s[80:83], 0 offen offset:0 sc1\n"
    "v_mfma_f32_16x16x32_bf16 v[220:223], v[136:139], v[160:163], v[220:223]\n"
    "v_mul_f32 v208, v208, v252\n"
    "v_mul_f32 v209, v209, v252\n"
    "v_mfma_f32_16x16x32_bf16 v[216:219], v[116:119], v[164:167], v[216:219]\n"
    "ds_read_b64_tr_b16 v[192:193], v2 offset:28672\n"
    "ds_read_b64_tr_b16 v[194:195], v3 offset:28672\n"
    "v_mfma_f32_16x16x32_bf16 v[220:223], v[140:143], v[164:167], v[220:223]\n"
    "v_mul_f32 v210, v210, v252\n"
    "v_mul_f32 v211, v211, v252\n"
    "v_mfma_f32_16x16x32_bf16 v[216:219], v[120:123], v[168:171], v[216:219]\n"
    "ds_read_b64_tr_b16 v[196:197], v2 offset:29696\n"
    "ds_read_b64_tr_b16 v[198:199], v3 offset:29696\n"
    "v_mfma_f32_16x16x32_bf16 v[220:223], v[144:147], v[168:171], v[220:223]\n"
    "v_mul_f32 v212, v212, v252\n"
    "v_mul_f32 v213, v213, v252\n"
    "v_mfma_f32_16x16x32_bf16 v[216:219], v[124:127], v[172:175], v[216:219]\n"
    "ds_read_b32 v253, v1 offset:64\n"
    "v_mul_f32 v214, v214, v252\n"
    "v_mfma_f32_16x16x32_bf16 v[220:223], v[148:151], v[172:175], v[220:223]\n"
    "v_mul_f32 v215, v215, v252\n"
    "v_cvt_pk_bf16_f32 v208, v208, v209\n"
    "v_cvt_pk_bf16_f32 v209, v210, v211\n"
    "v_cvt_pk_bf16_f32 v210, v212, v213\n"
    "v_cvt_pk_bf16_f32 v211, v214, v215\n"
    "buffer_store_dwordx4 v[208:211], v4, s[80:83], 0 offen offset:64 sc1\n"
    ".Lh10j:\n"
    "s_waitcnt lgkmcnt(1)\n"
    "v_mfma_f32_16x16x32_bf16 v[200:203], v[8:11], v[176:179], 0\n"
    "v_mul_f32 v216, v216, v252\n"
    "v_mul_f32 v217, v217, v252\n"
    "v_mfma_f32_16x16x32_bf16 v[204:207], v[32:35], v[176:179], 0\n"
    "v_mul_f32 v218, v218, v252\n"
    "v_mul_f32 v219, v219, v252\n"
    "v_mfma_f32_16x16x32_bf16 v[200:203], v[12:15], v[180:183], v[200:203]\n"
    "v_mul_f32 v220, v220, v252\n"
    "v_mul_f32 v221, v221, v252\n"
    "v_mfma_f32_16x16x32_bf16 v[204:207], v[36:39], v[180:183], v[204:207]\n"
    "v_mul_f32 v222, v222, v252\n"
    "v_mul_f32 v223, v223, v252\n"
    "v_mfma_f32_16x16x32_bf16 v[200:203], v[16:19], v[184:187], v[200:203]\n"
    "v_cvt_pk_bf16_f32 v216, v216, v217\n"
    "v_cvt_pk_bf16_f32 v217, v218, v219\n"
    "v_mfma_f32_16x16x32_bf16 v[204:207], v[40:43], v[184:187], v[204:207]\n"
    "v_cvt_pk_bf16_f32 v218, v220, v221\n"
    "v_cvt_pk_bf16_f32 v219, v222, v223\n"
    "v_mfma_f32_16x16x32_bf16 v[200:203], v[20:23], v[188:191], v[200:203]\n"
    "buffer_store_dwordx4 v[216:219], v4, s[80:83], 0 offen offset:128 sc1\n"
    "v_mfma_f32_16x16x32_bf16 v[204:207], v[44:47], v[188:191], v[204:207]\n"
    "v_mfma_f32_16x16x32_bf16 v[200:203], v[24:27], v[192:195], v[200:203]\n"
    "v_add_u32 v4, v6, v4\n"
    "v_mfma_f32_16x16x32_bf16 v[204:207], v[48:51], v[192:195], v[204:207]\n"
    "v_mfma_f32_16x16x32_bf16 v[200:203], v[28:31], v[196:199], v[200:203]\n"
    "v_mfma_f32_16x16x32_bf16 v[204:207], v[52:55], v[196:199], v[204:207]\n"
    "v_mfma_f32_16x16x32_bf16 v[208:211], v[56:59], v[176:179], 0\n"
    "v_mfma_f32_16x16x32_bf16 v[212:215], v[80:83], v[176:179], 0\n"
    "ds_read_b32 v228, v224 offset:128\n"
    "v_mfma_f32_16x16x32_bf16 v[208:211], v[60:63], v[180:183], v[208:211]\n"
    "v_mfma_f32_16x16x32_bf16 v[212:215], v[84:87], v[180:183], v[212:215]\n"
    "v_mfma_f32_16x16x32_bf16 v[208:211], v[64:67], v[184:187], v[208:211]\n"
    "s_waitcnt lgkmcnt(1)\n"
    "v_mul_f32 v200, v200, v253\n"
    "v_mfma_f32_16x16x32_bf16 v[212:215], v[88:91], v[184:187], v[212:215]\n"
    "v_mul_f32 v201, v201, v253\n"
    "v_mul_f32 v202, v202, v253\n"
    "v_mfma_f32_16x16x32_bf16 v[208:211], v[68:71], v[188:191], v[208:211]\n"
    "v_mul_f32 v203, v203, v253\n"
    "v_mul_f32 v204, v204, v253\n"
    "v_mfma_f32_16x16x32_bf16 v[212:215], v[92:95], v[188:191], v[212:215]\n"
    "v_mul_f32 v205, v205, v253\n"
    "v_mul_f32 v206, v206, v253\n"
    "v_mfma_f32_16x16x32_bf16 v[208:211], v[72:75], v[192:195], v[208:211]\n"
    "v_mul_f32 v207, v207, v253\n"
    "v_cvt_pk_bf16_f32 v200, v200, v201\n"
    "v_mfma_f32_16x16x32_bf16 v[212:215], v[96:99], v[192:195], v[212:215]\n"
    "v_cvt_pk_bf16_f32 v201, v202, v203\n"
    "v_cvt_pk_bf16_f32 v202, v204, v205\n"
    "v_mfma_f32_16x16x32_bf16 v[208:211], v[76:79], v[196:199], v[208:211]\n"
    "s_waitcnt lgkmcnt(0)\n"
    "v_cmp_gt_u32 vcc, 3, v228\n"
    "v_mfma_f32_16x16x32_bf16 v[212:215], v[100:103], v[196:199], v[212:215]\n"
    "v_cvt_pk_bf16_f32 v203, v206, v207\n"
    "s_cbranch_vccnz .Lh11\n"
    "v_mfma_f32_16x16x32_bf16 v[216:219], v[104:107], v[176:179], 0\n"
    "ds_read_b64_tr_b16 v[152:153], v2 offset:30720\n"
    "ds_read_b64_tr_b16 v[154:155], v3 offset:30720\n"
    "v_mfma_f32_16x16x32_bf16 v[220:223], v[128:131], v[176:179], 0\n"
    "ds_read_b64_tr_b16 v[156:157], v2 offset:31744\n"
    "ds_read_b64_tr_b16 v[158:159], v3 offset:31744\n"
    "v_mfma_f32_16x16x32_bf16 v[216:219], v[108:111], v[180:183], v[216:219]\n"
    "ds_read_b64_tr_b16 v[160:161], v2 offset:32768\n"
    "ds_read_b64_tr_b16 v[162:163], v3 offset:32768\n"
    "v_mfma_f32_16x16x32_bf16 v[220:223], v[132:135], v[180:183], v[220:223]\n"
    "ds_read_b64_tr_b16 v[164:165], v2 offset:33792\n"
    "ds_read_b64_tr_b16 v[166:167], v3 offset:33792\n"
    "v_mfma_f32_16x16x32_bf16 v[216:219], v[112:115], v[184:187], v[216:219]\n"
    "buffer_store_dwordx4 v[200:203], v5, s[80:83], 0 offen offset:0 sc1\n"
    "v_mfma_f32_16x16x32_bf16 v[220:223], v[136:139], v[184:187], v[220:223]\n"
    "v_mul_f32 v208, v208, v253\n"
    "v_mul_f32 v209, v209, v253\n"
    "v_mfma_f32_16x16x32_bf16 v[216:219], v[116:119], v[188:191], v[216:219]\n"
    "ds_read_b64_tr_b16 v[168:169], v2 offset:34816\n"
    "ds_read_b64_tr_b16 v[170:171], v3 offset:34816\n"
    "v_mfma_f32_16x16x32_bf16 v[220:223], v[140:143], v[188:191], v[220:223]\n"
    "v_mul_f32 v210, v210, v253\n"
    "v_mul_f32 v211, v211, v253\n"
    "v_mfma_f32_16x16x32_bf16 v[216:219], v[120:123], v[192:195], v[216:219]\n"
    "ds_read_b64_tr_b16 v[172:173], v2 offset:35840\n"
    "ds_read_b64_tr_b16 v[174:175], v3 offset:35840\n"
    "v_mfma_f32_16x16x32_bf16 v[220:223], v[144:147], v[192:195], v[220:223]\n"
    "v_mul_f32 v212, v212, v253\n"
    "v_mul_f32 v213, v213, v253\n"
    "v_mfma_f32_16x16x32_bf16 v[216:219], v[124:127], v[196:199], v[216:219]\n"
    "ds_read_b32 v252, v1 offset:128\n"
    "v_mul_f32 v214, v214, v253\n"
    "v_mfma_f32_16x16x32_bf16 v[220:223], v[148:151], v[196:199], v[220:223]\n"
    "v_mul_f32 v215, v215, v253\n"
    "v_cvt_pk_bf16_f32 v208, v208, v209\n"
    "v_cvt_pk_bf16_f32 v209, v210, v211\n"
    "v_cvt_pk_bf16_f32 v210, v212, v213\n"
    "v_cvt_pk_bf16_f32 v211, v214, v215\n"
    "buffer_store_dwordx4 v[208:211], v5, s[80:83], 0 offen offset:64 sc1\n"
    ".Lh11j:\n"
    "s_waitcnt lgkmcnt(1)\n"
    "v_mfma_f32_16x16x32_bf16 v[200:203], v[8:11], v[152:155], 0\n"
    "v_mul_f32 v216, v216, v253\n"
    "v_mul_f32 v217, v217, v253\n"
    "v_mfma_f32_16x16x32_bf16 v[204:207], v[32:35], v[152:155], 0\n"
    "ds_read_b32 v228, v224 offset:144\n"
    "v_mul_f32 v218, v218, v253\n"
    "v_mfma_f32_16x16x32_bf16 v[200:203], v[12:15], v[156:159], v[200:203]\n"
    "v_mul_f32 v219, v219, v253\n"
    "v_mul_f32 v220, v220, v253\n"
    "v_mfma_f32_16x16x32_bf16 v[204:207], v[36:39], v[156:159], v[204:207]\n"
    "v_mul_f32 v221, v221, v253\n"
    "v_mul_f32 v222, v222, v253\n"
    "v_mfma_f32_16x16x32_bf16 v[200:203], v[16:19], v[160:163], v[200:203]\n"
    "v_mul_f32 v223, v223, v253\n"
    "v_cvt_pk_bf16_f32 v216, v216, v217\n"
    "v_mfma_f32_16x16x32_bf16 v[204:207], v[40:43], v[160:163], v[204:207]\n"
    "v_cvt_pk_bf16_f32 v217, v218, v219\n"
    "v_cvt_pk_bf16_f32 v218, v220, v221\n"
    "v_mfma_f32_16x16x32_bf16 v[200:203], v[20:23], v[164:167], v[200:203]\n"
    "v_cvt_pk_bf16_f32 v219, v222, v223\n"
    "v_mfma_f32_16x16x32_bf16 v[204:207], v[44:47], v[164:167], v[204:207]\n"
    "buffer_store_dwordx4 v[216:219], v5, s[80:83], 0 offen offset:128 sc1\n"
    "v_mfma_f32_16x16x32_bf16 v[200:203], v[24:27], v[168:171], v[200:203]\n"
    "v_mfma_f32_16x16x32_bf16 v[204:207], v[48:51], v[168:171], v[204:207]\n"
    "v_add_u32 v5, v6, v5\n"
    "v_mfma_f32_16x16x32_bf16 v[200:203], v[28:31], v[172:175], v[200:203]\n"
    "s_waitcnt lgkmcnt(0)\n"
    "v_cmp_gt_u32 vcc, 3, v228\n"
    "v_mfma_f32_16x16x32_bf16 v[204:207], v[52:55], v[172:175], v[204:207]\n"
    "s_cbranch_vccnz .Lh12\n"
    "v_mfma_f32_16x16x32_bf16 v[208:211], v[56:59], v[152:155], 0\n"
    "ds_read_b64_tr_b16 v[176:177], v2 offset:18432\n"
    "ds_read_b64_tr_b16 v[178:179], v3 offset:18432\n"
    "v_mfma_f32_16x16x32_bf16 v[212:215], v[80:83], v[152:155], 0\n"
    "ds_read_b64_tr_b16 v[180:181], v2 offset:19456\n"
    "ds_read_b64_tr_b16 v[182:183], v3 offset:19456\n"
    "v_mfma_f32_16x16x32_bf16 v[208:211], v[60:63], v[156:159], v[208:211]\n"
    "ds_read_b64_tr_b16 v[184:185], v2 offset:20480\n"
    "ds_read_b64_tr_b16 v[186:187], v3 offset:20480\n"
    "v_mfma_f32_16x16x32_bf16 v[212:215], v[84:87], v[156:159], v[212:215]\n"
    "ds_read_b64_tr_b16 v[188:189], v2 offset:21504\n"
    "ds_read_b64_tr_b16 v[190:191], v3 offset:21504\n"
    "v_mfma_f32_16x16x32_bf16 v[208:211], v[64:67], v[160:163], v[208:211]\n"
    "v_mul_f32 v200, v200, v252\n"
    "v_mul_f32 v201, v201, v252\n"
    "v_mfma_f32_16x16x32_bf16 v[212:215], v[88:91], v[160:163], v[212:215]\n"
    "ds_read_b64_tr_b16 v[192:193], v2 offset:22528\n"
    "ds_read_b64_tr_b16 v[194:195], v3 offset:22528\n"
    "v_mfma_f32_16x16x32_bf16 v[208:211], v[68:71], v[164:167], v[208:211]\n"
    "v_mul_f32 v202, v202, v252\n"
    "v_mul_f32 v203, v203, v252\n"
    "v_mfma_f32_16x16x32_bf16 v[212:215], v[92:95], v[164:167], v[212:215]\n"
    "ds_read_b64_tr_b16 v[196:197], v2 offset:23552\n"
    "ds_read_b64_tr_b16 v[198:199], v3 offset:23552\n"
    "v_mfma_f32_16x16x32_bf16 v[208:211], v[72:75], v[168:171], v[208:211]\n"
    "v_mul_f32 v204, v204, v252\n"
    "v_mul_f32 v205, v205, v252\n"
    "v_mfma_f32_16x16x32_bf16 v[212:215], v[96:99], v[168:171], v[212:215]\n"
    "ds_read_b32 v253, v1 offset:0\n"
    "v_mul_f32 v206, v206, v252\n"
    "v_mfma_f32_16x16x32_bf16 v[208:211], v[76:79], v[172:175], v[208:211]\n"
    "v_mul_f32 v207, v207, v252\n"
    "v_cvt_pk_bf16_f32 v200, v200, v201\n"
    "v_mfma_f32_16x16x32_bf16 v[212:215], v[100:103], v[172:175], v[212:215]\n"
    "v_cvt_pk_bf16_f32 v201, v202, v203\n"
    "v_cvt_pk_bf16_f32 v202, v204, v205\n"
    "v_cvt_pk_bf16_f32 v203, v206, v207\n"
    "buffer_store_dwordx4 v[200:203], v4, s[80:83], 0 offen offset:0 sc1\n"
    ".Lh12j:\n"
    "s_waitcnt lgkmcnt(1)\n"
    "s_nop 0\n"
    "v_mfma_f32_16x16x32_bf16 v[200:203], v[8:11], v[176:179], 0\n"
    "v_mul_f32 v208, v208, v252\n"
    "v_mul_f32 v209, v209, v252\n"
    "v_mfma_f32_16x16x32_bf16 v[204:207], v[32:35], v[176:179], 0\n"
    "ds_read_b32 v228, v224 offset:160\n"
    "v_mul_f32 v210, v210, v252\n"
    "v_mfma_f32_16x16x32_bf16 v[200:203], v[12:15], v[180:183], v[200:203]\n"
    "v_mul_f32 v211, v211, v252\n"
    "v_mul_f32 v212, v212, v252\n"
    "v_mfma_f32_16x16x32_bf16 v[204:207], v[36:39], v[180:183], v[204:207]\n"
    "v_mul_f32 v213, v213, v252\n"
    "v_mul_f32 v214, v214, v252\n"
    "v_mfma_f32_16x16x32_bf16 v[200:203], v[16:19], v[184:187], v[200:203]\n"
    "v_mul_f32 v215, v215, v252\n"
    "v_cvt_pk_bf16_f32 v208, v208, v209\n"
    "v_mfma_f32_16x16x32_bf16 v[204:207], v[40:43], v[184:187], v[204:207]\n"
    "v_cvt_pk_bf16_f32 v209, v210, v211\n"
    "v_cvt_pk_bf16_f32 v210, v212, v213\n"
    "v_mfma_f32_16x16x32_bf16 v[200:203], v[20:23], v[188:191], v[200:203]\n"
    "v_cvt_pk_bf16_f32 v211, v214, v215\n"
    "v_mfma_f32_16x16x32_bf16 v[204:207], v[44:47], v[188:191], v[204:207]\n"
    "buffer_store_dwordx4 v[208:211], v4, s[80:83], 0 offen offset:64 sc1\n"
    "v_mfma_f32_16x16x32_bf16 v[200:203], v[24:27], v[192:195], v[200:203]\n"
    "v_mfma_f32_16x16x32_bf16 v[204:207], v[48:51], v[192:195], v[204:207]\n"
    "v_add_u32 v4, v7, v4\n"
    "v_mfma_f32_16x16x32_bf16 v[200:203], v[28:31], v[196:199], v[200:203]\n"
    "s_waitcnt lgkmcnt(0)\n"
    "v_cmp_gt_u32 vcc, 3, v228\n"
    "v_mfma_f32_16x16x32_bf16 v[204:207], v[52:55], v[196:199], v[204:207]\n"
    "s_cbranch_vccnz .Lh13\n"
    "v_mfma_f32_16x16x32_bf16 v[208:211], v[56:59], v[176:179], 0\n"
    "ds_read_b64_tr_b16 v[152:153], v2 offset:6144\n"
    "ds_read_b64_tr_b16 v[154:155], v3 offset:6144\n"
    "v_mfma_f32_16x16x32_bf16 v[212:215], v[80:83], v[176:179], 0\n"
    "ds_read_b64_tr_b16 v[156:157], v2 offset:7168\n"
    "ds_read_b64_tr_b16 v[158:159], v3 offset:7168\n"
    "v_mfma_f32_16x16x32_bf16 v[208:211], v[60:63], v[180:183], v[208:211]\n"
    "ds_read_b64_tr_b16 v[160:161], v2 offset:8192\n"
    "ds_read_b64_tr_b16 v[162:163], v3 offset:8192\n"
    "v_mfma_f32_16x16x32_bf16 v[212:215], v[84:87], v[180:183], v[212:215]\n"
    "ds_read_b64_tr_b16 v[164:165], v2 offset:9216\n"
    "ds_read_b64_tr_b16 v[166:167], v3 offset:9216\n"
    "v_mfma_f32_16x16x32_bf16 v[208:211], v[64:67], v[184:187], v[208:211]\n"
    "v_mul_f32 v200, v200, v253\n"
    "v_mul_f32 v201, v201, v253\n"
    "v_mfma_f32_16x16x32_bf16 v[212:215], v[88:91], v[184:187], v[212:215]\n"
    "ds_read_b64_tr_b16 v[168:169], v2 offset:10240\n"
    "ds_read_b64_tr_b16 v[170:171], v3 offset:10240\n"
    "v_mfma_f32_16x16x32_bf16 v[208:211], v[68:71], v[188:191], v[208:211]\n"
    "v_mul_f32 v202, v202, v253\n"
    "v_mul_f32 v203, v203, v253\n"
    "v_mfma_f32_16x16x32_bf16 v[212:215], v[92:95], v[188:191], v[212:215]\n"
    "ds_read_b64_tr_b16 v[172:173], v2 offset:11264\n"
    "ds_read_b64_tr_b16 v[174:175], v3 offset:11264\n"
    "v_mfma_f32_16x16x32_bf16 v[208:211], v[72:75], v[192:195], v[208:211]\n"
    "v_mul_f32 v204, v204, v253\n"
    "v_mul_f32 v205, v205, v253\n"
    "v_mfma_f32_16x16x32_bf16 v[212:215], v[96:99], v[192:195], v[212:215]\n"
    "ds_read_b32 v252, v1 offset:256\n"
    "v_mul_f32 v206, v206, v253\n"
    "v_mfma_f32_16x16x32_bf16 v[208:211], v[76:79], v[196:199], v[208:211]\n"
    "v_mul_f32 v207, v207, v253\n"
    "v_cvt_pk_bf16_f32 v200, v200, v201\n"
    "v_mfma_f32_16x16x32_bf16 v[212:215], v[100:103], v[196:199], v[212:215]\n"
    "v_cvt_pk_bf16_f32 v201, v202, v203\n"
    "v_cvt_pk_bf16_f32 v202, v204, v205\n"
    "v_cvt_pk_bf16_f32 v203, v206, v207\n"
    "buffer_store_dwordx4 v[200:203], v5, s[80:83], 0 offen offset:0 sc1\n"
    ".Lh13j:\n"
    "s_waitcnt lgkmcnt(1)\n"
    "s_nop 0\n"
    "v_mfma_f32_16x16x32_bf16 v[200:203], v[8:11], v[152:155], 0\n"
    "v_mul_f32 v208, v208, v253\n"
    "v_mul_f32 v209, v209, v253\n"
    "v_mfma_f32_16x16x32_bf16 v[204:207], v[32:35], v[152:155], 0\n"
    "ds_read_b32 v228, v224 offset:176\n"
    "v_mul_f32 v210, v210, v253\n"
    "v_mfma_f32_16x16x32_bf16 v[200:203], v[12:15], v[156:159], v[200:203]\n"
    "v_mul_f32 v211, v211, v253\n"
    "v_mul_f32 v212, v212, v253\n"
    "v_mfma_f32_16x16x32_bf16 v[204:207], v[36:39], v[156:159], v[204:207]\n"
    "v_mul_f32 v213, v213, v253\n"
    "v_mul_f32 v214, v214, v253\n"
    "v_mfma_f32_16x16x32_bf16 v[200:203], v[16:19], v[160:163], v[200:203]\n"
    "v_mul_f32 v215, v215, v253\n"
    "v_cvt_pk_bf16_f32 v208, v208, v209\n"
    "v_mfma_f32_16x16x32_bf16 v[204:207], v[40:43], v[160:163], v[204:207]\n"
    "v_cvt_pk_bf16_f32 v209, v210, v211\n"
    "v_cvt_pk_bf16_f32 v210, v212, v213\n"
    "v_mfma_f32_16x16x32_bf16 v[200:203], v[20:23], v[164:167], v[200:203]\n"
    "v_cvt_pk_bf16_f32 v211, v214, v215\n"
    "v_mfma_f32_16x16x32_bf16 v[204:207], v[44:47], v[164:167], v[204:207]\n"
    "buffer_store_dwordx4 v[208:211], v5, s[80:83], 0 offen offset:64 sc1\n"
    "v_mfma_f32_16x16x32_bf16 v[200:203], v[24:27], v[168:171], v[200:203]\n"
    "v_mfma_f32_16x16x32_bf16 v[204:207], v[48:51], v[168:171], v[204:207]\n"
    "v_add_u32 v5, v7, v5\n"
    "v_mfma_f32_16x16x32_bf16 v[200:203], v[28:31], v[172:175], v[200:203]\n"
    "s_waitcnt lgkmcnt(0)\n"
    "v_cmp_gt_u32 vcc, 3, v228\n"
    "v_mfma_f32_16x16x32_bf16 v[204:207], v[52:55], v[172:175], v[204:207]\n"
    "s_cbranch_vccnz .Lh14\n"
    "v_mfma_f32_16x16x32_bf16 v[208:211], v[56:59], v[152:155], 0\n"
    "ds_read_b64_tr_b16 v[176:177], v2 offset:12288\n"
    "ds_read_b64_tr_b16 v[178:179], v3 offset:12288\n"
    "v_mfma_f32_16x16x32_bf16 v[212:215], v[80:83], v[152:155], 0\n"
    "ds_read_b64_tr_b16 v[180:181], v2 offset:13312\n"
    "ds_read_b64_tr_b16 v[182:183], v3 offset:13312\n"
    "v_mfma_f32_16x16x32_bf16 v[208:211], v[60:63], v[156:159], v[208:211]\n"
    "ds_read_b64_tr_b16 v[184:185], v2 offset:14336\n"
    "ds_read_b64_tr_b16 v[186:187], v3 offset:14336\n"
    "v_mfma_f32_16x16x32_bf16 v[212:215], v[84:87], v[156:159], v[212:215]\n"
    "ds_read_b64_tr_b16 v[188:189], v2 offset:15360\n"
    "ds_read_b64_tr_b16 v[190:191], v3 offset:15360\n"
    "v_mfma_f32_16x16x32_bf16 v[208:211], v[64:67], v[160:163], v[208:211]\n"
    "v_mul_f32 v200, v200, v252\n"
    "v_mul_f32 v201, v201, v252\n"
    "v_mfma_f32_16x16x32_bf16 v[212:215], v[88:91], v[160:163], v[212:215]\n"
    "ds_read_b64_tr_b16 v[192:193], v2 offset:16384\n"
    "ds_read_b64_tr_b16 v[194:195], v3 offset:16384\n"
    "v_mfma_f32_16x16x32_bf16 v[208:211], v[68:71], v[164:167], v[208:211]\n"
    "v_mul_f32 v202, v202, v252\n"
    "v_mul_f32 v203, v203, v252\n"
    "v_mfma_f32_16x16x32_bf16 v[212:215], v[92:95], v[164:167], v[212:215]\n"
    "ds_read_b64_tr_b16 v[196:197], v2 offset:17408\n"
    "ds_read_b64_tr_b16 v[198:199], v3 offset:17408\n"
    "v_mfma_f32_16x16x32_bf16 v[208:211], v[72:75], v[168:171], v[208:211]\n"
    "v_mul_f32 v204, v204, v252\n"
    "v_mul_f32 v205, v205, v252\n"
    "v_mfma_f32_16x16x32_bf16 v[212:215], v[96:99], v[168:171], v[212:215]\n"
    "ds_read_b32 v253, v1 offset:320\n"
    "v_mul_f32 v206, v206, v252\n"
    "v_mfma_f32_16x16x32_bf16 v[208:211], v[76:79], v[172:175], v[208:211]\n"
    "v_mul_f32 v207, v207, v252\n"
    "v_cvt_pk_bf16_f32 v200, v200, v201\n"
    "v_mfma_f32_16x16x32_bf16 v[212:215], v[100:103], v[172:175], v[212:215]\n"
    "v_cvt_pk_bf16_f32 v201, v202, v203\n"
    "v_cvt_pk_bf16_f32 v202, v204, v205\n"
    "v_cvt_pk_bf16_f32 v203, v206, v207\n"
    "buffer_store_dwordx4 v[200:203], v4, s[80:83], 0 offen offset:0 sc1\n"
    ".Lh14j:\n"
    "s_cmp_lg_u32 s58, 0\n"
    "s_cbranch_scc1 .Lh_e12\n"
    "s_waitcnt lgkmcnt(1)\n"
    "v_mfma_f32_16x16x32_bf16 v[200:203], v[8:11], v[176:179], 0\n"
    "v_mul_f32 v208, v208, v252\n"
    "v_mul_f32 v209, v209, v252\n"
    "v_mfma_f32_16x16x32_bf16 v[204:207], v[32:35], v[176:179], 0\n"
    "ds_read_b32 v228, v224 offset:832\n"
    "v_mul_f32 v210, v210, v252\n"
    "v_mfma_f32_16x16x32_bf16 v[200:203], v[12:15], v[180:183], v[200:203]\n"
    "v_mul_f32 v211, v211, v252\n"
    "v_mul_f32 v212, v212, v252\n"
    "v_mfma_f32_16x16x32_bf16 v[204:207], v[36:39], v[180:183], v[204:207]\n"
    "v_mul_f32 v213, v213, v252\n"
    "v_mul_f32 v214, v214, v252\n"
    "v_mfma_f32_16x16x32_bf16 v[200:203], v[16:19], v[184:187], v[200:203]\n"
    "v_mul_f32 v215, v215, v252\n"
    "v_cvt_pk_bf16_f32 v208, v208, v209\n"
    "v_mfma_f32_16x16x32_bf16 v[204:207], v[40:43], v[184:187], v[204:207]\n"
    "v_cvt_pk_bf16_f32 v209, v210, v211\n"
    "v_cvt_pk_bf16_f32 v210, v212, v213\n"
    "v_mfma_f32_16x16x32_bf16 v[200:203], v[20:23], v[188:191], v[200:203]\n"
    "v_cvt_pk_bf16_f32 v211, v214, v215\n"
    "v_mfma_f32_16x16x32_bf16 v[204:207], v[44:47], v[188:191], v[204:207]\n"
    "buffer_store_dwordx4 v[208:211], v4, s[80:83], 0 offen offset:64 sc1\n"
    "v_mfma_f32_16x16x32_bf16 v[200:203], v[24:27], v[192:195], v[200:203]\n"
    "v_mfma_f32_16x16x32_bf16 v[204:207], v[48:51], v[192:195], v[204:207]\n"
    "v_add_u32 v4, v0, v4\n"
    "v_mfma_f32_16x16x32_bf16 v[200:203], v[28:31], v[196:199], v[200:203]\n"
    "s_waitcnt lgkmcnt(0)\n"
    "v_cmp_gt_u32 vcc, 4, v228\n"
    "v_mfma_f32_16x16x32_bf16 v[204:207], v[52:55], v[196:199], v[204:207]\n"
    "s_cbranch_vccnz .Lh15\n"
    "v_mfma_f32_16x16x32_bf16 v[208:211], v[56:59], v[176:179], 0\n"
    "ds_read_b64_tr_b16 v[152:153], v2 offset:0\n"
    "ds_read_b64_tr_b16 v[154:155], v3 offset:0\n"
    "v_mfma_f32_16x16x32_bf16 v[212:215], v[80:83], v[176:179], 0\n"
    "ds_read_b64_tr_b16 v[156:157], v2 offset:1024\n"
    "ds_read_b64_tr_b16 v[158:159], v3 offset:1024\n"
    "v_mfma_f32_16x16x32_bf16 v[208:211], v[60:63], v[180:183], v[208:211]\n"
    "ds_read_b64_tr_b16 v[160:161], v2 offset:2048\n"
    "ds_read_b64_tr_b16 v[162:163], v3 offset:2048\n"
    "v_mfma_f32_16x16x32_bf16 v[212:215], v[84:87], v[180:183], v[212:215]\n"
    "ds_read_b64_tr_b16 v[164:165], v2 offset:3072\n"
    "ds_read_b64_tr_b16 v[166:167], v3 offset:3072\n"
    "v_mfma_f32_16x16x32_bf16 v[208:211], v[64:67], v[184:187], v[208:211]\n"
    "v_mul_f32 v200, v200, v253\n"
    "v_mul_f32 v201, v201, v253\n"
    "v_mfma_f32_16x16x32_bf16 v[212:215], v[88:91], v[184:187], v[212:215]\n"
    "ds_read_b64_tr_b16 v[168:169], v2 offset:4096\n"
    "ds_read_b64_tr_b16 v[170:171], v3 offset:4096\n"
    "v_mfma_f32_16x16x32_bf16 v[208:211], v[68:71], v[188:191], v[208:211]\n"
    "v_mul_f32 v202, v202, v253\n"
    "v_mul_f32 v203, v203, v253\n"
    "v_mfma_f32_16x16x32_bf16 v[212:215], v[92:95], v[188:191], v[212:215]\n"
    "ds_read_b64_tr_b16 v[172:173], v2 offset:5120\n"
    "ds_read_b64_tr_b16 v[174:175], v3 offset:5120\n"
    "v_mfma_f32_16x16x32_bf16 v[208:211], v[72:75], v[192:195], v[208:211]\n"
    "v_mul_f32 v204, v204, v253\n"
    "v_mul_f32 v205, v205, v253\n"
    "v_mfma_f32_16x16x32_bf16 v[212:215], v[96:99], v[192:195], v[212:215]\n"
    "ds_read_b32 v252, v1 offset:192\n"
    "v_mul_f32 v206, v206, v253\n"
    "v_mfma_f32_16x16x32_bf16 v[208:211], v[76:79], v[196:199], v[208:211]\n"
    "v_mul_f32 v207, v207, v253\n"
    "v_cvt_pk_bf16_f32 v200, v200, v201\n"
    "v_mfma_f32_16x16x32_bf16 v[212:215], v[100:103], v[196:199], v[212:215]\n"
    "v_cvt_pk_bf16_f32 v201, v202, v203\n"
    "v_cvt_pk_bf16_f32 v202, v204, v205\n"
    "v_cvt_pk_bf16_f32 v203, v206, v207\n"
    "buffer_store_dwordx4 v[200:203], v5, s[80:83], 0 offen offset:0 sc1\n"
    ".Lh15j:\n"
    "s_waitcnt lgkmcnt(1)\n"
    "s_nop 0\n"
    "v_mfma_f32_16x16x32_bf16 v[200:203], v[8:11], v[152:155], 0\n"
    "v_mul_f32 v208, v208, v253\n"
    "v_mul_f32 v209, v209, v253\n"
    "v_mfma_f32_16x16x32_bf16 v[204:207], v[32:35], v[152:155], 0\n"
    "v_mul_f32 v210, v210, v253\n"
    "v_mul_f32 v211, v211, v253\n"
    "v_mfma_f32_16x16x32_bf16 v[200:203], v[12:15], v[156:159], v[200:203]\n"
    "v_mul_f32 v212, v212, v253\n"
    "v_mul_f32 v213, v213, v253\n"
    "v_mfma_f32_16x16x32_bf16 v[204:207], v[36:39], v[156:159], v[204:207]\n"
    "v_mul_f32 v214, v214, v253\n"
    "v_mul_f32 v215, v215, v253\n"
    "v_mfma_f32_16x16x32_bf16 v[200:203], v[16:19], v[160:163], v[200:203]\n"
    "v_cvt_pk_bf16_f32 v208, v208, v209\n"
    "v_cvt_pk_bf16_f32 v209, v210, v211\n"
    "v_mfma_f32_16x16x32_bf16 v[204:207], v[40:43], v[160:163], v[204:207]\n"
    "v_cvt_pk_bf16_f32 v210, v212, v213\n"
    "v_cvt_pk_bf16_f32 v211, v214, v215\n"
    "v_mfma_f32_16x16x32_bf16 v[200:203], v[20:23], v[164:167], v[200:203]\n"
    "buffer_store_dwordx4 v[208:211], v5, s[80:83], 0 offen offset:64 sc1\n"
    "v_mfma_f32_16x16x32_bf16 v[204:207], v[44:47], v[164:167], v[204:207]\n"
    "v_mfma_f32_16x16x32_bf16 v[200:203], v[24:27], v[168:171], v[200:203]\n"
    "v_mfma_f32_16x16x32_bf16 v[204:207], v[48:51], v[168:171], v[204:207]\n"
    "v_mfma_f32_16x16x32_bf16 v[200:203], v[28:31], v[172:175], v[200:203]\n"
    "v_mfma_f32_16x16x32_bf16 v[204:207], v[52:55], v[172:175], v[204:207]\n"
    "v_mfma_f32_16x16x32_bf16 v[208:211], v[56:59], v[152:155], 0\n"
    "v_mfma_f32_16x16x32_bf16 v[212:215], v[80:83], v[152:155], 0\n"
    "v_mfma_f32_16x16x32_bf16 v[208:211], v[60:63], v[156:159], v[208:211]\n"
    "v_mfma_f32_16x16x32_bf16 v[212:215], v[84:87], v[156:159], v[212:215]\n"
    "v_mfma_f32_16x16x32_bf16 v[208:211], v[64:67], v[160:163], v[208:211]\n"
    "v_mfma_f32_16x16x32_bf16 v[212:215], v[88:91], v[160:163], v[212:215]\n"
    "s_waitcnt lgkmcnt(0)\n"
    "v_mul_f32 v200, v200, v252\n"
    "v_mfma_f32_16x16x32_bf16 v[208:211], v[68:71], v[164:167], v[208:211]\n"
    "v_mul_f32 v201, v201, v252\n"
    "v_mul_f32 v202, v202, v252\n"
    "v_mfma_f32_16x16x32_bf16 v[212:215], v[92:95], v[164:167], v[212:215]\n"
    "v_mul_f32 v203, v203, v252\n"
    "v_mul_f32 v204, v204, v252\n"
    "v_mfma_f32_16x16x32_bf16 v[208:211], v[72:75], v[168:171], v[208:211]\n"
    "v_mul_f32 v205, v205, v252\n"
    "v_mul_f32 v206, v206, v252\n"
    "v_mfma_f32_16x16x32_bf16 v[212:215], v[96:99], v[168:171], v[212:215]\n"
    "v_mul_f32 v207, v207, v252\n"
    "v_cvt_pk_bf16_f32 v200, v200, v201\n"
    "v_mfma_f32_16x16x32_bf16 v[208:211], v[76:79], v[172:175], v[208:211]\n"
    "v_cvt_pk_bf16_f32 v201, v202, v203\n"
    "v_cvt_pk_bf16_f32 v202, v204, v205\n"
    "v_mfma_f32_16x16x32_bf16 v[212:215], v[100:103], v[172:175], v[212:215]\n"
    "v_cvt_pk_bf16_f32 v203, v206, v207\n"
    "buffer_store_dwordx4 v[200:203], v4, s[80:83], 0 offen offset:0 sc1\n"
    "s_nop 2\n"
    "v_mul_f32 v208, v208, v252\n"
    "v_mul_f32 v209, v209, v252\n"
    "v_mul_f32 v210, v210, v252\n"
    "v_mul_f32 v211, v211, v252\n"
    "v_mul_f32 v212, v212, v252\n"
    "v_mul_f32 v213, v213, v252\n"
    "v_mul_f32 v214, v214, v252\n"
    "v_mul_f32 v215, v215, v252\n"
    "v_cvt_pk_bf16_f32 v208, v208, v209\n"
    "v_cvt_pk_bf16_f32 v209, v210, v211\n"
    "v_cvt_pk_bf16_f32 v210, v212, v213\n"
    "v_cvt_pk_bf16_f32 v211, v214, v215\n"
    "buffer_store_dwordx4 v[208:211], v4, s[80:83], 0 offen offset:64 sc1\n"
    ".Lh_end:\n"
    "s_endpgm\n"
    ".Lh1:\n"
    "s_sub_u32 s52, s52, 1\n"
    "s_cmp_lt_i32 s52, 0\n"
    "s_cbranch_scc1 .Lh1b\n"
    "s_sleep 1\n"
    "ds_read_b32 v228, v224 offset:288\n"
    "s_waitcnt lgkmcnt(0)\n"
    "v_cmp_gt_u32 vcc, 1, v228\n"
    "s_cbranch_vccnz .Lh1\n"
    "s_branch .Lh1b\n"
    ".Lh2:\n"
    "s_sub_u32 s52, s52, 1\n"
    "s_cmp_lt_i32 s52, 0\n"
    "s_cbranch_scc1 .Lh2b\n"
    "s_sleep 1\n"
    "ds_read_b32 v228, v224 offset:0\n"
    "s_waitcnt lgkmcnt(0)\n"
    "v_cmp_gt_u32 vcc, 6, v228\n"
    "s_cbranch_vccnz .Lh2\n"
    "s_branch .Lh2b\n"
    ".Lh_re:\n"
    "s_sub_u32 s51, s51, 1\n"
    "s_cmp_le_i32 s51, 0\n"
    "s_cbranch_scc1 .Lh_ok\n"
    "s_sleep 2\n"
    "v_mov_b64 v[244:245], v[240:241]\n"
    "v_mov_b64 v[246:247], 0\n"
    "v_mov_b64 v[242:243], 0\n"
    "s_mov_b32 exec_hi, 15\n"
    "global_load_dwordx4 v[244:247], v236, s[40:41] sc1\n"
    "s_mov_b64 exec, -1\n"
    "global_load_dwordx4 v[8:11], v235, s[64:65] offset:-3072 sc1\n"
    "global_load_dwordx4 v[32:35], v235, s[66:67] offset:-3072 sc1\n"
    "global_load_dwordx4 v[12:15], v235, s[64:65] offset:-2048 sc1\n"
    "global_load_dwordx4 v[36:39], v235, s[66:67] offset:-2048 sc1\n"
    "global_load_dwordx4 v[16:19], v235, s[64:65] offset:-1024 sc1\n"
    "global_load_dwordx4 v[40:43], v235, s[66:67] offset:-1024 sc1\n"
    "global_load_dwordx4 v[20:23], v235, s[64:65] offset:0 sc1\n"
    "global_load_dwordx4 v[44:47], v235, s[66:67] offset:0 sc1\n"
    "global_load_dwordx4 v[24:27], v235, s[64:65] offset:1024 sc1\n"
    "global_load_dwordx4 v[48:51], v235, s[66:67] offset:1024 sc1\n"
    "global_load_dwordx4 v[28:31], v235, s[64:65] offset:2048 sc1\n"
    "global_load_dwordx4 v[52:55], v235, s[66:67] offset:2048 sc1\n"
    "global_load_dwordx4 v[56:59], v235, s[68:69] offset:-3072 sc1\n"
    "global_load_dwordx4 v[80:83], v235, s[70:71] offset:-3072 sc1\n"
    "global_load_dwordx4 v[60:63], v235, s[68:69] offset:-2048 sc1\n"
    "global_load_dwordx4 v[84:87], v235, s[70:71] offset:-2048 sc1\n"
    "global_load_dwordx4 v[64:67], v235, s[68:69] offset:-1024 sc1\n"
    "global_load_dwordx4 v[88:91], v235, s[70:71] offset:-1024 sc1\n"
    "global_load_dwordx4 v[68:71], v235, s[68:69] offset:0 sc1\n"
    "global_load_dwordx4 v[92:95], v235, s[70:71] offset:0 sc1\n"
    "global_load_dwordx4 v[72:75], v235, s[68:69] offset:1024 sc1\n"
    "global_load_dwordx4 v[96:99], v235, s[70:71] offset:1024 sc1\n"
    "global_load_dwordx4 v[76:79], v235, s[68:69] offset:2048 sc1\n"
    "global_load_dwordx4 v[100:103], v235, s[70:71] offset:2048 sc1\n"
    "global_load_dwordx4 v[104:107], v235, s[72:73] offset:-3072 sc1\n"
    "global_load_dwordx4 v[128:131], v235, s[74:75] offset:-3072 sc1\n"
    "global_load_dwordx4 v[108:111], v235, s[72:73] offset:-2048 sc1\n"
    "global_load_dwordx4 v[132:135], v235, s[74:75] offset:-2048 sc1\n"
    "global_load_dwordx4 v[112:115], v235, s[72:73] offset:-1024 sc1\n"
    "global_load_dwordx4 v[136:139], v235, s[74:75] offset:-1024 sc1\n"
    "global_load_dwordx4 v[116:119], v235, s[72:73] offset:0 sc1\n"
    "global_load_dwordx4 v[140:143], v235, s[74:75] offset:0 sc1\n"
    "global_load_dwordx4 v[120:123], v235, s[72:73] offset:1024 sc1\n"
    "global_load_dwordx4 v[144:147], v235, s[74:75] offset:1024 sc1\n"
    "global_load_dwordx4 v[124:127], v235, s[72:73] offset:2048 sc1\n"
    "global_load_dwordx4 v[148:151], v235, s[74:75] offset:2048 sc1\n"
    "s_waitcnt vmcnt(0)\n"
    "v_add3_u32 v242, v242, v8, v11\n"
    "v_sub_u32 v8, v8, v240\n"
    "v_add3_u32 v242, v242, v32, v35\n"
    "v_sub_u32 v32, v32, v240\n"
    "v_add3_u32 v242, v242, v12, v15\n"
    "v_sub_u32 v12, v12, v240\n"
    "v_add3_u32 v242, v242, v36, v39\n"
    "v_sub_u32 v36, v36, v240\n"
    "v_add3_u32 v242, v242, v16, v19\n"
    "v_sub_u32 v16, v16, v240\n"
    "v_add3_u32 v242, v242, v40, v43\n"
    "v_sub_u32 v40, v40, v240\n"
    "v_add3_u32 v242, v242, v20, v23\n"
    "v_sub_u32 v20, v20, v240\n"
    "v_add3_u32 v242, v242, v44, v47\n"
    "v_sub_u32 v44, v44, v240\n"
    "v_add3_u32 v242, v242, v24, v27\n"
    "v_sub_u32 v24, v24, v240\n"
    "v_add3_u32 v242, v242, v48, v51\n"
    "v_sub_u32 v48, v48, v240\n"
    "v_add3_u32 v242, v242, v28, v31\n"
    "v_sub_u32 v28, v28, v240\n"
    "v_add3_u32 v242, v242, v52, v55\n"
    "v_sub_u32 v52, v52, v240\n"
    "v_add3_u32 v242, v242, v56, v59\n"
    "v_sub_u32 v56, v56, v240\n"
    "v_add3_u32 v242, v242, v80, v83\n"
    "v_sub_u32 v80, v80, v240\n"
    "v_add3_u32 v242, v242, v60, v63\n"
    "v_sub_u32 v60, v60, v240\n"
    "v_add3_u32 v242, v242, v84, v87\n"
    "v_sub_u32 v84, v84, v240\n"
    "v_add3_u32 v242, v242, v64, v67\n"
    "v_sub_u32 v64, v64, v240\n"
    "v_add3_u32 v242, v242, v88, v91\n"
    "v_sub_u32 v88, v88, v240\n"
    "v_add3_u32 v242, v242, v68, v71\n"
    "v_sub_u32 v68, v68, v240\n"
    "v_add3_u32 v242, v242, v92, v95\n"
    "v_sub_u32 v92, v92, v240\n"
    "v_add3_u32 v242, v242, v72, v75\n"
    "v_sub_u32 v72, v72, v240\n"
    "v_add3_u32 v242, v242, v96, v99\n"
    "v_sub_u32 v96, v96, v240\n"
    "v_add3_u32 v242, v242, v76, v79\n"
    "v_sub_u32 v76, v76, v240\n"
    "v_add3_u32 v242, v242, v100, v103\n"
    "v_sub_u32 v100, v100, v240\n"
    "v_add3_u32 v242, v242, v104, v107\n"
    "v_sub_u32 v104, v104, v240\n"
    "v_add3_u32 v242, v242, v128, v131\n"
    "v_sub_u32 v128, v128, v240\n"
    "v_add3_u32 v242, v242, v108, v111\n"
    "v_sub_u32 v108, v108, v240\n"
    "v_add3_u32 v242, v242, v132, v135\n"
    "v_sub_u32 v132, v132, v240\n"
    "v_add3_u32 v242, v242, v112, v115\n"
    "v_sub_u32 v112, v112, v240\n"
    "v_add3_u32 v242, v242, v136, v139\n"
    "v_sub_u32 v136, v136, v240\n"
    "v_add3_u32 v242, v242, v116, v119\n"
    "v_sub_u32 v116, v116, v240\n"
    "v_add3_u32 v242, v242, v140, v143\n"
    "v_sub_u32 v140, v140, v240\n"
    "v_add3_u32 v242, v242, v120, v123\n"
    "v_sub_u32 v120, v120, v240\n"
    "v_add3_u32 v242, v242, v144, v147\n"
    "v_sub_u32 v144, v144, v240\n"
    "v_add3_u32 v242, v242, v124, v127\n"
    "v_sub_u32 v124, v124, v240\n"
    "v_add3_u32 v242, v242, v148, v151\n"
    "v_sub_u32 v148, v148, v240\n"
    "s_branch .Lh_ce\n"
    ".Lh_pl:\n"
    "s_sub_u32 s51, s51, 1\n"
    "s_cmp_le_i32 s51, 0\n"
    "s_cbranch_scc1 .Lh_ld\n"
    "s_sleep 4\n"
    "s_mov_b32 exec_hi, 15\n"
    "global_load_dwordx2 v[244:245], v236, s[40:41] sc1\n"
    "s_mov_b64 exec, -1\n"
    "s_waitcnt vmcnt(0)\n"
    "v_cmp_ne_u64 vcc, v[240:241], v[244:245]\n"
    "s_cbranch_vccnz .Lh_pl\n"
    "s_branch .Lh_ld\n"
    ".Lh_ld:\n"
    "v_mov_b64 v[244:245], v[240:241]\n"
    "v_mov_b64 v[246:247], 0\n"
    "v_mov_b64 v[242:243], 0\n"
    "s_mov_b32 exec_hi, 15\n"
    "global_load_dwordx4 v[244:247], v236, s[40:41] sc1\n"
    "s_mov_b64 exec, -1\n"
    "global_load_dwordx4 v[8:11], v235, s[64:65] offset:-3072 sc1\n"
    "global_load_dwordx4 v[32:35], v235, s[66:67] offset:-3072 sc1\n"
    "global_load_dwordx4 v[12:15], v235, s[64:65] offset:-2048 sc1\n"
    "global_load_dwordx4 v[36:39], v235, s[66:67] offset:-2048 sc1\n"
    "global_load_dwordx4 v[16:19], v235, s[64:65] offset:-1024 sc1\n"
    "global_load_dwordx4 v[40:43], v235, s[66:67] offset:-1024 sc1\n"
    "global_load_dwordx4 v[20:23], v235, s[64:65] offset:0 sc1\n"
    "global_load_dwordx4 v[44:47], v235, s[66:67] offset:0 sc1\n"
    "global_load_dwordx4 v[24:27], v235, s[64:65] offset:1024 sc1\n"
    "global_load_dwordx4 v[48:51], v235, s[66:67] offset:1024 sc1\n"
    "global_load_dwordx4 v[28:31], v235, s[64:65] offset:2048 sc1\n"
    "global_load_dwordx4 v[52:55], v235, s[66:67] offset:2048 sc1\n"
    "global_load_dwordx4 v[56:59], v235, s[68:69] offset:-3072 sc1\n"
    "global_load_dwordx4 v[80:83], v235, s[70:71] offset:-3072 sc1\n"
    "global_load_dwordx4 v[60:63], v235, s[68:69] offset:-2048 sc1\n"
    "global_load_dwordx4 v[84:87], v235, s[70:71] offset:-2048 sc1\n"
    "global_load_dwordx4 v[64:67], v235, s[68:69] offset:-1024 sc1\n"
    "global_load_dwordx4 v[88:91], v235, s[70:71] offset:-1024 sc1\n"
    "global_load_dwordx4 v[68:71], v235, s[68:69] offset:0 sc1\n"
    "global_load_dwordx4 v[92:95], v235, s[70:71] offset:0 sc1\n"
    "global_load_dwordx4 v[72:75], v235, s[68:69] offset:1024 sc1\n"
    "global_load_dwordx4 v[96:99], v235, s[70:71] offset:1024 sc1\n"
    "global_load_dwordx4 v[76:79], v235, s[68:69] offset:2048 sc1\n"
    "global_load_dwordx4 v[100:103], v235, s[70:71] offset:2048 sc1\n"
    "global_load_dwordx4 v[104:107], v235, s[72:73] offset:-3072 sc1\n"
    "global_load_dwordx4 v[128:131], v235, s[74:75] offset:-3072 sc1\n"
    "global_load_dwordx4 v[108:111], v235, s[72:73] offset:-2048 sc1\n"
    "global_load_dwordx4 v[132:135], v235, s[74:75] offset:-2048 sc1\n"
    "global_load_dwordx4 v[112:115], v235, s[72:73] offset:-1024 sc1\n"
    "global_load_dwordx4 v[136:139], v235, s[74:75] offset:-1024 sc1\n"
    "global_load_dwordx4 v[116:119], v235, s[72:73] offset:0 sc1\n"
    "global_load_dwordx4 v[140:143], v235, s[74:75] offset:0 sc1\n"
    "global_load_dwordx4 v[120:123], v235, s[72:73] offset:1024 sc1\n"
    "global_load_dwordx4 v[144:147], v235, s[74:75] offset:1024 sc1\n"
    "global_load_dwordx4 v[124:127], v235, s[72:73] offset:2048 sc1\n"
    "global_load_dwordx4 v[148:151], v235, s[74:75] offset:2048 sc1\n"
    "s_branch .Lh_rs\n"
    ".Lh3:\n"
    "s_sub_u32 s52, s52, 1\n"
    "s_cmp_lt_i32 s52, 0\n"
    "s_cbranch_scc1 .Lh3b\n"
    "s_sleep 1\n"
    "ds_read_b32 v228, v224 offset:128\n"
    "s_waitcnt lgkmcnt(0)\n"
    "v_cmp_gt_u32 vcc, 1, v228\n"
    "s_cbranch_vccnz .Lh3\n"
    "s_branch .Lh3b\n"
    ".Lh4:\n"
    "v_mfma_f32_16x16x32_bf16 v[216:219], v[104:107], v[152:155], 0\n"
    "buffer_store_dwordx4 v[200:203], v4, s[80:83], 0 offen offset:0 sc1\n"
    "v_mfma_f32_16x16x32_bf16 v[220:223], v[128:131], v[152:155], 0\n"
    "v_mul_f32 v208, v208, v252\n"
    "v_mul_f32 v209, v209, v252\n"
    "v_mfma_f32_16x16x32_bf16 v[216:219], v[108:111], v[156:159], v[216:219]\n"
    "v_mul_f32 v210, v210, v252\n"
    "v_mul_f32 v211, v211, v252\n"
    "v_mfma_f32_16x16x32_bf16 v[220:223], v[132:135], v[156:159], v[220:223]\n"
    "v_mul_f32 v212, v212, v252\n"
    "v_mul_f32 v213, v213, v252\n"
    "v_mfma_f32_16x16x32_bf16 v[216:219], v[112:115], v[160:163], v[216:219]\n"
    "v_mul_f32 v214, v214, v252\n"
    "v_mul_f32 v215, v215, v252\n"
    "v_mfma_f32_16x16x32_bf16 v[220:223], v[136:139], v[160:163], v[220:223]\n"
    "v_cvt_pk_bf16_f32 v208, v208, v209\n"
    "v_cvt_pk_bf16_f32 v209, v210, v211\n"
    "v_mfma_f32_16x16x32_bf16 v[216:219], v[116:119], v[164:167], v[216:219]\n"
    "v_cvt_pk_bf16_f32 v210, v212, v213\n"
    "v_cvt_pk_bf16_f32 v211, v214, v215\n"
    "v_mfma_f32_16x16x32_bf16 v[220:223], v[140:143], v[164:167], v[220:223]\n"
    "buffer_store_dwordx4 v[208:211], v4, s[80:83], 0 offen offset:64 sc1\n"
    "v_mfma_f32_16x16x32_bf16 v[216:219], v[120:123], v[168:171], v[216:219]\n"
    "v_mfma_f32_16x16x32_bf16 v[220:223], v[144:147], v[168:171], v[220:223]\n"
    "v_mfma_f32_16x16x32_bf16 v[216:219], v[124:127], v[172:175], v[216:219]\n"
    "v_mfma_f32_16x16x32_bf16 v[220:223], v[148:151], v[172:175], v[220:223]\n"
    ".Lh4r:\n"
    "s_sub_u32 s52, s52, 1\n"
    "s_cmp_lt_i32 s52, 0\n"
    "s_cbranch_scc1 .Lh4g\n"
    "s_sleep 1\n"
    "ds_read_b32 v228, v224 offset:144\n"
    "s_waitcnt lgkmcnt(0)\n"
    "v_cmp_gt_u32 vcc, 1, v228\n"
    "s_cbranch_vccnz .Lh4r\n"
    "s_branch .Lh4g\n"
    ".Lh4g:\n"
    "ds_read_b64_tr_b16 v[176:177], v2 offset:24576\n"
    "ds_read_b64_tr_b16 v[178:179], v3 offset:24576\n"
    "ds_read_b64_tr_b16 v[180:181], v2 offset:25600\n"
    "ds_read_b64_tr_b16 v[182:183], v3 offset:25600\n"
    "ds_read_b64_tr_b16 v[184:185], v2 offset:26624\n"
    "ds_read_b64_tr_b16 v[186:187], v3 offset:26624\n"
    "ds_read_b64_tr_b16 v[188:189], v2 offset:27648\n"
    "ds_read_b64_tr_b16 v[190:191], v3 offset:27648\n"
    "ds_read_b64_tr_b16 v[192:193], v2 offset:28672\n"
    "ds_read_b64_tr_b16 v[194:195], v3 offset:28672\n"
    "ds_read_b64_tr_b16 v[196:197], v2 offset:29696\n"
    "ds_read_b64_tr_b16 v[198:199], v3 offset:29696\n"
    "ds_read_b32 v253, v1 offset:64\n"
    "v_mov_b32 v227, 2\n"
    "ds_write_b32 v226, v227 offset:32\n"
    "s_branch .Lh4j\n"
    ".Lh5:\n"
    "v_mfma_f32_16x16x32_bf16 v[216:219], v[104:107], v[176:179], 0\n"
    "buffer_store_dwordx4 v[200:203], v5, s[80:83], 0 offen offset:0 sc1\n"
    "v_mfma_f32_16x16x32_bf16 v[220:223], v[128:131], v[176:179], 0\n"
    "v_mul_f32 v208, v208, v253\n"
    "v_mul_f32 v209, v209, v253\n"
    "v_mfma_f32_16x16x32_bf16 v[216:219], v[108:111], v[180:183], v[216:219]\n"
    "v_mul_f32 v210, v210, v253\n"
    "v_mul_f32 v211, v211, v253\n"
    "v_mfma_f32_16x16x32_bf16 v[220:223], v[132:135], v[180:183], v[220:223]\n"
    "v_mul_f32 v212, v212, v253\n"
    "v_mul_f32 v213, v213, v253\n"
    "v_mfma_f32_16x16x32_bf16 v[216:219], v[112:115], v[184:187], v[216:219]\n"
    "v_mul_f32 v214, v214, v253\n"
    "v_mul_f32 v215, v215, v253\n"
    "v_mfma_f32_16x16x32_bf16 v[220:223], v[136:139], v[184:187], v[220:223]\n"
    "v_cvt_pk_bf16_f32 v208, v208, v209\n"
    "v_cvt_pk_bf16_f32 v209, v210, v211\n"
    "v_mfma_f32_16x16x32_bf16 v[216:219], v[116:119], v[188:191], v[216:219]\n"
    "v_cvt_pk_bf16_f32 v210, v212, v213\n"
    "v_cvt_pk_bf16_f32 v211, v214, v215\n"
    "v_mfma_f32_16x16x32_bf16 v[220:223], v[140:143], v[188:191], v[220:223]\n"
    "buffer_store_dwordx4 v[208:211], v5, s[80:83], 0 offen offset:64 sc1\n"
    "v_mfma_f32_16x16x32_bf16 v[216:219], v[120:123], v[192:195], v[216:219]\n"
    "v_mfma_f32_16x16x32_bf16 v[220:223], v[144:147], v[192:195], v[220:223]\n"
    "v_mfma_f32_16x16x32_bf16 v[216:219], v[124:127], v[196:199], v[216:219]\n"
    "v_mfma_f32_16x16x32_bf16 v[220:223], v[148:151], v[196:199], v[220:223]\n"
    ".Lh5r:\n"
    "s_sub_u32 s52, s52, 1\n"
    "s_cmp_lt_i32 s52, 0\n"
    "s_cbranch_scc1 .Lh5g\n"
    "s_sleep 1\n"
    "ds_read_b32 v228, v224 offset:160\n"
    "s_waitcnt lgkmcnt(0)\n"
    "v_cmp_gt_u32 vcc, 1, v228\n"
    "s_cbranch_vccnz .Lh5r\n"
    "s_branch .Lh5g\n"
    ".Lh5g:\n"
    "ds_read_b64_tr_b16 v[152:153], v2 offset:30720\n"
    "ds_read_b64_tr_b16 v[154:155], v3 offset:30720\n"
    "ds_read_b64_tr_b16 v[156:157], v2 offset:31744\n"
    "ds_read_b64_tr_b16 v[158:159], v3 offset:31744\n"
    "ds_read_b64_tr_b16 v[160:161], v2 offset:32768\n"
    "ds_read_b64_tr_b16 v[162:163], v3 offset:32768\n"
    "ds_read_b64_tr_b16 v[164:165], v2 offset:33792\n"
    "ds_read_b64_tr_b16 v[166:167], v3 offset:33792\n"
    "ds_read_b64_tr_b16 v[168:169], v2 offset:34816\n"
    "ds_read_b64_tr_b16 v[170:171], v3 offset:34816\n"
    "ds_read_b64_tr_b16 v[172:173], v2 offset:35840\n"
    "ds_read_b64_tr_b16 v[174:175], v3 offset:35840\n"
    "ds_read_b32 v252, v1 offset:128\n"
    "v_mov_b32 v227, 3\n"
    "ds_write_b32 v226, v227 offset:32\n"
    "s_branch .Lh5j\n"
    ".Lh6:\n"
    "v_mfma_f32_16x16x32_bf16 v[216:219], v[104:107], v[152:155], 0\n"
    "buffer_store_dwordx4 v[200:203], v4, s[80:83], 0 offen offset:0 sc1\n"
    "v_mfma_f32_16x16x32_bf16 v[220:223], v[128:131], v[152:155], 0\n"
    "v_mul_f32 v208, v208, v252\n"
    "v_mul_f32 v209, v209, v252\n"
    "v_mfma_f32_16x16x32_bf16 v[216:219], v[108:111], v[156:159], v[216:219]\n"
    "v_mul_f32 v210, v210, v252\n"
    "v_mul_f32 v211, v211, v252\n"
    "v_mfma_f32_16x16x32_bf16 v[220:223], v[132:135], v[156:159], v[220:223]\n"
    "v_mul_f32 v212, v212, v252\n"
    "v_mul_f32 v213, v213, v252\n"
    "v_mfma_f32_16x16x32_bf16 v[216:219], v[112:115], v[160:163], v[216:219]\n"
    "v_mul_f32 v214, v214, v252\n"
    "v_mul_f32 v215, v215, v252\n"
    "v_mfma_f32_16x16x32_bf16 v[220:223], v[136:139], v[160:163], v[220:223]\n"
    "v_cvt_pk_bf16_f32 v208, v208, v209\n"
    "v_cvt_pk_bf16_f32 v209, v210, v211\n"
    "v_mfma_f32_16x16x32_bf16 v[216:219], v[116:119], v[164:167], v[216:219]\n"
    "v_cvt_pk_bf16_f32 v210, v212, v213\n"
    "v_cvt_pk_bf16_f32 v211, v214, v215\n"
    "v_mfma_f32_16x16x32_bf16 v[220:223], v[140:143], v[164:167], v[220:223]\n"
    "buffer_store_dwordx4 v[208:211], v4, s[80:83], 0 offen offset:64 sc1\n"
    "v_mfma_f32_16x16x32_bf16 v[216:219], v[120:123], v[168:171], v[216:219]\n"
    "v_mfma_f32_16x16x32_bf16 v[220:223], v[144:147], v[168:171], v[220:223]\n"
    "v_mfma_f32_16x16x32_bf16 v[216:219], v[124:127], v[172:175], v[216:219]\n"
    "v_mfma_f32_16x16x32_bf16 v[220:223], v[148:151], v[172:175], v[220:223]\n"
    ".Lh6r:\n"
    "s_sub_u32 s52, s52, 1\n"
    "s_cmp_lt_i32 s52, 0\n"
    "s_cbranch_scc1 .Lh6g\n"
    "s_sleep 1\n"
    "ds_read_b32 v228, v224 offset:176\n"
    "s_waitcnt lgkmcnt(0)\n"
    "v_cmp_gt_u32 vcc, 1, v228\n"
    "s_cbranch_vccnz .Lh6r\n"
    "s_branch .Lh6g\n"
    ".Lh6g:\n"
    "ds_read_b64_tr_b16 v[176:177], v2 offset:18432\n"
    "ds_read_b64_tr_b16 v[178:179], v3 offset:18432\n"
    "ds_read_b64_tr_b16 v[180:181], v2 offset:19456\n"
    "ds_read_b64_tr_b16 v[182:183], v3 offset:19456\n"
    "ds_read_b64_tr_b16 v[184:185], v2 offset:20480\n"
    "ds_read_b64_tr_b16 v[186:187], v3 offset:20480\n"
    "ds_read_b64_tr_b16 v[188:189], v2 offset:21504\n"
    "ds_read_b64_tr_b16 v[190:191], v3 offset:21504\n"
    "ds_read_b64_tr_b16 v[192:193], v2 offset:22528\n"
    "ds_read_b64_tr_b16 v[194:195], v3 offset:22528\n"
    "ds_read_b64_tr_b16 v[196:197], v2 offset:23552\n"
    "ds_read_b64_tr_b16 v[198:199], v3 offset:23552\n"
    "ds_read_b32 v253, v1 offset:0\n"
    "v_mov_b32 v227, 4\n"
    "ds_write_b32 v226, v227 offset:32\n"
    "s_branch .Lh6j\n"
    ".Lh7:\n"
    "v_mfma_f32_16x16x32_bf16 v[216:219], v[104:107], v[176:179], 0\n"
    "buffer_store_dwordx4 v[200:203], v5, s[80:83], 0 offen offset:0 sc1\n"
    "v_mfma_f32_16x16x32_bf16 v[220:223], v[128:131], v[176:179], 0\n"
    "v_mul_f32 v208, v208, v253\n"
    "v_mul_f32 v209, v209, v253\n"
    "v_mfma_f32_16x16x32_bf16 v[216:219], v[108:111], v[180:183], v[216:219]\n"
    "v_mul_f32 v210, v210, v253\n"
    "v_mul_f32 v211, v211, v253\n"
    "v_mfma_f32_16x16x32_bf16 v[220:223], v[132:135], v[180:183], v[220:223]\n"
    "v_mul_f32 v212, v212, v253\n"
    "v_mul_f32 v213, v213, v253\n"
    "v_mfma_f32_16x16x32_bf16 v[216:219], v[112:115], v[184:187], v[216:219]\n"
    "v_mul_f32 v214, v214, v253\n"
    "v_mul_f32 v215, v215, v253\n"
    "v_mfma_f32_16x16x32_bf16 v[220:223], v[136:139], v[184:187], v[220:223]\n"
    "v_cvt_pk_bf16_f32 v208, v208, v209\n"
    "v_cvt_pk_bf16_f32 v209, v210, v211\n"
    "v_mfma_f32_16x16x32_bf16 v[216:219], v[116:119], v[188:191], v[216:219]\n"
    "v_cvt_pk_bf16_f32 v210, v212, v213\n"
    "v_cvt_pk_bf16_f32 v211, v214, v215\n"
    "v_mfma_f32_16x16x32_bf16 v[220:223], v[140:143], v[188:191], v[220:223]\n"
    "buffer_store_dwordx4 v[208:211], v5, s[80:83], 0 offen offset:64 sc1\n"
    "v_mfma_f32_16x16x32_bf16 v[216:219], v[120:123], v[192:195], v[216:219]\n"
    "v_mfma_f32_16x16x32_bf16 v[220:223], v[144:147], v[192:195], v[220:223]\n"
    "v_mfma_f32_16x16x32_bf16 v[216:219], v[124:127], v[196:199], v[216:219]\n"
    "v_mfma_f32_16x16x32_bf16 v[220:223], v[148:151], v[196:199], v[220:223]\n"
    ".Lh7r:\n"
    "s_sub_u32 s52, s52, 1\n"
    "s_cmp_lt_i32 s52, 0\n"
    "s_cbranch_scc1 .Lh7g\n"
    "s_sleep 1\n"
    "ds_read_b32 v228, v224 offset:128\n"
    "s_waitcnt lgkmcnt(0)\n"
    "v_cmp_gt_u32 vcc, 2, v228\n"
    "s_cbranch_vccnz .Lh7r\n"
    "s_branch .Lh7g\n"
    ".Lh7g:\n"
    "ds_read_b64_tr_b16 v[152:153], v2 offset:24576\n"
    "ds_read_b64_tr_b16 v[154:155], v3 offset:24576\n"
    "ds_read_b64_tr_b16 v[156:157], v2 offset:25600\n"
    "ds_read_b64_tr_b16 v[158:159], v3 offset:25600\n"
    "ds_read_b64_tr_b16 v[160:161], v2 offset:26624\n"
    "ds_read_b64_tr_b16 v[162:163], v3 offset:26624\n"
    "ds_read_b64_tr_b16 v[164:165], v2 offset:27648\n"
    "ds_read_b64_tr_b16 v[166:167], v3 offset:27648\n"
    "ds_read_b64_tr_b16 v[168:169], v2 offset:28672\n"
    "ds_read_b64_tr_b16 v[170:171], v3 offset:28672\n"
    "ds_read_b64_tr_b16 v[172:173], v2 offset:29696\n"
    "ds_read_b64_tr_b16 v[174:175], v3 offset:29696\n"
    "ds_read_b32 v252, v1 offset:64\n"
    "v_mov_b32 v227, 5\n"
    "ds_write_b32 v226, v227 offset:32\n"
    "s_branch .Lh7j\n"
    ".Lh8:\n"
    "v_mfma_f32_16x16x32_bf16 v[216:219], v[104:107], v[152:155], 0\n"
    "buffer_store_dwordx4 v[200:203], v4, s[80:83], 0 offen offset:0 sc1\n"
    "v_mfma_f32_16x16x32_bf16 v[220:223], v[128:131], v[152:155], 0\n"
    "v_mul_f32 v208, v208, v252\n"
    "v_mul_f32 v209, v209, v252\n"
    "v_mfma_f32_16x16x32_bf16 v[216:219], v[108:111], v[156:159], v[216:219]\n"
    "v_mul_f32 v210, v210, v252\n"
    "v_mul_f32 v211, v211, v252\n"
    "v_mfma_f32_16x16x32_bf16 v[220:223], v[132:135], v[156:159], v[220:223]\n"
    "v_mul_f32 v212, v212, v252\n"
    "v_mul_f32 v213, v213, v252\n"
    "v_mfma_f32_16x16x32_bf16 v[216:219], v[112:115], v[160:163], v[216:219]\n"
    "v_mul_f32 v214, v214, v252\n"
    "v_mul_f32 v215, v215, v252\n"
    "v_mfma_f32_16x16x32_bf16 v[220:223], v[136:139], v[160:163], v[220:223]\n"
    "v_cvt_pk_bf16_f32 v208, v208, v209\n"
    "v_cvt_pk_bf16_f32 v209, v210, v211\n"
    "v_mfma_f32_16x16x32_bf16 v[216:219], v[116:119], v[164:167], v[216:219]\n"
    "v_cvt_pk_bf16_f32 v210, v212, v213\n"
    "v_cvt_pk_bf16_f32 v211, v214, v215\n"
    "v_mfma_f32_16x16x32_bf16 v[220:223], v[140:143], v[164:167], v[220:223]\n"
    "buffer_store_dwordx4 v[208:211], v4, s[80:83], 0 offen offset:64 sc1\n"
    "v_mfma_f32_16x16x32_bf16 v[216:219], v[120:123], v[168:171], v[216:219]\n"
    "v_mfma_f32_16x16x32_bf16 v[220:223], v[144:147], v[168:171], v[220:223]\n"
    "v_mfma_f32_16x16x32_bf16 v[216:219], v[124:127], v[172:175], v[216:219]\n"
    "v_mfma_f32_16x16x32_bf16 v[220:223], v[148:151], v[172:175], v[220:223]\n"
    ".Lh8r:\n"
    "s_sub_u32 s52, s52, 1\n"
    "s_cmp_lt_i32 s52, 0\n"
    "s_cbranch_scc1 .Lh8g\n"
    "s_sleep 1\n"
    "ds_read_b32 v228, v224 offset:144\n"
    "s_waitcnt lgkmcnt(0)\n"
    "v_cmp_gt_u32 vcc, 2, v228\n"
    "s_cbranch_vccnz .Lh8r\n"
    "s_branch .Lh8g\n"
    ".Lh8g:\n"
    "ds_read_b64_tr_b16 v[176:177], v2 offset:30720\n"
    "ds_read_b64_tr_b16 v[178:179], v3 offset:30720\n"
    "ds_read_b64_tr_b16 v[180:181], v2 offset:31744\n"
    "ds_read_b64_tr_b16 v[182:183], v3 offset:31744\n"
    "ds_read_b64_tr_b16 v[184:185], v2 offset:32768\n"
    "ds_read_b64_tr_b16 v[186:187], v3 offset:32768\n"
    "ds_read_b64_tr_b16 v[188:189], v2 offset:33792\n"
    "ds_read_b64_tr_b16 v[190:191], v3 offset:33792\n"
    "ds_read_b64_tr_b16 v[192:193], v2 offset:34816\n"
    "ds_read_b64_tr_b16 v[194:195], v3 offset:34816\n"
    "ds_read_b64_tr_b16 v[196:197], v2 offset:35840\n"
    "ds_read_b64_tr_b16 v[198:199], v3 offset:35840\n"
    "ds_read_b32 v253, v1 offset:128\n"
    "v_mov_b32 v227, 6\n"
    "ds_write_b32 v226, v227 offset:32\n"
    "s_branch .Lh8j\n"
    ".Lh9:\n"
    "v_mfma_f32_16x16x32_bf16 v[216:219], v[104:107], v[176:179], 0\n"
    "buffer_store_dwordx4 v[200:203], v5, s[80:83], 0 offen offset:0 sc1\n"
    "v_mfma_f32_16x16x32_bf16 v[220:223], v[128:131], v[176:179], 0\n"
    "v_mul_f32 v208, v208, v253\n"
    "v_mul_f32 v209, v209, v253\n"
    "v_mfma_f32_16x16x32_bf16 v[216:219], v[108:111], v[180:183], v[216:219]\n"
    "v_mul_f32 v210, v210, v253\n"
    "v_mul_f32 v211, v211, v253\n"
    "v_mfma_f32_16x16x32_bf16 v[220:223], v[132:135], v[180:183], v[220:223]\n"
    "v_mul_f32 v212, v212, v253\n"
    "v_mul_f32 v213, v213, v253\n"
    "v_mfma_f32_16x16x32_bf16 v[216:219], v[112:115], v[184:187], v[216:219]\n"
    "v_mul_f32 v214, v214, v253\n"
    "v_mul_f32 v215, v215, v253\n"
    "v_mfma_f32_16x16x32_bf16 v[220:223], v[136:139], v[184:187], v[220:223]\n"
    "v_cvt_pk_bf16_f32 v208, v208, v209\n"
    "v_cvt_pk_bf16_f32 v209, v210, v211\n"
    "v_mfma_f32_16x16x32_bf16 v[216:219], v[116:119], v[188:191], v[216:219]\n"
    "v_cvt_pk_bf16_f32 v210, v212, v213\n"
    "v_cvt_pk_bf16_f32 v211, v214, v215\n"
    "v_mfma_f32_16x16x32_bf16 v[220:223], v[140:143], v[188:191], v[220:223]\n"
    "buffer_store_dwordx4 v[208:211], v5, s[80:83], 0 offen offset:64 sc1\n"
    "v_mfma_f32_16x16x32_bf16 v[216:219], v[120:123], v[192:195], v[216:219]\n"
    "v_mfma_f32_16x16x32_bf16 v[220:223], v[144:147], v[192:195], v[220:223]\n"
    "v_mfma_f32_16x16x32_bf16 v[216:219], v[124:127], v[196:199], v[216:219]\n"
    "v_mfma_f32_16x16x32_bf16 v[220:223], v[148:151], v[196:199], v[220:223]\n"
    ".Lh9r:\n"
    "s_sub_u32 s52, s52, 1\n"
    "s_cmp_lt_i32 s52, 0\n"
    "s_cbranch_scc1 .Lh9g\n"
    "s_sleep 1\n"
    "ds_read_b32 v228, v224 offset:160\n"
    "s_waitcnt lgkmcnt(0)\n"
    "v_cmp_gt_u32 vcc, 2, v228\n"
    "s_cbranch_vccnz .Lh9r\n"
    "s_branch .Lh9g\n"
    ".Lh9g:\n"
    "ds_read_b64_tr_b16 v[152:153], v2 offset:18432\n"
    "ds_read_b64_tr_b16 v[154:155], v3 offset:18432\n"
    "ds_read_b64_tr_b16 v[156:157], v2 offset:19456\n"
    "ds_read_b64_tr_b16 v[158:159], v3 offset:19456\n"
    "ds_read_b64_tr_b16 v[160:161], v2 offset:20480\n"
    "ds_read_b64_tr_b16 v[162:163], v3 offset:20480\n"
    "ds_read_b64_tr_b16 v[164:165], v2 offset:21504\n"
    "ds_read_b64_tr_b16 v[166:167], v3 offset:21504\n"
    "ds_read_b64_tr_b16 v[168:169], v2 offset:22528\n"
    "ds_read_b64_tr_b16 v[170:171], v3 offset:22528\n"
    "ds_read_b64_tr_b16 v[172:173], v2 offset:23552\n"
    "ds_read_b64_tr_b16 v[174:175], v3 offset:23552\n"
    "ds_read_b32 v252, v1 offset:0\n"
    "v_mov_b32 v227, 7\n"
    "ds_write_b32 v226, v227 offset:32\n"
    "s_branch .Lh9j\n"
    ".Lh10:\n"
    "v_mfma_f32_16x16x32_bf16 v[216:219], v[104:107], v[152:155], 0\n"
    "buffer_store_dwordx4 v[200:203], v4, s[80:83], 0 offen offset:0 sc1\n"
    "v_mfma_f32_16x16x32_bf16 v[220:223], v[128:131], v[152:155], 0\n"
    "v_mul_f32 v208, v208, v252\n"
    "v_mul_f32 v209, v209, v252\n"
    "v_mfma_f32_16x16x32_bf16 v[216:219], v[108:111], v[156:159], v[216:219]\n"
    "v_mul_f32 v210, v210, v252\n"
    "v_mul_f32 v211, v211, v252\n"
    "v_mfma_f32_16x16x32_bf16 v[220:223], v[132:135], v[156:159], v[220:223]\n"
    "v_mul_f32 v212, v212, v252\n"
    "v_mul_f32 v213, v213, v252\n"
    "v_mfma_f32_16x16x32_bf16 v[216:219], v[112:115], v[160:163], v[216:219]\n"
    "v_mul_f32 v214, v214, v252\n"
    "v_mul_f32 v215, v215, v252\n"
    "v_mfma_f32_16x16x32_bf16 v[220:223], v[136:139], v[160:163], v[220:223]\n"
    "v_cvt_pk_bf16_f32 v208, v208, v209\n"
    "v_cvt_pk_bf16_f32 v209, v210, v211\n"
    "v_mfma_f32_16x16x32_bf16 v[216:219], v[116:119], v[164:167], v[216:219]\n"
    "v_cvt_pk_bf16_f32 v210, v212, v213\n"
    "v_cvt_pk_bf16_f32 v211, v214, v215\n"
    "v_mfma_f32_16x16x32_bf16 v[220:223], v[140:143], v[164:167], v[220:223]\n"
    "buffer_store_dwordx4 v[208:211], v4, s[80:83], 0 offen offset:64 sc1\n"
    "v_mfma_f32_16x16x32_bf16 v[216:219], v[120:123], v[168:171], v[216:219]\n"
    "v_mfma_f32_16x16x32_bf16 v[220:223], v[144:147], v[168:171], v[220:223]\n"
    "v_mfma_f32_16x16x32_bf16 v[216:219], v[124:127], v[172:175], v[216:219]\n"
    "v_mfma_f32_16x16x32_bf16 v[220:223], v[148:151], v[172:175], v[220:223]\n"
    ".Lh10r:\n"
    "s_sub_u32 s52, s52, 1\n"
    "s_cmp_lt_i32 s52, 0\n"
    "s_cbranch_scc1 .Lh10g\n"
    "s_sleep 1\n"
    "ds_read_b32 v228, v224 offset:176\n"
    "s_waitcnt lgkmcnt(0)\n"
    "v_cmp_gt_u32 vcc, 2, v228\n"
    "s_cbranch_vccnz .Lh10r\n"
    "s_branch .Lh10g\n"
    ".Lh10g:\n"
    "ds_read_b64_tr_b16 v[176:177], v2 offset:24576\n"
    "ds_read_b64_tr_b16 v[178:179], v3 offset:24576\n"
    "ds_read_b64_tr_b16 v[180:181], v2 offset:25600\n"
    "ds_read_b64_tr_b16 v[182:183], v3 offset:25600\n"
    "ds_read_b64_tr_b16 v[184:185], v2 offset:26624\n"
    "ds_read_b64_tr_b16 v[186:187], v3 offset:26624\n"
    "ds_read_b64_tr_b16 v[188:189], v2 offset:27648\n"
    "ds_read_b64_tr_b16 v[190:191], v3 offset:27648\n"
    "ds_read_b64_tr_b16 v[192:193], v2 offset:28672\n"
    "ds_read_b64_tr_b16 v[194:195], v3 offset:28672\n"
    "ds_read_b64_tr_b16 v[196:197], v2 offset:29696\n"
    "ds_read_b64_tr_b16 v[198:199], v3 offset:29696\n"
    "ds_read_b32 v253, v1 offset:64\n"
    "s_branch .Lh10j\n"
    ".Lh11:\n"
    "v_mfma_f32_16x16x32_bf16 v[216:219], v[104:107], v[176:179], 0\n"
    "buffer_store_dwordx4 v[200:203], v5, s[80:83], 0 offen offset:0 sc1\n"
    "v_mfma_f32_16x16x32_bf16 v[220:223], v[128:131], v[176:179], 0\n"
    "v_mul_f32 v208, v208, v253\n"
    "v_mul_f32 v209, v209, v253\n"
    "v_mfma_f32_16x16x32_bf16 v[216:219], v[108:111], v[180:183], v[216:219]\n"
    "v_mul_f32 v210, v210, v253\n"
    "v_mul_f32 v211, v211, v253\n"
    "v_mfma_f32_16x16x32_bf16 v[220:223], v[132:135], v[180:183], v[220:223]\n"
    "v_mul_f32 v212, v212, v253\n"
    "v_mul_f32 v213, v213, v253\n"
    "v_mfma_f32_16x16x32_bf16 v[216:219], v[112:115], v[184:187], v[216:219]\n"
    "v_mul_f32 v214, v214, v253\n"
    "v_mul_f32 v215, v215, v253\n"
    "v_mfma_f32_16x16x32_bf16 v[220:223], v[136:139], v[184:187], v[220:223]\n"
    "v_cvt_pk_bf16_f32 v208, v208, v209\n"
    "v_cvt_pk_bf16_f32 v209, v210, v211\n"
    "v_mfma_f32_16x16x32_bf16 v[216:219], v[116:119], v[188:191], v[216:219]\n"
    "v_cvt_pk_bf16_f32 v210, v212, v213\n"
    "v_cvt_pk_bf16_f32 v211, v214, v215\n"
    "v_mfma_f32_16x16x32_bf16 v[220:223], v[140:143], v[188:191], v[220:223]\n"
    "buffer_store_dwordx4 v[208:211], v5, s[80:83], 0 offen offset:64 sc1\n"
    "v_mfma_f32_16x16x32_bf16 v[216:219], v[120:123], v[192:195], v[216:219]\n"
    "v_mfma_f32_16x16x32_bf16 v[220:223], v[144:147], v[192:195], v[220:223]\n"
    "v_mfma_f32_16x16x32_bf16 v[216:219], v[124:127], v[196:199], v[216:219]\n"
    "v_mfma_f32_16x16x32_bf16 v[220:223], v[148:151], v[196:199], v[220:223]\n"
    ".Lh11r:\n"
    "s_sub_u32 s52, s52, 1\n"
    "s_cmp_lt_i32 s52, 0\n"
    "s_cbranch_scc1 .Lh11g\n"
    "s_sleep 1\n"
    "ds_read_b32 v228, v224 offset:128\n"
    "s_waitcnt lgkmcnt(0)\n"
    "v_cmp_gt_u32 vcc, 3, v228\n"
    "s_cbranch_vccnz .Lh11r\n"
    "s_branch .Lh11g\n"
    ".Lh11g:\n"
    "ds_read_b64_tr_b16 v[152:153], v2 offset:30720\n"
    "ds_read_b64_tr_b16 v[154:155], v3 offset:30720\n"
    "ds_read_b64_tr_b16 v[156:157], v2 offset:31744\n"
    "ds_read_b64_tr_b16 v[158:159], v3 offset:31744\n"
    "ds_read_b64_tr_b16 v[160:161], v2 offset:32768\n"
    "ds_read_b64_tr_b16 v[162:163], v3 offset:32768\n"
    "ds_read_b64_tr_b16 v[164:165], v2 offset:33792\n"
    "ds_read_b64_tr_b16 v[166:167], v3 offset:33792\n"
    "ds_read_b64_tr_b16 v[168:169], v2 offset:34816\n"
    "ds_read_b64_tr_b16 v[170:171], v3 offset:34816\n"
    "ds_read_b64_tr_b16 v[172:173], v2 offset:35840\n"
    "ds_read_b64_tr_b16 v[174:175], v3 offset:35840\n"
    "ds_read_b32 v252, v1 offset:128\n"
    "s_branch .Lh11j\n"
    ".Lh12:\n"
    "v_mfma_f32_16x16x32_bf16 v[208:211], v[56:59], v[152:155], 0\n"
    "v_mfma_f32_16x16x32_bf16 v[212:215], v[80:83], v[152:155], 0\n"
    "v_mfma_f32_16x16x32_bf16 v[208:211], v[60:63], v[156:159], v[208:211]\n"
    "v_mfma_f32_16x16x32_bf16 v[212:215], v[84:87], v[156:159], v[212:215]\n"
    "v_mul_f32 v200, v200, v252\n"
    "v_mul_f32 v201, v201, v252\n"
    "v_mfma_f32_16x16x32_bf16 v[208:211], v[64:67], v[160:163], v[208:211]\n"
    "v_mul_f32 v202, v202, v252\n"
    "v_mul_f32 v203, v203, v252\n"
    "v_mfma_f32_16x16x32_bf16 v[212:215], v[88:91], v[160:163], v[212:215]\n"
    "v_mul_f32 v204, v204, v252\n"
    "v_mul_f32 v205, v205, v252\n"
    "v_mfma_f32_16x16x32_bf16 v[208:211], v[68:71], v[164:167], v[208:211]\n"
    "v_mul_f32 v206, v206, v252\n"
    "v_mul_f32 v207, v207, v252\n"
    "v_mfma_f32_16x16x32_bf16 v[212:215], v[92:95], v[164:167], v[212:215]\n"
    "v_cvt_pk_bf16_f32 v200, v200, v201\n"
    "v_cvt_pk_bf16_f32 v201, v202, v203\n"
    "v_mfma_f32_16x16x32_bf16 v[208:211], v[72:75], v[168:171], v[208:211]\n"
    "v_cvt_pk_bf16_f32 v202, v204, v205\n"
    "v_cvt_pk_bf16_f32 v203, v206, v207\n"
    "v_mfma_f32_16x16x32_bf16 v[212:215], v[96:99], v[168:171], v[212:215]\n"
    "buffer_store_dwordx4 v[200:203], v4, s[80:83], 0 offen offset:0 sc1\n"
    "v_mfma_f32_16x16x32_bf16 v[208:211], v[76:79], v[172:175], v[208:211]\n"
    "v_mfma_f32_16x16x32_bf16 v[212:215], v[100:103], v[172:175], v[212:215]\n"
    ".Lh12r:\n"
    "s_sub_u32 s52, s52, 1\n"
    "s_cmp_lt_i32 s52, 0\n"
    "s_cbranch_scc1 .Lh12g\n"
    "s_sleep 1\n"
    "ds_read_b32 v228, v224 offset:144\n"
    "s_waitcnt lgkmcnt(0)\n"
    "v_cmp_gt_u32 vcc, 3, v228\n"
    "s_cbranch_vccnz .Lh12r\n"
    "s_branch .Lh12g\n"
    ".Lh12g:\n"
    "ds_read_b64_tr_b16 v[176:177], v2 offset:18432\n"
    "ds_read_b64_tr_b16 v[178:179], v3 offset:18432\n"
    "ds_read_b64_tr_b16 v[180:181], v2 offset:19456\n"
    "ds_read_b64_tr_b16 v[182:183], v3 offset:19456\n"
    "ds_read_b64_tr_b16 v[184:185], v2 offset:20480\n"
    "ds_read_b64_tr_b16 v[186:187], v3 offset:20480\n"
    "ds_read_b64_tr_b16 v[188:189], v2 offset:21504\n"
    "ds_read_b64_tr_b16 v[190:191], v3 offset:21504\n"
    "ds_read_b64_tr_b16 v[192:193], v2 offset:22528\n"
    "ds_read_b64_tr_b16 v[194:195], v3 offset:22528\n"
    "ds_read_b64_tr_b16 v[196:197], v2 offset:23552\n"
    "ds_read_b64_tr_b16 v[198:199], v3 offset:23552\n"
    "ds_read_b32 v253, v1 offset:0\n"
    "s_branch .Lh12j\n"
    ".Lh13:\n"
    "v_mfma_f32_16x16x32_bf16 v[208:211], v[56:59], v[176:179], 0\n"
    "v_mfma_f32_16x16x32_bf16 v[212:215], v[80:83], v[176:179], 0\n"
    "v_mfma_f32_16x16x32_bf16 v[208:211], v[60:63], v[180:183], v[208:211]\n"
    "v_mfma_f32_16x16x32_bf16 v[212:215], v[84:87], v[180:183], v[212:215]\n"
    "v_mul_f32 v200, v200, v253\n"
    "v_mul_f32 v201, v201, v253\n"
    "v_mfma_f32_16x16x32_bf16 v[208:211], v[64:67], v[184:187], v[208:211]\n"
    "v_mul_f32 v202, v202, v253\n"
    "v_mul_f32 v203, v203, v253\n"
    "v_mfma_f32_16x16x32_bf16 v[212:215], v[88:91], v[184:187], v[212:215]\n"
    "v_mul_f32 v204, v204, v253\n"
    "v_mul_f32 v205, v205, v253\n"
    "v_mfma_f32_16x16x32_bf16 v[208:211], v[68:71], v[188:191], v[208:211]\n"
    "v_mul_f32 v206, v206, v253\n"
    "v_mul_f32 v207, v207, v253\n"
    "v_mfma_f32_16x16x32_bf16 v[212:215], v[92:95], v[188:191], v[212:215]\n"
    "v_cvt_pk_bf16_f32 v200, v200, v201\n"
    "v_cvt_pk_bf16_f32 v201, v202, v203\n"
    "v_mfma_f32_16x16x32_bf16 v[208:211], v[72:75], v[192:195], v[208:211]\n"
    "v_cvt_pk_bf16_f32 v202, v204, v205\n"
    "v_cvt_pk_bf16_f32 v203, v206, v207\n"
    "v_mfma_f32_16x16x32_bf16 v[212:215], v[96:99], v[192:195], v[212:215]\n"
    "buffer_store_dwordx4 v[200:203], v5, s[80:83], 0 offen offset:0 sc1\n"
    "v_mfma_f32_16x16x32_bf16 v[208:211], v[76:79], v[196:199], v[208:211]\n"
    "v_mfma_f32_16x16x32_bf16 v[212:215], v[100:103], v[196:199], v[212:215]\n"
    ".Lh13r:\n"
    "s_sub_u32 s52, s52, 1\n"
    "s_cmp_lt_i32 s52, 0\n"
    "s_cbranch_scc1 .Lh13g\n"
    "s_sleep 1\n"
    "ds_read_b32 v228, v224 offset:160\n"
    "s_waitcnt lgkmcnt(0)\n"
    "v_cmp_gt_u32 vcc, 3, v228\n"
    "s_cbranch_vccnz .Lh13r\n"
    "s_branch .Lh13g\n"
    ".Lh13g:\n"
    "ds_read_b64_tr_b16 v[152:153], v2 offset:6144\n"
    "ds_read_b64_tr_b16 v[154:155], v3 offset:6144\n"
    "ds_read_b64_tr_b16 v[156:157], v2 offset:7168\n"
    "ds_read_b64_tr_b16 v[158:159], v3 offset:7168\n"
    "ds_read_b64_tr_b16 v[160:161], v2 offset:8192\n"
    "ds_read_b64_tr_b16 v[162:163], v3 offset:8192\n"
    "ds_read_b64_tr_b16 v[164:165], v2 offset:9216\n"
    "ds_read_b64_tr_b16 v[166:167], v3 offset:9216\n"
    "ds_read_b64_tr_b16 v[168:169], v2 offset:10240\n"
    "ds_read_b64_tr_b16 v[170:171], v3 offset:10240\n"
    "ds_read_b64_tr_b16 v[172:173], v2 offset:11264\n"
    "ds_read_b64_tr_b16 v[174:175], v3 offset:11264\n"
    "ds_read_b32 v252, v1 offset:256\n"
    "s_branch .Lh13j\n"
    ".Lh14:\n"
    "v_mfma_f32_16x16x32_bf16 v[208:211], v[56:59], v[152:155], 0\n"
    "v_mfma_f32_16x16x32_bf16 v[212:215], v[80:83], v[152:155], 0\n"
    "v_mfma_f32_16x16x32_bf16 v[208:211], v[60:63], v[156:159], v[208:211]\n"
    "v_mfma_f32_16x16x32_bf16 v[212:215], v[84:87], v[156:159], v[212:215]\n"
    "v_mul_f32 v200, v200, v252\n"
    "v_mul_f32 v201, v201, v252\n"
    "v_mfma_f32_16x16x32_bf16 v[208:211], v[64:67], v[160:163], v[208:211]\n"
    "v_mul_f32 v202, v202, v252\n"
    "v_mul_f32 v203, v203, v252\n"
    "v_mfma_f32_16x16x32_bf16 v[212:215], v[88:91], v[160:163], v[212:215]\n"
    "v_mul_f32 v204, v204, v252\n"
    "v_mul_f32 v205, v205, v252\n"
    "v_mfma_f32_16x16x32_bf16 v[208:211], v[68:71], v[164:167], v[208:211]\n"
    "v_mul_f32 v206, v206, v252\n"
    "v_mul_f32 v207, v207, v252\n"
    "v_mfma_f32_16x16x32_bf16 v[212:215], v[92:95], v[164:167], v[212:215]\n"
    "v_cvt_pk_bf16_f32 v200, v200, v201\n"
    "v_cvt_pk_bf16_f32 v201, v202, v203\n"
    "v_mfma_f32_16x16x32_bf16 v[208:211], v[72:75], v[168:171], v[208:211]\n"
    "v_cvt_pk_bf16_f32 v202, v204, v205\n"
    "v_cvt_pk_bf16_f32 v203, v206, v207\n"
    "v_mfma_f32_16x16x32_bf16 v[212:215], v[96:99], v[168:171], v[212:215]\n"
    "buffer_store_dwordx4 v[200:203], v4, s[80:83], 0 offen offset:0 sc1\n"
    "v_mfma_f32_16x16x32_bf16 v[208:211], v[76:79], v[172:175], v[208:211]\n"
    "v_mfma_f32_16x16x32_bf16 v[212:215], v[100:103], v[172:175], v[212:215]\n"
    ".Lh14r:\n"
    "s_sub_u32 s52, s52, 1\n"
    "s_cmp_lt_i32 s52, 0\n"
    "s_cbranch_scc1 .Lh14g\n"
    "s_sleep 1\n"
    "ds_read_b32 v228, v224 offset:176\n"
    "s_waitcnt lgkmcnt(0)\n"
    "v_cmp_gt_u32 vcc, 3, v228\n"
    "s_cbranch_vccnz .Lh14r\n"
    "s_branch .Lh14g\n"
    ".Lh14g:\n"
    "ds_read_b64_tr_b16 v[176:177], v2 offset:12288\n"
    "ds_read_b64_tr_b16 v[178:179], v3 offset:12288\n"
    "ds_read_b64_tr_b16 v[180:181], v2 offset:13312\n"
    "ds_read_b64_tr_b16 v[182:183], v3 offset:13312\n"
    "ds_read_b64_tr_b16 v[184:185], v2 offset:14336\n"
    "ds_read_b64_tr_b16 v[186:187], v3 offset:14336\n"
    "ds_read_b64_tr_b16 v[188:189], v2 offset:15360\n"
    "ds_read_b64_tr_b16 v[190:191], v3 offset:15360\n"
    "ds_read_b64_tr_b16 v[192:193], v2 offset:16384\n"
    "ds_read_b64_tr_b16 v[194:195], v3 offset:16384\n"
    "ds_read_b64_tr_b16 v[196:197], v2 offset:17408\n"
    "ds_read_b64_tr_b16 v[198:199], v3 offset:17408\n"
    "ds_read_b32 v253, v1 offset:320\n"
    "s_branch .Lh14j\n"
    ".Lh_e12:\n"
    "s_waitcnt lgkmcnt(1)\n"
    "v_mfma_f32_16x16x32_bf16 v[200:203], v[8:11], v[176:179], 0\n"
    "v_mul_f32 v208, v208, v252\n"
    "v_mul_f32 v209, v209, v252\n"
    "v_mfma_f32_16x16x32_bf16 v[204:207], v[32:35], v[176:179], 0\n"
    "v_mul_f32 v210, v210, v252\n"
    "v_mul_f32 v211, v211, v252\n"
    "v_mfma_f32_16x16x32_bf16 v[200:203], v[12:15], v[180:183], v[200:203]\n"
    "v_mul_f32 v212, v212, v252\n"
    "v_mul_f32 v213, v213, v252\n"
    "v_mfma_f32_16x16x32_bf16 v[204:207], v[36:39], v[180:183], v[204:207]\n"
    "v_mul_f32 v214, v214, v252\n"
    "v_mul_f32 v215, v215, v252\n"
    "v_mfma_f32_16x16x32_bf16 v[200:203], v[16:19], v[184:187], v[200:203]\n"
    "v_cvt_pk_bf16_f32 v208, v208, v209\n"
    "v_cvt_pk_bf16_f32 v209, v210, v211\n"
    "v_mfma_f32_16x16x32_bf16 v[204:207], v[40:43], v[184:187], v[204:207]\n"
    "v_cvt_pk_bf16_f32 v210, v212, v213\n"
    "v_cvt_pk_bf16_f32 v211, v214, v215\n"
    "v_mfma_f32_16x16x32_bf16 v[200:203], v[20:23], v[188:191], v[200:203]\n"
    "buffer_store_dwordx4 v[208:211], v4, s[80:83], 0 offen offset:64 sc1\n"
    "v_mfma_f32_16x16x32_bf16 v[204:207], v[44:47], v[188:191], v[204:207]\n"
    "v_mfma_f32_16x16x32_bf16 v[200:203], v[24:27], v[192:195], v[200:203]\n"
    "v_add_u32 v4, v0, v4\n"
    "v_mfma_f32_16x16x32_bf16 v[204:207], v[48:51], v[192:195], v[204:207]\n"
    "v_mfma_f32_16x16x32_bf16 v[200:203], v[28:31], v[196:199], v[200:203]\n"
    "v_mfma_f32_16x16x32_bf16 v[204:207], v[52:55], v[196:199], v[204:207]\n"
    "v_mfma_f32_16x16x32_bf16 v[208:211], v[56:59], v[176:179], 0\n"
    "v_mfma_f32_16x16x32_bf16 v[212:215], v[80:83], v[176:179], 0\n"
    "v_mfma_f32_16x16x32_bf16 v[208:211], v[60:63], v[180:183], v[208:211]\n"
    "v_mfma_f32_16x16x32_bf16 v[212:215], v[84:87], v[180:183], v[212:215]\n"
    "v_mfma_f32_16x16x32_bf16 v[208:211], v[64:67], v[184:187], v[208:211]\n"
    "v_mfma_f32_16x16x32_bf16 v[212:215], v[88:91], v[184:187], v[212:215]\n"
    "s_waitcnt lgkmcnt(0)\n"
    "v_mul_f32 v200, v200, v253\n"
    "v_mfma_f32_16x16x32_bf16 v[208:211], v[68:71], v[188:191], v[208:211]\n"
    "v_mul_f32 v201, v201, v253\n"
    "v_mul_f32 v202, v202, v253\n"
    "v_mfma_f32_16x16x32_bf16 v[212:215], v[92:95], v[188:191], v[212:215]\n"
    "v_mul_f32 v203, v203, v253\n"
    "v_mul_f32 v204, v204, v253\n"
    "v_mfma_f32_16x16x32_bf16 v[208:211], v[72:75], v[192:195], v[208:211]\n"
    "v_mul_f32 v205, v205, v253\n"
    "v_mul_f32 v206, v206, v253\n"
    "v_mfma_f32_16x16x32_bf16 v[212:215], v[96:99], v[192:195], v[212:215]\n"
    "v_mul_f32 v207, v207, v253\n"
    "v_cvt_pk_bf16_f32 v200, v200, v201\n"
    "v_mfma_f32_16x16x32_bf16 v[208:211], v[76:79], v[196:199], v[208:211]\n"
    "v_cvt_pk_bf16_f32 v201, v202, v203\n"
    "v_cvt_pk_bf16_f32 v202, v204, v205\n"
    "v_mfma_f32_16x16x32_bf16 v[212:215], v[100:103], v[196:199], v[212:215]\n"
    "v_cvt_pk_bf16_f32 v203, v206, v207\n"
    "buffer_store_dwordx4 v[200:203], v5, s[80:83], 0 offen offset:0 sc1\n"
    "s_nop 2\n"
    "v_mul_f32 v208, v208, v253\n"
    "v_mul_f32 v209, v209, v253\n"
    "v_mul_f32 v210, v210, v253\n"
    "v_mul_f32 v211, v211, v253\n"
    "v_mul_f32 v212, v212, v253\n"
    "v_mul_f32 v213, v213, v253\n"
    "v_mul_f32 v214, v214, v253\n"
    "v_mul_f32 v215, v215, v253\n"
    "v_cvt_pk_bf16_f32 v208, v208, v209\n"
    "v_cvt_pk_bf16_f32 v209, v210, v211\n"
    "v_cvt_pk_bf16_f32 v210, v212, v213\n"
    "v_cvt_pk_bf16_f32 v211, v214, v215\n"
    "buffer_store_dwordx4 v[208:211], v5, s[80:83], 0 offen offset:64 sc1\n"
    "s_branch .Lh_end\n"
    ".Lh15:\n"
    "v_mfma_f32_16x16x32_bf16 v[208:211], v[56:59], v[176:179], 0\n"
    "v_mfma_f32_16x16x32_bf16 v[212:215], v[80:83], v[176:179], 0\n"
    "v_mfma_f32_16x16x32_bf16 v[208:211], v[60:63], v[180:183], v[208:211]\n"
    "v_mfma_f32_16x16x32_bf16 v[212:215], v[84:87], v[180:183], v[212:215]\n"
    "v_mul_f32 v200, v200, v253\n"
    "v_mul_f32 v201, v201, v253\n"
    "v_mfma_f32_16x16x32_bf16 v[208:211], v[64:67], v[184:187], v[208:211]\n"
    "v_mul_f32 v202, v202, v253\n"
    "v_mul_f32 v203, v203, v253\n"
    "v_mfma_f32_16x16x32_bf16 v[212:215], v[88:91], v[184:187], v[212:215]\n"
    "v_mul_f32 v204, v204, v253\n"
    "v_mul_f32 v205, v205, v253\n"
    "v_mfma_f32_16x16x32_bf16 v[208:211], v[68:71], v[188:191], v[208:211]\n"
    "v_mul_f32 v206, v206, v253\n"
    "v_mul_f32 v207, v207, v253\n"
    "v_mfma_f32_16x16x32_bf16 v[212:215], v[92:95], v[188:191], v[212:215]\n"
    "v_cvt_pk_bf16_f32 v200, v200, v201\n"
    "v_cvt_pk_bf16_f32 v201, v202, v203\n"
    "v_mfma_f32_16x16x32_bf16 v[208:211], v[72:75], v[192:195], v[208:211]\n"
    "v_cvt_pk_bf16_f32 v202, v204, v205\n"
    "v_cvt_pk_bf16_f32 v203, v206, v207\n"
    "v_mfma_f32_16x16x32_bf16 v[212:215], v[96:99], v[192:195], v[212:215]\n"
    "buffer_store_dwordx4 v[200:203], v5, s[80:83], 0 offen offset:0 sc1\n"
    "v_mfma_f32_16x16x32_bf16 v[208:211], v[76:79], v[196:199], v[208:211]\n"
    "v_mfma_f32_16x16x32_bf16 v[212:215], v[100:103], v[196:199], v[212:215]\n"
    ".Lh15r:\n"
    "s_sub_u32 s52, s52, 1\n"
    "s_cmp_lt_i32 s52, 0\n"
    "s_cbranch_scc1 .Lh15g\n"
    "s_sleep 1\n"
    "ds_read_b32 v228, v224 offset:832\n"
    "s_waitcnt lgkmcnt(0)\n"
    "v_cmp_gt_u32 vcc, 4, v228\n"
    "s_cbranch_vccnz .Lh15r\n"
    "s_branch .Lh15g\n"
    ".Lh15g:\n"
    "ds_read_b64_tr_b16 v[152:153], v2 offset:0\n"
    "ds_read_b64_tr_b16 v[154:155], v3 offset:0\n"
    "ds_read_b64_tr_b16 v[156:157], v2 offset:1024\n"
    "ds_read_b64_tr_b16 v[158:159], v3 offset:1024\n"
    "ds_read_b64_tr_b16 v[160:161], v2 offset:2048\n"
    "ds_read_b64_tr_b16 v[162:163], v3 offset:2048\n"
    "ds_read_b64_tr_b16 v[164:165], v2 offset:3072\n"
    "ds_read_b64_tr_b16 v[166:167], v3 offset:3072\n"
    "ds_read_b64_tr_b16 v[168:169], v2 offset:4096\n"
    "ds_read_b64_tr_b16 v[170:171], v3 offset:4096\n"
    "ds_read_b64_tr_b16 v[172:173], v2 offset:5120\n"
    "ds_read_b64_tr_b16 v[174:175], v3 offset:5120\n"
    "ds_read_b32 v252, v1 offset:192\n"
    "s_branch .Lh15j\n"
    ".Lh_last:\n"
                :: "{v1}"(vi), "{v[2:3]}"(vz), "{v[4:7]}"(vo), "{v[224:227]}"(fl), "{v235}"(lf[0]), "{v236}"(lf[1]),
                   "{v[238:239]}"(xp), "{s[40:41]}"(ws), "{s[42:43]}"(xb), "{v[230:233]}"(xo), "{v234}"(xo4),
                   "{s84}"(UNI(axis_at(rm, 12) * IROWB)), "{s85}"(UNI(axis_at(rm, 13) * IROWB)),
                   "{s46}"(UNI(2 * rm.d * IROWB)), "{v0}"(d56), "{s48}"(UNI(WS_ACK + (2 * bid + h) * 128)),
                   "{s50}"(h), "{s58}"(UNI(t12)), "{s59}"(UNI(t0 + (bid & 4 ? 4500 : 5300))),
                   "{s87}"(UNI(t0 + 7000 - (bid & 4 ? 5300 - 4500 : 0))), "{v[248:249]}"(uu), "{s[80:83]}"(srd)
                : "memory", "v255", "s51", "s52", "s53", "s54", "s55", "s56", "s57", "s60", "s61", "s76", "s77", "s79", "s88", "s89", "s90", "s91", "s98", "s99", "s92", "s93", "s94", "s95", "vcc", "scc");
        }
    } else {
        const int a = (0x3210 >> (4 * wave - 16)) & 3, H = a & 1, n0 = a, u0 = a >> 1;
        const AttC c = att_const(lane, H, cedge);
        f32x4 cbM, cbF, cbL;
        const float NEG = -__builtin_inff();
#pragma unroll
        for (int r = 0; r < 4; ++r) {
            const bool ok = (c.pk >> (4 + r)) & 1;
            cbM[r] = ok ? 0.f : NEG;
            cbF[r] = ok && e == 0 ? 0.f : NEG;
            cbL[r] = ok && e == 1 ? 0.f : NEG;
        }
        const int sl = n0 >= 3 ? n0 - 3 : n0;
        const u32x4 va = {c.sb, c.v0, c.v1, (unsigned)(LQ + sl * 6144 + lane * 16)};
        const u32x4 vb = {(unsigned)(LZ + sl * 6144 + (16 * g + (i16 ^ (4 * g))) * 16), (c.pk & 15) * 0x1111u, vf, lane < 4 ? vf + 16 * a : OOB};
        const u64 slots = (redge ? 0x975A86420ull : 0x420CA86420ull) >> (4 * u0);
        asm volatile("s_nop 7\n"
    "s_sub_u32 s56, s45, 2\n"
    "s_setprio 0\n"
    "s_cmp_lg_u32 s70, 0\n"
    "s_cbranch_scc1 .La_ld\n"
    ".La_join:\n"
    "s_mov_b32 s52, 0x40000\n"
    "s_bfe_u32 s48, s60, 0x40000\n"
    "s_bfe_u32 s49, s60, 0x40004\n"
    "s_bfe_u32 s50, s60, 0x40008\n"
    "s_bfe_u32 s51, s60, 0x4000c\n"
    "s_mul_i32 s48, s48, 8896\n"
    "s_mul_i32 s49, s49, 8896\n"
    "s_mul_i32 s50, s50, 8896\n"
    "s_mul_i32 s51, s51, 8896\n"
    "s_lshr_b64 s[60:61], s[60:61], 8\n"
    "v_add_u32 v244, s48, v0\n"
    "v_add_u32 v245, s49, v0\n"
    "v_add_u32 v246, s50, v0\n"
    "v_add_u32 v247, s51, v0\n"
    "v_add_u32 v200, s48, v1\n"
    "v_add_u32 v201, s49, v1\n"
    "v_add_u32 v202, s48, v2\n"
    "v_add_u32 v203, s49, v2\n"
    "s_mov_b32 s69, 1\n"
    "s_mov_b32 s53, 0\n"
    "s_add_u32 s55, s45, 1\n"
    "s_lshr_b32 s57, s45, 2\n"
    "v_mov_b32 v248, 0x3f803f80\n"
    "v_mov_b32 v249, 0x3f803f80\n"
    "v_mov_b32 v250, 0x3f803f80\n"
    "v_mov_b32 v251, 0x3f803f80\n"
    "s_mul_i32 s62, s57, 6\n"
    "ds_read_b32 v253, v6 offset:0\n"
    "s_waitcnt lgkmcnt(0)\n"
    "v_cmp_gt_u32 vcc, s54, v253\n"
    "s_cbranch_vccnz .La1\n"
    ".La1b:\n"
    "ds_read_b32 v254, v6 offset:16\n"
    "s_waitcnt lgkmcnt(0)\n"
    "v_cmp_gt_u32 vcc, s55, v254\n"
    "s_cbranch_vccnz .La2\n"
    ".La2b:\n"
    "ds_read_b128 v[8:11], v3 offset:0\n"
    "ds_read_b128 v[56:59], v244 offset:0\n"
    "ds_read_b128 v[12:15], v3 offset:1024\n"
    "ds_read_b128 v[60:63], v244 offset:64\n"
    "ds_read_b128 v[16:19], v3 offset:2048\n"
    "ds_read_b128 v[64:67], v244 offset:128\n"
    "ds_read_b128 v[20:23], v3 offset:3072\n"
    "ds_read_b128 v[68:71], v244 offset:192\n"
    "ds_read_b128 v[24:27], v3 offset:4096\n"
    "ds_read_b128 v[28:31], v3 offset:5120\n"
    "ds_read_b128 v[32:35], v244 offset:256\n"
    "ds_read_b128 v[36:39], v244 offset:320\n"
    "ds_read_b128 v[40:43], v245 offset:0\n"
    "ds_read_b128 v[44:47], v245 offset:64\n"
    ".La_loop:\n"
    "s_setprio 1\n"
    "s_add_u32 s57, s57, 1\n"
    "s_waitcnt lgkmcnt(8)\n"
    "v_mfma_f32_16x16x32_bf16 v[72:75], v[56:59], v[8:11], v[236:239]\n"
    "ds_read_b128 v[48:51], v245 offset:128\n"
    "v_mov_b32 v252, s57\n"
    "v_mfma_f32_16x16x32_bf16 v[72:75], v[60:63], v[12:15], v[72:75]\n"
    "ds_read_b128 v[52:55], v245 offset:192\n"
    "v_add_u32 v4, s53, v4\n"
    "v_mfma_f32_16x16x32_bf16 v[72:75], v[64:67], v[16:19], v[72:75]\n"
    "ds_read_b128 v[56:59], v245 offset:256\n"
    "s_waitcnt lgkmcnt(5)\n"
    "v_mfma_f32_16x16x32_bf16 v[72:75], v[68:71], v[20:23], v[72:75]\n"
    "ds_read_b128 v[60:63], v245 offset:320\n"
    "ds_write_b32 v7, v252 offset:192\n"
    "v_mfma_f32_16x16x32_bf16 v[72:75], v[32:35], v[24:27], v[72:75]\n"
    "ds_read_b128 v[64:67], v244 offset:8896\n"
    "ds_read_b128 v[208:211], v7 offset:32768\n"
    "v_mfma_f32_16x16x32_bf16 v[72:75], v[36:39], v[28:31], v[72:75]\n"
    "ds_read_b128 v[68:71], v244 offset:8960\n"
    "s_waitcnt lgkmcnt(7)\n"
    "v_mfma_f32_16x16x32_bf16 v[76:79], v[40:43], v[8:11], v[232:235]\n"
    "ds_read_b128 v[32:35], v244 offset:9024\n"
    "ds_read_b128 v[212:215], v7 offset:32768\n"
    "v_mfma_f32_16x16x32_bf16 v[76:79], v[44:47], v[12:15], v[76:79]\n"
    "ds_read_b128 v[36:39], v244 offset:9088\n"
    "ds_read_b128 v[216:219], v7 offset:32768\n"
    "v_mfma_f32_16x16x32_bf16 v[76:79], v[48:51], v[16:19], v[76:79]\n"
    "ds_read_b128 v[40:43], v244 offset:9152\n"
    "s_waitcnt lgkmcnt(9)\n"
    "v_mfma_f32_16x16x32_bf16 v[76:79], v[52:55], v[20:23], v[76:79]\n"
    "ds_read_b128 v[44:47], v244 offset:9216\n"
    "v_max_f32 v72, v72, v73\n"
    "v_mfma_f32_16x16x32_bf16 v[76:79], v[56:59], v[24:27], v[76:79]\n"
    "ds_read_b128 v[48:51], v245 offset:8896\n"
    "v_max_f32 v74, v74, v75\n"
    "v_mfma_f32_16x16x32_bf16 v[76:79], v[60:63], v[28:31], v[76:79]\n"
    "ds_read_b128 v[52:55], v245 offset:8960\n"
    "s_waitcnt lgkmcnt(7)\n"
    "v_mfma_f32_16x16x32_bf16 v[80:83], v[64:67], v[8:11], v[232:235]\n"
    "ds_read_b128 v[56:59], v245 offset:9024\n"
    "v_max_f32 v206, v72, v74\n"
    "v_mfma_f32_16x16x32_bf16 v[80:83], v[68:71], v[12:15], v[80:83]\n"
    "ds_read_b128 v[60:63], v245 offset:9088\n"
    "ds_read_b128 v[220:223], v7 offset:32768\n"
    "v_mfma_f32_16x16x32_bf16 v[80:83], v[32:35], v[16:19], v[80:83]\n"
    "ds_read_b128 v[64:67], v245 offset:9152\n"
    "s_waitcnt lgkmcnt(6)\n"
    "v_mfma_f32_16x16x32_bf16 v[80:83], v[36:39], v[20:23], v[80:83]\n"
    "ds_read_b128 v[68:71], v245 offset:9216\n"
    "v_max_f32 v76, v76, v77\n"
    "v_mfma_f32_16x16x32_bf16 v[80:83], v[40:43], v[24:27], v[80:83]\n"
    "ds_read_b128 v[32:35], v246 offset:0\n"
    "v_max_f32 v78, v78, v79\n"
    "v_mfma_f32_16x16x32_bf16 v[80:83], v[44:47], v[28:31], v[80:83]\n"
    "ds_read_b128 v[36:39], v246 offset:64\n"
    "s_waitcnt lgkmcnt(6)\n"
    "v_mfma_f32_16x16x32_bf16 v[84:87], v[48:51], v[8:11], v[232:235]\n"
    "ds_read_b128 v[40:43], v246 offset:128\n"
    "v_max3_f32 v206, v206, v76, v78\n"
    "v_mfma_f32_16x16x32_bf16 v[84:87], v[52:55], v[12:15], v[84:87]\n"
    "ds_read_b128 v[44:47], v246 offset:192\n"
    "ds_read_b128 v[224:227], v7 offset:32768\n"
    "v_mfma_f32_16x16x32_bf16 v[84:87], v[56:59], v[16:19], v[84:87]\n"
    "ds_read_b128 v[48:51], v246 offset:256\n"
    "s_waitcnt lgkmcnt(6)\n"
    "v_mfma_f32_16x16x32_bf16 v[84:87], v[60:63], v[20:23], v[84:87]\n"
    "ds_read_b128 v[52:55], v246 offset:320\n"
    "v_max_f32 v80, v80, v81\n"
    "v_mfma_f32_16x16x32_bf16 v[84:87], v[64:67], v[24:27], v[84:87]\n"
    "ds_read_b128 v[56:59], v247 offset:0\n"
    "v_max_f32 v82, v82, v83\n"
    "v_mfma_f32_16x16x32_bf16 v[84:87], v[68:71], v[28:31], v[84:87]\n"
    "ds_read_b128 v[60:63], v247 offset:64\n"
    "s_waitcnt lgkmcnt(6)\n"
    "v_mfma_f32_16x16x32_bf16 v[88:91], v[32:35], v[8:11], v[232:235]\n"
    "ds_read_b128 v[64:67], v247 offset:128\n"
    "v_max3_f32 v206, v206, v80, v82\n"
    "v_mfma_f32_16x16x32_bf16 v[88:91], v[36:39], v[12:15], v[88:91]\n"
    "ds_read_b128 v[68:71], v247 offset:192\n"
    "ds_read_b128 v[228:231], v7 offset:32768\n"
    "v_mfma_f32_16x16x32_bf16 v[88:91], v[40:43], v[16:19], v[88:91]\n"
    "ds_read_b128 v[32:35], v247 offset:256\n"
    "s_waitcnt lgkmcnt(6)\n"
    "v_mfma_f32_16x16x32_bf16 v[88:91], v[44:47], v[20:23], v[88:91]\n"
    "ds_read_b128 v[36:39], v247 offset:320\n"
    "v_max_f32 v84, v84, v85\n"
    "v_mfma_f32_16x16x32_bf16 v[88:91], v[48:51], v[24:27], v[88:91]\n"
    "ds_read_b128 v[40:43], v246 offset:8896\n"
    "v_max_f32 v86, v86, v87\n"
    "v_mfma_f32_16x16x32_bf16 v[88:91], v[52:55], v[28:31], v[88:91]\n"
    "ds_read_b128 v[44:47], v246 offset:8960\n"
    "s_waitcnt lgkmcnt(6)\n"
    "v_mfma_f32_16x16x32_bf16 v[92:95], v[56:59], v[8:11], v[232:235]\n"
    "ds_read_b128 v[48:51], v246 offset:9024\n"
    "v_max3_f32 v206, v206, v84, v86\n"
    "v_mfma_f32_16x16x32_bf16 v[92:95], v[60:63], v[12:15], v[92:95]\n"
    "ds_read_b128 v[52:55], v246 offset:9088\n"
    "v_mfma_f32_16x16x32_bf16 v[92:95], v[64:67], v[16:19], v[92:95]\n"
    "ds_read_b128 v[56:59], v246 offset:9152\n"
    "s_waitcnt lgkmcnt(5)\n"
    "v_mfma_f32_16x16x32_bf16 v[92:95], v[68:71], v[20:23], v[92:95]\n"
    "ds_read_b128 v[60:63], v246 offset:9216\n"
    "v_max_f32 v88, v88, v89\n"
    "v_mfma_f32_16x16x32_bf16 v[92:95], v[32:35], v[24:27], v[92:95]\n"
    "ds_read_b128 v[64:67], v247 offset:8896\n"
    "v_max_f32 v90, v90, v91\n"
    "v_mfma_f32_16x16x32_bf16 v[92:95], v[36:39], v[28:31], v[92:95]\n"
    "ds_read_b128 v[68:71], v247 offset:8960\n"
    "s_waitcnt lgkmcnt(5)\n"
    "v_mfma_f32_16x16x32_bf16 v[96:99], v[40:43], v[8:11], v[232:235]\n"
    "ds_read_b128 v[32:35], v247 offset:9024\n"
    "v_max3_f32 v206, v206, v88, v90\n"
    "v_mfma_f32_16x16x32_bf16 v[96:99], v[44:47], v[12:15], v[96:99]\n"
    "ds_read_b128 v[36:39], v247 offset:9088\n"
    "v_mfma_f32_16x16x32_bf16 v[96:99], v[48:51], v[16:19], v[96:99]\n"
    "ds_read_b128 v[40:43], v247 offset:9152\n"
    "s_waitcnt lgkmcnt(5)\n"
    "v_mfma_f32_16x16x32_bf16 v[96:99], v[52:55], v[20:23], v[96:99]\n"
    "ds_read_b128 v[44:47], v247 offset:9216\n"
    "v_max_f32 v92, v92, v93\n"
    "v_mfma_f32_16x16x32_bf16 v[96:99], v[56:59], v[24:27], v[96:99]\n"
    "v_add_u32 v244, s50, v1\n"
    "v_add_u32 v245, s51, v1\n"
    "v_mfma_f32_16x16x32_bf16 v[96:99], v[60:63], v[28:31], v[96:99]\n"
    "v_add_u32 v246, s50, v2\n"
    "s_waitcnt lgkmcnt(3)\n"
    "v_mfma_f32_16x16x32_bf16 v[100:103], v[64:67], v[8:11], v[240:243]\n"
    "v_add_u32 v247, s51, v2\n"
    "v_max_f32 v94, v94, v95\n"
    "v_mfma_f32_16x16x32_bf16 v[100:103], v[68:71], v[12:15], v[100:103]\n"
    "ds_read_b64_tr_b16 v[48:49], v244 offset:160\n"
    "v_max3_f32 v206, v206, v92, v94\n"
    "v_mfma_f32_16x16x32_bf16 v[100:103], v[32:35], v[16:19], v[100:103]\n"
    "ds_read_b64_tr_b16 v[50:51], v245 offset:160\n"
    "s_waitcnt lgkmcnt(2)\n"
    "v_mfma_f32_16x16x32_bf16 v[100:103], v[36:39], v[20:23], v[100:103]\n"
    "ds_read_b64_tr_b16 v[52:53], v246 offset:160\n"
    "v_max_f32 v96, v96, v97\n"
    "v_mfma_f32_16x16x32_bf16 v[100:103], v[40:43], v[24:27], v[100:103]\n"
    "ds_read_b64_tr_b16 v[54:55], v247 offset:160\n"
    "v_max_f32 v98, v98, v99\n"
    "v_mfma_f32_16x16x32_bf16 v[100:103], v[44:47], v[28:31], v[100:103]\n"
    "ds_read_b64_tr_b16 v[56:57], v244 offset:192\n"
    "ds_read_b64_tr_b16 v[58:59], v245 offset:192\n"
    "s_cmp_lg_u32 s69, 0\n"
    "s_cbranch_scc1 .La_fill\n"
    ".La_filled:\n"
    "ds_write_b32 v7, v252 offset:64\n"
    "s_setprio 0\n"
    "v_max3_f32 v206, v206, v96, v98\n"
    "ds_read_b64_tr_b16 v[60:61], v246 offset:192\n"
    "v_max_f32 v100, v100, v101\n"
    "v_max_f32 v102, v102, v103\n"
    "v_max3_f32 v206, v206, v100, v102\n"
    "v_mov_b32 v207, v206\n"
    "ds_read_b64_tr_b16 v[62:63], v247 offset:192\n"
    "ds_read_b64_tr_b16 v[64:65], v244 offset:224\n"
    "v_permlane16_swap_b32 v206, v207\n"
    "v_max_f32 v206, v206, v207\n"
    "v_mov_b32 v207, v206\n"
    "ds_read_b64_tr_b16 v[66:67], v245 offset:224\n"
    "ds_read_b64_tr_b16 v[68:69], v246 offset:224\n"
    "v_permlane32_swap_b32 v206, v207\n"
    "v_max_f32 v206, v206, v207\n"
    "v_mul_f32 v207, s46, v206\n"
    "v_fma_f32 v72, v72, s47, v207\n"
    "v_fma_f32 v74, v74, s47, v207\n"
    "v_fma_f32 v76, v76, s47, v207\n"
    "v_fma_f32 v78, v78, s47, v207\n"
    "v_fma_f32 v80, v80, s47, v207\n"
    "v_fma_f32 v82, v82, s47, v207\n"
    "v_fma_f32 v84, v84, s47, v207\n"
    "v_fma_f32 v86, v86, s47, v207\n"
    "v_fma_f32 v88, v88, s47, v207\n"
    "v_fma_f32 v90, v90, s47, v207\n"
    "v_fma_f32 v92, v92, s47, v207\n"
    "v_fma_f32 v94, v94, s47, v207\n"
    "v_fma_f32 v96, v96, s47, v207\n"
    "v_fma_f32 v98, v98, s47, v207\n"
    "v_fma_f32 v100, v100, s47, v207\n"
    "v_fma_f32 v102, v102, s47, v207\n"
    "ds_read_b64_tr_b16 v[70:71], v247 offset:224\n"
    "ds_read_b64_tr_b16 v[32:33], v244 offset:96\n"
    "ds_read_b64_tr_b16 v[34:35], v245 offset:96\n"
    "ds_read_b64_tr_b16 v[36:37], v246 offset:96\n"
    "ds_read_b64_tr_b16 v[38:39], v247 offset:96\n"
    "ds_read_b64_tr_b16 v[40:41], v244 offset:128\n"
    "ds_read_b64_tr_b16 v[42:43], v245 offset:128\n"
    "ds_read_b64_tr_b16 v[44:45], v246 offset:128\n"
    "ds_read_b64_tr_b16 v[46:47], v247 offset:128\n"
    "ds_read_b64_tr_b16 v[8:9], v244 offset:0\n"
    "ds_read_b64_tr_b16 v[10:11], v245 offset:0\n"
    "ds_read_b64_tr_b16 v[12:13], v246 offset:0\n"
    "ds_read_b64_tr_b16 v[14:15], v247 offset:0\n"
    "ds_read_b64_tr_b16 v[16:17], v244 offset:32\n"
    "ds_read_b64_tr_b16 v[18:19], v245 offset:32\n"
    "ds_read_b64_tr_b16 v[20:21], v246 offset:32\n"
    "ds_read_b64_tr_b16 v[22:23], v247 offset:32\n"
    "ds_read_b64_tr_b16 v[24:25], v244 offset:64\n"
    "ds_read_b64_tr_b16 v[26:27], v245 offset:64\n"
    "ds_read_b64_tr_b16 v[28:29], v246 offset:64\n"
    "ds_read_b64_tr_b16 v[30:31], v247 offset:64\n"
    "ds_read_b32 v253, v6 offset:32\n"
    "v_exp_f32 v72, v72\n"
    "s_bfe_u32 s48, s60, 0x40000\n"
    "v_exp_f32 v74, v74\n"
    "s_bfe_u32 s49, s60, 0x40004\n"
    "v_exp_f32 v76, v76\n"
    "s_bfe_u32 s50, s60, 0x40008\n"
    "v_exp_f32 v78, v78\n"
    "s_bfe_u32 s51, s60, 0x4000c\n"
    "v_exp_f32 v80, v80\n"
    "s_mul_i32 s48, s48, 8896\n"
    "v_exp_f32 v82, v82\n"
    "s_mul_i32 s49, s49, 8896\n"
    "v_exp_f32 v84, v84\n"
    "s_mul_i32 s50, s50, 8896\n"
    "v_exp_f32 v86, v86\n"
    "s_mul_i32 s51, s51, 8896\n"
    "v_exp_f32 v88, v88\n"
    "s_lshr_b64 s[60:61], s[60:61], 8\n"
    "v_exp_f32 v90, v90\n"
    "s_add_u32 s45, s45, 4\n"
    "v_exp_f32 v92, v92\n"
    "s_add_u32 s54, s54, 2\n"
    "v_exp_f32 v94, v94\n"
    "s_add_u32 s55, s55, 4\n"
    "v_exp_f32 v96, v96\n"
    "s_cmp_eq_u32 s66, 2\n"
    "s_cselect_b32 s67, -2, 1\n"
    "v_exp_f32 v98, v98\n"
    "s_add_u32 s66, s66, s67\n"
    "v_exp_f32 v100, v100\n"
    "s_mul_i32 s68, s67, 6144\n"
    "v_exp_f32 v102, v102\n"
    "v_cvt_pk_bf16_f32 v200, v72, v74\n"
    "v_cvt_pk_bf16_f32 v201, v76, v78\n"
    "v_cvt_pk_bf16_f32 v202, v80, v82\n"
    "v_cvt_pk_bf16_f32 v203, v84, v86\n"
    "v_cvt_pk_bf16_f32 v204, v88, v90\n"
    "v_cvt_pk_bf16_f32 v205, v92, v94\n"
    "v_cvt_pk_bf16_f32 v206, v96, v98\n"
    "v_cvt_pk_bf16_f32 v207, v100, v102\n"
    "ds_read_b64_tr_b16 v[72:73], v244 offset:256\n"
    "ds_read_b64_tr_b16 v[74:75], v245 offset:256\n"
    "ds_read_b64_tr_b16 v[76:77], v246 offset:256\n"
    "ds_read_b64_tr_b16 v[78:79], v247 offset:256\n"
    "ds_read_b64_tr_b16 v[80:81], v244 offset:288\n"
    "ds_read_b64_tr_b16 v[82:83], v245 offset:288\n"
    "ds_read_b64_tr_b16 v[84:85], v246 offset:288\n"
    "ds_read_b64_tr_b16 v[86:87], v247 offset:288\n"
    "s_waitcnt lgkmcnt(8)\n"
    "s_setprio 1\n"
    "v_mfma_f32_16x16x32_bf16 v[100:103], v[248:251], v[200:203], 0\n"
    "v_cmp_gt_i32 vcc, s56, v253\n"
    "v_mfma_f32_16x16x32_bf16 v[100:103], v[248:251], v[204:207], v[100:103]\n"
    "v_smfmac_f32_16x16x64_bf16 v[220:223], v[200:203], v[128:135], v5\n"
    "ds_read_b128 v[128:131], v7 offset:32768\n"
    "v_smfmac_f32_16x16x64_bf16 v[224:227], v[200:203], v[136:143], v5\n"
    "ds_read_b128 v[132:135], v7 offset:32768\n"
    "v_smfmac_f32_16x16x64_bf16 v[228:231], v[200:203], v[144:151], v5\n"
    "ds_read_b128 v[136:139], v7 offset:32768\n"
    "v_smfmac_f32_16x16x64_bf16 v[208:211], v[200:203], v[104:111], v5\n"
    "ds_read_b128 v[140:143], v7 offset:32768\n"
    "v_smfmac_f32_16x16x64_bf16 v[212:215], v[200:203], v[112:119], v5\n"
    "ds_read_b128 v[144:147], v7 offset:32768\n"
    "v_smfmac_f32_16x16x64_bf16 v[216:219], v[200:203], v[120:127], v5\n"
    "ds_read_b128 v[148:151], v7 offset:32768\n"
    "v_smfmac_f32_16x16x64_bf16 v[208:211], v[204:207], v[8:15], v5\n"
    "ds_read_b64_tr_b16 v[88:89], v244 offset:320\n"
    "v_rcp_f32 v100, v100\n"
    "v_smfmac_f32_16x16x64_bf16 v[212:215], v[204:207], v[16:23], v5\n"
    "ds_read_b64_tr_b16 v[90:91], v245 offset:320\n"
    "v_lshrrev_b32 v101, 2, v3\n"
    "v_smfmac_f32_16x16x64_bf16 v[216:219], v[204:207], v[24:31], v5\n"
    "ds_read_b64_tr_b16 v[92:93], v246 offset:320\n"
    "v_and_or_b32 v101, v101, 60, s63\n"
    "v_smfmac_f32_16x16x64_bf16 v[220:223], v[204:207], v[32:39], v5\n"
    "ds_read_b64_tr_b16 v[94:95], v247 offset:320\n"
    "s_cbranch_vccnz .La3\n"
    ".La3b:\n"
    "v_smfmac_f32_16x16x64_bf16 v[224:227], v[204:207], v[40:47], v5\n"
    "ds_read_b64_tr_b16 v[96:97], v244 offset:352\n"
    "ds_read_b64_tr_b16 v[98:99], v245 offset:352\n"
    "v_smfmac_f32_16x16x64_bf16 v[228:231], v[204:207], v[48:55], v5\n"
    "s_waitcnt lgkmcnt(6)\n"
    "v_smfmac_f32_16x16x64_bf16 v[128:131], v[200:203], v[152:159], v5\n"
    "ds_write_b32 v101, v100 offset:0\n"
    "ds_read_b64_tr_b16 v[100:101], v246 offset:352\n"
    "v_smfmac_f32_16x16x64_bf16 v[132:135], v[200:203], v[160:167], v5\n"
    "ds_read_b64_tr_b16 v[102:103], v247 offset:352\n"
    "v_smfmac_f32_16x16x64_bf16 v[136:139], v[200:203], v[168:175], v5\n"
    "v_cvt_pk_bf16_f32 v208, v208, v209\n"
    "v_cvt_pk_bf16_f32 v209, v210, v211\n"
    "v_smfmac_f32_16x16x64_bf16 v[140:143], v[200:203], v[176:183], v5\n"
    "v_cvt_pk_bf16_f32 v210, v212, v213\n"
    "v_cvt_pk_bf16_f32 v211, v214, v215\n"
    "v_smfmac_f32_16x16x64_bf16 v[144:147], v[200:203], v[184:191], v5\n"
    "ds_write_b128 v4, v[208:211] offset:0\n"
    "v_smfmac_f32_16x16x64_bf16 v[148:151], v[200:203], v[192:199], v5\n"
    "v_cvt_pk_bf16_f32 v216, v216, v217\n"
    "v_cvt_pk_bf16_f32 v217, v218, v219\n"
    "v_smfmac_f32_16x16x64_bf16 v[128:131], v[204:207], v[56:63], v5\n"
    "ds_read_b32 v254, v6 offset:16\n"
    "ds_read_b32 v200, v6 offset:0\n"
    "v_smfmac_f32_16x16x64_bf16 v[132:135], v[204:207], v[64:71], v5\n"
    "v_cvt_pk_bf16_f32 v218, v220, v221\n"
    "s_waitcnt lgkmcnt(8)\n"
    "v_smfmac_f32_16x16x64_bf16 v[136:139], v[204:207], v[72:79], v5\n"
    "v_cvt_pk_bf16_f32 v219, v222, v223\n"
    "v_smfmac_f32_16x16x64_bf16 v[140:143], v[204:207], v[80:87], v5\n"
    "ds_write_b128 v4, v[216:219] offset:1024\n"
    "v_smfmac_f32_16x16x64_bf16 v[144:147], v[204:207], v[88:95], v5\n"
    "v_cvt_pk_bf16_f32 v224, v224, v225\n"
    "s_waitcnt lgkmcnt(4)\n"
    "v_smfmac_f32_16x16x64_bf16 v[148:151], v[204:207], v[96:103], v5\n"
    "v_cvt_pk_bf16_f32 v225, v226, v227\n"
    "v_cvt_pk_bf16_f32 v226, v228, v229\n"
    "s_setprio 0\n"
    "v_add_u32 v244, s48, v0\n"
    "v_add_u32 v245, s49, v0\n"
    "v_add_u32 v246, s50, v0\n"
    "v_add_u32 v247, s51, v0\n"
    "v_add_u32 v3, s68, v3\n"
    "v_cvt_pk_bf16_f32 v227, v230, v231\n"
    "ds_write_b128 v4, v[224:227] offset:2048\n"
    "s_waitcnt lgkmcnt(3)\n"
    "v_cmp_gt_u32 vcc, s55, v254\n"
    "s_waitcnt lgkmcnt(2)\n"
    "v_cmp_gt_u32 s[58:59], s54, v200\n"
    "s_add_u32 s56, s56, 4\n"
    "s_lshl_b32 s67, s67, 6\n"
    "s_add_u32 s63, s63, s67\n"
    "s_sub_u32 s53, s68, 18432\n"
    "s_add_u32 s67, s63, 192\n"
    "s_cmp_ge_u32 s45, 10\n"
    "s_cselect_b32 s53, s53, s68\n"
    "s_cselect_b32 s63, s67, s63\n"
    "s_cselect_b32 s56, -16, s56\n"
    "s_cmp_ge_u32 s45, 12\n"
    "s_cbranch_scc1 .La4\n"
    "s_or_b64 vcc, vcc, s[58:59]\n"
    "s_cbranch_scc1 .La4\n"
    "ds_read_b128 v[104:107], v3 offset:0\n"
    "ds_read_b128 v[152:155], v244 offset:0\n"
    "ds_read_b128 v[108:111], v3 offset:1024\n"
    "ds_read_b128 v[156:159], v244 offset:64\n"
    "ds_read_b128 v[112:115], v3 offset:2048\n"
    "ds_read_b128 v[160:163], v244 offset:128\n"
    "ds_read_b128 v[116:119], v3 offset:3072\n"
    "ds_read_b128 v[164:167], v244 offset:192\n"
    "ds_read_b128 v[120:123], v3 offset:4096\n"
    "ds_read_b128 v[124:127], v3 offset:5120\n"
    "v_cvt_pk_bf16_f32 v128, v128, v129\n"
    "v_cvt_pk_bf16_f32 v129, v130, v131\n"
    "v_cvt_pk_bf16_f32 v130, v132, v133\n"
    "v_cvt_pk_bf16_f32 v131, v134, v135\n"
    "ds_write_b128 v4, v[128:131] offset:3072\n"
    "v_cvt_pk_bf16_f32 v136, v136, v137\n"
    "v_cvt_pk_bf16_f32 v137, v138, v139\n"
    "v_cvt_pk_bf16_f32 v138, v140, v141\n"
    "v_cvt_pk_bf16_f32 v139, v142, v143\n"
    "ds_write_b128 v4, v[136:139] offset:4096\n"
    "v_cvt_pk_bf16_f32 v144, v144, v145\n"
    "v_cvt_pk_bf16_f32 v145, v146, v147\n"
    "v_cvt_pk_bf16_f32 v146, v148, v149\n"
    "v_cvt_pk_bf16_f32 v147, v150, v151\n"
    "ds_write_b128 v4, v[144:147] offset:5120\n"
    "ds_write_b32 v7, v252 offset:128\n"
    "ds_read_b128 v[128:131], v244 offset:256\n"
    "ds_read_b128 v[132:135], v244 offset:320\n"
    "ds_read_b128 v[136:139], v245 offset:0\n"
    "ds_read_b128 v[140:143], v245 offset:64\n"
    ".La4j:\n"
    "s_setprio 1\n"
    "s_add_u32 s57, s57, 1\n"
    "s_waitcnt lgkmcnt(8)\n"
    "v_mfma_f32_16x16x32_bf16 v[168:171], v[152:155], v[104:107], v[236:239]\n"
    "ds_read_b128 v[144:147], v245 offset:128\n"
    "v_mov_b32 v252, s57\n"
    "v_mfma_f32_16x16x32_bf16 v[168:171], v[156:159], v[108:111], v[168:171]\n"
    "ds_read_b128 v[148:151], v245 offset:192\n"
    "v_add_u32 v4, s53, v4\n"
    "v_mfma_f32_16x16x32_bf16 v[168:171], v[160:163], v[112:115], v[168:171]\n"
    "ds_read_b128 v[152:155], v245 offset:256\n"
    "s_waitcnt lgkmcnt(5)\n"
    "v_mfma_f32_16x16x32_bf16 v[168:171], v[164:167], v[116:119], v[168:171]\n"
    "ds_read_b128 v[156:159], v245 offset:320\n"
    "ds_write_b32 v7, v252 offset:192\n"
    "v_mfma_f32_16x16x32_bf16 v[168:171], v[128:131], v[120:123], v[168:171]\n"
    "ds_read_b128 v[160:163], v244 offset:8896\n"
    "ds_read_b128 v[208:211], v7 offset:32768\n"
    "v_mfma_f32_16x16x32_bf16 v[168:171], v[132:135], v[124:127], v[168:171]\n"
    "ds_read_b128 v[164:167], v244 offset:8960\n"
    "s_waitcnt lgkmcnt(7)\n"
    "v_mfma_f32_16x16x32_bf16 v[172:175], v[136:139], v[104:107], v[232:235]\n"
    "ds_read_b128 v[128:131], v244 offset:9024\n"
    "ds_read_b128 v[212:215], v7 offset:32768\n"
    "v_mfma_f32_16x16x32_bf16 v[172:175], v[140:143], v[108:111], v[172:175]\n"
    "ds_read_b128 v[132:135], v244 offset:9088\n"
    "ds_read_b128 v[216:219], v7 offset:32768\n"
    "v_mfma_f32_16x16x32_bf16 v[172:175], v[144:147], v[112:115], v[172:175]\n"
    "ds_read_b128 v[136:139], v244 offset:9152\n"
    "s_waitcnt lgkmcnt(9)\n"
    "v_mfma_f32_16x16x32_bf16 v[172:175], v[148:151], v[116:119], v[172:175]\n"
    "ds_read_b128 v[140:143], v244 offset:9216\n"
    "v_max_f32 v168, v168, v169\n"
    "v_mfma_f32_16x16x32_bf16 v[172:175], v[152:155], v[120:123], v[172:175]\n"
    "ds_read_b128 v[144:147], v245 offset:8896\n"
    "v_max_f32 v170, v170, v171\n"
    "v_mfma_f32_16x16x32_bf16 v[172:175], v[156:159], v[124:127], v[172:175]\n"
    "ds_read_b128 v[148:151], v245 offset:8960\n"
    "s_waitcnt lgkmcnt(7)\n"
    "v_mfma_f32_16x16x32_bf16 v[176:179], v[160:163], v[104:107], v[232:235]\n"
    "ds_read_b128 v[152:155], v245 offset:9024\n"
    "v_max_f32 v206, v168, v170\n"
    "v_mfma_f32_16x16x32_bf16 v[176:179], v[164:167], v[108:111], v[176:179]\n"
    "ds_read_b128 v[156:159], v245 offset:9088\n"
    "ds_read_b128 v[220:223], v7 offset:32768\n"
    "v_mfma_f32_16x16x32_bf16 v[176:179], v[128:131], v[112:115], v[176:179]\n"
    "ds_read_b128 v[160:163], v245 offset:9152\n"
    "s_waitcnt lgkmcnt(6)\n"
    "v_mfma_f32_16x16x32_bf16 v[176:179], v[132:135], v[116:119], v[176:179]\n"
    "ds_read_b128 v[164:167], v245 offset:9216\n"
    "v_max_f32 v172, v172, v173\n"
    "v_mfma_f32_16x16x32_bf16 v[176:179], v[136:139], v[120:123], v[176:179]\n"
    "ds_read_b128 v[128:131], v246 offset:0\n"
    "v_max_f32 v174, v174, v175\n"
    "v_mfma_f32_16x16x32_bf16 v[176:179], v[140:143], v[124:127], v[176:179]\n"
    "ds_read_b128 v[132:135], v246 offset:64\n"
    "s_waitcnt lgkmcnt(6)\n"
    "v_mfma_f32_16x16x32_bf16 v[180:183], v[144:147], v[104:107], v[232:235]\n"
    "ds_read_b128 v[136:139], v246 offset:128\n"
    "v_max3_f32 v206, v206, v172, v174\n"
    "v_mfma_f32_16x16x32_bf16 v[180:183], v[148:151], v[108:111], v[180:183]\n"
    "ds_read_b128 v[140:143], v246 offset:192\n"
    "ds_read_b128 v[224:227], v7 offset:32768\n"
    "v_mfma_f32_16x16x32_bf16 v[180:183], v[152:155], v[112:115], v[180:183]\n"
    "ds_read_b128 v[144:147], v246 offset:256\n"
    "s_waitcnt lgkmcnt(6)\n"
    "v_mfma_f32_16x16x32_bf16 v[180:183], v[156:159], v[116:119], v[180:183]\n"
    "ds_read_b128 v[148:151], v246 offset:320\n"
    "v_max_f32 v176, v176, v177\n"
    "v_mfma_f32_16x16x32_bf16 v[180:183], v[160:163], v[120:123], v[180:183]\n"
    "ds_read_b128 v[152:155], v247 offset:0\n"
    "v_max_f32 v178, v178, v179\n"
    "v_mfma_f32_16x16x32_bf16 v[180:183], v[164:167], v[124:127], v[180:183]\n"
    "ds_read_b128 v[156:159], v247 offset:64\n"
    "s_waitcnt lgkmcnt(6)\n"
    "v_mfma_f32_16x16x32_bf16 v[184:187], v[128:131], v[104:107], v[232:235]\n"
    "ds_read_b128 v[160:163], v247 offset:128\n"
    "v_max3_f32 v206, v206, v176, v178\n"
    "v_mfma_f32_16x16x32_bf16 v[184:187], v[132:135], v[108:111], v[184:187]\n"
    "ds_read_b128 v[164:167], v247 offset:192\n"
    "ds_read_b128 v[228:231], v7 offset:32768\n"
    "v_mfma_f32_16x16x32_bf16 v[184:187], v[136:139], v[112:115], v[184:187]\n"
    "ds_read_b128 v[128:131], v247 offset:256\n"
    "s_waitcnt lgkmcnt(6)\n"
    "v_mfma_f32_16x16x32_bf16 v[184:187], v[140:143], v[116:119], v[184:187]\n"
    "ds_read_b128 v[132:135], v247 offset:320\n"
    "v_max_f32 v180, v180, v181\n"
    "v_mfma_f32_16x16x32_bf16 v[184:187], v[144:147], v[120:123], v[184:187]\n"
    "ds_read_b128 v[136:139], v246 offset:8896\n"
    "v_max_f32 v182, v182, v183\n"
    "v_mfma_f32_16x16x32_bf16 v[184:187], v[148:151], v[124:127], v[184:187]\n"
    "ds_read_b128 v[140:143], v246 offset:8960\n"
    "s_waitcnt lgkmcnt(6)\n"
    "v_mfma_f32_16x16x32_bf16 v[188:191], v[152:155], v[104:107], v[232:235]\n"
    "ds_read_b128 v[144:147], v246 offset:9024\n"
    "v_max3_f32 v206, v206, v180, v182\n"
    "v_mfma_f32_16x16x32_bf16 v[188:191], v[156:159], v[108:111], v[188:191]\n"
    "ds_read_b128 v[148:151], v246 offset:9088\n"
    "v_mfma_f32_16x16x32_bf16 v[188:191], v[160:163], v[112:115], v[188:191]\n"
    "ds_read_b128 v[152:155], v246 offset:9152\n"
    "s_waitcnt lgkmcnt(5)\n"
    "v_mfma_f32_16x16x32_bf16 v[188:191], v[164:167], v[116:119], v[188:191]\n"
    "ds_read_b128 v[156:159], v246 offset:9216\n"
    "v_max_f32 v184, v184, v185\n"
    "v_mfma_f32_16x16x32_bf16 v[188:191], v[128:131], v[120:123], v[188:191]\n"
    "ds_read_b128 v[160:163], v247 offset:8896\n"
    "v_max_f32 v186, v186, v187\n"
    "v_mfma_f32_16x16x32_bf16 v[188:191], v[132:135], v[124:127], v[188:191]\n"
    "ds_read_b128 v[164:167], v247 offset:8960\n"
    "s_waitcnt lgkmcnt(5)\n"
    "v_mfma_f32_16x16x32_bf16 v[192:195], v[136:139], v[104:107], v[232:235]\n"
    "ds_read_b128 v[128:131], v247 offset:9024\n"
    "v_max3_f32 v206, v206, v184, v186\n"
    "v_mfma_f32_16x16x32_bf16 v[192:195], v[140:143], v[108:111], v[192:195]\n"
    "ds_read_b128 v[132:135], v247 offset:9088\n"
    "v_mfma_f32_16x16x32_bf16 v[192:195], v[144:147], v[112:115], v[192:195]\n"
    "ds_read_b128 v[136:139], v247 offset:9152\n"
    "s_waitcnt lgkmcnt(5)\n"
    "v_mfma_f32_16x16x32_bf16 v[192:195], v[148:151], v[116:119], v[192:195]\n"
    "ds_read_b128 v[140:143], v247 offset:9216\n"
    "v_max_f32 v188, v188, v189\n"
    "v_mfma_f32_16x16x32_bf16 v[192:195], v[152:155], v[120:123], v[192:195]\n"
    "v_add_u32 v244, s50, v1\n"
    "v_add_u32 v245, s51, v1\n"
    "v_mfma_f32_16x16x32_bf16 v[192:195], v[156:159], v[124:127], v[192:195]\n"
    "v_add_u32 v246, s50, v2\n"
    "s_waitcnt lgkmcnt(3)\n"
    "v_mfma_f32_16x16x32_bf16 v[196:199], v[160:163], v[104:107], v[240:243]\n"
    "v_add_u32 v247, s51, v2\n"
    "v_max_f32 v190, v190, v191\n"
    "v_mfma_f32_16x16x32_bf16 v[196:199], v[164:167], v[108:111], v[196:199]\n"
    "ds_read_b64_tr_b16 v[144:145], v244 offset:160\n"
    "v_max3_f32 v206, v206, v188, v190\n"
    "v_mfma_f32_16x16x32_bf16 v[196:199], v[128:131], v[112:115], v[196:199]\n"
    "ds_read_b64_tr_b16 v[146:147], v245 offset:160\n"
    "s_waitcnt lgkmcnt(2)\n"
    "v_mfma_f32_16x16x32_bf16 v[196:199], v[132:135], v[116:119], v[196:199]\n"
    "ds_read_b64_tr_b16 v[148:149], v246 offset:160\n"
    "v_max_f32 v192, v192, v193\n"
    "v_mfma_f32_16x16x32_bf16 v[196:199], v[136:139], v[120:123], v[196:199]\n"
    "ds_read_b64_tr_b16 v[150:151], v247 offset:160\n"
    "v_max_f32 v194, v194, v195\n"
    "v_mfma_f32_16x16x32_bf16 v[196:199], v[140:143], v[124:127], v[196:199]\n"
    "ds_read_b64_tr_b16 v[152:153], v244 offset:192\n"
    "ds_read_b64_tr_b16 v[154:155], v245 offset:192\n"
    "ds_write_b32 v7, v252 offset:64\n"
    "s_setprio 0\n"
    "v_max3_f32 v206, v206, v192, v194\n"
    "ds_read_b64_tr_b16 v[156:157], v246 offset:192\n"
    "ds_read_b64_tr_b16 v[158:159], v247 offset:192\n"
    "ds_read_b64_tr_b16 v[160:161], v244 offset:224\n"
    "v_max_f32 v196, v196, v197\n"
    "v_max_f32 v198, v198, v199\n"
    "v_max3_f32 v206, v206, v196, v198\n"
    "v_mov_b32 v207, v206\n"
    "ds_read_b64_tr_b16 v[162:163], v245 offset:224\n"
    "ds_read_b64_tr_b16 v[164:165], v246 offset:224\n"
    "v_permlane16_swap_b32 v206, v207\n"
    "v_max_f32 v206, v206, v207\n"
    "v_mov_b32 v207, v206\n"
    "ds_read_b64_tr_b16 v[166:167], v247 offset:224\n"
    "ds_read_b64_tr_b16 v[128:129], v244 offset:96\n"
    "v_permlane32_swap_b32 v206, v207\n"
    "v_max_f32 v206, v206, v207\n"
    "v_mul_f32 v207, s46, v206\n"
    "v_fma_f32 v168, v168, s47, v207\n"
    "v_fma_f32 v170, v170, s47, v207\n"
    "v_fma_f32 v172, v172, s47, v207\n"
    "v_fma_f32 v174, v174, s47, v207\n"
    "v_fma_f32 v176, v176, s47, v207\n"
    "v_fma_f32 v178, v178, s47, v207\n"
    "v_fma_f32 v180, v180, s47, v207\n"
    "v_fma_f32 v182, v182, s47, v207\n"
    "v_fma_f32 v184, v184, s47, v207\n"
    "v_fma_f32 v186, v186, s47, v207\n"
    "v_fma_f32 v188, v188, s47, v207\n"
    "v_fma_f32 v190, v190, s47, v207\n"
    "v_fma_f32 v192, v192, s47, v207\n"
    "v_fma_f32 v194, v194, s47, v207\n"
    "v_fma_f32 v196, v196, s47, v207\n"
    "v_fma_f32 v198, v198, s47, v207\n"
    "ds_read_b64_tr_b16 v[130:131], v245 offset:96\n"
    "ds_read_b64_tr_b16 v[132:133], v246 offset:96\n"
    "ds_read_b64_tr_b16 v[134:135], v247 offset:96\n"
    "ds_read_b64_tr_b16 v[136:137], v244 offset:128\n"
    "ds_read_b64_tr_b16 v[138:139], v245 offset:128\n"
    "ds_read_b64_tr_b16 v[140:141], v246 offset:128\n"
    "ds_read_b64_tr_b16 v[142:143], v247 offset:128\n"
    "ds_read_b64_tr_b16 v[104:105], v244 offset:0\n"
    "ds_read_b64_tr_b16 v[106:107], v245 offset:0\n"
    "ds_read_b64_tr_b16 v[108:109], v246 offset:0\n"
    "ds_read_b64_tr_b16 v[110:111], v247 offset:0\n"
    "ds_read_b64_tr_b16 v[112:113], v244 offset:32\n"
    "ds_read_b64_tr_b16 v[114:115], v245 offset:32\n"
    "ds_read_b64_tr_b16 v[116:117], v246 offset:32\n"
    "ds_read_b64_tr_b16 v[118:119], v247 offset:32\n"
    "ds_read_b64_tr_b16 v[120:121], v244 offset:64\n"
    "ds_read_b64_tr_b16 v[122:123], v245 offset:64\n"
    "ds_read_b64_tr_b16 v[124:125], v246 offset:64\n"
    "ds_read_b64_tr_b16 v[126:127], v247 offset:64\n"
    "ds_read_b32 v253, v6 offset:32\n"
    "v_exp_f32 v168, v168\n"
    "s_bfe_u32 s48, s60, 0x40000\n"
    "v_exp_f32 v170, v170\n"
    "s_bfe_u32 s49, s60, 0x40004\n"
    "v_exp_f32 v172, v172\n"
    "s_bfe_u32 s50, s60, 0x40008\n"
    "v_exp_f32 v174, v174\n"
    "s_bfe_u32 s51, s60, 0x4000c\n"
    "v_exp_f32 v176, v176\n"
    "s_mul_i32 s48, s48, 8896\n"
    "v_exp_f32 v178, v178\n"
    "s_mul_i32 s49, s49, 8896\n"
    "v_exp_f32 v180, v180\n"
    "s_mul_i32 s50, s50, 8896\n"
    "v_exp_f32 v182, v182\n"
    "s_mul_i32 s51, s51, 8896\n"
    "v_exp_f32 v184, v184\n"
    "s_lshr_b64 s[60:61], s[60:61], 8\n"
    "v_exp_f32 v186, v186\n"
    "s_add_u32 s45, s45, 4\n"
    "v_exp_f32 v188, v188\n"
    "s_add_u32 s54, s54, 2\n"
    "v_exp_f32 v190, v190\n"
    "s_add_u32 s55, s55, 4\n"
    "v_exp_f32 v192, v192\n"
    "s_cmp_eq_u32 s66, 2\n"
    "s_cselect_b32 s67, -2, 1\n"
    "v_exp_f32 v194, v194\n"
    "s_add_u32 s66, s66, s67\n"
    "v_exp_f32 v196, v196\n"
    "s_mul_i32 s68, s67, 6144\n"
    "v_exp_f32 v198, v198\n"
    "v_cvt_pk_bf16_f32 v200, v168, v170\n"
    "v_cvt_pk_bf16_f32 v201, v172, v174\n"
    "v_cvt_pk_bf16_f32 v202, v176, v178\n"
    "v_cvt_pk_bf16_f32 v203, v180, v182\n"
    "v_cvt_pk_bf16_f32 v204, v184, v186\n"
    "v_cvt_pk_bf16_f32 v205, v188, v190\n"
    "v_cvt_pk_bf16_f32 v206, v192, v194\n"
    "v_cvt_pk_bf16_f32 v207, v196, v198\n"
    "ds_read_b64_tr_b16 v[168:169], v244 offset:256\n"
    "ds_read_b64_tr_b16 v[170:171], v245 offset:256\n"
    "ds_read_b64_tr_b16 v[172:173], v246 offset:256\n"
    "ds_read_b64_tr_b16 v[174:175], v247 offset:256\n"
    "ds_read_b64_tr_b16 v[176:177], v244 offset:288\n"
    "ds_read_b64_tr_b16 v[178:179], v245 offset:288\n"
    "ds_read_b64_tr_b16 v[180:181], v246 offset:288\n"
    "ds_read_b64_tr_b16 v[182:183], v247 offset:288\n"
    "s_waitcnt lgkmcnt(8)\n"
    "s_setprio 1\n"
    "v_mfma_f32_16x16x32_bf16 v[196:199], v[248:251], v[200:203], 0\n"
    "v_cmp_gt_i32 vcc, s56, v253\n"
    "v_mfma_f32_16x16x32_bf16 v[196:199], v[248:251], v[204:207], v[196:199]\n"
    "v_smfmac_f32_16x16x64_bf16 v[220:223], v[200:203], v[32:39], v5\n"
    "ds_read_b128 v[32:35], v7 offset:32768\n"
    "v_smfmac_f32_16x16x64_bf16 v[224:227], v[200:203], v[40:47], v5\n"
    "ds_read_b128 v[36:39], v7 offset:32768\n"
    "v_smfmac_f32_16x16x64_bf16 v[228:231], v[200:203], v[48:55], v5\n"
    "ds_read_b128 v[40:43], v7 offset:32768\n"
    "v_smfmac_f32_16x16x64_bf16 v[208:211], v[200:203], v[8:15], v5\n"
    "ds_read_b128 v[44:47], v7 offset:32768\n"
    "v_smfmac_f32_16x16x64_bf16 v[212:215], v[200:203], v[16:23], v5\n"
    "ds_read_b128 v[48:51], v7 offset:32768\n"
    "v_smfmac_f32_16x16x64_bf16 v[216:219], v[200:203], v[24:31], v5\n"
    "ds_read_b128 v[52:55], v7 offset:32768\n"
    "v_smfmac_f32_16x16x64_bf16 v[208:211], v[204:207], v[104:111], v5\n"
    "ds_read_b64_tr_b16 v[184:185], v244 offset:320\n"
    "v_rcp_f32 v196, v196\n"
    "v_smfmac_f32_16x16x64_bf16 v[212:215], v[204:207], v[112:119], v5\n"
    "ds_read_b64_tr_b16 v[186:187], v245 offset:320\n"
    "v_lshrrev_b32 v197, 2, v3\n"
    "v_smfmac_f32_16x16x64_bf16 v[216:219], v[204:207], v[120:127], v5\n"
    "ds_read_b64_tr_b16 v[188:189], v246 offset:320\n"
    "v_and_or_b32 v197, v197, 60, s63\n"
    "v_smfmac_f32_16x16x64_bf16 v[220:223], v[204:207], v[128:135], v5\n"
    "ds_read_b64_tr_b16 v[190:191], v247 offset:320\n"
    "s_cbranch_vccnz .La7\n"
    ".La7b:\n"
    "v_smfmac_f32_16x16x64_bf16 v[224:227], v[204:207], v[136:143], v5\n"
    "ds_read_b64_tr_b16 v[192:193], v244 offset:352\n"
    "ds_read_b64_tr_b16 v[194:195], v245 offset:352\n"
    "v_smfmac_f32_16x16x64_bf16 v[228:231], v[204:207], v[144:151], v5\n"
    "s_waitcnt lgkmcnt(6)\n"
    "v_smfmac_f32_16x16x64_bf16 v[32:35], v[200:203], v[56:63], v5\n"
    "ds_write_b32 v197, v196 offset:0\n"
    "ds_read_b64_tr_b16 v[196:197], v246 offset:352\n"
    "v_smfmac_f32_16x16x64_bf16 v[36:39], v[200:203], v[64:71], v5\n"
    "ds_read_b64_tr_b16 v[198:199], v247 offset:352\n"
    "v_smfmac_f32_16x16x64_bf16 v[40:43], v[200:203], v[72:79], v5\n"
    "v_cvt_pk_bf16_f32 v208, v208, v209\n"
    "v_cvt_pk_bf16_f32 v209, v210, v211\n"
    "v_smfmac_f32_16x16x64_bf16 v[44:47], v[200:203], v[80:87], v5\n"
    "v_cvt_pk_bf16_f32 v210, v212, v213\n"
    "v_cvt_pk_bf16_f32 v211, v214, v215\n"
    "v_smfmac_f32_16x16x64_bf16 v[48:51], v[200:203], v[88:95], v5\n"
    "ds_write_b128 v4, v[208:211] offset:0\n"
    "v_smfmac_f32_16x16x64_bf16 v[52:55], v[200:203], v[96:103], v5\n"
    "v_cvt_pk_bf16_f32 v216, v216, v217\n"
    "v_cvt_pk_bf16_f32 v217, v218, v219\n"
    "v_smfmac_f32_16x16x64_bf16 v[32:35], v[204:207], v[152:159], v5\n"
    "ds_read_b32 v254, v6 offset:16\n"
    "ds_read_b32 v200, v6 offset:0\n"
    "v_smfmac_f32_16x16x64_bf16 v[36:39], v[204:207], v[160:167], v5\n"
    "v_cvt_pk_bf16_f32 v218, v220, v221\n"
    "s_waitcnt lgkmcnt(8)\n"
    "v_smfmac_f32_16x16x64_bf16 v[40:43], v[204:207], v[168:175], v5\n"
    "v_cvt_pk_bf16_f32 v219, v222, v223\n"
    "v_smfmac_f32_16x16x64_bf16 v[44:47], v[204:207], v[176:183], v5\n"
    "ds_write_b128 v4, v[216:219] offset:1024\n"
    "v_smfmac_f32_16x16x64_bf16 v[48:51], v[204:207], v[184:191], v5\n"
    "v_cvt_pk_bf16_f32 v224, v224, v225\n"
    "s_waitcnt lgkmcnt(4)\n"
    "v_smfmac_f32_16x16x64_bf16 v[52:55], v[204:207], v[192:199], v5\n"
    "v_cvt_pk_bf16_f32 v225, v226, v227\n"
    "v_cvt_pk_bf16_f32 v226, v228, v229\n"
    "s_setprio 0\n"
    "v_add_u32 v244, s48, v0\n"
    "v_add_u32 v245, s49, v0\n"
    "v_add_u32 v246, s50, v0\n"
    "v_add_u32 v247, s51, v0\n"
    "v_add_u32 v3, s68, v3\n"
    "v_cvt_pk_bf16_f32 v227, v230, v231\n"
    "ds_write_b128 v4, v[224:227] offset:2048\n"
    "s_waitcnt lgkmcnt(3)\n"
    "v_cmp_gt_u32 vcc, s55, v254\n"
    "s_waitcnt lgkmcnt(2)\n"
    "v_cmp_gt_u32 s[58:59], s54, v200\n"
    "s_add_u32 s56, s56, 4\n"
    "s_lshl_b32 s67, s67, 6\n"
    "s_add_u32 s63, s63, s67\n"
    "s_sub_u32 s53, s68, 18432\n"
    "s_add_u32 s67, s63, 192\n"
    "s_cmp_ge_u32 s45, 10\n"
    "s_cselect_b32 s53, s53, s68\n"
    "s_cselect_b32 s63, s67, s63\n"
    "s_cselect_b32 s56, -16, s56\n"
    "s_cmp_ge_u32 s45, 12\n"
    "s_cbranch_scc1 .La8\n"
    "s_or_b64 vcc, vcc, s[58:59]\n"
    "s_cbranch_scc1 .La8\n"
    "ds_read_b128 v[8:11], v3 offset:0\n"
    "ds_read_b128 v[56:59], v244 offset:0\n"
    "ds_read_b128 v[12:15], v3 offset:1024\n"
    "ds_read_b128 v[60:63], v244 offset:64\n"
    "ds_read_b128 v[16:19], v3 offset:2048\n"
    "ds_read_b128 v[64:67], v244 offset:128\n"
    "ds_read_b128 v[20:23], v3 offset:3072\n"
    "ds_read_b128 v[68:71], v244 offset:192\n"
    "ds_read_b128 v[24:27], v3 offset:4096\n"
    "ds_read_b128 v[28:31], v3 offset:5120\n"
    "v_cvt_pk_bf16_f32 v32, v32, v33\n"
    "v_cvt_pk_bf16_f32 v33, v34, v35\n"
    "v_cvt_pk_bf16_f32 v34, v36, v37\n"
    "v_cvt_pk_bf16_f32 v35, v38, v39\n"
    "ds_write_b128 v4, v[32:35] offset:3072\n"
    "v_cvt_pk_bf16_f32 v40, v40, v41\n"
    "v_cvt_pk_bf16_f32 v41, v42, v43\n"
    "v_cvt_pk_bf16_f32 v42, v44, v45\n"
    "v_cvt_pk_bf16_f32 v43, v46, v47\n"
    "ds_write_b128 v4, v[40:43] offset:4096\n"
    "v_cvt_pk_bf16_f32 v48, v48, v49\n"
    "v_cvt_pk_bf16_f32 v49, v50, v51\n"
    "v_cvt_pk_bf16_f32 v50, v52, v53\n"
    "v_cvt_pk_bf16_f32 v51, v54, v55\n"
    "ds_write_b128 v4, v[48:51] offset:5120\n"
    "ds_write_b32 v7, v252 offset:128\n"
    "ds_read_b128 v[32:35], v244 offset:256\n"
    "ds_read_b128 v[36:39], v244 offset:320\n"
    "ds_read_b128 v[40:43], v245 offset:0\n"
    "ds_read_b128 v[44:47], v245 offset:64\n"
    ".La8j:\n"
    "s_branch .La_loop\n"
    ".La_end:\n"
    "s_waitcnt lgkmcnt(0)\n"
    "s_endpgm\n"
    ".La_ld:\n"
    "s_mov_b32 s52, 0x40000\n"
    "ds_read_b32 v253, v6 offset:256\n"
    "s_waitcnt lgkmcnt(0)\n"
    "v_cmp_gt_u32 vcc, 1, v253\n"
    "s_cbranch_vccnz .Lb1\n"
    ".Lb1b:\n"
    "v_cmp_gt_u32 vcc, 2, v18\n"
    "v_mov_b32 v19, 0x100000\n"
    "v_mov_b32 v252, 1\n"
    "v_cndmask_b32 v254, v19, v6, vcc\n"
    "s_mul_i32 s48, s75, 9\n"
    "s_add_u32 s48, s48, s74\n"
    "s_add_u32 s50, s72, s48\n"
    "s_addc_u32 s51, s73, 0\n"
    "s_mov_b32 m0, 81088\n"
    "s_nop 0\n"
    "global_load_lds_dwordx4 v13, s[50:51]\n"
    "s_mov_b32 m0, 82112\n"
    "s_nop 0\n"
    "global_load_lds_dwordx4 v8, s[50:51]\n"
    "s_mov_b32 m0, 83136\n"
    "s_nop 0\n"
    "global_load_lds_dwordx4 v14, s[50:51]\n"
    "s_mov_b32 m0, 84160\n"
    "s_nop 0\n"
    "global_load_lds_dwordx4 v9, s[50:51]\n"
    "s_mov_b32 m0, 85184\n"
    "s_nop 0\n"
    "global_load_lds_dwordx4 v15, s[50:51]\n"
    "s_mov_b32 m0, 86208\n"
    "s_nop 0\n"
    "global_load_lds_dwordx4 v10, s[50:51]\n"
    "s_mov_b32 m0, 87232\n"
    "s_nop 0\n"
    "global_load_lds_dwordx4 v16, s[50:51]\n"
    "s_mov_b32 m0, 80064\n"
    "s_nop 0\n"
    "global_load_lds_dwordx4 v17, s[50:51]\n"
    "s_mov_b32 m0, 87840\n"
    "s_nop 0\n"
    "global_load_lds_dwordx4 v12, s[50:51]\n"
    "s_mul_i32 s48, s75, 10\n"
    "s_add_u32 s48, s48, s74\n"
    "s_add_u32 s50, s72, s48\n"
    "s_addc_u32 s51, s73, 0\n"
    "s_mov_b32 m0, 89984\n"
    "s_nop 0\n"
    "global_load_lds_dwordx4 v13, s[50:51]\n"
    "s_mov_b32 m0, 91008\n"
    "s_nop 0\n"
    "global_load_lds_dwordx4 v8, s[50:51]\n"
    "s_mov_b32 m0, 92032\n"
    "s_nop 0\n"
    "global_load_lds_dwordx4 v14, s[50:51]\n"
    "s_mov_b32 m0, 93056\n"
    "s_nop 0\n"
    "global_load_lds_dwordx4 v9, s[50:51]\n"
    "s_mov_b32 m0, 94080\n"
    "s_nop 0\n"
    "global_load_lds_dwordx4 v15, s[50:51]\n"
    "s_mov_b32 m0, 95104\n"
    "s_nop 0\n"
    "global_load_lds_dwordx4 v10, s[50:51]\n"
    "s_mov_b32 m0, 96128\n"
    "s_nop 0\n"
    "global_load_lds_dwordx4 v16, s[50:51]\n"
    "s_mov_b32 m0, 88960\n"
    "s_nop 0\n"
    "global_load_lds_dwordx4 v17, s[50:51]\n"
    "s_mov_b32 m0, 96736\n"
    "s_nop 0\n"
    "global_load_lds_dwordx4 v12, s[50:51]\n"
    "s_mul_i32 s48, s75, 11\n"
    "s_add_u32 s48, s48, s74\n"
    "s_add_u32 s50, s72, s48\n"
    "s_addc_u32 s51, s73, 0\n"
    "s_mov_b32 m0, 98880\n"
    "s_nop 0\n"
    "global_load_lds_dwordx4 v13, s[50:51]\n"
    "s_mov_b32 m0, 99904\n"
    "s_nop 0\n"
    "global_load_lds_dwordx4 v8, s[50:51]\n"
    "s_mov_b32 m0, 100928\n"
    "s_nop 0\n"
    "global_load_lds_dwordx4 v14, s[50:51]\n"
    "s_mov_b32 m0, 101952\n"
    "s_nop 0\n"
    "global_load_lds_dwordx4 v9, s[50:51]\n"
    "s_mov_b32 m0, 102976\n"
    "s_nop 0\n"
    "global_load_lds_dwordx4 v15, s[50:51]\n"
    "s_mov_b32 m0, 104000\n"
    "s_nop 0\n"
    "global_load_lds_dwordx4 v10, s[50:51]\n"
    "s_mov_b32 m0, 105024\n"
    "s_nop 0\n"
    "global_load_lds_dwordx4 v16, s[50:51]\n"
    "s_mov_b32 m0, 97856\n"
    "s_nop 0\n"
    "global_load_lds_dwordx4 v17, s[50:51]\n"
    "s_mov_b32 m0, 105632\n"
    "s_nop 0\n"
    "global_load_lds_dwordx4 v12, s[50:51]\n"
    "ds_write_b32 v6, v252 offset:288\n"
    "ds_read_b32 v253, v6 offset:0\n"
    "s_waitcnt lgkmcnt(0)\n"
    "v_cmp_gt_u32 vcc, 4, v253\n"
    "s_cbranch_vccnz .Lb2\n"
    ".Lb2b:\n"
    "s_waitcnt vmcnt(18)\n"
    "v_mov_b32 v252, 5\n"
    "ds_write_b32 v254, v252 offset:0\n"
    "s_waitcnt vmcnt(0)\n"
    "v_mov_b32 v252, 6\n"
    "ds_write_b32 v254, v252 offset:0\n"
    "s_branch .La_join\n"
    ".Lb1:\n"
    "s_sub_u32 s52, s52, 1\n"
    "s_cmp_lt_i32 s52, 0\n"
    "s_cbranch_scc1 .Lb1b\n"
    "s_sleep 1\n"
    "ds_read_b32 v253, v6 offset:256\n"
    "s_waitcnt lgkmcnt(0)\n"
    "v_cmp_gt_u32 vcc, 1, v253\n"
    "s_cbranch_vccnz .Lb1\n"
    "s_branch .Lb1b\n"
    ".Lb2:\n"
    "s_sub_u32 s52, s52, 1\n"
    "s_cmp_lt_i32 s52, 0\n"
    "s_cbranch_scc1 .Lb2b\n"
    "s_sleep 1\n"
    "ds_read_b32 v253, v6 offset:0\n"
    "s_waitcnt lgkmcnt(0)\n"
    "v_cmp_gt_u32 vcc, 4, v253\n"
    "s_cbranch_vccnz .Lb2\n"
    "s_branch .Lb2b\n"
    ".La1:\n"
    "s_sub_u32 s52, s52, 1\n"
    "s_cmp_lt_i32 s52, 0\n"
    "s_cbranch_scc1 .La1b\n"
    "s_sleep 1\n"
    "ds_read_b32 v253, v6 offset:0\n"
    "s_waitcnt lgkmcnt(0)\n"
    "v_cmp_gt_u32 vcc, s54, v253\n"
    "s_cbranch_vccnz .La1\n"
    "s_branch .La1b\n"
    ".La_fill:\n"
    "s_mov_b32 s69, 0\n"
    "ds_read_b64_tr_b16 v[104:105], v200 offset:0\n"
    "ds_read_b64_tr_b16 v[106:107], v201 offset:0\n"
    "ds_read_b64_tr_b16 v[108:109], v202 offset:0\n"
    "ds_read_b64_tr_b16 v[110:111], v203 offset:0\n"
    "ds_read_b64_tr_b16 v[112:113], v200 offset:32\n"
    "ds_read_b64_tr_b16 v[114:115], v201 offset:32\n"
    "ds_read_b64_tr_b16 v[116:117], v202 offset:32\n"
    "ds_read_b64_tr_b16 v[118:119], v203 offset:32\n"
    "ds_read_b64_tr_b16 v[120:121], v200 offset:64\n"
    "ds_read_b64_tr_b16 v[122:123], v201 offset:64\n"
    "ds_read_b64_tr_b16 v[124:125], v202 offset:64\n"
    "ds_read_b64_tr_b16 v[126:127], v203 offset:64\n"
    "ds_read_b64_tr_b16 v[128:129], v200 offset:96\n"
    "ds_read_b64_tr_b16 v[130:131], v201 offset:96\n"
    "ds_read_b64_tr_b16 v[132:133], v202 offset:96\n"
    "ds_read_b64_tr_b16 v[134:135], v203 offset:96\n"
    "ds_read_b64_tr_b16 v[136:137], v200 offset:128\n"
    "ds_read_b64_tr_b16 v[138:139], v201 offset:128\n"
    "ds_read_b64_tr_b16 v[140:141], v202 offset:128\n"
    "ds_read_b64_tr_b16 v[142:143], v203 offset:128\n"
    "ds_read_b64_tr_b16 v[144:145], v200 offset:160\n"
    "ds_read_b64_tr_b16 v[146:147], v201 offset:160\n"
    "ds_read_b64_tr_b16 v[148:149], v202 offset:160\n"
    "ds_read_b64_tr_b16 v[150:151], v203 offset:160\n"
    "ds_read_b64_tr_b16 v[152:153], v200 offset:192\n"
    "ds_read_b64_tr_b16 v[154:155], v201 offset:192\n"
    "ds_read_b64_tr_b16 v[156:157], v202 offset:192\n"
    "ds_read_b64_tr_b16 v[158:159], v203 offset:192\n"
    "ds_read_b64_tr_b16 v[160:161], v200 offset:224\n"
    "ds_read_b64_tr_b16 v[162:163], v201 offset:224\n"
    "ds_read_b64_tr_b16 v[164:165], v202 offset:224\n"
    "ds_read_b64_tr_b16 v[166:167], v203 offset:224\n"
    "ds_read_b64_tr_b16 v[168:169], v200 offset:256\n"
    "ds_read_b64_tr_b16 v[170:171], v201 offset:256\n"
    "ds_read_b64_tr_b16 v[172:173], v202 offset:256\n"
    "ds_read_b64_tr_b16 v[174:175], v203 offset:256\n"
    "ds_read_b64_tr_b16 v[176:177], v200 offset:288\n"
    "ds_read_b64_tr_b16 v[178:179], v201 offset:288\n"
    "ds_read_b64_tr_b16 v[180:181], v202 offset:288\n"
    "ds_read_b64_tr_b16 v[182:183], v203 offset:288\n"
    "ds_read_b64_tr_b16 v[184:185], v200 offset:320\n"
    "ds_read_b64_tr_b16 v[186:187], v201 offset:320\n"
    "ds_read_b64_tr_b16 v[188:189], v202 offset:320\n"
    "ds_read_b64_tr_b16 v[190:191], v203 offset:320\n"
    "ds_read_b64_tr_b16 v[192:193], v200 offset:352\n"
    "ds_read_b64_tr_b16 v[194:195], v201 offset:352\n"
    "ds_read_b64_tr_b16 v[196:197], v202 offset:352\n"
    "ds_read_b64_tr_b16 v[198:199], v203 offset:352\n"
    "s_branch .La_filled\n"
    ".La2:\n"
    "s_sub_u32 s52, s52, 1\n"
    "s_cmp_lt_i32 s52, 0\n"
    "s_cbranch_scc1 .La2b\n"
    "s_sleep 1\n"
    "ds_read_b32 v254, v6 offset:16\n"
    "s_waitcnt lgkmcnt(0)\n"
    "v_cmp_gt_u32 vcc, s55, v254\n"
    "s_cbranch_vccnz .La2\n"
    "s_branch .La2b\n"
    ".La3:\n"
    "s_sub_u32 s52, s52, 1\n"
    "s_cmp_lt_i32 s52, 0\n"
    "s_cbranch_scc1 .La3b\n"
    "s_sleep 1\n"
    "ds_read_b32 v253, v6 offset:32\n"
    "s_waitcnt lgkmcnt(0)\n"
    "v_cmp_gt_i32 vcc, s56, v253\n"
    "s_cbranch_vccnz .La3\n"
    "s_branch .La3b\n"
    ".La5:\n"
    "s_sub_u32 s52, s52, 1\n"
    "s_cmp_lt_i32 s52, 0\n"
    "s_cbranch_scc1 .La5b\n"
    "s_sleep 1\n"
    "ds_read_b32 v200, v6 offset:0\n"
    "s_waitcnt lgkmcnt(0)\n"
    "v_cmp_gt_u32 vcc, s54, v200\n"
    "s_cbranch_vccnz .La5\n"
    "s_branch .La5b\n"
    ".La6:\n"
    "s_sub_u32 s52, s52, 1\n"
    "s_cmp_lt_i32 s52, 0\n"
    "s_cbranch_scc1 .La6b\n"
    "s_sleep 1\n"
    "ds_read_b32 v254, v6 offset:16\n"
    "s_waitcnt lgkmcnt(0)\n"
    "v_cmp_gt_u32 vcc, s55, v254\n"
    "s_cbranch_vccnz .La6\n"
    "s_branch .La6b\n"
    ".La4:\n"
    "v_cvt_pk_bf16_f32 v128, v128, v129\n"
    "v_cvt_pk_bf16_f32 v129, v130, v131\n"
    "v_cvt_pk_bf16_f32 v130, v132, v133\n"
    "v_cvt_pk_bf16_f32 v131, v134, v135\n"
    "ds_write_b128 v4, v[128:131] offset:3072\n"
    "v_cvt_pk_bf16_f32 v136, v136, v137\n"
    "v_cvt_pk_bf16_f32 v137, v138, v139\n"
    "v_cvt_pk_bf16_f32 v138, v140, v141\n"
    "v_cvt_pk_bf16_f32 v139, v142, v143\n"
    "ds_write_b128 v4, v[136:139] offset:4096\n"
    "v_cvt_pk_bf16_f32 v144, v144, v145\n"
    "v_cvt_pk_bf16_f32 v145, v146, v147\n"
    "v_cvt_pk_bf16_f32 v146, v148, v149\n"
    "v_cvt_pk_bf16_f32 v147, v150, v151\n"
    "ds_write_b128 v4, v[144:147] offset:5120\n"
    "ds_write_b32 v7, v252 offset:128\n"
    "s_cmp_ge_u32 s45, 12\n"
    "s_cbranch_scc1 .La_end\n"
    "ds_read_b32 v200, v6 offset:0\n"
    "s_waitcnt lgkmcnt(0)\n"
    "v_cmp_gt_u32 vcc, s54, v200\n"
    "s_cbranch_vccnz .La5\n"
    ".La5b:\n"
    "ds_read_b32 v254, v6 offset:16\n"
    "s_waitcnt lgkmcnt(0)\n"
    "v_cmp_gt_u32 vcc, s55, v254\n"
    "s_cbranch_vccnz .La6\n"
    ".La6b:\n"
    "ds_read_b128 v[104:107], v3 offset:0\n"
    "ds_read_b128 v[152:155], v244 offset:0\n"
    "ds_read_b128 v[108:111], v3 offset:1024\n"
    "ds_read_b128 v[156:159], v244 offset:64\n"
    "ds_read_b128 v[112:115], v3 offset:2048\n"
    "ds_read_b128 v[160:163], v244 offset:128\n"
    "ds_read_b128 v[116:119], v3 offset:3072\n"
    "ds_read_b128 v[164:167], v244 offset:192\n"
    "ds_read_b128 v[120:123], v3 offset:4096\n"
    "ds_read_b128 v[124:127], v3 offset:5120\n"
    "ds_read_b128 v[128:131], v244 offset:256\n"
    "ds_read_b128 v[132:135], v244 offset:320\n"
    "ds_read_b128 v[136:139], v245 offset:0\n"
    "ds_read_b128 v[140:143], v245 offset:64\n"
    "s_branch .La4j\n"
    ".La7:\n"
    "s_sub_u32 s52, s52, 1\n"
    "s_cmp_lt_i32 s52, 0\n"
    "s_cbranch_scc1 .La7b\n"
    "s_sleep 1\n"
    "ds_read_b32 v253, v6 offset:32\n"
    "s_waitcnt lgkmcnt(0)\n"
    "v_cmp_gt_i32 vcc, s56, v253\n"
    "s_cbranch_vccnz .La7\n"
    "s_branch .La7b\n"
    ".La9:\n"
    "s_sub_u32 s52, s52, 1\n"
    "s_cmp_lt_i32 s52, 0\n"
    "s_cbranch_scc1 .La9b\n"
    "s_sleep 1\n"
    "ds_read_b32 v200, v6 offset:0\n"
    "s_waitcnt lgkmcnt(0)\n"
    "v_cmp_gt_u32 vcc, s54, v200\n"
    "s_cbranch_vccnz .La9\n"
    "s_branch .La9b\n"
    ".La10:\n"
    "s_sub_u32 s52, s52, 1\n"
    "s_cmp_lt_i32 s52, 0\n"
    "s_cbranch_scc1 .La10b\n"
    "s_sleep 1\n"
    "ds_read_b32 v254, v6 offset:16\n"
    "s_waitcnt lgkmcnt(0)\n"
    "v_cmp_gt_u32 vcc, s55, v254\n"
    "s_cbranch_vccnz .La10\n"
    "s_branch .La10b\n"
    ".La8:\n"
    "v_cvt_pk_bf16_f32 v32, v32, v33\n"
    "v_cvt_pk_bf16_f32 v33, v34, v35\n"
    "v_cvt_pk_bf16_f32 v34, v36, v37\n"
    "v_cvt_pk_bf16_f32 v35, v38, v39\n"
    "ds_write_b128 v4, v[32:35] offset:3072\n"
    "v_cvt_pk_bf16_f32 v40, v40, v41\n"
    "v_cvt_pk_bf16_f32 v41, v42, v43\n"
    "v_cvt_pk_bf16_f32 v42, v44, v45\n"
    "v_cvt_pk_bf16_f32 v43, v46, v47\n"
    "ds_write_b128 v4, v[40:43] offset:4096\n"
    "v_cvt_pk_bf16_f32 v48, v48, v49\n"
    "v_cvt_pk_bf16_f32 v49, v50, v51\n"
    "v_cvt_pk_bf16_f32 v50, v52, v53\n"
    "v_cvt_pk_bf16_f32 v51, v54, v55\n"
    "ds_write_b128 v4, v[48:51] offset:5120\n"
    "ds_write_b32 v7, v252 offset:128\n"
    "s_cmp_ge_u32 s45, 12\n"
    "s_cbranch_scc1 .La_end\n"
    "ds_read_b32 v200, v6 offset:0\n"
    "s_waitcnt lgkmcnt(0)\n"
    "v_cmp_gt_u32 vcc, s54, v200\n"
    "s_cbranch_vccnz .La9\n"
    ".La9b:\n"
    "ds_read_b32 v254, v6 offset:16\n"
    "s_waitcnt lgkmcnt(0)\n"
    "v_cmp_gt_u32 vcc, s55, v254\n"
    "s_cbranch_vccnz .La10\n"
    ".La10b:\n"
    "ds_read_b128 v[8:11], v3 offset:0\n"
    "ds_read_b128 v[56:59], v244 offset:0\n"
    "ds_read_b128 v[12:15], v3 offset:1024\n"
    "ds_read_b128 v[60:63], v244 offset:64\n"
    "ds_read_b128 v[16:19], v3 offset:2048\n"
    "ds_read_b128 v[64:67], v244 offset:128\n"
    "ds_read_b128 v[20:23], v3 offset:3072\n"
    "ds_read_b128 v[68:71], v244 offset:192\n"
    "ds_read_b128 v[24:27], v3 offset:4096\n"
    "ds_read_b128 v[28:31], v3 offset:5120\n"
    "ds_read_b128 v[32:35], v244 offset:256\n"
    "ds_read_b128 v[36:39], v244 offset:320\n"
    "ds_read_b128 v[40:43], v245 offset:0\n"
    "ds_read_b128 v[44:47], v245 offset:64\n"
    "s_branch .La8j\n"
    ".La_last:\n"
            :: "{v[8:11]}"(xo), "{v12}"(xo4), "{v13}"(xq[0]), "{v14}"(xq[1]), "{v15}"(xq[2]), "{v16}"(xq[3]), "{v17}"(xq4), "{v18}"(lane),
               "{s70}"(UNI(wave == 7)), "{s[72:73]}"(xb), "{s74}"(UNI(rm.o * IROWB)), "{s75}"(UNI(rm.d * IROWB)),
               "{v[0:3]}"(va), "{v[4:7]}"(vb), "{v[232:235]}"(cbM), "{v[236:239]}"(cbF), "{v[240:243]}"(cbL),
               "{s60}"(UNI(slots)), "{s61}"(UNI(slots >> 32)), "{s45}"(UNI(n0)), "{s54}"(UNI(u0 + 4)), "{s63}"(UNI((unsigned)(size_t)smem + LF + 320 + 64 * sl)), "{s66}"(UNI(sl)),
               "{s46}"(-0.10411754112863712f), "{s47}"(0.10411754112863712f)
            : "memory", "v254", "s48", "s49", "s50", "s51", "s52", "s53", "s55", "s56", "s57", "s58", "s59", "s62", "s67", "s68", "s69", "s88", "s89", "s90", "s91", "s98", "s99", "s92", "s93", "s94", "s95", "vcc", "scc");
    }
}

extern "C" void kernel_launch(void* const* d_in, const int* in_sizes, int n_in,
                              void* d_out, int out_size, void* d_ws, size_t ws_size,
                              hipStream_t stream) {
    na_main<<<256, 512, 0, stream>>>((const u16*)d_in[0], (const u16*)d_in[1], (const u16*)d_in[2], (u16*)d_out, (char*)d_ws + 0);
}
```

```cpp
#define PROBE -1
#include <hip/hip_runtime.h>
#include <stdint.h>

typedef unsigned short u16;
typedef unsigned long long u64;
typedef __attribute__((ext_vector_type(4))) float f32x4;
typedef __attribute__((ext_vector_type(4))) unsigned u32x4;
typedef __attribute__((ext_vector_type(2))) unsigned u32x2;
typedef __attribute__((ext_vector_type(8))) unsigned u32x8;

#define DEVINL __device__ __forceinline__
#define LAMBDA_INL __attribute__((always_inline))
#define UNI(x) __builtin_amdgcn_readfirstlane((int)(x))

static constexpr int IMW = 224;
static constexpr int PIXB = 384;
static constexpr int IROWB = IMW * PIXB;
static constexpr int ROWB = 8192;
static constexpr int LDS_E = 14 * ROWB;
static constexpr int EB = 12288;
static constexpr int LDS_TOT = 163840;
static constexpr int INVOFF = 6 * ROWB + 8000;
static constexpr unsigned ZOFF = 0u;
static constexpr int WS_W = 8192, WS_FL = WS_W + 144 * 1024, NREP = 8, FSTR = 128;

DEVINL int colad(int c) { return 800 * (c >> 1) + 384 * (c & 1); }
struct AxisMap { int o, d, x, edge; };
DEVINL AxisMap axis_map(int par, int t, bool alt) {
    const bool lo = par == 0 && t == 0, hi = par == 1 && t == 7, mir = alt ? (t & 1) == 0 : lo;
    AxisMap m;
    m.o = 28 * t + 13 * par + (mir ? 26 : -12);
    m.d = mir ? -2 : 2;
    m.x = lo ? 1 : 218;
    m.edge = lo || hi;
    return m;
}
DEVINL int axis_at(const AxisMap& m, int v) {
    const int reg = m.o + m.d * v, ext = m.x + 2 * min(v - 14, 2), sel = ((m.edge ? 13 : 99) - v) >> 31;
    return (ext & sel) | (reg & ~sel);
}

#define STAMP(k) do {} while (0)

__global__ __launch_bounds__(512, 2) void na_main(const u16* __restrict__ x_, const u16* __restrict__ wqkv_,
                                                  const u16* __restrict__ wo_, u16* __restrict__ out_, char* ws_) {
    __shared__ __attribute__((aligned(1024))) char smem[LDS_TOT];
    const unsigned pt0 = (unsigned)__builtin_amdgcn_s_memrealtime(), pc0 = (unsigned)__builtin_amdgcn_s_memtime();
    int tid = threadIdx.x, bid = blockIdx.x;
    u32x8 ka;
    u32x8 est;
    u32x2 ka4;
    unsigned xo0, xo1;
    int pf;
    asm volatile(
    "s_load_dwordx8 s[8:15], s[4:5], 0x0\n"
    "s_load_dwordx2 s[16:17], s[4:5], 0x20\n"
    "v_readfirstlane_b32 s7, v0\n"
    "v_and_b32 v3, 63, v0\n"
    "v_lshrrev_b32 v4, 2, v3\n"
    "s_lshr_b32 s7, s7, 6\n"
    "s_cmp_lg_u32 s7, 1\n"
    "s_cbranch_scc1 .Le_ncp\n"
    "s_getpc_b64 s[26:27]\n"
    ".Le_pc:\n"
    "s_lshr_b32 s18, s6, 3\n"
    "s_lshl_b32 s18, s18, 10\n"
    "s_mov_b32 s19, .Lg_last-.Le_pc\n"
    "s_max_u32 s19, s19, .La_last-.Le_pc\n"
    "s_and_b32 s19, s19, -16\n"
    "s_min_u32 s18, s18, s19\n"
    "v_lshl_add_u32 v5, v3, 4, s18\n"
    "s_mov_b32 m0, 147456\n"
    "s_and_b32 s26, s26, -16\n"
    "global_load_lds_dwordx4 v5, s[26:27]\n"
    ".Le_ncp:\n"
    "s_lshr_b32 s18, s6, 3\n"
    "s_and_b32 s19, s6, 7\n"
    "s_lshl2_add_u32 s3, s18, s19\n"
    "s_lshl_b32 s20, s19, 5\n"
    "s_sub_u32 s18, s18, s20\n"
    "s_add_u32 s18, s18, 352\n"
    "s_cmp_lt_u32 s19, 4\n"
    "s_cselect_b32 s3, s3, s18\n"
    "s_sub_u32 s18, s3, 72\n"
    "s_min_u32 s18, s18, s3\n"
    "s_mul_i32 s19, s18, 43\n"
    "s_lshr_b32 s19, s19, 8\n"
    "s_mul_i32 s20, s19, 6\n"
    "s_sub_u32 s20, s18, s20\n"
    "v_lshrrev_b32 v5, 4, v3\n"
    "v_sub_u32 v6, 0, v5\n"
    "v_xor_b32 v6, v6, v3\n"
    "v_and_b32 v6, 3, v6\n"
    "v_lshlrev_b32 v6, 4, v6\n"
    "s_mul_i32 s20, s20, 36864\n"
    "s_movk_i32 s23, 1152\n"
    "s_lshr_b32 s25, s19, 1\n"
    "v_mov_b32 v16, 155648\n"
    "v_mov_b32 v17, 0\n"
    "s_cmp_ge_u32 s3, 72\n"
    "s_cbranch_scc1 .Le_nb\n"
    "ds_write_b32 v16, v17\n"
    "s_waitcnt lgkmcnt(0)\n"
    "s_barrier\n"
    ".Le_nb:\n"
    "s_cmp_ge_u32 s7, 4\n"
    "s_cbranch_scc1 .Le_att\n"
    "s_cmp_lg_u32 s7, 0\n"
    "s_cbranch_scc1 .Le_gemm\n"
    "s_cmp_ge_u32 s3, 72\n"
    "s_cbranch_scc1 .Le_gemm\n"
    "v_lshl_add_u32 v7, v5, 2, v4\n"
    "v_mad_u32_u24 v7, v7, s23, v6\n"
    "s_and_b32 s24, s19, 1\n"
    "s_mul_i32 s25, s25, 36864\n"
    "s_mul_i32 s24, s24, 4608\n"
    "s_add_u32 s24, s24, s25\n"
    "s_add_u32 s24, s24, 384\n"
    "v_mov_b32 v9, 0\n"
    "s_mov_b32 m0, 114688\n"
    "s_waitcnt lgkmcnt(0)\n"
    "s_add_u32 s26, s10, s20\n"
    "s_addc_u32 s27, s11, 0\n"
    "global_load_dwordx2 v[12:13], v9, s[16:17] sc1\n"
    "s_add_u32 s28, s26, 4608\n"
    "s_addc_u32 s29, s27, 0\n"
    "s_add_u32 s30, s10, s24\n"
    "s_addc_u32 s31, s11, 0\n"
    "global_load_lds_dwordx4 v7, s[26:27] offset:0\n"
    "s_addk_i32 m0, 1024\n"
    "s_nop 0\n"
    "global_load_lds_dwordx4 v7, s[28:29] offset:0\n"
    "s_addk_i32 m0, 1024\n"
    "s_nop 0\n"
    "global_load_lds_dwordx4 v7, s[30:31] offset:0\n"
    "s_addk_i32 m0, 960\n"
    "s_nop 0\n"
    "global_load_lds_dwordx4 v7, s[26:27] offset:64\n"
    "s_addk_i32 m0, 1024\n"
    "s_nop 0\n"
    "global_load_lds_dwordx4 v7, s[28:29] offset:64\n"
    "s_addk_i32 m0, 1024\n"
    "s_nop 0\n"
    "global_load_lds_dwordx4 v7, s[30:31] offset:64\n"
    "s_addk_i32 m0, 960\n"
    "s_nop 0\n"
    "global_load_lds_dwordx4 v7, s[26:27] offset:128\n"
    "s_addk_i32 m0, 1024\n"
    "s_nop 0\n"
    "global_load_lds_dwordx4 v7, s[28:29] offset:128\n"
    "s_addk_i32 m0, 1024\n"
    "s_nop 0\n"
    "global_load_lds_dwordx4 v7, s[30:31] offset:128\n"
    "s_addk_i32 m0, 960\n"
    "s_nop 0\n"
    "global_load_lds_dwordx4 v7, s[26:27] offset:192\n"
    "s_addk_i32 m0, 1024\n"
    "s_nop 0\n"
    "global_load_lds_dwordx4 v7, s[28:29] offset:192\n"
    "s_addk_i32 m0, 1024\n"
    "s_nop 0\n"
    "global_load_lds_dwordx4 v7, s[30:31] offset:192\n"
    "s_addk_i32 m0, 960\n"
    "s_nop 0\n"
    "global_load_lds_dwordx4 v7, s[26:27] offset:256\n"
    "s_addk_i32 m0, 1024\n"
    "s_nop 0\n"
    "global_load_lds_dwordx4 v7, s[28:29] offset:256\n"
    "s_addk_i32 m0, 1024\n"
    "s_nop 0\n"
    "global_load_lds_dwordx4 v7, s[30:31] offset:256\n"
    "s_addk_i32 m0, 960\n"
    "s_nop 0\n"
    "global_load_lds_dwordx4 v7, s[26:27] offset:320\n"
    "s_addk_i32 m0, 1024\n"
    "s_nop 0\n"
    "global_load_lds_dwordx4 v7, s[28:29] offset:320\n"
    "s_addk_i32 m0, 1024\n"
    "s_nop 0\n"
    "global_load_lds_dwordx4 v7, s[30:31] offset:320\n"
    "v_bfe_u32 v8, v3, 2, 2\n"
    "v_sub_u32 v8, 0, v8\n"
    "v_xor_b32 v8, v8, v5\n"
    "v_and_b32 v8, 3, v8\n"
    "v_lshlrev_b32 v8, 4, v8\n"
    "v_and_b32 v10, 15, v3\n"
    "v_lshl_add_u32 v8, v10, 6, v8\n"
    "v_add_u32 v8, 114688, v8\n"
    "s_lshl_b32 s18, s3, 10\n"
    "s_add_u32 s18, s18, 8192\n"
    "v_lshl_add_u32 v9, v3, 4, s18\n"
    "s_lshl_b32 s18, s3, 7\n"
    "s_add_u32 s18, s18, 155648\n"
    "v_bfe_u32 v10, v3, 3, 3\n"
    "v_mul_u32_u24 v10, 18432, v10\n"
    "v_and_b32 v11, 7, v3\n"
    "v_lshl_add_u32 v10, v11, 4, v10\n"
    "v_add_u32 v10, s18, v10\n"
    "s_waitcnt vmcnt(9)\n"
    "ds_read_b128 v[20:23], v8 offset:0\n"
    "ds_read_b128 v[24:27], v8 offset:1024\n"
    "ds_read_b128 v[28:31], v8 offset:2048\n"
    "ds_read_b128 v[32:35], v8 offset:3072\n"
    "ds_read_b128 v[36:39], v8 offset:4096\n"
    "ds_read_b128 v[40:43], v8 offset:5120\n"
    "ds_read_b128 v[44:47], v8 offset:6144\n"
    "ds_read_b128 v[48:51], v8 offset:7168\n"
    "ds_read_b128 v[52:55], v8 offset:8192\n"
    "s_waitcnt vmcnt(0)\n"
    "ds_read_b128 v[56:59], v8 offset:9216\n"
    "ds_read_b128 v[60:63], v8 offset:10240\n"
    "ds_read_b128 v[64:67], v8 offset:11264\n"
    "ds_read_b128 v[68:71], v8 offset:12288\n"
    "ds_read_b128 v[72:75], v8 offset:13312\n"
    "ds_read_b128 v[76:79], v8 offset:14336\n"
    "s_waitcnt lgkmcnt(9)\n"
    "ds_read_b128 v[80:83], v8 offset:15360\n"
    "ds_read_b128 v[84:87], v8 offset:16384\n"
    "ds_read_b128 v[88:91], v8 offset:17408\n"
    "v_add_u32 v14, 0x7f4a7c15, v12\n"
    "v_add_u32 v15, 0x9e3779b9, v13\n"
    "v_add_u32 v12, 0x7f4a7c15, v12\n"
    "v_add_u32 v13, 0x9e3779b9, v13\n"
    "v_mfma_f32_16x16x32_bf16 v[92:95], v[20:23], v[28:31], 0\n"
    "v_mfma_f32_16x16x32_bf16 v[96:99], v[24:27], v[28:31], 0\n"
    "v_mfma_f32_16x16x32_bf16 v[92:95], v[32:35], v[40:43], v[92:95]\n"
    "v_mfma_f32_16x16x32_bf16 v[96:99], v[36:39], v[40:43], v[96:99]\n"
    "s_waitcnt lgkmcnt(9)\n"
    "v_mfma_f32_16x16x32_bf16 v[92:95], v[44:47], v[52:55], v[92:95]\n"
    "v_mfma_f32_16x16x32_bf16 v[96:99], v[48:51], v[52:55], v[96:99]\n"
    "s_waitcnt lgkmcnt(6)\n"
    "v_mfma_f32_16x16x32_bf16 v[92:95], v[56:59], v[64:67], v[92:95]\n"
    "v_mfma_f32_16x16x32_bf16 v[96:99], v[60:63], v[64:67], v[96:99]\n"
    "s_waitcnt lgkmcnt(3)\n"
    "v_mfma_f32_16x16x32_bf16 v[92:95], v[68:71], v[76:79], v[92:95]\n"
    "v_mfma_f32_16x16x32_bf16 v[96:99], v[72:75], v[76:79], v[96:99]\n"
    "s_waitcnt lgkmcnt(0)\n"
    "v_mfma_f32_16x16x32_bf16 v[92:95], v[80:83], v[88:91], v[92:95]\n"
    "v_mfma_f32_16x16x32_bf16 v[96:99], v[84:87], v[88:91], v[96:99]\n"
    "s_nop 6\n"
    "v_cvt_pk_bf16_f32 v92, v92, v93\n"
    "v_cvt_pk_bf16_f32 v93, v94, v95\n"
    "v_cvt_pk_bf16_f32 v94, v96, v97\n"
    "v_cvt_pk_bf16_f32 v95, v98, v99\n"
    "global_store_dwordx4 v9, v[92:95], s[16:17] sc1\n"
    "v_mov_b32 v17, 1\n"
    "ds_write_b32 v16, v17\n"
    "s_waitcnt vmcnt(0)\n"
    "global_store_dwordx4 v10, v[12:15], s[16:17] sc1\n"
    "v_mov_b32 v17, 2\n"
    "ds_write_b32 v16, v17\n"
    "s_waitcnt lgkmcnt(0)\n"
    "s_branch .Le_end\n"
    ".Le_att:\n"
    "s_sub_u32 s7, s7, 4\n"
    "s_bfe_u32 s18, s6, 0x10001\n"
    "s_bfe_u32 s19, s6, 0x30003\n"
    "s_lshl_b32 s20, s18, 3\n"
    "s_or_b32 s20, s20, s19\n"
    "s_mul_i32 s19, s19, 28\n"
    "s_mul_i32 s18, s18, 13\n"
    "s_add_u32 s19, s19, s18\n"
    "s_sub_u32 s19, s19, 12\n"
    "s_cmp_eq_u32 s20, 15\n"
    "s_cselect_b32 s23, 13, 99\n"
    "s_cmp_eq_u32 s20, 0\n"
    "s_cselect_b32 s19, 26, s19\n"
    "s_cselect_b32 s21, -2, 2\n"
    "s_cselect_b32 s22, 1, 218\n"
    "s_cselect_b32 s23, 13, s23\n"
    "s_lshl_b32 s24, s7, 7\n"
    "s_movk_i32 s25, 1311\n"
    "v_mov_b32 v11, s19\n"
    "v_add_u32 v4, s24, v3\n"
    "v_min_u32 v4, 499, v4\n"
    "v_mul_u32_u24 v5, s25, v4\n"
    "v_lshrrev_b32 v5, 16, v5\n"
    "v_mul_u32_u24 v6, 50, v5\n"
    "v_sub_u32 v4, v4, v6\n"
    "v_min_u32 v4, 47, v4\n"
    "v_add_u32 v6, 8, v4\n"
    "v_lshrrev_b32 v6, 5, v6\n"
    "v_lshl_add_u32 v5, v5, 1, v6\n"
    "v_mul_u32_u24 v6, 24, v6\n"
    "v_sub_u32 v4, v4, v6\n"
    "v_mad_i32_i24 v7, v5, s21, v11\n"
    "v_add_u32 v8, -14, v5\n"
    "v_min_u32 v8, 2, v8\n"
    "v_lshl_add_u32 v8, v8, 1, s22\n"
    "v_sub_u32 v9, s23, v5\n"
    "v_ashrrev_i32 v9, 31, v9\n"
    "v_bfi_b32 v7, v9, v8, v7\n"
    "v_mul_u32_u24 v7, 24, v7\n"
    "v_add_lshl_u32 v1, v7, v4, 4\n"
    "v_add_u32 v4, s24, v3\n"
    "v_add_u32 v4, 64, v4\n"
    "v_min_u32 v4, 499, v4\n"
    "v_mul_u32_u24 v5, s25, v4\n"
    "v_lshrrev_b32 v5, 16, v5\n"
    "v_mul_u32_u24 v6, 50, v5\n"
    "v_sub_u32 v4, v4, v6\n"
    "v_min_u32 v4, 47, v4\n"
    "v_add_u32 v6, 8, v4\n"
    "v_lshrrev_b32 v6, 5, v6\n"
    "v_lshl_add_u32 v5, v5, 1, v6\n"
    "v_mul_u32_u24 v6, 24, v6\n"
    "v_sub_u32 v4, v4, v6\n"
    "v_mad_i32_i24 v7, v5, s21, v11\n"
    "v_add_u32 v8, -14, v5\n"
    "v_min_u32 v8, 2, v8\n"
    "v_lshl_add_u32 v8, v8, 1, s22\n"
    "v_sub_u32 v9, s23, v5\n"
    "v_ashrrev_i32 v9, 31, v9\n"
    "v_bfi_b32 v7, v9, v8, v7\n"
    "v_mul_u32_u24 v7, 24, v7\n"
    "v_add_lshl_u32 v2, v7, v4, 4\n"
    "s_bfe_u32 s18, s6, 0x10002\n"
    "s_lshr_b32 s19, s6, 6\n"
    "s_and_b32 s20, s6, 1\n"
    "s_lshl_b32 s20, s20, 2\n"
    "s_add_u32 s19, s19, s20\n"
    "s_and_b32 s20, s19, 1\n"
    "s_mul_i32 s19, s19, 28\n"
    "s_mul_i32 s18, s18, 13\n"
    "s_add_u32 s19, s19, s18\n"
    "s_cmp_eq_u32 s20, 0\n"
    "s_cselect_b32 s18, 26, -12\n"
    "s_add_u32 s19, s19, s18\n"
    "s_cmp_eq_u32 s20, 0\n"
    "s_mov_b32 s21, 172032\n"
    "s_cselect_b32 s21, -172032, s21\n"
    "s_mul_i32 s19, s19, 86016\n"
    "s_lshl_b32 s24, s7, 11\n"
    "s_waitcnt lgkmcnt(0)\n"
    "s_cmp_ge_u32 s3, 144\n"
    "s_cbranch_scc1 .Le_go\n"
    "s_sleep 5\n"
    "s_cmp_ge_u32 s3, 72\n"
    "s_cbranch_scc1 .Le_go\n"
    ".Le_gow:\n"
    "ds_read_b32 v17, v16\n"
    "s_waitcnt lgkmcnt(0)\n"
    "v_readfirstlane_b32 s20, v17\n"
    "s_nop 3\n"
    "s_cmp_lt_u32 s20, 1\n"
    "s_cbranch_scc1 .Le_gow\n"
    ".Le_go:\n"
    "s_mul_i32 s20, s21, 3\n"
    "s_add_u32 s20, s20, s19\n"
    "s_add_u32 m0, s24, 24576\n"
    "s_add_u32 s26, s8, s20\n"
    "s_addc_u32 s27, s9, 0\n"
    "global_load_lds_dwordx4 v1, s[26:27]\n"
    "s_addk_i32 m0, 1024\n"
    "s_nop 0\n"
    "global_load_lds_dwordx4 v2, s[26:27]\n"
    "s_mul_i32 s20, s21, 4\n"
    "s_add_u32 s20, s20, s19\n"
    "s_add_u32 m0, s24, 32768\n"
    "s_add_u32 s26, s8, s20\n"
    "s_addc_u32 s27, s9, 0\n"
    "global_load_lds_dwordx4 v1, s[26:27]\n"
    "s_addk_i32 m0, 1024\n"
    "s_nop 0\n"
    "global_load_lds_dwordx4 v2, s[26:27]\n"
    "s_mul_i32 s20, s21, 0\n"
    "s_add_u32 s20, s20, s19\n"
    "s_add_u32 m0, s24, 0\n"
    "s_add_u32 s26, s8, s20\n"
    "s_addc_u32 s27, s9, 0\n"
    "global_load_lds_dwordx4 v1, s[26:27]\n"
    "s_addk_i32 m0, 1024\n"
    "s_nop 0\n"
    "global_load_lds_dwordx4 v2, s[26:27]\n"
    "s_branch .Le_end\n"
    ".Le_gemm:\n"
    "s_sub_u32 s18, s3, 72\n"
    "s_mul_i32 s21, s7, 256\n"
    "s_add_u32 s18, s18, s21\n"
    "v_mad_u32_u24 v7, v4, s23, v6\n"
    "v_and_b32 v11, 3, v3\n"
    "v_mul_u32_u24 v10, 24, v4\n"
    "v_add_lshl_u32 v10, v10, v11, 4\n"
    "s_lshl_b32 s24, s25, 6\n"
    "s_add_u32 s20, s20, 768\n"
    "s_mov_b32 m0, 114688\n"
    "s_waitcnt lgkmcnt(0)\n"
    "s_cmp_ge_u32 s18, 72\n"
    "s_cbranch_scc1 .Le_end\n"
    "s_setprio 3\n"
    "s_add_u32 s26, s10, s20\n"
    "s_addc_u32 s27, s11, 0\n"
    "s_add_u32 s28, s26, 18432\n"
    "s_addc_u32 s29, s27, 0\n"
    "s_add_u32 s30, s12, s24\n"
    "s_addc_u32 s31, s13, 0\n"
    "s_mov_b32 s18, 6\n"
    ".Le_p2:\n"
    "global_load_lds_dwordx4 v7, s[26:27]\n"
    "s_addk_i32 m0, 1024\n"
    "s_sub_u32 s18, s18, 1\n"
    "global_load_lds_dwordx4 v7, s[28:29]\n"
    "s_addk_i32 m0, 1024\n"
    "v_add_u32 v7, 64, v7\n"
    "global_load_lds_dwordx4 v10, s[30:31]\n"
    "s_addk_i32 m0, 1024\n"
    "v_add_u32 v10, 6144, v10\n"
    "global_load_lds_dwordx4 v10, s[30:31]\n"
    "s_addk_i32 m0, 1024\n"
    "v_add_u32 v10, 6144, v10\n"
    "s_cmp_lg_u32 s18, 0\n"
    "s_cbranch_scc1 .Le_p2\n"
    "s_setprio 0\n"
    ".Le_end:\n"
        :
          "={s[32:39]}"(est),
          "={s[8:15]}"(ka), "={s[16:17]}"(ka4), "={s3}"(pf), "={v1}"(xo0), "={v2}"(xo1), "+{v0}"(tid), "+{s6}"(bid)
        : "{s[4:5]}"(__builtin_amdgcn_kernarg_segment_ptr())
        : "memory", "scc", "s7", "s18", "s19", "s20", "s21", "s22", "s23", "s24", "s25", "s26", "s27", "s28", "s29", "s30", "s31",
          "v3", "v4", "v5", "v6", "v7", "v8", "v9", "v10", "v11", "v12", "v13", "v14", "v15", "v16", "v17", "v18", "v19", "v20", "v21", "v22", "v23", "v24", "v25", "v26", "v27", "v28", "v29", "v30", "v31", "v32", "v33", "v34", "v35", "v36", "v37", "v38", "v39", "v40", "v41", "v42", "v43", "v44", "v45", "v46", "v47", "v48", "v49", "v50", "v51", "v52", "v53", "v54", "v55", "v56", "v57", "v58", "v59", "v60", "v61", "v62", "v63", "v64", "v65", "v66", "v67", "v68", "v69", "v70", "v71", "v72", "v73", "v74", "v75", "v76", "v77", "v78", "v79", "v80", "v81", "v82", "v83", "v84", "v85", "v86", "v87", "v88", "v89", "v90", "v91", "v92", "v93", "v94", "v95", "v96", "v97", "v98", "v99");
    auto mkptr = [](unsigned lo, unsigned hi) LAMBDA_INL { return (char*)(((u64)(unsigned)UNI(hi) << 32) | (unsigned)UNI(lo)); };
    const char* xb = mkptr(ka[0], ka[1]);
    char* ws = mkptr(ka4[0], ka4[1]);
    bid = UNI(bid);
    pf = UNI(pf);
    const int lane = tid & 63;
    unsigned stp = 0;
    STAMP(3);
    const int wave = __builtin_amdgcn_readfirstlane(tid >> 6);
    const int i16 = lane & 15, g = lane >> 4;
    const unsigned ldsbase = (unsigned)(size_t)smem;
    const char* dbg = ws + (1 << 20) + (bid * 8 + wave) * 256;

    const int xcd = bid & 7, idx = bid >> 3;
    const AxisMap rm = axis_map(xcd >> 2, (xcd & 1) * 4 + (idx >> 3), 1);
    const AxisMap cm = axis_map((xcd >> 1) & 1, idx & 7, false);
    const int e = i16 >> 3, wq = min(i16 & 7, 6);

    if (wave < 4) {
        const int w = wave;
        const unsigned l16 = lane * 16;
        const unsigned dl = (i16 & 7) == 7 ? ZOFF : 0u, ds = (i16 & 7) == 7 ? 0x80000000u : 0u, dsp = ((lane >> 2) & 7) == 7 ? 0x80000000u : 0u;
        const u32x2 va = {dl + colad(3 + wq) + 16 * g + e * ROWB, dl + colad(10 + wq) + 16 * g + e * ROWB};
        const int hf = (w >> 1) ^ 1;
        const u32x4 vb = {(unsigned)(e ? -ROWB : 13 * ROWB), ldsbase + LDS_E + 2 * EB + hf * 6144 + l16,
                          ldsbase + LDS_E + 2 * EB + (1 - hf) * 6144 + l16, ldsbase + LDS_E + l16 + w * 1024};
        const int r0 = axis_at(rm, 3 + e);
        const int se = lane >> 5, sw = min((lane >> 2) & 7, 6), sj = lane & 3;
        constexpr int BPU = 6;
        const int r5 = axis_at(rm, (BPU == 5 ? 13 : 15) + se);
        const int rd = BPU == 5 ? axis_at(rm, 15 + se) - r5 : axis_at(rm, 13 + e) - axis_at(rm, 11 + e);
        const u32x4 vc = {ldsbase + LDS_E + l16 + (4 + (w >> 1)) * 1024 + 8 * (w & 1),
                          (unsigned)(r0 * IROWB + axis_at(cm, 3 + 7 * hf + wq) * PIXB + 64 * w + 16 * g + ds),
                          (unsigned)(r0 * IROWB + axis_at(cm, 10 - 7 * hf + wq) * PIXB + 64 * w + 16 * g + ds),
                          BPU == 7 ? (unsigned)((axis_at(rm, 15 + e) - axis_at(rm, 13 + e)) * IROWB)
                                   : (unsigned)(r5 * IROWB + axis_at(cm, 3 + 7 * hf + sw) * PIXB + 64 * w + 16 * sj + dsp)};
        const int fl = min(lane & 31, lane < 32 ? 17 : 23);
        const int fi = fl + ((11 - fl) >> 31 & (36 + (lane < 32 ? 6 * w : 12 * (w & 1)) - 12 * w)) + 12 * w;
        const u32x4 vd = {(unsigned)(r5 * IROWB + axis_at(cm, 10 - 7 * hf + sw) * PIXB + 64 * w + 16 * sj + dsp),
                          (unsigned)(4 * (16 * sj + (lane >> 2))), (unsigned)(rd * IROWB),
                          (unsigned)(WS_FL + ((bid & (NREP - 1)) * 144 + 72 * (lane >> 5) + fi) * FSTR)};
        const u32x4 vp = {ldsbase + LDS_E + i16 * 64 + 16 * (g ^ ((0 - (i16 >> 2)) & 3)),
                          ldsbase + LDS_E + 2048 + (8 * g + ((lane >> 2) & 3)) * 64 + 16 * (lane & 3) + 8 * ((pf / 6) & 1),
                          WS_W + pf * 1024 + l16, (unsigned)(WS_FL + ((lane >> 3) * 144 + pf) * 128 + (lane & 7) * 16)};
        const u32x2 vi = {ldsbase + INVOFF + hf * 64 + 4 * i16, ldsbase + INVOFF + (1 - hf) * 64 + 4 * i16};
        const u32x4 srd = {(unsigned)UNI(ka[6]), (unsigned)UNI(ka[7]) & 0xffffu, (unsigned)(IMW * IROWB), 0x00020000u};
        STAMP(19);
        asm volatile("s_nop 7\n"
    "s_mov_b32 s63, s62\n"
    "v_mov_b32 v26, v17\n"
    "v_mov_b32 v27, v17\n"
    "v_mov_b32 v0, 0\n"
    "s_nop 0\n"
    "v_permlane32_swap_b32 v26, v27\n"
    "v_mov_b32 v17, v27\n"
    "global_load_dwordx2 v[24:25], v0, s[40:41] sc1\n"
    "s_mul_i32 s52, s48, 12288\n"
    "s_mul_i32 s53, s48, 6144\n"
    "s_and_b32 s56, s48, 1\n"
    "s_mul_i32 s56, s56, 12288\n"
    "s_add_u32 s57, s52, 11264\n"
    "s_add_u32 s64, s40, s57\n"
    "s_addc_u32 s65, s41, 0\n"
    "s_add_u32 s57, s52, 17408\n"
    "s_add_u32 s66, s40, s57\n"
    "s_addc_u32 s67, s41, 0\n"
    "s_add_u32 s57, s53, 60416\n"
    "s_add_u32 s68, s40, s57\n"
    "s_addc_u32 s69, s41, 0\n"
    "s_add_u32 s57, s52, 84992\n"
    "s_add_u32 s70, s40, s57\n"
    "s_addc_u32 s71, s41, 0\n"
    "s_add_u32 s57, s52, 91136\n"
    "s_add_u32 s72, s40, s57\n"
    "s_addc_u32 s73, s41, 0\n"
    "s_add_u32 s57, s56, 134144\n"
    "s_add_u32 s74, s40, s57\n"
    "s_addc_u32 s75, s41, 0\n"
    "s_add_u32 s57, s56, 140288\n"
    "s_add_u32 s76, s40, s57\n"
    "s_addc_u32 s77, s41, 0\n"
    "v_add_u32 v4, 0x10000, v2\n"
    "v_add_u32 v5, 0x10000, v3\n"
    "s_mov_b32 s47, 1\n"
    "s_cmp_eq_u32 s49, 0\n"
    "s_cbranch_scc1 .Lg_np\n"
    "s_waitcnt vmcnt(1)\n"
    "ds_read_b64_tr_b16 v[168:169], v115 offset:0\n"
    "ds_read_b64_tr_b16 v[170:171], v115 offset:256\n"
    "ds_read_b128 v[120:123], v114 offset:0\n"
    "ds_read_b128 v[124:127], v114 offset:1024\n"
    "ds_read_b64_tr_b16 v[172:173], v115 offset:4096\n"
    "ds_read_b64_tr_b16 v[174:175], v115 offset:4352\n"
    "ds_read_b128 v[128:131], v114 offset:4096\n"
    "ds_read_b128 v[132:135], v114 offset:5120\n"
    "ds_read_b64_tr_b16 v[176:177], v115 offset:8192\n"
    "ds_read_b64_tr_b16 v[178:179], v115 offset:8448\n"
    "ds_read_b128 v[136:139], v114 offset:8192\n"
    "ds_read_b128 v[140:143], v114 offset:9216\n"
    "ds_read_b64_tr_b16 v[180:181], v115 offset:12288\n"
    "ds_read_b64_tr_b16 v[182:183], v115 offset:12544\n"
    "ds_read_b128 v[144:147], v114 offset:12288\n"
    "s_waitcnt lgkmcnt(9)\n"
    "ds_read_b128 v[148:151], v114 offset:13312\n"
    "ds_read_b64_tr_b16 v[184:185], v115 offset:16384\n"
    "ds_read_b64_tr_b16 v[186:187], v115 offset:16640\n"
    "ds_read_b128 v[152:155], v114 offset:16384\n"
    "ds_read_b128 v[156:159], v114 offset:17408\n"
    "ds_read_b64_tr_b16 v[188:189], v115 offset:20480\n"
    "s_waitcnt lgkmcnt(9)\n"
    "ds_read_b64_tr_b16 v[190:191], v115 offset:20736\n"
    "ds_read_b128 v[160:163], v114 offset:20480\n"
    "ds_read_b128 v[164:167], v114 offset:21504\n"
    "v_mfma_f32_16x16x32_bf16 v[192:195], v[120:123], v[168:171], 0\n"
    "v_mfma_f32_16x16x32_bf16 v[196:199], v[124:127], v[168:171], 0\n"
    "v_mfma_f32_16x16x32_bf16 v[192:195], v[128:131], v[172:175], v[192:195]\n"
    "v_mfma_f32_16x16x32_bf16 v[196:199], v[132:135], v[172:175], v[196:199]\n"
    "v_mfma_f32_16x16x32_bf16 v[192:195], v[136:139], v[176:179], v[192:195]\n"
    "v_mfma_f32_16x16x32_bf16 v[196:199], v[140:143], v[176:179], v[196:199]\n"
    "s_waitcnt lgkmcnt(9)\n"
    "v_mfma_f32_16x16x32_bf16 v[192:195], v[144:147], v[180:183], v[192:195]\n"
    "s_waitcnt lgkmcnt(8)\n"
    "v_mfma_f32_16x16x32_bf16 v[196:199], v[148:151], v[180:183], v[196:199]\n"
    "s_waitcnt lgkmcnt(5)\n"
    "v_mfma_f32_16x16x32_bf16 v[192:195], v[152:155], v[184:187], v[192:195]\n"
    "s_waitcnt lgkmcnt(4)\n"
    "v_mfma_f32_16x16x32_bf16 v[196:199], v[156:159], v[184:187], v[196:199]\n"
    "s_waitcnt lgkmcnt(1)\n"
    "v_mfma_f32_16x16x32_bf16 v[192:195], v[160:163], v[188:191], v[192:195]\n"
    "s_waitcnt lgkmcnt(0)\n"
    "v_mfma_f32_16x16x32_bf16 v[196:199], v[164:167], v[188:191], v[196:199]\n"
    "s_nop 5\n"
    "v_cvt_pk_bf16_f32 v192, v192, v193\n"
    "v_cvt_pk_bf16_f32 v193, v194, v195\n"
    "v_cvt_pk_bf16_f32 v194, v196, v197\n"
    "v_cvt_pk_bf16_f32 v195, v198, v199\n"
    "global_store_dwordx4 v116, v[192:195], s[40:41] sc1\n"
    "s_waitcnt vmcnt(0)\n"
    "v_add_u32 v20, 0x7f4a7c15, v24\n"
    "v_add_u32 v21, 0x9e3779b9, v25\n"
    "v_add_u32 v22, 0x7f4a7c15, v24\n"
    "v_add_u32 v23, 0x9e3779b9, v25\n"
    "global_store_dwordx4 v117, v[20:23], s[40:41] sc1\n"
    "s_mov_b32 s49, 0\n"
    "s_mov_b32 s47, 0\n"
    ".Lg_np:\n"
    "s_lshl_b32 s50, s50, 7\n"
    "s_add_u32 s50, s50, 393216\n"
    "s_cmp_eq_u32 s47, 0\n"
    "s_cbranch_scc1 .Lg_nd\n"
    "s_cmp_eq_u32 s45, 1\n"
    "s_cbranch_scc1 .Lg_nd\n"
    "s_sleep 14\n"
    "s_cmp_eq_u32 s45, 2\n"
    "s_cbranch_scc1 .Lg_nd\n"
    "s_sleep 10\n"
    ".Lg_nd:\n"
    "s_mov_b32 s51, 0x40000\n"
    "s_barrier\n"
    ".Lg_poll0:\n"
    "global_load_dwordx2 v[0:1], v26, s[40:41] sc1\n"
    ".Lg_chk:\n"
    "s_waitcnt vmcnt(0)\n"
    "v_add_u32 v20, 0x7f4a7c15, v24\n"
    "v_add_u32 v21, 0x9e3779b9, v25\n"
    "s_cmp_eq_u32 s49, 0\n"
    "s_cbranch_scc1 .Lg_nf\n"
    "v_mov_b32 v22, v20\n"
    "v_mov_b32 v23, v21\n"
    "global_store_dwordx4 v117, v[20:23], s[40:41] sc1\n"
    "s_mov_b32 s49, 0\n"
    ".Lg_nf:\n"
    "v_cmp_ne_u64 vcc, v[20:21], v[0:1]\n"
    "s_sub_u32 s51, s51, 1\n"
    "s_nop 3\n"
    "s_cmp_eq_u32 vcc_lo, 0\n"
    "s_cbranch_scc1 .Lg_polled0\n"
    "s_sleep 2\n"
    "s_cmp_lg_u32 s51, 0\n"
    "s_cbranch_scc1 .Lg_poll0\n"
    ".Lg_polled0:\n"
    "global_load_dwordx4 v[42:45], v18, s[64:65] offset:-3072 sc1\n"
    "global_load_dwordx4 v[46:49], v18, s[66:67] offset:-3072 sc1\n"
    "global_load_dwordx4 v[50:53], v18, s[68:69] offset:-3072 sc1\n"
    "global_load_dwordx4 v[54:57], v18, s[64:65] offset:-2048 sc1\n"
    "global_load_dwordx4 v[58:61], v18, s[66:67] offset:-2048 sc1\n"
    "global_load_dwordx4 v[62:65], v18, s[68:69] offset:-2048 sc1\n"
    "global_load_dwordx4 v[66:69], v18, s[64:65] offset:-1024 sc1\n"
    "global_load_dwordx4 v[70:73], v18, s[66:67] offset:-1024 sc1\n"
    "global_load_dwordx4 v[74:77], v18, s[68:69] offset:-1024 sc1\n"
    "global_load_dwordx4 v[78:81], v18, s[64:65] offset:0 sc1\n"
    "global_load_dwordx4 v[82:85], v18, s[66:67] offset:0 sc1\n"
    "global_load_dwordx4 v[86:89], v18, s[68:69] offset:0 sc1\n"
    "global_load_dwordx4 v[90:93], v18, s[64:65] offset:1024 sc1\n"
    "global_load_dwordx4 v[94:97], v18, s[66:67] offset:1024 sc1\n"
    "global_load_dwordx4 v[98:101], v18, s[68:69] offset:1024 sc1\n"
    "global_load_dwordx4 v[102:105], v18, s[64:65] offset:2048 sc1\n"
    "global_load_dwordx4 v[106:109], v18, s[66:67] offset:2048 sc1\n"
    "global_load_dwordx4 v[110:113], v18, s[68:69] offset:2048 sc1\n"
    "global_load_dwordx2 v[0:1], v17, s[40:41] sc1\n"
    "ds_read_b128 v[22:25], v2 offset:24576\n"
    "ds_read_b128 v[26:29], v3 offset:24576\n"
    "ds_read_b128 v[30:33], v2 offset:24640\n"
    "ds_read_b128 v[34:37], v3 offset:24640\n"
    "s_waitcnt vmcnt(16)\n"
    "s_waitcnt lgkmcnt(2)\n"
    "v_mfma_f32_16x16x32_bf16 v[210:213], v[42:45], v[22:25], 0\n"
    "ds_read_b128 v[38:41], v2 offset:24704\n"
    "v_mfma_f32_16x16x32_bf16 v[214:217], v[46:49], v[22:25], 0\n"
    "ds_read_b128 v[250:253], v3 offset:24704\n"
    "v_mfma_f32_16x16x32_bf16 v[218:221], v[50:53], v[22:25], 0\n"
    "ds_read_b128 v[22:25], v2 offset:24768\n"
    "v_mfma_f32_16x16x32_bf16 v[222:225], v[42:45], v[26:29], 0\n"
    "v_mfma_f32_16x16x32_bf16 v[226:229], v[46:49], v[26:29], 0\n"
    "v_mfma_f32_16x16x32_bf16 v[230:233], v[50:53], v[26:29], 0\n"
    "ds_read_b128 v[26:29], v3 offset:24768\n"
    "s_waitcnt vmcnt(13)\n"
    "s_waitcnt lgkmcnt(4)\n"
    "v_mfma_f32_16x16x32_bf16 v[210:213], v[54:57], v[30:33], v[210:213]\n"
    "v_mfma_f32_16x16x32_bf16 v[214:217], v[58:61], v[30:33], v[214:217]\n"
    "v_mfma_f32_16x16x32_bf16 v[218:221], v[62:65], v[30:33], v[218:221]\n"
    "ds_read_b128 v[30:33], v2 offset:24832\n"
    "v_mfma_f32_16x16x32_bf16 v[222:225], v[54:57], v[34:37], v[222:225]\n"
    "v_mfma_f32_16x16x32_bf16 v[226:229], v[58:61], v[34:37], v[226:229]\n"
    "v_mfma_f32_16x16x32_bf16 v[230:233], v[62:65], v[34:37], v[230:233]\n"
    "ds_read_b128 v[34:37], v3 offset:24832\n"
    "s_waitcnt vmcnt(10)\n"
    "s_waitcnt lgkmcnt(4)\n"
    "v_mfma_f32_16x16x32_bf16 v[210:213], v[66:69], v[38:41], v[210:213]\n"
    "v_mfma_f32_16x16x32_bf16 v[214:217], v[70:73], v[38:41], v[214:217]\n"
    "v_mfma_f32_16x16x32_bf16 v[218:221], v[74:77], v[38:41], v[218:221]\n"
    "ds_read_b128 v[38:41], v2 offset:24896\n"
    "v_mfma_f32_16x16x32_bf16 v[222:225], v[66:69], v[250:253], v[222:225]\n"
    "v_mfma_f32_16x16x32_bf16 v[226:229], v[70:73], v[250:253], v[226:229]\n"
    "v_mfma_f32_16x16x32_bf16 v[230:233], v[74:77], v[250:253], v[230:233]\n"
    "ds_read_b128 v[250:253], v3 offset:24896\n"
    "s_waitcnt vmcnt(7)\n"
    "s_waitcnt lgkmcnt(4)\n"
    "v_mfma_f32_16x16x32_bf16 v[210:213], v[78:81], v[22:25], v[210:213]\n"
    "v_mfma_f32_16x16x32_bf16 v[214:217], v[82:85], v[22:25], v[214:217]\n"
    "v_mfma_f32_16x16x32_bf16 v[218:221], v[86:89], v[22:25], v[218:221]\n"
    "v_mfma_f32_16x16x32_bf16 v[222:225], v[78:81], v[26:29], v[222:225]\n"
    "v_mfma_f32_16x16x32_bf16 v[226:229], v[82:85], v[26:29], v[226:229]\n"
    "v_mfma_f32_16x16x32_bf16 v[230:233], v[86:89], v[26:29], v[230:233]\n"
    "s_waitcnt vmcnt(4)\n"
    "s_waitcnt lgkmcnt(2)\n"
    "v_mfma_f32_16x16x32_bf16 v[210:213], v[90:93], v[30:33], v[210:213]\n"
    "v_mfma_f32_16x16x32_bf16 v[214:217], v[94:97], v[30:33], v[214:217]\n"
    "v_mfma_f32_16x16x32_bf16 v[218:221], v[98:101], v[30:33], v[218:221]\n"
    "v_mfma_f32_16x16x32_bf16 v[222:225], v[90:93], v[34:37], v[222:225]\n"
    "v_mfma_f32_16x16x32_bf16 v[226:229], v[94:97], v[34:37], v[226:229]\n"
    "v_mfma_f32_16x16x32_bf16 v[230:233], v[98:101], v[34:37], v[230:233]\n"
    "s_waitcnt vmcnt(1)\n"
    "s_waitcnt lgkmcnt(0)\n"
    "v_mfma_f32_16x16x32_bf16 v[210:213], v[102:105], v[38:41], v[210:213]\n"
    "v_mfma_f32_16x16x32_bf16 v[214:217], v[106:109], v[38:41], v[214:217]\n"
    "v_mfma_f32_16x16x32_bf16 v[218:221], v[110:113], v[38:41], v[218:221]\n"
    "v_mfma_f32_16x16x32_bf16 v[222:225], v[102:105], v[250:253], v[222:225]\n"
    "v_mfma_f32_16x16x32_bf16 v[226:229], v[106:109], v[250:253], v[226:229]\n"
    "v_mfma_f32_16x16x32_bf16 v[230:233], v[110:113], v[250:253], v[230:233]\n"
    "s_nop 2\n"
    "v_cvt_pk_bf16_f32 v210, v210, v211\n"
    "v_cvt_pk_bf16_f32 v211, v212, v213\n"
    "v_cvt_pk_bf16_f32 v212, v214, v215\n"
    "v_cvt_pk_bf16_f32 v213, v216, v217\n"
    "v_cvt_pk_bf16_f32 v218, v218, v219\n"
    "v_cvt_pk_bf16_f32 v219, v220, v221\n"
    "ds_write_b128 v9, v[210:213] offset:0\n"
    "ds_write_b64 v10, v[218:219] offset:0\n"
    "v_cvt_pk_bf16_f32 v222, v222, v223\n"
    "v_cvt_pk_bf16_f32 v223, v224, v225\n"
    "v_cvt_pk_bf16_f32 v224, v226, v227\n"
    "v_cvt_pk_bf16_f32 v225, v228, v229\n"
    "v_cvt_pk_bf16_f32 v230, v230, v231\n"
    "v_cvt_pk_bf16_f32 v231, v232, v233\n"
    "ds_write_b128 v9, v[222:225] offset:6144\n"
    "ds_write_b64 v10, v[230:231] offset:6144\n"
    "s_waitcnt lgkmcnt(0)\n"
    "s_barrier\n"
    "s_waitcnt vmcnt(0)\n"
    "v_cmp_ne_u64 vcc, v[20:21], v[0:1]\n"
    "s_nop 4\n"
    "s_cmp_eq_u32 vcc_hi, 0\n"
    "s_cbranch_scc1 .Lg_polled1\n"
    "s_mov_b32 s51, 0x40000\n"
    ".Lg_poll1:\n"
    "global_load_dwordx2 v[0:1], v17, s[40:41] sc1\n"
    "s_waitcnt vmcnt(0)\n"
    "v_cmp_ne_u64 vcc, v[20:21], v[0:1]\n"
    "s_sub_u32 s51, s51, 1\n"
    "s_nop 3\n"
    "s_cmp_eq_u32 vcc_hi, 0\n"
    "s_cbranch_scc1 .Lg_polled1\n"
    "s_sleep 2\n"
    "s_cmp_lg_u32 s51, 0\n"
    "s_cbranch_scc1 .Lg_poll1\n"
    ".Lg_polled1:\n"
    "ds_read_b128 v[22:25], v2 offset:40960\n"
    "ds_read_b128 v[26:29], v2 offset:41024\n"
    "ds_read_b128 v[30:33], v2 offset:41088\n"
    "ds_read_b128 v[34:37], v2 offset:41152\n"
    "s_waitcnt lgkmcnt(2)\n"
    "v_mfma_f32_16x16x32_bf16 v[234:237], v[42:45], v[22:25], 0\n"
    "ds_read_b128 v[38:41], v2 offset:41216\n"
    "global_load_dwordx4 v[114:117], v18, s[70:71] offset:-3072 sc1\n"
    "v_mfma_f32_16x16x32_bf16 v[238:241], v[46:49], v[22:25], 0\n"
    "ds_read_b128 v[250:253], v2 offset:41280\n"
    "v_mfma_f32_16x16x32_bf16 v[242:245], v[50:53], v[22:25], 0\n"
    "ds_read_b128 v[22:25], v3 offset:40960\n"
    "global_load_dwordx4 v[118:121], v18, s[72:73] offset:-3072 sc1\n"
    "v_mfma_f32_16x16x32_bf16 v[234:237], v[54:57], v[26:29], v[234:237]\n"
    "v_mfma_f32_16x16x32_bf16 v[238:241], v[58:61], v[26:29], v[238:241]\n"
    "v_mfma_f32_16x16x32_bf16 v[242:245], v[62:65], v[26:29], v[242:245]\n"
    "ds_read_b128 v[26:29], v3 offset:41024\n"
    "s_waitcnt lgkmcnt(4)\n"
    "v_mfma_f32_16x16x32_bf16 v[234:237], v[66:69], v[30:33], v[234:237]\n"
    "global_load_dwordx4 v[122:125], v18, s[74:75] offset:-3072 sc1\n"
    "v_mfma_f32_16x16x32_bf16 v[238:241], v[70:73], v[30:33], v[238:241]\n"
    "v_mfma_f32_16x16x32_bf16 v[242:245], v[74:77], v[30:33], v[242:245]\n"
    "ds_read_b128 v[30:33], v3 offset:41088\n"
    "v_mfma_f32_16x16x32_bf16 v[234:237], v[78:81], v[34:37], v[234:237]\n"
    "global_load_dwordx4 v[126:129], v18, s[76:77] offset:-3072 sc1\n"
    "v_mfma_f32_16x16x32_bf16 v[238:241], v[82:85], v[34:37], v[238:241]\n"
    "v_mfma_f32_16x16x32_bf16 v[242:245], v[86:89], v[34:37], v[242:245]\n"
    "ds_read_b128 v[34:37], v3 offset:41152\n"
    "s_waitcnt lgkmcnt(4)\n"
    "v_mfma_f32_16x16x32_bf16 v[234:237], v[90:93], v[38:41], v[234:237]\n"
    "global_load_dwordx4 v[130:133], v18, s[70:71] offset:-2048 sc1\n"
    "v_mfma_f32_16x16x32_bf16 v[238:241], v[94:97], v[38:41], v[238:241]\n"
    "v_mfma_f32_16x16x32_bf16 v[242:245], v[98:101], v[38:41], v[242:245]\n"
    "ds_read_b128 v[38:41], v3 offset:41216\n"
    "v_mfma_f32_16x16x32_bf16 v[234:237], v[102:105], v[250:253], v[234:237]\n"
    "global_load_dwordx4 v[134:137], v18, s[72:73] offset:-2048 sc1\n"
    "v_mfma_f32_16x16x32_bf16 v[238:241], v[106:109], v[250:253], v[238:241]\n"
    "v_mfma_f32_16x16x32_bf16 v[242:245], v[110:113], v[250:253], v[242:245]\n"
    "ds_read_b128 v[250:253], v3 offset:41280\n"
    "s_waitcnt lgkmcnt(4)\n"
    "v_mfma_f32_16x16x32_bf16 v[246:249], v[42:45], v[22:25], 0\n"
    "global_load_dwordx4 v[138:141], v18, s[74:75] offset:-2048 sc1\n"
    "v_mfma_f32_16x16x32_bf16 v[210:213], v[46:49], v[22:25], 0\n"
    "v_cvt_pk_bf16_f32 v234, v234, v235\n"
    "v_cvt_pk_bf16_f32 v235, v236, v237\n"
    "v_mfma_f32_16x16x32_bf16 v[214:217], v[50:53], v[22:25], 0\n"
    "v_cvt_pk_bf16_f32 v236, v238, v239\n"
    "v_mfma_f32_16x16x32_bf16 v[246:249], v[54:57], v[26:29], v[246:249]\n"
    "v_cvt_pk_bf16_f32 v237, v240, v241\n"
    "v_cvt_pk_bf16_f32 v242, v242, v243\n"
    "v_mfma_f32_16x16x32_bf16 v[210:213], v[58:61], v[26:29], v[210:213]\n"
    "v_cvt_pk_bf16_f32 v243, v244, v245\n"
    "v_mfma_f32_16x16x32_bf16 v[214:217], v[62:65], v[26:29], v[214:217]\n"
    "s_waitcnt lgkmcnt(2)\n"
    "v_mfma_f32_16x16x32_bf16 v[246:249], v[66:69], v[30:33], v[246:249]\n"
    "ds_write_b128 v9, v[234:237] offset:12288\n"
    "v_mfma_f32_16x16x32_bf16 v[210:213], v[70:73], v[30:33], v[210:213]\n"
    "ds_write_b64 v10, v[242:243] offset:12288\n"
    "v_mfma_f32_16x16x32_bf16 v[214:217], v[74:77], v[30:33], v[214:217]\n"
    "global_load_dwordx4 v[142:145], v18, s[76:77] offset:-2048 sc1\n"
    "v_mfma_f32_16x16x32_bf16 v[246:249], v[78:81], v[34:37], v[246:249]\n"
    "v_mfma_f32_16x16x32_bf16 v[210:213], v[82:85], v[34:37], v[210:213]\n"
    "v_mfma_f32_16x16x32_bf16 v[214:217], v[86:89], v[34:37], v[214:217]\n"
    "s_waitcnt lgkmcnt(2)\n"
    "v_mfma_f32_16x16x32_bf16 v[246:249], v[90:93], v[38:41], v[246:249]\n"
    "global_load_dwordx4 v[146:149], v18, s[70:71] offset:-1024 sc1\n"
    "v_mfma_f32_16x16x32_bf16 v[210:213], v[94:97], v[38:41], v[210:213]\n"
    "v_mfma_f32_16x16x32_bf16 v[214:217], v[98:101], v[38:41], v[214:217]\n"
    "v_mfma_f32_16x16x32_bf16 v[246:249], v[102:105], v[250:253], v[246:249]\n"
    "v_mfma_f32_16x16x32_bf16 v[210:213], v[106:109], v[250:253], v[210:213]\n"
    "global_load_dwordx4 v[150:153], v18, s[72:73] offset:-1024 sc1\n"
    "v_mfma_f32_16x16x32_bf16 v[214:217], v[110:113], v[250:253], v[214:217]\n"
    "s_nop 4\n"
    "v_cvt_pk_bf16_f32 v246, v246, v247\n"
    "v_cvt_pk_bf16_f32 v247, v248, v249\n"
    "v_cvt_pk_bf16_f32 v248, v210, v211\n"
    "v_cvt_pk_bf16_f32 v249, v212, v213\n"
    "v_cvt_pk_bf16_f32 v214, v214, v215\n"
    "v_cvt_pk_bf16_f32 v215, v216, v217\n"
    "ds_write_b128 v9, v[246:249] offset:18432\n"
    "ds_write_b64 v10, v[214:215] offset:18432\n"
    "s_waitcnt lgkmcnt(0)\n"
    "s_barrier\n"
    "s_cmp_lg_u32 s48, 0\n"
    "s_cbranch_scc1 .Lg_na\n"
    "v_bfe_u32 v17, v18, 4, 4\n"
    "v_lshl_add_u32 v17, v17, 3, s50\n"
    "global_store_dwordx2 v17, v[20:21], s[40:41] sc1\n"
    ".Lg_na:\n"
    "ds_read_b128 v[22:25], v2 offset:57344\n"
    "ds_read_b128 v[26:29], v2 offset:57408\n"
    "ds_read_b128 v[30:33], v2 offset:57472\n"
    "ds_read_b128 v[34:37], v2 offset:57536\n"
    "s_waitcnt lgkmcnt(2)\n"
    "v_mfma_f32_16x16x32_bf16 v[218:221], v[42:45], v[22:25], 0\n"
    "ds_read_b128 v[38:41], v2 offset:57600\n"
    "global_load_dwordx4 v[154:157], v18, s[74:75] offset:-1024 sc1\n"
    "v_mfma_f32_16x16x32_bf16 v[222:225], v[46:49], v[22:25], 0\n"
    "ds_read_b128 v[250:253], v2 offset:57664\n"
    "v_mfma_f32_16x16x32_bf16 v[226:229], v[50:53], v[22:25], 0\n"
    "ds_read_b128 v[22:25], v3 offset:57344\n"
    "global_load_dwordx4 v[158:161], v18, s[76:77] offset:-1024 sc1\n"
    "v_mfma_f32_16x16x32_bf16 v[218:221], v[54:57], v[26:29], v[218:221]\n"
    "v_mfma_f32_16x16x32_bf16 v[222:225], v[58:61], v[26:29], v[222:225]\n"
    "v_mfma_f32_16x16x32_bf16 v[226:229], v[62:65], v[26:29], v[226:229]\n"
    "ds_read_b128 v[26:29], v3 offset:57408\n"
    "s_waitcnt lgkmcnt(4)\n"
    "v_mfma_f32_16x16x32_bf16 v[218:221], v[66:69], v[30:33], v[218:221]\n"
    "global_load_dwordx4 v[162:165], v18, s[70:71] offset:0 sc1\n"
    "v_mfma_f32_16x16x32_bf16 v[222:225], v[70:73], v[30:33], v[222:225]\n"
    "v_mfma_f32_16x16x32_bf16 v[226:229], v[74:77], v[30:33], v[226:229]\n"
    "ds_read_b128 v[30:33], v3 offset:57472\n"
    "v_mfma_f32_16x16x32_bf16 v[218:221], v[78:81], v[34:37], v[218:221]\n"
    "global_load_dwordx4 v[166:169], v18, s[72:73] offset:0 sc1\n"
    "v_mfma_f32_16x16x32_bf16 v[222:225], v[82:85], v[34:37], v[222:225]\n"
    "v_mfma_f32_16x16x32_bf16 v[226:229], v[86:89], v[34:37], v[226:229]\n"
    "ds_read_b128 v[34:37], v3 offset:57536\n"
    "s_waitcnt lgkmcnt(4)\n"
    "v_mfma_f32_16x16x32_bf16 v[218:221], v[90:93], v[38:41], v[218:221]\n"
    "global_load_dwordx4 v[170:173], v18, s[74:75] offset:0 sc1\n"
    "v_mfma_f32_16x16x32_bf16 v[222:225], v[94:97], v[38:41], v[222:225]\n"
    "v_mfma_f32_16x16x32_bf16 v[226:229], v[98:101], v[38:41], v[226:229]\n"
    "ds_read_b128 v[38:41], v3 offset:57600\n"
    "v_mfma_f32_16x16x32_bf16 v[218:221], v[102:105], v[250:253], v[218:221]\n"
    "global_load_dwordx4 v[174:177], v18, s[76:77] offset:0 sc1\n"
    "v_mfma_f32_16x16x32_bf16 v[222:225], v[106:109], v[250:253], v[222:225]\n"
    "v_mfma_f32_16x16x32_bf16 v[226:229], v[110:113], v[250:253], v[226:229]\n"
    "ds_read_b128 v[250:253], v3 offset:57664\n"
    "s_waitcnt lgkmcnt(4)\n"
    "v_mfma_f32_16x16x32_bf16 v[230:233], v[42:45], v[22:25], 0\n"
    "global_load_dwordx4 v[178:181], v18, s[70:71] offset:1024 sc1\n"
    "v_mfma_f32_16x16x32_bf16 v[234:237], v[46:49], v[22:25], 0\n"
    "v_cvt_pk_bf16_f32 v218, v218, v219\n"
    "v_cvt_pk_bf16_f32 v219, v220, v221\n"
    "v_mfma_f32_16x16x32_bf16 v[238:241], v[50:53], v[22:25], 0\n"
    "v_cvt_pk_bf16_f32 v220, v222, v223\n"
    "v_mfma_f32_16x16x32_bf16 v[230:233], v[54:57], v[26:29], v[230:233]\n"
    "v_cvt_pk_bf16_f32 v221, v224, v225\n"
    "v_cvt_pk_bf16_f32 v226, v226, v227\n"
    "v_mfma_f32_16x16x32_bf16 v[234:237], v[58:61], v[26:29], v[234:237]\n"
    "v_cvt_pk_bf16_f32 v227, v228, v229\n"
    "v_mfma_f32_16x16x32_bf16 v[238:241], v[62:65], v[26:29], v[238:241]\n"
    "s_waitcnt lgkmcnt(2)\n"
    "v_mfma_f32_16x16x32_bf16 v[230:233], v[66:69], v[30:33], v[230:233]\n"
    "ds_write_b128 v9, v[218:221] offset:0\n"
    "v_mfma_f32_16x16x32_bf16 v[234:237], v[70:73], v[30:33], v[234:237]\n"
    "ds_write_b64 v10, v[226:227] offset:0\n"
    "v_mfma_f32_16x16x32_bf16 v[238:241], v[74:77], v[30:33], v[238:241]\n"
    "global_load_dwordx4 v[182:185], v18, s[72:73] offset:1024 sc1\n"
    "v_mfma_f32_16x16x32_bf16 v[230:233], v[78:81], v[34:37], v[230:233]\n"
    "v_mfma_f32_16x16x32_bf16 v[234:237], v[82:85], v[34:37], v[234:237]\n"
    "v_mfma_f32_16x16x32_bf16 v[238:241], v[86:89], v[34:37], v[238:241]\n"
    "s_waitcnt lgkmcnt(2)\n"
    "v_mfma_f32_16x16x32_bf16 v[230:233], v[90:93], v[38:41], v[230:233]\n"
    "global_load_dwordx4 v[186:189], v18, s[74:75] offset:1024 sc1\n"
    "v_mfma_f32_16x16x32_bf16 v[234:237], v[94:97], v[38:41], v[234:237]\n"
    "v_mfma_f32_16x16x32_bf16 v[238:241], v[98:101], v[38:41], v[238:241]\n"
    "v_mfma_f32_16x16x32_bf16 v[230:233], v[102:105], v[250:253], v[230:233]\n"
    "v_mfma_f32_16x16x32_bf16 v[234:237], v[106:109], v[250:253], v[234:237]\n"
    "global_load_dwordx4 v[190:193], v18, s[76:77] offset:1024 sc1\n"
    "v_mfma_f32_16x16x32_bf16 v[238:241], v[110:113], v[250:253], v[238:241]\n"
    "s_nop 4\n"
    "v_cvt_pk_bf16_f32 v230, v230, v231\n"
    "v_cvt_pk_bf16_f32 v231, v232, v233\n"
    "v_cvt_pk_bf16_f32 v232, v234, v235\n"
    "v_cvt_pk_bf16_f32 v233, v236, v237\n"
    "v_cvt_pk_bf16_f32 v238, v238, v239\n"
    "v_cvt_pk_bf16_f32 v239, v240, v241\n"
    "ds_write_b128 v9, v[230:233] offset:6144\n"
    "ds_write_b64 v10, v[238:239] offset:6144\n"
    "s_waitcnt lgkmcnt(0)\n"
    "s_barrier\n"
    "ds_read_b128 v[22:25], v4 offset:8192\n"
    "ds_read_b128 v[26:29], v4 offset:8256\n"
    "ds_read_b128 v[30:33], v4 offset:8320\n"
    "ds_read_b128 v[34:37], v4 offset:8384\n"
    "s_waitcnt lgkmcnt(2)\n"
    "v_mfma_f32_16x16x32_bf16 v[242:245], v[42:45], v[22:25], 0\n"
    "ds_read_b128 v[38:41], v4 offset:8448\n"
    "global_load_dwordx4 v[194:197], v18, s[70:71] offset:2048 sc1\n"
    "v_mfma_f32_16x16x32_bf16 v[246:249], v[46:49], v[22:25], 0\n"
    "ds_read_b128 v[250:253], v4 offset:8512\n"
    "v_mfma_f32_16x16x32_bf16 v[210:213], v[50:53], v[22:25], 0\n"
    "ds_read_b128 v[22:25], v5 offset:8192\n"
    "global_load_dwordx4 v[198:201], v18, s[72:73] offset:2048 sc1\n"
    "v_mfma_f32_16x16x32_bf16 v[242:245], v[54:57], v[26:29], v[242:245]\n"
    "v_mfma_f32_16x16x32_bf16 v[246:249], v[58:61], v[26:29], v[246:249]\n"
    "v_mfma_f32_16x16x32_bf16 v[210:213], v[62:65], v[26:29], v[210:213]\n"
    "ds_read_b128 v[26:29], v5 offset:8256\n"
    "s_waitcnt lgkmcnt(4)\n"
    "v_mfma_f32_16x16x32_bf16 v[242:245], v[66:69], v[30:33], v[242:245]\n"
    "global_load_dwordx4 v[202:205], v18, s[74:75] offset:2048 sc1\n"
    "v_mfma_f32_16x16x32_bf16 v[246:249], v[70:73], v[30:33], v[246:249]\n"
    "v_mfma_f32_16x16x32_bf16 v[210:213], v[74:77], v[30:33], v[210:213]\n"
    "ds_read_b128 v[30:33], v5 offset:8320\n"
    "v_mfma_f32_16x16x32_bf16 v[242:245], v[78:81], v[34:37], v[242:245]\n"
    "global_load_dwordx4 v[206:209], v18, s[76:77] offset:2048 sc1\n"
    "v_mfma_f32_16x16x32_bf16 v[246:249], v[82:85], v[34:37], v[246:249]\n"
    "v_mfma_f32_16x16x32_bf16 v[210:213], v[86:89], v[34:37], v[210:213]\n"
    "ds_read_b128 v[34:37], v5 offset:8384\n"
    "s_waitcnt lgkmcnt(4)\n"
    "v_mfma_f32_16x16x32_bf16 v[242:245], v[90:93], v[38:41], v[242:245]\n"
    "v_mfma_f32_16x16x32_bf16 v[246:249], v[94:97], v[38:41], v[246:249]\n"
    "v_mfma_f32_16x16x32_bf16 v[210:213], v[98:101], v[38:41], v[210:213]\n"
    "ds_read_b128 v[38:41], v5 offset:8448\n"
    "v_mfma_f32_16x16x32_bf16 v[242:245], v[102:105], v[250:253], v[242:245]\n"
    "v_mfma_f32_16x16x32_bf16 v[246:249], v[106:109], v[250:253], v[246:249]\n"
    "v_mfma_f32_16x16x32_bf16 v[210:213], v[110:113], v[250:253], v[210:213]\n"
    "ds_read_b128 v[250:253], v5 offset:8512\n"
    "s_waitcnt lgkmcnt(4)\n"
    "v_mfma_f32_16x16x32_bf16 v[214:217], v[42:45], v[22:25], 0\n"
    "v_mfma_f32_16x16x32_bf16 v[218:221], v[46:49], v[22:25], 0\n"
    "v_mfma_f32_16x16x32_bf16 v[222:225], v[50:53], v[22:25], 0\n"
    "ds_read_b128 v[22:25], v7 offset:0\n"
    "v_cvt_pk_bf16_f32 v242, v242, v243\n"
    "v_mfma_f32_16x16x32_bf16 v[214:217], v[54:57], v[26:29], v[214:217]\n"
    "v_cvt_pk_bf16_f32 v243, v244, v245\n"
    "v_cvt_pk_bf16_f32 v244, v246, v247\n"
    "v_mfma_f32_16x16x32_bf16 v[218:221], v[58:61], v[26:29], v[218:221]\n"
    "v_cvt_pk_bf16_f32 v245, v248, v249\n"
    "v_cvt_pk_bf16_f32 v210, v210, v211\n"
    "v_mfma_f32_16x16x32_bf16 v[222:225], v[62:65], v[26:29], v[222:225]\n"
    "ds_read_b128 v[26:29], v7 offset:1024\n"
    "s_waitcnt lgkmcnt(4)\n"
    "v_mfma_f32_16x16x32_bf16 v[214:217], v[66:69], v[30:33], v[214:217]\n"
    "v_cvt_pk_bf16_f32 v211, v212, v213\n"
    "v_mfma_f32_16x16x32_bf16 v[218:221], v[70:73], v[30:33], v[218:221]\n"
    "ds_write_b128 v9, v[242:245] offset:12288\n"
    "v_mfma_f32_16x16x32_bf16 v[222:225], v[74:77], v[30:33], v[222:225]\n"
    "ds_read_b128 v[30:33], v7 offset:2048\n"
    "v_mfma_f32_16x16x32_bf16 v[214:217], v[78:81], v[34:37], v[214:217]\n"
    "ds_write_b64 v10, v[210:211] offset:12288\n"
    "v_mfma_f32_16x16x32_bf16 v[218:221], v[82:85], v[34:37], v[218:221]\n"
    "ds_read_b32 v0, v254 offset:0\n"
    "v_mfma_f32_16x16x32_bf16 v[222:225], v[86:89], v[34:37], v[222:225]\n"
    "ds_read_b128 v[34:37], v7 offset:3072\n"
    "s_waitcnt lgkmcnt(7)\n"
    "v_mfma_f32_16x16x32_bf16 v[214:217], v[90:93], v[38:41], v[214:217]\n"
    "v_mfma_f32_16x16x32_bf16 v[218:221], v[94:97], v[38:41], v[218:221]\n"
    "v_mfma_f32_16x16x32_bf16 v[222:225], v[98:101], v[38:41], v[222:225]\n"
    "ds_read_b128 v[38:41], v7 offset:4096\n"
    "v_mfma_f32_16x16x32_bf16 v[214:217], v[102:105], v[250:253], v[214:217]\n"
    "v_mfma_f32_16x16x32_bf16 v[218:221], v[106:109], v[250:253], v[218:221]\n"
    "v_mfma_f32_16x16x32_bf16 v[222:225], v[110:113], v[250:253], v[222:225]\n"
    "ds_read_b128 v[250:253], v7 offset:5120\n"
    "s_waitcnt vmcnt(22)\n"
    "s_waitcnt lgkmcnt(7)\n"
    "v_mfma_f32_16x16x32_bf16 v[226:229], v[114:117], v[22:25], 0\n"
    "v_mfma_f32_16x16x32_bf16 v[230:233], v[118:121], v[22:25], 0\n"
    "ds_read_b128 v[22:25], v8 offset:0\n"
    "s_waitcnt vmcnt(18)\n"
    "v_cvt_pk_bf16_f32 v214, v214, v215\n"
    "v_mfma_f32_16x16x32_bf16 v[226:229], v[130:133], v[26:29], v[226:229]\n"
    "v_cvt_pk_bf16_f32 v215, v216, v217\n"
    "v_cvt_pk_bf16_f32 v216, v218, v219\n"
    "v_mfma_f32_16x16x32_bf16 v[230:233], v[134:137], v[26:29], v[230:233]\n"
    "ds_read_b128 v[26:29], v8 offset:1024\n"
    "s_waitcnt vmcnt(14)\n"
    "s_waitcnt lgkmcnt(4)\n"
    "v_mfma_f32_16x16x32_bf16 v[226:229], v[146:149], v[30:33], v[226:229]\n"
    "v_cvt_pk_bf16_f32 v217, v220, v221\n"
    "v_cvt_pk_bf16_f32 v222, v222, v223\n"
    "v_mfma_f32_16x16x32_bf16 v[230:233], v[150:153], v[30:33], v[230:233]\n"
    "ds_read_b128 v[30:33], v8 offset:2048\n"
    "s_waitcnt vmcnt(10)\n"
    "v_cvt_pk_bf16_f32 v223, v224, v225\n"
    "v_mfma_f32_16x16x32_bf16 v[226:229], v[162:165], v[34:37], v[226:229]\n"
    "ds_write_b128 v9, v[214:217] offset:18432\n"
    "v_mfma_f32_16x16x32_bf16 v[230:233], v[166:169], v[34:37], v[230:233]\n"
    "ds_read_b128 v[34:37], v8 offset:3072\n"
    "s_waitcnt vmcnt(6)\n"
    "s_waitcnt lgkmcnt(5)\n"
    "v_mfma_f32_16x16x32_bf16 v[226:229], v[178:181], v[38:41], v[226:229]\n"
    "ds_write_b64 v10, v[222:223] offset:18432\n"
    "v_mfma_f32_16x16x32_bf16 v[230:233], v[182:185], v[38:41], v[230:233]\n"
    "ds_read_b128 v[38:41], v8 offset:4096\n"
    "s_waitcnt vmcnt(2)\n"
    "ds_read_b32 v1, v255 offset:0\n"
    "v_mfma_f32_16x16x32_bf16 v[226:229], v[194:197], v[250:253], v[226:229]\n"
    "v_mfma_f32_16x16x32_bf16 v[230:233], v[198:201], v[250:253], v[230:233]\n"
    "ds_read_b128 v[250:253], v8 offset:5120\n"
    "s_waitcnt lgkmcnt(7)\n"
    "v_mfma_f32_16x16x32_bf16 v[234:237], v[114:117], v[22:25], 0\n"
    "v_mfma_f32_16x16x32_bf16 v[238:241], v[118:121], v[22:25], 0\n"
    "v_mfma_f32_16x16x32_bf16 v[242:245], v[122:125], v[22:25], 0\n"
    "v_mfma_f32_16x16x32_bf16 v[246:249], v[126:129], v[22:25], 0\n"
    "ds_read_b128 v[22:25], v4 offset:24576\n"
    "v_mul_f32 v226, v226, v0\n"
    "v_mfma_f32_16x16x32_bf16 v[234:237], v[130:133], v[26:29], v[234:237]\n"
    "v_mul_f32 v227, v227, v0\n"
    "v_mul_f32 v228, v228, v0\n"
    "v_mfma_f32_16x16x32_bf16 v[238:241], v[134:137], v[26:29], v[238:241]\n"
    "v_mul_f32 v229, v229, v0\n"
    "v_mul_f32 v230, v230, v0\n"
    "v_mfma_f32_16x16x32_bf16 v[242:245], v[138:141], v[26:29], v[242:245]\n"
    "v_mul_f32 v231, v231, v0\n"
    "v_mul_f32 v232, v232, v0\n"
    "v_mfma_f32_16x16x32_bf16 v[246:249], v[142:145], v[26:29], v[246:249]\n"
    "s_waitcnt lgkmcnt(1)\n"
    "s_barrier\n"
    "ds_read_b128 v[26:29], v4 offset:24640\n"
    "v_mfma_f32_16x16x32_bf16 v[234:237], v[146:149], v[30:33], v[234:237]\n"
    "v_mul_f32 v233, v233, v0\n"
    "v_cvt_pk_bf16_f32 v226, v226, v227\n"
    "v_mfma_f32_16x16x32_bf16 v[238:241], v[150:153], v[30:33], v[238:241]\n"
    "v_cvt_pk_bf16_f32 v227, v228, v229\n"
    "v_cvt_pk_bf16_f32 v228, v230, v231\n"
    "v_mfma_f32_16x16x32_bf16 v[242:245], v[154:157], v[30:33], v[242:245]\n"
    "v_cvt_pk_bf16_f32 v229, v232, v233\n"
    "v_mfma_f32_16x16x32_bf16 v[246:249], v[158:161], v[30:33], v[246:249]\n"
    "ds_read_b128 v[30:33], v4 offset:24704\n"
    "v_mfma_f32_16x16x32_bf16 v[234:237], v[162:165], v[34:37], v[234:237]\n"
    "buffer_store_dwordx4 v[226:229], v11, s[80:83], 0 offen sc1\n"
    "v_mfma_f32_16x16x32_bf16 v[238:241], v[166:169], v[34:37], v[238:241]\n"
    "v_mfma_f32_16x16x32_bf16 v[242:245], v[170:173], v[34:37], v[242:245]\n"
    "v_add_u32 v11, s46, v11\n"
    "v_mfma_f32_16x16x32_bf16 v[246:249], v[174:177], v[34:37], v[246:249]\n"
    "ds_read_b128 v[34:37], v4 offset:24768\n"
    "v_mfma_f32_16x16x32_bf16 v[234:237], v[178:181], v[38:41], v[234:237]\n"
    "v_mfma_f32_16x16x32_bf16 v[238:241], v[182:185], v[38:41], v[238:241]\n"
    "v_mfma_f32_16x16x32_bf16 v[242:245], v[186:189], v[38:41], v[242:245]\n"
    "v_mfma_f32_16x16x32_bf16 v[246:249], v[190:193], v[38:41], v[246:249]\n"
    "ds_read_b128 v[38:41], v4 offset:24832\n"
    "s_waitcnt vmcnt(1)\n"
    "v_mfma_f32_16x16x32_bf16 v[234:237], v[194:197], v[250:253], v[234:237]\n"
    "v_mfma_f32_16x16x32_bf16 v[238:241], v[198:201], v[250:253], v[238:241]\n"
    "v_mfma_f32_16x16x32_bf16 v[242:245], v[202:205], v[250:253], v[242:245]\n"
    "v_mfma_f32_16x16x32_bf16 v[246:249], v[206:209], v[250:253], v[246:249]\n"
    "ds_read_b128 v[250:253], v4 offset:24896\n"
    "s_waitcnt lgkmcnt(4)\n"
    "v_mfma_f32_16x16x32_bf16 v[210:213], v[42:45], v[22:25], 0\n"
    "v_mfma_f32_16x16x32_bf16 v[214:217], v[46:49], v[22:25], 0\n"
    "v_mfma_f32_16x16x32_bf16 v[218:221], v[50:53], v[22:25], 0\n"
    "ds_read_b128 v[22:25], v5 offset:24576\n"
    "v_mul_f32 v234, v234, v1\n"
    "v_mfma_f32_16x16x32_bf16 v[210:213], v[54:57], v[26:29], v[210:213]\n"
    "v_mul_f32 v235, v235, v1\n"
    "v_mul_f32 v236, v236, v1\n"
    "v_mfma_f32_16x16x32_bf16 v[214:217], v[58:61], v[26:29], v[214:217]\n"
    "v_mul_f32 v237, v237, v1\n"
    "v_mul_f32 v238, v238, v1\n"
    "v_mfma_f32_16x16x32_bf16 v[218:221], v[62:65], v[26:29], v[218:221]\n"
    "ds_read_b128 v[26:29], v5 offset:24640\n"
    "s_waitcnt lgkmcnt(4)\n"
    "v_mfma_f32_16x16x32_bf16 v[210:213], v[66:69], v[30:33], v[210:213]\n"
    "v_mul_f32 v239, v239, v1\n"
    "v_mul_f32 v240, v240, v1\n"
    "v_mfma_f32_16x16x32_bf16 v[214:217], v[70:73], v[30:33], v[214:217]\n"
    "v_mul_f32 v241, v241, v1\n"
    "v_cvt_pk_bf16_f32 v234, v234, v235\n"
    "v_mfma_f32_16x16x32_bf16 v[218:221], v[74:77], v[30:33], v[218:221]\n"
    "ds_read_b128 v[30:33], v5 offset:24704\n"
    "v_cvt_pk_bf16_f32 v235, v236, v237\n"
    "v_mfma_f32_16x16x32_bf16 v[210:213], v[78:81], v[34:37], v[210:213]\n"
    "v_cvt_pk_bf16_f32 v236, v238, v239\n"
    "v_cvt_pk_bf16_f32 v237, v240, v241\n"
    "v_mfma_f32_16x16x32_bf16 v[214:217], v[82:85], v[34:37], v[214:217]\n"
    "buffer_store_dwordx4 v[234:237], v12, s[80:83], 0 offen sc1\n"
    "v_mfma_f32_16x16x32_bf16 v[218:221], v[86:89], v[34:37], v[218:221]\n"
    "ds_read_b128 v[34:37], v5 offset:24768\n"
    "s_waitcnt lgkmcnt(4)\n"
    "v_mfma_f32_16x16x32_bf16 v[210:213], v[90:93], v[38:41], v[210:213]\n"
    "v_mul_f32 v242, v242, v1\n"
    "v_mul_f32 v243, v243, v1\n"
    "v_mfma_f32_16x16x32_bf16 v[214:217], v[94:97], v[38:41], v[214:217]\n"
    "v_mul_f32 v244, v244, v1\n"
    "v_mul_f32 v245, v245, v1\n"
    "v_mfma_f32_16x16x32_bf16 v[218:221], v[98:101], v[38:41], v[218:221]\n"
    "ds_read_b128 v[38:41], v5 offset:24832\n"
    "v_mul_f32 v246, v246, v1\n"
    "v_mfma_f32_16x16x32_bf16 v[210:213], v[102:105], v[250:253], v[210:213]\n"
    "v_mul_f32 v247, v247, v1\n"
    "v_mul_f32 v248, v248, v1\n"
    "v_mfma_f32_16x16x32_bf16 v[214:217], v[106:109], v[250:253], v[214:217]\n"
    "v_mul_f32 v249, v249, v1\n"
    "v_cvt_pk_bf16_f32 v242, v242, v243\n"
    "v_mfma_f32_16x16x32_bf16 v[218:221], v[110:113], v[250:253], v[218:221]\n"
    "ds_read_b128 v[250:253], v5 offset:24896\n"
    "s_waitcnt lgkmcnt(4)\n"
    "v_mfma_f32_16x16x32_bf16 v[222:225], v[42:45], v[22:25], 0\n"
    "v_cvt_pk_bf16_f32 v243, v244, v245\n"
    "v_cvt_pk_bf16_f32 v244, v246, v247\n"
    "v_mfma_f32_16x16x32_bf16 v[226:229], v[46:49], v[22:25], 0\n"
    "v_cvt_pk_bf16_f32 v245, v248, v249\n"
    "v_add_u32 v4, v6, v2\n"
    "v_mfma_f32_16x16x32_bf16 v[230:233], v[50:53], v[22:25], 0\n"
    "ds_read_b128 v[22:25], v7 offset:12288\n"
    "v_cvt_pk_bf16_f32 v210, v210, v211\n"
    "v_mfma_f32_16x16x32_bf16 v[222:225], v[54:57], v[26:29], v[222:225]\n"
    "buffer_store_dwordx4 v[242:245], v12, s[80:83], s44 offen sc1\n"
    "v_mfma_f32_16x16x32_bf16 v[226:229], v[58:61], v[26:29], v[226:229]\n"
    "v_cvt_pk_bf16_f32 v211, v212, v213\n"
    "v_add_u32 v12, s46, v12\n"
    "v_mfma_f32_16x16x32_bf16 v[230:233], v[62:65], v[26:29], v[230:233]\n"
    "ds_read_b128 v[26:29], v7 offset:13312\n"
    "s_waitcnt lgkmcnt(4)\n"
    "v_mfma_f32_16x16x32_bf16 v[222:225], v[66:69], v[30:33], v[222:225]\n"
    "v_cvt_pk_bf16_f32 v212, v214, v215\n"
    "v_cvt_pk_bf16_f32 v213, v216, v217\n"
    "v_mfma_f32_16x16x32_bf16 v[226:229], v[70:73], v[30:33], v[226:229]\n"
    "v_cvt_pk_bf16_f32 v218, v218, v219\n"
    "v_cvt_pk_bf16_f32 v219, v220, v221\n"
    "v_mfma_f32_16x16x32_bf16 v[230:233], v[74:77], v[30:33], v[230:233]\n"
    "ds_read_b128 v[30:33], v7 offset:14336\n"
    "v_add_u32 v5, v6, v3\n"
    "v_mfma_f32_16x16x32_bf16 v[222:225], v[78:81], v[34:37], v[222:225]\n"
    "ds_write_b128 v9, v[210:213] offset:0\n"
    "v_mfma_f32_16x16x32_bf16 v[226:229], v[82:85], v[34:37], v[226:229]\n"
    "ds_write_b64 v10, v[218:219] offset:0\n"
    "v_mfma_f32_16x16x32_bf16 v[230:233], v[86:89], v[34:37], v[230:233]\n"
    "ds_read_b128 v[34:37], v7 offset:15360\n"
    "s_waitcnt lgkmcnt(6)\n"
    "v_mfma_f32_16x16x32_bf16 v[222:225], v[90:93], v[38:41], v[222:225]\n"
    "ds_read_b32 v0, v254 offset:8192\n"
    "v_mfma_f32_16x16x32_bf16 v[226:229], v[94:97], v[38:41], v[226:229]\n"
    "v_mfma_f32_16x16x32_bf16 v[230:233], v[98:101], v[38:41], v[230:233]\n"
    "ds_read_b128 v[38:41], v7 offset:16384\n"
    "v_mfma_f32_16x16x32_bf16 v[222:225], v[102:105], v[250:253], v[222:225]\n"
    "v_mfma_f32_16x16x32_bf16 v[226:229], v[106:109], v[250:253], v[226:229]\n"
    "v_mfma_f32_16x16x32_bf16 v[230:233], v[110:113], v[250:253], v[230:233]\n"
    "ds_read_b128 v[250:253], v7 offset:17408\n"
    "s_waitcnt lgkmcnt(7)\n"
    "v_mfma_f32_16x16x32_bf16 v[234:237], v[114:117], v[22:25], 0\n"
    "v_mfma_f32_16x16x32_bf16 v[238:241], v[118:121], v[22:25], 0\n"
    "ds_read_b128 v[22:25], v8 offset:12288\n"
    "v_mfma_f32_16x16x32_bf16 v[234:237], v[130:133], v[26:29], v[234:237]\n"
    "v_cvt_pk_bf16_f32 v222, v222, v223\n"
    "v_cvt_pk_bf16_f32 v223, v224, v225\n"
    "v_mfma_f32_16x16x32_bf16 v[238:241], v[134:137], v[26:29], v[238:241]\n"
    "ds_read_b128 v[26:29], v8 offset:13312\n"
    "s_waitcnt lgkmcnt(5)\n"
    "v_mfma_f32_16x16x32_bf16 v[234:237], v[146:149], v[30:33], v[234:237]\n"
    "v_cvt_pk_bf16_f32 v224, v226, v227\n"
    "v_cvt_pk_bf16_f32 v225, v228, v229\n"
    "v_mfma_f32_16x16x32_bf16 v[238:241], v[150:153], v[30:33], v[238:241]\n"
    "ds_read_b128 v[30:33], v8 offset:14336\n"
    "v_cvt_pk_bf16_f32 v230, v230, v231\n"
    "v_mfma_f32_16x16x32_bf16 v[234:237], v[162:165], v[34:37], v[234:237]\n"
    "v_cvt_pk_bf16_f32 v231, v232, v233\n"
    "v_mfma_f32_16x16x32_bf16 v[238:241], v[166:169], v[34:37], v[238:241]\n"
    "ds_read_b128 v[34:37], v8 offset:15360\n"
    "s_waitcnt lgkmcnt(4)\n"
    "v_mfma_f32_16x16x32_bf16 v[234:237], v[178:181], v[38:41], v[234:237]\n"
    "ds_write_b128 v9, v[222:225] offset:6144\n"
    "v_mfma_f32_16x16x32_bf16 v[238:241], v[182:185], v[38:41], v[238:241]\n"
    "ds_read_b128 v[38:41], v8 offset:16384\n"
    "v_mfma_f32_16x16x32_bf16 v[234:237], v[194:197], v[250:253], v[234:237]\n"
    "ds_write_b64 v10, v[230:231] offset:6144\n"
    "v_mfma_f32_16x16x32_bf16 v[238:241], v[198:201], v[250:253], v[238:241]\n"
    "ds_read_b128 v[250:253], v8 offset:17408\n"
    "s_waitcnt lgkmcnt(6)\n"
    "v_mfma_f32_16x16x32_bf16 v[242:245], v[114:117], v[22:25], 0\n"
    "ds_read_b32 v1, v255 offset:8192\n"
    "v_mfma_f32_16x16x32_bf16 v[246:249], v[118:121], v[22:25], 0\n"
    "v_mfma_f32_16x16x32_bf16 v[210:213], v[122:125], v[22:25], 0\n"
    "v_mul_f32 v234, v234, v0\n"
    "v_mul_f32 v235, v235, v0\n"
    "v_mfma_f32_16x16x32_bf16 v[214:217], v[126:129], v[22:25], 0\n"
    "ds_read_b128 v[22:25], v4 offset:0\n"
    "v_mul_f32 v236, v236, v0\n"
    "v_mfma_f32_16x16x32_bf16 v[242:245], v[130:133], v[26:29], v[242:245]\n"
    "v_mul_f32 v237, v237, v0\n"
    "v_mul_f32 v238, v238, v0\n"
    "v_mfma_f32_16x16x32_bf16 v[246:249], v[134:137], v[26:29], v[246:249]\n"
    "v_mul_f32 v239, v239, v0\n"
    "v_mul_f32 v240, v240, v0\n"
    "v_mfma_f32_16x16x32_bf16 v[210:213], v[138:141], v[26:29], v[210:213]\n"
    "v_mul_f32 v241, v241, v0\n"
    "v_cvt_pk_bf16_f32 v234, v234, v235\n"
    "v_mfma_f32_16x16x32_bf16 v[214:217], v[142:145], v[26:29], v[214:217]\n"
    "s_waitcnt lgkmcnt(1)\n"
    "s_barrier\n"
    "ds_read_b128 v[26:29], v4 offset:64\n"
    "v_mfma_f32_16x16x32_bf16 v[242:245], v[146:149], v[30:33], v[242:245]\n"
    "v_cvt_pk_bf16_f32 v235, v236, v237\n"
    "v_cvt_pk_bf16_f32 v236, v238, v239\n"
    "v_mfma_f32_16x16x32_bf16 v[246:249], v[150:153], v[30:33], v[246:249]\n"
    "v_cvt_pk_bf16_f32 v237, v240, v241\n"
    "v_mfma_f32_16x16x32_bf16 v[210:213], v[154:157], v[30:33], v[210:213]\n"
    "buffer_store_dwordx4 v[234:237], v11, s[80:83], 0 offen sc1\n"
    "v_mfma_f32_16x16x32_bf16 v[214:217], v[158:161], v[30:33], v[214:217]\n"
    "ds_read_b128 v[30:33], v4 offset:128\n"
    "v_add_u32 v11, s46, v11\n"
    "v_mfma_f32_16x16x32_bf16 v[242:245], v[162:165], v[34:37], v[242:245]\n"
    "v_mfma_f32_16x16x32_bf16 v[246:249], v[166:169], v[34:37], v[246:249]\n"
    "v_mfma_f32_16x16x32_bf16 v[210:213], v[170:173], v[34:37], v[210:213]\n"
    "v_mfma_f32_16x16x32_bf16 v[214:217], v[174:177], v[34:37], v[214:217]\n"
    "ds_read_b128 v[34:37], v4 offset:192\n"
    "v_mfma_f32_16x16x32_bf16 v[242:245], v[178:181], v[38:41], v[242:245]\n"
    "v_mfma_f32_16x16x32_bf16 v[246:249], v[182:185], v[38:41], v[246:249]\n"
    "v_mfma_f32_16x16x32_bf16 v[210:213], v[186:189], v[38:41], v[210:213]\n"
    "v_mfma_f32_16x16x32_bf16 v[214:217], v[190:193], v[38:41], v[214:217]\n"
    "ds_read_b128 v[38:41], v4 offset:256\n"
    "v_mfma_f32_16x16x32_bf16 v[242:245], v[194:197], v[250:253], v[242:245]\n"
    "v_mfma_f32_16x16x32_bf16 v[246:249], v[198:201], v[250:253], v[246:249]\n"
    "v_mfma_f32_16x16x32_bf16 v[210:213], v[202:205], v[250:253], v[210:213]\n"
    "v_mfma_f32_16x16x32_bf16 v[214:217], v[206:209], v[250:253], v[214:217]\n"
    "ds_read_b128 v[250:253], v4 offset:320\n"
    "s_waitcnt lgkmcnt(4)\n"
    "v_mfma_f32_16x16x32_bf16 v[218:221], v[42:45], v[22:25], 0\n"
    "v_mfma_f32_16x16x32_bf16 v[222:225], v[46:49], v[22:25], 0\n"
    "v_mfma_f32_16x16x32_bf16 v[226:229], v[50:53], v[22:25], 0\n"
    "ds_read_b128 v[22:25], v5 offset:0\n"
    "v_mul_f32 v242, v242, v1\n"
    "v_mfma_f32_16x16x32_bf16 v[218:221], v[54:57], v[26:29], v[218:221]\n"
    "v_mul_f32 v243, v243, v1\n"
    "v_mul_f32 v244, v244, v1\n"
    "v_mfma_f32_16x16x32_bf16 v[222:225], v[58:61], v[26:29], v[222:225]\n"
    "v_mul_f32 v245, v245, v1\n"
    "v_mul_f32 v246, v246, v1\n"
    "v_mfma_f32_16x16x32_bf16 v[226:229], v[62:65], v[26:29], v[226:229]\n"
    "ds_read_b128 v[26:29], v5 offset:64\n"
    "s_waitcnt lgkmcnt(4)\n"
    "v_mfma_f32_16x16x32_bf16 v[218:221], v[66:69], v[30:33], v[218:221]\n"
    "v_mul_f32 v247, v247, v1\n"
    "v_mul_f32 v248, v248, v1\n"
    "v_mfma_f32_16x16x32_bf16 v[222:225], v[70:73], v[30:33], v[222:225]\n"
    "v_mul_f32 v249, v249, v1\n"
    "v_cvt_pk_bf16_f32 v242, v242, v243\n"
    "v_mfma_f32_16x16x32_bf16 v[226:229], v[74:77], v[30:33], v[226:229]\n"
    "ds_read_b128 v[30:33], v5 offset:128\n"
    "v_cvt_pk_bf16_f32 v243, v244, v245\n"
    "v_mfma_f32_16x16x32_bf16 v[218:221], v[78:81], v[34:37], v[218:221]\n"
    "v_cvt_pk_bf16_f32 v244, v246, v247\n"
    "v_cvt_pk_bf16_f32 v245, v248, v249\n"
    "v_mfma_f32_16x16x32_bf16 v[222:225], v[82:85], v[34:37], v[222:225]\n"
    "buffer_store_dwordx4 v[242:245], v12, s[80:83], 0 offen sc1\n"
    "v_mfma_f32_16x16x32_bf16 v[226:229], v[86:89], v[34:37], v[226:229]\n"
    "ds_read_b128 v[34:37], v5 offset:192\n"
    "s_waitcnt lgkmcnt(4)\n"
    "v_mfma_f32_16x16x32_bf16 v[218:221], v[90:93], v[38:41], v[218:221]\n"
    "v_mul_f32 v210, v210, v1\n"
    "v_mul_f32 v211, v211, v1\n"
    "v_mfma_f32_16x16x32_bf16 v[222:225], v[94:97], v[38:41], v[222:225]\n"
    "v_mul_f32 v212, v212, v1\n"
    "v_mul_f32 v213, v213, v1\n"
    "v_mfma_f32_16x16x32_bf16 v[226:229], v[98:101], v[38:41], v[226:229]\n"
    "ds_read_b128 v[38:41], v5 offset:256\n"
    "v_mul_f32 v214, v214, v1\n"
    "v_mfma_f32_16x16x32_bf16 v[218:221], v[102:105], v[250:253], v[218:221]\n"
    "v_mul_f32 v215, v215, v1\n"
    "v_mul_f32 v216, v216, v1\n"
    "v_mfma_f32_16x16x32_bf16 v[222:225], v[106:109], v[250:253], v[222:225]\n"
    "v_mul_f32 v217, v217, v1\n"
    "v_cvt_pk_bf16_f32 v210, v210, v211\n"
    "v_mfma_f32_16x16x32_bf16 v[226:229], v[110:113], v[250:253], v[226:229]\n"
    "ds_read_b128 v[250:253], v5 offset:320\n"
    "s_waitcnt lgkmcnt(4)\n"
    "v_mfma_f32_16x16x32_bf16 v[230:233], v[42:45], v[22:25], 0\n"
    "v_cvt_pk_bf16_f32 v211, v212, v213\n"
    "v_cvt_pk_bf16_f32 v212, v214, v215\n"
    "v_mfma_f32_16x16x32_bf16 v[234:237], v[46:49], v[22:25], 0\n"
    "v_cvt_pk_bf16_f32 v213, v216, v217\n"
    "v_cvt_pk_bf16_f32 v218, v218, v219\n"
    "v_mfma_f32_16x16x32_bf16 v[238:241], v[50:53], v[22:25], 0\n"
    "ds_read_b128 v[22:25], v7 offset:0\n"
    "v_cvt_pk_bf16_f32 v219, v220, v221\n"
    "v_mfma_f32_16x16x32_bf16 v[230:233], v[54:57], v[26:29], v[230:233]\n"
    "buffer_store_dwordx4 v[210:213], v12, s[80:83], s44 offen sc1\n"
    "v_mfma_f32_16x16x32_bf16 v[234:237], v[58:61], v[26:29], v[234:237]\n"
    "v_cvt_pk_bf16_f32 v220, v222, v223\n"
    "v_add_u32 v12, s46, v12\n"
    "v_mfma_f32_16x16x32_bf16 v[238:241], v[62:65], v[26:29], v[238:241]\n"
    "ds_read_b128 v[26:29], v7 offset:1024\n"
    "s_waitcnt lgkmcnt(4)\n"
    "v_mfma_f32_16x16x32_bf16 v[230:233], v[66:69], v[30:33], v[230:233]\n"
    "v_cvt_pk_bf16_f32 v221, v224, v225\n"
    "v_cvt_pk_bf16_f32 v226, v226, v227\n"
    "v_mfma_f32_16x16x32_bf16 v[234:237], v[70:73], v[30:33], v[234:237]\n"
    "v_cvt_pk_bf16_f32 v227, v228, v229\n"
    "v_mfma_f32_16x16x32_bf16 v[238:241], v[74:77], v[30:33], v[238:241]\n"
    "ds_read_b128 v[30:33], v7 offset:2048\n"
    "v_mfma_f32_16x16x32_bf16 v[230:233], v[78:81], v[34:37], v[230:233]\n"
    "ds_write_b128 v9, v[218:221] offset:12288\n"
    "v_mfma_f32_16x16x32_bf16 v[234:237], v[82:85], v[34:37], v[234:237]\n"
    "ds_write_b64 v10, v[226:227] offset:12288\n"
    "v_mfma_f32_16x16x32_bf16 v[238:241], v[86:89], v[34:37], v[238:241]\n"
    "ds_read_b128 v[34:37], v7 offset:3072\n"
    "s_waitcnt lgkmcnt(6)\n"
    "v_mfma_f32_16x16x32_bf16 v[230:233], v[90:93], v[38:41], v[230:233]\n"
    "ds_read_b32 v0, v254 offset:0\n"
    "v_mfma_f32_16x16x32_bf16 v[234:237], v[94:97], v[38:41], v[234:237]\n"
    "v_mfma_f32_16x16x32_bf16 v[238:241], v[98:101], v[38:41], v[238:241]\n"
    "ds_read_b128 v[38:41], v7 offset:4096\n"
    "v_mfma_f32_16x16x32_bf16 v[230:233], v[102:105], v[250:253], v[230:233]\n"
    "v_mfma_f32_16x16x32_bf16 v[234:237], v[106:109], v[250:253], v[234:237]\n"
    "v_mfma_f32_16x16x32_bf16 v[238:241], v[110:113], v[250:253], v[238:241]\n"
    "ds_read_b128 v[250:253], v7 offset:5120\n"
    "s_waitcnt lgkmcnt(7)\n"
    "v_mfma_f32_16x16x32_bf16 v[242:245], v[114:117], v[22:25], 0\n"
    "v_mfma_f32_16x16x32_bf16 v[246:249], v[118:121], v[22:25], 0\n"
    "ds_read_b128 v[22:25], v8 offset:0\n"
    "v_mfma_f32_16x16x32_bf16 v[242:245], v[130:133], v[26:29], v[242:245]\n"
    "v_cvt_pk_bf16_f32 v230, v230, v231\n"
    "v_cvt_pk_bf16_f32 v231, v232, v233\n"
    "v_mfma_f32_16x16x32_bf16 v[246:249], v[134:137], v[26:29], v[246:249]\n"
    "ds_read_b128 v[26:29], v8 offset:1024\n"
    "s_waitcnt lgkmcnt(5)\n"
    "v_mfma_f32_16x16x32_bf16 v[242:245], v[146:149], v[30:33], v[242:245]\n"
    "v_cvt_pk_bf16_f32 v232, v234, v235\n"
    "v_cvt_pk_bf16_f32 v233, v236, v237\n"
    "v_mfma_f32_16x16x32_bf16 v[246:249], v[150:153], v[30:33], v[246:249]\n"
    "ds_read_b128 v[30:33], v8 offset:2048\n"
    "v_cvt_pk_bf16_f32 v238, v238, v239\n"
    "v_mfma_f32_16x16x32_bf16 v[242:245], v[162:165], v[34:37], v[242:245]\n"
    "v_cvt_pk_bf16_f32 v239, v240, v241\n"
    "v_mfma_f32_16x16x32_bf16 v[246:249], v[166:169], v[34:37], v[246:249]\n"
    "ds_read_b128 v[34:37], v8 offset:3072\n"
    "s_waitcnt lgkmcnt(4)\n"
    "v_mfma_f32_16x16x32_bf16 v[242:245], v[178:181], v[38:41], v[242:245]\n"
    "ds_write_b128 v9, v[230:233] offset:18432\n"
    "v_mfma_f32_16x16x32_bf16 v[246:249], v[182:185], v[38:41], v[246:249]\n"
    "ds_read_b128 v[38:41], v8 offset:4096\n"
    "v_mfma_f32_16x16x32_bf16 v[242:245], v[194:197], v[250:253], v[242:245]\n"
    "ds_write_b64 v10, v[238:239] offset:18432\n"
    "v_mfma_f32_16x16x32_bf16 v[246:249], v[198:201], v[250:253], v[246:249]\n"
    "ds_read_b128 v[250:253], v8 offset:5120\n"
    "s_waitcnt lgkmcnt(6)\n"
    "v_mfma_f32_16x16x32_bf16 v[210:213], v[114:117], v[22:25], 0\n"
    "ds_read_b32 v1, v255 offset:0\n"
    "v_mfma_f32_16x16x32_bf16 v[214:217], v[118:121], v[22:25], 0\n"
    "v_mfma_f32_16x16x32_bf16 v[218:221], v[122:125], v[22:25], 0\n"
    "v_mul_f32 v242, v242, v0\n"
    "v_mul_f32 v243, v243, v0\n"
    "v_mfma_f32_16x16x32_bf16 v[222:225], v[126:129], v[22:25], 0\n"
    "ds_read_b128 v[22:25], v2 offset:8192\n"
    "v_mul_f32 v244, v244, v0\n"
    "v_mfma_f32_16x16x32_bf16 v[210:213], v[130:133], v[26:29], v[210:213]\n"
    "v_mul_f32 v245, v245, v0\n"
    "v_mul_f32 v246, v246, v0\n"
    "v_mfma_f32_16x16x32_bf16 v[214:217], v[134:137], v[26:29], v[214:217]\n"
    "v_mul_f32 v247, v247, v0\n"
    "v_mul_f32 v248, v248, v0\n"
    "v_mfma_f32_16x16x32_bf16 v[218:221], v[138:141], v[26:29], v[218:221]\n"
    "v_mul_f32 v249, v249, v0\n"
    "v_cvt_pk_bf16_f32 v242, v242, v243\n"
    "v_mfma_f32_16x16x32_bf16 v[222:225], v[142:145], v[26:29], v[222:225]\n"
    "s_waitcnt lgkmcnt(1)\n"
    "s_barrier\n"
    "ds_read_b128 v[26:29], v2 offset:8256\n"
    "v_mfma_f32_16x16x32_bf16 v[210:213], v[146:149], v[30:33], v[210:213]\n"
    "v_cvt_pk_bf16_f32 v243, v244, v245\n"
    "v_cvt_pk_bf16_f32 v244, v246, v247\n"
    "v_mfma_f32_16x16x32_bf16 v[214:217], v[150:153], v[30:33], v[214:217]\n"
    "v_cvt_pk_bf16_f32 v245, v248, v249\n"
    "v_mfma_f32_16x16x32_bf16 v[218:221], v[154:157], v[30:33], v[218:221]\n"
    "buffer_store_dwordx4 v[242:245], v11, s[80:83], 0 offen sc1\n"
    "v_mfma_f32_16x16x32_bf16 v[222:225], v[158:161], v[30:33], v[222:225]\n"
    "ds_read_b128 v[30:33], v2 offset:8320\n"
    "v_add_u32 v11, s46, v11\n"
    "v_mfma_f32_16x16x32_bf16 v[210:213], v[162:165], v[34:37], v[210:213]\n"
    "v_mfma_f32_16x16x32_bf16 v[214:217], v[166:169], v[34:37], v[214:217]\n"
    "v_mfma_f32_16x16x32_bf16 v[218:221], v[170:173], v[34:37], v[218:221]\n"
    "v_mfma_f32_16x16x32_bf16 v[222:225], v[174:177], v[34:37], v[222:225]\n"
    "ds_read_b128 v[34:37], v2 offset:8384\n"
    "v_mfma_f32_16x16x32_bf16 v[210:213], v[178:181], v[38:41], v[210:213]\n"
    "v_mfma_f32_16x16x32_bf16 v[214:217], v[182:185], v[38:41], v[214:217]\n"
    "v_mfma_f32_16x16x32_bf16 v[218:221], v[186:189], v[38:41], v[218:221]\n"
    "v_mfma_f32_16x16x32_bf16 v[222:225], v[190:193], v[38:41], v[222:225]\n"
    "ds_read_b128 v[38:41], v2 offset:8448\n"
    "v_mfma_f32_16x16x32_bf16 v[210:213], v[194:197], v[250:253], v[210:213]\n"
    "v_mfma_f32_16x16x32_bf16 v[214:217], v[198:201], v[250:253], v[214:217]\n"
    "v_mfma_f32_16x16x32_bf16 v[218:221], v[202:205], v[250:253], v[218:221]\n"
    "v_mfma_f32_16x16x32_bf16 v[222:225], v[206:209], v[250:253], v[222:225]\n"
    "ds_read_b128 v[250:253], v2 offset:8512\n"
    "s_waitcnt lgkmcnt(4)\n"
    "v_mfma_f32_16x16x32_bf16 v[226:229], v[42:45], v[22:25], 0\n"
    "v_mfma_f32_16x16x32_bf16 v[230:233], v[46:49], v[22:25], 0\n"
    "v_mfma_f32_16x16x32_bf16 v[234:237], v[50:53], v[22:25], 0\n"
    "ds_read_b128 v[22:25], v3 offset:8192\n"
    "v_mul_f32 v210, v210, v1\n"
    "v_mfma_f32_16x16x32_bf16 v[226:229], v[54:57], v[26:29], v[226:229]\n"
    "v_mul_f32 v211, v211, v1\n"
    "v_mul_f32 v212, v212, v1\n"
    "v_mfma_f32_16x16x32_bf16 v[230:233], v[58:61], v[26:29], v[230:233]\n"
    "v_mul_f32 v213, v213, v1\n"
    "v_mul_f32 v214, v214, v1\n"
    "v_mfma_f32_16x16x32_bf16 v[234:237], v[62:65], v[26:29], v[234:237]\n"
    "ds_read_b128 v[26:29], v3 offset:8256\n"
    "s_waitcnt lgkmcnt(4)\n"
    "v_mfma_f32_16x16x32_bf16 v[226:229], v[66:69], v[30:33], v[226:229]\n"
    "v_mul_f32 v215, v215, v1\n"
    "v_mul_f32 v216, v216, v1\n"
    "v_mfma_f32_16x16x32_bf16 v[230:233], v[70:73], v[30:33], v[230:233]\n"
    "v_mul_f32 v217, v217, v1\n"
    "v_cvt_pk_bf16_f32 v210, v210, v211\n"
    "v_mfma_f32_16x16x32_bf16 v[234:237], v[74:77], v[30:33], v[234:237]\n"
    "ds_read_b128 v[30:33], v3 offset:8320\n"
    "v_cvt_pk_bf16_f32 v211, v212, v213\n"
    "v_mfma_f32_16x16x32_bf16 v[226:229], v[78:81], v[34:37], v[226:229]\n"
    "v_cvt_pk_bf16_f32 v212, v214, v215\n"
    "v_cvt_pk_bf16_f32 v213, v216, v217\n"
    "v_mfma_f32_16x16x32_bf16 v[230:233], v[82:85], v[34:37], v[230:233]\n"
    "buffer_store_dwordx4 v[210:213], v12, s[80:83], 0 offen sc1\n"
    "v_mfma_f32_16x16x32_bf16 v[234:237], v[86:89], v[34:37], v[234:237]\n"
    "ds_read_b128 v[34:37], v3 offset:8384\n"
    "s_waitcnt lgkmcnt(4)\n"
    "v_mfma_f32_16x16x32_bf16 v[226:229], v[90:93], v[38:41], v[226:229]\n"
    "v_mul_f32 v218, v218, v1\n"
    "v_mul_f32 v219, v219, v1\n"
    "v_mfma_f32_16x16x32_bf16 v[230:233], v[94:97], v[38:41], v[230:233]\n"
    "v_mul_f32 v220, v220, v1\n"
    "v_mul_f32 v221, v221, v1\n"
    "v_mfma_f32_16x16x32_bf16 v[234:237], v[98:101], v[38:41], v[234:237]\n"
    "ds_read_b128 v[38:41], v3 offset:8448\n"
    "v_mul_f32 v222, v222, v1\n"
    "v_mfma_f32_16x16x32_bf16 v[226:229], v[102:105], v[250:253], v[226:229]\n"
    "v_mul_f32 v223, v223, v1\n"
    "v_mul_f32 v224, v224, v1\n"
    "v_mfma_f32_16x16x32_bf16 v[230:233], v[106:109], v[250:253], v[230:233]\n"
    "v_mul_f32 v225, v225, v1\n"
    "v_cvt_pk_bf16_f32 v218, v218, v219\n"
    "v_mfma_f32_16x16x32_bf16 v[234:237], v[110:113], v[250:253], v[234:237]\n"
    "ds_read_b128 v[250:253], v3 offset:8512\n"
    "s_waitcnt lgkmcnt(4)\n"
    "v_mfma_f32_16x16x32_bf16 v[238:241], v[42:45], v[22:25], 0\n"
    "v_cvt_pk_bf16_f32 v219, v220, v221\n"
    "v_cvt_pk_bf16_f32 v220, v222, v223\n"
    "v_mfma_f32_16x16x32_bf16 v[242:245], v[46:49], v[22:25], 0\n"
    "v_cvt_pk_bf16_f32 v221, v224, v225\n"
    "v_cvt_pk_bf16_f32 v226, v226, v227\n"
    "v_mfma_f32_16x16x32_bf16 v[246:249], v[50:53], v[22:25], 0\n"
    "ds_read_b128 v[22:25], v7 offset:12288\n"
    "v_cvt_pk_bf16_f32 v227, v228, v229\n"
    "v_mfma_f32_16x16x32_bf16 v[238:241], v[54:57], v[26:29], v[238:241]\n"
    "buffer_store_dwordx4 v[218:221], v12, s[80:83], s44 offen sc1\n"
    "v_mfma_f32_16x16x32_bf16 v[242:245], v[58:61], v[26:29], v[242:245]\n"
    "v_cvt_pk_bf16_f32 v228, v230, v231\n"
    "v_add_u32 v12, s46, v12\n"
    "v_mfma_f32_16x16x32_bf16 v[246:249], v[62:65], v[26:29], v[246:249]\n"
    "ds_read_b128 v[26:29], v7 offset:13312\n"
    "s_waitcnt lgkmcnt(4)\n"
    "v_mfma_f32_16x16x32_bf16 v[238:241], v[66:69], v[30:33], v[238:241]\n"
    "v_cvt_pk_bf16_f32 v229, v232, v233\n"
    "v_cvt_pk_bf16_f32 v234, v234, v235\n"
    "v_mfma_f32_16x16x32_bf16 v[242:245], v[70:73], v[30:33], v[242:245]\n"
    "v_cvt_pk_bf16_f32 v235, v236, v237\n"
    "v_mfma_f32_16x16x32_bf16 v[246:249], v[74:77], v[30:33], v[246:249]\n"
    "ds_read_b128 v[30:33], v7 offset:14336\n"
    "v_mfma_f32_16x16x32_bf16 v[238:241], v[78:81], v[34:37], v[238:241]\n"
    "ds_write_b128 v9, v[226:229] offset:0\n"
    "v_mfma_f32_16x16x32_bf16 v[242:245], v[82:85], v[34:37], v[242:245]\n"
    "ds_write_b64 v10, v[234:235] offset:0\n"
    "v_mfma_f32_16x16x32_bf16 v[246:249], v[86:89], v[34:37], v[246:249]\n"
    "ds_read_b128 v[34:37], v7 offset:15360\n"
    "s_waitcnt lgkmcnt(6)\n"
    "v_mfma_f32_16x16x32_bf16 v[238:241], v[90:93], v[38:41], v[238:241]\n"
    "ds_read_b32 v0, v254 offset:8192\n"
    "v_mfma_f32_16x16x32_bf16 v[242:245], v[94:97], v[38:41], v[242:245]\n"
    "v_mfma_f32_16x16x32_bf16 v[246:249], v[98:101], v[38:41], v[246:249]\n"
    "ds_read_b128 v[38:41], v7 offset:16384\n"
    "v_mfma_f32_16x16x32_bf16 v[238:241], v[102:105], v[250:253], v[238:241]\n"
    "v_mfma_f32_16x16x32_bf16 v[242:245], v[106:109], v[250:253], v[242:245]\n"
    "v_mfma_f32_16x16x32_bf16 v[246:249], v[110:113], v[250:253], v[246:249]\n"
    "ds_read_b128 v[250:253], v7 offset:17408\n"
    "s_waitcnt lgkmcnt(7)\n"
    "v_mfma_f32_16x16x32_bf16 v[210:213], v[114:117], v[22:25], 0\n"
    "ds_read_b128 v[66:69], v8 offset:12288\n"
    "v_mfma_f32_16x16x32_bf16 v[214:217], v[118:121], v[22:25], 0\n"
    "ds_read_b128 v[70:73], v8 offset:13312\n"
    "v_cvt_pk_bf16_f32 v238, v238, v239\n"
    "v_mfma_f32_16x16x32_bf16 v[210:213], v[130:133], v[26:29], v[210:213]\n"
    "ds_read_b128 v[74:77], v8 offset:14336\n"
    "v_cvt_pk_bf16_f32 v239, v240, v241\n"
    "v_mfma_f32_16x16x32_bf16 v[214:217], v[134:137], v[26:29], v[214:217]\n"
    "ds_read_b128 v[78:81], v8 offset:15360\n"
    "s_waitcnt lgkmcnt(7)\n"
    "v_mfma_f32_16x16x32_bf16 v[210:213], v[146:149], v[30:33], v[210:213]\n"
    "ds_read_b128 v[82:85], v8 offset:16384\n"
    "v_cvt_pk_bf16_f32 v240, v242, v243\n"
    "v_mfma_f32_16x16x32_bf16 v[214:217], v[150:153], v[30:33], v[214:217]\n"
    "ds_read_b128 v[86:89], v8 offset:17408\n"
    "v_cvt_pk_bf16_f32 v241, v244, v245\n"
    "v_mfma_f32_16x16x32_bf16 v[210:213], v[162:165], v[34:37], v[210:213]\n"
    "v_cvt_pk_bf16_f32 v246, v246, v247\n"
    "v_cvt_pk_bf16_f32 v247, v248, v249\n"
    "v_mfma_f32_16x16x32_bf16 v[214:217], v[166:169], v[34:37], v[214:217]\n"
    "s_waitcnt lgkmcnt(6)\n"
    "v_mfma_f32_16x16x32_bf16 v[210:213], v[178:181], v[38:41], v[210:213]\n"
    "ds_write_b128 v9, v[238:241] offset:6144\n"
    "v_mfma_f32_16x16x32_bf16 v[214:217], v[182:185], v[38:41], v[214:217]\n"
    "ds_write_b64 v10, v[246:247] offset:6144\n"
    "v_mfma_f32_16x16x32_bf16 v[210:213], v[194:197], v[250:253], v[210:213]\n"
    "ds_read_b32 v1, v255 offset:8192\n"
    "v_mfma_f32_16x16x32_bf16 v[214:217], v[198:201], v[250:253], v[214:217]\n"
    "s_waitcnt lgkmcnt(7)\n"
    "v_mfma_f32_16x16x32_bf16 v[218:221], v[114:117], v[66:69], 0\n"
    "v_mfma_f32_16x16x32_bf16 v[222:225], v[118:121], v[66:69], 0\n"
    "v_mfma_f32_16x16x32_bf16 v[226:229], v[122:125], v[66:69], 0\n"
    "v_mfma_f32_16x16x32_bf16 v[230:233], v[126:129], v[66:69], 0\n"
    "v_mfma_f32_16x16x32_bf16 v[218:221], v[130:133], v[70:73], v[218:221]\n"
    "v_mul_f32 v210, v210, v0\n"
    "v_mul_f32 v211, v211, v0\n"
    "v_mfma_f32_16x16x32_bf16 v[222:225], v[134:137], v[70:73], v[222:225]\n"
    "v_mul_f32 v212, v212, v0\n"
    "v_mul_f32 v213, v213, v0\n"
    "v_mfma_f32_16x16x32_bf16 v[226:229], v[138:141], v[70:73], v[226:229]\n"
    "v_mul_f32 v214, v214, v0\n"
    "v_mul_f32 v215, v215, v0\n"
    "v_mfma_f32_16x16x32_bf16 v[230:233], v[142:145], v[70:73], v[230:233]\n"
    "s_waitcnt lgkmcnt(5)\n"
    "v_mfma_f32_16x16x32_bf16 v[218:221], v[146:149], v[74:77], v[218:221]\n"
    "v_mul_f32 v216, v216, v0\n"
    "v_mul_f32 v217, v217, v0\n"
    "v_mfma_f32_16x16x32_bf16 v[222:225], v[150:153], v[74:77], v[222:225]\n"
    "v_cvt_pk_bf16_f32 v210, v210, v211\n"
    "v_cvt_pk_bf16_f32 v211, v212, v213\n"
    "v_mfma_f32_16x16x32_bf16 v[226:229], v[154:157], v[74:77], v[226:229]\n"
    "v_cvt_pk_bf16_f32 v212, v214, v215\n"
    "v_cvt_pk_bf16_f32 v213, v216, v217\n"
    "v_mfma_f32_16x16x32_bf16 v[230:233], v[158:161], v[74:77], v[230:233]\n"
    "s_waitcnt lgkmcnt(0)\n"
    "s_barrier\n"
    "ds_read_b128 v[90:93], v7 offset:0\n"
    "v_mfma_f32_16x16x32_bf16 v[218:221], v[162:165], v[78:81], v[218:221]\n"
    "ds_read_b128 v[94:97], v7 offset:1024\n"
    "v_mfma_f32_16x16x32_bf16 v[222:225], v[166:169], v[78:81], v[222:225]\n"
    "ds_read_b128 v[98:101], v7 offset:2048\n"
    "v_mfma_f32_16x16x32_bf16 v[226:229], v[170:173], v[78:81], v[226:229]\n"
    "ds_read_b128 v[102:105], v7 offset:3072\n"
    "v_mfma_f32_16x16x32_bf16 v[230:233], v[174:177], v[78:81], v[230:233]\n"
    "ds_read_b128 v[22:25], v7 offset:4096\n"
    "v_mfma_f32_16x16x32_bf16 v[218:221], v[178:181], v[82:85], v[218:221]\n"
    "ds_read_b128 v[26:29], v7 offset:5120\n"
    "v_mfma_f32_16x16x32_bf16 v[222:225], v[182:185], v[82:85], v[222:225]\n"
    "ds_read_b128 v[30:33], v8 offset:0\n"
    "v_mfma_f32_16x16x32_bf16 v[226:229], v[186:189], v[82:85], v[226:229]\n"
    "buffer_store_dwordx4 v[210:213], v11, s[80:83], 0 offen sc1\n"
    "v_mfma_f32_16x16x32_bf16 v[230:233], v[190:193], v[82:85], v[230:233]\n"
    "ds_read_b128 v[34:37], v8 offset:1024\n"
    "v_add_u32 v11, s46, v11\n"
    "v_mfma_f32_16x16x32_bf16 v[218:221], v[194:197], v[86:89], v[218:221]\n"
    "ds_read_b32 v106, v254 offset:0\n"
    "v_mfma_f32_16x16x32_bf16 v[222:225], v[198:201], v[86:89], v[222:225]\n"
    "v_mfma_f32_16x16x32_bf16 v[226:229], v[202:205], v[86:89], v[226:229]\n"
    "v_mfma_f32_16x16x32_bf16 v[230:233], v[206:209], v[86:89], v[230:233]\n"
    "ds_read_b128 v[38:41], v8 offset:2048\n"
    "s_waitcnt lgkmcnt(8)\n"
    "v_mfma_f32_16x16x32_bf16 v[234:237], v[114:117], v[90:93], 0\n"
    "v_mfma_f32_16x16x32_bf16 v[238:241], v[118:121], v[90:93], 0\n"
    "ds_read_b128 v[250:253], v8 offset:3072\n"
    "v_mul_f32 v218, v218, v1\n"
    "v_mfma_f32_16x16x32_bf16 v[234:237], v[130:133], v[94:97], v[234:237]\n"
    "v_mul_f32 v219, v219, v1\n"
    "v_mul_f32 v220, v220, v1\n"
    "v_mfma_f32_16x16x32_bf16 v[238:241], v[134:137], v[94:97], v[238:241]\n"
    "ds_read_b128 v[66:69], v8 offset:4096\n"
    "s_waitcnt lgkmcnt(8)\n"
    "v_mfma_f32_16x16x32_bf16 v[234:237], v[146:149], v[98:101], v[234:237]\n"
    "v_mul_f32 v221, v221, v1\n"
    "v_mul_f32 v222, v222, v1\n"
    "v_mfma_f32_16x16x32_bf16 v[238:241], v[150:153], v[98:101], v[238:241]\n"
    "ds_read_b128 v[70:73], v8 offset:5120\n"
    "v_mul_f32 v223, v223, v1\n"
    "v_mfma_f32_16x16x32_bf16 v[234:237], v[162:165], v[102:105], v[234:237]\n"
    "v_mul_f32 v224, v224, v1\n"
    "v_mul_f32 v225, v225, v1\n"
    "v_mfma_f32_16x16x32_bf16 v[238:241], v[166:169], v[102:105], v[238:241]\n"
    "s_waitcnt lgkmcnt(7)\n"
    "v_mfma_f32_16x16x32_bf16 v[234:237], v[178:181], v[22:25], v[234:237]\n"
    "v_cvt_pk_bf16_f32 v218, v218, v219\n"
    "v_cvt_pk_bf16_f32 v219, v220, v221\n"
    "v_mfma_f32_16x16x32_bf16 v[238:241], v[182:185], v[22:25], v[238:241]\n"
    "v_cvt_pk_bf16_f32 v220, v222, v223\n"
    "v_mfma_f32_16x16x32_bf16 v[234:237], v[194:197], v[26:29], v[234:237]\n"
    "v_cvt_pk_bf16_f32 v221, v224, v225\n"
    "v_mul_f32 v226, v226, v1\n"
    "v_mfma_f32_16x16x32_bf16 v[238:241], v[198:201], v[26:29], v[238:241]\n"
    "s_waitcnt lgkmcnt(5)\n"
    "v_mfma_f32_16x16x32_bf16 v[242:245], v[114:117], v[30:33], 0\n"
    "buffer_store_dwordx4 v[218:221], v12, s[80:83], 0 offen sc1\n"
    "v_mfma_f32_16x16x32_bf16 v[246:249], v[118:121], v[30:33], 0\n"
    "v_mul_f32 v227, v227, v1\n"
    "v_mul_f32 v228, v228, v1\n"
    "v_mfma_f32_16x16x32_bf16 v[210:213], v[122:125], v[30:33], 0\n"
    "v_mul_f32 v229, v229, v1\n"
    "v_mul_f32 v230, v230, v1\n"
    "v_mfma_f32_16x16x32_bf16 v[214:217], v[126:129], v[30:33], 0\n"
    "v_mul_f32 v231, v231, v1\n"
    "v_mfma_f32_16x16x32_bf16 v[242:245], v[130:133], v[34:37], v[242:245]\n"
    "v_mul_f32 v232, v232, v1\n"
    "v_mul_f32 v233, v233, v1\n"
    "v_mfma_f32_16x16x32_bf16 v[246:249], v[134:137], v[34:37], v[246:249]\n"
    "v_cvt_pk_bf16_f32 v226, v226, v227\n"
    "v_cvt_pk_bf16_f32 v227, v228, v229\n"
    "v_mfma_f32_16x16x32_bf16 v[210:213], v[138:141], v[34:37], v[210:213]\n"
    "v_cvt_pk_bf16_f32 v228, v230, v231\n"
    "v_cvt_pk_bf16_f32 v229, v232, v233\n"
    "v_mfma_f32_16x16x32_bf16 v[214:217], v[142:145], v[34:37], v[214:217]\n"
    "s_waitcnt lgkmcnt(2)\n"
    "v_mfma_f32_16x16x32_bf16 v[242:245], v[146:149], v[38:41], v[242:245]\n"
    "buffer_store_dwordx4 v[226:229], v12, s[80:83], s44 offen sc1\n"
    "v_mfma_f32_16x16x32_bf16 v[246:249], v[150:153], v[38:41], v[246:249]\n"
    "ds_read_b32 v107, v255 offset:0\n"
    "v_add_u32 v12, s46, v12\n"
    "v_mfma_f32_16x16x32_bf16 v[210:213], v[154:157], v[38:41], v[210:213]\n"
    "v_mul_f32 v234, v234, v106\n"
    "v_mul_f32 v235, v235, v106\n"
    "v_mfma_f32_16x16x32_bf16 v[214:217], v[158:161], v[38:41], v[214:217]\n"
    "v_mul_f32 v236, v236, v106\n"
    "v_mfma_f32_16x16x32_bf16 v[242:245], v[162:165], v[250:253], v[242:245]\n"
    "v_mul_f32 v237, v237, v106\n"
    "v_mul_f32 v238, v238, v106\n"
    "v_mfma_f32_16x16x32_bf16 v[246:249], v[166:169], v[250:253], v[246:249]\n"
    "v_mul_f32 v239, v239, v106\n"
    "v_mul_f32 v240, v240, v106\n"
    "v_mfma_f32_16x16x32_bf16 v[210:213], v[170:173], v[250:253], v[210:213]\n"
    "v_mul_f32 v241, v241, v106\n"
    "v_cvt_pk_bf16_f32 v234, v234, v235\n"
    "v_mfma_f32_16x16x32_bf16 v[214:217], v[174:177], v[250:253], v[214:217]\n"
    "s_waitcnt lgkmcnt(0)\n"
    "s_barrier\n"
    "ds_read_b128 v[74:77], v7 offset:12288\n"
    "ds_read_b128 v[78:81], v7 offset:13312\n"
    "v_mfma_f32_16x16x32_bf16 v[242:245], v[178:181], v[66:69], v[242:245]\n"
    "ds_read_b128 v[82:85], v7 offset:14336\n"
    "v_cvt_pk_bf16_f32 v235, v236, v237\n"
    "v_mfma_f32_16x16x32_bf16 v[246:249], v[182:185], v[66:69], v[246:249]\n"
    "ds_read_b128 v[86:89], v7 offset:15360\n"
    "v_cvt_pk_bf16_f32 v236, v238, v239\n"
    "v_mfma_f32_16x16x32_bf16 v[210:213], v[186:189], v[66:69], v[210:213]\n"
    "ds_read_b128 v[90:93], v7 offset:16384\n"
    "v_cvt_pk_bf16_f32 v237, v240, v241\n"
    "v_mfma_f32_16x16x32_bf16 v[214:217], v[190:193], v[66:69], v[214:217]\n"
    "ds_read_b128 v[94:97], v7 offset:17408\n"
    "v_mfma_f32_16x16x32_bf16 v[242:245], v[194:197], v[70:73], v[242:245]\n"
    "buffer_store_dwordx4 v[234:237], v11, s[80:83], 0 offen sc1\n"
    "v_mfma_f32_16x16x32_bf16 v[246:249], v[198:201], v[70:73], v[246:249]\n"
    "ds_read_b32 v108, v254 offset:8192\n"
    "v_add_u32 v11, v16, v11\n"
    "v_mfma_f32_16x16x32_bf16 v[210:213], v[202:205], v[70:73], v[210:213]\n"
    "v_mfma_f32_16x16x32_bf16 v[214:217], v[206:209], v[70:73], v[214:217]\n"
    "ds_read_b128 v[98:101], v8 offset:12288\n"
    "s_waitcnt lgkmcnt(6)\n"
    "v_mfma_f32_16x16x32_bf16 v[218:221], v[114:117], v[74:77], 0\n"
    "v_mul_f32 v242, v242, v107\n"
    "v_mul_f32 v243, v243, v107\n"
    "v_mfma_f32_16x16x32_bf16 v[222:225], v[118:121], v[74:77], 0\n"
    "ds_read_b128 v[102:105], v8 offset:13312\n"
    "v_mul_f32 v244, v244, v107\n"
    "v_mfma_f32_16x16x32_bf16 v[218:221], v[130:133], v[78:81], v[218:221]\n"
    "v_mul_f32 v245, v245, v107\n"
    "v_mul_f32 v246, v246, v107\n"
    "v_mfma_f32_16x16x32_bf16 v[222:225], v[134:137], v[78:81], v[222:225]\n"
    "ds_read_b128 v[22:25], v8 offset:14336\n"
    "s_waitcnt lgkmcnt(6)\n"
    "v_mfma_f32_16x16x32_bf16 v[218:221], v[146:149], v[82:85], v[218:221]\n"
    "v_mul_f32 v247, v247, v107\n"
    "v_mul_f32 v248, v248, v107\n"
    "v_mfma_f32_16x16x32_bf16 v[222:225], v[150:153], v[82:85], v[222:225]\n"
    "ds_read_b128 v[26:29], v8 offset:15360\n"
    "v_mul_f32 v249, v249, v107\n"
    "v_mfma_f32_16x16x32_bf16 v[218:221], v[162:165], v[86:89], v[218:221]\n"
    "v_cvt_pk_bf16_f32 v242, v242, v243\n"
    "v_cvt_pk_bf16_f32 v243, v244, v245\n"
    "v_mfma_f32_16x16x32_bf16 v[222:225], v[166:169], v[86:89], v[222:225]\n"
    "ds_read_b128 v[30:33], v8 offset:16384\n"
    "s_waitcnt lgkmcnt(6)\n"
    "v_mfma_f32_16x16x32_bf16 v[218:221], v[178:181], v[90:93], v[218:221]\n"
    "v_cvt_pk_bf16_f32 v244, v246, v247\n"
    "v_cvt_pk_bf16_f32 v245, v248, v249\n"
    "v_mfma_f32_16x16x32_bf16 v[222:225], v[182:185], v[90:93], v[222:225]\n"
    "ds_read_b128 v[34:37], v8 offset:17408\n"
    "v_mul_f32 v210, v210, v107\n"
    "v_mfma_f32_16x16x32_bf16 v[218:221], v[194:197], v[94:97], v[218:221]\n"
    "buffer_store_dwordx4 v[242:245], v12, s[80:83], 0 offen sc1\n"
    "v_mfma_f32_16x16x32_bf16 v[222:225], v[198:201], v[94:97], v[222:225]\n"
    "s_waitcnt lgkmcnt(4)\n"
    "v_mfma_f32_16x16x32_bf16 v[226:229], v[114:117], v[98:101], 0\n"
    "v_mul_f32 v211, v211, v107\n"
    "v_mul_f32 v212, v212, v107\n"
    "v_mfma_f32_16x16x32_bf16 v[230:233], v[118:121], v[98:101], 0\n"
    "v_mul_f32 v213, v213, v107\n"
    "v_mul_f32 v214, v214, v107\n"
    "v_mfma_f32_16x16x32_bf16 v[234:237], v[122:125], v[98:101], 0\n"
    "v_mul_f32 v215, v215, v107\n"
    "v_mul_f32 v216, v216, v107\n"
    "v_mfma_f32_16x16x32_bf16 v[238:241], v[126:129], v[98:101], 0\n"
    "v_mul_f32 v217, v217, v107\n"
    "v_mfma_f32_16x16x32_bf16 v[226:229], v[130:133], v[102:105], v[226:229]\n"
    "v_cvt_pk_bf16_f32 v210, v210, v211\n"
    "v_cvt_pk_bf16_f32 v211, v212, v213\n"
    "v_mfma_f32_16x16x32_bf16 v[230:233], v[134:137], v[102:105], v[230:233]\n"
    "v_cvt_pk_bf16_f32 v212, v214, v215\n"
    "v_cvt_pk_bf16_f32 v213, v216, v217\n"
    "v_mfma_f32_16x16x32_bf16 v[234:237], v[138:141], v[102:105], v[234:237]\n"
    "buffer_store_dwordx4 v[210:213], v12, s[80:83], s44 offen sc1\n"
    "v_mfma_f32_16x16x32_bf16 v[238:241], v[142:145], v[102:105], v[238:241]\n"
    "s_waitcnt lgkmcnt(2)\n"
    "v_mfma_f32_16x16x32_bf16 v[226:229], v[146:149], v[22:25], v[226:229]\n"
    "v_add_u32 v12, v16, v12\n"
    "v_mul_f32 v218, v218, v108\n"
    "v_mfma_f32_16x16x32_bf16 v[230:233], v[150:153], v[22:25], v[230:233]\n"
    "ds_read_b32 v109, v255 offset:8192\n"
    "v_mul_f32 v219, v219, v108\n"
    "v_mfma_f32_16x16x32_bf16 v[234:237], v[154:157], v[22:25], v[234:237]\n"
    "v_mul_f32 v220, v220, v108\n"
    "v_mul_f32 v221, v221, v108\n"
    "v_mfma_f32_16x16x32_bf16 v[238:241], v[158:161], v[22:25], v[238:241]\n"
    "v_mul_f32 v222, v222, v108\n"
    "v_mfma_f32_16x16x32_bf16 v[226:229], v[162:165], v[26:29], v[226:229]\n"
    "v_mul_f32 v223, v223, v108\n"
    "v_mul_f32 v224, v224, v108\n"
    "v_mfma_f32_16x16x32_bf16 v[230:233], v[166:169], v[26:29], v[230:233]\n"
    "v_mul_f32 v225, v225, v108\n"
    "v_cvt_pk_bf16_f32 v218, v218, v219\n"
    "v_mfma_f32_16x16x32_bf16 v[234:237], v[170:173], v[26:29], v[234:237]\n"
    "v_cvt_pk_bf16_f32 v219, v220, v221\n"
    "v_cvt_pk_bf16_f32 v220, v222, v223\n"
    "v_mfma_f32_16x16x32_bf16 v[238:241], v[174:177], v[26:29], v[238:241]\n"
    "s_waitcnt lgkmcnt(1)\n"
    "v_mfma_f32_16x16x32_bf16 v[226:229], v[178:181], v[30:33], v[226:229]\n"
    "v_cvt_pk_bf16_f32 v221, v224, v225\n"
    "v_mfma_f32_16x16x32_bf16 v[230:233], v[182:185], v[30:33], v[230:233]\n"
    "buffer_store_dwordx4 v[218:221], v11, s[80:83], 0 offen sc1\n"
    "v_mfma_f32_16x16x32_bf16 v[234:237], v[186:189], v[30:33], v[234:237]\n"
    "v_mfma_f32_16x16x32_bf16 v[238:241], v[190:193], v[30:33], v[238:241]\n"
    "v_mfma_f32_16x16x32_bf16 v[226:229], v[194:197], v[34:37], v[226:229]\n"
    "v_mfma_f32_16x16x32_bf16 v[230:233], v[198:201], v[34:37], v[230:233]\n"
    "v_mfma_f32_16x16x32_bf16 v[234:237], v[202:205], v[34:37], v[234:237]\n"
    "v_mfma_f32_16x16x32_bf16 v[238:241], v[206:209], v[34:37], v[238:241]\n"
    "s_waitcnt lgkmcnt(0)\n"
    "s_nop 3\n"
    "v_mul_f32 v226, v226, v109\n"
    "v_mul_f32 v227, v227, v109\n"
    "v_mul_f32 v228, v228, v109\n"
    "v_mul_f32 v229, v229, v109\n"
    "v_mul_f32 v230, v230, v109\n"
    "v_mul_f32 v231, v231, v109\n"
    "v_mul_f32 v232, v232, v109\n"
    "v_mul_f32 v233, v233, v109\n"
    "v_cvt_pk_bf16_f32 v226, v226, v227\n"
    "v_cvt_pk_bf16_f32 v227, v228, v229\n"
    "v_cvt_pk_bf16_f32 v228, v230, v231\n"
    "v_cvt_pk_bf16_f32 v229, v232, v233\n"
    "buffer_store_dwordx4 v[226:229], v12, s[80:83], 0 offen sc1\n"
    "v_mul_f32 v234, v234, v109\n"
    "v_mul_f32 v235, v235, v109\n"
    "v_mul_f32 v236, v236, v109\n"
    "v_mul_f32 v237, v237, v109\n"
    "v_mul_f32 v238, v238, v109\n"
    "v_mul_f32 v239, v239, v109\n"
    "v_mul_f32 v240, v240, v109\n"
    "v_mul_f32 v241, v241, v109\n"
    "v_cvt_pk_bf16_f32 v234, v234, v235\n"
    "v_cvt_pk_bf16_f32 v235, v236, v237\n"
    "v_cvt_pk_bf16_f32 v236, v238, v239\n"
    "v_cvt_pk_bf16_f32 v237, v240, v241\n"
    "buffer_store_dwordx4 v[234:237], v12, s[80:83], s44 offen sc1\n"
    "s_barrier\n"
    "ds_read_b128 v[38:41], v7 offset:0\n"
    "ds_read_b128 v[250:253], v7 offset:1024\n"
    "ds_read_b128 v[66:69], v7 offset:2048\n"
    "ds_read_b128 v[70:73], v7 offset:3072\n"
    "s_waitcnt lgkmcnt(2)\n"
    "v_mfma_f32_16x16x32_bf16 v[242:245], v[114:117], v[38:41], 0\n"
    "ds_read_b128 v[74:77], v7 offset:4096\n"
    "ds_read_b32 v110, v254 offset:0\n"
    "v_mfma_f32_16x16x32_bf16 v[246:249], v[118:121], v[38:41], 0\n"
    "ds_read_b128 v[78:81], v7 offset:5120\n"
    "v_mfma_f32_16x16x32_bf16 v[242:245], v[130:133], v[250:253], v[242:245]\n"
    "ds_read_b128 v[82:85], v8 offset:0\n"
    "v_mfma_f32_16x16x32_bf16 v[246:249], v[134:137], v[250:253], v[246:249]\n"
    "ds_read_b128 v[86:89], v8 offset:1024\n"
    "s_waitcnt lgkmcnt(5)\n"
    "v_mfma_f32_16x16x32_bf16 v[242:245], v[146:149], v[66:69], v[242:245]\n"
    "v_mfma_f32_16x16x32_bf16 v[246:249], v[150:153], v[66:69], v[246:249]\n"
    "ds_read_b128 v[90:93], v8 offset:2048\n"
    "ds_read_b32 v111, v255 offset:0\n"
    "v_mfma_f32_16x16x32_bf16 v[242:245], v[162:165], v[70:73], v[242:245]\n"
    "v_mfma_f32_16x16x32_bf16 v[246:249], v[166:169], v[70:73], v[246:249]\n"
    "ds_read_b128 v[94:97], v8 offset:3072\n"
    "s_waitcnt lgkmcnt(5)\n"
    "v_mfma_f32_16x16x32_bf16 v[242:245], v[178:181], v[74:77], v[242:245]\n"
    "v_mfma_f32_16x16x32_bf16 v[246:249], v[182:185], v[74:77], v[246:249]\n"
    "ds_read_b128 v[98:101], v8 offset:4096\n"
    "v_mfma_f32_16x16x32_bf16 v[242:245], v[194:197], v[78:81], v[242:245]\n"
    "v_mfma_f32_16x16x32_bf16 v[246:249], v[198:201], v[78:81], v[246:249]\n"
    "ds_read_b128 v[102:105], v8 offset:5120\n"
    "s_waitcnt lgkmcnt(6)\n"
    "v_mfma_f32_16x16x32_bf16 v[210:213], v[114:117], v[82:85], 0\n"
    "v_mfma_f32_16x16x32_bf16 v[214:217], v[118:121], v[82:85], 0\n"
    "s_waitcnt lgkmcnt(5)\n"
    "v_mfma_f32_16x16x32_bf16 v[210:213], v[130:133], v[86:89], v[210:213]\n"
    "v_mfma_f32_16x16x32_bf16 v[214:217], v[134:137], v[86:89], v[214:217]\n"
    "v_mul_f32 v242, v242, v110\n"
    "v_mul_f32 v243, v243, v110\n"
    "s_waitcnt lgkmcnt(4)\n"
    "v_mfma_f32_16x16x32_bf16 v[210:213], v[146:149], v[90:93], v[210:213]\n"
    "v_mul_f32 v244, v244, v110\n"
    "v_mul_f32 v245, v245, v110\n"
    "v_mfma_f32_16x16x32_bf16 v[214:217], v[150:153], v[90:93], v[214:217]\n"
    "v_mul_f32 v246, v246, v110\n"
    "v_mul_f32 v247, v247, v110\n"
    "s_waitcnt lgkmcnt(2)\n"
    "v_mfma_f32_16x16x32_bf16 v[210:213], v[162:165], v[94:97], v[210:213]\n"
    "v_mul_f32 v248, v248, v110\n"
    "v_mul_f32 v249, v249, v110\n"
    "v_mfma_f32_16x16x32_bf16 v[214:217], v[166:169], v[94:97], v[214:217]\n"
    "v_cvt_pk_bf16_f32 v242, v242, v243\n"
    "v_cvt_pk_bf16_f32 v243, v244, v245\n"
    "s_waitcnt lgkmcnt(1)\n"
    "v_mfma_f32_16x16x32_bf16 v[210:213], v[178:181], v[98:101], v[210:213]\n"
    "v_cvt_pk_bf16_f32 v244, v246, v247\n"
    "v_cvt_pk_bf16_f32 v245, v248, v249\n"
    "v_mfma_f32_16x16x32_bf16 v[214:217], v[182:185], v[98:101], v[214:217]\n"
    "ds_bpermute_b32 v42, v15, v242\n"
    "ds_bpermute_b32 v43, v15, v243\n"
    "s_waitcnt lgkmcnt(2)\n"
    "v_mfma_f32_16x16x32_bf16 v[210:213], v[194:197], v[102:105], v[210:213]\n"
    "ds_bpermute_b32 v44, v15, v244\n"
    "ds_bpermute_b32 v45, v15, v245\n"
    "v_mfma_f32_16x16x32_bf16 v[214:217], v[198:201], v[102:105], v[214:217]\n"
    "v_mfma_f32_16x16x32_bf16 v[218:221], v[122:125], v[82:85], 0\n"
    "v_mfma_f32_16x16x32_bf16 v[222:225], v[126:129], v[82:85], 0\n"
    "v_mfma_f32_16x16x32_bf16 v[218:221], v[138:141], v[86:89], v[218:221]\n"
    "v_mfma_f32_16x16x32_bf16 v[222:225], v[142:145], v[86:89], v[222:225]\n"
    "s_waitcnt lgkmcnt(0)\n"
    "v_mfma_f32_16x16x32_bf16 v[218:221], v[154:157], v[90:93], v[218:221]\n"
    "buffer_store_dwordx4 v[42:45], v13, s[80:83], 0 offen sc1\n"
    "v_mfma_f32_16x16x32_bf16 v[222:225], v[158:161], v[90:93], v[222:225]\n"
    "v_mul_f32 v210, v210, v111\n"
    "v_mul_f32 v211, v211, v111\n"
    "v_mfma_f32_16x16x32_bf16 v[218:221], v[170:173], v[94:97], v[218:221]\n"
    "v_mul_f32 v212, v212, v111\n"
    "v_mul_f32 v213, v213, v111\n"
    "v_mfma_f32_16x16x32_bf16 v[222:225], v[174:177], v[94:97], v[222:225]\n"
    "v_mul_f32 v214, v214, v111\n"
    "v_mul_f32 v215, v215, v111\n"
    "v_mfma_f32_16x16x32_bf16 v[218:221], v[186:189], v[98:101], v[218:221]\n"
    "v_mul_f32 v216, v216, v111\n"
    "v_mul_f32 v217, v217, v111\n"
    "v_mfma_f32_16x16x32_bf16 v[222:225], v[190:193], v[98:101], v[222:225]\n"
    "v_cvt_pk_bf16_f32 v210, v210, v211\n"
    "v_cvt_pk_bf16_f32 v211, v212, v213\n"
    "v_mfma_f32_16x16x32_bf16 v[218:221], v[202:205], v[102:105], v[218:221]\n"
    "v_cvt_pk_bf16_f32 v212, v214, v215\n"
    "v_cvt_pk_bf16_f32 v213, v216, v217\n"
    "v_mfma_f32_16x16x32_bf16 v[222:225], v[206:209], v[102:105], v[222:225]\n"
    "ds_bpermute_b32 v46, v15, v210\n"
    "ds_bpermute_b32 v47, v15, v211\n"
    "ds_bpermute_b32 v48, v15, v212\n"
    "ds_bpermute_b32 v49, v15, v213\n"
    "s_nop 0\n"
    "v_mul_f32 v218, v218, v111\n"
    "v_mul_f32 v219, v219, v111\n"
    "v_mul_f32 v220, v220, v111\n"
    "v_mul_f32 v221, v221, v111\n"
    "v_mul_f32 v222, v222, v111\n"
    "v_mul_f32 v223, v223, v111\n"
    "v_mul_f32 v224, v224, v111\n"
    "v_mul_f32 v225, v225, v111\n"
    "v_cvt_pk_bf16_f32 v218, v218, v219\n"
    "v_cvt_pk_bf16_f32 v219, v220, v221\n"
    "v_cvt_pk_bf16_f32 v220, v222, v223\n"
    "v_cvt_pk_bf16_f32 v221, v224, v225\n"
    "s_waitcnt lgkmcnt(0)\n"
    "buffer_store_dwordx4 v[46:49], v14, s[80:83], 0 offen sc1\n"
    "ds_bpermute_b32 v50, v15, v218\n"
    "ds_bpermute_b32 v51, v15, v219\n"
    "ds_bpermute_b32 v52, v15, v220\n"
    "ds_bpermute_b32 v53, v15, v221\n"
    "s_waitcnt lgkmcnt(0)\n"
    "buffer_store_dwordx4 v[50:53], v14, s[80:83], s44 offen sc1\n"
    "s_waitcnt vmcnt(0)\n"
    "s_sub_u32 s63, s63, s62\n"
    "s_add_u32 s63, s63, 4000\n"
    ".Lg_spin:\n"
    "s_sleep 1\n"
    "s_memrealtime s[90:91]\n"
    "s_waitcnt lgkmcnt(0)\n"
    "s_sub_u32 s90, s90, s62\n"
    "s_cmp_lt_u32 s90, s63\n"
    "s_cbranch_scc1 .Lg_spin\n"
    "s_endpgm\n"
    ".Lg_last:\n"
            :: "{v[2:3]}"(va), "{v[6:9]}"(vb), "{v[10:13]}"(vc), "{v[14:17]}"(vd), "{v18}"(l16), "{v[114:117]}"(vp),
               "{s[40:41]}"(ws), "{s[80:83]}"(srd), "{s44}"(256 + 64 * (w & 1) - 64 * w), "{v[254:255]}"(vi), "{s46}"(UNI(2 * rm.d * IROWB)),
               "{s48}"(w), "{s49}"(UNI(w == 0 && pf >= 72 && pf < 144)), "{s50}"(bid),
               "{s45}"(UNI(w == 0 && pf < 72 ? 1 : (bid & 4) >> 1))
               , "{v19}"(stp), "{s[54:55]}"(dbg), "{s62}"(pt0)
            : "memory", "v253", "s47", "s51", "s52", "s53", "s56", "s57", "s61", "s63", "s77", "s90", "s91", "vcc", "scc");
    } else {
        const int a = wave - 4, P = a >> 1, H = a & 1;
        const unsigned xo[2] = {xo0, xo1};
        const unsigned sb = ldsbase + colad(4 * H + i16) + 16 * g + ((H ? i16 < 3 : i16 > 12) ? ZOFF : 0u);
        const unsigned pbase = ldsbase + colad(4 * H + 4 * g + ((lane >> 2) & 3)) + 8 * (lane & 3);
        f32x4 cbM, cbF, cbL;
        {
            const float NEG = -__builtin_inff();
            const int wcol = 7 * H + wq;
            const int ocs = (cm.edge ? min(wcol, 7) : wcol) - 4 * H;
#pragma unroll
            for (int r = 0; r < 4; ++r) {
                const float cb = ((unsigned)(4 * g + r - ocs) <= 6u) ? 0.f : NEG;
                cbM[r] = cb;
                cbF[r] = e == 0 ? cb : NEG;
                cbL[r] = e == 1 ? cb : NEG;
            }
        }
        u32x4 va = {sb, pbase - sb, ldsbase + LDS_E + P * EB + H * 6144 + lane * 16, xo[0]};
        u32x4 sa0, sa1;
#pragma unroll
        for (int b = 0; b < 4; ++b) {
            sa0[b] = sb + (2 * P + b) * ROWB;
            sa1[b] = sb + (2 * P + 4 + b) * ROWB;
        }
        u32x2 vm = {(i16 & 7) == 7 ? 0xff800000u : 0u, ldsbase + INVOFF + P * ROWB + H * 64 + 4 * i16};
        u32x2 vf;
#pragma unroll
        for (int j = 0; j < 2; ++j) {
            const int hl = min(lane + 64 * j, 119), col = (hl * 43) >> 8;
            vf[j] = axis_at(rm, 16 + a) * IROWB + axis_at(cm, col) * PIXB + 64 * (hl - 6 * col);
        }
        const u64 slots = (rm.edge ? 0xCBA987DCBA98ull : 0x543210DCBA98ull) >> (8 * P);
        asm volatile("" : "+v"(va), "+v"(cbM), "+v"(cbF), "+v"(cbL), "+v"(sa0), "+v"(sa1), "+v"(vm));
        STAMP(18);
        asm volatile("s_nop 7\n"
    "s_cmp_eq_u32 s76, 0\n"
    "s_cbranch_scc1 .La_nh\n"
    "v_mov_b32 v94, 155648\n"
    ".La_h:\n"
    "ds_read_b32 v95, v94\n"
    "s_waitcnt lgkmcnt(0)\n"
    "v_readfirstlane_b32 s53, v95\n"
    "s_nop 3\n"
    "s_cmp_lt_u32 s53, 2\n"
    "s_cbranch_scc1 .La_h\n"
    ".La_nh:\n"
    "s_mul_i32 s53, s75, 1\n"
    "s_add_u32 s53, s53, s74\n"
    "s_add_u32 m0, s44, 8192\n"
    "s_add_u32 s54, s40, s53\n"
    "s_addc_u32 s55, s41, 0\n"
    "s_waitcnt vmcnt(4)\n"
    "global_load_lds_dwordx4 v3, s[54:55]\n"
    "s_addk_i32 m0, 1024\n"
    "s_nop 0\n"
    "global_load_lds_dwordx4 v4, s[54:55]\n"
    "s_mul_i32 s53, s75, 2\n"
    "s_add_u32 s53, s53, s74\n"
    "s_add_u32 m0, s44, 16384\n"
    "s_add_u32 s54, s40, s53\n"
    "s_addc_u32 s55, s41, 0\n"
    "s_waitcnt vmcnt(4)\n"
    "global_load_lds_dwordx4 v3, s[54:55]\n"
    "s_addk_i32 m0, 1024\n"
    "s_nop 0\n"
    "global_load_lds_dwordx4 v4, s[54:55]\n"
    "s_mul_i32 s53, s75, 5\n"
    "s_add_u32 s53, s53, s74\n"
    "s_add_u32 m0, s44, 40960\n"
    "s_add_u32 s54, s40, s53\n"
    "s_addc_u32 s55, s41, 0\n"
    "s_waitcnt vmcnt(4)\n"
    "global_load_lds_dwordx4 v3, s[54:55]\n"
    "s_addk_i32 m0, 1024\n"
    "s_nop 0\n"
    "global_load_lds_dwordx4 v4, s[54:55]\n"
    "s_waitcnt vmcnt(2)\n"
    "s_barrier\n"
    "s_mul_i32 s53, s75, 6\n"
    "s_add_u32 s53, s53, s74\n"
    "s_add_u32 m0, s44, 49152\n"
    "s_add_u32 s54, s40, s53\n"
    "s_addc_u32 s55, s41, 0\n"
    "s_waitcnt vmcnt(4)\n"
    "global_load_lds_dwordx4 v3, s[54:55]\n"
    "s_addk_i32 m0, 1024\n"
    "s_nop 0\n"
    "global_load_lds_dwordx4 v4, s[54:55]\n"
    "s_mul_i32 s53, s75, 7\n"
    "s_add_u32 s53, s53, s74\n"
    "s_add_u32 m0, s44, 57344\n"
    "s_add_u32 s54, s40, s53\n"
    "s_addc_u32 s55, s41, 0\n"
    "s_waitcnt vmcnt(4)\n"
    "global_load_lds_dwordx4 v3, s[54:55]\n"
    "s_addk_i32 m0, 1024\n"
    "s_nop 0\n"
    "global_load_lds_dwordx4 v4, s[54:55]\n"
    "s_cmp_eq_u32 s45, 0\n"
    "s_cbranch_scc1 .La_p0\n"
    "s_sleep 16\n"
    "s_mul_i32 s53, s75, 8\n"
    "s_add_u32 s53, s53, s74\n"
    "s_add_u32 m0, s44, 65536\n"
    "s_add_u32 s54, s40, s53\n"
    "s_addc_u32 s55, s41, 0\n"
    "s_waitcnt vmcnt(4)\n"
    "global_load_lds_dwordx4 v3, s[54:55]\n"
    "s_addk_i32 m0, 1024\n"
    "s_nop 0\n"
    "global_load_lds_dwordx4 v4, s[54:55]\n"
    "s_mul_i32 s53, s75, 9\n"
    "s_add_u32 s53, s53, s74\n"
    "s_add_u32 m0, s44, 73728\n"
    "s_add_u32 s54, s40, s53\n"
    "s_addc_u32 s55, s41, 0\n"
    "s_waitcnt vmcnt(4)\n"
    "global_load_lds_dwordx4 v3, s[54:55]\n"
    "s_addk_i32 m0, 1024\n"
    "s_nop 0\n"
    "global_load_lds_dwordx4 v4, s[54:55]\n"
    "s_waitcnt vmcnt(4)\n"
    "s_barrier\n"
    "s_mul_i32 s53, s75, 10\n"
    "s_add_u32 s53, s53, s74\n"
    "s_add_u32 m0, s44, 81920\n"
    "s_add_u32 s54, s40, s53\n"
    "s_addc_u32 s55, s41, 0\n"
    "global_load_lds_dwordx4 v3, s[54:55]\n"
    "s_addk_i32 m0, 1024\n"
    "s_nop 0\n"
    "global_load_lds_dwordx4 v4, s[54:55]\n"
    "s_mul_i32 s53, s75, 11\n"
    "s_add_u32 s53, s53, s74\n"
    "s_add_u32 m0, s44, 90112\n"
    "s_add_u32 s54, s40, s53\n"
    "s_addc_u32 s55, s41, 0\n"
    "global_load_lds_dwordx4 v3, s[54:55]\n"
    "s_addk_i32 m0, 1024\n"
    "s_nop 0\n"
    "global_load_lds_dwordx4 v4, s[54:55]\n"
    "s_mul_i32 s53, s75, 12\n"
    "s_add_u32 s53, s53, s74\n"
    "s_add_u32 m0, s44, 98304\n"
    "s_add_u32 s54, s40, s53\n"
    "s_addc_u32 s55, s41, 0\n"
    "global_load_lds_dwordx4 v3, s[54:55]\n"
    "s_addk_i32 m0, 1024\n"
    "s_nop 0\n"
    "global_load_lds_dwordx4 v4, s[54:55]\n"
    "s_mul_i32 s53, s75, 13\n"
    "s_add_u32 s53, s53, s74\n"
    "s_add_u32 m0, s44, 106496\n"
    "s_add_u32 s54, s40, s53\n"
    "s_addc_u32 s55, s41, 0\n"
    "global_load_lds_dwordx4 v3, s[54:55]\n"
    "s_addk_i32 m0, 1024\n"
    "s_nop 0\n"
    "global_load_lds_dwordx4 v4, s[54:55]\n"
    ".La_p0:\n"
    "v_add_u32 v94, v24, v1\n"
    "v_add_u32 v95, v25, v1\n"
    "v_add_u32 v96, v26, v1\n"
    "v_add_u32 v97, v27, v1\n"
    "ds_read_b64_tr_b16 v[156:157], v94 offset:0\n"
    "ds_read_b64_tr_b16 v[158:159], v95 offset:0\n"
    "ds_read_b64_tr_b16 v[160:161], v94 offset:32\n"
    "ds_read_b64_tr_b16 v[162:163], v95 offset:32\n"
    "ds_read_b64_tr_b16 v[164:165], v94 offset:64\n"
    "ds_read_b64_tr_b16 v[166:167], v95 offset:64\n"
    "ds_read_b64_tr_b16 v[168:169], v94 offset:96\n"
    "ds_read_b64_tr_b16 v[170:171], v95 offset:96\n"
    "ds_read_b64_tr_b16 v[172:173], v94 offset:128\n"
    "ds_read_b64_tr_b16 v[174:175], v95 offset:128\n"
    "ds_read_b64_tr_b16 v[176:177], v94 offset:160\n"
    "ds_read_b64_tr_b16 v[178:179], v95 offset:160\n"
    "ds_read_b64_tr_b16 v[180:181], v94 offset:192\n"
    "ds_read_b64_tr_b16 v[182:183], v95 offset:192\n"
    "ds_read_b64_tr_b16 v[184:185], v94 offset:224\n"
    "ds_read_b64_tr_b16 v[186:187], v95 offset:224\n"
    "ds_read_b64_tr_b16 v[188:189], v94 offset:256\n"
    "ds_read_b64_tr_b16 v[190:191], v95 offset:256\n"
    "ds_read_b64_tr_b16 v[192:193], v94 offset:288\n"
    "ds_read_b64_tr_b16 v[194:195], v95 offset:288\n"
    "ds_read_b64_tr_b16 v[196:197], v94 offset:320\n"
    "ds_read_b64_tr_b16 v[198:199], v95 offset:320\n"
    "ds_read_b64_tr_b16 v[200:201], v94 offset:352\n"
    "ds_read_b64_tr_b16 v[202:203], v95 offset:352\n"
    "ds_read_b64_tr_b16 v[204:205], v96 offset:0\n"
    "ds_read_b64_tr_b16 v[206:207], v97 offset:0\n"
    "ds_read_b64_tr_b16 v[208:209], v96 offset:32\n"
    "ds_read_b64_tr_b16 v[210:211], v97 offset:32\n"
    "ds_read_b64_tr_b16 v[212:213], v96 offset:64\n"
    "ds_read_b64_tr_b16 v[214:215], v97 offset:64\n"
    "ds_read_b64_tr_b16 v[216:217], v96 offset:96\n"
    "ds_read_b64_tr_b16 v[218:219], v97 offset:96\n"
    "ds_read_b64_tr_b16 v[220:221], v96 offset:128\n"
    "ds_read_b64_tr_b16 v[222:223], v97 offset:128\n"
    "ds_read_b64_tr_b16 v[224:225], v96 offset:160\n"
    "ds_read_b64_tr_b16 v[226:227], v97 offset:160\n"
    "ds_read_b64_tr_b16 v[228:229], v96 offset:192\n"
    "ds_read_b64_tr_b16 v[230:231], v97 offset:192\n"
    "ds_read_b64_tr_b16 v[232:233], v96 offset:224\n"
    "ds_read_b64_tr_b16 v[234:235], v97 offset:224\n"
    "ds_read_b64_tr_b16 v[236:237], v96 offset:256\n"
    "ds_read_b64_tr_b16 v[238:239], v97 offset:256\n"
    "ds_read_b64_tr_b16 v[240:241], v96 offset:288\n"
    "ds_read_b64_tr_b16 v[242:243], v97 offset:288\n"
    "ds_read_b64_tr_b16 v[244:245], v96 offset:320\n"
    "ds_read_b64_tr_b16 v[246:247], v97 offset:320\n"
    "ds_read_b64_tr_b16 v[248:249], v96 offset:352\n"
    "ds_read_b64_tr_b16 v[250:251], v97 offset:352\n"
    "s_cmp_lg_u32 s45, 0\n"
    "s_cbranch_scc1 .La_p1\n"
    "s_mul_i32 s53, s75, 8\n"
    "s_add_u32 s53, s53, s74\n"
    "s_add_u32 m0, s44, 65536\n"
    "s_add_u32 s54, s40, s53\n"
    "s_addc_u32 s55, s41, 0\n"
    "s_waitcnt vmcnt(4)\n"
    "global_load_lds_dwordx4 v3, s[54:55]\n"
    "s_addk_i32 m0, 1024\n"
    "s_nop 0\n"
    "global_load_lds_dwordx4 v4, s[54:55]\n"
    "s_mul_i32 s53, s75, 9\n"
    "s_add_u32 s53, s53, s74\n"
    "s_add_u32 m0, s44, 73728\n"
    "s_add_u32 s54, s40, s53\n"
    "s_addc_u32 s55, s41, 0\n"
    "s_waitcnt vmcnt(4)\n"
    "global_load_lds_dwordx4 v3, s[54:55]\n"
    "s_addk_i32 m0, 1024\n"
    "s_nop 0\n"
    "global_load_lds_dwordx4 v4, s[54:55]\n"
    "s_waitcnt vmcnt(4)\n"
    ".La_p1:\n"
    "v_mov_b32 v252, 0x3f803f80\n"
    "v_mov_b32 v253, 0x3f803f80\n"
    "v_mov_b32 v254, 0x3f803f80\n"
    "v_mov_b32 v255, 0x3f803f80\n"
    "s_setprio 1\n"
    "ds_read_b128 v[108:111], v27 offset:0\n"
    "ds_read_b128 v[112:115], v27 offset:64\n"
    "ds_read_b128 v[116:119], v27 offset:128\n"
    "ds_read_b128 v[120:123], v27 offset:192\n"
    "ds_read_b128 v[124:127], v27 offset:256\n"
    "ds_read_b128 v[128:131], v27 offset:320\n"
    "ds_read_b128 v[132:135], v28 offset:0\n"
    "ds_read_b128 v[136:139], v28 offset:64\n"
    ".La_loop:\n"
    "s_add_u32 s52, s45, -2\n"
    "s_cmp_lg_u32 s57, 0\n"
    "s_cbranch_scc1 .La_e0\n"
    ".La_ed0:\n"
    "s_cmp_gt_u32 s52, 2\n"
    "s_cbranch_scc1 .La_x0\n"
    "s_add_u32 s54, s40, s42\n"
    "s_addc_u32 s55, s41, 0\n"
    "s_mov_b32 m0, s44\n"
    "s_add_u32 s42, s42, s77\n"
    "global_load_lds_dwordx4 v3, s[54:55]\n"
    "s_add_u32 m0, s44, 1024\n"
    "s_min_u32 s42, s42, s43\n"
    "global_load_lds_dwordx4 v4, s[54:55]\n"
    "s_add_u32 s44, s44, 8192\n"
    "s_add_u32 s54, s40, s42\n"
    "s_addc_u32 s55, s41, 0\n"
    "s_mov_b32 m0, s44\n"
    "s_add_u32 s42, s42, s77\n"
    "global_load_lds_dwordx4 v3, s[54:55]\n"
    "s_add_u32 m0, s44, 1024\n"
    "s_min_u32 s42, s42, s43\n"
    "global_load_lds_dwordx4 v4, s[54:55]\n"
    "s_add_u32 s44, s44, 8192\n"
    "s_cmp_eq_u32 s52, 0\n"
    "s_cbranch_scc1 .La_pf0\n"
    ".La_pd0:\n"
    "s_waitcnt vmcnt(8)\n"
    "s_cmp_lg_u32 s52, 2\n"
    "s_cbranch_scc1 .La_wd0\n"
    "s_waitcnt vmcnt(6)\n"
    ".La_wd0:\n"
    "s_waitcnt lgkmcnt(0)\n"
    "s_barrier\n"
    "ds_read_b128 v[36:39], v2 offset:0\n"
    "ds_read_b128 v[40:43], v2 offset:1024\n"
    "ds_read_b128 v[44:47], v2 offset:2048\n"
    "ds_read_b128 v[48:51], v2 offset:3072\n"
    "ds_read_b128 v[52:55], v2 offset:4096\n"
    "ds_read_b128 v[56:59], v2 offset:5120\n"
    "s_waitcnt lgkmcnt(3)\n"
    "v_mfma_f32_16x16x32_bf16 v[72:75], v[108:111], v[36:39], v[12:15]\n"
    "ds_read_b128 v[140:143], v28 offset:128\n"
    "ds_read_b128 v[144:147], v28 offset:192\n"
    "v_add_u32 v32, v28, v1\n"
    "v_mfma_f32_16x16x32_bf16 v[72:75], v[112:115], v[40:43], v[72:75]\n"
    "ds_read_b128 v[148:151], v28 offset:256\n"
    "v_add_u32 v33, v29, v1\n"
    "v_mfma_f32_16x16x32_bf16 v[72:75], v[116:119], v[44:47], v[72:75]\n"
    "ds_read_b128 v[152:155], v28 offset:320\n"
    "v_add_u32 v34, v30, v1\n"
    "s_waitcnt lgkmcnt(4)\n"
    "v_mfma_f32_16x16x32_bf16 v[72:75], v[120:123], v[48:51], v[72:75]\n"
    "ds_read_b128 v[108:111], v24 offset:0\n"
    "v_add_u32 v35, v31, v1\n"
    "v_mfma_f32_16x16x32_bf16 v[72:75], v[124:127], v[52:55], v[72:75]\n"
    "ds_read_b128 v[112:115], v24 offset:64\n"
    "s_bfe_u32 s48, s60, 0x40000\n"
    "s_bfe_u32 s49, s60, 0x40004\n"
    "v_mfma_f32_16x16x32_bf16 v[72:75], v[128:131], v[56:59], v[72:75]\n"
    "ds_read_b128 v[116:119], v24 offset:128\n"
    "s_waitcnt lgkmcnt(6)\n"
    "v_mfma_f32_16x16x32_bf16 v[76:79], v[132:135], v[36:39], v[12:15]\n"
    "ds_read_b128 v[120:123], v24 offset:192\n"
    "s_bfe_u32 s50, s60, 0x40008\n"
    "s_bfe_u32 s51, s60, 0x4000c\n"
    "v_mfma_f32_16x16x32_bf16 v[76:79], v[136:139], v[40:43], v[76:79]\n"
    "ds_read_b128 v[124:127], v24 offset:256\n"
    "s_lshr_b64 s[60:61], s[60:61], 16\n"
    "v_mfma_f32_16x16x32_bf16 v[76:79], v[140:143], v[44:47], v[76:79]\n"
    "ds_read_b128 v[128:131], v24 offset:320\n"
    "v_max3_f32 v6, v72, v73, v74\n"
    "s_waitcnt lgkmcnt(6)\n"
    "v_mfma_f32_16x16x32_bf16 v[76:79], v[144:147], v[48:51], v[76:79]\n"
    "ds_read_b128 v[132:135], v25 offset:0\n"
    "v_max_f32 v6, v6, v75\n"
    "v_mfma_f32_16x16x32_bf16 v[76:79], v[148:151], v[52:55], v[76:79]\n"
    "ds_read_b128 v[136:139], v25 offset:64\n"
    "s_nop 1\n"
    "v_mfma_f32_16x16x32_bf16 v[76:79], v[152:155], v[56:59], v[76:79]\n"
    "ds_read_b128 v[140:143], v25 offset:128\n"
    "s_waitcnt lgkmcnt(6)\n"
    "v_mfma_f32_16x16x32_bf16 v[60:63], v[108:111], v[36:39], v[16:19]\n"
    "ds_read_b128 v[144:147], v25 offset:192\n"
    "s_nop 1\n"
    "v_mfma_f32_16x16x32_bf16 v[60:63], v[112:115], v[40:43], v[60:63]\n"
    "ds_read_b128 v[148:151], v25 offset:256\n"
    "v_max3_f32 v6, v6, v76, v77\n"
    "v_mfma_f32_16x16x32_bf16 v[60:63], v[116:119], v[44:47], v[60:63]\n"
    "ds_read_b128 v[152:155], v25 offset:320\n"
    "v_max3_f32 v6, v6, v78, v79\n"
    "s_waitcnt lgkmcnt(6)\n"
    "v_mfma_f32_16x16x32_bf16 v[60:63], v[120:123], v[48:51], v[60:63]\n"
    "ds_read_b128 v[108:111], v26 offset:0\n"
    "v_mfma_f32_16x16x32_bf16 v[60:63], v[124:127], v[52:55], v[60:63]\n"
    "ds_read_b128 v[112:115], v26 offset:64\n"
    "s_nop 1\n"
    "v_mfma_f32_16x16x32_bf16 v[60:63], v[128:131], v[56:59], v[60:63]\n"
    "ds_read_b128 v[116:119], v26 offset:128\n"
    "s_waitcnt lgkmcnt(6)\n"
    "v_mfma_f32_16x16x32_bf16 v[64:67], v[132:135], v[36:39], v[12:15]\n"
    "ds_read_b128 v[120:123], v26 offset:192\n"
    "s_nop 1\n"
    "v_mfma_f32_16x16x32_bf16 v[64:67], v[136:139], v[40:43], v[64:67]\n"
    "ds_read_b128 v[124:127], v26 offset:256\n"
    "v_max3_f32 v6, v6, v60, v61\n"
    "v_mfma_f32_16x16x32_bf16 v[64:67], v[140:143], v[44:47], v[64:67]\n"
    "ds_read_b128 v[128:131], v26 offset:320\n"
    "v_max3_f32 v6, v6, v62, v63\n"
    "s_waitcnt lgkmcnt(6)\n"
    "v_mfma_f32_16x16x32_bf16 v[64:67], v[144:147], v[48:51], v[64:67]\n"
    "ds_read_b128 v[132:135], v29 offset:0\n"
    "v_mfma_f32_16x16x32_bf16 v[64:67], v[148:151], v[52:55], v[64:67]\n"
    "ds_read_b128 v[136:139], v29 offset:64\n"
    "s_nop 1\n"
    "v_mfma_f32_16x16x32_bf16 v[64:67], v[152:155], v[56:59], v[64:67]\n"
    "ds_read_b128 v[140:143], v29 offset:128\n"
    "s_waitcnt lgkmcnt(6)\n"
    "v_mfma_f32_16x16x32_bf16 v[68:71], v[108:111], v[36:39], v[12:15]\n"
    "ds_read_b128 v[144:147], v29 offset:192\n"
    "s_nop 1\n"
    "v_mfma_f32_16x16x32_bf16 v[68:71], v[112:115], v[40:43], v[68:71]\n"
    "ds_read_b128 v[148:151], v29 offset:256\n"
    "v_max3_f32 v6, v6, v64, v65\n"
    "v_mfma_f32_16x16x32_bf16 v[68:71], v[116:119], v[44:47], v[68:71]\n"
    "ds_read_b128 v[152:155], v29 offset:320\n"
    "v_max3_f32 v6, v6, v66, v67\n"
    "s_waitcnt lgkmcnt(6)\n"
    "v_mfma_f32_16x16x32_bf16 v[68:71], v[120:123], v[48:51], v[68:71]\n"
    "ds_read_b128 v[108:111], v30 offset:0\n"
    "v_mfma_f32_16x16x32_bf16 v[68:71], v[124:127], v[52:55], v[68:71]\n"
    "ds_read_b128 v[112:115], v30 offset:64\n"
    "s_nop 1\n"
    "v_mfma_f32_16x16x32_bf16 v[68:71], v[128:131], v[56:59], v[68:71]\n"
    "ds_read_b128 v[116:119], v30 offset:128\n"
    "s_waitcnt lgkmcnt(6)\n"
    "v_mfma_f32_16x16x32_bf16 v[80:83], v[132:135], v[36:39], v[12:15]\n"
    "ds_read_b128 v[120:123], v30 offset:192\n"
    "s_nop 1\n"
    "v_mfma_f32_16x16x32_bf16 v[80:83], v[136:139], v[40:43], v[80:83]\n"
    "ds_read_b128 v[124:127], v30 offset:256\n"
    "v_max3_f32 v6, v6, v68, v69\n"
    "v_mfma_f32_16x16x32_bf16 v[80:83], v[140:143], v[44:47], v[80:83]\n"
    "ds_read_b128 v[128:131], v30 offset:320\n"
    "v_max3_f32 v6, v6, v70, v71\n"
    "s_waitcnt lgkmcnt(6)\n"
    "v_mfma_f32_16x16x32_bf16 v[80:83], v[144:147], v[48:51], v[80:83]\n"
    "ds_read_b128 v[132:135], v31 offset:0\n"
    "v_mfma_f32_16x16x32_bf16 v[80:83], v[148:151], v[52:55], v[80:83]\n"
    "ds_read_b128 v[136:139], v31 offset:64\n"
    "s_nop 1\n"
    "v_mfma_f32_16x16x32_bf16 v[80:83], v[152:155], v[56:59], v[80:83]\n"
    "ds_read_b128 v[140:143], v31 offset:128\n"
    "s_waitcnt lgkmcnt(6)\n"
    "v_mfma_f32_16x16x32_bf16 v[84:87], v[108:111], v[36:39], v[12:15]\n"
    "ds_read_b128 v[144:147], v31 offset:192\n"
    "s_nop 1\n"
    "v_mfma_f32_16x16x32_bf16 v[84:87], v[112:115], v[40:43], v[84:87]\n"
    "ds_read_b128 v[148:151], v31 offset:256\n"
    "v_max3_f32 v6, v6, v80, v81\n"
    "v_mfma_f32_16x16x32_bf16 v[84:87], v[116:119], v[44:47], v[84:87]\n"
    "ds_read_b128 v[152:155], v31 offset:320\n"
    "v_max3_f32 v6, v6, v82, v83\n"
    "s_waitcnt lgkmcnt(6)\n"
    "v_mfma_f32_16x16x32_bf16 v[84:87], v[120:123], v[48:51], v[84:87]\n"
    "v_mfma_f32_16x16x32_bf16 v[84:87], v[124:127], v[52:55], v[84:87]\n"
    "s_nop 2\n"
    "v_mfma_f32_16x16x32_bf16 v[84:87], v[128:131], v[56:59], v[84:87]\n"
    "s_waitcnt lgkmcnt(3)\n"
    "v_mfma_f32_16x16x32_bf16 v[88:91], v[132:135], v[36:39], v[20:23]\n"
    "s_nop 2\n"
    "v_mfma_f32_16x16x32_bf16 v[88:91], v[136:139], v[40:43], v[88:91]\n"
    "v_mfma_f32_16x16x32_bf16 v[88:91], v[140:143], v[44:47], v[88:91]\n"
    "s_nop 1\n"
    "s_waitcnt lgkmcnt(0)\n"
    "v_mfma_f32_16x16x32_bf16 v[88:91], v[144:147], v[48:51], v[88:91]\n"
    "v_max3_f32 v6, v6, v84, v85\n"
    "v_max3_f32 v6, v6, v86, v87\n"
    "v_mfma_f32_16x16x32_bf16 v[88:91], v[148:151], v[52:55], v[88:91]\n"
    "s_nop 2\n"
    "v_mfma_f32_16x16x32_bf16 v[88:91], v[152:155], v[56:59], v[88:91]\n"
    "s_setprio 0\n"
    "v_mov_b32 v24, v28\n"
    "v_mov_b32 v25, v29\n"
    "v_mov_b32 v26, v30\n"
    "v_mov_b32 v27, v31\n"
    "v_lshl_add_u32 v28, s48, 13, v0\n"
    "v_lshl_add_u32 v29, s49, 13, v0\n"
    "s_nop 0\n"
    "v_max3_f32 v6, v6, v88, v89\n"
    "v_max3_f32 v6, v6, v90, v91\n"
    "v_mov_b32 v8, v6\n"
    "v_lshl_add_u32 v30, s50, 13, v0\n"
    "v_lshl_add_u32 v31, s51, 13, v0\n"
    "v_permlane16_swap_b32 v6, v8\n"
    "v_max_f32 v6, v6, v8\n"
    "v_mov_b32 v8, v6\n"
    "s_nop 1\n"
    "v_permlane32_swap_b32 v6, v8\n"
    "v_max_f32 v6, v6, v8\n"
    "v_fma_f32 v7, v6, s46, v92\n"
    "v_fma_f32 v60, v60, s47, v7\n"
    "v_fma_f32 v61, v61, s47, v7\n"
    "v_fma_f32 v62, v62, s47, v7\n"
    "v_fma_f32 v63, v63, s47, v7\n"
    "v_fma_f32 v64, v64, s47, v7\n"
    "v_fma_f32 v65, v65, s47, v7\n"
    "v_fma_f32 v66, v66, s47, v7\n"
    "v_fma_f32 v67, v67, s47, v7\n"
    "v_exp_f32 v60, v60\n"
    "v_exp_f32 v61, v61\n"
    "v_exp_f32 v62, v62\n"
    "v_exp_f32 v63, v63\n"
    "v_exp_f32 v64, v64\n"
    "v_exp_f32 v65, v65\n"
    "v_exp_f32 v66, v66\n"
    "v_exp_f32 v67, v67\n"
    "v_cvt_pk_bf16_f32 v60, v60, v61\n"
    "v_cvt_pk_bf16_f32 v61, v62, v63\n"
    "v_cvt_pk_bf16_f32 v62, v64, v65\n"
    "v_cvt_pk_bf16_f32 v63, v66, v67\n"
    "v_fma_f32 v68, v68, s47, v7\n"
    "v_fma_f32 v69, v69, s47, v7\n"
    "v_fma_f32 v70, v70, s47, v7\n"
    "v_fma_f32 v71, v71, s47, v7\n"
    "v_fma_f32 v72, v72, s47, v7\n"
    "v_fma_f32 v73, v73, s47, v7\n"
    "v_fma_f32 v74, v74, s47, v7\n"
    "v_fma_f32 v75, v75, s47, v7\n"
    "v_exp_f32 v68, v68\n"
    "v_exp_f32 v69, v69\n"
    "v_exp_f32 v70, v70\n"
    "v_exp_f32 v71, v71\n"
    "v_exp_f32 v72, v72\n"
    "v_exp_f32 v73, v73\n"
    "v_exp_f32 v74, v74\n"
    "v_exp_f32 v75, v75\n"
    "v_cvt_pk_bf16_f32 v68, v68, v69\n"
    "v_cvt_pk_bf16_f32 v69, v70, v71\n"
    "v_cvt_pk_bf16_f32 v70, v72, v73\n"
    "v_cvt_pk_bf16_f32 v71, v74, v75\n"
    "s_add_u32 s52, s45, -1\n"
    "s_cmp_lg_u32 s57, 0\n"
    "s_cbranch_scc1 .La_e1\n"
    ".La_ed1:\n"
    "s_cmp_gt_u32 s52, 2\n"
    "s_cbranch_scc1 .La_x1\n"
    "s_add_u32 s54, s40, s42\n"
    "s_addc_u32 s55, s41, 0\n"
    "s_mov_b32 m0, s44\n"
    "s_add_u32 s42, s42, s77\n"
    "global_load_lds_dwordx4 v3, s[54:55]\n"
    "s_add_u32 m0, s44, 1024\n"
    "s_min_u32 s42, s42, s43\n"
    "global_load_lds_dwordx4 v4, s[54:55]\n"
    "s_add_u32 s44, s44, 8192\n"
    "s_add_u32 s54, s40, s42\n"
    "s_addc_u32 s55, s41, 0\n"
    "s_mov_b32 m0, s44\n"
    "s_add_u32 s42, s42, s77\n"
    "global_load_lds_dwordx4 v3, s[54:55]\n"
    "s_add_u32 m0, s44, 1024\n"
    "s_min_u32 s42, s42, s43\n"
    "global_load_lds_dwordx4 v4, s[54:55]\n"
    "s_add_u32 s44, s44, 8192\n"
    "s_cmp_eq_u32 s52, 0\n"
    "s_cbranch_scc1 .La_pf1\n"
    ".La_pd1:\n"
    "s_waitcnt vmcnt(8)\n"
    "s_cmp_lg_u32 s52, 2\n"
    "s_cbranch_scc1 .La_wd1\n"
    "s_waitcnt vmcnt(6)\n"
    ".La_wd1:\n"
    "s_waitcnt lgkmcnt(0)\n"
    "s_barrier\n"
    "s_setprio 1\n"
    "v_mfma_f32_16x16x32_bf16 v[108:111], v[156:159], v[60:63], 0\n"
    "ds_read_b64_tr_b16 v[156:157], v32 offset:0\n"
    "ds_read_b64_tr_b16 v[158:159], v33 offset:0\n"
    "v_fma_f32 v76, v76, s47, v7\n"
    "v_fma_f32 v77, v77, s47, v7\n"
    "v_mfma_f32_16x16x32_bf16 v[112:115], v[160:163], v[60:63], 0\n"
    "ds_read_b64_tr_b16 v[160:161], v32 offset:32\n"
    "ds_read_b64_tr_b16 v[162:163], v33 offset:32\n"
    "v_fma_f32 v78, v78, s47, v7\n"
    "v_fma_f32 v79, v79, s47, v7\n"
    "v_mfma_f32_16x16x32_bf16 v[116:119], v[164:167], v[60:63], 0\n"
    "ds_read_b64_tr_b16 v[164:165], v32 offset:64\n"
    "ds_read_b64_tr_b16 v[166:167], v33 offset:64\n"
    "v_fma_f32 v80, v80, s47, v7\n"
    "v_fma_f32 v81, v81, s47, v7\n"
    "v_mfma_f32_16x16x32_bf16 v[120:123], v[168:171], v[60:63], 0\n"
    "ds_read_b64_tr_b16 v[168:169], v32 offset:96\n"
    "ds_read_b64_tr_b16 v[170:171], v33 offset:96\n"
    "v_fma_f32 v82, v82, s47, v7\n"
    "v_fma_f32 v83, v83, s47, v7\n"
    "v_mfma_f32_16x16x32_bf16 v[124:127], v[172:175], v[60:63], 0\n"
    "ds_read_b64_tr_b16 v[172:173], v32 offset:128\n"
    "ds_read_b64_tr_b16 v[174:175], v33 offset:128\n"
    "v_exp_f32 v76, v76\n"
    "v_mfma_f32_16x16x32_bf16 v[128:131], v[176:179], v[60:63], 0\n"
    "ds_read_b64_tr_b16 v[176:177], v32 offset:160\n"
    "ds_read_b64_tr_b16 v[178:179], v33 offset:160\n"
    "v_exp_f32 v77, v77\n"
    "v_mfma_f32_16x16x32_bf16 v[132:135], v[180:183], v[60:63], 0\n"
    "ds_read_b64_tr_b16 v[180:181], v32 offset:192\n"
    "ds_read_b64_tr_b16 v[182:183], v33 offset:192\n"
    "v_exp_f32 v78, v78\n"
    "v_mfma_f32_16x16x32_bf16 v[136:139], v[184:187], v[60:63], 0\n"
    "s_waitcnt lgkmcnt(8)\n"
    "ds_read_b64_tr_b16 v[184:185], v32 offset:224\n"
    "ds_read_b64_tr_b16 v[186:187], v33 offset:224\n"
    "v_exp_f32 v79, v79\n"
    "v_mfma_f32_16x16x32_bf16 v[140:143], v[188:191], v[60:63], 0\n"
    "ds_read_b64_tr_b16 v[188:189], v32 offset:256\n"
    "ds_read_b64_tr_b16 v[190:191], v33 offset:256\n"
    "v_exp_f32 v80, v80\n"
    "v_mfma_f32_16x16x32_bf16 v[144:147], v[192:195], v[60:63], 0\n"
    "ds_read_b64_tr_b16 v[192:193], v32 offset:288\n"
    "ds_read_b64_tr_b16 v[194:195], v33 offset:288\n"
    "v_exp_f32 v81, v81\n"
    "v_mfma_f32_16x16x32_bf16 v[148:151], v[196:199], v[60:63], 0\n"
    "s_waitcnt lgkmcnt(8)\n"
    "ds_read_b64_tr_b16 v[196:197], v32 offset:320\n"
    "ds_read_b64_tr_b16 v[198:199], v33 offset:320\n"
    "v_exp_f32 v82, v82\n"
    "v_mfma_f32_16x16x32_bf16 v[152:155], v[200:203], v[60:63], 0\n"
    "ds_read_b64_tr_b16 v[200:201], v32 offset:352\n"
    "ds_read_b64_tr_b16 v[202:203], v33 offset:352\n"
    "v_exp_f32 v83, v83\n"
    "v_mfma_f32_16x16x32_bf16 v[8:11], v[252:255], v[60:63], 0\n"
    "v_cvt_pk_bf16_f32 v76, v76, v77\n"
    "v_cvt_pk_bf16_f32 v77, v78, v79\n"
    "v_mfma_f32_16x16x32_bf16 v[108:111], v[204:207], v[68:71], v[108:111]\n"
    "ds_read_b64_tr_b16 v[204:205], v34 offset:0\n"
    "ds_read_b64_tr_b16 v[206:207], v35 offset:0\n"
    "v_cvt_pk_bf16_f32 v78, v80, v81\n"
    "v_cvt_pk_bf16_f32 v79, v82, v83\n"
    "v_mfma_f32_16x16x32_bf16 v[112:115], v[208:211], v[68:71], v[112:115]\n"
    "s_waitcnt lgkmcnt(8)\n"
    "ds_read_b64_tr_b16 v[208:209], v34 offset:32\n"
    "ds_read_b64_tr_b16 v[210:211], v35 offset:32\n"
    "v_fma_f32 v84, v84, s47, v7\n"
    "v_fma_f32 v85, v85, s47, v7\n"
    "v_mfma_f32_16x16x32_bf16 v[116:119], v[212:215], v[68:71], v[116:119]\n"
    "ds_read_b64_tr_b16 v[212:213], v34 offset:64\n"
    "ds_read_b64_tr_b16 v[214:215], v35 offset:64\n"
    "v_fma_f32 v86, v86, s47, v7\n"
    "v_fma_f32 v87, v87, s47, v7\n"
    "v_mfma_f32_16x16x32_bf16 v[120:123], v[216:219], v[68:71], v[120:123]\n"
    "ds_read_b64_tr_b16 v[216:217], v34 offset:96\n"
    "ds_read_b64_tr_b16 v[218:219], v35 offset:96\n"
    "v_fma_f32 v88, v88, s47, v7\n"
    "v_fma_f32 v89, v89, s47, v7\n"
    "v_mfma_f32_16x16x32_bf16 v[124:127], v[220:223], v[68:71], v[124:127]\n"
    "s_waitcnt lgkmcnt(8)\n"
    "ds_read_b64_tr_b16 v[220:221], v34 offset:128\n"
    "ds_read_b64_tr_b16 v[222:223], v35 offset:128\n"
    "v_fma_f32 v90, v90, s47, v7\n"
    "v_fma_f32 v91, v91, s47, v7\n"
    "v_mfma_f32_16x16x32_bf16 v[128:131], v[224:227], v[68:71], v[128:131]\n"
    "ds_read_b64_tr_b16 v[224:225], v34 offset:160\n"
    "ds_read_b64_tr_b16 v[226:227], v35 offset:160\n"
    "v_exp_f32 v84, v84\n"
    "v_mfma_f32_16x16x32_bf16 v[132:135], v[228:231], v[68:71], v[132:135]\n"
    "ds_read_b64_tr_b16 v[228:229], v34 offset:192\n"
    "ds_read_b64_tr_b16 v[230:231], v35 offset:192\n"
    "v_exp_f32 v85, v85\n"
    "v_mfma_f32_16x16x32_bf16 v[136:139], v[232:235], v[68:71], v[136:139]\n"
    "s_waitcnt lgkmcnt(8)\n"
    "ds_read_b64_tr_b16 v[232:233], v34 offset:224\n"
    "ds_read_b64_tr_b16 v[234:235], v35 offset:224\n"
    "v_exp_f32 v86, v86\n"
    "v_mfma_f32_16x16x32_bf16 v[140:143], v[236:239], v[68:71], v[140:143]\n"
    "ds_read_b64_tr_b16 v[236:237], v34 offset:256\n"
    "ds_read_b64_tr_b16 v[238:239], v35 offset:256\n"
    "v_exp_f32 v87, v87\n"
    "v_mfma_f32_16x16x32_bf16 v[144:147], v[240:243], v[68:71], v[144:147]\n"
    "ds_read_b64_tr_b16 v[240:241], v34 offset:288\n"
    "ds_read_b64_tr_b16 v[242:243], v35 offset:288\n"
    "v_exp_f32 v88, v88\n"
    "v_mfma_f32_16x16x32_bf16 v[148:151], v[244:247], v[68:71], v[148:151]\n"
    "s_waitcnt lgkmcnt(8)\n"
    "ds_read_b64_tr_b16 v[244:245], v34 offset:320\n"
    "ds_read_b64_tr_b16 v[246:247], v35 offset:320\n"
    "v_exp_f32 v89, v89\n"
    "v_mfma_f32_16x16x32_bf16 v[152:155], v[248:251], v[68:71], v[152:155]\n"
    "ds_read_b64_tr_b16 v[248:249], v34 offset:352\n"
    "ds_read_b64_tr_b16 v[250:251], v35 offset:352\n"
    "v_exp_f32 v90, v90\n"
    "v_mfma_f32_16x16x32_bf16 v[8:11], v[252:255], v[68:71], v[8:11]\n"
    "v_exp_f32 v91, v91\n"
    "v_mfma_f32_16x16x32_bf16 v[8:11], v[252:255], v[76:79], v[8:11]\n"
    "v_cvt_pk_bf16_f32 v84, v84, v85\n"
    "v_cvt_pk_bf16_f32 v85, v86, v87\n"
    "s_setprio 0\n"
    "v_cvt_pk_bf16_f32 v86, v88, v89\n"
    "v_cvt_pk_bf16_f32 v87, v90, v91\n"
    "s_setprio 1\n"
    "s_nop 0\n"
    "v_mfma_f32_16x16x32_bf16 v[8:11], v[252:255], v[84:87], v[8:11]\n"
    "v_mfma_f32_16x16x32_bf16 v[108:111], v[156:159], v[76:79], v[108:111]\n"
    "v_mfma_f32_16x16x32_bf16 v[108:111], v[204:207], v[84:87], v[108:111]\n"
    "v_mfma_f32_16x16x32_bf16 v[112:115], v[160:163], v[76:79], v[112:115]\n"
    "v_mfma_f32_16x16x32_bf16 v[112:115], v[208:211], v[84:87], v[112:115]\n"
    "v_mfma_f32_16x16x32_bf16 v[116:119], v[164:167], v[76:79], v[116:119]\n"
    "v_mfma_f32_16x16x32_bf16 v[116:119], v[212:215], v[84:87], v[116:119]\n"
    "v_mfma_f32_16x16x32_bf16 v[120:123], v[168:171], v[76:79], v[120:123]\n"
    "v_mfma_f32_16x16x32_bf16 v[120:123], v[216:219], v[84:87], v[120:123]\n"
    "v_rcp_f32 v8, v8\n"
    "s_waitcnt lgkmcnt(8)\n"
    "v_mfma_f32_16x16x32_bf16 v[124:127], v[172:175], v[76:79], v[124:127]\n"
    "ds_write_b32 v93, v8 offset:0\n"
    "v_mfma_f32_16x16x32_bf16 v[124:127], v[220:223], v[84:87], v[124:127]\n"
    "v_cvt_pk_bf16_f32 v108, v108, v109\n"
    "v_cvt_pk_bf16_f32 v109, v110, v111\n"
    "v_mfma_f32_16x16x32_bf16 v[128:131], v[176:179], v[76:79], v[128:131]\n"
    "v_cvt_pk_bf16_f32 v110, v112, v113\n"
    "v_cvt_pk_bf16_f32 v111, v114, v115\n"
    "v_mfma_f32_16x16x32_bf16 v[128:131], v[224:227], v[84:87], v[128:131]\n"
    "ds_write_b128 v2, v[108:111] offset:24576\n"
    "s_waitcnt lgkmcnt(6)\n"
    "v_mfma_f32_16x16x32_bf16 v[132:135], v[180:183], v[76:79], v[132:135]\n"
    "v_cvt_pk_bf16_f32 v116, v116, v117\n"
    "v_cvt_pk_bf16_f32 v117, v118, v119\n"
    "v_mfma_f32_16x16x32_bf16 v[132:135], v[228:231], v[84:87], v[132:135]\n"
    "v_cvt_pk_bf16_f32 v118, v120, v121\n"
    "v_cvt_pk_bf16_f32 v119, v122, v123\n"
    "v_mfma_f32_16x16x32_bf16 v[136:139], v[184:187], v[76:79], v[136:139]\n"
    "ds_write_b128 v2, v[116:119] offset:25600\n"
    "v_mfma_f32_16x16x32_bf16 v[136:139], v[232:235], v[84:87], v[136:139]\n"
    "ds_read_b128 v[108:111], v27 offset:0\n"
    "ds_read_b128 v[112:115], v27 offset:64\n"
    "s_waitcnt lgkmcnt(5)\n"
    "v_mfma_f32_16x16x32_bf16 v[140:143], v[188:191], v[76:79], v[140:143]\n"
    "v_cvt_pk_bf16_f32 v124, v124, v125\n"
    "v_cvt_pk_bf16_f32 v125, v126, v127\n"
    "v_mfma_f32_16x16x32_bf16 v[140:143], v[236:239], v[84:87], v[140:143]\n"
    "v_cvt_pk_bf16_f32 v126, v128, v129\n"
    "v_cvt_pk_bf16_f32 v127, v130, v131\n"
    "v_mfma_f32_16x16x32_bf16 v[144:147], v[192:195], v[76:79], v[144:147]\n"
    "ds_write_b128 v2, v[124:127] offset:26624\n"
    "v_mfma_f32_16x16x32_bf16 v[144:147], v[240:243], v[84:87], v[144:147]\n"
    "ds_read_b128 v[116:119], v27 offset:128\n"
    "ds_read_b128 v[120:123], v27 offset:192\n"
    "v_mfma_f32_16x16x32_bf16 v[148:151], v[196:199], v[76:79], v[148:151]\n"
    "v_cvt_pk_bf16_f32 v132, v132, v133\n"
    "v_cvt_pk_bf16_f32 v133, v134, v135\n"
    "v_mfma_f32_16x16x32_bf16 v[148:151], v[244:247], v[84:87], v[148:151]\n"
    "v_cvt_pk_bf16_f32 v134, v136, v137\n"
    "v_cvt_pk_bf16_f32 v135, v138, v139\n"
    "v_mfma_f32_16x16x32_bf16 v[152:155], v[200:203], v[76:79], v[152:155]\n"
    "ds_write_b128 v2, v[132:135] offset:27648\n"
    "v_mfma_f32_16x16x32_bf16 v[152:155], v[248:251], v[84:87], v[152:155]\n"
    "ds_read_b128 v[124:127], v27 offset:256\n"
    "ds_read_b128 v[128:131], v27 offset:320\n"
    "s_setprio 0\n"
    "v_cvt_pk_bf16_f32 v140, v140, v141\n"
    "v_cvt_pk_bf16_f32 v141, v142, v143\n"
    "v_cvt_pk_bf16_f32 v142, v144, v145\n"
    "v_cvt_pk_bf16_f32 v143, v146, v147\n"
    "ds_write_b128 v2, v[140:143] offset:28672\n"
    "ds_read_b128 v[132:135], v28 offset:0\n"
    "ds_read_b128 v[136:139], v28 offset:64\n"
    "v_cvt_pk_bf16_f32 v148, v148, v149\n"
    "v_cvt_pk_bf16_f32 v149, v150, v151\n"
    "v_cvt_pk_bf16_f32 v150, v152, v153\n"
    "v_cvt_pk_bf16_f32 v151, v154, v155\n"
    "ds_write_b128 v2, v[148:151] offset:29696\n"
    "s_setprio 1\n"
    "s_add_u32 s45, s45, 2\n"
    "s_cmp_lt_u32 s45, 7\n"
    "s_cbranch_scc1 .La_loop\n"
    "s_waitcnt lgkmcnt(0)\n"
    "s_barrier\n"
    "s_cmp_eq_u32 s45, 8\n"
    "s_cbranch_scc1 .La_end\n"
    "s_cmp_eq_u32 s57, 0\n"
    "s_cbranch_scc0 .La_lead\n"
    "s_barrier\n"
    ".La_end:\n"
    "s_endpgm\n"
    ".La_e0:\n"
    "s_cmp_lt_i32 s52, 1\n"
    "s_cbranch_scc1 .La_ed0\n"
    "s_cmp_eq_u32 s57, 1\n"
    "s_cbranch_scc1 .La_ei0\n"
    "s_waitcnt vmcnt(0)\n"
    "v_add_u32 v106, 0x7f4a7c15, v106\n"
    "v_add_u32 v107, 0x9e3779b9, v107\n"
    "v_cmp_ne_u64 s[64:65], v[106:107], v[96:97]\n"
    "v_cmp_ne_u64 s[66:67], v[106:107], v[100:101]\n"
    "v_cmp_ne_u64 s[68:69], v[106:107], v[102:103]\n"
    "v_cmp_ne_u64 s[70:71], v[106:107], v[104:105]\n"
    "s_nop 3\n"
    "s_or_b64 s[64:65], s[64:65], s[66:67]\n"
    "s_or_b64 s[68:69], s[68:69], s[70:71]\n"
    "s_or_b64 s[64:65], s[64:65], s[68:69]\n"
    "s_cbranch_scc1 .La_ei0\n"
    "v_mov_b32 v104, v106\n"
    "v_mov_b32 v105, v107\n"
    "global_store_dwordx4 v94, v[104:107], s[62:63] sc1\n"
    "s_nop 1\n"
    "s_mov_b32 s57, 0\n"
    "s_branch .La_ed0\n"
    ".La_ei0:\n"
    "v_mbcnt_lo_u32_b32 v95, -1, 0\n"
    "v_mbcnt_hi_u32_b32 v95, -1, v95\n"
    "v_and_b32 v94, 7, v95\n"
    "v_lshlrev_b32 v94, 4, v94\n"
    "v_lshlrev_b32 v95, 9, v95\n"
    "v_add_u32 v95, 393216, v95\n"
    "v_mov_b32 v99, 0\n"
    "global_load_dwordx2 v[106:107], v99, s[62:63] sc1\n"
    "global_load_dwordx2 v[96:97], v95, s[62:63] offset:0 sc1\n"
    "global_load_dwordx2 v[100:101], v95, s[62:63] offset:128 sc1\n"
    "global_load_dwordx2 v[102:103], v95, s[62:63] offset:256 sc1\n"
    "global_load_dwordx2 v[104:105], v95, s[62:63] offset:384 sc1\n"
    "s_mov_b32 s57, 2\n"
    "s_branch .La_ed0\n"
    ".La_pf0:\n"
    "s_mov_b32 s53, s42\n"
    "global_load_dword v98, v102, s[40:41]\n"
    "global_load_dword v98, v103, s[40:41]\n"
    "s_branch .La_pd0\n"
    ".La_w80:\n"
    "s_waitcnt vmcnt(8)\n"
    "s_branch .La_wd0\n"
    ".La_x0:\n"
    "s_cmp_eq_i32 s52, -2\n"
    "s_cbranch_scc1 .La_wd0\n"
    "s_cmp_eq_i32 s52, -1\n"
    "s_cbranch_scc1 .La_y0\n"
    "s_waitcnt vmcnt(4)\n"
    "s_cmp_eq_i32 s52, 3\n"
    "s_cbranch_scc1 .La_wd0\n"
    "s_waitcnt vmcnt(0)\n"
    "s_branch .La_wd0\n"
    ".La_y0:\n"
    "s_branch .La_w80\n"
    ".La_e1:\n"
    "s_cmp_lt_i32 s52, 1\n"
    "s_cbranch_scc1 .La_ed1\n"
    "s_cmp_eq_u32 s57, 1\n"
    "s_cbranch_scc1 .La_ei1\n"
    "s_waitcnt vmcnt(0)\n"
    "v_add_u32 v106, 0x7f4a7c15, v106\n"
    "v_add_u32 v107, 0x9e3779b9, v107\n"
    "v_cmp_ne_u64 s[64:65], v[106:107], v[96:97]\n"
    "v_cmp_ne_u64 s[66:67], v[106:107], v[100:101]\n"
    "v_cmp_ne_u64 s[68:69], v[106:107], v[102:103]\n"
    "v_cmp_ne_u64 s[70:71], v[106:107], v[104:105]\n"
    "s_nop 3\n"
    "s_or_b64 s[64:65], s[64:65], s[66:67]\n"
    "s_or_b64 s[68:69], s[68:69], s[70:71]\n"
    "s_or_b64 s[64:65], s[64:65], s[68:69]\n"
    "s_cbranch_scc1 .La_ei1\n"
    "v_mov_b32 v104, v106\n"
    "v_mov_b32 v105, v107\n"
    "global_store_dwordx4 v94, v[104:107], s[62:63] sc1\n"
    "s_nop 1\n"
    "s_mov_b32 s57, 0\n"
    "s_branch .La_ed1\n"
    ".La_ei1:\n"
    "v_mbcnt_lo_u32_b32 v95, -1, 0\n"
    "v_mbcnt_hi_u32_b32 v95, -1, v95\n"
    "v_and_b32 v94, 7, v95\n"
    "v_lshlrev_b32 v94, 4, v94\n"
    "v_lshlrev_b32 v95, 9, v95\n"
    "v_add_u32 v95, 393216, v95\n"
    "v_mov_b32 v99, 0\n"
    "global_load_dwordx2 v[106:107], v99, s[62:63] sc1\n"
    "global_load_dwordx2 v[96:97], v95, s[62:63] offset:0 sc1\n"
    "global_load_dwordx2 v[100:101], v95, s[62:63] offset:128 sc1\n"
    "global_load_dwordx2 v[102:103], v95, s[62:63] offset:256 sc1\n"
    "global_load_dwordx2 v[104:105], v95, s[62:63] offset:384 sc1\n"
    "s_mov_b32 s57, 2\n"
    "s_branch .La_ed1\n"
    ".La_pf1:\n"
    "s_mov_b32 s53, s42\n"
    "global_load_dword v98, v102, s[40:41]\n"
    "global_load_dword v98, v103, s[40:41]\n"
    "s_branch .La_pd1\n"
    ".La_w81:\n"
    "s_waitcnt vmcnt(8)\n"
    "s_branch .La_wd1\n"
    ".La_x1:\n"
    "s_cmp_eq_i32 s52, -2\n"
    "s_cbranch_scc1 .La_wd1\n"
    "s_cmp_eq_i32 s52, -1\n"
    "s_cbranch_scc1 .La_y1\n"
    "s_waitcnt vmcnt(4)\n"
    "s_cmp_eq_i32 s52, 3\n"
    "s_cbranch_scc1 .La_wd1\n"
    "s_waitcnt vmcnt(0)\n"
    "s_branch .La_wd1\n"
    ".La_y1:\n"
    "s_mul_i32 s53, s75, 10\n"
    "s_add_u32 s53, s53, s74\n"
    "s_add_u32 m0, s44, 81920\n"
    "s_add_u32 s54, s40, s53\n"
    "s_addc_u32 s55, s41, 0\n"
    "global_load_lds_dwordx4 v3, s[54:55]\n"
    "s_addk_i32 m0, 1024\n"
    "s_nop 0\n"
    "global_load_lds_dwordx4 v4, s[54:55]\n"
    "s_mul_i32 s53, s75, 11\n"
    "s_add_u32 s53, s53, s74\n"
    "s_add_u32 m0, s44, 90112\n"
    "s_add_u32 s54, s40, s53\n"
    "s_addc_u32 s55, s41, 0\n"
    "global_load_lds_dwordx4 v3, s[54:55]\n"
    "s_addk_i32 m0, 1024\n"
    "s_nop 0\n"
    "global_load_lds_dwordx4 v4, s[54:55]\n"
    "s_mul_i32 s53, s75, 12\n"
    "s_add_u32 s53, s53, s74\n"
    "s_add_u32 m0, s44, 98304\n"
    "s_add_u32 s54, s40, s53\n"
    "s_addc_u32 s55, s41, 0\n"
    "global_load_lds_dwordx4 v3, s[54:55]\n"
    "s_addk_i32 m0, 1024\n"
    "s_nop 0\n"
    "global_load_lds_dwordx4 v4, s[54:55]\n"
    "s_mul_i32 s53, s75, 13\n"
    "s_add_u32 s53, s53, s74\n"
    "s_add_u32 m0, s44, 106496\n"
    "s_add_u32 s54, s40, s53\n"
    "s_addc_u32 s55, s41, 0\n"
    "global_load_lds_dwordx4 v3, s[54:55]\n"
    "s_addk_i32 m0, 1024\n"
    "s_nop 0\n"
    "global_load_lds_dwordx4 v4, s[54:55]\n"
    "s_branch .La_w81\n"
    ".La_lead:\n"
    "s_barrier\n"
    "s_mov_b32 s50, 0x40000\n"
    ".La_lp:\n"
    "s_waitcnt vmcnt(0)\n"
    "s_sub_u32 s50, s50, 1\n"
    "s_cmp_eq_u32 s50, 0\n"
    "s_cbranch_scc1 .La_end\n"
    "s_sleep 4\n"
    "v_mbcnt_lo_u32_b32 v95, -1, 0\n"
    "v_mbcnt_hi_u32_b32 v95, -1, v95\n"
    "v_and_b32 v94, 7, v95\n"
    "v_lshlrev_b32 v94, 4, v94\n"
    "v_lshlrev_b32 v95, 9, v95\n"
    "v_add_u32 v95, 393216, v95\n"
    "v_mov_b32 v99, 0\n"
    "global_load_dwordx2 v[106:107], v99, s[62:63] sc1\n"
    "global_load_dwordx2 v[96:97], v95, s[62:63] offset:0 sc1\n"
    "global_load_dwordx2 v[100:101], v95, s[62:63] offset:128 sc1\n"
    "global_load_dwordx2 v[102:103], v95, s[62:63] offset:256 sc1\n"
    "global_load_dwordx2 v[104:105], v95, s[62:63] offset:384 sc1\n"
    "s_waitcnt vmcnt(0)\n"
    "v_add_u32 v106, 0x7f4a7c15, v106\n"
    "v_add_u32 v107, 0x9e3779b9, v107\n"
    "v_cmp_ne_u64 s[64:65], v[106:107], v[96:97]\n"
    "v_cmp_ne_u64 s[66:67], v[106:107], v[100:101]\n"
    "v_cmp_ne_u64 s[68:69], v[106:107], v[102:103]\n"
    "v_cmp_ne_u64 s[70:71], v[106:107], v[104:105]\n"
    "s_nop 3\n"
    "s_or_b64 s[64:65], s[64:65], s[66:67]\n"
    "s_or_b64 s[68:69], s[68:69], s[70:71]\n"
    "s_or_b64 s[64:65], s[64:65], s[68:69]\n"
    "s_cbranch_scc1 .La_lp\n"
    "v_mov_b32 v104, v106\n"
    "v_mov_b32 v105, v107\n"
    "global_store_dwordx4 v94, v[104:107], s[62:63] sc1\n"
    "s_nop 1\n"
    "s_branch .La_end\n"
    ".La_last:\n"
            :: "{v[0:3]}"(va), "{v4}"(xo[1]), "{v[12:15]}"(cbM), "{v[16:19]}"(cbF), "{v[20:23]}"(cbL),
               "{v[24:27]}"(sa0), "{v[28:31]}"(sa1), "{v[92:93]}"(vm), "{v[102:103]}"(vf), "{s60}"(UNI(slots)), "{s61}"(UNI(slots >> 32)),
               "{s[40:41]}"(xb), "{s42}"(UNI(axis_at(rm, 14) * IROWB)), "{s43}"(UNI(rm.edge ? (rm.x + 4) * IROWB : 0x7fffffff)),
               "{s44}"(ldsbase + 2048 * a), "{s45}"(P), "{s46}"(-0.10411754112863712f), "{s47}"(0.10411754112863712f),
               "{s57}"(UNI(bid == 254 && a == 3)), "{s[62:63]}"(ws),
               "{s74}"(UNI(rm.o * IROWB)), "{s75}"(UNI(rm.d * IROWB)), "{s76}"(UNI(pf < 72)), "{s77}"(UNI((rm.edge ? 2 : rm.d) * IROWB))
               , "{v5}"(stp), "{s[58:59]}"(dbg), "{s72}"(pt0), "{s73}"(UNI(est[0]))
            : "memory", "v255", "s48", "s49", "s50", "s51", "s52", "s53", "s54", "s55", "s56", "s71", "s90", "s91", "vcc", "scc");
    }
}

extern "C" void kernel_launch(void* const* d_in, const int* in_sizes, int n_in,
                              void* d_out, int out_size, void* d_ws, size_t ws_size,
                              hipStream_t stream) {
    na_main<<<256, 512, 0, stream>>>((const u16*)d_in[0], (const u16*)d_in[1], (const u16*)d_in[2], (u16*)d_out, (char*)d_ws);
}
```

```cpp
#include <hip/hip_runtime.h>
#include <stdint.h>

typedef unsigned short u16;
typedef unsigned long long u64;
typedef __attribute__((ext_vector_type(4))) float f32x4;
typedef __attribute__((ext_vector_type(4))) unsigned u32x4;
typedef __attribute__((ext_vector_type(2))) unsigned u32x2;
typedef __attribute__((ext_vector_type(8))) unsigned u32x8;

#define DEVINL __device__ __forceinline__
#define LAMBDA_INL __attribute__((always_inline))
#define UNI(x) __builtin_amdgcn_readfirstlane((int)(x))

static constexpr int IMW = 224;
static constexpr int PIXB = 384;
static constexpr int IROWB = IMW * PIXB;
static constexpr int RP = 8896;
static constexpr int LQ = 14 * RP, LZ = LQ + 3 * 6144, LF = LZ + 3 * 6144;
static constexpr int LDS_TOT = 163840;
static constexpr unsigned OOB = 0x100000u;
static constexpr int WS_W = 8192, WS_FL = WS_W + 144 * 1024, NREP = 8, WS_ACK = 393216;

DEVINL int pixad(int c) { return 800 * ((c + 1) >> 1) + 384 * ((c + 1) & 1) - 384; }
struct AxisMap { int o, d, x, last; };
DEVINL int axis_at(const AxisMap& m, int v) {
    const int reg = m.o + m.d * v, ext = m.x + 2 * min(v - m.last - 1, 2), sel = (m.last - v) >> 31;
    return (ext & sel) | (reg & ~sel);
}
DEVINL int scol(int i) { return (int)((0xD5B3E6C4A2F79180ull >> (4 * i)) & 15); }

struct AttC { unsigned sb, v0, v1, pk; };
DEVINL AttC att_const(int lane, int H, int cedge) {
    const int i16 = lane & 15, g = lane >> 4, e = (i16 + 4) >> 3 & 1, qj = i16 - 4 * ((i16 + 4) >> 3), sc = scol(i16);
    const int ws0 = (cedge ? min(8 * H + qj, 9) : 8 * H + qj) - 8 * H;
    AttC c;
    c.pk = 8 | e << 8;
#pragma unroll
    for (int r = 0; r < 4; ++r) {
        const unsigned ok = (unsigned)(scol(4 * g + r) - ws0) <= 6u;
        c.pk |= ok << (4 + r) | (r & 1 ? ok << (r - 1) : 0u);
    }
    c.sb = pixad(8 * H + (sc < 14 ? sc : sc - 8)) + 16 * g;
    c.v0 = pixad(8 * H + scol(4 * (g >> 1) + ((lane >> 2) & 3))) + (g & 1) * RP + 8 * (lane & 3);
    c.v1 = pixad(8 * H + scol(8 + 4 * (g >> 1) + ((lane >> 2) & 3))) + (g & 1) * RP + 8 * (lane & 3);
    return c;
}

__global__ __launch_bounds__(512, 2) void na_main(const u16* __restrict__ x_, const u16* __restrict__ wqkv_,
                                                  const u16* __restrict__ wo_, u16* __restrict__ out_, char* ws_) {
    __shared__ __attribute__((aligned(1024))) char smem[LDS_TOT];
    int tid = threadIdx.x, bid = blockIdx.x;
    u32x8 ka;
    u32x2 ka4;
    u32x4 ax, xo, xq;
    unsigned xo4, xq4, t0;
    asm volatile(
    "s_load_dwordx8 s[8:15], s[0:1], 0x0\n"
    "s_load_dwordx2 s[16:17], s[0:1], 0x20\n"
    "s_memtime s[4:5]\n"
    "v_readfirstlane_b32 s7, v0\n"
    "s_lshr_b32 s24, s2, 3\n"
    "v_and_b32 v6, 63, v0\n"
    "s_getpc_b64 s[28:29]\n"
    ".Le_pc:\n"
    "s_lshl_b32 s25, s24, 10\n"
    "s_mov_b32 m0, 162816\n"
    "s_cmp_lg_u32 s7, 128\n"
    "s_cbranch_scc1 .Le_ncp\n"
    "v_lshl_add_u32 v7, v6, 4, s25\n"
    "global_load_lds_dwordx4 v7, s[28:29]\n"
    ".Le_ncp:\n"
    "s_lshr_b32 s27, s2, 6\n"
    "s_and_b32 s26, s24, 7\n"
    "s_lshr_b32 s7, s7, 6\n"
    "s_add_u32 s3, s27, 14\n"
    "s_cmp_eq_u32 s26, 7\n"
    "s_cselect_b32 s3, s3, 99\n"
    "s_cmp_lt_u32 s24, 16\n"
    "s_cselect_b32 s3, s24, s3\n"
    "s_cmp_lg_u32 s7, 7\n"
    "s_cbranch_scc1 .Le_np\n"
    "s_cmp_ge_u32 s3, 18\n"
    "s_cbranch_scc1 .Le_7\n"
    "s_and_b32 s23, s2, 3\n"
    "s_mul_i32 s23, s23, 18\n"
    "s_add_u32 s3, s3, s23\n"
    "s_mul_i32 s44, s3, 43\n"
    "s_lshr_b32 s44, s44, 8\n"
    "s_mul_i32 s25, s44, 6\n"
    "s_sub_u32 s25, s3, s25\n"
    "v_lshrrev_b32 v4, 2, v6\n"
    "v_lshrrev_b32 v5, 4, v6\n"
    "v_sub_u32 v3, 0, v5\n"
    "v_xor_b32 v3, v3, v6\n"
    "v_and_b32 v3, 3, v3\n"
    "v_lshlrev_b32 v3, 4, v3\n"
    "s_mul_i32 s25, s25, 36864\n"
    "s_movk_i32 s23, 1152\n"
    "s_lshr_b32 s42, s44, 1\n"
    "s_and_b32 s43, s44, 1\n"
    "v_mov_b32 v9, 0\n"
    "s_mov_b32 m0, 124544\n"
    "s_bitcmp1_b32 s2, 2\n"
    "s_cbranch_scc1 .Le_r2\n"
    "v_lshl_add_u32 v7, v5, 2, v4\n"
    "v_mad_u32_u24 v7, v7, s23, v3\n"
    "s_lshl3_add_u32 s44, s42, s43\n"
    "s_mul_i32 s44, s44, 4608\n"
    "s_addk_i32 s44, 384\n"
    "s_mov_b32 s23, 6\n"
    "s_waitcnt lgkmcnt(0)\n"
    "s_add_u32 s26, s10, s25\n"
    "s_addc_u32 s27, s11, 0\n"
    "global_load_dwordx2 v[12:13], v9, s[16:17] sc1\n"
    "s_add_u32 s28, s26, 4608\n"
    "s_addc_u32 s29, s27, 0\n"
    "s_add_u32 s30, s10, s44\n"
    "s_addc_u32 s31, s11, 0\n"
    ".Le_l1:\n"
    "global_load_lds_dwordx4 v7, s[26:27]\n"
    "s_addk_i32 m0, 1024\n"
    "s_sub_u32 s23, s23, 1\n"
    "global_load_lds_dwordx4 v7, s[28:29]\n"
    "s_addk_i32 m0, 1024\n"
    "s_nop 0\n"
    "global_load_lds_dwordx4 v7, s[30:31]\n"
    "s_addk_i32 m0, 1024\n"
    "s_cmp_lg_u32 s23, 0\n"
    "v_add_u32 v7, 64, v7\n"
    "s_cbranch_scc1 .Le_l1\n"
    "s_branch .Le_p1\n"
    ".Le_r2:\n"
    "v_bfe_u32 v100, v6, 2, 2\n"
    "v_and_b32 v7, 1, v5\n"
    "v_lshl_add_u32 v7, v7, 4, v100\n"
    "v_lshrrev_b32 v16, 5, v6\n"
    "v_lshl_add_u32 v7, v16, 3, v7\n"
    "v_mad_u32_u24 v7, v7, s23, v3\n"
    "v_and_b32 v19, 3, v6\n"
    "v_mul_u32_u24 v16, 24, v4\n"
    "v_add_lshl_u32 v16, v16, v19, 4\n"
    "s_lshl_b32 s31, s42, 6\n"
    "s_addk_i32 s25, 768\n"
    "s_mov_b32 s23, 6\n"
    "s_waitcnt lgkmcnt(0)\n"
    "s_add_u32 s26, s10, s25\n"
    "s_addc_u32 s27, s11, 0\n"
    "global_load_dwordx2 v[12:13], v9, s[16:17] sc1\n"
    "s_add_u32 s28, s26, 4608\n"
    "s_addc_u32 s29, s27, 0\n"
    "s_add_u32 s30, s12, s31\n"
    "s_addc_u32 s31, s13, 0\n"
    ".Le_l2:\n"
    "global_load_lds_dwordx4 v7, s[26:27]\n"
    "s_addk_i32 m0, 1024\n"
    "s_sub_u32 s23, s23, 1\n"
    "global_load_lds_dwordx4 v7, s[28:29]\n"
    "s_addk_i32 m0, 1024\n"
    "v_add_u32 v7, 64, v7\n"
    "global_load_lds_dwordx4 v16, s[30:31]\n"
    "s_addk_i32 m0, 1024\n"
    "v_add_u32 v16, 6144, v16\n"
    "global_load_lds_dwordx4 v16, s[30:31]\n"
    "s_addk_i32 m0, 1024\n"
    "v_add_u32 v16, 6144, v16\n"
    "s_cmp_lg_u32 s23, 0\n"
    "s_cbranch_scc1 .Le_l2\n"
    "s_branch .Le_p2\n"
    ".Le_np:\n"
    "s_cmp_eq_u32 s7, 4\n"
    "s_cbranch_scc1 .Le_a\n"
    ".Le_xo:\n"
    "s_and_b32 s25, s2, 1\n"
    "s_lshr_b32 s23, 0x14a, s7\n"
    "s_and_b32 s23, s23, 1\n"
    "s_lshl2_add_u32 s29, s25, s27\n"
    "s_mul_i32 s22, s23, 486\n"
    "s_cmp_eq_u32 s26, 7\n"
    "s_cselect_b32 s26, s29, s26\n"
    "s_cselect_b32 s31, 1, s27\n"
    "s_cselect_b32 s18, 1, 0\n"
    "s_cmp_eq_u32 s26, 7\n"
    "s_cbranch_scc1 .Le_sp\n"
    "v_lshl_add_u32 v1, s23, 6, v6\n"
    "v_add_u32 v5, s22, v6\n"
    "s_movk_i32 s22, 1311\n"
    "v_add_u32 v2, 64 + 24 + 128, v1\n"
    "v_add_u32 v3, 64 + 24 + 256, v1\n"
    "v_add_u32 v4, 64 + 24 + 384, v1\n"
    "v_add_u32 v1, 64 + 24, v1\n"
    "v_add_u32 v5, 24, v5\n"
    "v_mul_u32_u24 v7, s22, v1\n"
    "s_lshl_b32 s29, s26, 5\n"
    "v_mul_u32_u24 v8, s22, v2\n"
    "s_sub_u32 s20, 222, s29\n"
    "v_mul_u32_u24 v9, s22, v3\n"
    "s_add_u32 s29, s29, 1\n"
    "v_mul_u32_u24 v10, s22, v4\n"
    "s_bitcmp1_b32 s2, 1\n"
    "v_mul_u32_u24 v11, s22, v5\n"
    "s_cselect_b32 s20, s29, s20\n"
    "v_not_b32 v12, 49\n"
    "v_lshrrev_b32 v7, 16, v7\n"
    "s_cselect_b32 s21, 2, -2\n"
    "v_lshrrev_b32 v8, 16, v8\n"
    "s_cselect_b32 s30, 218, 1\n"
    "v_lshrrev_b32 v9, 16, v9\n"
    "v_lshrrev_b32 v10, 16, v10\n"
    "v_lshrrev_b32 v11, 16, v11\n"
    "s_sub_u32 s29, s20, s21\n"
    "s_mul_i32 s29, s29, 384\n"
    "s_mul_i32 s22, s21, 768\n"
    "v_mad_i32_i24 v1, v7, v12, v1\n"
    "s_lshl_b32 s28, s27, 3\n"
    "v_mad_i32_i24 v2, v8, v12, v2\n"
    "s_lshr_b32 s28, 0x1837321d, s28\n"
    "v_mad_i32_i24 v3, v9, v12, v3\n"
    "s_and_b32 s28, s28, 0xff\n"
    "v_mad_i32_i24 v4, v10, v12, v4\n"
    "v_mad_i32_i24 v5, v11, v12, v5\n"
    "v_mov_b32 v18, s29\n"
    "s_add_u32 s29, s21, -1\n"
    "s_mul_i32 s29, s29, 384\n"
    "v_add_u32 v19, s29, v18\n"
    "v_min_u32 v1, 47, v1\n"
    "s_mul_i32 s29, s25, 50\n"
    "v_min_u32 v2, 47, v2\n"
    "s_add_u32 s28, s28, s29\n"
    "v_min_u32 v3, 47, v3\n"
    "s_cmp_lg_u32 s18, 0\n"
    "v_min_u32 v4, 47, v4\n"
    "s_cselect_b32 s28, 0, s28\n"
    "v_min_u32 v5, 47, v5\n"
    "v_cmp_lt_u32 s[42:43], 23, v1\n"
    "v_cmp_lt_u32 s[44:45], 23, v2\n"
    "v_cmp_lt_u32 s[46:47], 23, v3\n"
    "v_cmp_lt_u32 s[48:49], 23, v4\n"
    "v_cmp_lt_u32 s[50:51], 23, v5\n"
    "s_lshl_b32 s28, s28, 1\n"
    "s_sub_u32 s18, 222, s28\n"
    "s_add_u32 s28, s28, 1\n"
    "s_lshr_b32 s29, s2, 2\n"
    "v_cndmask_b32 v13, v18, v19, s[42:43]\n"
    "v_cndmask_b32 v14, v18, v19, s[44:45]\n"
    "v_cndmask_b32 v15, v18, v19, s[46:47]\n"
    "v_cndmask_b32 v16, v18, v19, s[48:49]\n"
    "v_cndmask_b32 v17, v18, v19, s[50:51]\n"
    "s_xor_b32 s31, s29, s31\n"
    "s_bitcmp1_b32 s2, 2\n"
    "s_cselect_b32 s18, s28, s18\n"
    "v_mad_i32_i24 v13, v7, s22, v13\n"
    "v_mad_i32_i24 v14, v8, s22, v14\n"
    "v_mad_i32_i24 v15, v9, s22, v15\n"
    "v_mad_i32_i24 v16, v10, s22, v16\n"
    "v_mad_i32_i24 v17, v11, s22, v17\n"
    "s_bitcmp1_b32 s31, 0\n"
    "s_cselect_b32 s19, -2, 2\n"
    "s_cmp_eq_u32 s26, 6\n"
    "s_cbranch_scc1 .Le_ce\n"
    ".Le_cd:\n"
    "v_lshl_add_u32 v1, v1, 4, v13\n"
    "v_lshl_add_u32 v2, v2, 4, v14\n"
    "v_lshl_add_u32 v3, v3, 4, v15\n"
    "v_lshl_add_u32 v4, v4, 4, v16\n"
    "v_lshl_add_u32 v5, v5, 4, v17\n"
    "s_cmp_lt_u32 s7, 4\n"
    "s_cbranch_scc1 .Le_b\n"
    "s_cmp_ge_u32 s7, 7\n"
    "s_cbranch_scc1 .Le_72\n"
    "s_mul_i32 s25, s19, 3\n"
    "s_add_u32 s25, s25, s18\n"
    "s_mul_i32 s25, s25, 86016\n"
    "s_mul_i32 s28, s19, 86016\n"
    "s_lshl_b32 s24, s23, 10\n"
    "s_addk_i32 s24, 27712\n"
    "s_mov_b32 s29, 6\n"
    "s_cmp_lt_u32 s3, 18\n"
    "s_cselect_b32 s31, 3, -1\n"
    "s_cselect_b32 s30, 2, 0\n"
    "s_bitcmp1_b32 s2, 2\n"
    "s_cselect_b32 s31, -1, s31\n"
    "s_cselect_b32 s30, s30, 0\n"
    "s_or_b32 s30, s30, s23\n"
    "s_barrier\n"
    "s_waitcnt lgkmcnt(0)\n"
    ".Le_xl:\n"
    "s_cmp_eq_u32 s29, s31\n"
    "s_cbranch_scc1 .Le_h\n"
    ".Le_go:\n"
    "s_mov_b32 m0, s24\n"
    "s_add_u32 s26, s8, s25\n"
    "s_addc_u32 s27, s9, 0\n"
    "global_load_lds_dwordx4 v1, s[26:27]\n"
    "s_addk_i32 m0, 2048\n"
    "s_add_u32 s25, s25, s28\n"
    "global_load_lds_dwordx4 v2, s[26:27]\n"
    "s_addk_i32 m0, 2048\n"
    "s_add_u32 s24, s24, 8896\n"
    "global_load_lds_dwordx4 v3, s[26:27]\n"
    "s_addk_i32 m0, 2048\n"
    "s_cmp_lg_u32 s23, 0\n"
    "s_cbranch_scc1 .Le_x3\n"
    "global_load_lds_dwordx4 v4, s[26:27]\n"
    ".Le_x3:\n"
    "s_sub_u32 s29, s29, 1\n"
    "s_cmp_lg_u32 s29, 0\n"
    "s_cbranch_scc1 .Le_xl\n"
    "s_branch .Le_g2s\n"
    ".Le_h:\n"
    "v_mov_b32 v16, 161456\n"
    "ds_read_b32 v7, v16\n"
    "s_waitcnt lgkmcnt(0)\n"
    "v_cmp_gt_u32 vcc, 1, v7\n"
    "s_cbranch_vccnz .Le_h\n"
    "s_branch .Le_go\n"
    ".Le_a:\n"
    "s_add_u32 s25, s25, 32768\n"
    "s_mov_b32 s23, .Lg_last-.Le_pc\n"
    "s_max_u32 s23, s23, .Lh_last-.Le_pc\n"
    "s_max_u32 s23, s23, .La_last-.Le_pc\n"
    "s_max_u32 s23, s23, .Ls_last-.Le_pc\n"
    "s_min_u32 s25, s25, s23\n"
    "v_lshl_add_u32 v7, v6, 4, s25\n"
    "global_load_lds_dwordx4 v7, s[28:29]\n"
    "s_cmp_lt_u32 s3, 18\n"
    "s_cselect_b32 s23, 0, 8\n"
    "v_lshrrev_b32 v10, 1, v6\n"
    "v_cmp_eq_u32 vcc, 1, v10\n"
    "v_lshrrev_b32 v7, 2, v6\n"
    "v_lshrrev_b32 v8, v7, s23\n"
    "v_mov_b32 v9, 0\n"
    "v_and_b32 v8, 1, v8\n"
    "v_lshlrev_b32 v8, 1, v8\n"
    "v_cndmask_b32 v8, v8, 7, vcc\n"
    "v_cmp_eq_u32 vcc, 5, v10\n"
    "v_lshlrev_b32 v7, 2, v6\n"
    "v_add_u32 v7, 161408, v7\n"
    "v_cndmask_b32 v8, v8, 15, vcc\n"
    "ds_write_b32 v7, v9 offset:64\n"
    "ds_write_b32 v7, v9 offset:768\n"
    "ds_write_b32 v7, v8\n"
    "s_waitcnt lgkmcnt(0)\n"
    "s_branch .Le_b\n"
    ".Le_ce:\n"
    "s_movk_i32 s29, 384\n"
    "v_cndmask_b32 v20, 0, 1, s[42:43]\n"
    "v_lshl_add_u32 v21, v7, 1, v20\n"
    "v_cmp_lt_u32 vcc, 16, v21\n"
    "v_add_u32 v22, -17, v21\n"
    "v_min_u32 v22, 2, v22\n"
    "v_lshl_add_u32 v22, v22, 1, s30\n"
    "v_sub_u32 v22, v22, v20\n"
    "v_mul_i32_i24 v22, s29, v22\n"
    "v_cndmask_b32 v13, v13, v22, vcc\n"
    "v_cndmask_b32 v20, 0, 1, s[44:45]\n"
    "v_lshl_add_u32 v21, v8, 1, v20\n"
    "v_cmp_lt_u32 vcc, 16, v21\n"
    "v_add_u32 v22, -17, v21\n"
    "v_min_u32 v22, 2, v22\n"
    "v_lshl_add_u32 v22, v22, 1, s30\n"
    "v_sub_u32 v22, v22, v20\n"
    "v_mul_i32_i24 v22, s29, v22\n"
    "v_cndmask_b32 v14, v14, v22, vcc\n"
    "v_cndmask_b32 v20, 0, 1, s[46:47]\n"
    "v_lshl_add_u32 v21, v9, 1, v20\n"
    "v_cmp_lt_u32 vcc, 16, v21\n"
    "v_add_u32 v22, -17, v21\n"
    "v_min_u32 v22, 2, v22\n"
    "v_lshl_add_u32 v22, v22, 1, s30\n"
    "v_sub_u32 v22, v22, v20\n"
    "v_mul_i32_i24 v22, s29, v22\n"
    "v_cndmask_b32 v15, v15, v22, vcc\n"
    "v_cndmask_b32 v20, 0, 1, s[48:49]\n"
    "v_lshl_add_u32 v21, v10, 1, v20\n"
    "v_cmp_lt_u32 vcc, 16, v21\n"
    "v_add_u32 v22, -17, v21\n"
    "v_min_u32 v22, 2, v22\n"
    "v_lshl_add_u32 v22, v22, 1, s30\n"
    "v_sub_u32 v22, v22, v20\n"
    "v_mul_i32_i24 v22, s29, v22\n"
    "v_cndmask_b32 v16, v16, v22, vcc\n"
    "v_cndmask_b32 v20, 0, 1, s[50:51]\n"
    "v_lshl_add_u32 v21, v11, 1, v20\n"
    "v_cmp_lt_u32 vcc, 16, v21\n"
    "v_add_u32 v22, -17, v21\n"
    "v_min_u32 v22, 2, v22\n"
    "v_lshl_add_u32 v22, v22, 1, s30\n"
    "v_sub_u32 v22, v22, v20\n"
    "v_mul_i32_i24 v22, s29, v22\n"
    "v_cndmask_b32 v17, v17, v22, vcc\n"
    "s_branch .Le_cd\n"
    ".Le_p1:\n"
    "s_barrier\n"
    "v_bfe_u32 v8, v6, 2, 2\n"
    "v_sub_u32 v8, 0, v8\n"
    "v_xor_b32 v8, v8, v5\n"
    "v_and_b32 v8, 3, v8\n"
    "v_lshlrev_b32 v8, 4, v8\n"
    "v_and_b32 v10, 15, v6\n"
    "v_lshl_add_u32 v8, v10, 6, v8\n"
    "v_add_u32 v8, 124544, v8\n"
    "s_waitcnt vmcnt(9)\n"
    "v_add_u32 v12, 0x7f4a7c15, v12\n"
    "v_add_u32 v13, 0x9e3779b9, v13\n"
    "s_lshl_b32 s23, s3, 10\n"
    "s_add_u32 s23, s23, 8192\n"
    "v_lshl_add_u32 v9, v6, 4, s23\n"
    "s_lshl_b32 s23, s3, 7\n"
    "s_add_u32 s23, s23, 155648\n"
    "v_and_b32 v11, 7, v6\n"
    "v_mov_b32 v17, 2\n"
    "ds_read_b128 v[20:23], v8 offset:0\n"
    "ds_read_b128 v[24:27], v8 offset:1024\n"
    "ds_read_b128 v[28:31], v8 offset:2048\n"
    "ds_read_b128 v[32:35], v8 offset:3072\n"
    "ds_read_b128 v[36:39], v8 offset:4096\n"
    "ds_read_b128 v[40:43], v8 offset:5120\n"
    "ds_read_b128 v[44:47], v8 offset:6144\n"
    "ds_read_b128 v[48:51], v8 offset:7168\n"
    "ds_read_b128 v[52:55], v8 offset:8192\n"
    "s_waitcnt vmcnt(6)\n"
    "ds_read_b128 v[56:59], v8 offset:9216\n"
    "ds_read_b128 v[60:63], v8 offset:10240\n"
    "ds_read_b128 v[64:67], v8 offset:11264\n"
    "s_waitcnt lgkmcnt(9)\n"
    "v_mfma_f32_16x16x32_bf16 v[92:95], v[20:23], v[28:31], 0\n"
    "v_mfma_f32_16x16x32_bf16 v[96:99], v[24:27], v[28:31], 0\n"
    "s_waitcnt lgkmcnt(6)\n"
    "v_mfma_f32_16x16x32_bf16 v[92:95], v[32:35], v[40:43], v[92:95]\n"
    "v_mfma_f32_16x16x32_bf16 v[96:99], v[36:39], v[40:43], v[96:99]\n"
    "s_waitcnt vmcnt(3)\n"
    "ds_read_b128 v[68:71], v8 offset:12288\n"
    "ds_read_b128 v[72:75], v8 offset:13312\n"
    "ds_read_b128 v[76:79], v8 offset:14336\n"
    "s_waitcnt lgkmcnt(6)\n"
    "v_mfma_f32_16x16x32_bf16 v[92:95], v[44:47], v[52:55], v[92:95]\n"
    "v_mfma_f32_16x16x32_bf16 v[96:99], v[48:51], v[52:55], v[96:99]\n"
    "s_waitcnt lgkmcnt(3)\n"
    "v_mfma_f32_16x16x32_bf16 v[92:95], v[56:59], v[64:67], v[92:95]\n"
    "v_mfma_f32_16x16x32_bf16 v[96:99], v[60:63], v[64:67], v[96:99]\n"
    "s_waitcnt vmcnt(0)\n"
    "ds_read_b128 v[80:83], v8 offset:15360\n"
    "ds_read_b128 v[84:87], v8 offset:16384\n"
    "ds_read_b128 v[88:91], v8 offset:17408\n"
    "s_waitcnt lgkmcnt(3)\n"
    "v_mfma_f32_16x16x32_bf16 v[92:95], v[68:71], v[76:79], v[92:95]\n"
    "v_mfma_f32_16x16x32_bf16 v[96:99], v[72:75], v[76:79], v[96:99]\n"
    "s_waitcnt lgkmcnt(0)\n"
    "v_mfma_f32_16x16x32_bf16 v[92:95], v[80:83], v[88:91], v[92:95]\n"
    "v_mfma_f32_16x16x32_bf16 v[96:99], v[84:87], v[88:91], v[96:99]\n"
    "s_nop 7\n"
    "s_branch .Le_pub1\n"
    ".Le_pub:\n"
    "s_lshl_b32 s23, s3, 10\n"
    "s_add_u32 s23, s23, 8192\n"
    "v_lshl_add_u32 v9, v6, 4, s23\n"
    "s_lshl_b32 s23, s3, 7\n"
    "s_add_u32 s23, s23, 155648\n"
    "v_and_b32 v11, 7, v6\n"
    "v_mov_b32 v17, 2\n"
    "s_nop 1\n"
    ".Le_pub1:\n"
    "v_cvt_pk_bf16_f32 v92, v92, v93\n"
    "v_cvt_pk_bf16_f32 v93, v94, v95\n"
    "v_cvt_pk_bf16_f32 v94, v96, v97\n"
    "v_cvt_pk_bf16_f32 v95, v98, v99\n"
    "v_add_u32 v92, v92, v12\n"
    "global_store_dwordx4 v9, v[92:95], s[16:17] sc1\n"
    "v_add_u32 v14, v92, v95\n"
    "v_add_u32 v15, v92, v95\n"
    "v_lshlrev_b32 v18, 2, v11\n"
    "v_and_b32 v18, 12, v18\n"
    "v_add_u32 v18, 161456, v18\n"
    "v_lshrrev_b32 v10, 3, v6\n"
    "ds_write_b32 v18, v17\n"
    "v_mul_u32_u24 v10, 18432, v10\n"
    "v_lshl_add_u32 v10, v11, 4, v10\n"
    "v_add_u32 v10, s23, v10\n"
    "v_permlane32_swap_b32 v14, v15\n"
    "v_add_u32 v14, v14, v15\n"
    "s_nop 1\n"
    "v_add_u32_dpp v14, v14, v14 row_ror:8 row_mask:0xf bank_mask:0xf\n"
    "s_nop 1\n"
    "v_add_u32_dpp v14, v14, v14 row_ror:4 row_mask:0xf bank_mask:0xf\n"
    "s_nop 1\n"
    "v_add_u32_dpp v14, v14, v14 row_ror:2 row_mask:0xf bank_mask:0xf\n"
    "s_nop 1\n"
    "v_add_u32_dpp v14, v14, v14 row_ror:1 row_mask:0xf bank_mask:0xf\n"
    "v_mov_b32 v15, v14\n"
    "s_nop 1\n"
    "v_permlane16_swap_b32 v14, v15\n"
    "v_add_u32 v14, v14, v15\n"
    "global_store_dwordx4 v10, v[12:15], s[16:17] sc1\n"
    "s_branch .Le_71\n"
    ".Le_p2:\n"
    "s_barrier\n"
    "v_bfe_u32 v8, v6, 2, 2\n"
    "v_sub_u32 v8, 0, v8\n"
    "v_xor_b32 v8, v8, v5\n"
    "v_and_b32 v8, 3, v8\n"
    "v_lshlrev_b32 v8, 4, v8\n"
    "v_and_b32 v10, 15, v6\n"
    "v_lshl_add_u32 v8, v10, 6, v8\n"
    "v_add_u32 v8, 124544, v8\n"
    "v_lshl_add_u32 v7, v5, 3, v100\n"
    "v_lshlrev_b32 v7, 6, v7\n"
    "v_lshl_add_u32 v7, v19, 4, v7\n"
    "s_lshl_b32 s23, s43, 3\n"
    "s_add_u32 s23, s23, 126592\n"
    "v_add_u32 v7, s23, v7\n"
    "s_add_u32 s3, s3, 72\n"
    "s_waitcnt vmcnt(0)\n"
    "v_add_u32 v12, 0x7f4a7c15, v12\n"
    "v_add_u32 v13, 0x9e3779b9, v13\n"
    "ds_read_b128 v[20:23], v8 offset:0\n"
    "ds_read_b128 v[24:27], v8 offset:1024\n"
    "ds_read_b64_tr_b16 v[28:29], v7 offset:0\n"
    "ds_read_b64_tr_b16 v[30:31], v7 offset:256\n"
    "ds_read_b128 v[32:35], v8 offset:4096\n"
    "ds_read_b128 v[36:39], v8 offset:5120\n"
    "ds_read_b64_tr_b16 v[40:41], v7 offset:4096\n"
    "ds_read_b64_tr_b16 v[42:43], v7 offset:4352\n"
    "ds_read_b128 v[44:47], v8 offset:8192\n"
    "ds_read_b128 v[48:51], v8 offset:9216\n"
    "ds_read_b64_tr_b16 v[52:53], v7 offset:8192\n"
    "ds_read_b64_tr_b16 v[54:55], v7 offset:8448\n"
    "ds_read_b128 v[56:59], v8 offset:12288\n"
    "ds_read_b128 v[60:63], v8 offset:13312\n"
    "ds_read_b64_tr_b16 v[64:65], v7 offset:12288\n"
    "ds_read_b64_tr_b16 v[66:67], v7 offset:12544\n"
    "ds_read_b128 v[68:71], v8 offset:16384\n"
    "ds_read_b128 v[72:75], v8 offset:17408\n"
    "ds_read_b64_tr_b16 v[76:77], v7 offset:16384\n"
    "ds_read_b64_tr_b16 v[78:79], v7 offset:16640\n"
    "ds_read_b128 v[80:83], v8 offset:20480\n"
    "ds_read_b128 v[84:87], v8 offset:21504\n"
    "ds_read_b64_tr_b16 v[88:89], v7 offset:20480\n"
    "ds_read_b64_tr_b16 v[90:91], v7 offset:20736\n"
    "s_waitcnt lgkmcnt(14)\n"
    "v_mfma_f32_16x16x32_bf16 v[92:95], v[20:23], v[28:31], 0\n"
    "v_mfma_f32_16x16x32_bf16 v[96:99], v[24:27], v[28:31], 0\n"
    "v_mfma_f32_16x16x32_bf16 v[92:95], v[32:35], v[40:43], v[92:95]\n"
    "v_mfma_f32_16x16x32_bf16 v[96:99], v[36:39], v[40:43], v[96:99]\n"
    "s_waitcnt lgkmcnt(12)\n"
    "v_mfma_f32_16x16x32_bf16 v[92:95], v[44:47], v[52:55], v[92:95]\n"
    "v_mfma_f32_16x16x32_bf16 v[96:99], v[48:51], v[52:55], v[96:99]\n"
    "s_waitcnt lgkmcnt(8)\n"
    "v_mfma_f32_16x16x32_bf16 v[92:95], v[56:59], v[64:67], v[92:95]\n"
    "v_mfma_f32_16x16x32_bf16 v[96:99], v[60:63], v[64:67], v[96:99]\n"
    "s_waitcnt lgkmcnt(4)\n"
    "v_mfma_f32_16x16x32_bf16 v[92:95], v[68:71], v[76:79], v[92:95]\n"
    "v_mfma_f32_16x16x32_bf16 v[96:99], v[72:75], v[76:79], v[96:99]\n"
    "s_waitcnt lgkmcnt(0)\n"
    "v_mfma_f32_16x16x32_bf16 v[92:95], v[80:83], v[88:91], v[92:95]\n"
    "v_mfma_f32_16x16x32_bf16 v[96:99], v[84:87], v[88:91], v[96:99]\n"
    "s_branch .Le_pub\n"
    ".Le_7:\n"
    "s_barrier\n"
    ".Le_71:\n"
    "s_lshr_b32 s27, s2, 6\n"
    "s_bfe_u32 s26, s2, 0x30003\n"
    "s_branch .Le_xo\n"
    ".Le_72:\n"
    "s_cmp_eq_u32 s7, 8\n"
    "s_cbranch_scc1 .Le_w\n"
    "s_mov_b32 s7, 8\n"
    "v_mov_b32 v101, v1\n"
    "v_mov_b32 v102, v2\n"
    "v_mov_b32 v103, v3\n"
    "v_mov_b32 v104, v4\n"
    "v_mov_b32 v105, v5\n"
    "s_branch .Le_71\n"
    ".Le_sp:\n"
    "s_cmp_lt_u32 s7, 7\n"
    "s_cbranch_scc1 .Le_b\n"
    "s_branch .Le_w\n"
    ".Le_g2s:\n"
    "s_mul_i32 s44, s18, 86016\n"
    "s_mul_i32 s45, s19, 86016\n"
    "s_lshl_b32 s56, s23, 2\n"
    "s_add_u32 s56, s56, 161408\n"
    "v_lshlrev_b32 v16, 20, v6\n"
    "v_add_u32 v16, s56, v16\n"
    "s_cmp_eq_u32 s30, 1\n"
    "s_cbranch_scc1 .Li_s1\n"
    "s_cmp_eq_u32 s30, 2\n"
    "s_cbranch_scc1 .Li_s2\n"
    "s_cmp_eq_u32 s30, 3\n"
    "s_cbranch_scc1 .Li_s3\n"
    "s_waitcnt vmcnt(16)\n"
    "v_mov_b32 v17, 1\n"
    "ds_write_b32 v16, v17 offset:0\n"
    "s_waitcnt vmcnt(8)\n"
    "v_mov_b32 v17, 2\n"
    "ds_write_b32 v16, v17 offset:0\n"
    "s_mul_i32 s56, s45, 2\n"
    "s_add_u32 s56, s56, s44\n"
    "s_add_u32 s60, s8, s56\n"
    "s_addc_u32 s61, s9, 0\n"
    "s_mov_b32 m0, 18816\n"
    "s_nop 0\n"
    "global_load_lds_dwordx4 v1, s[60:61]\n"
    "s_mov_b32 m0, 20864\n"
    "s_nop 0\n"
    "global_load_lds_dwordx4 v2, s[60:61]\n"
    "s_mov_b32 m0, 22912\n"
    "s_nop 0\n"
    "global_load_lds_dwordx4 v3, s[60:61]\n"
    "s_mov_b32 m0, 24960\n"
    "s_nop 0\n"
    "global_load_lds_dwordx4 v4, s[60:61]\n"
    "s_mov_b32 m0, 17792\n"
    "s_nop 0\n"
    "global_load_lds_dwordx4 v5, s[60:61]\n"
    "s_mul_i32 s56, s45, 1\n"
    "s_add_u32 s56, s56, s44\n"
    "s_add_u32 s60, s8, s56\n"
    "s_addc_u32 s61, s9, 0\n"
    "s_mov_b32 m0, 9920\n"
    "s_nop 0\n"
    "global_load_lds_dwordx4 v1, s[60:61]\n"
    "s_mov_b32 m0, 11968\n"
    "s_nop 0\n"
    "global_load_lds_dwordx4 v2, s[60:61]\n"
    "s_mov_b32 m0, 14016\n"
    "s_nop 0\n"
    "global_load_lds_dwordx4 v3, s[60:61]\n"
    "s_mov_b32 m0, 16064\n"
    "s_nop 0\n"
    "global_load_lds_dwordx4 v4, s[60:61]\n"
    "s_mov_b32 m0, 8896\n"
    "s_nop 0\n"
    "global_load_lds_dwordx4 v5, s[60:61]\n"
    "s_mul_i32 s56, s45, 0\n"
    "s_add_u32 s56, s56, s44\n"
    "s_add_u32 s60, s8, s56\n"
    "s_addc_u32 s61, s9, 0\n"
    "s_mov_b32 m0, 1024\n"
    "s_nop 0\n"
    "global_load_lds_dwordx4 v1, s[60:61]\n"
    "s_mov_b32 m0, 3072\n"
    "s_nop 0\n"
    "global_load_lds_dwordx4 v2, s[60:61]\n"
    "s_mov_b32 m0, 5120\n"
    "s_nop 0\n"
    "global_load_lds_dwordx4 v3, s[60:61]\n"
    "s_mov_b32 m0, 7168\n"
    "s_nop 0\n"
    "global_load_lds_dwordx4 v4, s[60:61]\n"
    "s_mov_b32 m0, 0\n"
    "s_nop 0\n"
    "global_load_lds_dwordx4 v5, s[60:61]\n"
    "s_mul_i32 s56, s45, 3\n"
    "s_add_u32 s56, s56, s44\n"
    "s_add_u32 s60, s8, s56\n"
    "s_addc_u32 s61, s9, 0\n"
    "s_mov_b32 m0, 26688\n"
    "s_nop 0\n"
    "global_load_lds_dwordx4 v5, s[60:61]\n"
    "s_mul_i32 s56, s45, 4\n"
    "s_add_u32 s56, s56, s44\n"
    "s_add_u32 s60, s8, s56\n"
    "s_addc_u32 s61, s9, 0\n"
    "s_mov_b32 m0, 35584\n"
    "s_nop 0\n"
    "global_load_lds_dwordx4 v5, s[60:61]\n"
    "s_mul_i32 s56, s45, 5\n"
    "s_add_u32 s56, s56, s44\n"
    "s_add_u32 s60, s8, s56\n"
    "s_addc_u32 s61, s9, 0\n"
    "s_mov_b32 m0, 44480\n"
    "s_nop 0\n"
    "global_load_lds_dwordx4 v5, s[60:61]\n"
    "s_mul_i32 s56, s45, 6\n"
    "s_add_u32 s56, s56, s44\n"
    "s_add_u32 s60, s8, s56\n"
    "s_addc_u32 s61, s9, 0\n"
    "s_mov_b32 m0, 53376\n"
    "s_nop 0\n"
    "global_load_lds_dwordx4 v5, s[60:61]\n"
    "s_mul_i32 s56, s45, 7\n"
    "s_add_u32 s56, s56, s44\n"
    "s_add_u32 s60, s8, s56\n"
    "s_addc_u32 s61, s9, 0\n"
    "s_mov_b32 m0, 62272\n"
    "s_nop 0\n"
    "global_load_lds_dwordx4 v5, s[60:61]\n"
    "s_mul_i32 s56, s45, 8\n"
    "s_add_u32 s56, s56, s44\n"
    "s_add_u32 s60, s8, s56\n"
    "s_addc_u32 s61, s9, 0\n"
    "s_mov_b32 m0, 71168\n"
    "s_nop 0\n"
    "global_load_lds_dwordx4 v5, s[60:61]\n"
    "s_waitcnt vmcnt(21)\n"
    "v_mov_b32 v17, 3\n"
    "ds_write_b32 v16, v17 offset:0\n"
    "s_waitcnt vmcnt(0)\n"
    "v_mov_b32 v17, 4\n"
    "ds_write_b32 v16, v17 offset:0\n"
    "s_branch .Li_sd\n"
    ".Li_s1:\n"
    "s_waitcnt vmcnt(12)\n"
    "v_mov_b32 v17, 1\n"
    "ds_write_b32 v16, v17 offset:0\n"
    "s_waitcnt vmcnt(6)\n"
    "v_mov_b32 v17, 2\n"
    "ds_write_b32 v16, v17 offset:0\n"
    "s_mul_i32 s56, s45, 2\n"
    "s_add_u32 s56, s56, s44\n"
    "s_add_u32 s60, s8, s56\n"
    "s_addc_u32 s61, s9, 0\n"
    "s_mov_b32 m0, 19840\n"
    "s_nop 0\n"
    "global_load_lds_dwordx4 v1, s[60:61]\n"
    "s_mov_b32 m0, 21888\n"
    "s_nop 0\n"
    "global_load_lds_dwordx4 v2, s[60:61]\n"
    "s_mov_b32 m0, 23936\n"
    "s_nop 0\n"
    "global_load_lds_dwordx4 v3, s[60:61]\n"
    "s_mov_b32 m0, 25568\n"
    "s_nop 0\n"
    "global_load_lds_dwordx4 v5, s[60:61]\n"
    "s_mul_i32 s56, s45, 1\n"
    "s_add_u32 s56, s56, s44\n"
    "s_add_u32 s60, s8, s56\n"
    "s_addc_u32 s61, s9, 0\n"
    "s_mov_b32 m0, 10944\n"
    "s_nop 0\n"
    "global_load_lds_dwordx4 v1, s[60:61]\n"
    "s_mov_b32 m0, 12992\n"
    "s_nop 0\n"
    "global_load_lds_dwordx4 v2, s[60:61]\n"
    "s_mov_b32 m0, 15040\n"
    "s_nop 0\n"
    "global_load_lds_dwordx4 v3, s[60:61]\n"
    "s_mov_b32 m0, 16672\n"
    "s_nop 0\n"
    "global_load_lds_dwordx4 v5, s[60:61]\n"
    "s_mul_i32 s56, s45, 0\n"
    "s_add_u32 s56, s56, s44\n"
    "s_add_u32 s60, s8, s56\n"
    "s_addc_u32 s61, s9, 0\n"
    "s_mov_b32 m0, 2048\n"
    "s_nop 0\n"
    "global_load_lds_dwordx4 v1, s[60:61]\n"
    "s_mov_b32 m0, 4096\n"
    "s_nop 0\n"
    "global_load_lds_dwordx4 v2, s[60:61]\n"
    "s_mov_b32 m0, 6144\n"
    "s_nop 0\n"
    "global_load_lds_dwordx4 v3, s[60:61]\n"
    "s_mov_b32 m0, 7776\n"
    "s_nop 0\n"
    "global_load_lds_dwordx4 v5, s[60:61]\n"
    "s_mul_i32 s56, s45, 3\n"
    "s_add_u32 s56, s56, s44\n"
    "s_add_u32 s60, s8, s56\n"
    "s_addc_u32 s61, s9, 0\n"
    "s_mov_b32 m0, 34464\n"
    "s_nop 0\n"
    "global_load_lds_dwordx4 v5, s[60:61]\n"
    "s_mul_i32 s56, s45, 4\n"
    "s_add_u32 s56, s56, s44\n"
    "s_add_u32 s60, s8, s56\n"
    "s_addc_u32 s61, s9, 0\n"
    "s_mov_b32 m0, 43360\n"
    "s_nop 0\n"
    "global_load_lds_dwordx4 v5, s[60:61]\n"
    "s_mul_i32 s56, s45, 5\n"
    "s_add_u32 s56, s56, s44\n"
    "s_add_u32 s60, s8, s56\n"
    "s_addc_u32 s61, s9, 0\n"
    "s_mov_b32 m0, 52256\n"
    "s_nop 0\n"
    "global_load_lds_dwordx4 v5, s[60:61]\n"
    "s_mul_i32 s56, s45, 6\n"
    "s_add_u32 s56, s56, s44\n"
    "s_add_u32 s60, s8, s56\n"
    "s_addc_u32 s61, s9, 0\n"
    "s_mov_b32 m0, 61152\n"
    "s_nop 0\n"
    "global_load_lds_dwordx4 v5, s[60:61]\n"
    "s_mul_i32 s56, s45, 7\n"
    "s_add_u32 s56, s56, s44\n"
    "s_add_u32 s60, s8, s56\n"
    "s_addc_u32 s61, s9, 0\n"
    "s_mov_b32 m0, 70048\n"
    "s_nop 0\n"
    "global_load_lds_dwordx4 v5, s[60:61]\n"
    "s_mul_i32 s56, s45, 8\n"
    "s_add_u32 s56, s56, s44\n"
    "s_add_u32 s60, s8, s56\n"
    "s_addc_u32 s61, s9, 0\n"
    "s_mov_b32 m0, 78944\n"
    "s_nop 0\n"
    "global_load_lds_dwordx4 v5, s[60:61]\n"
    "s_waitcnt vmcnt(18)\n"
    "v_mov_b32 v17, 3\n"
    "ds_write_b32 v16, v17 offset:0\n"
    "s_waitcnt vmcnt(0)\n"
    "v_mov_b32 v17, 4\n"
    "ds_write_b32 v16, v17 offset:0\n"
    "s_branch .Li_sd\n"
    ".Li_s2:\n"
    "s_waitcnt vmcnt(16)\n"
    "v_mov_b32 v17, 1\n"
    "ds_write_b32 v16, v17 offset:0\n"
    "s_waitcnt vmcnt(8)\n"
    "v_mov_b32 v17, 2\n"
    "ds_write_b32 v16, v17 offset:0\n"
    "s_waitcnt vmcnt(0)\n"
    "v_mov_b32 v17, 3\n"
    "ds_write_b32 v16, v17 offset:0\n"
    "s_branch .Li_sd\n"
    ".Li_s3:\n"
    "s_waitcnt vmcnt(12)\n"
    "v_mov_b32 v17, 1\n"
    "ds_write_b32 v16, v17 offset:0\n"
    "s_waitcnt vmcnt(6)\n"
    "v_mov_b32 v17, 2\n"
    "ds_write_b32 v16, v17 offset:0\n"
    "s_waitcnt vmcnt(0)\n"
    "v_mov_b32 v17, 3\n"
    "ds_write_b32 v16, v17 offset:0\n"
    "s_branch .Li_sd\n"
    ".Li_sd:\n"
    "s_setprio 3\n"
    "s_branch .Le_w\n"
    ".Le_b:\n"
    "s_barrier\n"
    ".Le_w:\n"
    "s_waitcnt lgkmcnt(0)\n"
        :
          "={s[8:15]}"(ka), "={s[16:17]}"(ka4), "={s4}"(t0), "={s18}"(ax[0]), "={s19}"(ax[1]), "={s20}"(ax[2]), "={s21}"(ax[3]),
          "={v1}"(xo[0]), "={v2}"(xo[1]), "={v3}"(xo[2]), "={v4}"(xo[3]), "={v5}"(xo4),
          "={v101}"(xq[0]), "={v102}"(xq[1]), "={v103}"(xq[2]), "={v104}"(xq[3]), "={v105}"(xq4), "+{v0}"(tid), "+{s2}"(bid)
        : "{s[0:1]}"(__builtin_amdgcn_kernarg_segment_ptr())
        : "memory", "scc", "vcc", "s3", "s5", "s7", "s22", "s42", "s43", "s44", "s45", "s46", "s47", "s48", "s49", "s50", "s51", "s87", "s56", "s60", "s61", "s23", "s24", "s25", "s26", "s27", "s28", "s29", "s30", "s31",
          "v6", "v7", "v8", "v9", "v10", "v11", "v12", "v13", "v14", "v15", "v16", "v17", "v18", "v19", "v20", "v21", "v22", "v23", "v24", "v25", "v26", "v27", "v28", "v29", "v30", "v31", "v32", "v33", "v34", "v35", "v36", "v37", "v38", "v39", "v40", "v41", "v42", "v43", "v44", "v45", "v46", "v47", "v48", "v49", "v50", "v51", "v52", "v53", "v54", "v55", "v56", "v57", "v58", "v59", "v60", "v61", "v62", "v63", "v64", "v65", "v66", "v67", "v68", "v69", "v70", "v71", "v72", "v73", "v74", "v75", "v76", "v77", "v78", "v79", "v80", "v81", "v82", "v83", "v84", "v85", "v86", "v87", "v88", "v89", "v90", "v91", "v92", "v93", "v94", "v95", "v96", "v97", "v98", "v99", "v100");
    auto mkptr = [](unsigned lo, unsigned hi) LAMBDA_INL { return (char*)(((u64)(unsigned)UNI(hi) << 32) | (unsigned)UNI(lo)); };
    const char* xb = mkptr(ka[0], ka[1]);
    char* ws = mkptr(ka4[0], ka4[1]);
    bid = UNI(bid);
    int lane = tid & 63;
    const int wave = __builtin_amdgcn_readfirstlane(tid >> 6);
    const int tq = bid >> 6, ts = (bid >> 3) & 7, strip = ts == 7 ? tq + 4 * (bid & 1) : ts;
    const int t12 = tq < 2 || ts == 7, hs = tq == 3 && ts != 7, redge = tq == 1 && ts != 7 && (bid & 1), cedge = strip == 6;
    const AxisMap rm = {UNI(ax[0]), UNI(ax[1]), bid & 4 ? 218 : 1, redge ? 11 : 99};
    const AxisMap cm = {UNI(ax[2]), UNI(ax[3]), bid & 2 ? 218 : 1, cedge ? 15 : 99};
    const unsigned vf = LF + 4 * (lane & 3);
    const u32x4 fl = {vf, lane < 2 ? LF + 4 * (wave & 1) + 8 * lane : OOB, lane < 1 ? LF + 4 * wave : OOB, 0};
    const int i16 = lane & 15, g = lane >> 4;
    const int e = (i16 + 4) >> 3 & 1, qj = i16 - 4 * ((i16 + 4) >> 3);

    if (strip == 7) {
        asm volatile("s_nop 7\n"
    "s_cmp_lg_u32 s46, 6\n"
    "s_cbranch_scc1 .Ls_end\n"
    "v_mov_b32 v1, 0\n"
    "global_load_dwordx2 v[12:13], v1, s[40:41] sc1\n"
    "v_and_b32 v2, 15, v0\n"
    "v_lshlrev_b32 v2, 4, v2\n"
    "s_waitcnt vmcnt(0)\n"
    "v_add_u32 v6, 0x7f4a7c15, v12\n"
    "v_add_u32 v7, 0x9e3779b9, v13\n"
    "v_add_u32 v4, 0x7f4a7c15, v12\n"
    "v_add_u32 v5, 0x9e3779b9, v13\n"
    "v_add_u32 v3, s48, v2\n"
    "global_store_dwordx4 v3, v[4:7], s[40:41] sc1\n"
    "s_cmp_eq_u32 s49, 0\n"
    "s_cbranch_scc1 .Ls_end\n"
    "v_and_b32 v2, 0x70, v2\n"
    "v_lshlrev_b32 v3, 10, v0\n"
    "v_add_u32 v3, 393216, v3\n"
    "s_mov_b32 s52, 0x10000\n"
    "s_sleep 110\n"
    ".Ls_p:\n"
    "s_sleep 8\n"
    "global_load_dwordx2 v[8:9], v3, s[40:41] offset:0 sc1\n"
    "global_load_dwordx2 v[10:11], v3, s[40:41] offset:128 sc1\n"
    "global_load_dwordx2 v[12:13], v3, s[40:41] offset:256 sc1\n"
    "global_load_dwordx2 v[14:15], v3, s[40:41] offset:384 sc1\n"
    "global_load_dwordx2 v[16:17], v3, s[40:41] offset:512 sc1\n"
    "global_load_dwordx2 v[18:19], v3, s[40:41] offset:640 sc1\n"
    "global_load_dwordx2 v[20:21], v3, s[40:41] offset:768 sc1\n"
    "global_load_dwordx2 v[22:23], v3, s[40:41] offset:896 sc1\n"
    "s_mov_b64 s[46:47], 0\n"
    "s_waitcnt vmcnt(0)\n"
    "v_cmp_ne_u64 vcc, v[4:5], v[8:9]\n"
    "s_or_b64 s[46:47], s[46:47], vcc\n"
    "v_cmp_ne_u64 vcc, v[4:5], v[10:11]\n"
    "s_or_b64 s[46:47], s[46:47], vcc\n"
    "v_cmp_ne_u64 vcc, v[4:5], v[12:13]\n"
    "s_or_b64 s[46:47], s[46:47], vcc\n"
    "v_cmp_ne_u64 vcc, v[4:5], v[14:15]\n"
    "s_or_b64 s[46:47], s[46:47], vcc\n"
    "v_cmp_ne_u64 vcc, v[4:5], v[16:17]\n"
    "s_or_b64 s[46:47], s[46:47], vcc\n"
    "v_cmp_ne_u64 vcc, v[4:5], v[18:19]\n"
    "s_or_b64 s[46:47], s[46:47], vcc\n"
    "v_cmp_ne_u64 vcc, v[4:5], v[20:21]\n"
    "s_or_b64 s[46:47], s[46:47], vcc\n"
    "v_cmp_ne_u64 vcc, v[4:5], v[22:23]\n"
    "s_or_b64 s[46:47], s[46:47], vcc\n"
    "s_cbranch_scc0 .Ls_c\n"
    "s_sub_u32 s52, s52, 1\n"
    "s_cmp_lg_u32 s52, 0\n"
    "s_cbranch_scc1 .Ls_p\n"
    ".Ls_c:\n"
    "global_store_dwordx4 v2, v[4:7], s[40:41] sc1\n"
    ".Ls_end:\n"
    "s_endpgm\n"
    ".Ls_last:\n"
            :: "{v0}"(lane), "{s[40:41]}"(ws), "{s46}"(wave), "{s48}"(UNI(WS_ACK + 2 * bid * 128)), "{s49}"(UNI(bid == 253))
            : "memory", "v23", "s47", "s52", "vcc", "scc");
    } else if (wave < 4) {
        const int h = wave & 1;
        const unsigned l16 = lane * 16;
        const int nf = min(lane, 35) + 36 * h;
        const unsigned z0 = LQ + (lane & 3) * 256 + (4 * ((2 * (g >> 1)) ^ (lane & 3)) + ((lane >> 2) & 3)) * 16 + (g & 1) * 8;
        const u32x2 vz = {z0, z0 ^ 64};
        const unsigned vi = LF + 320 + 4 * ((i16 + 8) & 15);
        const unsigned c0 = axis_at(cm, 3 + qj) * PIXB + 16 * g, c1 = axis_at(cm, 11 + qj) * PIXB + 16 * g;
        const u32x2 dd = {(unsigned)((axis_at(rm, 11 + e) - axis_at(rm, 9 + e)) * IROWB), (unsigned)((axis_at(rm, 13 + e) - axis_at(rm, 11 + e)) * IROWB)};
        const unsigned d56 = 2 * rm.d * IROWB + (hs ? c1 - c0 : 0);
        const u32x4 srd = {(unsigned)UNI(ka[6]), (unsigned)UNI(ka[7]) & 0xffffu, (unsigned)(IMW * IROWB), 0x00020000u};
        if (wave >= 2) {
            const unsigned b = pixad(3 + qj) + e * RP + 16 * g;
            const u32x2 va = {b, b + 3200};
            const u32x2 vb = {(unsigned)(e ? -9 * RP : 5 * RP), LQ + l16 + h * 3072};
            const u32x2 lf = {l16, (unsigned)(WS_FL + ((bid & (NREP - 1)) * 144 + nf) * 128)};
            constexpr int N0 = 9, UE = (N0 + 1) / 2, UO = N0 / 2;
            u32x4 to;
            if (h != 0) {
                to = (u32x4){axis_at(rm, 3 + 2 * UE + e) * IROWB + c0, axis_at(rm, 3 + 2 * UO + e) * IROWB + c1, dd[0], dd[1]};
            } else {
                const AttC c = att_const(lane, hs, cedge);
                to = (u32x4){c.sb, c.v0, c.v1, c.pk};
            }
            asm volatile("s_nop 7\n"
    "s_mov_b32 s52, 0x40000\n"
    "s_load_dwordx2 s[78:79], s[40:41], 0x0\n"
    "s_mul_i32 s53, s50, 36864\n"
    "s_add_u32 s57, s53, 11264\n"
    "s_add_u32 s64, s40, s57\n"
    "s_addc_u32 s65, s41, 0\n"
    "s_add_u32 s57, s53, 17408\n"
    "s_add_u32 s66, s40, s57\n"
    "s_addc_u32 s67, s41, 0\n"
    "s_add_u32 s57, s53, 23552\n"
    "s_add_u32 s68, s40, s57\n"
    "s_addc_u32 s69, s41, 0\n"
    "s_add_u32 s57, s53, 29696\n"
    "s_add_u32 s70, s40, s57\n"
    "s_addc_u32 s71, s41, 0\n"
    "s_add_u32 s57, s53, 35840\n"
    "s_add_u32 s72, s40, s57\n"
    "s_addc_u32 s73, s41, 0\n"
    "s_add_u32 s57, s53, 41984\n"
    "s_add_u32 s74, s40, s57\n"
    "s_addc_u32 s75, s41, 0\n"
    "v_add_u32 v4, 71168, v2\n"
    "v_add_u32 v5, 71168, v3\n"
    "s_lshl_b32 s54, s50, 10\n"
    "s_add_u32 s54, s54, 1024\n"
    "s_mul_i32 s56, s50, 7776\n"
    "s_cmp_eq_u32 s50, 0\n"
    "s_cbranch_scc1 .Lg_x0\n"
    "v_mov_b32 v233, v234\n"
    ".Lg_x0:\n"
    "s_max_u32 s55, s56, 7168\n"
    "v_add_u32 v254, -8, v226\n"
    ".Lg_dl:\n"
    "s_sleep 1\n"
    "s_memtime s[76:77]\n"
    "s_waitcnt lgkmcnt(0)\n"
    "s_sub_u32 s76, s76, s59\n"
    "s_cmp_lt_i32 s76, 0\n"
    "s_cbranch_scc1 .Lg_dl\n"
    "s_waitcnt lgkmcnt(0)\n"
    "s_add_u32 s78, s78, 0x7f4a7c15\n"
    "s_add_u32 s79, s79, 0x9e3779b9\n"
    "v_mov_b64 v[240:241], s[78:79]\n"
    "s_mov_b32 s51, 0x4000\n"
    ".Lg_retry:\n"
    "v_mov_b64 v[244:245], v[240:241]\n"
    "v_mov_b64 v[246:247], 0\n"
    "v_mov_b64 v[242:243], 0\n"
    "s_mov_b32 exec_hi, 15\n"
    "global_load_dwordx4 v[244:247], v236, s[40:41] sc1\n"
    "s_mov_b64 exec, -1\n"
    "ds_read_b32 v228, v224 offset:48\n"
    "s_waitcnt lgkmcnt(0)\n"
    "v_cmp_gt_u32 vcc, 1, v228\n"
    "s_cbranch_vccnz .Lg1\n"
    ".Lg1b:\n"
    "global_load_dwordx4 v[8:11], v235, s[64:65] offset:-3072 sc1\n"
    "global_load_dwordx4 v[32:35], v235, s[66:67] offset:-3072 sc1\n"
    "global_load_dwordx4 v[12:15], v235, s[64:65] offset:-2048 sc1\n"
    "global_load_dwordx4 v[36:39], v235, s[66:67] offset:-2048 sc1\n"
    "global_load_dwordx4 v[16:19], v235, s[64:65] offset:-1024 sc1\n"
    "global_load_dwordx4 v[40:43], v235, s[66:67] offset:-1024 sc1\n"
    "global_load_dwordx4 v[20:23], v235, s[64:65] offset:0 sc1\n"
    "global_load_dwordx4 v[44:47], v235, s[66:67] offset:0 sc1\n"
    "global_load_dwordx4 v[24:27], v235, s[64:65] offset:1024 sc1\n"
    "global_load_dwordx4 v[48:51], v235, s[66:67] offset:1024 sc1\n"
    "global_load_dwordx4 v[28:31], v235, s[64:65] offset:2048 sc1\n"
    "global_load_dwordx4 v[52:55], v235, s[66:67] offset:2048 sc1\n"
    "global_load_dwordx4 v[56:59], v235, s[68:69] offset:-3072 sc1\n"
    "global_load_dwordx4 v[80:83], v235, s[70:71] offset:-3072 sc1\n"
    "global_load_dwordx4 v[60:63], v235, s[68:69] offset:-2048 sc1\n"
    "global_load_dwordx4 v[84:87], v235, s[70:71] offset:-2048 sc1\n"
    "global_load_dwordx4 v[64:67], v235, s[68:69] offset:-1024 sc1\n"
    "global_load_dwordx4 v[88:91], v235, s[70:71] offset:-1024 sc1\n"
    "global_load_dwordx4 v[68:71], v235, s[68:69] offset:0 sc1\n"
    "global_load_dwordx4 v[92:95], v235, s[70:71] offset:0 sc1\n"
    "s_waitcnt vmcnt(20)\n"
    "v_cmp_ne_u64 vcc, v[240:241], v[244:245]\n"
    "s_cmp_gt_i32 s51, 0\n"
    "s_cbranch_scc0 .Lg_pd\n"
    "s_cbranch_vccnz .Lg_poll\n"
    ".Lg_pd:\n"
    "ds_read_b32 v228, v224 offset:0\n"
    "s_waitcnt lgkmcnt(0)\n"
    "v_cmp_gt_u32 vcc, 1, v228\n"
    "s_cbranch_vccnz .Lg2\n"
    ".Lg2b:\n"
    "ds_read_b128 v[152:155], v2 offset:26688\n"
    "ds_read_b128 v[156:159], v2 offset:26752\n"
    "ds_read_b128 v[160:163], v2 offset:26816\n"
    "ds_read_b128 v[164:167], v2 offset:26880\n"
    "ds_read_b128 v[168:171], v2 offset:26944\n"
    "ds_read_b128 v[172:175], v2 offset:27008\n"
    "s_waitcnt vmcnt(18)\n"
    "v_add3_u32 v242, v242, v8, v11\n"
    "v_sub_u32 v8, v8, v240\n"
    "v_add3_u32 v242, v242, v32, v35\n"
    "v_sub_u32 v32, v32, v240\n"
    "s_waitcnt vmcnt(16)\n"
    "v_add3_u32 v242, v242, v12, v15\n"
    "v_sub_u32 v12, v12, v240\n"
    "s_waitcnt lgkmcnt(5)\n"
    "v_mfma_f32_16x16x32_bf16 v[200:203], v[8:11], v[152:155], 0\n"
    "global_load_dwordx4 v[72:75], v235, s[68:69] offset:1024 sc1\n"
    "v_add3_u32 v242, v242, v36, v39\n"
    "v_sub_u32 v36, v36, v240\n"
    "v_mfma_f32_16x16x32_bf16 v[204:207], v[32:35], v[152:155], 0\n"
    "global_load_dwordx4 v[96:99], v235, s[70:71] offset:1024 sc1\n"
    "s_waitcnt vmcnt(16)\n"
    "v_add3_u32 v242, v242, v16, v19\n"
    "v_sub_u32 v16, v16, v240\n"
    "s_waitcnt lgkmcnt(4)\n"
    "v_mfma_f32_16x16x32_bf16 v[200:203], v[12:15], v[156:159], v[200:203]\n"
    "global_load_dwordx4 v[76:79], v235, s[68:69] offset:2048 sc1\n"
    "v_add3_u32 v242, v242, v40, v43\n"
    "v_sub_u32 v40, v40, v240\n"
    "v_mfma_f32_16x16x32_bf16 v[204:207], v[36:39], v[156:159], v[204:207]\n"
    "global_load_dwordx4 v[100:103], v235, s[70:71] offset:2048 sc1\n"
    "s_waitcnt vmcnt(16)\n"
    "v_add3_u32 v242, v242, v20, v23\n"
    "v_sub_u32 v20, v20, v240\n"
    "s_waitcnt lgkmcnt(3)\n"
    "v_mfma_f32_16x16x32_bf16 v[200:203], v[16:19], v[160:163], v[200:203]\n"
    "global_load_dwordx4 v[104:107], v235, s[72:73] offset:-3072 sc1\n"
    "v_add3_u32 v242, v242, v44, v47\n"
    "v_sub_u32 v44, v44, v240\n"
    "v_mfma_f32_16x16x32_bf16 v[204:207], v[40:43], v[160:163], v[204:207]\n"
    "global_load_dwordx4 v[128:131], v235, s[74:75] offset:-3072 sc1\n"
    "s_waitcnt vmcnt(16)\n"
    "v_add3_u32 v242, v242, v24, v27\n"
    "v_sub_u32 v24, v24, v240\n"
    "s_waitcnt lgkmcnt(2)\n"
    "v_mfma_f32_16x16x32_bf16 v[200:203], v[20:23], v[164:167], v[200:203]\n"
    "global_load_dwordx4 v[108:111], v235, s[72:73] offset:-2048 sc1\n"
    "v_add3_u32 v242, v242, v48, v51\n"
    "v_sub_u32 v48, v48, v240\n"
    "v_mfma_f32_16x16x32_bf16 v[204:207], v[44:47], v[164:167], v[204:207]\n"
    "global_load_dwordx4 v[132:135], v235, s[74:75] offset:-2048 sc1\n"
    "s_waitcnt vmcnt(16)\n"
    "v_add3_u32 v242, v242, v28, v31\n"
    "v_sub_u32 v28, v28, v240\n"
    "s_waitcnt lgkmcnt(1)\n"
    "v_mfma_f32_16x16x32_bf16 v[200:203], v[24:27], v[168:171], v[200:203]\n"
    "global_load_dwordx4 v[112:115], v235, s[72:73] offset:-1024 sc1\n"
    "v_add3_u32 v242, v242, v52, v55\n"
    "v_sub_u32 v52, v52, v240\n"
    "v_mfma_f32_16x16x32_bf16 v[204:207], v[48:51], v[168:171], v[204:207]\n"
    "global_load_dwordx4 v[136:139], v235, s[74:75] offset:-1024 sc1\n"
    "s_waitcnt lgkmcnt(0)\n"
    "v_mfma_f32_16x16x32_bf16 v[200:203], v[28:31], v[172:175], v[200:203]\n"
    "global_load_dwordx4 v[116:119], v235, s[72:73] offset:0 sc1\n"
    "v_mov_b32 v227, 1\n"
    "v_mfma_f32_16x16x32_bf16 v[204:207], v[52:55], v[172:175], v[204:207]\n"
    "global_load_dwordx4 v[140:143], v235, s[74:75] offset:0 sc1\n"
    "ds_write_b32 v225, v227 offset:256\n"
    "s_waitcnt vmcnt(18)\n"
    "v_add3_u32 v242, v242, v56, v59\n"
    "v_sub_u32 v56, v56, v240\n"
    "v_add3_u32 v242, v242, v80, v83\n"
    "v_sub_u32 v80, v80, v240\n"
    "s_waitcnt vmcnt(16)\n"
    "v_add3_u32 v242, v242, v60, v63\n"
    "v_sub_u32 v60, v60, v240\n"
    "v_mfma_f32_16x16x32_bf16 v[208:211], v[56:59], v[152:155], 0\n"
    "global_load_dwordx4 v[120:123], v235, s[72:73] offset:1024 sc1\n"
    "v_add3_u32 v242, v242, v84, v87\n"
    "v_sub_u32 v84, v84, v240\n"
    "v_mfma_f32_16x16x32_bf16 v[212:215], v[80:83], v[152:155], 0\n"
    "global_load_dwordx4 v[144:147], v235, s[74:75] offset:1024 sc1\n"
    "s_waitcnt vmcnt(16)\n"
    "v_add3_u32 v242, v242, v64, v67\n"
    "v_sub_u32 v64, v64, v240\n"
    "v_mfma_f32_16x16x32_bf16 v[208:211], v[60:63], v[156:159], v[208:211]\n"
    "global_load_dwordx4 v[124:127], v235, s[72:73] offset:2048 sc1\n"
    "v_add3_u32 v242, v242, v88, v91\n"
    "v_sub_u32 v88, v88, v240\n"
    "v_mfma_f32_16x16x32_bf16 v[212:215], v[84:87], v[156:159], v[212:215]\n"
    "global_load_dwordx4 v[148:151], v235, s[74:75] offset:2048 sc1\n"
    "s_waitcnt vmcnt(16)\n"
    "v_add3_u32 v242, v242, v68, v71\n"
    "v_sub_u32 v68, v68, v240\n"
    "v_mfma_f32_16x16x32_bf16 v[208:211], v[64:67], v[160:163], v[208:211]\n"
    "v_cvt_pk_bf16_f32 v200, v200, v201\n"
    "v_add3_u32 v242, v242, v92, v95\n"
    "v_sub_u32 v92, v92, v240\n"
    "v_mfma_f32_16x16x32_bf16 v[212:215], v[88:91], v[160:163], v[212:215]\n"
    "v_cvt_pk_bf16_f32 v201, v202, v203\n"
    "s_waitcnt vmcnt(14)\n"
    "v_add3_u32 v242, v242, v72, v75\n"
    "v_sub_u32 v72, v72, v240\n"
    "v_mfma_f32_16x16x32_bf16 v[208:211], v[68:71], v[164:167], v[208:211]\n"
    "v_cvt_pk_bf16_f32 v202, v204, v205\n"
    "v_add3_u32 v242, v242, v96, v99\n"
    "v_sub_u32 v96, v96, v240\n"
    "v_mfma_f32_16x16x32_bf16 v[212:215], v[92:95], v[164:167], v[212:215]\n"
    "v_cvt_pk_bf16_f32 v203, v206, v207\n"
    "s_waitcnt vmcnt(12)\n"
    "v_add3_u32 v242, v242, v76, v79\n"
    "v_sub_u32 v76, v76, v240\n"
    "v_mfma_f32_16x16x32_bf16 v[208:211], v[72:75], v[168:171], v[208:211]\n"
    "v_add3_u32 v242, v242, v100, v103\n"
    "v_sub_u32 v100, v100, v240\n"
    "v_mfma_f32_16x16x32_bf16 v[212:215], v[96:99], v[168:171], v[212:215]\n"
    "v_mfma_f32_16x16x32_bf16 v[208:211], v[76:79], v[172:175], v[208:211]\n"
    "ds_write_b128 v7, v[200:203] offset:0\n"
    "v_mfma_f32_16x16x32_bf16 v[212:215], v[100:103], v[172:175], v[212:215]\n"
    "s_waitcnt vmcnt(10)\n"
    "v_add3_u32 v242, v242, v104, v107\n"
    "v_sub_u32 v104, v104, v240\n"
    "v_add3_u32 v242, v242, v128, v131\n"
    "v_sub_u32 v128, v128, v240\n"
    "s_waitcnt vmcnt(8)\n"
    "v_add3_u32 v242, v242, v108, v111\n"
    "v_sub_u32 v108, v108, v240\n"
    "v_mfma_f32_16x16x32_bf16 v[216:219], v[104:107], v[152:155], 0\n"
    "ds_read_b128 v[176:179], v3 offset:26688\n"
    "v_add3_u32 v242, v242, v132, v135\n"
    "v_sub_u32 v132, v132, v240\n"
    "v_mfma_f32_16x16x32_bf16 v[220:223], v[128:131], v[152:155], 0\n"
    "ds_read_b128 v[180:183], v3 offset:26752\n"
    "s_waitcnt vmcnt(6)\n"
    "v_add3_u32 v242, v242, v112, v115\n"
    "v_sub_u32 v112, v112, v240\n"
    "v_mfma_f32_16x16x32_bf16 v[216:219], v[108:111], v[156:159], v[216:219]\n"
    "ds_read_b128 v[184:187], v3 offset:26816\n"
    "v_add3_u32 v242, v242, v136, v139\n"
    "v_sub_u32 v136, v136, v240\n"
    "v_mfma_f32_16x16x32_bf16 v[220:223], v[132:135], v[156:159], v[220:223]\n"
    "ds_read_b128 v[188:191], v3 offset:26880\n"
    "s_waitcnt vmcnt(4)\n"
    "v_add3_u32 v242, v242, v116, v119\n"
    "v_sub_u32 v116, v116, v240\n"
    "v_mfma_f32_16x16x32_bf16 v[216:219], v[112:115], v[160:163], v[216:219]\n"
    "ds_read_b128 v[192:195], v3 offset:26944\n"
    "v_add3_u32 v242, v242, v140, v143\n"
    "v_sub_u32 v140, v140, v240\n"
    "v_mfma_f32_16x16x32_bf16 v[220:223], v[136:139], v[160:163], v[220:223]\n"
    "ds_read_b128 v[196:199], v3 offset:27008\n"
    "s_waitcnt vmcnt(2)\n"
    "v_add3_u32 v242, v242, v120, v123\n"
    "v_sub_u32 v120, v120, v240\n"
    "v_mfma_f32_16x16x32_bf16 v[216:219], v[116:119], v[164:167], v[216:219]\n"
    "v_cvt_pk_bf16_f32 v208, v208, v209\n"
    "v_add3_u32 v242, v242, v144, v147\n"
    "v_sub_u32 v144, v144, v240\n"
    "v_mfma_f32_16x16x32_bf16 v[220:223], v[140:143], v[164:167], v[220:223]\n"
    "v_cvt_pk_bf16_f32 v209, v210, v211\n"
    "s_waitcnt vmcnt(0)\n"
    "v_add3_u32 v242, v242, v124, v127\n"
    "v_sub_u32 v124, v124, v240\n"
    "v_mfma_f32_16x16x32_bf16 v[216:219], v[120:123], v[168:171], v[216:219]\n"
    "v_cvt_pk_bf16_f32 v210, v212, v213\n"
    "v_add3_u32 v242, v242, v148, v151\n"
    "v_sub_u32 v148, v148, v240\n"
    "v_mfma_f32_16x16x32_bf16 v[220:223], v[144:147], v[168:171], v[220:223]\n"
    "v_cvt_pk_bf16_f32 v211, v214, v215\n"
    "v_mfma_f32_16x16x32_bf16 v[216:219], v[124:127], v[172:175], v[216:219]\n"
    "ds_write_b128 v7, v[208:211] offset:1024\n"
    "v_mfma_f32_16x16x32_bf16 v[220:223], v[148:151], v[172:175], v[220:223]\n"
    "v_cmp_ne_u64 s[76:77], v[240:241], v[244:245]\n"
    "v_sub_u32 v242, v242, v246\n"
    "v_mov_b32 v243, v242\n"
    "s_nop 1\n"
    "v_permlane32_swap_b32 v242, v243\n"
    "v_add_u32 v242, v242, v243\n"
    "v_cvt_pk_bf16_f32 v216, v216, v217\n"
    "v_cvt_pk_bf16_f32 v217, v218, v219\n"
    "v_add_u32_dpp v242, v242, v242 row_ror:8 row_mask:0xf bank_mask:0xf\n"
    "v_cvt_pk_bf16_f32 v218, v220, v221\n"
    "v_cvt_pk_bf16_f32 v219, v222, v223\n"
    "v_add_u32_dpp v242, v242, v242 row_ror:4 row_mask:0xf bank_mask:0xf\n"
    "ds_write_b128 v7, v[216:219] offset:2048\n"
    "s_nop 0\n"
    "v_add_u32_dpp v242, v242, v242 row_ror:2 row_mask:0xf bank_mask:0xf\n"
    "s_nop 1\n"
    "v_add_u32_dpp v242, v242, v242 row_ror:1 row_mask:0xf bank_mask:0xf\n"
    "v_mov_b32 v243, v242\n"
    "s_nop 1\n"
    "v_permlane16_swap_b32 v242, v243\n"
    "v_add_u32 v242, v242, v243\n"
    "v_cmp_ne_u32 vcc, 0, v242\n"
    "v_mov_b32 v227, 1\n"
    "s_cmp_lg_u64 vcc, 0\n"
    "s_cbranch_scc1 .Lg_fail\n"
    "s_cmp_lg_u64 s[76:77], 0\n"
    "s_cbranch_scc1 .Lg_fail\n"
    ".Lg_ok:\n"
    "ds_write_b32 v225, v227 offset:16\n"
    "v_mov_b32 v242, 0\n"
    "s_waitcnt lgkmcnt(3)\n"
    "v_mfma_f32_16x16x32_bf16 v[200:203], v[8:11], v[176:179], 0\n"
    "v_mfma_f32_16x16x32_bf16 v[204:207], v[32:35], v[176:179], 0\n"
    "v_mfma_f32_16x16x32_bf16 v[200:203], v[12:15], v[180:183], v[200:203]\n"
    "v_mfma_f32_16x16x32_bf16 v[204:207], v[36:39], v[180:183], v[204:207]\n"
    "v_mfma_f32_16x16x32_bf16 v[200:203], v[16:19], v[184:187], v[200:203]\n"
    "v_mfma_f32_16x16x32_bf16 v[204:207], v[40:43], v[184:187], v[204:207]\n"
    "v_mfma_f32_16x16x32_bf16 v[200:203], v[20:23], v[188:191], v[200:203]\n"
    "v_mfma_f32_16x16x32_bf16 v[204:207], v[44:47], v[188:191], v[204:207]\n"
    "v_mfma_f32_16x16x32_bf16 v[200:203], v[24:27], v[192:195], v[200:203]\n"
    "v_mfma_f32_16x16x32_bf16 v[204:207], v[48:51], v[192:195], v[204:207]\n"
    "v_mfma_f32_16x16x32_bf16 v[200:203], v[28:31], v[196:199], v[200:203]\n"
    "v_mfma_f32_16x16x32_bf16 v[204:207], v[52:55], v[196:199], v[204:207]\n"
    "v_mfma_f32_16x16x32_bf16 v[208:211], v[56:59], v[176:179], 0\n"
    "v_mfma_f32_16x16x32_bf16 v[212:215], v[80:83], v[176:179], 0\n"
    "ds_read_b32 v229, v224 offset:0\n"
    "v_mfma_f32_16x16x32_bf16 v[208:211], v[60:63], v[180:183], v[208:211]\n"
    "v_mfma_f32_16x16x32_bf16 v[212:215], v[84:87], v[180:183], v[212:215]\n"
    "v_mfma_f32_16x16x32_bf16 v[208:211], v[64:67], v[184:187], v[208:211]\n"
    "v_mfma_f32_16x16x32_bf16 v[212:215], v[88:91], v[184:187], v[212:215]\n"
    "v_cvt_pk_bf16_f32 v200, v200, v201\n"
    "v_cvt_pk_bf16_f32 v201, v202, v203\n"
    "v_mfma_f32_16x16x32_bf16 v[208:211], v[68:71], v[188:191], v[208:211]\n"
    "v_cvt_pk_bf16_f32 v202, v204, v205\n"
    "v_cvt_pk_bf16_f32 v203, v206, v207\n"
    "v_mfma_f32_16x16x32_bf16 v[212:215], v[92:95], v[188:191], v[212:215]\n"
    "ds_write_b128 v7, v[200:203] offset:6144\n"
    "v_mfma_f32_16x16x32_bf16 v[208:211], v[72:75], v[192:195], v[208:211]\n"
    "v_mfma_f32_16x16x32_bf16 v[212:215], v[96:99], v[192:195], v[212:215]\n"
    "s_waitcnt lgkmcnt(1)\n"
    "v_cmp_gt_u32 vcc, 2, v229\n"
    "v_mfma_f32_16x16x32_bf16 v[208:211], v[76:79], v[196:199], v[208:211]\n"
    "v_mfma_f32_16x16x32_bf16 v[212:215], v[100:103], v[196:199], v[212:215]\n"
    "s_cbranch_vccnz .Lg3\n"
    ".Lg3b:\n"
    "v_mfma_f32_16x16x32_bf16 v[216:219], v[104:107], v[176:179], 0\n"
    "ds_read_b128 v[152:155], v2 offset:44480\n"
    "ds_read_b128 v[156:159], v2 offset:44544\n"
    "v_mfma_f32_16x16x32_bf16 v[220:223], v[128:131], v[176:179], 0\n"
    "ds_read_b128 v[160:163], v2 offset:44608\n"
    "ds_read_b128 v[164:167], v2 offset:44672\n"
    "v_mfma_f32_16x16x32_bf16 v[216:219], v[108:111], v[180:183], v[216:219]\n"
    "ds_read_b128 v[168:171], v2 offset:44736\n"
    "ds_read_b128 v[172:175], v2 offset:44800\n"
    "v_mfma_f32_16x16x32_bf16 v[220:223], v[132:135], v[180:183], v[220:223]\n"
    "v_cvt_pk_bf16_f32 v208, v208, v209\n"
    "v_cvt_pk_bf16_f32 v209, v210, v211\n"
    "v_mfma_f32_16x16x32_bf16 v[216:219], v[112:115], v[184:187], v[216:219]\n"
    "v_cvt_pk_bf16_f32 v210, v212, v213\n"
    "v_cvt_pk_bf16_f32 v211, v214, v215\n"
    "v_mfma_f32_16x16x32_bf16 v[220:223], v[136:139], v[184:187], v[220:223]\n"
    "ds_write_b128 v7, v[208:211] offset:7168\n"
    "v_mfma_f32_16x16x32_bf16 v[216:219], v[116:119], v[188:191], v[216:219]\n"
    "v_mfma_f32_16x16x32_bf16 v[220:223], v[140:143], v[188:191], v[220:223]\n"
    "v_mfma_f32_16x16x32_bf16 v[216:219], v[120:123], v[192:195], v[216:219]\n"
    "v_mfma_f32_16x16x32_bf16 v[220:223], v[144:147], v[192:195], v[220:223]\n"
    "v_mfma_f32_16x16x32_bf16 v[216:219], v[124:127], v[196:199], v[216:219]\n"
    "v_mfma_f32_16x16x32_bf16 v[220:223], v[148:151], v[196:199], v[220:223]\n"
    "s_waitcnt lgkmcnt(1)\n"
    "v_mfma_f32_16x16x32_bf16 v[200:203], v[8:11], v[152:155], 0\n"
    "v_mov_b32 v227, 2\n"
    "v_mfma_f32_16x16x32_bf16 v[204:207], v[32:35], v[152:155], 0\n"
    "v_mfma_f32_16x16x32_bf16 v[200:203], v[12:15], v[156:159], v[200:203]\n"
    "v_mfma_f32_16x16x32_bf16 v[204:207], v[36:39], v[156:159], v[204:207]\n"
    "v_mfma_f32_16x16x32_bf16 v[200:203], v[16:19], v[160:163], v[200:203]\n"
    "v_cvt_pk_bf16_f32 v216, v216, v217\n"
    "v_cvt_pk_bf16_f32 v217, v218, v219\n"
    "v_mfma_f32_16x16x32_bf16 v[204:207], v[40:43], v[160:163], v[204:207]\n"
    "v_cvt_pk_bf16_f32 v218, v220, v221\n"
    "v_cvt_pk_bf16_f32 v219, v222, v223\n"
    "v_mfma_f32_16x16x32_bf16 v[200:203], v[20:23], v[164:167], v[200:203]\n"
    "ds_write_b128 v7, v[216:219] offset:8192\n"
    "v_mfma_f32_16x16x32_bf16 v[204:207], v[44:47], v[164:167], v[204:207]\n"
    "ds_write_b32 v225, v227 offset:16\n"
    "v_mfma_f32_16x16x32_bf16 v[200:203], v[24:27], v[168:171], v[200:203]\n"
    "v_mfma_f32_16x16x32_bf16 v[204:207], v[48:51], v[168:171], v[204:207]\n"
    "v_mfma_f32_16x16x32_bf16 v[200:203], v[28:31], v[172:175], v[200:203]\n"
    "v_mfma_f32_16x16x32_bf16 v[204:207], v[52:55], v[172:175], v[204:207]\n"
    "v_mfma_f32_16x16x32_bf16 v[208:211], v[56:59], v[152:155], 0\n"
    "v_mfma_f32_16x16x32_bf16 v[212:215], v[80:83], v[152:155], 0\n"
    "v_mfma_f32_16x16x32_bf16 v[208:211], v[60:63], v[156:159], v[208:211]\n"
    "v_mfma_f32_16x16x32_bf16 v[212:215], v[84:87], v[156:159], v[212:215]\n"
    "v_mfma_f32_16x16x32_bf16 v[208:211], v[64:67], v[160:163], v[208:211]\n"
    "v_mfma_f32_16x16x32_bf16 v[212:215], v[88:91], v[160:163], v[212:215]\n"
    "v_mfma_f32_16x16x32_bf16 v[208:211], v[68:71], v[164:167], v[208:211]\n"
    "v_cvt_pk_bf16_f32 v200, v200, v201\n"
    "v_cvt_pk_bf16_f32 v201, v202, v203\n"
    "v_mfma_f32_16x16x32_bf16 v[212:215], v[92:95], v[164:167], v[212:215]\n"
    "v_cvt_pk_bf16_f32 v202, v204, v205\n"
    "v_cvt_pk_bf16_f32 v203, v206, v207\n"
    "v_mfma_f32_16x16x32_bf16 v[208:211], v[72:75], v[168:171], v[208:211]\n"
    "ds_write_b128 v7, v[200:203] offset:12288\n"
    "v_mfma_f32_16x16x32_bf16 v[212:215], v[96:99], v[168:171], v[212:215]\n"
    "v_mfma_f32_16x16x32_bf16 v[208:211], v[76:79], v[172:175], v[208:211]\n"
    "v_mfma_f32_16x16x32_bf16 v[212:215], v[100:103], v[172:175], v[212:215]\n"
    "v_mfma_f32_16x16x32_bf16 v[216:219], v[104:107], v[152:155], 0\n"
    "ds_read_b128 v[176:179], v3 offset:44480\n"
    "ds_read_b128 v[180:183], v3 offset:44544\n"
    "v_mfma_f32_16x16x32_bf16 v[220:223], v[128:131], v[152:155], 0\n"
    "ds_read_b128 v[184:187], v3 offset:44608\n"
    "ds_read_b128 v[188:191], v3 offset:44672\n"
    "v_mfma_f32_16x16x32_bf16 v[216:219], v[108:111], v[156:159], v[216:219]\n"
    "ds_read_b128 v[192:195], v3 offset:44736\n"
    "ds_read_b128 v[196:199], v3 offset:44800\n"
    "v_mfma_f32_16x16x32_bf16 v[220:223], v[132:135], v[156:159], v[220:223]\n"
    "v_cvt_pk_bf16_f32 v208, v208, v209\n"
    "v_cvt_pk_bf16_f32 v209, v210, v211\n"
    "v_mfma_f32_16x16x32_bf16 v[216:219], v[112:115], v[160:163], v[216:219]\n"
    "v_cvt_pk_bf16_f32 v210, v212, v213\n"
    "v_cvt_pk_bf16_f32 v211, v214, v215\n"
    "v_mfma_f32_16x16x32_bf16 v[220:223], v[136:139], v[160:163], v[220:223]\n"
    "ds_write_b128 v7, v[208:211] offset:13312\n"
    "v_mfma_f32_16x16x32_bf16 v[216:219], v[116:119], v[164:167], v[216:219]\n"
    "v_mfma_f32_16x16x32_bf16 v[220:223], v[140:143], v[164:167], v[220:223]\n"
    "v_mfma_f32_16x16x32_bf16 v[216:219], v[120:123], v[168:171], v[216:219]\n"
    "v_mfma_f32_16x16x32_bf16 v[220:223], v[144:147], v[168:171], v[220:223]\n"
    "v_mfma_f32_16x16x32_bf16 v[216:219], v[124:127], v[172:175], v[216:219]\n"
    "v_mfma_f32_16x16x32_bf16 v[220:223], v[148:151], v[172:175], v[220:223]\n"
    "s_waitcnt lgkmcnt(1)\n"
    "v_mfma_f32_16x16x32_bf16 v[200:203], v[8:11], v[176:179], 0\n"
    "v_mov_b32 v227, 3\n"
    "v_mfma_f32_16x16x32_bf16 v[204:207], v[32:35], v[176:179], 0\n"
    "v_mfma_f32_16x16x32_bf16 v[200:203], v[12:15], v[180:183], v[200:203]\n"
    "v_mfma_f32_16x16x32_bf16 v[204:207], v[36:39], v[180:183], v[204:207]\n"
    "v_mfma_f32_16x16x32_bf16 v[200:203], v[16:19], v[184:187], v[200:203]\n"
    "v_cvt_pk_bf16_f32 v216, v216, v217\n"
    "v_cvt_pk_bf16_f32 v217, v218, v219\n"
    "v_mfma_f32_16x16x32_bf16 v[204:207], v[40:43], v[184:187], v[204:207]\n"
    "v_cvt_pk_bf16_f32 v218, v220, v221\n"
    "v_cvt_pk_bf16_f32 v219, v222, v223\n"
    "v_mfma_f32_16x16x32_bf16 v[200:203], v[20:23], v[188:191], v[200:203]\n"
    "ds_write_b128 v7, v[216:219] offset:14336\n"
    "v_mfma_f32_16x16x32_bf16 v[204:207], v[44:47], v[188:191], v[204:207]\n"
    "ds_write_b32 v225, v227 offset:16\n"
    "ds_read_b32 v228, v224 offset:192\n"
    "v_mfma_f32_16x16x32_bf16 v[200:203], v[24:27], v[192:195], v[200:203]\n"
    "v_mfma_f32_16x16x32_bf16 v[204:207], v[48:51], v[192:195], v[204:207]\n"
    "v_mfma_f32_16x16x32_bf16 v[200:203], v[28:31], v[196:199], v[200:203]\n"
    "v_mfma_f32_16x16x32_bf16 v[204:207], v[52:55], v[196:199], v[204:207]\n"
    "v_mfma_f32_16x16x32_bf16 v[208:211], v[56:59], v[176:179], 0\n"
    "v_mfma_f32_16x16x32_bf16 v[212:215], v[80:83], v[176:179], 0\n"
    "ds_read_b32 v229, v224 offset:0\n"
    "v_mfma_f32_16x16x32_bf16 v[208:211], v[60:63], v[180:183], v[208:211]\n"
    "v_mfma_f32_16x16x32_bf16 v[212:215], v[84:87], v[180:183], v[212:215]\n"
    "s_waitcnt lgkmcnt(1)\n"
    "v_cmp_gt_u32 vcc, 1, v228\n"
    "v_mfma_f32_16x16x32_bf16 v[208:211], v[64:67], v[184:187], v[208:211]\n"
    "v_cvt_pk_bf16_f32 v200, v200, v201\n"
    "s_cbranch_vccnz .Lg5\n"
    ".Lg5b:\n"
    "v_mfma_f32_16x16x32_bf16 v[212:215], v[88:91], v[184:187], v[212:215]\n"
    "v_cvt_pk_bf16_f32 v201, v202, v203\n"
    "v_cvt_pk_bf16_f32 v202, v204, v205\n"
    "v_mfma_f32_16x16x32_bf16 v[208:211], v[68:71], v[188:191], v[208:211]\n"
    "v_cvt_pk_bf16_f32 v203, v206, v207\n"
    "v_mfma_f32_16x16x32_bf16 v[212:215], v[92:95], v[188:191], v[212:215]\n"
    "ds_write_b128 v7, v[200:203] offset:0\n"
    "v_mfma_f32_16x16x32_bf16 v[208:211], v[72:75], v[192:195], v[208:211]\n"
    "v_mfma_f32_16x16x32_bf16 v[212:215], v[96:99], v[192:195], v[212:215]\n"
    "s_waitcnt lgkmcnt(1)\n"
    "v_cmp_gt_u32 vcc, 3, v229\n"
    "v_mfma_f32_16x16x32_bf16 v[208:211], v[76:79], v[196:199], v[208:211]\n"
    "v_mfma_f32_16x16x32_bf16 v[212:215], v[100:103], v[196:199], v[212:215]\n"
    "s_cbranch_vccnz .Lg4\n"
    ".Lg4b:\n"
    "v_mfma_f32_16x16x32_bf16 v[216:219], v[104:107], v[176:179], 0\n"
    "ds_read_b128 v[152:155], v2 offset:62272\n"
    "ds_read_b128 v[156:159], v2 offset:62336\n"
    "v_mfma_f32_16x16x32_bf16 v[220:223], v[128:131], v[176:179], 0\n"
    "ds_read_b128 v[160:163], v2 offset:62400\n"
    "ds_read_b128 v[164:167], v2 offset:62464\n"
    "v_mfma_f32_16x16x32_bf16 v[216:219], v[108:111], v[180:183], v[216:219]\n"
    "ds_read_b128 v[168:171], v2 offset:62528\n"
    "ds_read_b128 v[172:175], v2 offset:62592\n"
    "v_mfma_f32_16x16x32_bf16 v[220:223], v[132:135], v[180:183], v[220:223]\n"
    "v_cvt_pk_bf16_f32 v208, v208, v209\n"
    "v_cvt_pk_bf16_f32 v209, v210, v211\n"
    "v_mfma_f32_16x16x32_bf16 v[216:219], v[112:115], v[184:187], v[216:219]\n"
    "v_cvt_pk_bf16_f32 v210, v212, v213\n"
    "v_cvt_pk_bf16_f32 v211, v214, v215\n"
    "v_mfma_f32_16x16x32_bf16 v[220:223], v[136:139], v[184:187], v[220:223]\n"
    "ds_write_b128 v7, v[208:211] offset:1024\n"
    "v_mfma_f32_16x16x32_bf16 v[216:219], v[116:119], v[188:191], v[216:219]\n"
    "v_mfma_f32_16x16x32_bf16 v[220:223], v[140:143], v[188:191], v[220:223]\n"
    "v_mfma_f32_16x16x32_bf16 v[216:219], v[120:123], v[192:195], v[216:219]\n"
    "v_mfma_f32_16x16x32_bf16 v[220:223], v[144:147], v[192:195], v[220:223]\n"
    "v_mfma_f32_16x16x32_bf16 v[216:219], v[124:127], v[196:199], v[216:219]\n"
    "v_mfma_f32_16x16x32_bf16 v[220:223], v[148:151], v[196:199], v[220:223]\n"
    "s_waitcnt lgkmcnt(1)\n"
    "v_mfma_f32_16x16x32_bf16 v[200:203], v[8:11], v[152:155], 0\n"
    "v_mov_b32 v227, 4\n"
    "v_mfma_f32_16x16x32_bf16 v[204:207], v[32:35], v[152:155], 0\n"
    "v_mfma_f32_16x16x32_bf16 v[200:203], v[12:15], v[156:159], v[200:203]\n"
    "v_mfma_f32_16x16x32_bf16 v[204:207], v[36:39], v[156:159], v[204:207]\n"
    "v_mfma_f32_16x16x32_bf16 v[200:203], v[16:19], v[160:163], v[200:203]\n"
    "v_cvt_pk_bf16_f32 v216, v216, v217\n"
    "v_cvt_pk_bf16_f32 v217, v218, v219\n"
    "v_mfma_f32_16x16x32_bf16 v[204:207], v[40:43], v[160:163], v[204:207]\n"
    "v_cvt_pk_bf16_f32 v218, v220, v221\n"
    "v_cvt_pk_bf16_f32 v219, v222, v223\n"
    "v_mfma_f32_16x16x32_bf16 v[200:203], v[20:23], v[164:167], v[200:203]\n"
    "ds_write_b128 v7, v[216:219] offset:2048\n"
    "v_mfma_f32_16x16x32_bf16 v[204:207], v[44:47], v[164:167], v[204:207]\n"
    "ds_write_b32 v225, v227 offset:16\n"
    "ds_read_b32 v228, v224 offset:208\n"
    "v_mfma_f32_16x16x32_bf16 v[200:203], v[24:27], v[168:171], v[200:203]\n"
    "ds_read_b32 v239, v224 offset:64\n"
    "v_mfma_f32_16x16x32_bf16 v[204:207], v[48:51], v[168:171], v[204:207]\n"
    "v_mfma_f32_16x16x32_bf16 v[200:203], v[28:31], v[172:175], v[200:203]\n"
    "v_mfma_f32_16x16x32_bf16 v[204:207], v[52:55], v[172:175], v[204:207]\n"
    "v_mfma_f32_16x16x32_bf16 v[208:211], v[56:59], v[152:155], 0\n"
    "v_mfma_f32_16x16x32_bf16 v[212:215], v[80:83], v[152:155], 0\n"
    "v_mfma_f32_16x16x32_bf16 v[208:211], v[60:63], v[156:159], v[208:211]\n"
    "v_mfma_f32_16x16x32_bf16 v[212:215], v[84:87], v[156:159], v[212:215]\n"
    "s_waitcnt lgkmcnt(1)\n"
    "v_cmp_gt_u32 vcc, 1, v228\n"
    "v_mfma_f32_16x16x32_bf16 v[208:211], v[64:67], v[160:163], v[208:211]\n"
    "v_cvt_pk_bf16_f32 v200, v200, v201\n"
    "s_cbranch_vccnz .Lg6\n"
    ".Lg6b:\n"
    "v_mfma_f32_16x16x32_bf16 v[212:215], v[88:91], v[160:163], v[212:215]\n"
    "v_cvt_pk_bf16_f32 v201, v202, v203\n"
    "v_cvt_pk_bf16_f32 v202, v204, v205\n"
    "v_mfma_f32_16x16x32_bf16 v[208:211], v[68:71], v[164:167], v[208:211]\n"
    "v_cvt_pk_bf16_f32 v203, v206, v207\n"
    "v_mfma_f32_16x16x32_bf16 v[212:215], v[92:95], v[164:167], v[212:215]\n"
    "ds_write_b128 v7, v[200:203] offset:6144\n"
    "v_mfma_f32_16x16x32_bf16 v[208:211], v[72:75], v[168:171], v[208:211]\n"
    "v_mfma_f32_16x16x32_bf16 v[212:215], v[96:99], v[168:171], v[212:215]\n"
    "v_mfma_f32_16x16x32_bf16 v[208:211], v[76:79], v[172:175], v[208:211]\n"
    "v_mfma_f32_16x16x32_bf16 v[212:215], v[100:103], v[172:175], v[212:215]\n"
    "v_mfma_f32_16x16x32_bf16 v[216:219], v[104:107], v[152:155], 0\n"
    "ds_read_b128 v[176:179], v3 offset:62272\n"
    "ds_read_b128 v[180:183], v3 offset:62336\n"
    "v_mfma_f32_16x16x32_bf16 v[220:223], v[128:131], v[152:155], 0\n"
    "ds_read_b128 v[184:187], v3 offset:62400\n"
    "ds_read_b128 v[188:191], v3 offset:62464\n"
    "v_mfma_f32_16x16x32_bf16 v[216:219], v[108:111], v[156:159], v[216:219]\n"
    "ds_read_b128 v[192:195], v3 offset:62528\n"
    "ds_read_b128 v[196:199], v3 offset:62592\n"
    "v_mfma_f32_16x16x32_bf16 v[220:223], v[132:135], v[156:159], v[220:223]\n"
    "v_cvt_pk_bf16_f32 v208, v208, v209\n"
    "v_cvt_pk_bf16_f32 v209, v210, v211\n"
    "v_mfma_f32_16x16x32_bf16 v[216:219], v[112:115], v[160:163], v[216:219]\n"
    "v_cvt_pk_bf16_f32 v210, v212, v213\n"
    "v_cvt_pk_bf16_f32 v211, v214, v215\n"
    "v_mfma_f32_16x16x32_bf16 v[220:223], v[136:139], v[160:163], v[220:223]\n"
    "ds_write_b128 v7, v[208:211] offset:7168\n"
    "v_mfma_f32_16x16x32_bf16 v[216:219], v[116:119], v[164:167], v[216:219]\n"
    "s_waitcnt lgkmcnt(8)\n"
    "v_cmp_gt_u32 vcc, 1, v239\n"
    "v_mfma_f32_16x16x32_bf16 v[220:223], v[140:143], v[164:167], v[220:223]\n"
    "v_mfma_f32_16x16x32_bf16 v[216:219], v[120:123], v[168:171], v[216:219]\n"
    "s_cbranch_vccnz .Lg7\n"
    ".Lg7b:\n"
    "v_mfma_f32_16x16x32_bf16 v[220:223], v[144:147], v[168:171], v[220:223]\n"
    "v_mfma_f32_16x16x32_bf16 v[216:219], v[124:127], v[172:175], v[216:219]\n"
    "v_mfma_f32_16x16x32_bf16 v[220:223], v[148:151], v[172:175], v[220:223]\n"
    "s_waitcnt lgkmcnt(1)\n"
    "v_mfma_f32_16x16x32_bf16 v[200:203], v[8:11], v[176:179], 0\n"
    "v_mov_b32 v227, 5\n"
    "v_mfma_f32_16x16x32_bf16 v[204:207], v[32:35], v[176:179], 0\n"
    "v_mfma_f32_16x16x32_bf16 v[200:203], v[12:15], v[180:183], v[200:203]\n"
    "v_mfma_f32_16x16x32_bf16 v[204:207], v[36:39], v[180:183], v[204:207]\n"
    "v_mfma_f32_16x16x32_bf16 v[200:203], v[16:19], v[184:187], v[200:203]\n"
    "v_cvt_pk_bf16_f32 v216, v216, v217\n"
    "v_cvt_pk_bf16_f32 v217, v218, v219\n"
    "v_mfma_f32_16x16x32_bf16 v[204:207], v[40:43], v[184:187], v[204:207]\n"
    "v_cvt_pk_bf16_f32 v218, v220, v221\n"
    "v_cvt_pk_bf16_f32 v219, v222, v223\n"
    "v_mfma_f32_16x16x32_bf16 v[200:203], v[20:23], v[188:191], v[200:203]\n"
    "ds_write_b128 v7, v[216:219] offset:8192\n"
    "v_mfma_f32_16x16x32_bf16 v[204:207], v[44:47], v[188:191], v[204:207]\n"
    "ds_write_b32 v225, v227 offset:16\n"
    "ds_read_b32 v228, v224 offset:224\n"
    "v_mfma_f32_16x16x32_bf16 v[200:203], v[24:27], v[192:195], v[200:203]\n"
    "ds_read_b32 v239, v224 offset:80\n"
    "v_mfma_f32_16x16x32_bf16 v[204:207], v[48:51], v[192:195], v[204:207]\n"
    "v_mfma_f32_16x16x32_bf16 v[200:203], v[28:31], v[196:199], v[200:203]\n"
    "v_mfma_f32_16x16x32_bf16 v[204:207], v[52:55], v[196:199], v[204:207]\n"
    "v_mfma_f32_16x16x32_bf16 v[208:211], v[56:59], v[176:179], 0\n"
    "v_mfma_f32_16x16x32_bf16 v[212:215], v[80:83], v[176:179], 0\n"
    "ds_read_b32 v229, v224 offset:0\n"
    "v_mfma_f32_16x16x32_bf16 v[208:211], v[60:63], v[180:183], v[208:211]\n"
    "v_mfma_f32_16x16x32_bf16 v[212:215], v[84:87], v[180:183], v[212:215]\n"
    "s_waitcnt lgkmcnt(2)\n"
    "v_cmp_gt_u32 vcc, 1, v228\n"
    "v_mfma_f32_16x16x32_bf16 v[208:211], v[64:67], v[184:187], v[208:211]\n"
    "v_cvt_pk_bf16_f32 v200, v200, v201\n"
    "s_cbranch_vccnz .Lg9\n"
    ".Lg9b:\n"
    "v_mfma_f32_16x16x32_bf16 v[212:215], v[88:91], v[184:187], v[212:215]\n"
    "v_cvt_pk_bf16_f32 v201, v202, v203\n"
    "v_cvt_pk_bf16_f32 v202, v204, v205\n"
    "v_mfma_f32_16x16x32_bf16 v[208:211], v[68:71], v[188:191], v[208:211]\n"
    "v_cvt_pk_bf16_f32 v203, v206, v207\n"
    "v_mfma_f32_16x16x32_bf16 v[212:215], v[92:95], v[188:191], v[212:215]\n"
    "ds_write_b128 v7, v[200:203] offset:12288\n"
    "v_mfma_f32_16x16x32_bf16 v[208:211], v[72:75], v[192:195], v[208:211]\n"
    "v_mfma_f32_16x16x32_bf16 v[212:215], v[96:99], v[192:195], v[212:215]\n"
    "s_waitcnt lgkmcnt(1)\n"
    "v_cmp_gt_u32 vcc, 6, v229\n"
    "v_mfma_f32_16x16x32_bf16 v[208:211], v[76:79], v[196:199], v[208:211]\n"
    "v_mfma_f32_16x16x32_bf16 v[212:215], v[100:103], v[196:199], v[212:215]\n"
    "s_cbranch_vccnz .Lg8\n"
    ".Lg8b:\n"
    "v_mfma_f32_16x16x32_bf16 v[216:219], v[104:107], v[176:179], 0\n"
    "ds_read_b128 v[152:155], v4 offset:8896\n"
    "ds_read_b128 v[156:159], v4 offset:8960\n"
    "v_mfma_f32_16x16x32_bf16 v[220:223], v[128:131], v[176:179], 0\n"
    "ds_read_b128 v[160:163], v4 offset:9024\n"
    "ds_read_b128 v[164:167], v4 offset:9088\n"
    "v_mfma_f32_16x16x32_bf16 v[216:219], v[108:111], v[180:183], v[216:219]\n"
    "ds_read_b128 v[168:171], v4 offset:9152\n"
    "ds_read_b128 v[172:175], v4 offset:9216\n"
    "v_mfma_f32_16x16x32_bf16 v[220:223], v[132:135], v[180:183], v[220:223]\n"
    "v_cvt_pk_bf16_f32 v208, v208, v209\n"
    "v_cvt_pk_bf16_f32 v209, v210, v211\n"
    "v_mfma_f32_16x16x32_bf16 v[216:219], v[112:115], v[184:187], v[216:219]\n"
    "v_cvt_pk_bf16_f32 v210, v212, v213\n"
    "v_cvt_pk_bf16_f32 v211, v214, v215\n"
    "v_mfma_f32_16x16x32_bf16 v[220:223], v[136:139], v[184:187], v[220:223]\n"
    "ds_write_b128 v7, v[208:211] offset:13312\n"
    "v_mfma_f32_16x16x32_bf16 v[216:219], v[116:119], v[188:191], v[216:219]\n"
    "v_cmp_gt_u32 vcc, 1, v239\n"
    "v_mfma_f32_16x16x32_bf16 v[220:223], v[140:143], v[188:191], v[220:223]\n"
    "s_cbranch_vccnz .Lg10\n"
    ".Lg10b:\n"
    "v_mfma_f32_16x16x32_bf16 v[216:219], v[120:123], v[192:195], v[216:219]\n"
    "s_add_u32 s60, s42, s44\n"
    "s_addc_u32 s61, s43, 0\n"
    "v_mfma_f32_16x16x32_bf16 v[220:223], v[144:147], v[192:195], v[220:223]\n"
    "s_add_u32 m0, s54, 0\n"
    "v_mfma_f32_16x16x32_bf16 v[216:219], v[124:127], v[196:199], v[216:219]\n"
    "global_load_lds_dwordx4 v230, s[60:61]\n"
    "s_add_u32 m0, s54, 2048\n"
    "v_mfma_f32_16x16x32_bf16 v[220:223], v[148:151], v[196:199], v[220:223]\n"
    "global_load_lds_dwordx4 v231, s[60:61]\n"
    "s_add_u32 m0, s54, 4096\n"
    "s_waitcnt lgkmcnt(1)\n"
    "v_mfma_f32_16x16x32_bf16 v[200:203], v[8:11], v[152:155], 0\n"
    "v_mov_b32 v227, 6\n"
    "v_cvt_pk_bf16_f32 v216, v216, v217\n"
    "v_mfma_f32_16x16x32_bf16 v[204:207], v[32:35], v[152:155], 0\n"
    "v_cvt_pk_bf16_f32 v217, v218, v219\n"
    "v_cvt_pk_bf16_f32 v218, v220, v221\n"
    "v_mfma_f32_16x16x32_bf16 v[200:203], v[12:15], v[156:159], v[200:203]\n"
    "v_cvt_pk_bf16_f32 v219, v222, v223\n"
    "v_mfma_f32_16x16x32_bf16 v[204:207], v[36:39], v[156:159], v[204:207]\n"
    "ds_write_b128 v7, v[216:219] offset:14336\n"
    "v_mfma_f32_16x16x32_bf16 v[200:203], v[16:19], v[160:163], v[200:203]\n"
    "ds_write_b32 v225, v227 offset:16\n"
    "ds_read_b32 v228, v224 offset:240\n"
    "v_mfma_f32_16x16x32_bf16 v[204:207], v[40:43], v[160:163], v[204:207]\n"
    "ds_read_b32 v239, v224 offset:96\n"
    "global_load_lds_dwordx4 v232, s[60:61]\n"
    "v_mfma_f32_16x16x32_bf16 v[200:203], v[20:23], v[164:167], v[200:203]\n"
    "s_add_u32 m0, s55, 0\n"
    "v_mfma_f32_16x16x32_bf16 v[204:207], v[44:47], v[164:167], v[204:207]\n"
    "global_load_lds_dwordx4 v233, s[60:61]\n"
    "s_add_u32 m0, s56, 0\n"
    "v_mfma_f32_16x16x32_bf16 v[200:203], v[24:27], v[168:171], v[200:203]\n"
    "global_load_lds_dwordx4 v234, s[60:61]\n"
    "v_mfma_f32_16x16x32_bf16 v[204:207], v[48:51], v[168:171], v[204:207]\n"
    "s_add_u32 s60, s42, s45\n"
    "s_addc_u32 s61, s43, 0\n"
    "v_mfma_f32_16x16x32_bf16 v[200:203], v[28:31], v[172:175], v[200:203]\n"
    "s_add_u32 m0, s54, 8896\n"
    "v_mfma_f32_16x16x32_bf16 v[204:207], v[52:55], v[172:175], v[204:207]\n"
    "global_load_lds_dwordx4 v230, s[60:61]\n"
    "s_add_u32 m0, s54, 10944\n"
    "v_mfma_f32_16x16x32_bf16 v[208:211], v[56:59], v[152:155], 0\n"
    "s_waitcnt lgkmcnt(1)\n"
    "v_cmp_gt_u32 vcc, 1, v228\n"
    "v_mfma_f32_16x16x32_bf16 v[212:215], v[80:83], v[152:155], 0\n"
    "v_cvt_pk_bf16_f32 v200, v200, v201\n"
    "s_cbranch_vccnz .Lg11\n"
    ".Lg11b:\n"
    "v_mfma_f32_16x16x32_bf16 v[208:211], v[60:63], v[156:159], v[208:211]\n"
    "v_cvt_pk_bf16_f32 v201, v202, v203\n"
    "v_cvt_pk_bf16_f32 v202, v204, v205\n"
    "v_mfma_f32_16x16x32_bf16 v[212:215], v[84:87], v[156:159], v[212:215]\n"
    "v_cvt_pk_bf16_f32 v203, v206, v207\n"
    "v_mfma_f32_16x16x32_bf16 v[208:211], v[64:67], v[160:163], v[208:211]\n"
    "ds_write_b128 v7, v[200:203] offset:0\n"
    "v_mfma_f32_16x16x32_bf16 v[212:215], v[88:91], v[160:163], v[212:215]\n"
    "global_load_lds_dwordx4 v231, s[60:61]\n"
    "s_add_u32 m0, s54, 12992\n"
    "v_mfma_f32_16x16x32_bf16 v[208:211], v[68:71], v[164:167], v[208:211]\n"
    "global_load_lds_dwordx4 v232, s[60:61]\n"
    "s_add_u32 m0, s55, 8896\n"
    "v_mfma_f32_16x16x32_bf16 v[212:215], v[92:95], v[164:167], v[212:215]\n"
    "global_load_lds_dwordx4 v233, s[60:61]\n"
    "s_add_u32 m0, s56, 8896\n"
    "v_mfma_f32_16x16x32_bf16 v[208:211], v[72:75], v[168:171], v[208:211]\n"
    "global_load_lds_dwordx4 v234, s[60:61]\n"
    "v_mfma_f32_16x16x32_bf16 v[212:215], v[96:99], v[168:171], v[212:215]\n"
    "v_mfma_f32_16x16x32_bf16 v[208:211], v[76:79], v[172:175], v[208:211]\n"
    "v_mfma_f32_16x16x32_bf16 v[212:215], v[100:103], v[172:175], v[212:215]\n"
    "v_mfma_f32_16x16x32_bf16 v[216:219], v[104:107], v[152:155], 0\n"
    "ds_read_b128 v[176:179], v5 offset:8896\n"
    "ds_read_b128 v[180:183], v5 offset:8960\n"
    "v_mfma_f32_16x16x32_bf16 v[220:223], v[128:131], v[152:155], 0\n"
    "ds_read_b128 v[184:187], v5 offset:9024\n"
    "ds_read_b128 v[188:191], v5 offset:9088\n"
    "v_mfma_f32_16x16x32_bf16 v[216:219], v[108:111], v[156:159], v[216:219]\n"
    "ds_read_b128 v[192:195], v5 offset:9152\n"
    "ds_read_b128 v[196:199], v5 offset:9216\n"
    "v_mfma_f32_16x16x32_bf16 v[220:223], v[132:135], v[156:159], v[220:223]\n"
    "v_cvt_pk_bf16_f32 v208, v208, v209\n"
    "v_cvt_pk_bf16_f32 v209, v210, v211\n"
    "v_mfma_f32_16x16x32_bf16 v[216:219], v[112:115], v[160:163], v[216:219]\n"
    "v_cvt_pk_bf16_f32 v210, v212, v213\n"
    "v_cvt_pk_bf16_f32 v211, v214, v215\n"
    "v_mfma_f32_16x16x32_bf16 v[220:223], v[136:139], v[160:163], v[220:223]\n"
    "ds_write_b128 v7, v[208:211] offset:1024\n"
    "v_mfma_f32_16x16x32_bf16 v[216:219], v[116:119], v[164:167], v[216:219]\n"
    "s_waitcnt lgkmcnt(8)\n"
    "v_cmp_gt_u32 vcc, 1, v239\n"
    "v_mfma_f32_16x16x32_bf16 v[220:223], v[140:143], v[164:167], v[220:223]\n"
    "v_mfma_f32_16x16x32_bf16 v[216:219], v[120:123], v[168:171], v[216:219]\n"
    "s_cbranch_vccnz .Lg12\n"
    ".Lg12b:\n"
    "v_mfma_f32_16x16x32_bf16 v[220:223], v[144:147], v[168:171], v[220:223]\n"
    "v_mfma_f32_16x16x32_bf16 v[216:219], v[124:127], v[172:175], v[216:219]\n"
    "v_mfma_f32_16x16x32_bf16 v[220:223], v[148:151], v[172:175], v[220:223]\n"
    "s_waitcnt lgkmcnt(1)\n"
    "v_mfma_f32_16x16x32_bf16 v[200:203], v[8:11], v[176:179], 0\n"
    "v_mov_b32 v227, 7\n"
    "v_mfma_f32_16x16x32_bf16 v[204:207], v[32:35], v[176:179], 0\n"
    "v_mfma_f32_16x16x32_bf16 v[200:203], v[12:15], v[180:183], v[200:203]\n"
    "v_mfma_f32_16x16x32_bf16 v[204:207], v[36:39], v[180:183], v[204:207]\n"
    "v_mfma_f32_16x16x32_bf16 v[200:203], v[16:19], v[184:187], v[200:203]\n"
    "v_cvt_pk_bf16_f32 v216, v216, v217\n"
    "v_cvt_pk_bf16_f32 v217, v218, v219\n"
    "v_mfma_f32_16x16x32_bf16 v[204:207], v[40:43], v[184:187], v[204:207]\n"
    "v_cvt_pk_bf16_f32 v218, v220, v221\n"
    "v_cvt_pk_bf16_f32 v219, v222, v223\n"
    "v_mfma_f32_16x16x32_bf16 v[200:203], v[20:23], v[188:191], v[200:203]\n"
    "ds_write_b128 v7, v[216:219] offset:2048\n"
    "v_mfma_f32_16x16x32_bf16 v[204:207], v[44:47], v[188:191], v[204:207]\n"
    "ds_write_b32 v225, v227 offset:16\n"
    "ds_read_b32 v228, v224 offset:192\n"
    "v_mfma_f32_16x16x32_bf16 v[200:203], v[24:27], v[192:195], v[200:203]\n"
    "ds_read_b32 v239, v224 offset:112\n"
    "v_mfma_f32_16x16x32_bf16 v[204:207], v[48:51], v[192:195], v[204:207]\n"
    "v_mfma_f32_16x16x32_bf16 v[200:203], v[28:31], v[196:199], v[200:203]\n"
    "v_mfma_f32_16x16x32_bf16 v[204:207], v[52:55], v[196:199], v[204:207]\n"
    "v_mfma_f32_16x16x32_bf16 v[208:211], v[56:59], v[176:179], 0\n"
    "v_mfma_f32_16x16x32_bf16 v[212:215], v[80:83], v[176:179], 0\n"
    "ds_read_b32 v229, v224 offset:0\n"
    "v_mfma_f32_16x16x32_bf16 v[208:211], v[60:63], v[180:183], v[208:211]\n"
    "v_mfma_f32_16x16x32_bf16 v[212:215], v[84:87], v[180:183], v[212:215]\n"
    "s_waitcnt lgkmcnt(2)\n"
    "v_cmp_gt_u32 vcc, 2, v228\n"
    "v_mfma_f32_16x16x32_bf16 v[208:211], v[64:67], v[184:187], v[208:211]\n"
    "v_cvt_pk_bf16_f32 v200, v200, v201\n"
    "s_cbranch_vccnz .Lg14\n"
    ".Lg14b:\n"
    "v_mfma_f32_16x16x32_bf16 v[212:215], v[88:91], v[184:187], v[212:215]\n"
    "v_cvt_pk_bf16_f32 v201, v202, v203\n"
    "v_cvt_pk_bf16_f32 v202, v204, v205\n"
    "v_mfma_f32_16x16x32_bf16 v[208:211], v[68:71], v[188:191], v[208:211]\n"
    "v_cvt_pk_bf16_f32 v203, v206, v207\n"
    "v_mfma_f32_16x16x32_bf16 v[212:215], v[92:95], v[188:191], v[212:215]\n"
    "ds_write_b128 v7, v[200:203] offset:6144\n"
    "v_mfma_f32_16x16x32_bf16 v[208:211], v[72:75], v[192:195], v[208:211]\n"
    "v_mfma_f32_16x16x32_bf16 v[212:215], v[96:99], v[192:195], v[212:215]\n"
    "s_waitcnt lgkmcnt(1)\n"
    "v_cmp_gt_u32 vcc, 7, v229\n"
    "v_mfma_f32_16x16x32_bf16 v[208:211], v[76:79], v[196:199], v[208:211]\n"
    "v_mfma_f32_16x16x32_bf16 v[212:215], v[100:103], v[196:199], v[212:215]\n"
    "s_cbranch_vccnz .Lg13\n"
    ".Lg13b:\n"
    "v_mfma_f32_16x16x32_bf16 v[216:219], v[104:107], v[176:179], 0\n"
    "ds_read_b128 v[152:155], v4 offset:26688\n"
    "ds_read_b128 v[156:159], v4 offset:26752\n"
    "v_mfma_f32_16x16x32_bf16 v[220:223], v[128:131], v[176:179], 0\n"
    "ds_read_b128 v[160:163], v4 offset:26816\n"
    "ds_read_b128 v[164:167], v4 offset:26880\n"
    "v_mfma_f32_16x16x32_bf16 v[216:219], v[108:111], v[180:183], v[216:219]\n"
    "ds_read_b128 v[168:171], v4 offset:26944\n"
    "ds_read_b128 v[172:175], v4 offset:27008\n"
    "v_mfma_f32_16x16x32_bf16 v[220:223], v[132:135], v[180:183], v[220:223]\n"
    "v_cvt_pk_bf16_f32 v208, v208, v209\n"
    "v_cvt_pk_bf16_f32 v209, v210, v211\n"
    "v_mfma_f32_16x16x32_bf16 v[216:219], v[112:115], v[184:187], v[216:219]\n"
    "v_cvt_pk_bf16_f32 v210, v212, v213\n"
    "v_cvt_pk_bf16_f32 v211, v214, v215\n"
    "v_mfma_f32_16x16x32_bf16 v[220:223], v[136:139], v[184:187], v[220:223]\n"
    "ds_write_b128 v7, v[208:211] offset:7168\n"
    "v_mfma_f32_16x16x32_bf16 v[216:219], v[116:119], v[188:191], v[216:219]\n"
    "v_cmp_gt_u32 vcc, 1, v239\n"
    "v_mfma_f32_16x16x32_bf16 v[220:223], v[140:143], v[188:191], v[220:223]\n"
    "s_cbranch_vccnz .Lg15\n"
    ".Lg15b:\n"
    "v_mfma_f32_16x16x32_bf16 v[216:219], v[120:123], v[192:195], v[216:219]\n"
    "s_add_u32 s60, s42, s46\n"
    "s_addc_u32 s61, s43, 0\n"
    "v_mfma_f32_16x16x32_bf16 v[220:223], v[144:147], v[192:195], v[220:223]\n"
    "s_add_u32 m0, s54, 17792\n"
    "v_mfma_f32_16x16x32_bf16 v[216:219], v[124:127], v[196:199], v[216:219]\n"
    "global_load_lds_dwordx4 v230, s[60:61]\n"
    "s_add_u32 m0, s54, 19840\n"
    "v_mfma_f32_16x16x32_bf16 v[220:223], v[148:151], v[196:199], v[220:223]\n"
    "global_load_lds_dwordx4 v231, s[60:61]\n"
    "s_add_u32 m0, s54, 21888\n"
    "s_waitcnt lgkmcnt(1)\n"
    "global_load_lds_dwordx4 v232, s[60:61]\n"
    "s_add_u32 m0, s55, 17792\n"
    "s_nop 0\n"
    "global_load_lds_dwordx4 v233, s[60:61]\n"
    "s_add_u32 m0, s56, 17792\n"
    "s_nop 0\n"
    "global_load_lds_dwordx4 v234, s[60:61]\n"
    "s_add_u32 s60, s42, s47\n"
    "s_addc_u32 s61, s43, 0\n"
    "s_add_u32 m0, s54, 26688\n"
    "s_nop 0\n"
    "global_load_lds_dwordx4 v230, s[60:61]\n"
    "s_add_u32 m0, s54, 28736\n"
    "s_nop 0\n"
    "global_load_lds_dwordx4 v231, s[60:61]\n"
    "s_add_u32 m0, s54, 30784\n"
    "s_nop 0\n"
    "global_load_lds_dwordx4 v232, s[60:61]\n"
    "s_add_u32 m0, s55, 26688\n"
    "s_nop 0\n"
    "global_load_lds_dwordx4 v233, s[60:61]\n"
    "s_add_u32 m0, s56, 26688\n"
    "s_nop 0\n"
    "global_load_lds_dwordx4 v234, s[60:61]\n"
    "v_mfma_f32_16x16x32_bf16 v[200:203], v[8:11], v[152:155], 0\n"
    "v_cvt_pk_bf16_f32 v216, v216, v217\n"
    "v_cvt_pk_bf16_f32 v217, v218, v219\n"
    "v_mfma_f32_16x16x32_bf16 v[204:207], v[32:35], v[152:155], 0\n"
    "v_cvt_pk_bf16_f32 v218, v220, v221\n"
    "v_cvt_pk_bf16_f32 v219, v222, v223\n"
    "v_mfma_f32_16x16x32_bf16 v[200:203], v[12:15], v[156:159], v[200:203]\n"
    "ds_write_b128 v7, v[216:219] offset:8192\n"
    "v_mfma_f32_16x16x32_bf16 v[204:207], v[36:39], v[156:159], v[204:207]\n"
    "v_mov_b32 v227, 8\n"
    "v_mfma_f32_16x16x32_bf16 v[200:203], v[16:19], v[160:163], v[200:203]\n"
    "ds_write_b32 v225, v227 offset:16\n"
    "ds_read_b32 v228, v224 offset:208\n"
    "v_mfma_f32_16x16x32_bf16 v[204:207], v[40:43], v[160:163], v[204:207]\n"
    "ds_read_b32 v239, v224 offset:64\n"
    "s_waitcnt vmcnt(10)\n"
    "v_mfma_f32_16x16x32_bf16 v[200:203], v[20:23], v[164:167], v[200:203]\n"
    "v_mov_b32 v227, 8\n"
    "v_mfma_f32_16x16x32_bf16 v[204:207], v[44:47], v[164:167], v[204:207]\n"
    "ds_write_b32 v226, v227 offset:0\n"
    "v_mfma_f32_16x16x32_bf16 v[200:203], v[24:27], v[168:171], v[200:203]\n"
    "v_mfma_f32_16x16x32_bf16 v[204:207], v[48:51], v[168:171], v[204:207]\n"
    "v_mfma_f32_16x16x32_bf16 v[200:203], v[28:31], v[172:175], v[200:203]\n"
    "v_mfma_f32_16x16x32_bf16 v[204:207], v[52:55], v[172:175], v[204:207]\n"
    "v_mfma_f32_16x16x32_bf16 v[208:211], v[56:59], v[152:155], 0\n"
    "s_waitcnt lgkmcnt(2)\n"
    "v_cmp_gt_u32 vcc, 2, v228\n"
    "v_mfma_f32_16x16x32_bf16 v[212:215], v[80:83], v[152:155], 0\n"
    "v_mfma_f32_16x16x32_bf16 v[208:211], v[60:63], v[156:159], v[208:211]\n"
    "s_cbranch_vccnz .Lg16\n"
    ".Lg16b:\n"
    "v_mfma_f32_16x16x32_bf16 v[212:215], v[84:87], v[156:159], v[212:215]\n"
    "v_cvt_pk_bf16_f32 v200, v200, v201\n"
    "v_cvt_pk_bf16_f32 v201, v202, v203\n"
    "v_mfma_f32_16x16x32_bf16 v[208:211], v[64:67], v[160:163], v[208:211]\n"
    "v_cvt_pk_bf16_f32 v202, v204, v205\n"
    "v_cvt_pk_bf16_f32 v203, v206, v207\n"
    "v_mfma_f32_16x16x32_bf16 v[212:215], v[88:91], v[160:163], v[212:215]\n"
    "ds_write_b128 v7, v[200:203] offset:12288\n"
    "v_mfma_f32_16x16x32_bf16 v[208:211], v[68:71], v[164:167], v[208:211]\n"
    "v_mfma_f32_16x16x32_bf16 v[212:215], v[92:95], v[164:167], v[212:215]\n"
    "v_mfma_f32_16x16x32_bf16 v[208:211], v[72:75], v[168:171], v[208:211]\n"
    "v_mfma_f32_16x16x32_bf16 v[212:215], v[96:99], v[168:171], v[212:215]\n"
    "v_mfma_f32_16x16x32_bf16 v[208:211], v[76:79], v[172:175], v[208:211]\n"
    "v_mfma_f32_16x16x32_bf16 v[212:215], v[100:103], v[172:175], v[212:215]\n"
    "v_mfma_f32_16x16x32_bf16 v[216:219], v[104:107], v[152:155], 0\n"
    "ds_read_b128 v[176:179], v5 offset:26688\n"
    "ds_read_b128 v[180:183], v5 offset:26752\n"
    "v_mfma_f32_16x16x32_bf16 v[220:223], v[128:131], v[152:155], 0\n"
    "ds_read_b128 v[184:187], v5 offset:26816\n"
    "ds_read_b128 v[188:191], v5 offset:26880\n"
    "v_mfma_f32_16x16x32_bf16 v[216:219], v[108:111], v[156:159], v[216:219]\n"
    "ds_read_b128 v[192:195], v5 offset:26944\n"
    "ds_read_b128 v[196:199], v5 offset:27008\n"
    "v_mfma_f32_16x16x32_bf16 v[220:223], v[132:135], v[156:159], v[220:223]\n"
    "v_cvt_pk_bf16_f32 v208, v208, v209\n"
    "v_cvt_pk_bf16_f32 v209, v210, v211\n"
    "v_mfma_f32_16x16x32_bf16 v[216:219], v[112:115], v[160:163], v[216:219]\n"
    "v_cvt_pk_bf16_f32 v210, v212, v213\n"
    "v_cvt_pk_bf16_f32 v211, v214, v215\n"
    "v_mfma_f32_16x16x32_bf16 v[220:223], v[136:139], v[160:163], v[220:223]\n"
    "ds_write_b128 v7, v[208:211] offset:13312\n"
    "v_mfma_f32_16x16x32_bf16 v[216:219], v[116:119], v[164:167], v[216:219]\n"
    "s_waitcnt lgkmcnt(9)\n"
    "v_cmp_gt_u32 vcc, 2, v239\n"
    "v_mfma_f32_16x16x32_bf16 v[220:223], v[140:143], v[164:167], v[220:223]\n"
    "v_mfma_f32_16x16x32_bf16 v[216:219], v[120:123], v[168:171], v[216:219]\n"
    "s_cbranch_vccnz .Lg17\n"
    ".Lg17b:\n"
    "v_mfma_f32_16x16x32_bf16 v[220:223], v[144:147], v[168:171], v[220:223]\n"
    "v_mfma_f32_16x16x32_bf16 v[216:219], v[124:127], v[172:175], v[216:219]\n"
    "v_mfma_f32_16x16x32_bf16 v[220:223], v[148:151], v[172:175], v[220:223]\n"
    "s_waitcnt lgkmcnt(1)\n"
    "v_mfma_f32_16x16x32_bf16 v[200:203], v[8:11], v[176:179], 0\n"
    "v_mov_b32 v227, 9\n"
    "v_mfma_f32_16x16x32_bf16 v[204:207], v[32:35], v[176:179], 0\n"
    "v_mfma_f32_16x16x32_bf16 v[200:203], v[12:15], v[180:183], v[200:203]\n"
    "v_mfma_f32_16x16x32_bf16 v[204:207], v[36:39], v[180:183], v[204:207]\n"
    "v_mfma_f32_16x16x32_bf16 v[200:203], v[16:19], v[184:187], v[200:203]\n"
    "v_cvt_pk_bf16_f32 v216, v216, v217\n"
    "v_cvt_pk_bf16_f32 v217, v218, v219\n"
    "v_mfma_f32_16x16x32_bf16 v[204:207], v[40:43], v[184:187], v[204:207]\n"
    "v_cvt_pk_bf16_f32 v218, v220, v221\n"
    "v_cvt_pk_bf16_f32 v219, v222, v223\n"
    "v_mfma_f32_16x16x32_bf16 v[200:203], v[20:23], v[188:191], v[200:203]\n"
    "ds_write_b128 v7, v[216:219] offset:14336\n"
    "v_mfma_f32_16x16x32_bf16 v[204:207], v[44:47], v[188:191], v[204:207]\n"
    "ds_write_b32 v225, v227 offset:16\n"
    "ds_read_b32 v228, v224 offset:224\n"
    "v_mfma_f32_16x16x32_bf16 v[200:203], v[24:27], v[192:195], v[200:203]\n"
    "ds_read_b32 v239, v224 offset:80\n"
    "v_mfma_f32_16x16x32_bf16 v[204:207], v[48:51], v[192:195], v[204:207]\n"
    "v_mfma_f32_16x16x32_bf16 v[200:203], v[28:31], v[196:199], v[200:203]\n"
    "v_mfma_f32_16x16x32_bf16 v[204:207], v[52:55], v[196:199], v[204:207]\n"
    "v_mfma_f32_16x16x32_bf16 v[208:211], v[56:59], v[176:179], 0\n"
    "v_mfma_f32_16x16x32_bf16 v[212:215], v[80:83], v[176:179], 0\n"
    "ds_read_b32 v229, v224 offset:0\n"
    "v_mfma_f32_16x16x32_bf16 v[208:211], v[60:63], v[180:183], v[208:211]\n"
    "v_mfma_f32_16x16x32_bf16 v[212:215], v[84:87], v[180:183], v[212:215]\n"
    "s_waitcnt lgkmcnt(2)\n"
    "v_cmp_gt_u32 vcc, 2, v228\n"
    "v_mfma_f32_16x16x32_bf16 v[208:211], v[64:67], v[184:187], v[208:211]\n"
    "v_cvt_pk_bf16_f32 v200, v200, v201\n"
    "s_cbranch_vccnz .Lg19\n"
    ".Lg19b:\n"
    "v_mfma_f32_16x16x32_bf16 v[212:215], v[88:91], v[184:187], v[212:215]\n"
    "v_cvt_pk_bf16_f32 v201, v202, v203\n"
    "v_cvt_pk_bf16_f32 v202, v204, v205\n"
    "v_mfma_f32_16x16x32_bf16 v[208:211], v[68:71], v[188:191], v[208:211]\n"
    "v_cvt_pk_bf16_f32 v203, v206, v207\n"
    "v_mfma_f32_16x16x32_bf16 v[212:215], v[92:95], v[188:191], v[212:215]\n"
    "ds_write_b128 v7, v[200:203] offset:0\n"
    "v_mfma_f32_16x16x32_bf16 v[208:211], v[72:75], v[192:195], v[208:211]\n"
    "v_mfma_f32_16x16x32_bf16 v[212:215], v[96:99], v[192:195], v[212:215]\n"
    "s_waitcnt lgkmcnt(1)\n"
    "v_cmp_gt_u32 vcc, 8, v229\n"
    "v_mfma_f32_16x16x32_bf16 v[208:211], v[76:79], v[196:199], v[208:211]\n"
    "v_mfma_f32_16x16x32_bf16 v[212:215], v[100:103], v[196:199], v[212:215]\n"
    "s_cbranch_vccnz .Lg18\n"
    ".Lg18b:\n"
    "v_mfma_f32_16x16x32_bf16 v[216:219], v[104:107], v[176:179], 0\n"
    "v_add_u32 v4, v6, v4\n"
    "v_cmp_gt_u32 vcc, 2, v239\n"
    "v_mfma_f32_16x16x32_bf16 v[220:223], v[128:131], v[176:179], 0\n"
    "ds_read_b128 v[152:155], v4 offset:0\n"
    "ds_read_b128 v[156:159], v4 offset:64\n"
    "v_mfma_f32_16x16x32_bf16 v[216:219], v[108:111], v[180:183], v[216:219]\n"
    "ds_read_b128 v[160:163], v4 offset:128\n"
    "ds_read_b128 v[164:167], v4 offset:192\n"
    "v_mfma_f32_16x16x32_bf16 v[220:223], v[132:135], v[180:183], v[220:223]\n"
    "ds_read_b128 v[168:171], v4 offset:256\n"
    "ds_read_b128 v[172:175], v4 offset:320\n"
    "v_mfma_f32_16x16x32_bf16 v[216:219], v[112:115], v[184:187], v[216:219]\n"
    "v_cvt_pk_bf16_f32 v208, v208, v209\n"
    "v_cvt_pk_bf16_f32 v209, v210, v211\n"
    "v_mfma_f32_16x16x32_bf16 v[220:223], v[136:139], v[184:187], v[220:223]\n"
    "v_cvt_pk_bf16_f32 v210, v212, v213\n"
    "v_cvt_pk_bf16_f32 v211, v214, v215\n"
    "v_mfma_f32_16x16x32_bf16 v[216:219], v[116:119], v[188:191], v[216:219]\n"
    "ds_write_b128 v7, v[208:211] offset:1024\n"
    "v_mfma_f32_16x16x32_bf16 v[220:223], v[140:143], v[188:191], v[220:223]\n"
    "s_cbranch_vccnz .Lg20\n"
    ".Lg20b:\n"
    "v_mfma_f32_16x16x32_bf16 v[216:219], v[120:123], v[192:195], v[216:219]\n"
    "s_add_u32 s60, s42, s48\n"
    "s_addc_u32 s61, s43, 0\n"
    "v_mfma_f32_16x16x32_bf16 v[220:223], v[144:147], v[192:195], v[220:223]\n"
    "s_add_u32 m0, s54, 35584\n"
    "v_mfma_f32_16x16x32_bf16 v[216:219], v[124:127], v[196:199], v[216:219]\n"
    "global_load_lds_dwordx4 v230, s[60:61]\n"
    "s_add_u32 m0, s54, 37632\n"
    "v_mfma_f32_16x16x32_bf16 v[220:223], v[148:151], v[196:199], v[220:223]\n"
    "global_load_lds_dwordx4 v231, s[60:61]\n"
    "s_add_u32 m0, s54, 39680\n"
    "s_waitcnt lgkmcnt(1)\n"
    "global_load_lds_dwordx4 v232, s[60:61]\n"
    "s_add_u32 m0, s55, 35584\n"
    "s_nop 0\n"
    "global_load_lds_dwordx4 v233, s[60:61]\n"
    "s_add_u32 m0, s56, 35584\n"
    "s_nop 0\n"
    "global_load_lds_dwordx4 v234, s[60:61]\n"
    "s_add_u32 s60, s42, s49\n"
    "s_addc_u32 s61, s43, 0\n"
    "s_add_u32 m0, s54, 44480\n"
    "s_nop 0\n"
    "global_load_lds_dwordx4 v230, s[60:61]\n"
    "s_add_u32 m0, s54, 46528\n"
    "s_nop 0\n"
    "global_load_lds_dwordx4 v231, s[60:61]\n"
    "s_add_u32 m0, s54, 48576\n"
    "s_nop 0\n"
    "global_load_lds_dwordx4 v232, s[60:61]\n"
    "s_add_u32 m0, s55, 44480\n"
    "s_nop 0\n"
    "global_load_lds_dwordx4 v233, s[60:61]\n"
    "s_add_u32 m0, s56, 44480\n"
    "s_nop 0\n"
    "global_load_lds_dwordx4 v234, s[60:61]\n"
    "v_mfma_f32_16x16x32_bf16 v[200:203], v[8:11], v[152:155], 0\n"
    "v_cvt_pk_bf16_f32 v216, v216, v217\n"
    "v_cvt_pk_bf16_f32 v217, v218, v219\n"
    "v_mfma_f32_16x16x32_bf16 v[204:207], v[32:35], v[152:155], 0\n"
    "v_cvt_pk_bf16_f32 v218, v220, v221\n"
    "v_cvt_pk_bf16_f32 v219, v222, v223\n"
    "v_mfma_f32_16x16x32_bf16 v[200:203], v[12:15], v[156:159], v[200:203]\n"
    "ds_write_b128 v7, v[216:219] offset:2048\n"
    "v_mfma_f32_16x16x32_bf16 v[204:207], v[36:39], v[156:159], v[204:207]\n"
    "v_mov_b32 v227, 10\n"
    "v_mfma_f32_16x16x32_bf16 v[200:203], v[16:19], v[160:163], v[200:203]\n"
    "ds_write_b32 v225, v227 offset:16\n"
    "ds_read_b32 v228, v224 offset:240\n"
    "v_mfma_f32_16x16x32_bf16 v[204:207], v[40:43], v[160:163], v[204:207]\n"
    "s_waitcnt vmcnt(10)\n"
    "v_mov_b32 v227, 9\n"
    "v_mfma_f32_16x16x32_bf16 v[200:203], v[20:23], v[164:167], v[200:203]\n"
    "ds_write_b32 v226, v227 offset:0\n"
    "v_mfma_f32_16x16x32_bf16 v[204:207], v[44:47], v[164:167], v[204:207]\n"
    "v_mfma_f32_16x16x32_bf16 v[200:203], v[24:27], v[168:171], v[200:203]\n"
    "v_mfma_f32_16x16x32_bf16 v[204:207], v[48:51], v[168:171], v[204:207]\n"
    "v_mfma_f32_16x16x32_bf16 v[200:203], v[28:31], v[172:175], v[200:203]\n"
    "v_mfma_f32_16x16x32_bf16 v[204:207], v[52:55], v[172:175], v[204:207]\n"
    "v_mfma_f32_16x16x32_bf16 v[208:211], v[56:59], v[152:155], 0\n"
    "s_waitcnt lgkmcnt(1)\n"
    "v_cmp_gt_u32 vcc, 2, v228\n"
    "v_mfma_f32_16x16x32_bf16 v[212:215], v[80:83], v[152:155], 0\n"
    "v_mfma_f32_16x16x32_bf16 v[208:211], v[60:63], v[156:159], v[208:211]\n"
    "s_cbranch_vccnz .Lg21\n"
    ".Lg21b:\n"
    "v_mfma_f32_16x16x32_bf16 v[212:215], v[84:87], v[156:159], v[212:215]\n"
    "v_cvt_pk_bf16_f32 v200, v200, v201\n"
    "v_cvt_pk_bf16_f32 v201, v202, v203\n"
    "v_mfma_f32_16x16x32_bf16 v[208:211], v[64:67], v[160:163], v[208:211]\n"
    "v_cvt_pk_bf16_f32 v202, v204, v205\n"
    "v_cvt_pk_bf16_f32 v203, v206, v207\n"
    "v_mfma_f32_16x16x32_bf16 v[212:215], v[88:91], v[160:163], v[212:215]\n"
    "ds_write_b128 v7, v[200:203] offset:6144\n"
    "v_mfma_f32_16x16x32_bf16 v[208:211], v[68:71], v[164:167], v[208:211]\n"
    "v_mfma_f32_16x16x32_bf16 v[212:215], v[92:95], v[164:167], v[212:215]\n"
    "v_mfma_f32_16x16x32_bf16 v[208:211], v[72:75], v[168:171], v[208:211]\n"
    "v_mfma_f32_16x16x32_bf16 v[212:215], v[96:99], v[168:171], v[212:215]\n"
    "v_mfma_f32_16x16x32_bf16 v[208:211], v[76:79], v[172:175], v[208:211]\n"
    "v_mfma_f32_16x16x32_bf16 v[212:215], v[100:103], v[172:175], v[212:215]\n"
    "v_mfma_f32_16x16x32_bf16 v[216:219], v[104:107], v[152:155], 0\n"
    "v_add_u32 v5, v6, v5\n"
    "v_mfma_f32_16x16x32_bf16 v[220:223], v[128:131], v[152:155], 0\n"
    "ds_read_b128 v[176:179], v5 offset:0\n"
    "ds_read_b128 v[180:183], v5 offset:64\n"
    "v_mfma_f32_16x16x32_bf16 v[216:219], v[108:111], v[156:159], v[216:219]\n"
    "ds_read_b128 v[184:187], v5 offset:128\n"
    "ds_read_b128 v[188:191], v5 offset:192\n"
    "v_mfma_f32_16x16x32_bf16 v[220:223], v[132:135], v[156:159], v[220:223]\n"
    "ds_read_b128 v[192:195], v5 offset:256\n"
    "ds_read_b128 v[196:199], v5 offset:320\n"
    "v_mfma_f32_16x16x32_bf16 v[216:219], v[112:115], v[160:163], v[216:219]\n"
    "v_cvt_pk_bf16_f32 v208, v208, v209\n"
    "v_cvt_pk_bf16_f32 v209, v210, v211\n"
    "v_mfma_f32_16x16x32_bf16 v[220:223], v[136:139], v[160:163], v[220:223]\n"
    "v_cvt_pk_bf16_f32 v210, v212, v213\n"
    "v_cvt_pk_bf16_f32 v211, v214, v215\n"
    "v_mfma_f32_16x16x32_bf16 v[216:219], v[116:119], v[164:167], v[216:219]\n"
    "ds_write_b128 v7, v[208:211] offset:7168\n"
    "v_mfma_f32_16x16x32_bf16 v[220:223], v[140:143], v[164:167], v[220:223]\n"
    "v_mfma_f32_16x16x32_bf16 v[216:219], v[120:123], v[168:171], v[216:219]\n"
    "v_mfma_f32_16x16x32_bf16 v[220:223], v[144:147], v[168:171], v[220:223]\n"
    "v_mfma_f32_16x16x32_bf16 v[216:219], v[124:127], v[172:175], v[216:219]\n"
    "v_mfma_f32_16x16x32_bf16 v[220:223], v[148:151], v[172:175], v[220:223]\n"
    "s_waitcnt lgkmcnt(1)\n"
    "s_lshl1_add_u32 s53, s58, s50\n"
    "s_cmp_eq_u32 s53, 2\n"
    "s_cbranch_scc1 .Lg_e12a\n"
    "s_cmp_eq_u32 s53, 3\n"
    "s_cbranch_scc1 .Lg_e12b\n"
    "v_mfma_f32_16x16x32_bf16 v[200:203], v[8:11], v[176:179], 0\n"
    "v_cvt_pk_bf16_f32 v216, v216, v217\n"
    "v_cvt_pk_bf16_f32 v217, v218, v219\n"
    "v_mfma_f32_16x16x32_bf16 v[204:207], v[32:35], v[176:179], 0\n"
    "v_cvt_pk_bf16_f32 v218, v220, v221\n"
    "v_cvt_pk_bf16_f32 v219, v222, v223\n"
    "v_mfma_f32_16x16x32_bf16 v[200:203], v[12:15], v[180:183], v[200:203]\n"
    "ds_write_b128 v7, v[216:219] offset:8192\n"
    "v_mfma_f32_16x16x32_bf16 v[204:207], v[36:39], v[180:183], v[204:207]\n"
    "v_mov_b32 v227, 11\n"
    "v_mfma_f32_16x16x32_bf16 v[200:203], v[16:19], v[184:187], v[200:203]\n"
    "ds_write_b32 v225, v227 offset:16\n"
    "ds_read_b32 v228, v224 offset:192\n"
    "v_mfma_f32_16x16x32_bf16 v[204:207], v[40:43], v[184:187], v[204:207]\n"
    "v_mfma_f32_16x16x32_bf16 v[200:203], v[20:23], v[188:191], v[200:203]\n"
    "v_mfma_f32_16x16x32_bf16 v[204:207], v[44:47], v[188:191], v[204:207]\n"
    "v_mfma_f32_16x16x32_bf16 v[200:203], v[24:27], v[192:195], v[200:203]\n"
    "v_mfma_f32_16x16x32_bf16 v[204:207], v[48:51], v[192:195], v[204:207]\n"
    "v_mfma_f32_16x16x32_bf16 v[200:203], v[28:31], v[196:199], v[200:203]\n"
    "v_mfma_f32_16x16x32_bf16 v[204:207], v[52:55], v[196:199], v[204:207]\n"
    "v_mfma_f32_16x16x32_bf16 v[208:211], v[56:59], v[176:179], 0\n"
    "s_waitcnt lgkmcnt(0)\n"
    "v_cmp_gt_u32 vcc, 3, v228\n"
    "v_mfma_f32_16x16x32_bf16 v[212:215], v[80:83], v[176:179], 0\n"
    "ds_read_b32 v229, v224 offset:0\n"
    "s_cbranch_vccnz .Lg26\n"
    ".Lg26b:\n"
    "v_mfma_f32_16x16x32_bf16 v[208:211], v[60:63], v[180:183], v[208:211]\n"
    "v_cvt_pk_bf16_f32 v200, v200, v201\n"
    "v_cvt_pk_bf16_f32 v201, v202, v203\n"
    "v_mfma_f32_16x16x32_bf16 v[212:215], v[84:87], v[180:183], v[212:215]\n"
    "v_cvt_pk_bf16_f32 v202, v204, v205\n"
    "v_cvt_pk_bf16_f32 v203, v206, v207\n"
    "v_mfma_f32_16x16x32_bf16 v[208:211], v[64:67], v[184:187], v[208:211]\n"
    "ds_write_b128 v7, v[200:203] offset:12288\n"
    "v_mfma_f32_16x16x32_bf16 v[212:215], v[88:91], v[184:187], v[212:215]\n"
    "v_mfma_f32_16x16x32_bf16 v[208:211], v[68:71], v[188:191], v[208:211]\n"
    "v_mfma_f32_16x16x32_bf16 v[212:215], v[92:95], v[188:191], v[212:215]\n"
    "v_mfma_f32_16x16x32_bf16 v[208:211], v[72:75], v[192:195], v[208:211]\n"
    "v_mfma_f32_16x16x32_bf16 v[212:215], v[96:99], v[192:195], v[212:215]\n"
    "s_waitcnt lgkmcnt(1)\n"
    "v_cmp_gt_u32 vcc, 9, v229\n"
    "v_mfma_f32_16x16x32_bf16 v[208:211], v[76:79], v[196:199], v[208:211]\n"
    "v_mfma_f32_16x16x32_bf16 v[212:215], v[100:103], v[196:199], v[212:215]\n"
    "s_cbranch_vccnz .Lg25\n"
    ".Lg25b:\n"
    "v_mfma_f32_16x16x32_bf16 v[216:219], v[104:107], v[176:179], 0\n"
    "v_add_u32 v2, s84, v2\n"
    "v_mfma_f32_16x16x32_bf16 v[220:223], v[128:131], v[176:179], 0\n"
    "ds_read_b128 v[152:155], v2 offset:8896\n"
    "ds_read_b128 v[156:159], v2 offset:8960\n"
    "v_mfma_f32_16x16x32_bf16 v[216:219], v[108:111], v[180:183], v[216:219]\n"
    "ds_read_b128 v[160:163], v2 offset:9024\n"
    "ds_read_b128 v[164:167], v2 offset:9088\n"
    "v_mfma_f32_16x16x32_bf16 v[220:223], v[132:135], v[180:183], v[220:223]\n"
    "ds_read_b128 v[168:171], v2 offset:9152\n"
    "ds_read_b128 v[172:175], v2 offset:9216\n"
    "v_mfma_f32_16x16x32_bf16 v[216:219], v[112:115], v[184:187], v[216:219]\n"
    "v_cvt_pk_bf16_f32 v208, v208, v209\n"
    "v_cvt_pk_bf16_f32 v209, v210, v211\n"
    "v_mfma_f32_16x16x32_bf16 v[220:223], v[136:139], v[184:187], v[220:223]\n"
    "v_cvt_pk_bf16_f32 v210, v212, v213\n"
    "v_cvt_pk_bf16_f32 v211, v214, v215\n"
    "v_mfma_f32_16x16x32_bf16 v[216:219], v[116:119], v[188:191], v[216:219]\n"
    "ds_write_b128 v7, v[208:211] offset:13312\n"
    "v_mfma_f32_16x16x32_bf16 v[220:223], v[140:143], v[188:191], v[220:223]\n"
    "v_mfma_f32_16x16x32_bf16 v[216:219], v[120:123], v[192:195], v[216:219]\n"
    "v_mfma_f32_16x16x32_bf16 v[220:223], v[144:147], v[192:195], v[220:223]\n"
    "v_mfma_f32_16x16x32_bf16 v[216:219], v[124:127], v[196:199], v[216:219]\n"
    "v_mfma_f32_16x16x32_bf16 v[220:223], v[148:151], v[196:199], v[220:223]\n"
    "s_waitcnt lgkmcnt(1)\n"
    "s_cmp_eq_u32 s50, 0\n"
    "s_cbranch_scc1 .Lg_e13a\n"
    "v_mfma_f32_16x16x32_bf16 v[200:203], v[8:11], v[152:155], 0\n"
    "v_mov_b32 v227, 12\n"
    "v_mfma_f32_16x16x32_bf16 v[204:207], v[32:35], v[152:155], 0\n"
    "v_mfma_f32_16x16x32_bf16 v[200:203], v[12:15], v[156:159], v[200:203]\n"
    "v_cvt_pk_bf16_f32 v216, v216, v217\n"
    "v_cvt_pk_bf16_f32 v217, v218, v219\n"
    "v_mfma_f32_16x16x32_bf16 v[204:207], v[36:39], v[156:159], v[204:207]\n"
    "v_cvt_pk_bf16_f32 v218, v220, v221\n"
    "v_cvt_pk_bf16_f32 v219, v222, v223\n"
    "v_mfma_f32_16x16x32_bf16 v[200:203], v[16:19], v[160:163], v[200:203]\n"
    "ds_write_b128 v7, v[216:219] offset:14336\n"
    "v_mfma_f32_16x16x32_bf16 v[204:207], v[40:43], v[160:163], v[204:207]\n"
    "ds_write_b32 v225, v227 offset:16\n"
    "ds_read_b32 v228, v224 offset:208\n"
    "v_mfma_f32_16x16x32_bf16 v[200:203], v[20:23], v[164:167], v[200:203]\n"
    "s_waitcnt vmcnt(0)\n"
    "v_mov_b32 v227, 10\n"
    "v_mfma_f32_16x16x32_bf16 v[204:207], v[44:47], v[164:167], v[204:207]\n"
    "ds_write_b32 v226, v227 offset:0\n"
    "v_mfma_f32_16x16x32_bf16 v[200:203], v[24:27], v[168:171], v[200:203]\n"
    "v_mfma_f32_16x16x32_bf16 v[204:207], v[48:51], v[168:171], v[204:207]\n"
    "v_mfma_f32_16x16x32_bf16 v[200:203], v[28:31], v[172:175], v[200:203]\n"
    "v_mfma_f32_16x16x32_bf16 v[204:207], v[52:55], v[172:175], v[204:207]\n"
    "ds_read_b32 v239, v224 offset:272\n"
    "s_waitcnt lgkmcnt(0)\n"
    "v_cmp_gt_u32 vcc, 1, v239\n"
    "s_cbranch_vccnz .Lg29\n"
    ".Lg29b:\n"
    "v_mfma_f32_16x16x32_bf16 v[208:211], v[56:59], v[152:155], 0\n"
    "v_cmp_gt_u32 vcc, 3, v228\n"
    "v_mfma_f32_16x16x32_bf16 v[212:215], v[80:83], v[152:155], 0\n"
    "s_cbranch_vccnz .Lg28\n"
    ".Lg28b:\n"
    "v_cvt_pk_bf16_f32 v200, v200, v201\n"
    "v_mfma_f32_16x16x32_bf16 v[208:211], v[60:63], v[156:159], v[208:211]\n"
    "v_cvt_pk_bf16_f32 v201, v202, v203\n"
    "v_cvt_pk_bf16_f32 v202, v204, v205\n"
    "v_mfma_f32_16x16x32_bf16 v[212:215], v[84:87], v[156:159], v[212:215]\n"
    "v_cvt_pk_bf16_f32 v203, v206, v207\n"
    "v_mfma_f32_16x16x32_bf16 v[208:211], v[64:67], v[160:163], v[208:211]\n"
    "ds_write_b128 v7, v[200:203] offset:0\n"
    "v_mfma_f32_16x16x32_bf16 v[212:215], v[88:91], v[160:163], v[212:215]\n"
    "s_add_u32 s60, s40, 109568\n"
    "s_addc_u32 s61, s41, 0\n"
    "v_mfma_f32_16x16x32_bf16 v[208:211], v[68:71], v[164:167], v[208:211]\n"
    "s_add_u32 s66, s60, 6144\n"
    "s_addc_u32 s67, s61, 0\n"
    "v_mfma_f32_16x16x32_bf16 v[212:215], v[92:95], v[164:167], v[212:215]\n"
    "global_load_dwordx4 v[8:11], v235, s[60:61] offset:-3072 sc1\n"
    "global_load_dwordx4 v[32:35], v235, s[66:67] offset:-3072 sc1\n"
    "v_mfma_f32_16x16x32_bf16 v[208:211], v[72:75], v[168:171], v[208:211]\n"
    "global_load_dwordx4 v[12:15], v235, s[60:61] offset:-2048 sc1\n"
    "global_load_dwordx4 v[36:39], v235, s[66:67] offset:-2048 sc1\n"
    "v_mfma_f32_16x16x32_bf16 v[212:215], v[96:99], v[168:171], v[212:215]\n"
    "global_load_dwordx4 v[16:19], v235, s[60:61] offset:-1024 sc1\n"
    "global_load_dwordx4 v[40:43], v235, s[66:67] offset:-1024 sc1\n"
    "v_mfma_f32_16x16x32_bf16 v[208:211], v[76:79], v[172:175], v[208:211]\n"
    "global_load_dwordx4 v[20:23], v235, s[60:61] offset:0 sc1\n"
    "global_load_dwordx4 v[44:47], v235, s[66:67] offset:0 sc1\n"
    "v_mfma_f32_16x16x32_bf16 v[212:215], v[100:103], v[172:175], v[212:215]\n"
    "global_load_dwordx4 v[24:27], v235, s[60:61] offset:1024 sc1\n"
    "global_load_dwordx4 v[48:51], v235, s[66:67] offset:1024 sc1\n"
    "v_mfma_f32_16x16x32_bf16 v[216:219], v[104:107], v[152:155], 0\n"
    "global_load_dwordx4 v[28:31], v235, s[60:61] offset:2048 sc1\n"
    "global_load_dwordx4 v[52:55], v235, s[66:67] offset:2048 sc1\n"
    "v_mfma_f32_16x16x32_bf16 v[220:223], v[128:131], v[152:155], 0\n"
    "v_cvt_pk_bf16_f32 v208, v208, v209\n"
    "v_cvt_pk_bf16_f32 v209, v210, v211\n"
    "v_mfma_f32_16x16x32_bf16 v[216:219], v[108:111], v[156:159], v[216:219]\n"
    "v_cvt_pk_bf16_f32 v210, v212, v213\n"
    "v_cvt_pk_bf16_f32 v211, v214, v215\n"
    "v_mfma_f32_16x16x32_bf16 v[220:223], v[132:135], v[156:159], v[220:223]\n"
    "ds_write_b128 v7, v[208:211] offset:1024\n"
    "v_mfma_f32_16x16x32_bf16 v[216:219], v[112:115], v[160:163], v[216:219]\n"
    "s_add_u32 s60, s40, 146432\n"
    "s_addc_u32 s61, s41, 0\n"
    "v_mfma_f32_16x16x32_bf16 v[220:223], v[136:139], v[160:163], v[220:223]\n"
    "s_add_u32 s66, s60, 6144\n"
    "s_addc_u32 s67, s61, 0\n"
    "v_mfma_f32_16x16x32_bf16 v[216:219], v[116:119], v[164:167], v[216:219]\n"
    "global_load_dwordx4 v[56:59], v235, s[60:61] offset:-3072 sc1\n"
    "global_load_dwordx4 v[80:83], v235, s[66:67] offset:-3072 sc1\n"
    "v_mfma_f32_16x16x32_bf16 v[220:223], v[140:143], v[164:167], v[220:223]\n"
    "global_load_dwordx4 v[60:63], v235, s[60:61] offset:-2048 sc1\n"
    "global_load_dwordx4 v[84:87], v235, s[66:67] offset:-2048 sc1\n"
    "v_mfma_f32_16x16x32_bf16 v[216:219], v[120:123], v[168:171], v[216:219]\n"
    "global_load_dwordx4 v[64:67], v235, s[60:61] offset:-1024 sc1\n"
    "global_load_dwordx4 v[88:91], v235, s[66:67] offset:-1024 sc1\n"
    "v_mfma_f32_16x16x32_bf16 v[220:223], v[144:147], v[168:171], v[220:223]\n"
    "global_load_dwordx4 v[68:71], v235, s[60:61] offset:0 sc1\n"
    "global_load_dwordx4 v[92:95], v235, s[66:67] offset:0 sc1\n"
    "v_mfma_f32_16x16x32_bf16 v[216:219], v[124:127], v[172:175], v[216:219]\n"
    "global_load_dwordx4 v[72:75], v235, s[60:61] offset:1024 sc1\n"
    "global_load_dwordx4 v[96:99], v235, s[66:67] offset:1024 sc1\n"
    "v_mfma_f32_16x16x32_bf16 v[220:223], v[148:151], v[172:175], v[220:223]\n"
    "global_load_dwordx4 v[76:79], v235, s[60:61] offset:2048 sc1\n"
    "global_load_dwordx4 v[100:103], v235, s[66:67] offset:2048 sc1\n"
    "s_nop 2\n"
    "v_cvt_pk_bf16_f32 v216, v216, v217\n"
    "v_cvt_pk_bf16_f32 v217, v218, v219\n"
    "s_nop 0\n"
    "v_cvt_pk_bf16_f32 v218, v220, v221\n"
    "v_cvt_pk_bf16_f32 v219, v222, v223\n"
    "ds_write_b128 v7, v[216:219] offset:2048\n"
    "v_mov_b32 v227, 13\n"
    "ds_write_b32 v225, v227 offset:16\n"
    ".Lg_to:\n"
    "s_waitcnt vmcnt(12)\n"
    "v_sub_u32 v8, v8, v240\n"
    "v_sub_u32 v12, v12, v240\n"
    "v_sub_u32 v16, v16, v240\n"
    "v_sub_u32 v20, v20, v240\n"
    "v_sub_u32 v24, v24, v240\n"
    "v_sub_u32 v28, v28, v240\n"
    "v_sub_u32 v32, v32, v240\n"
    "v_sub_u32 v36, v36, v240\n"
    "v_sub_u32 v40, v40, v240\n"
    "v_sub_u32 v44, v44, v240\n"
    "v_sub_u32 v48, v48, v240\n"
    "v_sub_u32 v52, v52, v240\n"
    "s_waitcnt vmcnt(0)\n"
    "v_sub_u32 v56, v56, v240\n"
    "v_sub_u32 v60, v60, v240\n"
    "v_sub_u32 v64, v64, v240\n"
    "v_sub_u32 v68, v68, v240\n"
    "v_sub_u32 v72, v72, v240\n"
    "v_sub_u32 v76, v76, v240\n"
    "v_sub_u32 v80, v80, v240\n"
    "v_sub_u32 v84, v84, v240\n"
    "v_sub_u32 v88, v88, v240\n"
    "v_sub_u32 v92, v92, v240\n"
    "v_sub_u32 v96, v96, v240\n"
    "v_sub_u32 v100, v100, v240\n"
    "ds_read_b32 v228, v224 offset:144\n"
    "s_waitcnt lgkmcnt(0)\n"
    "v_cmp_gt_u32 vcc, 3, v228\n"
    "s_cbranch_vccnz .Lg30\n"
    ".Lg30b:\n"
    "ds_read_b64_tr_b16 v[176:177], v0 offset:18432\n"
    "ds_read_b64_tr_b16 v[178:179], v1 offset:18432\n"
    "ds_read_b64_tr_b16 v[180:181], v0 offset:19456\n"
    "ds_read_b64_tr_b16 v[182:183], v1 offset:19456\n"
    "ds_read_b64_tr_b16 v[184:185], v0 offset:20480\n"
    "ds_read_b64_tr_b16 v[186:187], v1 offset:20480\n"
    "ds_read_b64_tr_b16 v[188:189], v0 offset:21504\n"
    "ds_read_b64_tr_b16 v[190:191], v1 offset:21504\n"
    "ds_read_b64_tr_b16 v[192:193], v0 offset:22528\n"
    "ds_read_b64_tr_b16 v[194:195], v1 offset:22528\n"
    "ds_read_b64_tr_b16 v[196:197], v0 offset:23552\n"
    "ds_read_b64_tr_b16 v[198:199], v1 offset:23552\n"
    "ds_read_b32 v253, v238 offset:0\n"
    "s_waitcnt lgkmcnt(9)\n"
    "v_mfma_f32_16x16x32_bf16 v[200:203], v[8:11], v[176:179], 0\n"
    "v_mfma_f32_16x16x32_bf16 v[204:207], v[32:35], v[176:179], 0\n"
    "ds_read_b32 v228, v224 offset:160\n"
    "v_mfma_f32_16x16x32_bf16 v[200:203], v[12:15], v[180:183], v[200:203]\n"
    "v_mfma_f32_16x16x32_bf16 v[204:207], v[36:39], v[180:183], v[204:207]\n"
    "s_waitcnt lgkmcnt(8)\n"
    "v_mfma_f32_16x16x32_bf16 v[200:203], v[16:19], v[184:187], v[200:203]\n"
    "v_mfma_f32_16x16x32_bf16 v[204:207], v[40:43], v[184:187], v[204:207]\n"
    "s_waitcnt lgkmcnt(6)\n"
    "v_mfma_f32_16x16x32_bf16 v[200:203], v[20:23], v[188:191], v[200:203]\n"
    "v_mfma_f32_16x16x32_bf16 v[204:207], v[44:47], v[188:191], v[204:207]\n"
    "s_waitcnt lgkmcnt(4)\n"
    "v_mfma_f32_16x16x32_bf16 v[200:203], v[24:27], v[192:195], v[200:203]\n"
    "v_mfma_f32_16x16x32_bf16 v[204:207], v[48:51], v[192:195], v[204:207]\n"
    "s_waitcnt lgkmcnt(2)\n"
    "v_mfma_f32_16x16x32_bf16 v[200:203], v[28:31], v[196:199], v[200:203]\n"
    "s_waitcnt lgkmcnt(0)\n"
    "v_cmp_gt_u32 vcc, 3, v228\n"
    "v_mfma_f32_16x16x32_bf16 v[204:207], v[52:55], v[196:199], v[204:207]\n"
    "s_cbranch_vccnz .Lg31\n"
    "v_mfma_f32_16x16x32_bf16 v[208:211], v[56:59], v[176:179], 0\n"
    "ds_read_b64_tr_b16 v[152:153], v0 offset:6144\n"
    "ds_read_b64_tr_b16 v[154:155], v1 offset:6144\n"
    "v_mfma_f32_16x16x32_bf16 v[212:215], v[80:83], v[176:179], 0\n"
    "ds_read_b64_tr_b16 v[156:157], v0 offset:7168\n"
    "ds_read_b64_tr_b16 v[158:159], v1 offset:7168\n"
    "v_mfma_f32_16x16x32_bf16 v[208:211], v[60:63], v[180:183], v[208:211]\n"
    "ds_read_b64_tr_b16 v[160:161], v0 offset:8192\n"
    "ds_read_b64_tr_b16 v[162:163], v1 offset:8192\n"
    "v_mfma_f32_16x16x32_bf16 v[212:215], v[84:87], v[180:183], v[212:215]\n"
    "ds_read_b64_tr_b16 v[164:165], v0 offset:9216\n"
    "ds_read_b64_tr_b16 v[166:167], v1 offset:9216\n"
    "v_mfma_f32_16x16x32_bf16 v[208:211], v[64:67], v[184:187], v[208:211]\n"
    "v_mul_f32 v200, v200, v253\n"
    "v_mul_f32 v201, v201, v253\n"
    "v_mfma_f32_16x16x32_bf16 v[212:215], v[88:91], v[184:187], v[212:215]\n"
    "ds_read_b64_tr_b16 v[168:169], v0 offset:10240\n"
    "ds_read_b64_tr_b16 v[170:171], v1 offset:10240\n"
    "v_mfma_f32_16x16x32_bf16 v[208:211], v[68:71], v[188:191], v[208:211]\n"
    "v_mul_f32 v202, v202, v253\n"
    "v_mul_f32 v203, v203, v253\n"
    "v_mfma_f32_16x16x32_bf16 v[212:215], v[92:95], v[188:191], v[212:215]\n"
    "ds_read_b64_tr_b16 v[172:173], v0 offset:11264\n"
    "ds_read_b64_tr_b16 v[174:175], v1 offset:11264\n"
    "v_mfma_f32_16x16x32_bf16 v[208:211], v[72:75], v[192:195], v[208:211]\n"
    "v_mul_f32 v204, v204, v253\n"
    "v_mul_f32 v205, v205, v253\n"
    "v_mfma_f32_16x16x32_bf16 v[212:215], v[96:99], v[192:195], v[212:215]\n"
    "ds_read_b32 v252, v238 offset:256\n"
    "v_mul_f32 v206, v206, v253\n"
    "v_mfma_f32_16x16x32_bf16 v[208:211], v[76:79], v[196:199], v[208:211]\n"
    "v_mul_f32 v207, v207, v253\n"
    "v_cvt_pk_bf16_f32 v200, v200, v201\n"
    "v_mfma_f32_16x16x32_bf16 v[212:215], v[100:103], v[196:199], v[212:215]\n"
    "v_cvt_pk_bf16_f32 v201, v202, v203\n"
    "v_cvt_pk_bf16_f32 v202, v204, v205\n"
    "v_cvt_pk_bf16_f32 v203, v206, v207\n"
    "buffer_store_dwordx4 v[200:203], v249, s[80:83], 0 offen offset:128 sc1\n"
    ".Lg31j:\n"
    "s_waitcnt lgkmcnt(1)\n"
    "s_nop 0\n"
    "v_mfma_f32_16x16x32_bf16 v[200:203], v[8:11], v[152:155], 0\n"
    "v_mul_f32 v208, v208, v253\n"
    "v_mul_f32 v209, v209, v253\n"
    "v_mfma_f32_16x16x32_bf16 v[204:207], v[32:35], v[152:155], 0\n"
    "ds_read_b32 v228, v224 offset:176\n"
    "v_mul_f32 v210, v210, v253\n"
    "v_mfma_f32_16x16x32_bf16 v[200:203], v[12:15], v[156:159], v[200:203]\n"
    "v_mul_f32 v211, v211, v253\n"
    "v_mul_f32 v212, v212, v253\n"
    "v_mfma_f32_16x16x32_bf16 v[204:207], v[36:39], v[156:159], v[204:207]\n"
    "v_mul_f32 v213, v213, v253\n"
    "v_mul_f32 v214, v214, v253\n"
    "v_mfma_f32_16x16x32_bf16 v[200:203], v[16:19], v[160:163], v[200:203]\n"
    "v_mul_f32 v215, v215, v253\n"
    "v_cvt_pk_bf16_f32 v208, v208, v209\n"
    "v_mfma_f32_16x16x32_bf16 v[204:207], v[40:43], v[160:163], v[204:207]\n"
    "v_cvt_pk_bf16_f32 v209, v210, v211\n"
    "v_cvt_pk_bf16_f32 v210, v212, v213\n"
    "v_mfma_f32_16x16x32_bf16 v[200:203], v[20:23], v[164:167], v[200:203]\n"
    "v_cvt_pk_bf16_f32 v211, v214, v215\n"
    "v_mfma_f32_16x16x32_bf16 v[204:207], v[44:47], v[164:167], v[204:207]\n"
    "buffer_store_dwordx4 v[208:211], v249, s[80:83], 0 offen offset:320 sc1\n"
    "v_mfma_f32_16x16x32_bf16 v[200:203], v[24:27], v[168:171], v[200:203]\n"
    "v_mfma_f32_16x16x32_bf16 v[204:207], v[48:51], v[168:171], v[204:207]\n"
    "v_add_u32 v249, v251, v249\n"
    "v_mfma_f32_16x16x32_bf16 v[200:203], v[28:31], v[172:175], v[200:203]\n"
    "s_waitcnt lgkmcnt(0)\n"
    "v_cmp_gt_u32 vcc, 3, v228\n"
    "v_mfma_f32_16x16x32_bf16 v[204:207], v[52:55], v[172:175], v[204:207]\n"
    "s_cbranch_vccnz .Lg32\n"
    "v_mfma_f32_16x16x32_bf16 v[208:211], v[56:59], v[152:155], 0\n"
    "ds_read_b64_tr_b16 v[176:177], v0 offset:12288\n"
    "ds_read_b64_tr_b16 v[178:179], v1 offset:12288\n"
    "v_mfma_f32_16x16x32_bf16 v[212:215], v[80:83], v[152:155], 0\n"
    "ds_read_b64_tr_b16 v[180:181], v0 offset:13312\n"
    "ds_read_b64_tr_b16 v[182:183], v1 offset:13312\n"
    "v_mfma_f32_16x16x32_bf16 v[208:211], v[60:63], v[156:159], v[208:211]\n"
    "ds_read_b64_tr_b16 v[184:185], v0 offset:14336\n"
    "ds_read_b64_tr_b16 v[186:187], v1 offset:14336\n"
    "v_mfma_f32_16x16x32_bf16 v[212:215], v[84:87], v[156:159], v[212:215]\n"
    "ds_read_b64_tr_b16 v[188:189], v0 offset:15360\n"
    "ds_read_b64_tr_b16 v[190:191], v1 offset:15360\n"
    "v_mfma_f32_16x16x32_bf16 v[208:211], v[64:67], v[160:163], v[208:211]\n"
    "v_mul_f32 v200, v200, v252\n"
    "v_mul_f32 v201, v201, v252\n"
    "v_mfma_f32_16x16x32_bf16 v[212:215], v[88:91], v[160:163], v[212:215]\n"
    "ds_read_b64_tr_b16 v[192:193], v0 offset:16384\n"
    "ds_read_b64_tr_b16 v[194:195], v1 offset:16384\n"
    "v_mfma_f32_16x16x32_bf16 v[208:211], v[68:71], v[164:167], v[208:211]\n"
    "v_mul_f32 v202, v202, v252\n"
    "v_mul_f32 v203, v203, v252\n"
    "v_mfma_f32_16x16x32_bf16 v[212:215], v[92:95], v[164:167], v[212:215]\n"
    "ds_read_b64_tr_b16 v[196:197], v0 offset:17408\n"
    "ds_read_b64_tr_b16 v[198:199], v1 offset:17408\n"
    "v_mfma_f32_16x16x32_bf16 v[208:211], v[72:75], v[168:171], v[208:211]\n"
    "v_mul_f32 v204, v204, v252\n"
    "v_mul_f32 v205, v205, v252\n"
    "v_mfma_f32_16x16x32_bf16 v[212:215], v[96:99], v[168:171], v[212:215]\n"
    "ds_read_b32 v253, v238 offset:320\n"
    "v_mul_f32 v206, v206, v252\n"
    "v_mfma_f32_16x16x32_bf16 v[208:211], v[76:79], v[172:175], v[208:211]\n"
    "v_mul_f32 v207, v207, v252\n"
    "v_cvt_pk_bf16_f32 v200, v200, v201\n"
    "v_mfma_f32_16x16x32_bf16 v[212:215], v[100:103], v[172:175], v[212:215]\n"
    "v_cvt_pk_bf16_f32 v201, v202, v203\n"
    "v_cvt_pk_bf16_f32 v202, v204, v205\n"
    "v_cvt_pk_bf16_f32 v203, v206, v207\n"
    "buffer_store_dwordx4 v[200:203], v248, s[80:83], 0 offen offset:128 sc1\n"
    ".Lg32j:\n"
    "s_cmp_lg_u32 s58, 0\n"
    "s_cbranch_scc1 .Lg_t12\n"
    "s_waitcnt lgkmcnt(1)\n"
    "v_mfma_f32_16x16x32_bf16 v[200:203], v[8:11], v[176:179], 0\n"
    "v_mul_f32 v208, v208, v252\n"
    "v_mul_f32 v209, v209, v252\n"
    "v_mfma_f32_16x16x32_bf16 v[204:207], v[32:35], v[176:179], 0\n"
    "ds_read_b32 v228, v224 offset:832\n"
    "v_mul_f32 v210, v210, v252\n"
    "v_mfma_f32_16x16x32_bf16 v[200:203], v[12:15], v[180:183], v[200:203]\n"
    "v_mul_f32 v211, v211, v252\n"
    "v_mul_f32 v212, v212, v252\n"
    "v_mfma_f32_16x16x32_bf16 v[204:207], v[36:39], v[180:183], v[204:207]\n"
    "v_mul_f32 v213, v213, v252\n"
    "v_mul_f32 v214, v214, v252\n"
    "v_mfma_f32_16x16x32_bf16 v[200:203], v[16:19], v[184:187], v[200:203]\n"
    "v_mul_f32 v215, v215, v252\n"
    "v_cvt_pk_bf16_f32 v208, v208, v209\n"
    "v_mfma_f32_16x16x32_bf16 v[204:207], v[40:43], v[184:187], v[204:207]\n"
    "v_cvt_pk_bf16_f32 v209, v210, v211\n"
    "v_cvt_pk_bf16_f32 v210, v212, v213\n"
    "v_mfma_f32_16x16x32_bf16 v[200:203], v[20:23], v[188:191], v[200:203]\n"
    "v_cvt_pk_bf16_f32 v211, v214, v215\n"
    "v_mfma_f32_16x16x32_bf16 v[204:207], v[44:47], v[188:191], v[204:207]\n"
    "buffer_store_dwordx4 v[208:211], v248, s[80:83], 0 offen offset:320 sc1\n"
    "v_mfma_f32_16x16x32_bf16 v[200:203], v[24:27], v[192:195], v[200:203]\n"
    "v_mfma_f32_16x16x32_bf16 v[204:207], v[48:51], v[192:195], v[204:207]\n"
    "v_add_u32 v248, v255, v248\n"
    "v_mfma_f32_16x16x32_bf16 v[200:203], v[28:31], v[196:199], v[200:203]\n"
    "s_waitcnt lgkmcnt(0)\n"
    "v_cmp_gt_u32 vcc, 4, v228\n"
    "v_mfma_f32_16x16x32_bf16 v[204:207], v[52:55], v[196:199], v[204:207]\n"
    "s_cbranch_vccnz .Lg33\n"
    "v_mfma_f32_16x16x32_bf16 v[208:211], v[56:59], v[176:179], 0\n"
    "ds_read_b64_tr_b16 v[152:153], v0 offset:0\n"
    "ds_read_b64_tr_b16 v[154:155], v1 offset:0\n"
    "v_mfma_f32_16x16x32_bf16 v[212:215], v[80:83], v[176:179], 0\n"
    "ds_read_b64_tr_b16 v[156:157], v0 offset:1024\n"
    "ds_read_b64_tr_b16 v[158:159], v1 offset:1024\n"
    "v_mfma_f32_16x16x32_bf16 v[208:211], v[60:63], v[180:183], v[208:211]\n"
    "ds_read_b64_tr_b16 v[160:161], v0 offset:2048\n"
    "ds_read_b64_tr_b16 v[162:163], v1 offset:2048\n"
    "v_mfma_f32_16x16x32_bf16 v[212:215], v[84:87], v[180:183], v[212:215]\n"
    "ds_read_b64_tr_b16 v[164:165], v0 offset:3072\n"
    "ds_read_b64_tr_b16 v[166:167], v1 offset:3072\n"
    "v_mfma_f32_16x16x32_bf16 v[208:211], v[64:67], v[184:187], v[208:211]\n"
    "v_mul_f32 v200, v200, v253\n"
    "v_mul_f32 v201, v201, v253\n"
    "v_mfma_f32_16x16x32_bf16 v[212:215], v[88:91], v[184:187], v[212:215]\n"
    "ds_read_b64_tr_b16 v[168:169], v0 offset:4096\n"
    "ds_read_b64_tr_b16 v[170:171], v1 offset:4096\n"
    "v_mfma_f32_16x16x32_bf16 v[208:211], v[68:71], v[188:191], v[208:211]\n"
    "v_mul_f32 v202, v202, v253\n"
    "v_mul_f32 v203, v203, v253\n"
    "v_mfma_f32_16x16x32_bf16 v[212:215], v[92:95], v[188:191], v[212:215]\n"
    "ds_read_b64_tr_b16 v[172:173], v0 offset:5120\n"
    "ds_read_b64_tr_b16 v[174:175], v1 offset:5120\n"
    "v_mfma_f32_16x16x32_bf16 v[208:211], v[72:75], v[192:195], v[208:211]\n"
    "v_mul_f32 v204, v204, v253\n"
    "v_mul_f32 v205, v205, v253\n"
    "v_mfma_f32_16x16x32_bf16 v[212:215], v[96:99], v[192:195], v[212:215]\n"
    "ds_read_b32 v252, v238 offset:192\n"
    "v_mul_f32 v206, v206, v253\n"
    "v_mfma_f32_16x16x32_bf16 v[208:211], v[76:79], v[196:199], v[208:211]\n"
    "v_mul_f32 v207, v207, v253\n"
    "v_cvt_pk_bf16_f32 v200, v200, v201\n"
    "v_mfma_f32_16x16x32_bf16 v[212:215], v[100:103], v[196:199], v[212:215]\n"
    "v_cvt_pk_bf16_f32 v201, v202, v203\n"
    "v_cvt_pk_bf16_f32 v202, v204, v205\n"
    "v_cvt_pk_bf16_f32 v203, v206, v207\n"
    "buffer_store_dwordx4 v[200:203], v249, s[80:83], 0 offen offset:128 sc1\n"
    ".Lg33j:\n"
    "s_waitcnt lgkmcnt(1)\n"
    "s_nop 0\n"
    "v_mfma_f32_16x16x32_bf16 v[200:203], v[8:11], v[152:155], 0\n"
    "v_mul_f32 v208, v208, v253\n"
    "v_mul_f32 v209, v209, v253\n"
    "v_mfma_f32_16x16x32_bf16 v[204:207], v[32:35], v[152:155], 0\n"
    "v_mul_f32 v210, v210, v253\n"
    "v_mul_f32 v211, v211, v253\n"
    "v_mfma_f32_16x16x32_bf16 v[200:203], v[12:15], v[156:159], v[200:203]\n"
    "v_mul_f32 v212, v212, v253\n"
    "v_mul_f32 v213, v213, v253\n"
    "v_mfma_f32_16x16x32_bf16 v[204:207], v[36:39], v[156:159], v[204:207]\n"
    "v_mul_f32 v214, v214, v253\n"
    "v_mul_f32 v215, v215, v253\n"
    "v_mfma_f32_16x16x32_bf16 v[200:203], v[16:19], v[160:163], v[200:203]\n"
    "v_cvt_pk_bf16_f32 v208, v208, v209\n"
    "v_cvt_pk_bf16_f32 v209, v210, v211\n"
    "v_mfma_f32_16x16x32_bf16 v[204:207], v[40:43], v[160:163], v[204:207]\n"
    "v_cvt_pk_bf16_f32 v210, v212, v213\n"
    "v_cvt_pk_bf16_f32 v211, v214, v215\n"
    "v_mfma_f32_16x16x32_bf16 v[200:203], v[20:23], v[164:167], v[200:203]\n"
    "buffer_store_dwordx4 v[208:211], v249, s[80:83], 0 offen offset:320 sc1\n"
    "v_mfma_f32_16x16x32_bf16 v[204:207], v[44:47], v[164:167], v[204:207]\n"
    "v_mfma_f32_16x16x32_bf16 v[200:203], v[24:27], v[168:171], v[200:203]\n"
    "v_mfma_f32_16x16x32_bf16 v[204:207], v[48:51], v[168:171], v[204:207]\n"
    "v_mfma_f32_16x16x32_bf16 v[200:203], v[28:31], v[172:175], v[200:203]\n"
    "v_mfma_f32_16x16x32_bf16 v[204:207], v[52:55], v[172:175], v[204:207]\n"
    "v_mfma_f32_16x16x32_bf16 v[208:211], v[56:59], v[152:155], 0\n"
    "v_mfma_f32_16x16x32_bf16 v[212:215], v[80:83], v[152:155], 0\n"
    "v_mfma_f32_16x16x32_bf16 v[208:211], v[60:63], v[156:159], v[208:211]\n"
    "v_mfma_f32_16x16x32_bf16 v[212:215], v[84:87], v[156:159], v[212:215]\n"
    "v_mfma_f32_16x16x32_bf16 v[208:211], v[64:67], v[160:163], v[208:211]\n"
    "v_mfma_f32_16x16x32_bf16 v[212:215], v[88:91], v[160:163], v[212:215]\n"
    "s_waitcnt lgkmcnt(0)\n"
    "v_mul_f32 v200, v200, v252\n"
    "v_mfma_f32_16x16x32_bf16 v[208:211], v[68:71], v[164:167], v[208:211]\n"
    "v_mul_f32 v201, v201, v252\n"
    "v_mul_f32 v202, v202, v252\n"
    "v_mfma_f32_16x16x32_bf16 v[212:215], v[92:95], v[164:167], v[212:215]\n"
    "v_mul_f32 v203, v203, v252\n"
    "v_mul_f32 v204, v204, v252\n"
    "v_mfma_f32_16x16x32_bf16 v[208:211], v[72:75], v[168:171], v[208:211]\n"
    "v_mul_f32 v205, v205, v252\n"
    "v_mul_f32 v206, v206, v252\n"
    "v_mfma_f32_16x16x32_bf16 v[212:215], v[96:99], v[168:171], v[212:215]\n"
    "v_mul_f32 v207, v207, v252\n"
    "v_cvt_pk_bf16_f32 v200, v200, v201\n"
    "v_mfma_f32_16x16x32_bf16 v[208:211], v[76:79], v[172:175], v[208:211]\n"
    "v_cvt_pk_bf16_f32 v201, v202, v203\n"
    "v_cvt_pk_bf16_f32 v202, v204, v205\n"
    "v_mfma_f32_16x16x32_bf16 v[212:215], v[100:103], v[172:175], v[212:215]\n"
    "v_cvt_pk_bf16_f32 v203, v206, v207\n"
    "buffer_store_dwordx4 v[200:203], v248, s[80:83], 0 offen offset:128 sc1\n"
    "s_nop 2\n"
    "v_mul_f32 v208, v208, v252\n"
    "v_mul_f32 v209, v209, v252\n"
    "v_mul_f32 v210, v210, v252\n"
    "v_mul_f32 v211, v211, v252\n"
    "v_mul_f32 v212, v212, v252\n"
    "v_mul_f32 v213, v213, v252\n"
    "v_mul_f32 v214, v214, v252\n"
    "v_mul_f32 v215, v215, v252\n"
    "v_cvt_pk_bf16_f32 v208, v208, v209\n"
    "v_cvt_pk_bf16_f32 v209, v210, v211\n"
    "v_cvt_pk_bf16_f32 v210, v212, v213\n"
    "v_cvt_pk_bf16_f32 v211, v214, v215\n"
    "buffer_store_dwordx4 v[208:211], v248, s[80:83], 0 offen offset:320 sc1\n"
    ".Lg_end:\n"
    "s_waitcnt lgkmcnt(0)\n"
    "s_endpgm\n"
    ".Lg1:\n"
    "s_sub_u32 s52, s52, 1\n"
    "s_cmp_lt_i32 s52, 0\n"
    "s_cbranch_scc1 .Lg1b\n"
    "ds_read_b32 v228, v224 offset:48\n"
    "s_waitcnt lgkmcnt(0)\n"
    "v_cmp_gt_u32 vcc, 1, v228\n"
    "s_cbranch_vccnz .Lg1\n"
    "s_branch .Lg1b\n"
    ".Lg_poll:\n"
    "s_sub_u32 s51, s51, 1\n"
    "s_cmp_le_i32 s51, 0\n"
    "s_cbranch_scc1 .Lg_retry\n"
    "s_sleep 4\n"
    "s_mov_b32 exec_hi, 15\n"
    "global_load_dwordx2 v[244:245], v236, s[40:41] sc1\n"
    "s_mov_b64 exec, -1\n"
    "s_waitcnt vmcnt(0)\n"
    "v_cmp_ne_u64 vcc, v[240:241], v[244:245]\n"
    "s_cbranch_vccnz .Lg_poll\n"
    "s_branch .Lg_retry\n"
    ".Lg_fail:\n"
    "s_sub_u32 s51, s51, 1\n"
    "s_sleep 2\n"
    "s_cmp_gt_i32 s51, 0\n"
    "s_cbranch_scc1 .Lg_retry\n"
    "s_branch .Lg_ok\n"
    ".Lg2:\n"
    "s_sub_u32 s52, s52, 1\n"
    "s_cmp_lt_i32 s52, 0\n"
    "s_cbranch_scc1 .Lg2b\n"
    "ds_read_b32 v228, v224 offset:0\n"
    "s_waitcnt lgkmcnt(0)\n"
    "v_cmp_gt_u32 vcc, 1, v228\n"
    "s_cbranch_vccnz .Lg2\n"
    "s_branch .Lg2b\n"
    ".Lg3:\n"
    "s_sub_u32 s52, s52, 1\n"
    "s_cmp_lt_i32 s52, 0\n"
    "s_cbranch_scc1 .Lg3b\n"
    "ds_read_b32 v229, v224 offset:0\n"
    "s_waitcnt lgkmcnt(0)\n"
    "v_cmp_gt_u32 vcc, 2, v229\n"
    "s_cbranch_vccnz .Lg3\n"
    "s_branch .Lg3b\n"
    ".Lg4:\n"
    "s_sub_u32 s52, s52, 1\n"
    "s_cmp_lt_i32 s52, 0\n"
    "s_cbranch_scc1 .Lg4b\n"
    "ds_read_b32 v229, v224 offset:0\n"
    "s_waitcnt lgkmcnt(0)\n"
    "v_cmp_gt_u32 vcc, 3, v229\n"
    "s_cbranch_vccnz .Lg4\n"
    "s_branch .Lg4b\n"
    ".Lg5:\n"
    "s_sub_u32 s52, s52, 1\n"
    "s_cmp_lt_i32 s52, 0\n"
    "s_cbranch_scc1 .Lg5b\n"
    "ds_read_b32 v228, v224 offset:192\n"
    "s_waitcnt lgkmcnt(0)\n"
    "v_cmp_gt_u32 vcc, 1, v228\n"
    "s_cbranch_vccnz .Lg5\n"
    "s_branch .Lg5b\n"
    ".Lg6:\n"
    "s_sub_u32 s52, s52, 1\n"
    "s_cmp_lt_i32 s52, 0\n"
    "s_cbranch_scc1 .Lg6b\n"
    "ds_read_b32 v228, v224 offset:208\n"
    "s_waitcnt lgkmcnt(0)\n"
    "v_cmp_gt_u32 vcc, 1, v228\n"
    "s_cbranch_vccnz .Lg6\n"
    "s_branch .Lg6b\n"
    ".Lg7:\n"
    "s_sub_u32 s52, s52, 1\n"
    "s_cmp_lt_i32 s52, 0\n"
    "s_cbranch_scc1 .Lg7b\n"
    "ds_read_b32 v239, v224 offset:64\n"
    "s_waitcnt lgkmcnt(0)\n"
    "v_cmp_gt_u32 vcc, 1, v239\n"
    "s_cbranch_vccnz .Lg7\n"
    "s_branch .Lg7b\n"
    ".Lg8:\n"
    "s_sub_u32 s52, s52, 1\n"
    "s_cmp_lt_i32 s52, 0\n"
    "s_cbranch_scc1 .Lg8b\n"
    "ds_read_b32 v229, v224 offset:0\n"
    "s_waitcnt lgkmcnt(0)\n"
    "v_cmp_gt_u32 vcc, 6, v229\n"
    "s_cbranch_vccnz .Lg8\n"
    "s_branch .Lg8b\n"
    ".Lg9:\n"
    "s_sub_u32 s52, s52, 1\n"
    "s_cmp_lt_i32 s52, 0\n"
    "s_cbranch_scc1 .Lg9b\n"
    "ds_read_b32 v228, v224 offset:224\n"
    "s_waitcnt lgkmcnt(0)\n"
    "v_cmp_gt_u32 vcc, 1, v228\n"
    "s_cbranch_vccnz .Lg9\n"
    "s_branch .Lg9b\n"
    ".Lg10:\n"
    "s_sub_u32 s52, s52, 1\n"
    "s_cmp_lt_i32 s52, 0\n"
    "s_cbranch_scc1 .Lg10b\n"
    "ds_read_b32 v239, v224 offset:80\n"
    "s_waitcnt lgkmcnt(0)\n"
    "v_cmp_gt_u32 vcc, 1, v239\n"
    "s_cbranch_vccnz .Lg10\n"
    "s_branch .Lg10b\n"
    ".Lg11:\n"
    "s_sub_u32 s52, s52, 1\n"
    "s_cmp_lt_i32 s52, 0\n"
    "s_cbranch_scc1 .Lg11b\n"
    "ds_read_b32 v228, v224 offset:240\n"
    "s_waitcnt lgkmcnt(0)\n"
    "v_cmp_gt_u32 vcc, 1, v228\n"
    "s_cbranch_vccnz .Lg11\n"
    "s_branch .Lg11b\n"
    ".Lg12:\n"
    "s_sub_u32 s52, s52, 1\n"
    "s_cmp_lt_i32 s52, 0\n"
    "s_cbranch_scc1 .Lg12b\n"
    "ds_read_b32 v239, v224 offset:96\n"
    "s_waitcnt lgkmcnt(0)\n"
    "v_cmp_gt_u32 vcc, 1, v239\n"
    "s_cbranch_vccnz .Lg12\n"
    "s_branch .Lg12b\n"
    ".Lg13:\n"
    "s_sub_u32 s52, s52, 1\n"
    "s_cmp_lt_i32 s52, 0\n"
    "s_cbranch_scc1 .Lg13b\n"
    "ds_read_b32 v229, v224 offset:0\n"
    "s_waitcnt lgkmcnt(0)\n"
    "v_cmp_gt_u32 vcc, 7, v229\n"
    "s_cbranch_vccnz .Lg13\n"
    "s_branch .Lg13b\n"
    ".Lg14:\n"
    "s_sub_u32 s52, s52, 1\n"
    "s_cmp_lt_i32 s52, 0\n"
    "s_cbranch_scc1 .Lg14b\n"
    "ds_read_b32 v228, v224 offset:192\n"
    "s_waitcnt lgkmcnt(0)\n"
    "v_cmp_gt_u32 vcc, 2, v228\n"
    "s_cbranch_vccnz .Lg14\n"
    "s_branch .Lg14b\n"
    ".Lg15:\n"
    "s_sub_u32 s52, s52, 1\n"
    "s_cmp_lt_i32 s52, 0\n"
    "s_cbranch_scc1 .Lg15b\n"
    "ds_read_b32 v239, v224 offset:112\n"
    "s_waitcnt lgkmcnt(0)\n"
    "v_cmp_gt_u32 vcc, 1, v239\n"
    "s_cbranch_vccnz .Lg15\n"
    "s_branch .Lg15b\n"
    ".Lg16:\n"
    "s_sub_u32 s52, s52, 1\n"
    "s_cmp_lt_i32 s52, 0\n"
    "s_cbranch_scc1 .Lg16b\n"
    "ds_read_b32 v228, v224 offset:208\n"
    "s_waitcnt lgkmcnt(0)\n"
    "v_cmp_gt_u32 vcc, 2, v228\n"
    "s_cbranch_vccnz .Lg16\n"
    "s_branch .Lg16b\n"
    ".Lg17:\n"
    "s_sub_u32 s52, s52, 1\n"
    "s_cmp_lt_i32 s52, 0\n"
    "s_cbranch_scc1 .Lg17b\n"
    "ds_read_b32 v239, v224 offset:64\n"
    "s_waitcnt lgkmcnt(0)\n"
    "v_cmp_gt_u32 vcc, 2, v239\n"
    "s_cbranch_vccnz .Lg17\n"
    "s_branch .Lg17b\n"
    ".Lg18:\n"
    "s_sub_u32 s52, s52, 1\n"
    "s_cmp_lt_i32 s52, 0\n"
    "s_cbranch_scc1 .Lg18b\n"
    "ds_read_b32 v229, v224 offset:0\n"
    "s_waitcnt lgkmcnt(0)\n"
    "v_cmp_gt_u32 vcc, 8, v229\n"
    "s_cbranch_vccnz .Lg18\n"
    "s_branch .Lg18b\n"
    ".Lg19:\n"
    "s_sub_u32 s52, s52, 1\n"
    "s_cmp_lt_i32 s52, 0\n"
    "s_cbranch_scc1 .Lg19b\n"
    "ds_read_b32 v228, v224 offset:224\n"
    "s_waitcnt lgkmcnt(0)\n"
    "v_cmp_gt_u32 vcc, 2, v228\n"
    "s_cbranch_vccnz .Lg19\n"
    "s_branch .Lg19b\n"
    ".Lg20:\n"
    "s_sub_u32 s52, s52, 1\n"
    "s_cmp_lt_i32 s52, 0\n"
    "s_cbranch_scc1 .Lg20b\n"
    "ds_read_b32 v239, v224 offset:80\n"
    "s_waitcnt lgkmcnt(0)\n"
    "v_cmp_gt_u32 vcc, 2, v239\n"
    "s_cbranch_vccnz .Lg20\n"
    "s_branch .Lg20b\n"
    ".Lg21:\n"
    "s_sub_u32 s52, s52, 1\n"
    "s_cmp_lt_i32 s52, 0\n"
    "s_cbranch_scc1 .Lg21b\n"
    "ds_read_b32 v228, v224 offset:240\n"
    "s_waitcnt lgkmcnt(0)\n"
    "v_cmp_gt_u32 vcc, 2, v228\n"
    "s_cbranch_vccnz .Lg21\n"
    "s_branch .Lg21b\n"
    ".Lg22:\n"
    "s_sub_u32 s52, s52, 1\n"
    "s_cmp_lt_i32 s52, 0\n"
    "s_cbranch_scc1 .Lg22b\n"
    "ds_read_b32 v228, v224 offset:192\n"
    "s_waitcnt lgkmcnt(0)\n"
    "v_cmp_gt_u32 vcc, 3, v228\n"
    "s_cbranch_vccnz .Lg22\n"
    "s_branch .Lg22b\n"
    ".Lg_e12a:\n"
    "v_mfma_f32_16x16x32_bf16 v[200:203], v[8:11], v[176:179], 0\n"
    "v_cvt_pk_bf16_f32 v216, v216, v217\n"
    "v_cvt_pk_bf16_f32 v217, v218, v219\n"
    "v_mfma_f32_16x16x32_bf16 v[204:207], v[32:35], v[176:179], 0\n"
    "v_cvt_pk_bf16_f32 v218, v220, v221\n"
    "v_cvt_pk_bf16_f32 v219, v222, v223\n"
    "v_mfma_f32_16x16x32_bf16 v[200:203], v[12:15], v[180:183], v[200:203]\n"
    "ds_write_b128 v7, v[216:219] offset:8192\n"
    "v_mfma_f32_16x16x32_bf16 v[204:207], v[36:39], v[180:183], v[204:207]\n"
    "v_mov_b32 v227, 11\n"
    "v_mfma_f32_16x16x32_bf16 v[200:203], v[16:19], v[184:187], v[200:203]\n"
    "ds_write_b32 v225, v227 offset:16\n"
    "ds_read_b32 v228, v224 offset:192\n"
    "v_mfma_f32_16x16x32_bf16 v[204:207], v[40:43], v[184:187], v[204:207]\n"
    "v_mfma_f32_16x16x32_bf16 v[200:203], v[20:23], v[188:191], v[200:203]\n"
    "v_mfma_f32_16x16x32_bf16 v[204:207], v[44:47], v[188:191], v[204:207]\n"
    "v_mfma_f32_16x16x32_bf16 v[200:203], v[24:27], v[192:195], v[200:203]\n"
    "v_mfma_f32_16x16x32_bf16 v[204:207], v[48:51], v[192:195], v[204:207]\n"
    "v_mfma_f32_16x16x32_bf16 v[200:203], v[28:31], v[196:199], v[200:203]\n"
    "v_mfma_f32_16x16x32_bf16 v[204:207], v[52:55], v[196:199], v[204:207]\n"
    "v_mfma_f32_16x16x32_bf16 v[208:211], v[56:59], v[176:179], 0\n"
    "s_waitcnt lgkmcnt(0)\n"
    "v_cmp_gt_u32 vcc, 3, v228\n"
    "v_mfma_f32_16x16x32_bf16 v[212:215], v[80:83], v[176:179], 0\n"
    "v_mfma_f32_16x16x32_bf16 v[208:211], v[60:63], v[180:183], v[208:211]\n"
    "s_cbranch_vccnz .Lg22\n"
    ".Lg22b:\n"
    "v_mfma_f32_16x16x32_bf16 v[212:215], v[84:87], v[180:183], v[212:215]\n"
    "v_cvt_pk_bf16_f32 v200, v200, v201\n"
    "v_cvt_pk_bf16_f32 v201, v202, v203\n"
    "v_mfma_f32_16x16x32_bf16 v[208:211], v[64:67], v[184:187], v[208:211]\n"
    "v_cvt_pk_bf16_f32 v202, v204, v205\n"
    "v_cvt_pk_bf16_f32 v203, v206, v207\n"
    "v_mfma_f32_16x16x32_bf16 v[212:215], v[88:91], v[184:187], v[212:215]\n"
    "ds_write_b128 v7, v[200:203] offset:12288\n"
    "v_mfma_f32_16x16x32_bf16 v[208:211], v[68:71], v[188:191], v[208:211]\n"
    "v_mfma_f32_16x16x32_bf16 v[212:215], v[92:95], v[188:191], v[212:215]\n"
    "v_mfma_f32_16x16x32_bf16 v[208:211], v[72:75], v[192:195], v[208:211]\n"
    "v_mfma_f32_16x16x32_bf16 v[212:215], v[96:99], v[192:195], v[212:215]\n"
    "v_mfma_f32_16x16x32_bf16 v[208:211], v[76:79], v[196:199], v[208:211]\n"
    "v_mfma_f32_16x16x32_bf16 v[212:215], v[100:103], v[196:199], v[212:215]\n"
    "v_mfma_f32_16x16x32_bf16 v[216:219], v[104:107], v[176:179], 0\n"
    "v_mfma_f32_16x16x32_bf16 v[220:223], v[128:131], v[176:179], 0\n"
    "v_mfma_f32_16x16x32_bf16 v[216:219], v[108:111], v[180:183], v[216:219]\n"
    "v_mfma_f32_16x16x32_bf16 v[220:223], v[132:135], v[180:183], v[220:223]\n"
    "v_mfma_f32_16x16x32_bf16 v[216:219], v[112:115], v[184:187], v[216:219]\n"
    "v_mfma_f32_16x16x32_bf16 v[220:223], v[136:139], v[184:187], v[220:223]\n"
    "v_mfma_f32_16x16x32_bf16 v[216:219], v[116:119], v[188:191], v[216:219]\n"
    "v_cvt_pk_bf16_f32 v208, v208, v209\n"
    "v_cvt_pk_bf16_f32 v209, v210, v211\n"
    "v_mfma_f32_16x16x32_bf16 v[220:223], v[140:143], v[188:191], v[220:223]\n"
    "v_cvt_pk_bf16_f32 v210, v212, v213\n"
    "v_cvt_pk_bf16_f32 v211, v214, v215\n"
    "v_mfma_f32_16x16x32_bf16 v[216:219], v[120:123], v[192:195], v[216:219]\n"
    "ds_write_b128 v7, v[208:211] offset:13312\n"
    "v_mfma_f32_16x16x32_bf16 v[220:223], v[144:147], v[192:195], v[220:223]\n"
    "v_mfma_f32_16x16x32_bf16 v[216:219], v[124:127], v[196:199], v[216:219]\n"
    "v_mfma_f32_16x16x32_bf16 v[220:223], v[148:151], v[196:199], v[220:223]\n"
    "s_nop 6\n"
    "v_cvt_pk_bf16_f32 v216, v216, v217\n"
    "v_cvt_pk_bf16_f32 v217, v218, v219\n"
    "v_cvt_pk_bf16_f32 v218, v220, v221\n"
    "v_cvt_pk_bf16_f32 v219, v222, v223\n"
    "ds_write_b128 v7, v[216:219] offset:14336\n"
    "v_mov_b32 v227, 12\n"
    "ds_write_b32 v225, v227 offset:16\n"
    "s_branch .Lg_end\n"
    ".Lg23:\n"
    "s_sub_u32 s52, s52, 1\n"
    "s_cmp_lt_i32 s52, 0\n"
    "s_cbranch_scc1 .Lg23b\n"
    "ds_read_b32 v228, v224 offset:192\n"
    "s_waitcnt lgkmcnt(0)\n"
    "v_cmp_gt_u32 vcc, 3, v228\n"
    "s_cbranch_vccnz .Lg23\n"
    "s_branch .Lg23b\n"
    ".Lg24:\n"
    "s_sub_u32 s52, s52, 1\n"
    "s_cmp_lt_i32 s52, 0\n"
    "s_cbranch_scc1 .Lg24b\n"
    "ds_read_b32 v239, v224 offset:272\n"
    "s_waitcnt lgkmcnt(0)\n"
    "v_cmp_gt_u32 vcc, 1, v239\n"
    "s_cbranch_vccnz .Lg24\n"
    "s_branch .Lg24b\n"
    ".Lg_e12b:\n"
    "v_mfma_f32_16x16x32_bf16 v[200:203], v[8:11], v[176:179], 0\n"
    "v_cvt_pk_bf16_f32 v216, v216, v217\n"
    "v_cvt_pk_bf16_f32 v217, v218, v219\n"
    "v_mfma_f32_16x16x32_bf16 v[204:207], v[32:35], v[176:179], 0\n"
    "v_cvt_pk_bf16_f32 v218, v220, v221\n"
    "v_cvt_pk_bf16_f32 v219, v222, v223\n"
    "v_mfma_f32_16x16x32_bf16 v[200:203], v[12:15], v[180:183], v[200:203]\n"
    "ds_write_b128 v7, v[216:219] offset:8192\n"
    "v_mfma_f32_16x16x32_bf16 v[204:207], v[36:39], v[180:183], v[204:207]\n"
    "v_mov_b32 v227, 11\n"
    "v_mfma_f32_16x16x32_bf16 v[200:203], v[16:19], v[184:187], v[200:203]\n"
    "ds_write_b32 v225, v227 offset:16\n"
    "ds_read_b32 v228, v224 offset:192\n"
    "v_mfma_f32_16x16x32_bf16 v[204:207], v[40:43], v[184:187], v[204:207]\n"
    "v_mfma_f32_16x16x32_bf16 v[200:203], v[20:23], v[188:191], v[200:203]\n"
    "v_mfma_f32_16x16x32_bf16 v[204:207], v[44:47], v[188:191], v[204:207]\n"
    "v_mfma_f32_16x16x32_bf16 v[200:203], v[24:27], v[192:195], v[200:203]\n"
    "v_mfma_f32_16x16x32_bf16 v[204:207], v[48:51], v[192:195], v[204:207]\n"
    "v_mfma_f32_16x16x32_bf16 v[200:203], v[28:31], v[196:199], v[200:203]\n"
    "v_mfma_f32_16x16x32_bf16 v[204:207], v[52:55], v[196:199], v[204:207]\n"
    "ds_read_b32 v239, v224 offset:272\n"
    "s_waitcnt lgkmcnt(0)\n"
    "v_cmp_gt_u32 vcc, 1, v239\n"
    "s_cbranch_vccnz .Lg24\n"
    ".Lg24b:\n"
    "v_mfma_f32_16x16x32_bf16 v[208:211], v[56:59], v[176:179], 0\n"
    "v_cmp_gt_u32 vcc, 3, v228\n"
    "v_mfma_f32_16x16x32_bf16 v[212:215], v[80:83], v[176:179], 0\n"
    "s_cbranch_vccnz .Lg23\n"
    ".Lg23b:\n"
    "v_cvt_pk_bf16_f32 v200, v200, v201\n"
    "v_mfma_f32_16x16x32_bf16 v[208:211], v[60:63], v[180:183], v[208:211]\n"
    "v_cvt_pk_bf16_f32 v201, v202, v203\n"
    "v_cvt_pk_bf16_f32 v202, v204, v205\n"
    "v_mfma_f32_16x16x32_bf16 v[212:215], v[84:87], v[180:183], v[212:215]\n"
    "v_cvt_pk_bf16_f32 v203, v206, v207\n"
    "v_mfma_f32_16x16x32_bf16 v[208:211], v[64:67], v[184:187], v[208:211]\n"
    "ds_write_b128 v7, v[200:203] offset:12288\n"
    "v_mfma_f32_16x16x32_bf16 v[212:215], v[88:91], v[184:187], v[212:215]\n"
    "s_add_u32 s60, s40, 109568\n"
    "s_addc_u32 s61, s41, 0\n"
    "v_mfma_f32_16x16x32_bf16 v[208:211], v[68:71], v[188:191], v[208:211]\n"
    "s_add_u32 s66, s60, 6144\n"
    "s_addc_u32 s67, s61, 0\n"
    "v_mfma_f32_16x16x32_bf16 v[212:215], v[92:95], v[188:191], v[212:215]\n"
    "global_load_dwordx4 v[8:11], v235, s[60:61] offset:-3072 sc1\n"
    "global_load_dwordx4 v[32:35], v235, s[66:67] offset:-3072 sc1\n"
    "v_mfma_f32_16x16x32_bf16 v[208:211], v[72:75], v[192:195], v[208:211]\n"
    "global_load_dwordx4 v[12:15], v235, s[60:61] offset:-2048 sc1\n"
    "global_load_dwordx4 v[36:39], v235, s[66:67] offset:-2048 sc1\n"
    "v_mfma_f32_16x16x32_bf16 v[212:215], v[96:99], v[192:195], v[212:215]\n"
    "global_load_dwordx4 v[16:19], v235, s[60:61] offset:-1024 sc1\n"
    "global_load_dwordx4 v[40:43], v235, s[66:67] offset:-1024 sc1\n"
    "v_mfma_f32_16x16x32_bf16 v[208:211], v[76:79], v[196:199], v[208:211]\n"
    "global_load_dwordx4 v[20:23], v235, s[60:61] offset:0 sc1\n"
    "global_load_dwordx4 v[44:47], v235, s[66:67] offset:0 sc1\n"
    "v_mfma_f32_16x16x32_bf16 v[212:215], v[100:103], v[196:199], v[212:215]\n"
    "global_load_dwordx4 v[24:27], v235, s[60:61] offset:1024 sc1\n"
    "global_load_dwordx4 v[48:51], v235, s[66:67] offset:1024 sc1\n"
    "v_mfma_f32_16x16x32_bf16 v[216:219], v[104:107], v[176:179], 0\n"
    "global_load_dwordx4 v[28:31], v235, s[60:61] offset:2048 sc1\n"
    "global_load_dwordx4 v[52:55], v235, s[66:67] offset:2048 sc1\n"
    "v_mfma_f32_16x16x32_bf16 v[220:223], v[128:131], v[176:179], 0\n"
    "v_cvt_pk_bf16_f32 v208, v208, v209\n"
    "v_cvt_pk_bf16_f32 v209, v210, v211\n"
    "v_mfma_f32_16x16x32_bf16 v[216:219], v[108:111], v[180:183], v[216:219]\n"
    "v_cvt_pk_bf16_f32 v210, v212, v213\n"
    "v_cvt_pk_bf16_f32 v211, v214, v215\n"
    "v_mfma_f32_16x16x32_bf16 v[220:223], v[132:135], v[180:183], v[220:223]\n"
    "ds_write_b128 v7, v[208:211] offset:13312\n"
    "v_mfma_f32_16x16x32_bf16 v[216:219], v[112:115], v[184:187], v[216:219]\n"
    "s_add_u32 s60, s40, 146432\n"
    "s_addc_u32 s61, s41, 0\n"
    "v_mfma_f32_16x16x32_bf16 v[220:223], v[136:139], v[184:187], v[220:223]\n"
    "s_add_u32 s66, s60, 6144\n"
    "s_addc_u32 s67, s61, 0\n"
    "v_mfma_f32_16x16x32_bf16 v[216:219], v[116:119], v[188:191], v[216:219]\n"
    "global_load_dwordx4 v[56:59], v235, s[60:61] offset:-3072 sc1\n"
    "global_load_dwordx4 v[80:83], v235, s[66:67] offset:-3072 sc1\n"
    "v_mfma_f32_16x16x32_bf16 v[220:223], v[140:143], v[188:191], v[220:223]\n"
    "global_load_dwordx4 v[60:63], v235, s[60:61] offset:-2048 sc1\n"
    "global_load_dwordx4 v[84:87], v235, s[66:67] offset:-2048 sc1\n"
    "v_mfma_f32_16x16x32_bf16 v[216:219], v[120:123], v[192:195], v[216:219]\n"
    "global_load_dwordx4 v[64:67], v235, s[60:61] offset:-1024 sc1\n"
    "global_load_dwordx4 v[88:91], v235, s[66:67] offset:-1024 sc1\n"
    "v_mfma_f32_16x16x32_bf16 v[220:223], v[144:147], v[192:195], v[220:223]\n"
    "global_load_dwordx4 v[68:71], v235, s[60:61] offset:0 sc1\n"
    "global_load_dwordx4 v[92:95], v235, s[66:67] offset:0 sc1\n"
    "v_mfma_f32_16x16x32_bf16 v[216:219], v[124:127], v[196:199], v[216:219]\n"
    "global_load_dwordx4 v[72:75], v235, s[60:61] offset:1024 sc1\n"
    "global_load_dwordx4 v[96:99], v235, s[66:67] offset:1024 sc1\n"
    "v_mfma_f32_16x16x32_bf16 v[220:223], v[148:151], v[196:199], v[220:223]\n"
    "global_load_dwordx4 v[76:79], v235, s[60:61] offset:2048 sc1\n"
    "global_load_dwordx4 v[100:103], v235, s[66:67] offset:2048 sc1\n"
    "s_nop 2\n"
    "v_cvt_pk_bf16_f32 v216, v216, v217\n"
    "v_cvt_pk_bf16_f32 v217, v218, v219\n"
    "s_nop 0\n"
    "v_cvt_pk_bf16_f32 v218, v220, v221\n"
    "v_cvt_pk_bf16_f32 v219, v222, v223\n"
    "ds_write_b128 v7, v[216:219] offset:14336\n"
    "v_mov_b32 v227, 12\n"
    "ds_write_b32 v225, v227 offset:16\n"
    "s_branch .Lg_to\n"
    ".Lg25:\n"
    "s_sub_u32 s52, s52, 1\n"
    "s_cmp_lt_i32 s52, 0\n"
    "s_cbranch_scc1 .Lg25b\n"
    "ds_read_b32 v229, v224 offset:0\n"
    "s_waitcnt lgkmcnt(0)\n"
    "v_cmp_gt_u32 vcc, 9, v229\n"
    "s_cbranch_vccnz .Lg25\n"
    "s_branch .Lg25b\n"
    ".Lg26:\n"
    "s_sub_u32 s52, s52, 1\n"
    "s_cmp_lt_i32 s52, 0\n"
    "s_cbranch_scc1 .Lg26b\n"
    "ds_read_b32 v228, v224 offset:192\n"
    "s_waitcnt lgkmcnt(0)\n"
    "v_cmp_gt_u32 vcc, 3, v228\n"
    "s_cbranch_vccnz .Lg26\n"
    "s_branch .Lg26b\n"
    ".Lg27:\n"
    "s_sub_u32 s52, s52, 1\n"
    "s_cmp_lt_i32 s52, 0\n"
    "s_cbranch_scc1 .Lg27b\n"
    "ds_read_b32 v228, v224 offset:208\n"
    "s_waitcnt lgkmcnt(0)\n"
    "v_cmp_gt_u32 vcc, 3, v228\n"
    "s_cbranch_vccnz .Lg27\n"
    "s_branch .Lg27b\n"
    ".Lg_e13a:\n"
    "s_setprio 2\n"
    "v_mfma_f32_16x16x32_bf16 v[200:203], v[8:11], v[152:155], 0\n"
    "v_mov_b32 v227, 12\n"
    "v_mfma_f32_16x16x32_bf16 v[204:207], v[32:35], v[152:155], 0\n"
    "v_cvt_pk_bf16_f32 v216, v216, v217\n"
    "v_cvt_pk_bf16_f32 v217, v218, v219\n"
    "v_mfma_f32_16x16x32_bf16 v[200:203], v[12:15], v[156:159], v[200:203]\n"
    "v_cvt_pk_bf16_f32 v218, v220, v221\n"
    "v_cvt_pk_bf16_f32 v219, v222, v223\n"
    "v_mfma_f32_16x16x32_bf16 v[204:207], v[36:39], v[156:159], v[204:207]\n"
    "ds_write_b128 v7, v[216:219] offset:14336\n"
    "v_mfma_f32_16x16x32_bf16 v[200:203], v[16:19], v[160:163], v[200:203]\n"
    "ds_write_b32 v225, v227 offset:16\n"
    "ds_read_b32 v228, v224 offset:208\n"
    "v_mfma_f32_16x16x32_bf16 v[204:207], v[40:43], v[160:163], v[204:207]\n"
    "s_waitcnt vmcnt(0)\n"
    "v_mov_b32 v227, 10\n"
    "v_mfma_f32_16x16x32_bf16 v[200:203], v[20:23], v[164:167], v[200:203]\n"
    "ds_write_b32 v226, v227 offset:0\n"
    "v_mfma_f32_16x16x32_bf16 v[204:207], v[44:47], v[164:167], v[204:207]\n"
    "v_mfma_f32_16x16x32_bf16 v[200:203], v[24:27], v[168:171], v[200:203]\n"
    "v_mfma_f32_16x16x32_bf16 v[204:207], v[48:51], v[168:171], v[204:207]\n"
    "v_mfma_f32_16x16x32_bf16 v[200:203], v[28:31], v[172:175], v[200:203]\n"
    "v_mfma_f32_16x16x32_bf16 v[204:207], v[52:55], v[172:175], v[204:207]\n"
    "v_mfma_f32_16x16x32_bf16 v[208:211], v[56:59], v[152:155], 0\n"
    "s_waitcnt lgkmcnt(1)\n"
    "v_cmp_gt_u32 vcc, 3, v228\n"
    "v_mfma_f32_16x16x32_bf16 v[212:215], v[80:83], v[152:155], 0\n"
    "v_mfma_f32_16x16x32_bf16 v[208:211], v[60:63], v[156:159], v[208:211]\n"
    "s_cbranch_vccnz .Lg27\n"
    ".Lg27b:\n"
    "v_mfma_f32_16x16x32_bf16 v[212:215], v[84:87], v[156:159], v[212:215]\n"
    "v_cvt_pk_bf16_f32 v200, v200, v201\n"
    "v_cvt_pk_bf16_f32 v201, v202, v203\n"
    "v_mfma_f32_16x16x32_bf16 v[208:211], v[64:67], v[160:163], v[208:211]\n"
    "v_cvt_pk_bf16_f32 v202, v204, v205\n"
    "v_cvt_pk_bf16_f32 v203, v206, v207\n"
    "v_mfma_f32_16x16x32_bf16 v[212:215], v[88:91], v[160:163], v[212:215]\n"
    "ds_write_b128 v7, v[200:203] offset:0\n"
    "v_mfma_f32_16x16x32_bf16 v[208:211], v[68:71], v[164:167], v[208:211]\n"
    "v_mfma_f32_16x16x32_bf16 v[212:215], v[92:95], v[164:167], v[212:215]\n"
    "v_mfma_f32_16x16x32_bf16 v[208:211], v[72:75], v[168:171], v[208:211]\n"
    "v_mfma_f32_16x16x32_bf16 v[212:215], v[96:99], v[168:171], v[212:215]\n"
    "v_mfma_f32_16x16x32_bf16 v[208:211], v[76:79], v[172:175], v[208:211]\n"
    "v_mfma_f32_16x16x32_bf16 v[212:215], v[100:103], v[172:175], v[212:215]\n"
    "v_mfma_f32_16x16x32_bf16 v[216:219], v[104:107], v[152:155], 0\n"
    "v_mfma_f32_16x16x32_bf16 v[220:223], v[128:131], v[152:155], 0\n"
    "v_mfma_f32_16x16x32_bf16 v[216:219], v[108:111], v[156:159], v[216:219]\n"
    "v_mfma_f32_16x16x32_bf16 v[220:223], v[132:135], v[156:159], v[220:223]\n"
    "v_mfma_f32_16x16x32_bf16 v[216:219], v[112:115], v[160:163], v[216:219]\n"
    "v_mfma_f32_16x16x32_bf16 v[220:223], v[136:139], v[160:163], v[220:223]\n"
    "v_mfma_f32_16x16x32_bf16 v[216:219], v[116:119], v[164:167], v[216:219]\n"
    "v_cvt_pk_bf16_f32 v208, v208, v209\n"
    "v_cvt_pk_bf16_f32 v209, v210, v211\n"
    "v_mfma_f32_16x16x32_bf16 v[220:223], v[140:143], v[164:167], v[220:223]\n"
    "v_cvt_pk_bf16_f32 v210, v212, v213\n"
    "v_cvt_pk_bf16_f32 v211, v214, v215\n"
    "v_mfma_f32_16x16x32_bf16 v[216:219], v[120:123], v[168:171], v[216:219]\n"
    "ds_write_b128 v7, v[208:211] offset:1024\n"
    "v_mfma_f32_16x16x32_bf16 v[220:223], v[144:147], v[168:171], v[220:223]\n"
    "v_mfma_f32_16x16x32_bf16 v[216:219], v[124:127], v[172:175], v[216:219]\n"
    "v_mfma_f32_16x16x32_bf16 v[220:223], v[148:151], v[172:175], v[220:223]\n"
    "s_nop 6\n"
    "v_cvt_pk_bf16_f32 v216, v216, v217\n"
    "v_cvt_pk_bf16_f32 v217, v218, v219\n"
    "v_cvt_pk_bf16_f32 v218, v220, v221\n"
    "v_cvt_pk_bf16_f32 v219, v222, v223\n"
    "ds_write_b128 v7, v[216:219] offset:2048\n"
    "v_mov_b32 v227, 13\n"
    "ds_write_b32 v225, v227 offset:16\n"
    "s_setprio 0\n"
    "s_branch .Lg_att\n"
    ".Lg28:\n"
    "s_sub_u32 s52, s52, 1\n"
    "s_cmp_lt_i32 s52, 0\n"
    "s_cbranch_scc1 .Lg28b\n"
    "ds_read_b32 v228, v224 offset:208\n"
    "s_waitcnt lgkmcnt(0)\n"
    "v_cmp_gt_u32 vcc, 3, v228\n"
    "s_cbranch_vccnz .Lg28\n"
    "s_branch .Lg28b\n"
    ".Lg29:\n"
    "s_sub_u32 s52, s52, 1\n"
    "s_cmp_lt_i32 s52, 0\n"
    "s_cbranch_scc1 .Lg29b\n"
    "ds_read_b32 v239, v224 offset:272\n"
    "s_waitcnt lgkmcnt(0)\n"
    "v_cmp_gt_u32 vcc, 1, v239\n"
    "s_cbranch_vccnz .Lg29\n"
    "s_branch .Lg29b\n"
    ".Lg_att:\n"
    "s_waitcnt vmcnt(0)\n"
    "v_mov_b32 v8, 0xff800000\n"
    "v_add_u32 v3, 124544, v235\n"
    "v_mov_b32 v6, v224\n"
    "v_lshrrev_b32 v9, 6, v235\n"
    "v_and_b32 v9, 12, v9\n"
    "v_lshlrev_b32 v9, 4, v9\n"
    "v_xor_b32 v4, v9, v235\n"
    "v_add_u32 v4, 124544, v4\n"
    "v_cmp_gt_u32 vcc, 64, v235\n"
    "v_add_u32 v7, 704, v224\n"
    "v_mov_b32 v9, 0x100000\n"
    "v_mov_b32 v0, v248\n"
    "v_cndmask_b32 v7, v9, v7, vcc\n"
    "v_mov_b32 v1, v249\n"
    "v_mov_b32 v2, v250\n"
    "v_and_b32 v5, 15, v251\n"
    "v_mul_u32_u24 v5, 0x1111, v5\n"
    "v_bfe_i32 v10, v251, 8, 1\n"
    "v_bfe_i32 v9, v251, 4, 1\n"
    "v_bfi_b32 v232, v9, 0, v8\n"
    "v_bfi_b32 v236, v10, v8, v232\n"
    "v_bfi_b32 v240, v10, v232, v8\n"
    "v_bfe_i32 v9, v251, 5, 1\n"
    "v_bfi_b32 v233, v9, 0, v8\n"
    "v_bfi_b32 v237, v10, v8, v233\n"
    "v_bfi_b32 v241, v10, v233, v8\n"
    "v_bfe_i32 v9, v251, 6, 1\n"
    "v_bfi_b32 v234, v9, 0, v8\n"
    "v_bfi_b32 v238, v10, v8, v234\n"
    "v_bfi_b32 v242, v10, v234, v8\n"
    "v_bfe_i32 v9, v251, 7, 1\n"
    "v_bfi_b32 v235, v9, 0, v8\n"
    "v_bfi_b32 v239, v10, v8, v235\n"
    "v_bfi_b32 v243, v10, v235, v8\n"
    "s_mov_b32 s45, 12\n"
    "s_mov_b32 s54, 10\n"
    "s_movk_i32 s60, 0x420c\n"
    "s_mov_b32 s61, 0\n"
    "s_mov_b32 s46, 0xbdd53b94\n"
    "s_mov_b32 s47, 0x3dd53b94\n"
    "s_mov_b32 s63, 161920\n"
    "s_mov_b32 s66, 0\n"
    "s_mov_b32 s56, -16\n"
    "s_branch .La_join\n"
    ".Lg30:\n"
    "s_sub_u32 s52, s52, 1\n"
    "s_cmp_lt_i32 s52, 0\n"
    "s_cbranch_scc1 .Lg30b\n"
    "ds_read_b32 v228, v224 offset:144\n"
    "s_waitcnt lgkmcnt(0)\n"
    "v_cmp_gt_u32 vcc, 3, v228\n"
    "s_cbranch_vccnz .Lg30\n"
    "s_branch .Lg30b\n"
    ".Lg31:\n"
    "v_mfma_f32_16x16x32_bf16 v[208:211], v[56:59], v[176:179], 0\n"
    "v_mfma_f32_16x16x32_bf16 v[212:215], v[80:83], v[176:179], 0\n"
    "v_mfma_f32_16x16x32_bf16 v[208:211], v[60:63], v[180:183], v[208:211]\n"
    "v_mfma_f32_16x16x32_bf16 v[212:215], v[84:87], v[180:183], v[212:215]\n"
    "v_mul_f32 v200, v200, v253\n"
    "v_mul_f32 v201, v201, v253\n"
    "v_mfma_f32_16x16x32_bf16 v[208:211], v[64:67], v[184:187], v[208:211]\n"
    "v_mul_f32 v202, v202, v253\n"
    "v_mul_f32 v203, v203, v253\n"
    "v_mfma_f32_16x16x32_bf16 v[212:215], v[88:91], v[184:187], v[212:215]\n"
    "ds_read_b32 v228, v224 offset:160\n"
    "v_mul_f32 v204, v204, v253\n"
    "v_mfma_f32_16x16x32_bf16 v[208:211], v[68:71], v[188:191], v[208:211]\n"
    "v_mul_f32 v205, v205, v253\n"
    "v_mul_f32 v206, v206, v253\n"
    "v_mfma_f32_16x16x32_bf16 v[212:215], v[92:95], v[188:191], v[212:215]\n"
    "v_mul_f32 v207, v207, v253\n"
    "v_cvt_pk_bf16_f32 v200, v200, v201\n"
    "v_mfma_f32_16x16x32_bf16 v[208:211], v[72:75], v[192:195], v[208:211]\n"
    "v_cvt_pk_bf16_f32 v201, v202, v203\n"
    "v_cvt_pk_bf16_f32 v202, v204, v205\n"
    "v_mfma_f32_16x16x32_bf16 v[212:215], v[96:99], v[192:195], v[212:215]\n"
    "v_cvt_pk_bf16_f32 v203, v206, v207\n"
    "v_mfma_f32_16x16x32_bf16 v[208:211], v[76:79], v[196:199], v[208:211]\n"
    "buffer_store_dwordx4 v[200:203], v249, s[80:83], 0 offen offset:128 sc1\n"
    "v_mfma_f32_16x16x32_bf16 v[212:215], v[100:103], v[196:199], v[212:215]\n"
    "s_waitcnt lgkmcnt(0)\n"
    "v_cmp_gt_u32 vcc, 3, v228\n"
    "s_cbranch_vccz .Lg31g\n"
    ".Lg31r:\n"
    "s_sub_u32 s52, s52, 1\n"
    "s_cmp_lt_i32 s52, 0\n"
    "s_cbranch_scc1 .Lg31g\n"
    "ds_read_b32 v228, v224 offset:160\n"
    "s_waitcnt lgkmcnt(0)\n"
    "v_cmp_gt_u32 vcc, 3, v228\n"
    "s_cbranch_vccnz .Lg31r\n"
    "s_branch .Lg31g\n"
    ".Lg31g:\n"
    "ds_read_b64_tr_b16 v[152:153], v0 offset:6144\n"
    "ds_read_b64_tr_b16 v[154:155], v1 offset:6144\n"
    "ds_read_b64_tr_b16 v[156:157], v0 offset:7168\n"
    "ds_read_b64_tr_b16 v[158:159], v1 offset:7168\n"
    "ds_read_b64_tr_b16 v[160:161], v0 offset:8192\n"
    "ds_read_b64_tr_b16 v[162:163], v1 offset:8192\n"
    "ds_read_b64_tr_b16 v[164:165], v0 offset:9216\n"
    "ds_read_b64_tr_b16 v[166:167], v1 offset:9216\n"
    "ds_read_b64_tr_b16 v[168:169], v0 offset:10240\n"
    "ds_read_b64_tr_b16 v[170:171], v1 offset:10240\n"
    "ds_read_b64_tr_b16 v[172:173], v0 offset:11264\n"
    "ds_read_b64_tr_b16 v[174:175], v1 offset:11264\n"
    "ds_read_b32 v252, v238 offset:256\n"
    "s_branch .Lg31j\n"
    ".Lg32:\n"
    "v_mfma_f32_16x16x32_bf16 v[208:211], v[56:59], v[152:155], 0\n"
    "v_mfma_f32_16x16x32_bf16 v[212:215], v[80:83], v[152:155], 0\n"
    "v_mfma_f32_16x16x32_bf16 v[208:211], v[60:63], v[156:159], v[208:211]\n"
    "v_mfma_f32_16x16x32_bf16 v[212:215], v[84:87], v[156:159], v[212:215]\n"
    "v_mul_f32 v200, v200, v252\n"
    "v_mul_f32 v201, v201, v252\n"
    "v_mfma_f32_16x16x32_bf16 v[208:211], v[64:67], v[160:163], v[208:211]\n"
    "v_mul_f32 v202, v202, v252\n"
    "v_mul_f32 v203, v203, v252\n"
    "v_mfma_f32_16x16x32_bf16 v[212:215], v[88:91], v[160:163], v[212:215]\n"
    "ds_read_b32 v228, v224 offset:176\n"
    "v_mul_f32 v204, v204, v252\n"
    "v_mfma_f32_16x16x32_bf16 v[208:211], v[68:71], v[164:167], v[208:211]\n"
    "v_mul_f32 v205, v205, v252\n"
    "v_mul_f32 v206, v206, v252\n"
    "v_mfma_f32_16x16x32_bf16 v[212:215], v[92:95], v[164:167], v[212:215]\n"
    "v_mul_f32 v207, v207, v252\n"
    "v_cvt_pk_bf16_f32 v200, v200, v201\n"
    "v_mfma_f32_16x16x32_bf16 v[208:211], v[72:75], v[168:171], v[208:211]\n"
    "v_cvt_pk_bf16_f32 v201, v202, v203\n"
    "v_cvt_pk_bf16_f32 v202, v204, v205\n"
    "v_mfma_f32_16x16x32_bf16 v[212:215], v[96:99], v[168:171], v[212:215]\n"
    "v_cvt_pk_bf16_f32 v203, v206, v207\n"
    "v_mfma_f32_16x16x32_bf16 v[208:211], v[76:79], v[172:175], v[208:211]\n"
    "buffer_store_dwordx4 v[200:203], v248, s[80:83], 0 offen offset:128 sc1\n"
    "v_mfma_f32_16x16x32_bf16 v[212:215], v[100:103], v[172:175], v[212:215]\n"
    "s_waitcnt lgkmcnt(0)\n"
    "v_cmp_gt_u32 vcc, 3, v228\n"
    "s_cbranch_vccz .Lg32g\n"
    ".Lg32r:\n"
    "s_sub_u32 s52, s52, 1\n"
    "s_cmp_lt_i32 s52, 0\n"
    "s_cbranch_scc1 .Lg32g\n"
    "ds_read_b32 v228, v224 offset:176\n"
    "s_waitcnt lgkmcnt(0)\n"
    "v_cmp_gt_u32 vcc, 3, v228\n"
    "s_cbranch_vccnz .Lg32r\n"
    "s_branch .Lg32g\n"
    ".Lg32g:\n"
    "ds_read_b64_tr_b16 v[176:177], v0 offset:12288\n"
    "ds_read_b64_tr_b16 v[178:179], v1 offset:12288\n"
    "ds_read_b64_tr_b16 v[180:181], v0 offset:13312\n"
    "ds_read_b64_tr_b16 v[182:183], v1 offset:13312\n"
    "ds_read_b64_tr_b16 v[184:185], v0 offset:14336\n"
    "ds_read_b64_tr_b16 v[186:187], v1 offset:14336\n"
    "ds_read_b64_tr_b16 v[188:189], v0 offset:15360\n"
    "ds_read_b64_tr_b16 v[190:191], v1 offset:15360\n"
    "ds_read_b64_tr_b16 v[192:193], v0 offset:16384\n"
    "ds_read_b64_tr_b16 v[194:195], v1 offset:16384\n"
    "ds_read_b64_tr_b16 v[196:197], v0 offset:17408\n"
    "ds_read_b64_tr_b16 v[198:199], v1 offset:17408\n"
    "ds_read_b32 v253, v238 offset:320\n"
    "s_branch .Lg32j\n"
    ".Lg_t12:\n"
    "s_waitcnt lgkmcnt(1)\n"
    "v_mfma_f32_16x16x32_bf16 v[200:203], v[8:11], v[176:179], 0\n"
    "v_mul_f32 v208, v208, v252\n"
    "v_mul_f32 v209, v209, v252\n"
    "v_mfma_f32_16x16x32_bf16 v[204:207], v[32:35], v[176:179], 0\n"
    "v_mul_f32 v210, v210, v252\n"
    "v_mul_f32 v211, v211, v252\n"
    "v_mfma_f32_16x16x32_bf16 v[200:203], v[12:15], v[180:183], v[200:203]\n"
    "v_mul_f32 v212, v212, v252\n"
    "v_mul_f32 v213, v213, v252\n"
    "v_mfma_f32_16x16x32_bf16 v[204:207], v[36:39], v[180:183], v[204:207]\n"
    "v_mul_f32 v214, v214, v252\n"
    "v_mul_f32 v215, v215, v252\n"
    "v_mfma_f32_16x16x32_bf16 v[200:203], v[16:19], v[184:187], v[200:203]\n"
    "v_cvt_pk_bf16_f32 v208, v208, v209\n"
    "v_cvt_pk_bf16_f32 v209, v210, v211\n"
    "v_mfma_f32_16x16x32_bf16 v[204:207], v[40:43], v[184:187], v[204:207]\n"
    "v_cvt_pk_bf16_f32 v210, v212, v213\n"
    "v_cvt_pk_bf16_f32 v211, v214, v215\n"
    "v_mfma_f32_16x16x32_bf16 v[200:203], v[20:23], v[188:191], v[200:203]\n"
    "buffer_store_dwordx4 v[208:211], v248, s[80:83], 0 offen offset:320 sc1\n"
    "v_mfma_f32_16x16x32_bf16 v[204:207], v[44:47], v[188:191], v[204:207]\n"
    "v_mfma_f32_16x16x32_bf16 v[200:203], v[24:27], v[192:195], v[200:203]\n"
    "v_add_u32 v248, v255, v248\n"
    "v_mfma_f32_16x16x32_bf16 v[204:207], v[48:51], v[192:195], v[204:207]\n"
    "v_mfma_f32_16x16x32_bf16 v[200:203], v[28:31], v[196:199], v[200:203]\n"
    "v_mfma_f32_16x16x32_bf16 v[204:207], v[52:55], v[196:199], v[204:207]\n"
    "v_mfma_f32_16x16x32_bf16 v[208:211], v[56:59], v[176:179], 0\n"
    "v_mfma_f32_16x16x32_bf16 v[212:215], v[80:83], v[176:179], 0\n"
    "v_mfma_f32_16x16x32_bf16 v[208:211], v[60:63], v[180:183], v[208:211]\n"
    "v_mfma_f32_16x16x32_bf16 v[212:215], v[84:87], v[180:183], v[212:215]\n"
    "v_mfma_f32_16x16x32_bf16 v[208:211], v[64:67], v[184:187], v[208:211]\n"
    "v_mfma_f32_16x16x32_bf16 v[212:215], v[88:91], v[184:187], v[212:215]\n"
    "s_waitcnt lgkmcnt(0)\n"
    "v_mul_f32 v200, v200, v253\n"
    "v_mfma_f32_16x16x32_bf16 v[208:211], v[68:71], v[188:191], v[208:211]\n"
    "v_mul_f32 v201, v201, v253\n"
    "v_mul_f32 v202, v202, v253\n"
    "v_mfma_f32_16x16x32_bf16 v[212:215], v[92:95], v[188:191], v[212:215]\n"
    "v_mul_f32 v203, v203, v253\n"
    "v_mul_f32 v204, v204, v253\n"
    "v_mfma_f32_16x16x32_bf16 v[208:211], v[72:75], v[192:195], v[208:211]\n"
    "v_mul_f32 v205, v205, v253\n"
    "v_mul_f32 v206, v206, v253\n"
    "v_mfma_f32_16x16x32_bf16 v[212:215], v[96:99], v[192:195], v[212:215]\n"
    "v_mul_f32 v207, v207, v253\n"
    "v_cvt_pk_bf16_f32 v200, v200, v201\n"
    "v_mfma_f32_16x16x32_bf16 v[208:211], v[76:79], v[196:199], v[208:211]\n"
    "v_cvt_pk_bf16_f32 v201, v202, v203\n"
    "v_cvt_pk_bf16_f32 v202, v204, v205\n"
    "v_mfma_f32_16x16x32_bf16 v[212:215], v[100:103], v[196:199], v[212:215]\n"
    "v_cvt_pk_bf16_f32 v203, v206, v207\n"
    "buffer_store_dwordx4 v[200:203], v249, s[80:83], 0 offen offset:128 sc1\n"
    "s_nop 2\n"
    "v_mul_f32 v208, v208, v253\n"
    "v_mul_f32 v209, v209, v253\n"
    "v_mul_f32 v210, v210, v253\n"
    "v_mul_f32 v211, v211, v253\n"
    "v_mul_f32 v212, v212, v253\n"
    "v_mul_f32 v213, v213, v253\n"
    "v_mul_f32 v214, v214, v253\n"
    "v_mul_f32 v215, v215, v253\n"
    "v_cvt_pk_bf16_f32 v208, v208, v209\n"
    "v_cvt_pk_bf16_f32 v209, v210, v211\n"
    "v_cvt_pk_bf16_f32 v210, v212, v213\n"
    "v_cvt_pk_bf16_f32 v211, v214, v215\n"
    "buffer_store_dwordx4 v[208:211], v249, s[80:83], 0 offen offset:320 sc1\n"
    "s_branch .Lg_end\n"
    ".Lg33:\n"
    "v_mfma_f32_16x16x32_bf16 v[208:211], v[56:59], v[176:179], 0\n"
    "v_mfma_f32_16x16x32_bf16 v[212:215], v[80:83], v[176:179], 0\n"
    "v_mfma_f32_16x16x32_bf16 v[208:211], v[60:63], v[180:183], v[208:211]\n"
    "v_mfma_f32_16x16x32_bf16 v[212:215], v[84:87], v[180:183], v[212:215]\n"
    "v_mul_f32 v200, v200, v253\n"
    "v_mul_f32 v201, v201, v253\n"
    "v_mfma_f32_16x16x32_bf16 v[208:211], v[64:67], v[184:187], v[208:211]\n"
    "v_mul_f32 v202, v202, v253\n"
    "v_mul_f32 v203, v203, v253\n"
    "v_mfma_f32_16x16x32_bf16 v[212:215], v[88:91], v[184:187], v[212:215]\n"
    "ds_read_b32 v228, v224 offset:832\n"
    "v_mul_f32 v204, v204, v253\n"
    "v_mfma_f32_16x16x32_bf16 v[208:211], v[68:71], v[188:191], v[208:211]\n"
    "v_mul_f32 v205, v205, v253\n"
    "v_mul_f32 v206, v206, v253\n"
    "v_mfma_f32_16x16x32_bf16 v[212:215], v[92:95], v[188:191], v[212:215]\n"
    "v_mul_f32 v207, v207, v253\n"
    "v_cvt_pk_bf16_f32 v200, v200, v201\n"
    "v_mfma_f32_16x16x32_bf16 v[208:211], v[72:75], v[192:195], v[208:211]\n"
    "v_cvt_pk_bf16_f32 v201, v202, v203\n"
    "v_cvt_pk_bf16_f32 v202, v204, v205\n"
    "v_mfma_f32_16x16x32_bf16 v[212:215], v[96:99], v[192:195], v[212:215]\n"
    "v_cvt_pk_bf16_f32 v203, v206, v207\n"
    "v_mfma_f32_16x16x32_bf16 v[208:211], v[76:79], v[196:199], v[208:211]\n"
    "buffer_store_dwordx4 v[200:203], v249, s[80:83], 0 offen offset:128 sc1\n"
    "v_mfma_f32_16x16x32_bf16 v[212:215], v[100:103], v[196:199], v[212:215]\n"
    "s_waitcnt lgkmcnt(0)\n"
    "v_cmp_gt_u32 vcc, 4, v228\n"
    "s_cbranch_vccz .Lg33g\n"
    ".Lg33r:\n"
    "s_sub_u32 s52, s52, 1\n"
    "s_cmp_lt_i32 s52, 0\n"
    "s_cbranch_scc1 .Lg33g\n"
    "ds_read_b32 v228, v224 offset:832\n"
    "s_waitcnt lgkmcnt(0)\n"
    "v_cmp_gt_u32 vcc, 4, v228\n"
    "s_cbranch_vccnz .Lg33r\n"
    "s_branch .Lg33g\n"
    ".Lg33g:\n"
    "ds_read_b64_tr_b16 v[152:153], v0 offset:0\n"
    "ds_read_b64_tr_b16 v[154:155], v1 offset:0\n"
    "ds_read_b64_tr_b16 v[156:157], v0 offset:1024\n"
    "ds_read_b64_tr_b16 v[158:159], v1 offset:1024\n"
    "ds_read_b64_tr_b16 v[160:161], v0 offset:2048\n"
    "ds_read_b64_tr_b16 v[162:163], v1 offset:2048\n"
    "ds_read_b64_tr_b16 v[164:165], v0 offset:3072\n"
    "ds_read_b64_tr_b16 v[166:167], v1 offset:3072\n"
    "ds_read_b64_tr_b16 v[168:169], v0 offset:4096\n"
    "ds_read_b64_tr_b16 v[170:171], v1 offset:4096\n"
    "ds_read_b64_tr_b16 v[172:173], v0 offset:5120\n"
    "ds_read_b64_tr_b16 v[174:175], v1 offset:5120\n"
    "ds_read_b32 v252, v238 offset:192\n"
    "s_branch .Lg33j\n"
    ".Lg_last:\n"
                :: "{v[0:1]}"(vz), "{v238}"(vi), "{v[248:251]}"(to), "{s[80:83]}"(srd), "{v[2:3]}"(va), "{v[6:7]}"(vb), "{v[224:227]}"(fl), "{v[230:233]}"(xo), "{v234}"(xo4), "{v235}"(lf[0]), "{v236}"(lf[1]),
                   "{s[40:41]}"(ws), "{s[42:43]}"(xb), "{s44}"(UNI(axis_at(rm, 14) * IROWB)), "{s45}"(UNI(axis_at(rm, 15) * IROWB)),
                   "{s46}"(UNI(axis_at(rm, 16) * IROWB)), "{s47}"(UNI(axis_at(rm, 17) * IROWB)), "{s48}"(UNI(axis_at(rm, t12 ? 4 : 18) * IROWB)),
                   "{s49}"(UNI(axis_at(rm, t12 ? 5 : 19) * IROWB)), "{s84}"(UNI(3200 * hs)), "{s85}"(UNI(2 * rm.d * IROWB)), "{v255}"(d56), "{s50}"(h), "{s58}"(UNI(t12)), "{s59}"(UNI(t0 + (bid & 4 ? 4400 : 5200)))
                : "memory", "v253", "s51", "s52", "s53", "s54", "s55", "s56", "s57", "s60", "s61", "s79", "s90", "s91", "s98", "s99", "s92", "s93", "s94", "s95", "vcc", "scc");
        } else {
            const unsigned r3 = axis_at(rm, 3 + e) * IROWB + 192 * h;
            const u32x4 vo = {r3 + c0, r3 + c1, dd[0], dd[1]};
            const u32x2 uu = {(unsigned)(pixad(3 + qj) + (3 + e) * RP + 16 * g), LQ + l16 + h * 3072 + 2048};
            u32x2 xp;
#pragma unroll
            for (int k = 0; k < 2; ++k) {
                const int i = 72 * h + min(lane + 64 * k, 71), r = i / 48, c = (i - 48 * r) / 3;
                xp[k] = axis_at(rm, 14 + r) * IROWB + axis_at(cm, 3 + c) * PIXB + 128 * (i - 48 * r - 3 * c);
            }
            u32x2 lf = {l16, (unsigned)(WS_FL + ((bid & (NREP - 1)) * 144 + 72 + nf) * 128)};
            asm volatile("s_nop 7\n"
    "s_mov_b32 s52, 0x40000\n"
    "s_load_dwordx2 s[78:79], s[40:41], 0x0\n"
    "s_mul_i32 s53, s50, 36864\n"
    "s_add_u32 s57, s53, 84992\n"
    "s_add_u32 s64, s40, s57\n"
    "s_addc_u32 s65, s41, 0\n"
    "s_add_u32 s57, s53, 91136\n"
    "s_add_u32 s66, s40, s57\n"
    "s_addc_u32 s67, s41, 0\n"
    "s_add_u32 s57, s53, 97280\n"
    "s_add_u32 s68, s40, s57\n"
    "s_addc_u32 s69, s41, 0\n"
    "s_add_u32 s57, s53, 103424\n"
    "s_add_u32 s70, s40, s57\n"
    "s_addc_u32 s71, s41, 0\n"
    "s_add_u32 s57, s53, 109568\n"
    "s_add_u32 s72, s40, s57\n"
    "s_addc_u32 s73, s41, 0\n"
    "s_add_u32 s57, s53, 115712\n"
    "s_add_u32 s74, s40, s57\n"
    "s_addc_u32 s75, s41, 0\n"
    "s_sub_u32 s88, s72, 73728\n"
    "s_subb_u32 s89, s73, 0\n"
    "s_sub_u32 s90, s74, 73728\n"
    "s_subb_u32 s91, s75, 0\n"
    "v_add_u32 v251, -6144, v236\n"
    "s_cmp_eq_u32 s50, 0\n"
    "s_cbranch_scc1 .Lh_x0\n"
    "v_mov_b32 v233, v234\n"
    ".Lh_x0:\n"
    "s_waitcnt lgkmcnt(0)\n"
    "s_add_u32 s78, s78, 0x7f4a7c15\n"
    "s_add_u32 s79, s79, 0x9e3779b9\n"
    "v_mov_b64 v[240:241], s[78:79]\n"
    "s_mov_b32 s51, 0x4000\n"
    "ds_read_b32 v228, v224 offset:288\n"
    "s_waitcnt lgkmcnt(0)\n"
    "v_cmp_gt_u32 vcc, 1, v228\n"
    "s_cbranch_vccnz .Lh1\n"
    ".Lh1b:\n"
    ".Lh_d2:\n"
    "s_sleep 1\n"
    "s_memtime s[76:77]\n"
    "s_waitcnt lgkmcnt(0)\n"
    "s_sub_u32 s76, s76, s87\n"
    "s_cmp_lt_i32 s76, 0\n"
    "s_cbranch_scc1 .Lh_d2\n"
    "s_mov_b32 s51, 0x4000\n"
    "v_mov_b64 v[244:245], v[240:241]\n"
    "v_mov_b64 v[246:247], 0\n"
    "v_mov_b64 v[242:243], 0\n"
    "s_mov_b32 exec_hi, 15\n"
    "global_load_dwordx4 v[244:247], v236, s[40:41] sc1\n"
    "s_mov_b64 exec, -1\n"
    "global_load_dwordx4 v[8:11], v235, s[64:65] offset:-3072 sc1\n"
    "global_load_dwordx4 v[32:35], v235, s[66:67] offset:-3072 sc1\n"
    "global_load_dwordx4 v[12:15], v235, s[64:65] offset:-2048 sc1\n"
    "global_load_dwordx4 v[36:39], v235, s[66:67] offset:-2048 sc1\n"
    "global_load_dwordx4 v[16:19], v235, s[64:65] offset:-1024 sc1\n"
    "global_load_dwordx4 v[40:43], v235, s[66:67] offset:-1024 sc1\n"
    "global_load_dwordx4 v[20:23], v235, s[64:65] offset:0 sc1\n"
    "global_load_dwordx4 v[44:47], v235, s[66:67] offset:0 sc1\n"
    "global_load_dwordx4 v[24:27], v235, s[64:65] offset:1024 sc1\n"
    "global_load_dwordx4 v[48:51], v235, s[66:67] offset:1024 sc1\n"
    "global_load_dwordx4 v[28:31], v235, s[64:65] offset:2048 sc1\n"
    "global_load_dwordx4 v[52:55], v235, s[66:67] offset:2048 sc1\n"
    "global_load_dwordx4 v[56:59], v235, s[68:69] offset:-3072 sc1\n"
    "global_load_dwordx4 v[80:83], v235, s[70:71] offset:-3072 sc1\n"
    "global_load_dwordx4 v[60:63], v235, s[68:69] offset:-2048 sc1\n"
    "global_load_dwordx4 v[84:87], v235, s[70:71] offset:-2048 sc1\n"
    "global_load_dwordx4 v[64:67], v235, s[68:69] offset:-1024 sc1\n"
    "global_load_dwordx4 v[88:91], v235, s[70:71] offset:-1024 sc1\n"
    "global_load_dwordx4 v[68:71], v235, s[68:69] offset:0 sc1\n"
    "global_load_dwordx4 v[92:95], v235, s[70:71] offset:0 sc1\n"
    "s_waitcnt vmcnt(20)\n"
    "v_cmp_ne_u64 vcc, v[240:241], v[244:245]\n"
    "s_cbranch_vccnz .Lh_pl\n"
    "global_load_dwordx4 v[72:75], v235, s[68:69] offset:1024 sc1\n"
    "global_load_dwordx4 v[96:99], v235, s[70:71] offset:1024 sc1\n"
    "global_load_dwordx4 v[76:79], v235, s[68:69] offset:2048 sc1\n"
    "global_load_dwordx4 v[100:103], v235, s[70:71] offset:2048 sc1\n"
    "global_load_dwordx4 v[104:107], v235, s[72:73] offset:-3072 sc1\n"
    "global_load_dwordx4 v[128:131], v235, s[74:75] offset:-3072 sc1\n"
    "global_load_dwordx4 v[108:111], v235, s[72:73] offset:-2048 sc1\n"
    "global_load_dwordx4 v[132:135], v235, s[74:75] offset:-2048 sc1\n"
    "global_load_dwordx4 v[112:115], v235, s[72:73] offset:-1024 sc1\n"
    "global_load_dwordx4 v[136:139], v235, s[74:75] offset:-1024 sc1\n"
    "global_load_dwordx4 v[116:119], v235, s[72:73] offset:0 sc1\n"
    "global_load_dwordx4 v[140:143], v235, s[74:75] offset:0 sc1\n"
    "global_load_dwordx4 v[120:123], v235, s[72:73] offset:1024 sc1\n"
    "global_load_dwordx4 v[144:147], v235, s[74:75] offset:1024 sc1\n"
    "global_load_dwordx4 v[124:127], v235, s[72:73] offset:2048 sc1\n"
    "global_load_dwordx4 v[148:151], v235, s[74:75] offset:2048 sc1\n"
    ".Lh_rs:\n"
    "s_lshl_b32 s54, s50, 10\n"
    "s_add_u32 s54, s54, 1024\n"
    "s_mul_i32 s56, s50, 7776\n"
    "s_max_u32 s55, s56, 7168\n"
    "s_add_u32 s60, s42, s84\n"
    "s_addc_u32 s61, s43, 0\n"
    "s_add_u32 m0, s54, 106752\n"
    "s_nop 0\n"
    "global_load_lds_dwordx4 v230, s[60:61]\n"
    "s_add_u32 m0, s54, 108800\n"
    "s_nop 0\n"
    "global_load_lds_dwordx4 v231, s[60:61]\n"
    "s_add_u32 m0, s54, 110848\n"
    "s_nop 0\n"
    "global_load_lds_dwordx4 v232, s[60:61]\n"
    "s_add_u32 m0, s55, 106752\n"
    "s_nop 0\n"
    "global_load_lds_dwordx4 v233, s[60:61]\n"
    "s_add_u32 m0, s56, 106752\n"
    "s_nop 0\n"
    "global_load_lds_dwordx4 v234, s[60:61]\n"
    "s_add_u32 s60, s42, s85\n"
    "s_addc_u32 s61, s43, 0\n"
    "s_add_u32 m0, s54, 115648\n"
    "s_nop 0\n"
    "global_load_lds_dwordx4 v230, s[60:61]\n"
    "s_add_u32 m0, s54, 117696\n"
    "s_nop 0\n"
    "global_load_lds_dwordx4 v231, s[60:61]\n"
    "s_add_u32 m0, s54, 119744\n"
    "s_nop 0\n"
    "global_load_lds_dwordx4 v232, s[60:61]\n"
    "s_add_u32 m0, s55, 115648\n"
    "s_nop 0\n"
    "global_load_lds_dwordx4 v233, s[60:61]\n"
    "s_add_u32 m0, s56, 115648\n"
    "s_nop 0\n"
    "global_load_lds_dwordx4 v234, s[60:61]\n"
    "global_load_dword v250, v238, s[42:43]\n"
    "global_load_dword v250, v239, s[42:43]\n"
    "s_waitcnt vmcnt(44)\n"
    "v_add3_u32 v242, v242, v8, v11\n"
    "v_sub_u32 v8, v8, v240\n"
    "v_add3_u32 v242, v242, v32, v35\n"
    "v_sub_u32 v32, v32, v240\n"
    "v_add3_u32 v242, v242, v12, v15\n"
    "v_sub_u32 v12, v12, v240\n"
    "v_add3_u32 v242, v242, v36, v39\n"
    "v_sub_u32 v36, v36, v240\n"
    "s_waitcnt vmcnt(40)\n"
    "v_add3_u32 v242, v242, v16, v19\n"
    "v_sub_u32 v16, v16, v240\n"
    "v_add3_u32 v242, v242, v40, v43\n"
    "v_sub_u32 v40, v40, v240\n"
    "v_add3_u32 v242, v242, v20, v23\n"
    "v_sub_u32 v20, v20, v240\n"
    "v_add3_u32 v242, v242, v44, v47\n"
    "v_sub_u32 v44, v44, v240\n"
    "s_waitcnt vmcnt(36)\n"
    "v_add3_u32 v242, v242, v24, v27\n"
    "v_sub_u32 v24, v24, v240\n"
    "v_add3_u32 v242, v242, v48, v51\n"
    "v_sub_u32 v48, v48, v240\n"
    "v_add3_u32 v242, v242, v28, v31\n"
    "v_sub_u32 v28, v28, v240\n"
    "v_add3_u32 v242, v242, v52, v55\n"
    "v_sub_u32 v52, v52, v240\n"
    "s_waitcnt vmcnt(32)\n"
    "v_add3_u32 v242, v242, v56, v59\n"
    "v_sub_u32 v56, v56, v240\n"
    "v_add3_u32 v242, v242, v80, v83\n"
    "v_sub_u32 v80, v80, v240\n"
    "v_add3_u32 v242, v242, v60, v63\n"
    "v_sub_u32 v60, v60, v240\n"
    "v_add3_u32 v242, v242, v84, v87\n"
    "v_sub_u32 v84, v84, v240\n"
    "s_waitcnt vmcnt(28)\n"
    "v_add3_u32 v242, v242, v64, v67\n"
    "v_sub_u32 v64, v64, v240\n"
    "v_add3_u32 v242, v242, v88, v91\n"
    "v_sub_u32 v88, v88, v240\n"
    "v_add3_u32 v242, v242, v68, v71\n"
    "v_sub_u32 v68, v68, v240\n"
    "v_add3_u32 v242, v242, v92, v95\n"
    "v_sub_u32 v92, v92, v240\n"
    "s_waitcnt vmcnt(24)\n"
    "v_add3_u32 v242, v242, v72, v75\n"
    "v_sub_u32 v72, v72, v240\n"
    "v_add3_u32 v242, v242, v96, v99\n"
    "v_sub_u32 v96, v96, v240\n"
    "v_add3_u32 v242, v242, v76, v79\n"
    "v_sub_u32 v76, v76, v240\n"
    "v_add3_u32 v242, v242, v100, v103\n"
    "v_sub_u32 v100, v100, v240\n"
    "s_waitcnt vmcnt(20)\n"
    "v_add3_u32 v242, v242, v104, v107\n"
    "v_sub_u32 v104, v104, v240\n"
    "v_add3_u32 v242, v242, v128, v131\n"
    "v_sub_u32 v128, v128, v240\n"
    "v_add3_u32 v242, v242, v108, v111\n"
    "v_sub_u32 v108, v108, v240\n"
    "v_add3_u32 v242, v242, v132, v135\n"
    "v_sub_u32 v132, v132, v240\n"
    "s_waitcnt vmcnt(16)\n"
    "v_add3_u32 v242, v242, v112, v115\n"
    "v_sub_u32 v112, v112, v240\n"
    "v_add3_u32 v242, v242, v136, v139\n"
    "v_sub_u32 v136, v136, v240\n"
    "v_add3_u32 v242, v242, v116, v119\n"
    "v_sub_u32 v116, v116, v240\n"
    "v_add3_u32 v242, v242, v140, v143\n"
    "v_sub_u32 v140, v140, v240\n"
    "s_waitcnt vmcnt(12)\n"
    "v_add3_u32 v242, v242, v120, v123\n"
    "v_sub_u32 v120, v120, v240\n"
    "v_add3_u32 v242, v242, v144, v147\n"
    "v_sub_u32 v144, v144, v240\n"
    "v_add3_u32 v242, v242, v124, v127\n"
    "v_sub_u32 v124, v124, v240\n"
    "v_add3_u32 v242, v242, v148, v151\n"
    "v_sub_u32 v148, v148, v240\n"
    ".Lh_ce:\n"
    "v_cmp_ne_u64 s[76:77], v[240:241], v[244:245]\n"
    "v_sub_u32 v242, v242, v246\n"
    "v_mov_b32 v243, v242\n"
    "s_nop 1\n"
    "v_permlane32_swap_b32 v242, v243\n"
    "v_add_u32 v242, v242, v243\n"
    "s_nop 1\n"
    "v_add_u32_dpp v242, v242, v242 row_ror:8 row_mask:0xf bank_mask:0xf\n"
    "s_nop 1\n"
    "v_add_u32_dpp v242, v242, v242 row_ror:4 row_mask:0xf bank_mask:0xf\n"
    "s_nop 1\n"
    "v_add_u32_dpp v242, v242, v242 row_ror:2 row_mask:0xf bank_mask:0xf\n"
    "s_nop 1\n"
    "v_add_u32_dpp v242, v242, v242 row_ror:1 row_mask:0xf bank_mask:0xf\n"
    "v_mov_b32 v243, v242\n"
    "s_nop 1\n"
    "v_permlane16_swap_b32 v242, v243\n"
    "v_add_u32 v242, v242, v243\n"
    "v_cmp_ne_u32 vcc, 0, v242\n"
    "s_nop 1\n"
    "s_or_b64 vcc, vcc, s[76:77]\n"
    "s_cbranch_scc1 .Lh_re\n"
    ".Lh_ok:\n"
    "v_mov_b32 v227, 1\n"
    "ds_write_b32 v225, v227 offset:272\n"
    "ds_read_b32 v228, v224 offset:0\n"
    "s_waitcnt lgkmcnt(0)\n"
    "v_cmp_gt_u32 vcc, 6, v228\n"
    "s_cbranch_vccnz .Lh2\n"
    ".Lh2b:\n"
    "s_waitcnt vmcnt(2)\n"
    "v_mov_b32 v227, 99\n"
    "ds_write_b32 v226, v227 offset:0\n"
    "s_setprio 0\n"
    "ds_read_b32 v228, v224 offset:128\n"
    "s_waitcnt lgkmcnt(0)\n"
    "v_cmp_gt_u32 vcc, 1, v228\n"
    "s_cbranch_vccnz .Lh3\n"
    ".Lh3b:\n"
    "ds_read_b64_tr_b16 v[152:153], v2 offset:18432\n"
    "ds_read_b64_tr_b16 v[154:155], v3 offset:18432\n"
    "ds_read_b64_tr_b16 v[156:157], v2 offset:19456\n"
    "ds_read_b64_tr_b16 v[158:159], v3 offset:19456\n"
    "ds_read_b64_tr_b16 v[160:161], v2 offset:20480\n"
    "ds_read_b64_tr_b16 v[162:163], v3 offset:20480\n"
    "ds_read_b64_tr_b16 v[164:165], v2 offset:21504\n"
    "ds_read_b64_tr_b16 v[166:167], v3 offset:21504\n"
    "ds_read_b64_tr_b16 v[168:169], v2 offset:22528\n"
    "ds_read_b64_tr_b16 v[170:171], v3 offset:22528\n"
    "ds_read_b64_tr_b16 v[172:173], v2 offset:23552\n"
    "ds_read_b64_tr_b16 v[174:175], v3 offset:23552\n"
    "ds_read_b32 v252, v1 offset:0\n"
    "v_mov_b32 v227, 1\n"
    "ds_write_b32 v226, v227 offset:32\n"
    "v_mov_b32 v242, v240\n"
    "v_mov_b32 v243, v241\n"
    "v_and_b32 v229, 0x70, v235\n"
    "v_add_u32 v229, s48, v229\n"
    "global_store_dwordx4 v229, v[240:243], s[40:41] sc1\n"
    "s_waitcnt lgkmcnt(10)\n"
    "v_mfma_f32_16x16x32_bf16 v[200:203], v[8:11], v[152:155], 0\n"
    "v_mfma_f32_16x16x32_bf16 v[204:207], v[32:35], v[152:155], 0\n"
    "v_mfma_f32_16x16x32_bf16 v[200:203], v[12:15], v[156:159], v[200:203]\n"
    "v_mfma_f32_16x16x32_bf16 v[204:207], v[36:39], v[156:159], v[204:207]\n"
    "s_waitcnt lgkmcnt(8)\n"
    "v_mfma_f32_16x16x32_bf16 v[200:203], v[16:19], v[160:163], v[200:203]\n"
    "v_mfma_f32_16x16x32_bf16 v[204:207], v[40:43], v[160:163], v[204:207]\n"
    "s_waitcnt lgkmcnt(6)\n"
    "v_mfma_f32_16x16x32_bf16 v[200:203], v[20:23], v[164:167], v[200:203]\n"
    "v_mfma_f32_16x16x32_bf16 v[204:207], v[44:47], v[164:167], v[204:207]\n"
    "s_waitcnt lgkmcnt(4)\n"
    "v_mfma_f32_16x16x32_bf16 v[200:203], v[24:27], v[168:171], v[200:203]\n"
    "v_mfma_f32_16x16x32_bf16 v[204:207], v[48:51], v[168:171], v[204:207]\n"
    "s_waitcnt lgkmcnt(2)\n"
    "v_mfma_f32_16x16x32_bf16 v[200:203], v[28:31], v[172:175], v[200:203]\n"
    "v_mfma_f32_16x16x32_bf16 v[204:207], v[52:55], v[172:175], v[204:207]\n"
    "v_mfma_f32_16x16x32_bf16 v[208:211], v[56:59], v[152:155], 0\n"
    "v_mfma_f32_16x16x32_bf16 v[212:215], v[80:83], v[152:155], 0\n"
    "ds_read_b32 v228, v224 offset:144\n"
    "v_mfma_f32_16x16x32_bf16 v[208:211], v[60:63], v[156:159], v[208:211]\n"
    "v_mfma_f32_16x16x32_bf16 v[212:215], v[84:87], v[156:159], v[212:215]\n"
    "v_mfma_f32_16x16x32_bf16 v[208:211], v[64:67], v[160:163], v[208:211]\n"
    "s_waitcnt lgkmcnt(2)\n"
    "v_mul_f32 v200, v200, v252\n"
    "v_mfma_f32_16x16x32_bf16 v[212:215], v[88:91], v[160:163], v[212:215]\n"
    "v_mul_f32 v201, v201, v252\n"
    "v_mul_f32 v202, v202, v252\n"
    "v_mfma_f32_16x16x32_bf16 v[208:211], v[68:71], v[164:167], v[208:211]\n"
    "v_mul_f32 v203, v203, v252\n"
    "v_mul_f32 v204, v204, v252\n"
    "v_mfma_f32_16x16x32_bf16 v[212:215], v[92:95], v[164:167], v[212:215]\n"
    "v_mul_f32 v205, v205, v252\n"
    "v_mul_f32 v206, v206, v252\n"
    "v_mfma_f32_16x16x32_bf16 v[208:211], v[72:75], v[168:171], v[208:211]\n"
    "v_mul_f32 v207, v207, v252\n"
    "v_cvt_pk_bf16_f32 v200, v200, v201\n"
    "v_mfma_f32_16x16x32_bf16 v[212:215], v[96:99], v[168:171], v[212:215]\n"
    "v_cvt_pk_bf16_f32 v201, v202, v203\n"
    "v_cvt_pk_bf16_f32 v202, v204, v205\n"
    "v_mfma_f32_16x16x32_bf16 v[208:211], v[76:79], v[172:175], v[208:211]\n"
    "s_waitcnt lgkmcnt(0)\n"
    "v_cmp_gt_u32 vcc, 1, v228\n"
    "v_mfma_f32_16x16x32_bf16 v[212:215], v[100:103], v[172:175], v[212:215]\n"
    "v_cvt_pk_bf16_f32 v203, v206, v207\n"
    "s_cbranch_vccnz .Lh4\n"
    "v_mfma_f32_16x16x32_bf16 v[216:219], v[104:107], v[152:155], 0\n"
    "ds_read_b64_tr_b16 v[176:177], v2 offset:24576\n"
    "ds_read_b64_tr_b16 v[178:179], v3 offset:24576\n"
    "v_mfma_f32_16x16x32_bf16 v[220:223], v[128:131], v[152:155], 0\n"
    "ds_read_b64_tr_b16 v[180:181], v2 offset:25600\n"
    "ds_read_b64_tr_b16 v[182:183], v3 offset:25600\n"
    "v_mfma_f32_16x16x32_bf16 v[216:219], v[108:111], v[156:159], v[216:219]\n"
    "ds_read_b64_tr_b16 v[184:185], v2 offset:26624\n"
    "ds_read_b64_tr_b16 v[186:187], v3 offset:26624\n"
    "v_mfma_f32_16x16x32_bf16 v[220:223], v[132:135], v[156:159], v[220:223]\n"
    "ds_read_b64_tr_b16 v[188:189], v2 offset:27648\n"
    "ds_read_b64_tr_b16 v[190:191], v3 offset:27648\n"
    "v_mfma_f32_16x16x32_bf16 v[216:219], v[112:115], v[160:163], v[216:219]\n"
    "buffer_store_dwordx4 v[200:203], v4, s[80:83], 0 offen offset:0 sc1\n"
    "v_mfma_f32_16x16x32_bf16 v[220:223], v[136:139], v[160:163], v[220:223]\n"
    "v_mul_f32 v208, v208, v252\n"
    "v_mul_f32 v209, v209, v252\n"
    "v_mfma_f32_16x16x32_bf16 v[216:219], v[116:119], v[164:167], v[216:219]\n"
    "ds_read_b64_tr_b16 v[192:193], v2 offset:28672\n"
    "ds_read_b64_tr_b16 v[194:195], v3 offset:28672\n"
    "v_mfma_f32_16x16x32_bf16 v[220:223], v[140:143], v[164:167], v[220:223]\n"
    "v_mul_f32 v210, v210, v252\n"
    "v_mul_f32 v211, v211, v252\n"
    "v_mfma_f32_16x16x32_bf16 v[216:219], v[120:123], v[168:171], v[216:219]\n"
    "ds_read_b64_tr_b16 v[196:197], v2 offset:29696\n"
    "ds_read_b64_tr_b16 v[198:199], v3 offset:29696\n"
    "v_mfma_f32_16x16x32_bf16 v[220:223], v[144:147], v[168:171], v[220:223]\n"
    "v_mul_f32 v212, v212, v252\n"
    "v_mov_b32 v227, 2\n"
    "v_mfma_f32_16x16x32_bf16 v[216:219], v[124:127], v[172:175], v[216:219]\n"
    "ds_read_b32 v253, v1 offset:64\n"
    "v_mul_f32 v213, v213, v252\n"
    "v_mfma_f32_16x16x32_bf16 v[220:223], v[148:151], v[172:175], v[220:223]\n"
    "v_mul_f32 v214, v214, v252\n"
    "v_mul_f32 v215, v215, v252\n"
    "ds_write_b32 v226, v227 offset:32\n"
    "v_cvt_pk_bf16_f32 v208, v208, v209\n"
    "v_cvt_pk_bf16_f32 v209, v210, v211\n"
    "v_cvt_pk_bf16_f32 v210, v212, v213\n"
    "v_cvt_pk_bf16_f32 v211, v214, v215\n"
    "buffer_store_dwordx4 v[208:211], v4, s[80:83], 0 offen offset:64 sc1\n"
    ".Lh4j:\n"
    "s_waitcnt lgkmcnt(2)\n"
    "v_mfma_f32_16x16x32_bf16 v[200:203], v[8:11], v[176:179], 0\n"
    "v_mul_f32 v216, v216, v252\n"
    "v_mul_f32 v217, v217, v252\n"
    "v_mfma_f32_16x16x32_bf16 v[204:207], v[32:35], v[176:179], 0\n"
    "v_mul_f32 v218, v218, v252\n"
    "v_mul_f32 v219, v219, v252\n"
    "v_mfma_f32_16x16x32_bf16 v[200:203], v[12:15], v[180:183], v[200:203]\n"
    "v_mul_f32 v220, v220, v252\n"
    "v_mul_f32 v221, v221, v252\n"
    "v_mfma_f32_16x16x32_bf16 v[204:207], v[36:39], v[180:183], v[204:207]\n"
    "v_mul_f32 v222, v222, v252\n"
    "v_mul_f32 v223, v223, v252\n"
    "v_mfma_f32_16x16x32_bf16 v[200:203], v[16:19], v[184:187], v[200:203]\n"
    "v_cvt_pk_bf16_f32 v216, v216, v217\n"
    "v_cvt_pk_bf16_f32 v217, v218, v219\n"
    "v_mfma_f32_16x16x32_bf16 v[204:207], v[40:43], v[184:187], v[204:207]\n"
    "v_cvt_pk_bf16_f32 v218, v220, v221\n"
    "v_cvt_pk_bf16_f32 v219, v222, v223\n"
    "v_mfma_f32_16x16x32_bf16 v[200:203], v[20:23], v[188:191], v[200:203]\n"
    "buffer_store_dwordx4 v[216:219], v4, s[80:83], 0 offen offset:128 sc1\n"
    "v_mfma_f32_16x16x32_bf16 v[204:207], v[44:47], v[188:191], v[204:207]\n"
    "v_mfma_f32_16x16x32_bf16 v[200:203], v[24:27], v[192:195], v[200:203]\n"
    "v_add_u32 v4, s46, v4\n"
    "v_mfma_f32_16x16x32_bf16 v[204:207], v[48:51], v[192:195], v[204:207]\n"
    "v_mfma_f32_16x16x32_bf16 v[200:203], v[28:31], v[196:199], v[200:203]\n"
    "v_mfma_f32_16x16x32_bf16 v[204:207], v[52:55], v[196:199], v[204:207]\n"
    "v_mfma_f32_16x16x32_bf16 v[208:211], v[56:59], v[176:179], 0\n"
    "v_mfma_f32_16x16x32_bf16 v[212:215], v[80:83], v[176:179], 0\n"
    "ds_read_b32 v228, v224 offset:160\n"
    "v_mfma_f32_16x16x32_bf16 v[208:211], v[60:63], v[180:183], v[208:211]\n"
    "v_mfma_f32_16x16x32_bf16 v[212:215], v[84:87], v[180:183], v[212:215]\n"
    "v_mfma_f32_16x16x32_bf16 v[208:211], v[64:67], v[184:187], v[208:211]\n"
    "s_waitcnt lgkmcnt(2)\n"
    "v_mul_f32 v200, v200, v253\n"
    "v_mfma_f32_16x16x32_bf16 v[212:215], v[88:91], v[184:187], v[212:215]\n"
    "v_mul_f32 v201, v201, v253\n"
    "v_mul_f32 v202, v202, v253\n"
    "v_mfma_f32_16x16x32_bf16 v[208:211], v[68:71], v[188:191], v[208:211]\n"
    "v_mul_f32 v203, v203, v253\n"
    "v_mul_f32 v204, v204, v253\n"
    "v_mfma_f32_16x16x32_bf16 v[212:215], v[92:95], v[188:191], v[212:215]\n"
    "v_mul_f32 v205, v205, v253\n"
    "v_mul_f32 v206, v206, v253\n"
    "v_mfma_f32_16x16x32_bf16 v[208:211], v[72:75], v[192:195], v[208:211]\n"
    "v_mul_f32 v207, v207, v253\n"
    "v_cvt_pk_bf16_f32 v200, v200, v201\n"
    "v_mfma_f32_16x16x32_bf16 v[212:215], v[96:99], v[192:195], v[212:215]\n"
    "v_cvt_pk_bf16_f32 v201, v202, v203\n"
    "v_cvt_pk_bf16_f32 v202, v204, v205\n"
    "v_mfma_f32_16x16x32_bf16 v[208:211], v[76:79], v[196:199], v[208:211]\n"
    "s_waitcnt lgkmcnt(0)\n"
    "v_cmp_gt_u32 vcc, 1, v228\n"
    "v_mfma_f32_16x16x32_bf16 v[212:215], v[100:103], v[196:199], v[212:215]\n"
    "v_cvt_pk_bf16_f32 v203, v206, v207\n"
    "s_cbranch_vccnz .Lh5\n"
    "v_mfma_f32_16x16x32_bf16 v[216:219], v[104:107], v[176:179], 0\n"
    "ds_read_b64_tr_b16 v[152:153], v2 offset:30720\n"
    "ds_read_b64_tr_b16 v[154:155], v3 offset:30720\n"
    "v_mfma_f32_16x16x32_bf16 v[220:223], v[128:131], v[176:179], 0\n"
    "ds_read_b64_tr_b16 v[156:157], v2 offset:31744\n"
    "ds_read_b64_tr_b16 v[158:159], v3 offset:31744\n"
    "v_mfma_f32_16x16x32_bf16 v[216:219], v[108:111], v[180:183], v[216:219]\n"
    "ds_read_b64_tr_b16 v[160:161], v2 offset:32768\n"
    "ds_read_b64_tr_b16 v[162:163], v3 offset:32768\n"
    "v_mfma_f32_16x16x32_bf16 v[220:223], v[132:135], v[180:183], v[220:223]\n"
    "ds_read_b64_tr_b16 v[164:165], v2 offset:33792\n"
    "ds_read_b64_tr_b16 v[166:167], v3 offset:33792\n"
    "v_mfma_f32_16x16x32_bf16 v[216:219], v[112:115], v[184:187], v[216:219]\n"
    "buffer_store_dwordx4 v[200:203], v5, s[80:83], 0 offen offset:0 sc1\n"
    "v_mfma_f32_16x16x32_bf16 v[220:223], v[136:139], v[184:187], v[220:223]\n"
    "v_mul_f32 v208, v208, v253\n"
    "v_mul_f32 v209, v209, v253\n"
    "v_mfma_f32_16x16x32_bf16 v[216:219], v[116:119], v[188:191], v[216:219]\n"
    "ds_read_b64_tr_b16 v[168:169], v2 offset:34816\n"
    "ds_read_b64_tr_b16 v[170:171], v3 offset:34816\n"
    "v_mfma_f32_16x16x32_bf16 v[220:223], v[140:143], v[188:191], v[220:223]\n"
    "v_mul_f32 v210, v210, v253\n"
    "v_mul_f32 v211, v211, v253\n"
    "v_mfma_f32_16x16x32_bf16 v[216:219], v[120:123], v[192:195], v[216:219]\n"
    "ds_read_b64_tr_b16 v[172:173], v2 offset:35840\n"
    "ds_read_b64_tr_b16 v[174:175], v3 offset:35840\n"
    "v_mfma_f32_16x16x32_bf16 v[220:223], v[144:147], v[192:195], v[220:223]\n"
    "v_mul_f32 v212, v212, v253\n"
    "v_mov_b32 v227, 3\n"
    "v_mfma_f32_16x16x32_bf16 v[216:219], v[124:127], v[196:199], v[216:219]\n"
    "ds_read_b32 v252, v1 offset:128\n"
    "v_mul_f32 v213, v213, v253\n"
    "v_mfma_f32_16x16x32_bf16 v[220:223], v[148:151], v[196:199], v[220:223]\n"
    "v_mul_f32 v214, v214, v253\n"
    "v_mul_f32 v215, v215, v253\n"
    "ds_write_b32 v226, v227 offset:32\n"
    "v_cvt_pk_bf16_f32 v208, v208, v209\n"
    "v_cvt_pk_bf16_f32 v209, v210, v211\n"
    "v_cvt_pk_bf16_f32 v210, v212, v213\n"
    "v_cvt_pk_bf16_f32 v211, v214, v215\n"
    "buffer_store_dwordx4 v[208:211], v5, s[80:83], 0 offen offset:64 sc1\n"
    ".Lh5j:\n"
    "s_waitcnt lgkmcnt(2)\n"
    "v_mfma_f32_16x16x32_bf16 v[200:203], v[8:11], v[152:155], 0\n"
    "v_mul_f32 v216, v216, v253\n"
    "v_mul_f32 v217, v217, v253\n"
    "v_mfma_f32_16x16x32_bf16 v[204:207], v[32:35], v[152:155], 0\n"
    "v_mul_f32 v218, v218, v253\n"
    "v_mul_f32 v219, v219, v253\n"
    "v_mfma_f32_16x16x32_bf16 v[200:203], v[12:15], v[156:159], v[200:203]\n"
    "v_mul_f32 v220, v220, v253\n"
    "v_mul_f32 v221, v221, v253\n"
    "v_mfma_f32_16x16x32_bf16 v[204:207], v[36:39], v[156:159], v[204:207]\n"
    "v_mul_f32 v222, v222, v253\n"
    "v_mul_f32 v223, v223, v253\n"
    "v_mfma_f32_16x16x32_bf16 v[200:203], v[16:19], v[160:163], v[200:203]\n"
    "v_cvt_pk_bf16_f32 v216, v216, v217\n"
    "v_cvt_pk_bf16_f32 v217, v218, v219\n"
    "v_mfma_f32_16x16x32_bf16 v[204:207], v[40:43], v[160:163], v[204:207]\n"
    "v_cvt_pk_bf16_f32 v218, v220, v221\n"
    "v_cvt_pk_bf16_f32 v219, v222, v223\n"
    "v_mfma_f32_16x16x32_bf16 v[200:203], v[20:23], v[164:167], v[200:203]\n"
    "buffer_store_dwordx4 v[216:219], v5, s[80:83], 0 offen offset:128 sc1\n"
    "v_mfma_f32_16x16x32_bf16 v[204:207], v[44:47], v[164:167], v[204:207]\n"
    "v_mfma_f32_16x16x32_bf16 v[200:203], v[24:27], v[168:171], v[200:203]\n"
    "v_add_u32 v5, s46, v5\n"
    "v_mfma_f32_16x16x32_bf16 v[204:207], v[48:51], v[168:171], v[204:207]\n"
    "v_mfma_f32_16x16x32_bf16 v[200:203], v[28:31], v[172:175], v[200:203]\n"
    "v_mfma_f32_16x16x32_bf16 v[204:207], v[52:55], v[172:175], v[204:207]\n"
    "v_mfma_f32_16x16x32_bf16 v[208:211], v[56:59], v[152:155], 0\n"
    "v_mfma_f32_16x16x32_bf16 v[212:215], v[80:83], v[152:155], 0\n"
    "ds_read_b32 v228, v224 offset:176\n"
    "v_mfma_f32_16x16x32_bf16 v[208:211], v[60:63], v[156:159], v[208:211]\n"
    "v_mfma_f32_16x16x32_bf16 v[212:215], v[84:87], v[156:159], v[212:215]\n"
    "v_mfma_f32_16x16x32_bf16 v[208:211], v[64:67], v[160:163], v[208:211]\n"
    "s_waitcnt lgkmcnt(2)\n"
    "v_mul_f32 v200, v200, v252\n"
    "v_mfma_f32_16x16x32_bf16 v[212:215], v[88:91], v[160:163], v[212:215]\n"
    "v_mul_f32 v201, v201, v252\n"
    "v_mul_f32 v202, v202, v252\n"
    "v_mfma_f32_16x16x32_bf16 v[208:211], v[68:71], v[164:167], v[208:211]\n"
    "v_mul_f32 v203, v203, v252\n"
    "v_mul_f32 v204, v204, v252\n"
    "v_mfma_f32_16x16x32_bf16 v[212:215], v[92:95], v[164:167], v[212:215]\n"
    "v_mul_f32 v205, v205, v252\n"
    "v_mul_f32 v206, v206, v252\n"
    "v_mfma_f32_16x16x32_bf16 v[208:211], v[72:75], v[168:171], v[208:211]\n"
    "v_mul_f32 v207, v207, v252\n"
    "v_cvt_pk_bf16_f32 v200, v200, v201\n"
    "v_mfma_f32_16x16x32_bf16 v[212:215], v[96:99], v[168:171], v[212:215]\n"
    "v_cvt_pk_bf16_f32 v201, v202, v203\n"
    "v_cvt_pk_bf16_f32 v202, v204, v205\n"
    "v_mfma_f32_16x16x32_bf16 v[208:211], v[76:79], v[172:175], v[208:211]\n"
    "s_waitcnt lgkmcnt(0)\n"
    "v_cmp_gt_u32 vcc, 1, v228\n"
    "v_mfma_f32_16x16x32_bf16 v[212:215], v[100:103], v[172:175], v[212:215]\n"
    "v_cvt_pk_bf16_f32 v203, v206, v207\n"
    "s_cbranch_vccnz .Lh6\n"
    "v_mfma_f32_16x16x32_bf16 v[216:219], v[104:107], v[152:155], 0\n"
    "ds_read_b64_tr_b16 v[176:177], v2 offset:18432\n"
    "ds_read_b64_tr_b16 v[178:179], v3 offset:18432\n"
    "v_mfma_f32_16x16x32_bf16 v[220:223], v[128:131], v[152:155], 0\n"
    "ds_read_b64_tr_b16 v[180:181], v2 offset:19456\n"
    "ds_read_b64_tr_b16 v[182:183], v3 offset:19456\n"
    "v_mfma_f32_16x16x32_bf16 v[216:219], v[108:111], v[156:159], v[216:219]\n"
    "ds_read_b64_tr_b16 v[184:185], v2 offset:20480\n"
    "ds_read_b64_tr_b16 v[186:187], v3 offset:20480\n"
    "v_mfma_f32_16x16x32_bf16 v[220:223], v[132:135], v[156:159], v[220:223]\n"
    "ds_read_b64_tr_b16 v[188:189], v2 offset:21504\n"
    "ds_read_b64_tr_b16 v[190:191], v3 offset:21504\n"
    "v_mfma_f32_16x16x32_bf16 v[216:219], v[112:115], v[160:163], v[216:219]\n"
    "buffer_store_dwordx4 v[200:203], v4, s[80:83], 0 offen offset:0 sc1\n"
    "v_mfma_f32_16x16x32_bf16 v[220:223], v[136:139], v[160:163], v[220:223]\n"
    "v_mul_f32 v208, v208, v252\n"
    "v_mul_f32 v209, v209, v252\n"
    "v_mfma_f32_16x16x32_bf16 v[216:219], v[116:119], v[164:167], v[216:219]\n"
    "ds_read_b64_tr_b16 v[192:193], v2 offset:22528\n"
    "ds_read_b64_tr_b16 v[194:195], v3 offset:22528\n"
    "v_mfma_f32_16x16x32_bf16 v[220:223], v[140:143], v[164:167], v[220:223]\n"
    "v_mul_f32 v210, v210, v252\n"
    "v_mul_f32 v211, v211, v252\n"
    "v_mfma_f32_16x16x32_bf16 v[216:219], v[120:123], v[168:171], v[216:219]\n"
    "ds_read_b64_tr_b16 v[196:197], v2 offset:23552\n"
    "ds_read_b64_tr_b16 v[198:199], v3 offset:23552\n"
    "v_mfma_f32_16x16x32_bf16 v[220:223], v[144:147], v[168:171], v[220:223]\n"
    "v_mul_f32 v212, v212, v252\n"
    "v_mov_b32 v227, 4\n"
    "v_mfma_f32_16x16x32_bf16 v[216:219], v[124:127], v[172:175], v[216:219]\n"
    "ds_read_b32 v253, v1 offset:0\n"
    "v_mul_f32 v213, v213, v252\n"
    "v_mfma_f32_16x16x32_bf16 v[220:223], v[148:151], v[172:175], v[220:223]\n"
    "v_mul_f32 v214, v214, v252\n"
    "v_mul_f32 v215, v215, v252\n"
    "ds_write_b32 v226, v227 offset:32\n"
    "v_cvt_pk_bf16_f32 v208, v208, v209\n"
    "v_cvt_pk_bf16_f32 v209, v210, v211\n"
    "v_cvt_pk_bf16_f32 v210, v212, v213\n"
    "v_cvt_pk_bf16_f32 v211, v214, v215\n"
    "buffer_store_dwordx4 v[208:211], v4, s[80:83], 0 offen offset:64 sc1\n"
    ".Lh6j:\n"
    "s_waitcnt lgkmcnt(2)\n"
    "v_mfma_f32_16x16x32_bf16 v[200:203], v[8:11], v[176:179], 0\n"
    "v_mul_f32 v216, v216, v252\n"
    "v_mul_f32 v217, v217, v252\n"
    "v_mfma_f32_16x16x32_bf16 v[204:207], v[32:35], v[176:179], 0\n"
    "v_mul_f32 v218, v218, v252\n"
    "v_mul_f32 v219, v219, v252\n"
    "v_mfma_f32_16x16x32_bf16 v[200:203], v[12:15], v[180:183], v[200:203]\n"
    "v_mul_f32 v220, v220, v252\n"
    "v_mul_f32 v221, v221, v252\n"
    "v_mfma_f32_16x16x32_bf16 v[204:207], v[36:39], v[180:183], v[204:207]\n"
    "v_mul_f32 v222, v222, v252\n"
    "v_mul_f32 v223, v223, v252\n"
    "v_mfma_f32_16x16x32_bf16 v[200:203], v[16:19], v[184:187], v[200:203]\n"
    "v_cvt_pk_bf16_f32 v216, v216, v217\n"
    "v_cvt_pk_bf16_f32 v217, v218, v219\n"
    "v_mfma_f32_16x16x32_bf16 v[204:207], v[40:43], v[184:187], v[204:207]\n"
    "v_cvt_pk_bf16_f32 v218, v220, v221\n"
    "v_cvt_pk_bf16_f32 v219, v222, v223\n"
    "v_mfma_f32_16x16x32_bf16 v[200:203], v[20:23], v[188:191], v[200:203]\n"
    "buffer_store_dwordx4 v[216:219], v4, s[80:83], 0 offen offset:128 sc1\n"
    "v_mfma_f32_16x16x32_bf16 v[204:207], v[44:47], v[188:191], v[204:207]\n"
    "v_mfma_f32_16x16x32_bf16 v[200:203], v[24:27], v[192:195], v[200:203]\n"
    "v_add_u32 v4, s46, v4\n"
    "v_mfma_f32_16x16x32_bf16 v[204:207], v[48:51], v[192:195], v[204:207]\n"
    "v_mfma_f32_16x16x32_bf16 v[200:203], v[28:31], v[196:199], v[200:203]\n"
    "v_mfma_f32_16x16x32_bf16 v[204:207], v[52:55], v[196:199], v[204:207]\n"
    "v_mfma_f32_16x16x32_bf16 v[208:211], v[56:59], v[176:179], 0\n"
    "v_mfma_f32_16x16x32_bf16 v[212:215], v[80:83], v[176:179], 0\n"
    "ds_read_b32 v228, v224 offset:128\n"
    "v_mfma_f32_16x16x32_bf16 v[208:211], v[60:63], v[180:183], v[208:211]\n"
    "v_mfma_f32_16x16x32_bf16 v[212:215], v[84:87], v[180:183], v[212:215]\n"
    "v_mfma_f32_16x16x32_bf16 v[208:211], v[64:67], v[184:187], v[208:211]\n"
    "s_waitcnt lgkmcnt(2)\n"
    "v_mul_f32 v200, v200, v253\n"
    "v_mfma_f32_16x16x32_bf16 v[212:215], v[88:91], v[184:187], v[212:215]\n"
    "v_mul_f32 v201, v201, v253\n"
    "v_mul_f32 v202, v202, v253\n"
    "v_mfma_f32_16x16x32_bf16 v[208:211], v[68:71], v[188:191], v[208:211]\n"
    "v_mul_f32 v203, v203, v253\n"
    "v_mul_f32 v204, v204, v253\n"
    "v_mfma_f32_16x16x32_bf16 v[212:215], v[92:95], v[188:191], v[212:215]\n"
    "v_mul_f32 v205, v205, v253\n"
    "v_mul_f32 v206, v206, v253\n"
    "v_mfma_f32_16x16x32_bf16 v[208:211], v[72:75], v[192:195], v[208:211]\n"
    "v_mul_f32 v207, v207, v253\n"
    "v_cvt_pk_bf16_f32 v200, v200, v201\n"
    "v_mfma_f32_16x16x32_bf16 v[212:215], v[96:99], v[192:195], v[212:215]\n"
    "v_cvt_pk_bf16_f32 v201, v202, v203\n"
    "v_cvt_pk_bf16_f32 v202, v204, v205\n"
    "v_mfma_f32_16x16x32_bf16 v[208:211], v[76:79], v[196:199], v[208:211]\n"
    "s_waitcnt lgkmcnt(0)\n"
    "v_cmp_gt_u32 vcc, 2, v228\n"
    "v_mfma_f32_16x16x32_bf16 v[212:215], v[100:103], v[196:199], v[212:215]\n"
    "v_cvt_pk_bf16_f32 v203, v206, v207\n"
    "s_cbranch_vccnz .Lh7\n"
    "v_mfma_f32_16x16x32_bf16 v[216:219], v[104:107], v[176:179], 0\n"
    "ds_read_b64_tr_b16 v[152:153], v2 offset:24576\n"
    "ds_read_b64_tr_b16 v[154:155], v3 offset:24576\n"
    "v_mfma_f32_16x16x32_bf16 v[220:223], v[128:131], v[176:179], 0\n"
    "ds_read_b64_tr_b16 v[156:157], v2 offset:25600\n"
    "ds_read_b64_tr_b16 v[158:159], v3 offset:25600\n"
    "v_mfma_f32_16x16x32_bf16 v[216:219], v[108:111], v[180:183], v[216:219]\n"
    "ds_read_b64_tr_b16 v[160:161], v2 offset:26624\n"
    "ds_read_b64_tr_b16 v[162:163], v3 offset:26624\n"
    "v_mfma_f32_16x16x32_bf16 v[220:223], v[132:135], v[180:183], v[220:223]\n"
    "ds_read_b64_tr_b16 v[164:165], v2 offset:27648\n"
    "ds_read_b64_tr_b16 v[166:167], v3 offset:27648\n"
    "v_mfma_f32_16x16x32_bf16 v[216:219], v[112:115], v[184:187], v[216:219]\n"
    "buffer_store_dwordx4 v[200:203], v5, s[80:83], 0 offen offset:0 sc1\n"
    "v_mfma_f32_16x16x32_bf16 v[220:223], v[136:139], v[184:187], v[220:223]\n"
    "v_mul_f32 v208, v208, v253\n"
    "v_mul_f32 v209, v209, v253\n"
    "v_mfma_f32_16x16x32_bf16 v[216:219], v[116:119], v[188:191], v[216:219]\n"
    "ds_read_b64_tr_b16 v[168:169], v2 offset:28672\n"
    "ds_read_b64_tr_b16 v[170:171], v3 offset:28672\n"
    "v_mfma_f32_16x16x32_bf16 v[220:223], v[140:143], v[188:191], v[220:223]\n"
    "v_mul_f32 v210, v210, v253\n"
    "v_mul_f32 v211, v211, v253\n"
    "v_mfma_f32_16x16x32_bf16 v[216:219], v[120:123], v[192:195], v[216:219]\n"
    "ds_read_b64_tr_b16 v[172:173], v2 offset:29696\n"
    "ds_read_b64_tr_b16 v[174:175], v3 offset:29696\n"
    "v_mfma_f32_16x16x32_bf16 v[220:223], v[144:147], v[192:195], v[220:223]\n"
    "v_mul_f32 v212, v212, v253\n"
    "v_mov_b32 v227, 5\n"
    "v_mfma_f32_16x16x32_bf16 v[216:219], v[124:127], v[196:199], v[216:219]\n"
    "ds_read_b32 v252, v1 offset:64\n"
    "v_mul_f32 v213, v213, v253\n"
    "v_mfma_f32_16x16x32_bf16 v[220:223], v[148:151], v[196:199], v[220:223]\n"
    "v_mul_f32 v214, v214, v253\n"
    "v_mul_f32 v215, v215, v253\n"
    "ds_write_b32 v226, v227 offset:32\n"
    "v_cvt_pk_bf16_f32 v208, v208, v209\n"
    "v_cvt_pk_bf16_f32 v209, v210, v211\n"
    "v_cvt_pk_bf16_f32 v210, v212, v213\n"
    "v_cvt_pk_bf16_f32 v211, v214, v215\n"
    "buffer_store_dwordx4 v[208:211], v5, s[80:83], 0 offen offset:64 sc1\n"
    ".Lh7j:\n"
    "s_waitcnt lgkmcnt(2)\n"
    "v_mfma_f32_16x16x32_bf16 v[200:203], v[8:11], v[152:155], 0\n"
    "v_mul_f32 v216, v216, v253\n"
    "v_mul_f32 v217, v217, v253\n"
    "v_mfma_f32_16x16x32_bf16 v[204:207], v[32:35], v[152:155], 0\n"
    "v_mul_f32 v218, v218, v253\n"
    "v_mul_f32 v219, v219, v253\n"
    "v_mfma_f32_16x16x32_bf16 v[200:203], v[12:15], v[156:159], v[200:203]\n"
    "v_mul_f32 v220, v220, v253\n"
    "v_mul_f32 v221, v221, v253\n"
    "v_mfma_f32_16x16x32_bf16 v[204:207], v[36:39], v[156:159], v[204:207]\n"
    "v_mul_f32 v222, v222, v253\n"
    "v_mul_f32 v223, v223, v253\n"
    "v_mfma_f32_16x16x32_bf16 v[200:203], v[16:19], v[160:163], v[200:203]\n"
    "v_cvt_pk_bf16_f32 v216, v216, v217\n"
    "v_cvt_pk_bf16_f32 v217, v218, v219\n"
    "v_mfma_f32_16x16x32_bf16 v[204:207], v[40:43], v[160:163], v[204:207]\n"
    "v_cvt_pk_bf16_f32 v218, v220, v221\n"
    "v_cvt_pk_bf16_f32 v219, v222, v223\n"
    "v_mfma_f32_16x16x32_bf16 v[200:203], v[20:23], v[164:167], v[200:203]\n"
    "buffer_store_dwordx4 v[216:219], v5, s[80:83], 0 offen offset:128 sc1\n"
    "v_mfma_f32_16x16x32_bf16 v[204:207], v[44:47], v[164:167], v[204:207]\n"
    "v_mfma_f32_16x16x32_bf16 v[200:203], v[24:27], v[168:171], v[200:203]\n"
    "v_add_u32 v5, s46, v5\n"
    "v_mfma_f32_16x16x32_bf16 v[204:207], v[48:51], v[168:171], v[204:207]\n"
    "v_mfma_f32_16x16x32_bf16 v[200:203], v[28:31], v[172:175], v[200:203]\n"
    "v_mfma_f32_16x16x32_bf16 v[204:207], v[52:55], v[172:175], v[204:207]\n"
    "v_mfma_f32_16x16x32_bf16 v[208:211], v[56:59], v[152:155], 0\n"
    "v_mfma_f32_16x16x32_bf16 v[212:215], v[80:83], v[152:155], 0\n"
    "ds_read_b32 v228, v224 offset:144\n"
    "v_mfma_f32_16x16x32_bf16 v[208:211], v[60:63], v[156:159], v[208:211]\n"
    "v_mfma_f32_16x16x32_bf16 v[212:215], v[84:87], v[156:159], v[212:215]\n"
    "v_mfma_f32_16x16x32_bf16 v[208:211], v[64:67], v[160:163], v[208:211]\n"
    "s_waitcnt lgkmcnt(2)\n"
    "v_mul_f32 v200, v200, v252\n"
    "v_mfma_f32_16x16x32_bf16 v[212:215], v[88:91], v[160:163], v[212:215]\n"
    "v_mul_f32 v201, v201, v252\n"
    "v_mul_f32 v202, v202, v252\n"
    "v_mfma_f32_16x16x32_bf16 v[208:211], v[68:71], v[164:167], v[208:211]\n"
    "v_mul_f32 v203, v203, v252\n"
    "v_mul_f32 v204, v204, v252\n"
    "v_mfma_f32_16x16x32_bf16 v[212:215], v[92:95], v[164:167], v[212:215]\n"
    "v_mul_f32 v205, v205, v252\n"
    "v_mul_f32 v206, v206, v252\n"
    "v_mfma_f32_16x16x32_bf16 v[208:211], v[72:75], v[168:171], v[208:211]\n"
    "v_mul_f32 v207, v207, v252\n"
    "v_cvt_pk_bf16_f32 v200, v200, v201\n"
    "v_mfma_f32_16x16x32_bf16 v[212:215], v[96:99], v[168:171], v[212:215]\n"
    "v_cvt_pk_bf16_f32 v201, v202, v203\n"
    "v_cvt_pk_bf16_f32 v202, v204, v205\n"
    "v_mfma_f32_16x16x32_bf16 v[208:211], v[76:79], v[172:175], v[208:211]\n"
    "s_waitcnt lgkmcnt(0)\n"
    "v_cmp_gt_u32 vcc, 2, v228\n"
    "v_mfma_f32_16x16x32_bf16 v[212:215], v[100:103], v[172:175], v[212:215]\n"
    "v_cvt_pk_bf16_f32 v203, v206, v207\n"
    "s_cbranch_vccnz .Lh8\n"
    "v_mfma_f32_16x16x32_bf16 v[216:219], v[104:107], v[152:155], 0\n"
    "ds_read_b64_tr_b16 v[176:177], v2 offset:30720\n"
    "ds_read_b64_tr_b16 v[178:179], v3 offset:30720\n"
    "v_mfma_f32_16x16x32_bf16 v[220:223], v[128:131], v[152:155], 0\n"
    "ds_read_b64_tr_b16 v[180:181], v2 offset:31744\n"
    "ds_read_b64_tr_b16 v[182:183], v3 offset:31744\n"
    "v_mfma_f32_16x16x32_bf16 v[216:219], v[108:111], v[156:159], v[216:219]\n"
    "ds_read_b64_tr_b16 v[184:185], v2 offset:32768\n"
    "ds_read_b64_tr_b16 v[186:187], v3 offset:32768\n"
    "v_mfma_f32_16x16x32_bf16 v[220:223], v[132:135], v[156:159], v[220:223]\n"
    "ds_read_b64_tr_b16 v[188:189], v2 offset:33792\n"
    "ds_read_b64_tr_b16 v[190:191], v3 offset:33792\n"
    "v_mfma_f32_16x16x32_bf16 v[216:219], v[112:115], v[160:163], v[216:219]\n"
    "buffer_store_dwordx4 v[200:203], v4, s[80:83], 0 offen offset:0 sc1\n"
    "v_mfma_f32_16x16x32_bf16 v[220:223], v[136:139], v[160:163], v[220:223]\n"
    "v_mul_f32 v208, v208, v252\n"
    "v_mul_f32 v209, v209, v252\n"
    "v_mfma_f32_16x16x32_bf16 v[216:219], v[116:119], v[164:167], v[216:219]\n"
    "ds_read_b64_tr_b16 v[192:193], v2 offset:34816\n"
    "ds_read_b64_tr_b16 v[194:195], v3 offset:34816\n"
    "v_mfma_f32_16x16x32_bf16 v[220:223], v[140:143], v[164:167], v[220:223]\n"
    "v_mul_f32 v210, v210, v252\n"
    "v_mul_f32 v211, v211, v252\n"
    "v_mfma_f32_16x16x32_bf16 v[216:219], v[120:123], v[168:171], v[216:219]\n"
    "ds_read_b64_tr_b16 v[196:197], v2 offset:35840\n"
    "ds_read_b64_tr_b16 v[198:199], v3 offset:35840\n"
    "v_mfma_f32_16x16x32_bf16 v[220:223], v[144:147], v[168:171], v[220:223]\n"
    "v_mul_f32 v212, v212, v252\n"
    "v_mov_b32 v227, 6\n"
    "v_mfma_f32_16x16x32_bf16 v[216:219], v[124:127], v[172:175], v[216:219]\n"
    "ds_read_b32 v253, v1 offset:128\n"
    "v_mul_f32 v213, v213, v252\n"
    "v_mfma_f32_16x16x32_bf16 v[220:223], v[148:151], v[172:175], v[220:223]\n"
    "v_mul_f32 v214, v214, v252\n"
    "v_mul_f32 v215, v215, v252\n"
    "ds_write_b32 v226, v227 offset:32\n"
    "v_cvt_pk_bf16_f32 v208, v208, v209\n"
    "v_cvt_pk_bf16_f32 v209, v210, v211\n"
    "v_cvt_pk_bf16_f32 v210, v212, v213\n"
    "v_cvt_pk_bf16_f32 v211, v214, v215\n"
    "buffer_store_dwordx4 v[208:211], v4, s[80:83], 0 offen offset:64 sc1\n"
    ".Lh8j:\n"
    "s_waitcnt lgkmcnt(2)\n"
    "v_mfma_f32_16x16x32_bf16 v[200:203], v[8:11], v[176:179], 0\n"
    "v_mul_f32 v216, v216, v252\n"
    "v_mul_f32 v217, v217, v252\n"
    "v_mfma_f32_16x16x32_bf16 v[204:207], v[32:35], v[176:179], 0\n"
    "v_mul_f32 v218, v218, v252\n"
    "v_mul_f32 v219, v219, v252\n"
    "v_mfma_f32_16x16x32_bf16 v[200:203], v[12:15], v[180:183], v[200:203]\n"
    "v_mul_f32 v220, v220, v252\n"
    "v_mul_f32 v221, v221, v252\n"
    "v_mfma_f32_16x16x32_bf16 v[204:207], v[36:39], v[180:183], v[204:207]\n"
    "v_mul_f32 v222, v222, v252\n"
    "v_mul_f32 v223, v223, v252\n"
    "v_mfma_f32_16x16x32_bf16 v[200:203], v[16:19], v[184:187], v[200:203]\n"
    "v_cvt_pk_bf16_f32 v216, v216, v217\n"
    "v_cvt_pk_bf16_f32 v217, v218, v219\n"
    "v_mfma_f32_16x16x32_bf16 v[204:207], v[40:43], v[184:187], v[204:207]\n"
    "v_cvt_pk_bf16_f32 v218, v220, v221\n"
    "v_cvt_pk_bf16_f32 v219, v222, v223\n"
    "v_mfma_f32_16x16x32_bf16 v[200:203], v[20:23], v[188:191], v[200:203]\n"
    "buffer_store_dwordx4 v[216:219], v4, s[80:83], 0 offen offset:128 sc1\n"
    "v_mfma_f32_16x16x32_bf16 v[204:207], v[44:47], v[188:191], v[204:207]\n"
    "v_mfma_f32_16x16x32_bf16 v[200:203], v[24:27], v[192:195], v[200:203]\n"
    "v_add_u32 v4, s46, v4\n"
    "v_mfma_f32_16x16x32_bf16 v[204:207], v[48:51], v[192:195], v[204:207]\n"
    "v_mfma_f32_16x16x32_bf16 v[200:203], v[28:31], v[196:199], v[200:203]\n"
    "v_mfma_f32_16x16x32_bf16 v[204:207], v[52:55], v[196:199], v[204:207]\n"
    "v_mfma_f32_16x16x32_bf16 v[208:211], v[56:59], v[176:179], 0\n"
    "v_mfma_f32_16x16x32_bf16 v[212:215], v[80:83], v[176:179], 0\n"
    "ds_read_b32 v228, v224 offset:160\n"
    "v_mfma_f32_16x16x32_bf16 v[208:211], v[60:63], v[180:183], v[208:211]\n"
    "v_mfma_f32_16x16x32_bf16 v[212:215], v[84:87], v[180:183], v[212:215]\n"
    "v_mfma_f32_16x16x32_bf16 v[208:211], v[64:67], v[184:187], v[208:211]\n"
    "s_waitcnt lgkmcnt(2)\n"
    "v_mul_f32 v200, v200, v253\n"
    "v_mfma_f32_16x16x32_bf16 v[212:215], v[88:91], v[184:187], v[212:215]\n"
    "v_mul_f32 v201, v201, v253\n"
    "v_mul_f32 v202, v202, v253\n"
    "v_mfma_f32_16x16x32_bf16 v[208:211], v[68:71], v[188:191], v[208:211]\n"
    "v_mul_f32 v203, v203, v253\n"
    "v_mul_f32 v204, v204, v253\n"
    "v_mfma_f32_16x16x32_bf16 v[212:215], v[92:95], v[188:191], v[212:215]\n"
    "v_mul_f32 v205, v205, v253\n"
    "v_mul_f32 v206, v206, v253\n"
    "v_mfma_f32_16x16x32_bf16 v[208:211], v[72:75], v[192:195], v[208:211]\n"
    "v_mul_f32 v207, v207, v253\n"
    "v_cvt_pk_bf16_f32 v200, v200, v201\n"
    "v_mfma_f32_16x16x32_bf16 v[212:215], v[96:99], v[192:195], v[212:215]\n"
    "v_cvt_pk_bf16_f32 v201, v202, v203\n"
    "v_cvt_pk_bf16_f32 v202, v204, v205\n"
    "v_mfma_f32_16x16x32_bf16 v[208:211], v[76:79], v[196:199], v[208:211]\n"
    "s_waitcnt lgkmcnt(0)\n"
    "v_cmp_gt_u32 vcc, 2, v228\n"
    "v_mfma_f32_16x16x32_bf16 v[212:215], v[100:103], v[196:199], v[212:215]\n"
    "v_cvt_pk_bf16_f32 v203, v206, v207\n"
    "s_cbranch_vccnz .Lh9\n"
    "v_mfma_f32_16x16x32_bf16 v[216:219], v[104:107], v[176:179], 0\n"
    "ds_read_b64_tr_b16 v[152:153], v2 offset:18432\n"
    "ds_read_b64_tr_b16 v[154:155], v3 offset:18432\n"
    "v_mfma_f32_16x16x32_bf16 v[220:223], v[128:131], v[176:179], 0\n"
    "ds_read_b64_tr_b16 v[156:157], v2 offset:19456\n"
    "ds_read_b64_tr_b16 v[158:159], v3 offset:19456\n"
    "v_mfma_f32_16x16x32_bf16 v[216:219], v[108:111], v[180:183], v[216:219]\n"
    "ds_read_b64_tr_b16 v[160:161], v2 offset:20480\n"
    "ds_read_b64_tr_b16 v[162:163], v3 offset:20480\n"
    "v_mfma_f32_16x16x32_bf16 v[220:223], v[132:135], v[180:183], v[220:223]\n"
    "ds_read_b64_tr_b16 v[164:165], v2 offset:21504\n"
    "ds_read_b64_tr_b16 v[166:167], v3 offset:21504\n"
    "v_mfma_f32_16x16x32_bf16 v[216:219], v[112:115], v[184:187], v[216:219]\n"
    "buffer_store_dwordx4 v[200:203], v5, s[80:83], 0 offen offset:0 sc1\n"
    "v_mfma_f32_16x16x32_bf16 v[220:223], v[136:139], v[184:187], v[220:223]\n"
    "v_mul_f32 v208, v208, v253\n"
    "v_mul_f32 v209, v209, v253\n"
    "v_mfma_f32_16x16x32_bf16 v[216:219], v[116:119], v[188:191], v[216:219]\n"
    "ds_read_b64_tr_b16 v[168:169], v2 offset:22528\n"
    "ds_read_b64_tr_b16 v[170:171], v3 offset:22528\n"
    "v_mfma_f32_16x16x32_bf16 v[220:223], v[140:143], v[188:191], v[220:223]\n"
    "v_mul_f32 v210, v210, v253\n"
    "v_mul_f32 v211, v211, v253\n"
    "v_mfma_f32_16x16x32_bf16 v[216:219], v[120:123], v[192:195], v[216:219]\n"
    "ds_read_b64_tr_b16 v[172:173], v2 offset:23552\n"
    "ds_read_b64_tr_b16 v[174:175], v3 offset:23552\n"
    "v_mfma_f32_16x16x32_bf16 v[220:223], v[144:147], v[192:195], v[220:223]\n"
    "v_mul_f32 v212, v212, v253\n"
    "v_mov_b32 v227, 7\n"
    "v_mfma_f32_16x16x32_bf16 v[216:219], v[124:127], v[196:199], v[216:219]\n"
    "ds_read_b32 v252, v1 offset:0\n"
    "v_mul_f32 v213, v213, v253\n"
    "v_mfma_f32_16x16x32_bf16 v[220:223], v[148:151], v[196:199], v[220:223]\n"
    "v_mul_f32 v214, v214, v253\n"
    "v_mul_f32 v215, v215, v253\n"
    "ds_write_b32 v226, v227 offset:32\n"
    "v_cvt_pk_bf16_f32 v208, v208, v209\n"
    "v_cvt_pk_bf16_f32 v209, v210, v211\n"
    "v_cvt_pk_bf16_f32 v210, v212, v213\n"
    "v_cvt_pk_bf16_f32 v211, v214, v215\n"
    "buffer_store_dwordx4 v[208:211], v5, s[80:83], 0 offen offset:64 sc1\n"
    ".Lh9j:\n"
    "s_waitcnt lgkmcnt(2)\n"
    "v_mfma_f32_16x16x32_bf16 v[200:203], v[8:11], v[152:155], 0\n"
    "v_mul_f32 v216, v216, v253\n"
    "v_mul_f32 v217, v217, v253\n"
    "v_mfma_f32_16x16x32_bf16 v[204:207], v[32:35], v[152:155], 0\n"
    "v_mul_f32 v218, v218, v253\n"
    "v_mul_f32 v219, v219, v253\n"
    "v_mfma_f32_16x16x32_bf16 v[200:203], v[12:15], v[156:159], v[200:203]\n"
    "v_mul_f32 v220, v220, v253\n"
    "v_mul_f32 v221, v221, v253\n"
    "v_mfma_f32_16x16x32_bf16 v[204:207], v[36:39], v[156:159], v[204:207]\n"
    "v_mul_f32 v222, v222, v253\n"
    "v_mul_f32 v223, v223, v253\n"
    "v_mfma_f32_16x16x32_bf16 v[200:203], v[16:19], v[160:163], v[200:203]\n"
    "v_cvt_pk_bf16_f32 v216, v216, v217\n"
    "v_cvt_pk_bf16_f32 v217, v218, v219\n"
    "v_mfma_f32_16x16x32_bf16 v[204:207], v[40:43], v[160:163], v[204:207]\n"
    "v_cvt_pk_bf16_f32 v218, v220, v221\n"
    "v_cvt_pk_bf16_f32 v219, v222, v223\n"
    "v_mfma_f32_16x16x32_bf16 v[200:203], v[20:23], v[164:167], v[200:203]\n"
    "buffer_store_dwordx4 v[216:219], v5, s[80:83], 0 offen offset:128 sc1\n"
    "v_mfma_f32_16x16x32_bf16 v[204:207], v[44:47], v[164:167], v[204:207]\n"
    "v_mfma_f32_16x16x32_bf16 v[200:203], v[24:27], v[168:171], v[200:203]\n"
    "v_add_u32 v5, s46, v5\n"
    "v_mfma_f32_16x16x32_bf16 v[204:207], v[48:51], v[168:171], v[204:207]\n"
    "v_mfma_f32_16x16x32_bf16 v[200:203], v[28:31], v[172:175], v[200:203]\n"
    "v_mfma_f32_16x16x32_bf16 v[204:207], v[52:55], v[172:175], v[204:207]\n"
    "v_mfma_f32_16x16x32_bf16 v[208:211], v[56:59], v[152:155], 0\n"
    "v_mfma_f32_16x16x32_bf16 v[212:215], v[80:83], v[152:155], 0\n"
    "ds_read_b32 v228, v224 offset:176\n"
    "v_mfma_f32_16x16x32_bf16 v[208:211], v[60:63], v[156:159], v[208:211]\n"
    "v_mfma_f32_16x16x32_bf16 v[212:215], v[84:87], v[156:159], v[212:215]\n"
    "v_mfma_f32_16x16x32_bf16 v[208:211], v[64:67], v[160:163], v[208:211]\n"
    "s_waitcnt lgkmcnt(2)\n"
    "v_mul_f32 v200, v200, v252\n"
    "v_mfma_f32_16x16x32_bf16 v[212:215], v[88:91], v[160:163], v[212:215]\n"
    "v_mul_f32 v201, v201, v252\n"
    "v_mul_f32 v202, v202, v252\n"
    "v_mfma_f32_16x16x32_bf16 v[208:211], v[68:71], v[164:167], v[208:211]\n"
    "v_mul_f32 v203, v203, v252\n"
    "v_mul_f32 v204, v204, v252\n"
    "v_mfma_f32_16x16x32_bf16 v[212:215], v[92:95], v[164:167], v[212:215]\n"
    "v_mul_f32 v205, v205, v252\n"
    "v_mul_f32 v206, v206, v252\n"
    "v_mfma_f32_16x16x32_bf16 v[208:211], v[72:75], v[168:171], v[208:211]\n"
    "v_mul_f32 v207, v207, v252\n"
    "v_cvt_pk_bf16_f32 v200, v200, v201\n"
    "v_mfma_f32_16x16x32_bf16 v[212:215], v[96:99], v[168:171], v[212:215]\n"
    "v_cvt_pk_bf16_f32 v201, v202, v203\n"
    "v_cvt_pk_bf16_f32 v202, v204, v205\n"
    "v_mfma_f32_16x16x32_bf16 v[208:211], v[76:79], v[172:175], v[208:211]\n"
    "s_waitcnt lgkmcnt(0)\n"
    "v_cmp_gt_u32 vcc, 2, v228\n"
    "v_mfma_f32_16x16x32_bf16 v[212:215], v[100:103], v[172:175], v[212:215]\n"
    "v_cvt_pk_bf16_f32 v203, v206, v207\n"
    "s_cbranch_vccnz .Lh10\n"
    "v_mfma_f32_16x16x32_bf16 v[216:219], v[104:107], v[152:155], 0\n"
    "ds_read_b64_tr_b16 v[176:177], v2 offset:24576\n"
    "ds_read_b64_tr_b16 v[178:179], v3 offset:24576\n"
    "v_mfma_f32_16x16x32_bf16 v[220:223], v[128:131], v[152:155], 0\n"
    "ds_read_b64_tr_b16 v[180:181], v2 offset:25600\n"
    "ds_read_b64_tr_b16 v[182:183], v3 offset:25600\n"
    "v_mfma_f32_16x16x32_bf16 v[216:219], v[108:111], v[156:159], v[216:219]\n"
    "ds_read_b64_tr_b16 v[184:185], v2 offset:26624\n"
    "ds_read_b64_tr_b16 v[186:187], v3 offset:26624\n"
    "v_mfma_f32_16x16x32_bf16 v[220:223], v[132:135], v[156:159], v[220:223]\n"
    "ds_read_b64_tr_b16 v[188:189], v2 offset:27648\n"
    "ds_read_b64_tr_b16 v[190:191], v3 offset:27648\n"
    "v_mfma_f32_16x16x32_bf16 v[216:219], v[112:115], v[160:163], v[216:219]\n"
    "buffer_store_dwordx4 v[200:203], v4, s[80:83], 0 offen offset:0 sc1\n"
    "v_mfma_f32_16x16x32_bf16 v[220:223], v[136:139], v[160:163], v[220:223]\n"
    "v_mul_f32 v208, v208, v252\n"
    "v_mul_f32 v209, v209, v252\n"
    "v_mfma_f32_16x16x32_bf16 v[216:219], v[116:119], v[164:167], v[216:219]\n"
    "ds_read_b64_tr_b16 v[192:193], v2 offset:28672\n"
    "ds_read_b64_tr_b16 v[194:195], v3 offset:28672\n"
    "v_mfma_f32_16x16x32_bf16 v[220:223], v[140:143], v[164:167], v[220:223]\n"
    "v_mul_f32 v210, v210, v252\n"
    "v_mul_f32 v211, v211, v252\n"
    "v_mfma_f32_16x16x32_bf16 v[216:219], v[120:123], v[168:171], v[216:219]\n"
    "ds_read_b64_tr_b16 v[196:197], v2 offset:29696\n"
    "ds_read_b64_tr_b16 v[198:199], v3 offset:29696\n"
    "v_mfma_f32_16x16x32_bf16 v[220:223], v[144:147], v[168:171], v[220:223]\n"
    "v_mul_f32 v212, v212, v252\n"
    "v_mul_f32 v213, v213, v252\n"
    "v_mfma_f32_16x16x32_bf16 v[216:219], v[124:127], v[172:175], v[216:219]\n"
    "ds_read_b32 v253, v1 offset:64\n"
    "v_mul_f32 v214, v214, v252\n"
    "v_mfma_f32_16x16x32_bf16 v[220:223], v[148:151], v[172:175], v[220:223]\n"
    "v_mul_f32 v215, v215, v252\n"
    "v_cvt_pk_bf16_f32 v208, v208, v209\n"
    "v_cvt_pk_bf16_f32 v209, v210, v211\n"
    "v_cvt_pk_bf16_f32 v210, v212, v213\n"
    "v_cvt_pk_bf16_f32 v211, v214, v215\n"
    "buffer_store_dwordx4 v[208:211], v4, s[80:83], 0 offen offset:64 sc1\n"
    ".Lh10j:\n"
    "s_waitcnt lgkmcnt(1)\n"
    "v_mfma_f32_16x16x32_bf16 v[200:203], v[8:11], v[176:179], 0\n"
    "v_mul_f32 v216, v216, v252\n"
    "v_mul_f32 v217, v217, v252\n"
    "v_mfma_f32_16x16x32_bf16 v[204:207], v[32:35], v[176:179], 0\n"
    "v_mul_f32 v218, v218, v252\n"
    "v_mul_f32 v219, v219, v252\n"
    "v_mfma_f32_16x16x32_bf16 v[200:203], v[12:15], v[180:183], v[200:203]\n"
    "v_mul_f32 v220, v220, v252\n"
    "v_mul_f32 v221, v221, v252\n"
    "v_mfma_f32_16x16x32_bf16 v[204:207], v[36:39], v[180:183], v[204:207]\n"
    "v_mul_f32 v222, v222, v252\n"
    "v_mul_f32 v223, v223, v252\n"
    "v_mfma_f32_16x16x32_bf16 v[200:203], v[16:19], v[184:187], v[200:203]\n"
    "v_cvt_pk_bf16_f32 v216, v216, v217\n"
    "v_cvt_pk_bf16_f32 v217, v218, v219\n"
    "v_mfma_f32_16x16x32_bf16 v[204:207], v[40:43], v[184:187], v[204:207]\n"
    "v_cvt_pk_bf16_f32 v218, v220, v221\n"
    "v_cvt_pk_bf16_f32 v219, v222, v223\n"
    "v_mfma_f32_16x16x32_bf16 v[200:203], v[20:23], v[188:191], v[200:203]\n"
    "buffer_store_dwordx4 v[216:219], v4, s[80:83], 0 offen offset:128 sc1\n"
    "v_mfma_f32_16x16x32_bf16 v[204:207], v[44:47], v[188:191], v[204:207]\n"
    "v_mfma_f32_16x16x32_bf16 v[200:203], v[24:27], v[192:195], v[200:203]\n"
    "v_add_u32 v4, v6, v4\n"
    "v_mfma_f32_16x16x32_bf16 v[204:207], v[48:51], v[192:195], v[204:207]\n"
    "v_mfma_f32_16x16x32_bf16 v[200:203], v[28:31], v[196:199], v[200:203]\n"
    "v_mfma_f32_16x16x32_bf16 v[204:207], v[52:55], v[196:199], v[204:207]\n"
    "v_mfma_f32_16x16x32_bf16 v[208:211], v[56:59], v[176:179], 0\n"
    "v_mfma_f32_16x16x32_bf16 v[212:215], v[80:83], v[176:179], 0\n"
    "ds_read_b32 v228, v224 offset:128\n"
    "v_mfma_f32_16x16x32_bf16 v[208:211], v[60:63], v[180:183], v[208:211]\n"
    "v_mfma_f32_16x16x32_bf16 v[212:215], v[84:87], v[180:183], v[212:215]\n"
    "v_mfma_f32_16x16x32_bf16 v[208:211], v[64:67], v[184:187], v[208:211]\n"
    "s_waitcnt lgkmcnt(1)\n"
    "v_mul_f32 v200, v200, v253\n"
    "v_mfma_f32_16x16x32_bf16 v[212:215], v[88:91], v[184:187], v[212:215]\n"
    "v_mul_f32 v201, v201, v253\n"
    "v_mul_f32 v202, v202, v253\n"
    "v_mfma_f32_16x16x32_bf16 v[208:211], v[68:71], v[188:191], v[208:211]\n"
    "v_mul_f32 v203, v203, v253\n"
    "v_mul_f32 v204, v204, v253\n"
    "v_mfma_f32_16x16x32_bf16 v[212:215], v[92:95], v[188:191], v[212:215]\n"
    "v_mul_f32 v205, v205, v253\n"
    "v_mul_f32 v206, v206, v253\n"
    "v_mfma_f32_16x16x32_bf16 v[208:211], v[72:75], v[192:195], v[208:211]\n"
    "v_mul_f32 v207, v207, v253\n"
    "v_cvt_pk_bf16_f32 v200, v200, v201\n"
    "v_mfma_f32_16x16x32_bf16 v[212:215], v[96:99], v[192:195], v[212:215]\n"
    "v_cvt_pk_bf16_f32 v201, v202, v203\n"
    "v_cvt_pk_bf16_f32 v202, v204, v205\n"
    "v_mfma_f32_16x16x32_bf16 v[208:211], v[76:79], v[196:199], v[208:211]\n"
    "s_waitcnt lgkmcnt(0)\n"
    "v_cmp_gt_u32 vcc, 3, v228\n"
    "v_mfma_f32_16x16x32_bf16 v[212:215], v[100:103], v[196:199], v[212:215]\n"
    "v_cvt_pk_bf16_f32 v203, v206, v207\n"
    "s_cbranch_vccnz .Lh11\n"
    "v_mfma_f32_16x16x32_bf16 v[216:219], v[104:107], v[176:179], 0\n"
    "ds_read_b64_tr_b16 v[152:153], v2 offset:30720\n"
    "ds_read_b64_tr_b16 v[154:155], v3 offset:30720\n"
    "v_mfma_f32_16x16x32_bf16 v[220:223], v[128:131], v[176:179], 0\n"
    "ds_read_b64_tr_b16 v[156:157], v2 offset:31744\n"
    "ds_read_b64_tr_b16 v[158:159], v3 offset:31744\n"
    "v_mfma_f32_16x16x32_bf16 v[216:219], v[108:111], v[180:183], v[216:219]\n"
    "ds_read_b64_tr_b16 v[160:161], v2 offset:32768\n"
    "ds_read_b64_tr_b16 v[162:163], v3 offset:32768\n"
    "v_mfma_f32_16x16x32_bf16 v[220:223], v[132:135], v[180:183], v[220:223]\n"
    "ds_read_b64_tr_b16 v[164:165], v2 offset:33792\n"
    "ds_read_b64_tr_b16 v[166:167], v3 offset:33792\n"
    "v_mfma_f32_16x16x32_bf16 v[216:219], v[112:115], v[184:187], v[216:219]\n"
    "buffer_store_dwordx4 v[200:203], v5, s[80:83], 0 offen offset:0 sc1\n"
    "v_mfma_f32_16x16x32_bf16 v[220:223], v[136:139], v[184:187], v[220:223]\n"
    "v_mul_f32 v208, v208, v253\n"
    "v_mul_f32 v209, v209, v253\n"
    "v_mfma_f32_16x16x32_bf16 v[216:219], v[116:119], v[188:191], v[216:219]\n"
    "ds_read_b64_tr_b16 v[168:169], v2 offset:34816\n"
    "ds_read_b64_tr_b16 v[170:171], v3 offset:34816\n"
    "v_mfma_f32_16x16x32_bf16 v[220:223], v[140:143], v[188:191], v[220:223]\n"
    "v_mul_f32 v210, v210, v253\n"
    "v_mul_f32 v211, v211, v253\n"
    "v_mfma_f32_16x16x32_bf16 v[216:219], v[120:123], v[192:195], v[216:219]\n"
    "ds_read_b64_tr_b16 v[172:173], v2 offset:35840\n"
    "ds_read_b64_tr_b16 v[174:175], v3 offset:35840\n"
    "v_mfma_f32_16x16x32_bf16 v[220:223], v[144:147], v[192:195], v[220:223]\n"
    "v_mul_f32 v212, v212, v253\n"
    "v_mul_f32 v213, v213, v253\n"
    "v_mfma_f32_16x16x32_bf16 v[216:219], v[124:127], v[196:199], v[216:219]\n"
    "ds_read_b32 v252, v1 offset:128\n"
    "v_mul_f32 v214, v214, v253\n"
    "v_mfma_f32_16x16x32_bf16 v[220:223], v[148:151], v[196:199], v[220:223]\n"
    "v_mul_f32 v215, v215, v253\n"
    "v_cvt_pk_bf16_f32 v208, v208, v209\n"
    "v_cvt_pk_bf16_f32 v209, v210, v211\n"
    "v_cvt_pk_bf16_f32 v210, v212, v213\n"
    "v_cvt_pk_bf16_f32 v211, v214, v215\n"
    "buffer_store_dwordx4 v[208:211], v5, s[80:83], 0 offen offset:64 sc1\n"
    ".Lh11j:\n"
    "s_waitcnt lgkmcnt(1)\n"
    "v_mfma_f32_16x16x32_bf16 v[200:203], v[8:11], v[152:155], 0\n"
    "v_mul_f32 v216, v216, v253\n"
    "v_mul_f32 v217, v217, v253\n"
    "v_mfma_f32_16x16x32_bf16 v[204:207], v[32:35], v[152:155], 0\n"
    "v_mul_f32 v218, v218, v253\n"
    "v_mul_f32 v219, v219, v253\n"
    "v_mfma_f32_16x16x32_bf16 v[200:203], v[12:15], v[156:159], v[200:203]\n"
    "v_mul_f32 v220, v220, v253\n"
    "v_mul_f32 v221, v221, v253\n"
    "v_mfma_f32_16x16x32_bf16 v[204:207], v[36:39], v[156:159], v[204:207]\n"
    "v_mul_f32 v222, v222, v253\n"
    "v_mul_f32 v223, v223, v253\n"
    "v_mfma_f32_16x16x32_bf16 v[200:203], v[16:19], v[160:163], v[200:203]\n"
    "v_cvt_pk_bf16_f32 v216, v216, v217\n"
    "v_cvt_pk_bf16_f32 v217, v218, v219\n"
    "v_mfma_f32_16x16x32_bf16 v[204:207], v[40:43], v[160:163], v[204:207]\n"
    "v_cvt_pk_bf16_f32 v218, v220, v221\n"
    "v_cvt_pk_bf16_f32 v219, v222, v223\n"
    "v_mfma_f32_16x16x32_bf16 v[200:203], v[20:23], v[164:167], v[200:203]\n"
    "buffer_store_dwordx4 v[216:219], v5, s[80:83], 0 offen offset:128 sc1\n"
    "v_mfma_f32_16x16x32_bf16 v[204:207], v[44:47], v[164:167], v[204:207]\n"
    "v_mfma_f32_16x16x32_bf16 v[200:203], v[24:27], v[168:171], v[200:203]\n"
    "v_add_u32 v5, v6, v5\n"
    "v_mfma_f32_16x16x32_bf16 v[204:207], v[48:51], v[168:171], v[204:207]\n"
    "v_mfma_f32_16x16x32_bf16 v[200:203], v[28:31], v[172:175], v[200:203]\n"
    "v_mfma_f32_16x16x32_bf16 v[204:207], v[52:55], v[172:175], v[204:207]\n"
    "v_mfma_f32_16x16x32_bf16 v[208:211], v[56:59], v[152:155], 0\n"
    "v_mfma_f32_16x16x32_bf16 v[212:215], v[80:83], v[152:155], 0\n"
    "ds_read_b32 v228, v224 offset:144\n"
    "v_mfma_f32_16x16x32_bf16 v[208:211], v[60:63], v[156:159], v[208:211]\n"
    "v_mfma_f32_16x16x32_bf16 v[212:215], v[84:87], v[156:159], v[212:215]\n"
    "v_mfma_f32_16x16x32_bf16 v[208:211], v[64:67], v[160:163], v[208:211]\n"
    "s_waitcnt lgkmcnt(1)\n"
    "v_mul_f32 v200, v200, v252\n"
    "v_mfma_f32_16x16x32_bf16 v[212:215], v[88:91], v[160:163], v[212:215]\n"
    "v_mul_f32 v201, v201, v252\n"
    "v_mul_f32 v202, v202, v252\n"
    "v_mfma_f32_16x16x32_bf16 v[208:211], v[68:71], v[164:167], v[208:211]\n"
    "v_mul_f32 v203, v203, v252\n"
    "v_mul_f32 v204, v204, v252\n"
    "v_mfma_f32_16x16x32_bf16 v[212:215], v[92:95], v[164:167], v[212:215]\n"
    "v_mul_f32 v205, v205, v252\n"
    "v_mul_f32 v206, v206, v252\n"
    "v_mfma_f32_16x16x32_bf16 v[208:211], v[72:75], v[168:171], v[208:211]\n"
    "v_mul_f32 v207, v207, v252\n"
    "v_cvt_pk_bf16_f32 v200, v200, v201\n"
    "v_mfma_f32_16x16x32_bf16 v[212:215], v[96:99], v[168:171], v[212:215]\n"
    "v_cvt_pk_bf16_f32 v201, v202, v203\n"
    "v_cvt_pk_bf16_f32 v202, v204, v205\n"
    "v_mfma_f32_16x16x32_bf16 v[208:211], v[76:79], v[172:175], v[208:211]\n"
    "s_waitcnt lgkmcnt(0)\n"
    "v_cmp_gt_u32 vcc, 3, v228\n"
    "v_mfma_f32_16x16x32_bf16 v[212:215], v[100:103], v[172:175], v[212:215]\n"
    "v_cvt_pk_bf16_f32 v203, v206, v207\n"
    "s_cbranch_vccnz .Lh12\n"
    "v_mfma_f32_16x16x32_bf16 v[216:219], v[104:107], v[152:155], 0\n"
    "ds_read_b64_tr_b16 v[176:177], v2 offset:18432\n"
    "ds_read_b64_tr_b16 v[178:179], v3 offset:18432\n"
    "v_mfma_f32_16x16x32_bf16 v[220:223], v[128:131], v[152:155], 0\n"
    "ds_read_b64_tr_b16 v[180:181], v2 offset:19456\n"
    "ds_read_b64_tr_b16 v[182:183], v3 offset:19456\n"
    "v_mfma_f32_16x16x32_bf16 v[216:219], v[108:111], v[156:159], v[216:219]\n"
    "ds_read_b64_tr_b16 v[184:185], v2 offset:20480\n"
    "ds_read_b64_tr_b16 v[186:187], v3 offset:20480\n"
    "v_mfma_f32_16x16x32_bf16 v[220:223], v[132:135], v[156:159], v[220:223]\n"
    "ds_read_b64_tr_b16 v[188:189], v2 offset:21504\n"
    "ds_read_b64_tr_b16 v[190:191], v3 offset:21504\n"
    "v_mfma_f32_16x16x32_bf16 v[216:219], v[112:115], v[160:163], v[216:219]\n"
    "buffer_store_dwordx4 v[200:203], v4, s[80:83], 0 offen offset:0 sc1\n"
    "v_mfma_f32_16x16x32_bf16 v[220:223], v[136:139], v[160:163], v[220:223]\n"
    "v_mul_f32 v208, v208, v252\n"
    "v_mul_f32 v209, v209, v252\n"
    "v_mfma_f32_16x16x32_bf16 v[216:219], v[116:119], v[164:167], v[216:219]\n"
    "ds_read_b64_tr_b16 v[192:193], v2 offset:22528\n"
    "ds_read_b64_tr_b16 v[194:195], v3 offset:22528\n"
    "v_mfma_f32_16x16x32_bf16 v[220:223], v[140:143], v[164:167], v[220:223]\n"
    "v_mul_f32 v210, v210, v252\n"
    "v_mul_f32 v211, v211, v252\n"
    "v_mfma_f32_16x16x32_bf16 v[216:219], v[120:123], v[168:171], v[216:219]\n"
    "ds_read_b64_tr_b16 v[196:197], v2 offset:23552\n"
    "ds_read_b64_tr_b16 v[198:199], v3 offset:23552\n"
    "v_mfma_f32_16x16x32_bf16 v[220:223], v[144:147], v[168:171], v[220:223]\n"
    "v_mul_f32 v212, v212, v252\n"
    "v_mul_f32 v213, v213, v252\n"
    "v_mfma_f32_16x16x32_bf16 v[216:219], v[124:127], v[172:175], v[216:219]\n"
    "ds_read_b32 v253, v1 offset:0\n"
    "v_mul_f32 v214, v214, v252\n"
    "v_mfma_f32_16x16x32_bf16 v[220:223], v[148:151], v[172:175], v[220:223]\n"
    "v_mul_f32 v215, v215, v252\n"
    "v_cvt_pk_bf16_f32 v208, v208, v209\n"
    "v_cvt_pk_bf16_f32 v209, v210, v211\n"
    "v_cvt_pk_bf16_f32 v210, v212, v213\n"
    "v_cvt_pk_bf16_f32 v211, v214, v215\n"
    "buffer_store_dwordx4 v[208:211], v4, s[80:83], 0 offen offset:64 sc1\n"
    ".Lh12j:\n"
    "s_waitcnt lgkmcnt(1)\n"
    "v_mfma_f32_16x16x32_bf16 v[200:203], v[8:11], v[176:179], 0\n"
    "v_mul_f32 v216, v216, v252\n"
    "v_mul_f32 v217, v217, v252\n"
    "v_mfma_f32_16x16x32_bf16 v[204:207], v[32:35], v[176:179], 0\n"
    "ds_read_b32 v228, v224 offset:160\n"
    "v_mul_f32 v218, v218, v252\n"
    "v_mfma_f32_16x16x32_bf16 v[200:203], v[12:15], v[180:183], v[200:203]\n"
    "v_mul_f32 v219, v219, v252\n"
    "v_mul_f32 v220, v220, v252\n"
    "v_mfma_f32_16x16x32_bf16 v[204:207], v[36:39], v[180:183], v[204:207]\n"
    "v_mul_f32 v221, v221, v252\n"
    "v_mul_f32 v222, v222, v252\n"
    "v_mfma_f32_16x16x32_bf16 v[200:203], v[16:19], v[184:187], v[200:203]\n"
    "v_mul_f32 v223, v223, v252\n"
    "v_cvt_pk_bf16_f32 v216, v216, v217\n"
    "v_mfma_f32_16x16x32_bf16 v[204:207], v[40:43], v[184:187], v[204:207]\n"
    "v_cvt_pk_bf16_f32 v217, v218, v219\n"
    "v_cvt_pk_bf16_f32 v218, v220, v221\n"
    "v_mfma_f32_16x16x32_bf16 v[200:203], v[20:23], v[188:191], v[200:203]\n"
    "v_cvt_pk_bf16_f32 v219, v222, v223\n"
    "v_mfma_f32_16x16x32_bf16 v[204:207], v[44:47], v[188:191], v[204:207]\n"
    "buffer_store_dwordx4 v[216:219], v4, s[80:83], 0 offen offset:128 sc1\n"
    "v_mfma_f32_16x16x32_bf16 v[200:203], v[24:27], v[192:195], v[200:203]\n"
    "v_mfma_f32_16x16x32_bf16 v[204:207], v[48:51], v[192:195], v[204:207]\n"
    "v_add_u32 v4, v7, v4\n"
    "v_mfma_f32_16x16x32_bf16 v[200:203], v[28:31], v[196:199], v[200:203]\n"
    "s_waitcnt lgkmcnt(0)\n"
    "v_cmp_gt_u32 vcc, 3, v228\n"
    "v_mfma_f32_16x16x32_bf16 v[204:207], v[52:55], v[196:199], v[204:207]\n"
    "s_cbranch_vccnz .Lh13\n"
    "v_mfma_f32_16x16x32_bf16 v[208:211], v[56:59], v[176:179], 0\n"
    "ds_read_b64_tr_b16 v[152:153], v2 offset:6144\n"
    "ds_read_b64_tr_b16 v[154:155], v3 offset:6144\n"
    "v_mfma_f32_16x16x32_bf16 v[212:215], v[80:83], v[176:179], 0\n"
    "ds_read_b64_tr_b16 v[156:157], v2 offset:7168\n"
    "ds_read_b64_tr_b16 v[158:159], v3 offset:7168\n"
    "v_mfma_f32_16x16x32_bf16 v[208:211], v[60:63], v[180:183], v[208:211]\n"
    "ds_read_b64_tr_b16 v[160:161], v2 offset:8192\n"
    "ds_read_b64_tr_b16 v[162:163], v3 offset:8192\n"
    "v_mfma_f32_16x16x32_bf16 v[212:215], v[84:87], v[180:183], v[212:215]\n"
    "ds_read_b64_tr_b16 v[164:165], v2 offset:9216\n"
    "ds_read_b64_tr_b16 v[166:167], v3 offset:9216\n"
    "v_mfma_f32_16x16x32_bf16 v[208:211], v[64:67], v[184:187], v[208:211]\n"
    "v_mul_f32 v200, v200, v253\n"
    "v_mul_f32 v201, v201, v253\n"
    "v_mfma_f32_16x16x32_bf16 v[212:215], v[88:91], v[184:187], v[212:215]\n"
    "ds_read_b64_tr_b16 v[168:169], v2 offset:10240\n"
    "ds_read_b64_tr_b16 v[170:171], v3 offset:10240\n"
    "v_mfma_f32_16x16x32_bf16 v[208:211], v[68:71], v[188:191], v[208:211]\n"
    "v_mul_f32 v202, v202, v253\n"
    "v_mul_f32 v203, v203, v253\n"
    "v_mfma_f32_16x16x32_bf16 v[212:215], v[92:95], v[188:191], v[212:215]\n"
    "ds_read_b64_tr_b16 v[172:173], v2 offset:11264\n"
    "ds_read_b64_tr_b16 v[174:175], v3 offset:11264\n"
    "v_mfma_f32_16x16x32_bf16 v[208:211], v[72:75], v[192:195], v[208:211]\n"
    "v_mul_f32 v204, v204, v253\n"
    "v_mul_f32 v205, v205, v253\n"
    "v_mfma_f32_16x16x32_bf16 v[212:215], v[96:99], v[192:195], v[212:215]\n"
    "ds_read_b32 v252, v1 offset:256\n"
    "v_mul_f32 v206, v206, v253\n"
    "v_mfma_f32_16x16x32_bf16 v[208:211], v[76:79], v[196:199], v[208:211]\n"
    "v_mul_f32 v207, v207, v253\n"
    "v_cvt_pk_bf16_f32 v200, v200, v201\n"
    "v_mfma_f32_16x16x32_bf16 v[212:215], v[100:103], v[196:199], v[212:215]\n"
    "v_cvt_pk_bf16_f32 v201, v202, v203\n"
    "v_cvt_pk_bf16_f32 v202, v204, v205\n"
    "v_cvt_pk_bf16_f32 v203, v206, v207\n"
    "buffer_store_dwordx4 v[200:203], v5, s[80:83], 0 offen offset:0 sc1\n"
    ".Lh13j:\n"
    "s_waitcnt lgkmcnt(1)\n"
    "s_nop 0\n"
    "v_mfma_f32_16x16x32_bf16 v[200:203], v[8:11], v[152:155], 0\n"
    "v_mul_f32 v208, v208, v253\n"
    "v_mul_f32 v209, v209, v253\n"
    "v_mfma_f32_16x16x32_bf16 v[204:207], v[32:35], v[152:155], 0\n"
    "ds_read_b32 v228, v224 offset:176\n"
    "v_mul_f32 v210, v210, v253\n"
    "v_mfma_f32_16x16x32_bf16 v[200:203], v[12:15], v[156:159], v[200:203]\n"
    "v_mul_f32 v211, v211, v253\n"
    "v_mul_f32 v212, v212, v253\n"
    "v_mfma_f32_16x16x32_bf16 v[204:207], v[36:39], v[156:159], v[204:207]\n"
    "v_mul_f32 v213, v213, v253\n"
    "v_mul_f32 v214, v214, v253\n"
    "v_mfma_f32_16x16x32_bf16 v[200:203], v[16:19], v[160:163], v[200:203]\n"
    "v_mul_f32 v215, v215, v253\n"
    "v_cvt_pk_bf16_f32 v208, v208, v209\n"
    "v_mfma_f32_16x16x32_bf16 v[204:207], v[40:43], v[160:163], v[204:207]\n"
    "v_cvt_pk_bf16_f32 v209, v210, v211\n"
    "v_cvt_pk_bf16_f32 v210, v212, v213\n"
    "v_mfma_f32_16x16x32_bf16 v[200:203], v[20:23], v[164:167], v[200:203]\n"
    "v_cvt_pk_bf16_f32 v211, v214, v215\n"
    "v_mfma_f32_16x16x32_bf16 v[204:207], v[44:47], v[164:167], v[204:207]\n"
    "buffer_store_dwordx4 v[208:211], v5, s[80:83], 0 offen offset:64 sc1\n"
    "v_mfma_f32_16x16x32_bf16 v[200:203], v[24:27], v[168:171], v[200:203]\n"
    "v_mfma_f32_16x16x32_bf16 v[204:207], v[48:51], v[168:171], v[204:207]\n"
    "v_add_u32 v5, v7, v5\n"
    "v_mfma_f32_16x16x32_bf16 v[200:203], v[28:31], v[172:175], v[200:203]\n"
    "s_waitcnt lgkmcnt(0)\n"
    "v_cmp_gt_u32 vcc, 3, v228\n"
    "v_mfma_f32_16x16x32_bf16 v[204:207], v[52:55], v[172:175], v[204:207]\n"
    "s_cbranch_vccnz .Lh14\n"
    "v_mfma_f32_16x16x32_bf16 v[208:211], v[56:59], v[152:155], 0\n"
    "ds_read_b64_tr_b16 v[176:177], v2 offset:12288\n"
    "ds_read_b64_tr_b16 v[178:179], v3 offset:12288\n"
    "v_mfma_f32_16x16x32_bf16 v[212:215], v[80:83], v[152:155], 0\n"
    "ds_read_b64_tr_b16 v[180:181], v2 offset:13312\n"
    "ds_read_b64_tr_b16 v[182:183], v3 offset:13312\n"
    "v_mfma_f32_16x16x32_bf16 v[208:211], v[60:63], v[156:159], v[208:211]\n"
    "ds_read_b64_tr_b16 v[184:185], v2 offset:14336\n"
    "ds_read_b64_tr_b16 v[186:187], v3 offset:14336\n"
    "v_mfma_f32_16x16x32_bf16 v[212:215], v[84:87], v[156:159], v[212:215]\n"
    "ds_read_b64_tr_b16 v[188:189], v2 offset:15360\n"
    "ds_read_b64_tr_b16 v[190:191], v3 offset:15360\n"
    "v_mfma_f32_16x16x32_bf16 v[208:211], v[64:67], v[160:163], v[208:211]\n"
    "v_mul_f32 v200, v200, v252\n"
    "v_mul_f32 v201, v201, v252\n"
    "v_mfma_f32_16x16x32_bf16 v[212:215], v[88:91], v[160:163], v[212:215]\n"
    "ds_read_b64_tr_b16 v[192:193], v2 offset:16384\n"
    "ds_read_b64_tr_b16 v[194:195], v3 offset:16384\n"
    "v_mfma_f32_16x16x32_bf16 v[208:211], v[68:71], v[164:167], v[208:211]\n"
    "v_mul_f32 v202, v202, v252\n"
    "v_mul_f32 v203, v203, v252\n"
    "v_mfma_f32_16x16x32_bf16 v[212:215], v[92:95], v[164:167], v[212:215]\n"
    "ds_read_b64_tr_b16 v[196:197], v2 offset:17408\n"
    "ds_read_b64_tr_b16 v[198:199], v3 offset:17408\n"
    "v_mfma_f32_16x16x32_bf16 v[208:211], v[72:75], v[168:171], v[208:211]\n"
    "v_mul_f32 v204, v204, v252\n"
    "v_mul_f32 v205, v205, v252\n"
    "v_mfma_f32_16x16x32_bf16 v[212:215], v[96:99], v[168:171], v[212:215]\n"
    "ds_read_b32 v253, v1 offset:320\n"
    "v_mul_f32 v206, v206, v252\n"
    "v_mfma_f32_16x16x32_bf16 v[208:211], v[76:79], v[172:175], v[208:211]\n"
    "v_mul_f32 v207, v207, v252\n"
    "v_cvt_pk_bf16_f32 v200, v200, v201\n"
    "v_mfma_f32_16x16x32_bf16 v[212:215], v[100:103], v[172:175], v[212:215]\n"
    "v_cvt_pk_bf16_f32 v201, v202, v203\n"
    "v_cvt_pk_bf16_f32 v202, v204, v205\n"
    "v_cvt_pk_bf16_f32 v203, v206, v207\n"
    "buffer_store_dwordx4 v[200:203], v4, s[80:83], 0 offen offset:0 sc1\n"
    ".Lh14j:\n"
    "s_cmp_lg_u32 s58, 0\n"
    "s_cbranch_scc1 .Lh_e12\n"
    "s_waitcnt lgkmcnt(1)\n"
    "v_mfma_f32_16x16x32_bf16 v[200:203], v[8:11], v[176:179], 0\n"
    "v_mul_f32 v208, v208, v252\n"
    "v_mul_f32 v209, v209, v252\n"
    "v_mfma_f32_16x16x32_bf16 v[204:207], v[32:35], v[176:179], 0\n"
    "ds_read_b32 v228, v224 offset:832\n"
    "v_mul_f32 v210, v210, v252\n"
    "v_mfma_f32_16x16x32_bf16 v[200:203], v[12:15], v[180:183], v[200:203]\n"
    "v_mul_f32 v211, v211, v252\n"
    "v_mul_f32 v212, v212, v252\n"
    "v_mfma_f32_16x16x32_bf16 v[204:207], v[36:39], v[180:183], v[204:207]\n"
    "v_mul_f32 v213, v213, v252\n"
    "v_mul_f32 v214, v214, v252\n"
    "v_mfma_f32_16x16x32_bf16 v[200:203], v[16:19], v[184:187], v[200:203]\n"
    "v_mul_f32 v215, v215, v252\n"
    "v_cvt_pk_bf16_f32 v208, v208, v209\n"
    "v_mfma_f32_16x16x32_bf16 v[204:207], v[40:43], v[184:187], v[204:207]\n"
    "v_cvt_pk_bf16_f32 v209, v210, v211\n"
    "v_cvt_pk_bf16_f32 v210, v212, v213\n"
    "v_mfma_f32_16x16x32_bf16 v[200:203], v[20:23], v[188:191], v[200:203]\n"
    "v_cvt_pk_bf16_f32 v211, v214, v215\n"
    "v_mfma_f32_16x16x32_bf16 v[204:207], v[44:47], v[188:191], v[204:207]\n"
    "buffer_store_dwordx4 v[208:211], v4, s[80:83], 0 offen offset:64 sc1\n"
    "v_mfma_f32_16x16x32_bf16 v[200:203], v[24:27], v[192:195], v[200:203]\n"
    "v_mfma_f32_16x16x32_bf16 v[204:207], v[48:51], v[192:195], v[204:207]\n"
    "v_add_u32 v4, v0, v4\n"
    "v_mfma_f32_16x16x32_bf16 v[200:203], v[28:31], v[196:199], v[200:203]\n"
    "s_waitcnt lgkmcnt(0)\n"
    "v_cmp_gt_u32 vcc, 4, v228\n"
    "v_mfma_f32_16x16x32_bf16 v[204:207], v[52:55], v[196:199], v[204:207]\n"
    "s_cbranch_vccnz .Lh15\n"
    "v_mfma_f32_16x16x32_bf16 v[208:211], v[56:59], v[176:179], 0\n"
    "ds_read_b64_tr_b16 v[152:153], v2 offset:0\n"
    "ds_read_b64_tr_b16 v[154:155], v3 offset:0\n"
    "v_mfma_f32_16x16x32_bf16 v[212:215], v[80:83], v[176:179], 0\n"
    "ds_read_b64_tr_b16 v[156:157], v2 offset:1024\n"
    "ds_read_b64_tr_b16 v[158:159], v3 offset:1024\n"
    "v_mfma_f32_16x16x32_bf16 v[208:211], v[60:63], v[180:183], v[208:211]\n"
    "ds_read_b64_tr_b16 v[160:161], v2 offset:2048\n"
    "ds_read_b64_tr_b16 v[162:163], v3 offset:2048\n"
    "v_mfma_f32_16x16x32_bf16 v[212:215], v[84:87], v[180:183], v[212:215]\n"
    "ds_read_b64_tr_b16 v[164:165], v2 offset:3072\n"
    "ds_read_b64_tr_b16 v[166:167], v3 offset:3072\n"
    "v_mfma_f32_16x16x32_bf16 v[208:211], v[64:67], v[184:187], v[208:211]\n"
    "v_mul_f32 v200, v200, v253\n"
    "v_mul_f32 v201, v201, v253\n"
    "v_mfma_f32_16x16x32_bf16 v[212:215], v[88:91], v[184:187], v[212:215]\n"
    "ds_read_b64_tr_b16 v[168:169], v2 offset:4096\n"
    "ds_read_b64_tr_b16 v[170:171], v3 offset:4096\n"
    "v_mfma_f32_16x16x32_bf16 v[208:211], v[68:71], v[188:191], v[208:211]\n"
    "v_mul_f32 v202, v202, v253\n"
    "v_mul_f32 v203, v203, v253\n"
    "v_mfma_f32_16x16x32_bf16 v[212:215], v[92:95], v[188:191], v[212:215]\n"
    "ds_read_b64_tr_b16 v[172:173], v2 offset:5120\n"
    "ds_read_b64_tr_b16 v[174:175], v3 offset:5120\n"
    "v_mfma_f32_16x16x32_bf16 v[208:211], v[72:75], v[192:195], v[208:211]\n"
    "v_mul_f32 v204, v204, v253\n"
    "v_mul_f32 v205, v205, v253\n"
    "v_mfma_f32_16x16x32_bf16 v[212:215], v[96:99], v[192:195], v[212:215]\n"
    "ds_read_b32 v252, v1 offset:192\n"
    "v_mul_f32 v206, v206, v253\n"
    "v_mfma_f32_16x16x32_bf16 v[208:211], v[76:79], v[196:199], v[208:211]\n"
    "v_mul_f32 v207, v207, v253\n"
    "v_cvt_pk_bf16_f32 v200, v200, v201\n"
    "v_mfma_f32_16x16x32_bf16 v[212:215], v[100:103], v[196:199], v[212:215]\n"
    "v_cvt_pk_bf16_f32 v201, v202, v203\n"
    "v_cvt_pk_bf16_f32 v202, v204, v205\n"
    "v_cvt_pk_bf16_f32 v203, v206, v207\n"
    "buffer_store_dwordx4 v[200:203], v5, s[80:83], 0 offen offset:0 sc1\n"
    ".Lh15j:\n"
    "s_waitcnt lgkmcnt(1)\n"
    "s_nop 0\n"
    "v_mfma_f32_16x16x32_bf16 v[200:203], v[8:11], v[152:155], 0\n"
    "v_mul_f32 v208, v208, v253\n"
    "v_mul_f32 v209, v209, v253\n"
    "v_mfma_f32_16x16x32_bf16 v[204:207], v[32:35], v[152:155], 0\n"
    "v_mul_f32 v210, v210, v253\n"
    "v_mul_f32 v211, v211, v253\n"
    "v_mfma_f32_16x16x32_bf16 v[200:203], v[12:15], v[156:159], v[200:203]\n"
    "v_mul_f32 v212, v212, v253\n"
    "v_mul_f32 v213, v213, v253\n"
    "v_mfma_f32_16x16x32_bf16 v[204:207], v[36:39], v[156:159], v[204:207]\n"
    "v_mul_f32 v214, v214, v253\n"
    "v_mul_f32 v215, v215, v253\n"
    "v_mfma_f32_16x16x32_bf16 v[200:203], v[16:19], v[160:163], v[200:203]\n"
    "v_cvt_pk_bf16_f32 v208, v208, v209\n"
    "v_cvt_pk_bf16_f32 v209, v210, v211\n"
    "v_mfma_f32_16x16x32_bf16 v[204:207], v[40:43], v[160:163], v[204:207]\n"
    "v_cvt_pk_bf16_f32 v210, v212, v213\n"
    "v_cvt_pk_bf16_f32 v211, v214, v215\n"
    "v_mfma_f32_16x16x32_bf16 v[200:203], v[20:23], v[164:167], v[200:203]\n"
    "buffer_store_dwordx4 v[208:211], v5, s[80:83], 0 offen offset:64 sc1\n"
    "v_mfma_f32_16x16x32_bf16 v[204:207], v[44:47], v[164:167], v[204:207]\n"
    "v_mfma_f32_16x16x32_bf16 v[200:203], v[24:27], v[168:171], v[200:203]\n"
    "v_mfma_f32_16x16x32_bf16 v[204:207], v[48:51], v[168:171], v[204:207]\n"
    "v_mfma_f32_16x16x32_bf16 v[200:203], v[28:31], v[172:175], v[200:203]\n"
    "v_mfma_f32_16x16x32_bf16 v[204:207], v[52:55], v[172:175], v[204:207]\n"
    "v_mfma_f32_16x16x32_bf16 v[208:211], v[56:59], v[152:155], 0\n"
    "v_mfma_f32_16x16x32_bf16 v[212:215], v[80:83], v[152:155], 0\n"
    "v_mfma_f32_16x16x32_bf16 v[208:211], v[60:63], v[156:159], v[208:211]\n"
    "v_mfma_f32_16x16x32_bf16 v[212:215], v[84:87], v[156:159], v[212:215]\n"
    "v_mfma_f32_16x16x32_bf16 v[208:211], v[64:67], v[160:163], v[208:211]\n"
    "v_mfma_f32_16x16x32_bf16 v[212:215], v[88:91], v[160:163], v[212:215]\n"
    "s_waitcnt lgkmcnt(0)\n"
    "v_mul_f32 v200, v200, v252\n"
    "v_mfma_f32_16x16x32_bf16 v[208:211], v[68:71], v[164:167], v[208:211]\n"
    "v_mul_f32 v201, v201, v252\n"
    "v_mul_f32 v202, v202, v252\n"
    "v_mfma_f32_16x16x32_bf16 v[212:215], v[92:95], v[164:167], v[212:215]\n"
    "v_mul_f32 v203, v203, v252\n"
    "v_mul_f32 v204, v204, v252\n"
    "v_mfma_f32_16x16x32_bf16 v[208:211], v[72:75], v[168:171], v[208:211]\n"
    "v_mul_f32 v205, v205, v252\n"
    "v_mul_f32 v206, v206, v252\n"
    "v_mfma_f32_16x16x32_bf16 v[212:215], v[96:99], v[168:171], v[212:215]\n"
    "v_mul_f32 v207, v207, v252\n"
    "v_cvt_pk_bf16_f32 v200, v200, v201\n"
    "v_mfma_f32_16x16x32_bf16 v[208:211], v[76:79], v[172:175], v[208:211]\n"
    "v_cvt_pk_bf16_f32 v201, v202, v203\n"
    "v_cvt_pk_bf16_f32 v202, v204, v205\n"
    "v_mfma_f32_16x16x32_bf16 v[212:215], v[100:103], v[172:175], v[212:215]\n"
    "v_cvt_pk_bf16_f32 v203, v206, v207\n"
    "buffer_store_dwordx4 v[200:203], v4, s[80:83], 0 offen offset:0 sc1\n"
    "s_nop 2\n"
    "v_mul_f32 v208, v208, v252\n"
    "v_mul_f32 v209, v209, v252\n"
    "v_mul_f32 v210, v210, v252\n"
    "v_mul_f32 v211, v211, v252\n"
    "v_mul_f32 v212, v212, v252\n"
    "v_mul_f32 v213, v213, v252\n"
    "v_mul_f32 v214, v214, v252\n"
    "v_mul_f32 v215, v215, v252\n"
    "v_cvt_pk_bf16_f32 v208, v208, v209\n"
    "v_cvt_pk_bf16_f32 v209, v210, v211\n"
    "v_cvt_pk_bf16_f32 v210, v212, v213\n"
    "v_cvt_pk_bf16_f32 v211, v214, v215\n"
    "buffer_store_dwordx4 v[208:211], v4, s[80:83], 0 offen offset:64 sc1\n"
    ".Lh_end:\n"
    "s_endpgm\n"
    ".Lh1:\n"
    "s_sub_u32 s52, s52, 1\n"
    "s_cmp_lt_i32 s52, 0\n"
    "s_cbranch_scc1 .Lh1b\n"
    "ds_read_b32 v228, v224 offset:288\n"
    "s_waitcnt lgkmcnt(0)\n"
    "v_cmp_gt_u32 vcc, 1, v228\n"
    "s_cbranch_vccnz .Lh1\n"
    "s_branch .Lh1b\n"
    ".Lh2:\n"
    "s_sub_u32 s52, s52, 1\n"
    "s_cmp_lt_i32 s52, 0\n"
    "s_cbranch_scc1 .Lh2b\n"
    "ds_read_b32 v228, v224 offset:0\n"
    "s_waitcnt lgkmcnt(0)\n"
    "v_cmp_gt_u32 vcc, 6, v228\n"
    "s_cbranch_vccnz .Lh2\n"
    "s_branch .Lh2b\n"
    ".Lh_re:\n"
    "s_sub_u32 s51, s51, 1\n"
    "s_cmp_le_i32 s51, 0\n"
    "s_cbranch_scc1 .Lh_ok\n"
    "s_sleep 2\n"
    "v_mov_b64 v[244:245], v[240:241]\n"
    "v_mov_b64 v[246:247], 0\n"
    "v_mov_b64 v[242:243], 0\n"
    "s_mov_b32 exec_hi, 15\n"
    "global_load_dwordx4 v[244:247], v236, s[40:41] sc1\n"
    "s_mov_b64 exec, -1\n"
    "global_load_dwordx4 v[8:11], v235, s[64:65] offset:-3072 sc1\n"
    "global_load_dwordx4 v[32:35], v235, s[66:67] offset:-3072 sc1\n"
    "global_load_dwordx4 v[12:15], v235, s[64:65] offset:-2048 sc1\n"
    "global_load_dwordx4 v[36:39], v235, s[66:67] offset:-2048 sc1\n"
    "global_load_dwordx4 v[16:19], v235, s[64:65] offset:-1024 sc1\n"
    "global_load_dwordx4 v[40:43], v235, s[66:67] offset:-1024 sc1\n"
    "global_load_dwordx4 v[20:23], v235, s[64:65] offset:0 sc1\n"
    "global_load_dwordx4 v[44:47], v235, s[66:67] offset:0 sc1\n"
    "global_load_dwordx4 v[24:27], v235, s[64:65] offset:1024 sc1\n"
    "global_load_dwordx4 v[48:51], v235, s[66:67] offset:1024 sc1\n"
    "global_load_dwordx4 v[28:31], v235, s[64:65] offset:2048 sc1\n"
    "global_load_dwordx4 v[52:55], v235, s[66:67] offset:2048 sc1\n"
    "global_load_dwordx4 v[56:59], v235, s[68:69] offset:-3072 sc1\n"
    "global_load_dwordx4 v[80:83], v235, s[70:71] offset:-3072 sc1\n"
    "global_load_dwordx4 v[60:63], v235, s[68:69] offset:-2048 sc1\n"
    "global_load_dwordx4 v[84:87], v235, s[70:71] offset:-2048 sc1\n"
    "global_load_dwordx4 v[64:67], v235, s[68:69] offset:-1024 sc1\n"
    "global_load_dwordx4 v[88:91], v235, s[70:71] offset:-1024 sc1\n"
    "global_load_dwordx4 v[68:71], v235, s[68:69] offset:0 sc1\n"
    "global_load_dwordx4 v[92:95], v235, s[70:71] offset:0 sc1\n"
    "global_load_dwordx4 v[72:75], v235, s[68:69] offset:1024 sc1\n"
    "global_load_dwordx4 v[96:99], v235, s[70:71] offset:1024 sc1\n"
    "global_load_dwordx4 v[76:79], v235, s[68:69] offset:2048 sc1\n"
    "global_load_dwordx4 v[100:103], v235, s[70:71] offset:2048 sc1\n"
    "global_load_dwordx4 v[104:107], v235, s[72:73] offset:-3072 sc1\n"
    "global_load_dwordx4 v[128:131], v235, s[74:75] offset:-3072 sc1\n"
    "global_load_dwordx4 v[108:111], v235, s[72:73] offset:-2048 sc1\n"
    "global_load_dwordx4 v[132:135], v235, s[74:75] offset:-2048 sc1\n"
    "global_load_dwordx4 v[112:115], v235, s[72:73] offset:-1024 sc1\n"
    "global_load_dwordx4 v[136:139], v235, s[74:75] offset:-1024 sc1\n"
    "global_load_dwordx4 v[116:119], v235, s[72:73] offset:0 sc1\n"
    "global_load_dwordx4 v[140:143], v235, s[74:75] offset:0 sc1\n"
    "global_load_dwordx4 v[120:123], v235, s[72:73] offset:1024 sc1\n"
    "global_load_dwordx4 v[144:147], v235, s[74:75] offset:1024 sc1\n"
    "global_load_dwordx4 v[124:127], v235, s[72:73] offset:2048 sc1\n"
    "global_load_dwordx4 v[148:151], v235, s[74:75] offset:2048 sc1\n"
    "s_waitcnt vmcnt(0)\n"
    "v_add3_u32 v242, v242, v8, v11\n"
    "v_sub_u32 v8, v8, v240\n"
    "v_add3_u32 v242, v242, v32, v35\n"
    "v_sub_u32 v32, v32, v240\n"
    "v_add3_u32 v242, v242, v12, v15\n"
    "v_sub_u32 v12, v12, v240\n"
    "v_add3_u32 v242, v242, v36, v39\n"
    "v_sub_u32 v36, v36, v240\n"
    "v_add3_u32 v242, v242, v16, v19\n"
    "v_sub_u32 v16, v16, v240\n"
    "v_add3_u32 v242, v242, v40, v43\n"
    "v_sub_u32 v40, v40, v240\n"
    "v_add3_u32 v242, v242, v20, v23\n"
    "v_sub_u32 v20, v20, v240\n"
    "v_add3_u32 v242, v242, v44, v47\n"
    "v_sub_u32 v44, v44, v240\n"
    "v_add3_u32 v242, v242, v24, v27\n"
    "v_sub_u32 v24, v24, v240\n"
    "v_add3_u32 v242, v242, v48, v51\n"
    "v_sub_u32 v48, v48, v240\n"
    "v_add3_u32 v242, v242, v28, v31\n"
    "v_sub_u32 v28, v28, v240\n"
    "v_add3_u32 v242, v242, v52, v55\n"
    "v_sub_u32 v52, v52, v240\n"
    "v_add3_u32 v242, v242, v56, v59\n"
    "v_sub_u32 v56, v56, v240\n"
    "v_add3_u32 v242, v242, v80, v83\n"
    "v_sub_u32 v80, v80, v240\n"
    "v_add3_u32 v242, v242, v60, v63\n"
    "v_sub_u32 v60, v60, v240\n"
    "v_add3_u32 v242, v242, v84, v87\n"
    "v_sub_u32 v84, v84, v240\n"
    "v_add3_u32 v242, v242, v64, v67\n"
    "v_sub_u32 v64, v64, v240\n"
    "v_add3_u32 v242, v242, v88, v91\n"
    "v_sub_u32 v88, v88, v240\n"
    "v_add3_u32 v242, v242, v68, v71\n"
    "v_sub_u32 v68, v68, v240\n"
    "v_add3_u32 v242, v242, v92, v95\n"
    "v_sub_u32 v92, v92, v240\n"
    "v_add3_u32 v242, v242, v72, v75\n"
    "v_sub_u32 v72, v72, v240\n"
    "v_add3_u32 v242, v242, v96, v99\n"
    "v_sub_u32 v96, v96, v240\n"
    "v_add3_u32 v242, v242, v76, v79\n"
    "v_sub_u32 v76, v76, v240\n"
    "v_add3_u32 v242, v242, v100, v103\n"
    "v_sub_u32 v100, v100, v240\n"
    "v_add3_u32 v242, v242, v104, v107\n"
    "v_sub_u32 v104, v104, v240\n"
    "v_add3_u32 v242, v242, v128, v131\n"
    "v_sub_u32 v128, v128, v240\n"
    "v_add3_u32 v242, v242, v108, v111\n"
    "v_sub_u32 v108, v108, v240\n"
    "v_add3_u32 v242, v242, v132, v135\n"
    "v_sub_u32 v132, v132, v240\n"
    "v_add3_u32 v242, v242, v112, v115\n"
    "v_sub_u32 v112, v112, v240\n"
    "v_add3_u32 v242, v242, v136, v139\n"
    "v_sub_u32 v136, v136, v240\n"
    "v_add3_u32 v242, v242, v116, v119\n"
    "v_sub_u32 v116, v116, v240\n"
    "v_add3_u32 v242, v242, v140, v143\n"
    "v_sub_u32 v140, v140, v240\n"
    "v_add3_u32 v242, v242, v120, v123\n"
    "v_sub_u32 v120, v120, v240\n"
    "v_add3_u32 v242, v242, v144, v147\n"
    "v_sub_u32 v144, v144, v240\n"
    "v_add3_u32 v242, v242, v124, v127\n"
    "v_sub_u32 v124, v124, v240\n"
    "v_add3_u32 v242, v242, v148, v151\n"
    "v_sub_u32 v148, v148, v240\n"
    "s_branch .Lh_ce\n"
    ".Lh_pl:\n"
    "s_sub_u32 s51, s51, 1\n"
    "s_cmp_le_i32 s51, 0\n"
    "s_cbranch_scc1 .Lh_ld\n"
    "s_sleep 4\n"
    "s_mov_b32 exec_hi, 15\n"
    "global_load_dwordx2 v[244:245], v236, s[40:41] sc1\n"
    "s_mov_b64 exec, -1\n"
    "s_waitcnt vmcnt(0)\n"
    "v_cmp_ne_u64 vcc, v[240:241], v[244:245]\n"
    "s_cbranch_vccnz .Lh_pl\n"
    "s_branch .Lh_ld\n"
    ".Lh_ld:\n"
    "v_mov_b64 v[244:245], v[240:241]\n"
    "v_mov_b64 v[246:247], 0\n"
    "v_mov_b64 v[242:243], 0\n"
    "s_mov_b32 exec_hi, 15\n"
    "global_load_dwordx4 v[244:247], v236, s[40:41] sc1\n"
    "s_mov_b64 exec, -1\n"
    "global_load_dwordx4 v[8:11], v235, s[64:65] offset:-3072 sc1\n"
    "global_load_dwordx4 v[32:35], v235, s[66:67] offset:-3072 sc1\n"
    "global_load_dwordx4 v[12:15], v235, s[64:65] offset:-2048 sc1\n"
    "global_load_dwordx4 v[36:39], v235, s[66:67] offset:-2048 sc1\n"
    "global_load_dwordx4 v[16:19], v235, s[64:65] offset:-1024 sc1\n"
    "global_load_dwordx4 v[40:43], v235, s[66:67] offset:-1024 sc1\n"
    "global_load_dwordx4 v[20:23], v235, s[64:65] offset:0 sc1\n"
    "global_load_dwordx4 v[44:47], v235, s[66:67] offset:0 sc1\n"
    "global_load_dwordx4 v[24:27], v235, s[64:65] offset:1024 sc1\n"
    "global_load_dwordx4 v[48:51], v235, s[66:67] offset:1024 sc1\n"
    "global_load_dwordx4 v[28:31], v235, s[64:65] offset:2048 sc1\n"
    "global_load_dwordx4 v[52:55], v235, s[66:67] offset:2048 sc1\n"
    "global_load_dwordx4 v[56:59], v235, s[68:69] offset:-3072 sc1\n"
    "global_load_dwordx4 v[80:83], v235, s[70:71] offset:-3072 sc1\n"
    "global_load_dwordx4 v[60:63], v235, s[68:69] offset:-2048 sc1\n"
    "global_load_dwordx4 v[84:87], v235, s[70:71] offset:-2048 sc1\n"
    "global_load_dwordx4 v[64:67], v235, s[68:69] offset:-1024 sc1\n"
    "global_load_dwordx4 v[88:91], v235, s[70:71] offset:-1024 sc1\n"
    "global_load_dwordx4 v[68:71], v235, s[68:69] offset:0 sc1\n"
    "global_load_dwordx4 v[92:95], v235, s[70:71] offset:0 sc1\n"
    "global_load_dwordx4 v[72:75], v235, s[68:69] offset:1024 sc1\n"
    "global_load_dwordx4 v[96:99], v235, s[70:71] offset:1024 sc1\n"
    "global_load_dwordx4 v[76:79], v235, s[68:69] offset:2048 sc1\n"
    "global_load_dwordx4 v[100:103], v235, s[70:71] offset:2048 sc1\n"
    "global_load_dwordx4 v[104:107], v235, s[72:73] offset:-3072 sc1\n"
    "global_load_dwordx4 v[128:131], v235, s[74:75] offset:-3072 sc1\n"
    "global_load_dwordx4 v[108:111], v235, s[72:73] offset:-2048 sc1\n"
    "global_load_dwordx4 v[132:135], v235, s[74:75] offset:-2048 sc1\n"
    "global_load_dwordx4 v[112:115], v235, s[72:73] offset:-1024 sc1\n"
    "global_load_dwordx4 v[136:139], v235, s[74:75] offset:-1024 sc1\n"
    "global_load_dwordx4 v[116:119], v235, s[72:73] offset:0 sc1\n"
    "global_load_dwordx4 v[140:143], v235, s[74:75] offset:0 sc1\n"
    "global_load_dwordx4 v[120:123], v235, s[72:73] offset:1024 sc1\n"
    "global_load_dwordx4 v[144:147], v235, s[74:75] offset:1024 sc1\n"
    "global_load_dwordx4 v[124:127], v235, s[72:73] offset:2048 sc1\n"
    "global_load_dwordx4 v[148:151], v235, s[74:75] offset:2048 sc1\n"
    "s_branch .Lh_rs\n"
    ".Lh3:\n"
    "s_sub_u32 s52, s52, 1\n"
    "s_cmp_lt_i32 s52, 0\n"
    "s_cbranch_scc1 .Lh3b\n"
    "ds_read_b32 v228, v224 offset:128\n"
    "s_waitcnt lgkmcnt(0)\n"
    "v_cmp_gt_u32 vcc, 1, v228\n"
    "s_cbranch_vccnz .Lh3\n"
    "s_branch .Lh3b\n"
    ".Lh4:\n"
    "v_mfma_f32_16x16x32_bf16 v[216:219], v[104:107], v[152:155], 0\n"
    "buffer_store_dwordx4 v[200:203], v4, s[80:83], 0 offen offset:0 sc1\n"
    "v_mfma_f32_16x16x32_bf16 v[220:223], v[128:131], v[152:155], 0\n"
    "v_mul_f32 v208, v208, v252\n"
    "v_mul_f32 v209, v209, v252\n"
    "v_mfma_f32_16x16x32_bf16 v[216:219], v[108:111], v[156:159], v[216:219]\n"
    "v_mul_f32 v210, v210, v252\n"
    "v_mul_f32 v211, v211, v252\n"
    "v_mfma_f32_16x16x32_bf16 v[220:223], v[132:135], v[156:159], v[220:223]\n"
    "v_mul_f32 v212, v212, v252\n"
    "v_mul_f32 v213, v213, v252\n"
    "v_mfma_f32_16x16x32_bf16 v[216:219], v[112:115], v[160:163], v[216:219]\n"
    "v_mul_f32 v214, v214, v252\n"
    "v_mul_f32 v215, v215, v252\n"
    "v_mfma_f32_16x16x32_bf16 v[220:223], v[136:139], v[160:163], v[220:223]\n"
    "ds_read_b32 v228, v224 offset:144\n"
    "v_cvt_pk_bf16_f32 v208, v208, v209\n"
    "v_mfma_f32_16x16x32_bf16 v[216:219], v[116:119], v[164:167], v[216:219]\n"
    "v_cvt_pk_bf16_f32 v209, v210, v211\n"
    "v_cvt_pk_bf16_f32 v210, v212, v213\n"
    "v_mfma_f32_16x16x32_bf16 v[220:223], v[140:143], v[164:167], v[220:223]\n"
    "v_cvt_pk_bf16_f32 v211, v214, v215\n"
    "v_mfma_f32_16x16x32_bf16 v[216:219], v[120:123], v[168:171], v[216:219]\n"
    "buffer_store_dwordx4 v[208:211], v4, s[80:83], 0 offen offset:64 sc1\n"
    "v_mfma_f32_16x16x32_bf16 v[220:223], v[144:147], v[168:171], v[220:223]\n"
    "v_mfma_f32_16x16x32_bf16 v[216:219], v[124:127], v[172:175], v[216:219]\n"
    "v_mfma_f32_16x16x32_bf16 v[220:223], v[148:151], v[172:175], v[220:223]\n"
    "s_waitcnt lgkmcnt(0)\n"
    "v_cmp_gt_u32 vcc, 1, v228\n"
    "s_cbranch_vccz .Lh4g\n"
    ".Lh4r:\n"
    "s_sub_u32 s52, s52, 1\n"
    "s_cmp_lt_i32 s52, 0\n"
    "s_cbranch_scc1 .Lh4g\n"
    "ds_read_b32 v228, v224 offset:144\n"
    "s_waitcnt lgkmcnt(0)\n"
    "v_cmp_gt_u32 vcc, 1, v228\n"
    "s_cbranch_vccnz .Lh4r\n"
    "s_branch .Lh4g\n"
    ".Lh4g:\n"
    "ds_read_b64_tr_b16 v[176:177], v2 offset:24576\n"
    "ds_read_b64_tr_b16 v[178:179], v3 offset:24576\n"
    "ds_read_b64_tr_b16 v[180:181], v2 offset:25600\n"
    "ds_read_b64_tr_b16 v[182:183], v3 offset:25600\n"
    "ds_read_b64_tr_b16 v[184:185], v2 offset:26624\n"
    "ds_read_b64_tr_b16 v[186:187], v3 offset:26624\n"
    "ds_read_b64_tr_b16 v[188:189], v2 offset:27648\n"
    "ds_read_b64_tr_b16 v[190:191], v3 offset:27648\n"
    "ds_read_b64_tr_b16 v[192:193], v2 offset:28672\n"
    "ds_read_b64_tr_b16 v[194:195], v3 offset:28672\n"
    "ds_read_b64_tr_b16 v[196:197], v2 offset:29696\n"
    "ds_read_b64_tr_b16 v[198:199], v3 offset:29696\n"
    "ds_read_b32 v253, v1 offset:64\n"
    "v_mov_b32 v227, 2\n"
    "ds_write_b32 v226, v227 offset:32\n"
    "s_branch .Lh4j\n"
    ".Lh5:\n"
    "v_mfma_f32_16x16x32_bf16 v[216:219], v[104:107], v[176:179], 0\n"
    "buffer_store_dwordx4 v[200:203], v5, s[80:83], 0 offen offset:0 sc1\n"
    "v_mfma_f32_16x16x32_bf16 v[220:223], v[128:131], v[176:179], 0\n"
    "v_mul_f32 v208, v208, v253\n"
    "v_mul_f32 v209, v209, v253\n"
    "v_mfma_f32_16x16x32_bf16 v[216:219], v[108:111], v[180:183], v[216:219]\n"
    "v_mul_f32 v210, v210, v253\n"
    "v_mul_f32 v211, v211, v253\n"
    "v_mfma_f32_16x16x32_bf16 v[220:223], v[132:135], v[180:183], v[220:223]\n"
    "v_mul_f32 v212, v212, v253\n"
    "v_mul_f32 v213, v213, v253\n"
    "v_mfma_f32_16x16x32_bf16 v[216:219], v[112:115], v[184:187], v[216:219]\n"
    "v_mul_f32 v214, v214, v253\n"
    "v_mul_f32 v215, v215, v253\n"
    "v_mfma_f32_16x16x32_bf16 v[220:223], v[136:139], v[184:187], v[220:223]\n"
    "ds_read_b32 v228, v224 offset:160\n"
    "v_cvt_pk_bf16_f32 v208, v208, v209\n"
    "v_mfma_f32_16x16x32_bf16 v[216:219], v[116:119], v[188:191], v[216:219]\n"
    "v_cvt_pk_bf16_f32 v209, v210, v211\n"
    "v_cvt_pk_bf16_f32 v210, v212, v213\n"
    "v_mfma_f32_16x16x32_bf16 v[220:223], v[140:143], v[188:191], v[220:223]\n"
    "v_cvt_pk_bf16_f32 v211, v214, v215\n"
    "v_mfma_f32_16x16x32_bf16 v[216:219], v[120:123], v[192:195], v[216:219]\n"
    "buffer_store_dwordx4 v[208:211], v5, s[80:83], 0 offen offset:64 sc1\n"
    "v_mfma_f32_16x16x32_bf16 v[220:223], v[144:147], v[192:195], v[220:223]\n"
    "v_mfma_f32_16x16x32_bf16 v[216:219], v[124:127], v[196:199], v[216:219]\n"
    "v_mfma_f32_16x16x32_bf16 v[220:223], v[148:151], v[196:199], v[220:223]\n"
    "s_waitcnt lgkmcnt(0)\n"
    "v_cmp_gt_u32 vcc, 1, v228\n"
    "s_cbranch_vccz .Lh5g\n"
    ".Lh5r:\n"
    "s_sub_u32 s52, s52, 1\n"
    "s_cmp_lt_i32 s52, 0\n"
    "s_cbranch_scc1 .Lh5g\n"
    "ds_read_b32 v228, v224 offset:160\n"
    "s_waitcnt lgkmcnt(0)\n"
    "v_cmp_gt_u32 vcc, 1, v228\n"
    "s_cbranch_vccnz .Lh5r\n"
    "s_branch .Lh5g\n"
    ".Lh5g:\n"
    "ds_read_b64_tr_b16 v[152:153], v2 offset:30720\n"
    "ds_read_b64_tr_b16 v[154:155], v3 offset:30720\n"
    "ds_read_b64_tr_b16 v[156:157], v2 offset:31744\n"
    "ds_read_b64_tr_b16 v[158:159], v3 offset:31744\n"
    "ds_read_b64_tr_b16 v[160:161], v2 offset:32768\n"
    "ds_read_b64_tr_b16 v[162:163], v3 offset:32768\n"
    "ds_read_b64_tr_b16 v[164:165], v2 offset:33792\n"
    "ds_read_b64_tr_b16 v[166:167], v3 offset:33792\n"
    "ds_read_b64_tr_b16 v[168:169], v2 offset:34816\n"
    "ds_read_b64_tr_b16 v[170:171], v3 offset:34816\n"
    "ds_read_b64_tr_b16 v[172:173], v2 offset:35840\n"
    "ds_read_b64_tr_b16 v[174:175], v3 offset:35840\n"
    "ds_read_b32 v252, v1 offset:128\n"
    "v_mov_b32 v227, 3\n"
    "ds_write_b32 v226, v227 offset:32\n"
    "s_branch .Lh5j\n"
    ".Lh6:\n"
    "v_mfma_f32_16x16x32_bf16 v[216:219], v[104:107], v[152:155], 0\n"
    "buffer_store_dwordx4 v[200:203], v4, s[80:83], 0 offen offset:0 sc1\n"
    "v_mfma_f32_16x16x32_bf16 v[220:223], v[128:131], v[152:155], 0\n"
    "v_mul_f32 v208, v208, v252\n"
    "v_mul_f32 v209, v209, v252\n"
    "v_mfma_f32_16x16x32_bf16 v[216:219], v[108:111], v[156:159], v[216:219]\n"
    "v_mul_f32 v210, v210, v252\n"
    "v_mul_f32 v211, v211, v252\n"
    "v_mfma_f32_16x16x32_bf16 v[220:223], v[132:135], v[156:159], v[220:223]\n"
    "v_mul_f32 v212, v212, v252\n"
    "v_mul_f32 v213, v213, v252\n"
    "v_mfma_f32_16x16x32_bf16 v[216:219], v[112:115], v[160:163], v[216:219]\n"
    "v_mul_f32 v214, v214, v252\n"
    "v_mul_f32 v215, v215, v252\n"
    "v_mfma_f32_16x16x32_bf16 v[220:223], v[136:139], v[160:163], v[220:223]\n"
    "ds_read_b32 v228, v224 offset:176\n"
    "v_cvt_pk_bf16_f32 v208, v208, v209\n"
    "v_mfma_f32_16x16x32_bf16 v[216:219], v[116:119], v[164:167], v[216:219]\n"
    "v_cvt_pk_bf16_f32 v209, v210, v211\n"
    "v_cvt_pk_bf16_f32 v210, v212, v213\n"
    "v_mfma_f32_16x16x32_bf16 v[220:223], v[140:143], v[164:167], v[220:223]\n"
    "v_cvt_pk_bf16_f32 v211, v214, v215\n"
    "v_mfma_f32_16x16x32_bf16 v[216:219], v[120:123], v[168:171], v[216:219]\n"
    "buffer_store_dwordx4 v[208:211], v4, s[80:83], 0 offen offset:64 sc1\n"
    "v_mfma_f32_16x16x32_bf16 v[220:223], v[144:147], v[168:171], v[220:223]\n"
    "v_mfma_f32_16x16x32_bf16 v[216:219], v[124:127], v[172:175], v[216:219]\n"
    "v_mfma_f32_16x16x32_bf16 v[220:223], v[148:151], v[172:175], v[220:223]\n"
    "s_waitcnt lgkmcnt(0)\n"
    "v_cmp_gt_u32 vcc, 1, v228\n"
    "s_cbranch_vccz .Lh6g\n"
    ".Lh6r:\n"
    "s_sub_u32 s52, s52, 1\n"
    "s_cmp_lt_i32 s52, 0\n"
    "s_cbranch_scc1 .Lh6g\n"
    "ds_read_b32 v228, v224 offset:176\n"
    "s_waitcnt lgkmcnt(0)\n"
    "v_cmp_gt_u32 vcc, 1, v228\n"
    "s_cbranch_vccnz .Lh6r\n"
    "s_branch .Lh6g\n"
    ".Lh6g:\n"
    "ds_read_b64_tr_b16 v[176:177], v2 offset:18432\n"
    "ds_read_b64_tr_b16 v[178:179], v3 offset:18432\n"
    "ds_read_b64_tr_b16 v[180:181], v2 offset:19456\n"
    "ds_read_b64_tr_b16 v[182:183], v3 offset:19456\n"
    "ds_read_b64_tr_b16 v[184:185], v2 offset:20480\n"
    "ds_read_b64_tr_b16 v[186:187], v3 offset:20480\n"
    "ds_read_b64_tr_b16 v[188:189], v2 offset:21504\n"
    "ds_read_b64_tr_b16 v[190:191], v3 offset:21504\n"
    "ds_read_b64_tr_b16 v[192:193], v2 offset:22528\n"
    "ds_read_b64_tr_b16 v[194:195], v3 offset:22528\n"
    "ds_read_b64_tr_b16 v[196:197], v2 offset:23552\n"
    "ds_read_b64_tr_b16 v[198:199], v3 offset:23552\n"
    "ds_read_b32 v253, v1 offset:0\n"
    "v_mov_b32 v227, 4\n"
    "ds_write_b32 v226, v227 offset:32\n"
    "s_branch .Lh6j\n"
    ".Lh7:\n"
    "v_mfma_f32_16x16x32_bf16 v[216:219], v[104:107], v[176:179], 0\n"
    "buffer_store_dwordx4 v[200:203], v5, s[80:83], 0 offen offset:0 sc1\n"
    "v_mfma_f32_16x16x32_bf16 v[220:223], v[128:131], v[176:179], 0\n"
    "v_mul_f32 v208, v208, v253\n"
    "v_mul_f32 v209, v209, v253\n"
    "v_mfma_f32_16x16x32_bf16 v[216:219], v[108:111], v[180:183], v[216:219]\n"
    "v_mul_f32 v210, v210, v253\n"
    "v_mul_f32 v211, v211, v253\n"
    "v_mfma_f32_16x16x32_bf16 v[220:223], v[132:135], v[180:183], v[220:223]\n"
    "v_mul_f32 v212, v212, v253\n"
    "v_mul_f32 v213, v213, v253\n"
    "v_mfma_f32_16x16x32_bf16 v[216:219], v[112:115], v[184:187], v[216:219]\n"
    "v_mul_f32 v214, v214, v253\n"
    "v_mul_f32 v215, v215, v253\n"
    "v_mfma_f32_16x16x32_bf16 v[220:223], v[136:139], v[184:187], v[220:223]\n"
    "ds_read_b32 v228, v224 offset:128\n"
    "v_cvt_pk_bf16_f32 v208, v208, v209\n"
    "v_mfma_f32_16x16x32_bf16 v[216:219], v[116:119], v[188:191], v[216:219]\n"
    "v_cvt_pk_bf16_f32 v209, v210, v211\n"
    "v_cvt_pk_bf16_f32 v210, v212, v213\n"
    "v_mfma_f32_16x16x32_bf16 v[220:223], v[140:143], v[188:191], v[220:223]\n"
    "v_cvt_pk_bf16_f32 v211, v214, v215\n"
    "v_mfma_f32_16x16x32_bf16 v[216:219], v[120:123], v[192:195], v[216:219]\n"
    "buffer_store_dwordx4 v[208:211], v5, s[80:83], 0 offen offset:64 sc1\n"
    "v_mfma_f32_16x16x32_bf16 v[220:223], v[144:147], v[192:195], v[220:223]\n"
    "v_mfma_f32_16x16x32_bf16 v[216:219], v[124:127], v[196:199], v[216:219]\n"
    "v_mfma_f32_16x16x32_bf16 v[220:223], v[148:151], v[196:199], v[220:223]\n"
    "s_waitcnt lgkmcnt(0)\n"
    "v_cmp_gt_u32 vcc, 2, v228\n"
    "s_cbranch_vccz .Lh7g\n"
    ".Lh7r:\n"
    "s_sub_u32 s52, s52, 1\n"
    "s_cmp_lt_i32 s52, 0\n"
    "s_cbranch_scc1 .Lh7g\n"
    "ds_read_b32 v228, v224 offset:128\n"
    "s_waitcnt lgkmcnt(0)\n"
    "v_cmp_gt_u32 vcc, 2, v228\n"
    "s_cbranch_vccnz .Lh7r\n"
    "s_branch .Lh7g\n"
    ".Lh7g:\n"
    "ds_read_b64_tr_b16 v[152:153], v2 offset:24576\n"
    "ds_read_b64_tr_b16 v[154:155], v3 offset:24576\n"
    "ds_read_b64_tr_b16 v[156:157], v2 offset:25600\n"
    "ds_read_b64_tr_b16 v[158:159], v3 offset:25600\n"
    "ds_read_b64_tr_b16 v[160:161], v2 offset:26624\n"
    "ds_read_b64_tr_b16 v[162:163], v3 offset:26624\n"
    "ds_read_b64_tr_b16 v[164:165], v2 offset:27648\n"
    "ds_read_b64_tr_b16 v[166:167], v3 offset:27648\n"
    "ds_read_b64_tr_b16 v[168:169], v2 offset:28672\n"
    "ds_read_b64_tr_b16 v[170:171], v3 offset:28672\n"
    "ds_read_b64_tr_b16 v[172:173], v2 offset:29696\n"
    "ds_read_b64_tr_b16 v[174:175], v3 offset:29696\n"
    "ds_read_b32 v252, v1 offset:64\n"
    "v_mov_b32 v227, 5\n"
    "ds_write_b32 v226, v227 offset:32\n"
    "s_branch .Lh7j\n"
    ".Lh8:\n"
    "v_mfma_f32_16x16x32_bf16 v[216:219], v[104:107], v[152:155], 0\n"
    "buffer_store_dwordx4 v[200:203], v4, s[80:83], 0 offen offset:0 sc1\n"
    "v_mfma_f32_16x16x32_bf16 v[220:223], v[128:131], v[152:155], 0\n"
    "v_mul_f32 v208, v208, v252\n"
    "v_mul_f32 v209, v209, v252\n"
    "v_mfma_f32_16x16x32_bf16 v[216:219], v[108:111], v[156:159], v[216:219]\n"
    "v_mul_f32 v210, v210, v252\n"
    "v_mul_f32 v211, v211, v252\n"
    "v_mfma_f32_16x16x32_bf16 v[220:223], v[132:135], v[156:159], v[220:223]\n"
    "v_mul_f32 v212, v212, v252\n"
    "v_mul_f32 v213, v213, v252\n"
    "v_mfma_f32_16x16x32_bf16 v[216:219], v[112:115], v[160:163], v[216:219]\n"
    "v_mul_f32 v214, v214, v252\n"
    "v_mul_f32 v215, v215, v252\n"
    "v_mfma_f32_16x16x32_bf16 v[220:223], v[136:139], v[160:163], v[220:223]\n"
    "ds_read_b32 v228, v224 offset:144\n"
    "v_cvt_pk_bf16_f32 v208, v208, v209\n"
    "v_mfma_f32_16x16x32_bf16 v[216:219], v[116:119], v[164:167], v[216:219]\n"
    "v_cvt_pk_bf16_f32 v209, v210, v211\n"
    "v_cvt_pk_bf16_f32 v210, v212, v213\n"
    "v_mfma_f32_16x16x32_bf16 v[220:223], v[140:143], v[164:167], v[220:223]\n"
    "v_cvt_pk_bf16_f32 v211, v214, v215\n"
    "v_mfma_f32_16x16x32_bf16 v[216:219], v[120:123], v[168:171], v[216:219]\n"
    "buffer_store_dwordx4 v[208:211], v4, s[80:83], 0 offen offset:64 sc1\n"
    "v_mfma_f32_16x16x32_bf16 v[220:223], v[144:147], v[168:171], v[220:223]\n"
    "v_mfma_f32_16x16x32_bf16 v[216:219], v[124:127], v[172:175], v[216:219]\n"
    "v_mfma_f32_16x16x32_bf16 v[220:223], v[148:151], v[172:175], v[220:223]\n"
    "s_waitcnt lgkmcnt(0)\n"
    "v_cmp_gt_u32 vcc, 2, v228\n"
    "s_cbranch_vccz .Lh8g\n"
    ".Lh8r:\n"
    "s_sub_u32 s52, s52, 1\n"
    "s_cmp_lt_i32 s52, 0\n"
    "s_cbranch_scc1 .Lh8g\n"
    "ds_read_b32 v228, v224 offset:144\n"
    "s_waitcnt lgkmcnt(0)\n"
    "v_cmp_gt_u32 vcc, 2, v228\n"
    "s_cbranch_vccnz .Lh8r\n"
    "s_branch .Lh8g\n"
    ".Lh8g:\n"
    "ds_read_b64_tr_b16 v[176:177], v2 offset:30720\n"
    "ds_read_b64_tr_b16 v[178:179], v3 offset:30720\n"
    "ds_read_b64_tr_b16 v[180:181], v2 offset:31744\n"
    "ds_read_b64_tr_b16 v[182:183], v3 offset:31744\n"
    "ds_read_b64_tr_b16 v[184:185], v2 offset:32768\n"
    "ds_read_b64_tr_b16 v[186:187], v3 offset:32768\n"
    "ds_read_b64_tr_b16 v[188:189], v2 offset:33792\n"
    "ds_read_b64_tr_b16 v[190:191], v3 offset:33792\n"
    "ds_read_b64_tr_b16 v[192:193], v2 offset:34816\n"
    "ds_read_b64_tr_b16 v[194:195], v3 offset:34816\n"
    "ds_read_b64_tr_b16 v[196:197], v2 offset:35840\n"
    "ds_read_b64_tr_b16 v[198:199], v3 offset:35840\n"
    "ds_read_b32 v253, v1 offset:128\n"
    "v_mov_b32 v227, 6\n"
    "ds_write_b32 v226, v227 offset:32\n"
    "s_branch .Lh8j\n"
    ".Lh9:\n"
    "v_mfma_f32_16x16x32_bf16 v[216:219], v[104:107], v[176:179], 0\n"
    "buffer_store_dwordx4 v[200:203], v5, s[80:83], 0 offen offset:0 sc1\n"
    "v_mfma_f32_16x16x32_bf16 v[220:223], v[128:131], v[176:179], 0\n"
    "v_mul_f32 v208, v208, v253\n"
    "v_mul_f32 v209, v209, v253\n"
    "v_mfma_f32_16x16x32_bf16 v[216:219], v[108:111], v[180:183], v[216:219]\n"
    "v_mul_f32 v210, v210, v253\n"
    "v_mul_f32 v211, v211, v253\n"
    "v_mfma_f32_16x16x32_bf16 v[220:223], v[132:135], v[180:183], v[220:223]\n"
    "v_mul_f32 v212, v212, v253\n"
    "v_mul_f32 v213, v213, v253\n"
    "v_mfma_f32_16x16x32_bf16 v[216:219], v[112:115], v[184:187], v[216:219]\n"
    "v_mul_f32 v214, v214, v253\n"
    "v_mul_f32 v215, v215, v253\n"
    "v_mfma_f32_16x16x32_bf16 v[220:223], v[136:139], v[184:187], v[220:223]\n"
    "ds_read_b32 v228, v224 offset:160\n"
    "v_cvt_pk_bf16_f32 v208, v208, v209\n"
    "v_mfma_f32_16x16x32_bf16 v[216:219], v[116:119], v[188:191], v[216:219]\n"
    "v_cvt_pk_bf16_f32 v209, v210, v211\n"
    "v_cvt_pk_bf16_f32 v210, v212, v213\n"
    "v_mfma_f32_16x16x32_bf16 v[220:223], v[140:143], v[188:191], v[220:223]\n"
    "v_cvt_pk_bf16_f32 v211, v214, v215\n"
    "v_mfma_f32_16x16x32_bf16 v[216:219], v[120:123], v[192:195], v[216:219]\n"
    "buffer_store_dwordx4 v[208:211], v5, s[80:83], 0 offen offset:64 sc1\n"
    "v_mfma_f32_16x16x32_bf16 v[220:223], v[144:147], v[192:195], v[220:223]\n"
    "v_mfma_f32_16x16x32_bf16 v[216:219], v[124:127], v[196:199], v[216:219]\n"
    "v_mfma_f32_16x16x32_bf16 v[220:223], v[148:151], v[196:199], v[220:223]\n"
    "s_waitcnt lgkmcnt(0)\n"
    "v_cmp_gt_u32 vcc, 2, v228\n"
    "s_cbranch_vccz .Lh9g\n"
    ".Lh9r:\n"
    "s_sub_u32 s52, s52, 1\n"
    "s_cmp_lt_i32 s52, 0\n"
    "s_cbranch_scc1 .Lh9g\n"
    "ds_read_b32 v228, v224 offset:160\n"
    "s_waitcnt lgkmcnt(0)\n"
    "v_cmp_gt_u32 vcc, 2, v228\n"
    "s_cbranch_vccnz .Lh9r\n"
    "s_branch .Lh9g\n"
    ".Lh9g:\n"
    "ds_read_b64_tr_b16 v[152:153], v2 offset:18432\n"
    "ds_read_b64_tr_b16 v[154:155], v3 offset:18432\n"
    "ds_read_b64_tr_b16 v[156:157], v2 offset:19456\n"
    "ds_read_b64_tr_b16 v[158:159], v3 offset:19456\n"
    "ds_read_b64_tr_b16 v[160:161], v2 offset:20480\n"
    "ds_read_b64_tr_b16 v[162:163], v3 offset:20480\n"
    "ds_read_b64_tr_b16 v[164:165], v2 offset:21504\n"
    "ds_read_b64_tr_b16 v[166:167], v3 offset:21504\n"
    "ds_read_b64_tr_b16 v[168:169], v2 offset:22528\n"
    "ds_read_b64_tr_b16 v[170:171], v3 offset:22528\n"
    "ds_read_b64_tr_b16 v[172:173], v2 offset:23552\n"
    "ds_read_b64_tr_b16 v[174:175], v3 offset:23552\n"
    "ds_read_b32 v252, v1 offset:0\n"
    "v_mov_b32 v227, 7\n"
    "ds_write_b32 v226, v227 offset:32\n"
    "s_branch .Lh9j\n"
    ".Lh10:\n"
    "v_mfma_f32_16x16x32_bf16 v[216:219], v[104:107], v[152:155], 0\n"
    "buffer_store_dwordx4 v[200:203], v4, s[80:83], 0 offen offset:0 sc1\n"
    "v_mfma_f32_16x16x32_bf16 v[220:223], v[128:131], v[152:155], 0\n"
    "v_mul_f32 v208, v208, v252\n"
    "v_mul_f32 v209, v209, v252\n"
    "v_mfma_f32_16x16x32_bf16 v[216:219], v[108:111], v[156:159], v[216:219]\n"
    "v_mul_f32 v210, v210, v252\n"
    "v_mul_f32 v211, v211, v252\n"
    "v_mfma_f32_16x16x32_bf16 v[220:223], v[132:135], v[156:159], v[220:223]\n"
    "v_mul_f32 v212, v212, v252\n"
    "v_mul_f32 v213, v213, v252\n"
    "v_mfma_f32_16x16x32_bf16 v[216:219], v[112:115], v[160:163], v[216:219]\n"
    "v_mul_f32 v214, v214, v252\n"
    "v_mul_f32 v215, v215, v252\n"
    "v_mfma_f32_16x16x32_bf16 v[220:223], v[136:139], v[160:163], v[220:223]\n"
    "ds_read_b32 v228, v224 offset:176\n"
    "v_cvt_pk_bf16_f32 v208, v208, v209\n"
    "v_mfma_f32_16x16x32_bf16 v[216:219], v[116:119], v[164:167], v[216:219]\n"
    "v_cvt_pk_bf16_f32 v209, v210, v211\n"
    "v_cvt_pk_bf16_f32 v210, v212, v213\n"
    "v_mfma_f32_16x16x32_bf16 v[220:223], v[140:143], v[164:167], v[220:223]\n"
    "v_cvt_pk_bf16_f32 v211, v214, v215\n"
    "v_mfma_f32_16x16x32_bf16 v[216:219], v[120:123], v[168:171], v[216:219]\n"
    "buffer_store_dwordx4 v[208:211], v4, s[80:83], 0 offen offset:64 sc1\n"
    "v_mfma_f32_16x16x32_bf16 v[220:223], v[144:147], v[168:171], v[220:223]\n"
    "v_mfma_f32_16x16x32_bf16 v[216:219], v[124:127], v[172:175], v[216:219]\n"
    "v_mfma_f32_16x16x32_bf16 v[220:223], v[148:151], v[172:175], v[220:223]\n"
    "s_waitcnt lgkmcnt(0)\n"
    "v_cmp_gt_u32 vcc, 2, v228\n"
    "s_cbranch_vccz .Lh10g\n"
    ".Lh10r:\n"
    "s_sub_u32 s52, s52, 1\n"
    "s_cmp_lt_i32 s52, 0\n"
    "s_cbranch_scc1 .Lh10g\n"
    "ds_read_b32 v228, v224 offset:176\n"
    "s_waitcnt lgkmcnt(0)\n"
    "v_cmp_gt_u32 vcc, 2, v228\n"
    "s_cbranch_vccnz .Lh10r\n"
    "s_branch .Lh10g\n"
    ".Lh10g:\n"
    "ds_read_b64_tr_b16 v[176:177], v2 offset:24576\n"
    "ds_read_b64_tr_b16 v[178:179], v3 offset:24576\n"
    "ds_read_b64_tr_b16 v[180:181], v2 offset:25600\n"
    "ds_read_b64_tr_b16 v[182:183], v3 offset:25600\n"
    "ds_read_b64_tr_b16 v[184:185], v2 offset:26624\n"
    "ds_read_b64_tr_b16 v[186:187], v3 offset:26624\n"
    "ds_read_b64_tr_b16 v[188:189], v2 offset:27648\n"
    "ds_read_b64_tr_b16 v[190:191], v3 offset:27648\n"
    "ds_read_b64_tr_b16 v[192:193], v2 offset:28672\n"
    "ds_read_b64_tr_b16 v[194:195], v3 offset:28672\n"
    "ds_read_b64_tr_b16 v[196:197], v2 offset:29696\n"
    "ds_read_b64_tr_b16 v[198:199], v3 offset:29696\n"
    "ds_read_b32 v253, v1 offset:64\n"
    "s_branch .Lh10j\n"
    ".Lh11:\n"
    "v_mfma_f32_16x16x32_bf16 v[216:219], v[104:107], v[176:179], 0\n"
    "buffer_store_dwordx4 v[200:203], v5, s[80:83], 0 offen offset:0 sc1\n"
    "v_mfma_f32_16x16x32_bf16 v[220:223], v[128:131], v[176:179], 0\n"
    "v_mul_f32 v208, v208, v253\n"
    "v_mul_f32 v209, v209, v253\n"
    "v_mfma_f32_16x16x32_bf16 v[216:219], v[108:111], v[180:183], v[216:219]\n"
    "v_mul_f32 v210, v210, v253\n"
    "v_mul_f32 v211, v211, v253\n"
    "v_mfma_f32_16x16x32_bf16 v[220:223], v[132:135], v[180:183], v[220:223]\n"
    "v_mul_f32 v212, v212, v253\n"
    "v_mul_f32 v213, v213, v253\n"
    "v_mfma_f32_16x16x32_bf16 v[216:219], v[112:115], v[184:187], v[216:219]\n"
    "v_mul_f32 v214, v214, v253\n"
    "v_mul_f32 v215, v215, v253\n"
    "v_mfma_f32_16x16x32_bf16 v[220:223], v[136:139], v[184:187], v[220:223]\n"
    "ds_read_b32 v228, v224 offset:128\n"
    "v_cvt_pk_bf16_f32 v208, v208, v209\n"
    "v_mfma_f32_16x16x32_bf16 v[216:219], v[116:119], v[188:191], v[216:219]\n"
    "v_cvt_pk_bf16_f32 v209, v210, v211\n"
    "v_cvt_pk_bf16_f32 v210, v212, v213\n"
    "v_mfma_f32_16x16x32_bf16 v[220:223], v[140:143], v[188:191], v[220:223]\n"
    "v_cvt_pk_bf16_f32 v211, v214, v215\n"
    "v_mfma_f32_16x16x32_bf16 v[216:219], v[120:123], v[192:195], v[216:219]\n"
    "buffer_store_dwordx4 v[208:211], v5, s[80:83], 0 offen offset:64 sc1\n"
    "v_mfma_f32_16x16x32_bf16 v[220:223], v[144:147], v[192:195], v[220:223]\n"
    "v_mfma_f32_16x16x32_bf16 v[216:219], v[124:127], v[196:199], v[216:219]\n"
    "v_mfma_f32_16x16x32_bf16 v[220:223], v[148:151], v[196:199], v[220:223]\n"
    "s_waitcnt lgkmcnt(0)\n"
    "v_cmp_gt_u32 vcc, 3, v228\n"
    "s_cbranch_vccz .Lh11g\n"
    ".Lh11r:\n"
    "s_sub_u32 s52, s52, 1\n"
    "s_cmp_lt_i32 s52, 0\n"
    "s_cbranch_scc1 .Lh11g\n"
    "ds_read_b32 v228, v224 offset:128\n"
    "s_waitcnt lgkmcnt(0)\n"
    "v_cmp_gt_u32 vcc, 3, v228\n"
    "s_cbranch_vccnz .Lh11r\n"
    "s_branch .Lh11g\n"
    ".Lh11g:\n"
    "ds_read_b64_tr_b16 v[152:153], v2 offset:30720\n"
    "ds_read_b64_tr_b16 v[154:155], v3 offset:30720\n"
    "ds_read_b64_tr_b16 v[156:157], v2 offset:31744\n"
    "ds_read_b64_tr_b16 v[158:159], v3 offset:31744\n"
    "ds_read_b64_tr_b16 v[160:161], v2 offset:32768\n"
    "ds_read_b64_tr_b16 v[162:163], v3 offset:32768\n"
    "ds_read_b64_tr_b16 v[164:165], v2 offset:33792\n"
    "ds_read_b64_tr_b16 v[166:167], v3 offset:33792\n"
    "ds_read_b64_tr_b16 v[168:169], v2 offset:34816\n"
    "ds_read_b64_tr_b16 v[170:171], v3 offset:34816\n"
    "ds_read_b64_tr_b16 v[172:173], v2 offset:35840\n"
    "ds_read_b64_tr_b16 v[174:175], v3 offset:35840\n"
    "ds_read_b32 v252, v1 offset:128\n"
    "s_branch .Lh11j\n"
    ".Lh12:\n"
    "v_mfma_f32_16x16x32_bf16 v[216:219], v[104:107], v[152:155], 0\n"
    "buffer_store_dwordx4 v[200:203], v4, s[80:83], 0 offen offset:0 sc1\n"
    "v_mfma_f32_16x16x32_bf16 v[220:223], v[128:131], v[152:155], 0\n"
    "v_mul_f32 v208, v208, v252\n"
    "v_mul_f32 v209, v209, v252\n"
    "v_mfma_f32_16x16x32_bf16 v[216:219], v[108:111], v[156:159], v[216:219]\n"
    "v_mul_f32 v210, v210, v252\n"
    "v_mul_f32 v211, v211, v252\n"
    "v_mfma_f32_16x16x32_bf16 v[220:223], v[132:135], v[156:159], v[220:223]\n"
    "v_mul_f32 v212, v212, v252\n"
    "v_mul_f32 v213, v213, v252\n"
    "v_mfma_f32_16x16x32_bf16 v[216:219], v[112:115], v[160:163], v[216:219]\n"
    "v_mul_f32 v214, v214, v252\n"
    "v_mul_f32 v215, v215, v252\n"
    "v_mfma_f32_16x16x32_bf16 v[220:223], v[136:139], v[160:163], v[220:223]\n"
    "ds_read_b32 v228, v224 offset:144\n"
    "v_cvt_pk_bf16_f32 v208, v208, v209\n"
    "v_mfma_f32_16x16x32_bf16 v[216:219], v[116:119], v[164:167], v[216:219]\n"
    "v_cvt_pk_bf16_f32 v209, v210, v211\n"
    "v_cvt_pk_bf16_f32 v210, v212, v213\n"
    "v_mfma_f32_16x16x32_bf16 v[220:223], v[140:143], v[164:167], v[220:223]\n"
    "v_cvt_pk_bf16_f32 v211, v214, v215\n"
    "v_mfma_f32_16x16x32_bf16 v[216:219], v[120:123], v[168:171], v[216:219]\n"
    "buffer_store_dwordx4 v[208:211], v4, s[80:83], 0 offen offset:64 sc1\n"
    "v_mfma_f32_16x16x32_bf16 v[220:223], v[144:147], v[168:171], v[220:223]\n"
    "v_mfma_f32_16x16x32_bf16 v[216:219], v[124:127], v[172:175], v[216:219]\n"
    "v_mfma_f32_16x16x32_bf16 v[220:223], v[148:151], v[172:175], v[220:223]\n"
    "s_waitcnt lgkmcnt(0)\n"
    "v_cmp_gt_u32 vcc, 3, v228\n"
    "s_cbranch_vccz .Lh12g\n"
    ".Lh12r:\n"
    "s_sub_u32 s52, s52, 1\n"
    "s_cmp_lt_i32 s52, 0\n"
    "s_cbranch_scc1 .Lh12g\n"
    "ds_read_b32 v228, v224 offset:144\n"
    "s_waitcnt lgkmcnt(0)\n"
    "v_cmp_gt_u32 vcc, 3, v228\n"
    "s_cbranch_vccnz .Lh12r\n"
    "s_branch .Lh12g\n"
    ".Lh12g:\n"
    "ds_read_b64_tr_b16 v[176:177], v2 offset:18432\n"
    "ds_read_b64_tr_b16 v[178:179], v3 offset:18432\n"
    "ds_read_b64_tr_b16 v[180:181], v2 offset:19456\n"
    "ds_read_b64_tr_b16 v[182:183], v3 offset:19456\n"
    "ds_read_b64_tr_b16 v[184:185], v2 offset:20480\n"
    "ds_read_b64_tr_b16 v[186:187], v3 offset:20480\n"
    "ds_read_b64_tr_b16 v[188:189], v2 offset:21504\n"
    "ds_read_b64_tr_b16 v[190:191], v3 offset:21504\n"
    "ds_read_b64_tr_b16 v[192:193], v2 offset:22528\n"
    "ds_read_b64_tr_b16 v[194:195], v3 offset:22528\n"
    "ds_read_b64_tr_b16 v[196:197], v2 offset:23552\n"
    "ds_read_b64_tr_b16 v[198:199], v3 offset:23552\n"
    "ds_read_b32 v253, v1 offset:0\n"
    "s_branch .Lh12j\n"
    ".Lh13:\n"
    "v_mfma_f32_16x16x32_bf16 v[208:211], v[56:59], v[176:179], 0\n"
    "v_mfma_f32_16x16x32_bf16 v[212:215], v[80:83], v[176:179], 0\n"
    "v_mfma_f32_16x16x32_bf16 v[208:211], v[60:63], v[180:183], v[208:211]\n"
    "v_mfma_f32_16x16x32_bf16 v[212:215], v[84:87], v[180:183], v[212:215]\n"
    "v_mul_f32 v200, v200, v253\n"
    "v_mul_f32 v201, v201, v253\n"
    "v_mfma_f32_16x16x32_bf16 v[208:211], v[64:67], v[184:187], v[208:211]\n"
    "v_mul_f32 v202, v202, v253\n"
    "v_mul_f32 v203, v203, v253\n"
    "v_mfma_f32_16x16x32_bf16 v[212:215], v[88:91], v[184:187], v[212:215]\n"
    "ds_read_b32 v228, v224 offset:160\n"
    "v_mul_f32 v204, v204, v253\n"
    "v_mfma_f32_16x16x32_bf16 v[208:211], v[68:71], v[188:191], v[208:211]\n"
    "v_mul_f32 v205, v205, v253\n"
    "v_mul_f32 v206, v206, v253\n"
    "v_mfma_f32_16x16x32_bf16 v[212:215], v[92:95], v[188:191], v[212:215]\n"
    "v_mul_f32 v207, v207, v253\n"
    "v_cvt_pk_bf16_f32 v200, v200, v201\n"
    "v_mfma_f32_16x16x32_bf16 v[208:211], v[72:75], v[192:195], v[208:211]\n"
    "v_cvt_pk_bf16_f32 v201, v202, v203\n"
    "v_cvt_pk_bf16_f32 v202, v204, v205\n"
    "v_mfma_f32_16x16x32_bf16 v[212:215], v[96:99], v[192:195], v[212:215]\n"
    "v_cvt_pk_bf16_f32 v203, v206, v207\n"
    "v_mfma_f32_16x16x32_bf16 v[208:211], v[76:79], v[196:199], v[208:211]\n"
    "buffer_store_dwordx4 v[200:203], v5, s[80:83], 0 offen offset:0 sc1\n"
    "v_mfma_f32_16x16x32_bf16 v[212:215], v[100:103], v[196:199], v[212:215]\n"
    "s_waitcnt lgkmcnt(0)\n"
    "v_cmp_gt_u32 vcc, 3, v228\n"
    "s_cbranch_vccz .Lh13g\n"
    ".Lh13r:\n"
    "s_sub_u32 s52, s52, 1\n"
    "s_cmp_lt_i32 s52, 0\n"
    "s_cbranch_scc1 .Lh13g\n"
    "ds_read_b32 v228, v224 offset:160\n"
    "s_waitcnt lgkmcnt(0)\n"
    "v_cmp_gt_u32 vcc, 3, v228\n"
    "s_cbranch_vccnz .Lh13r\n"
    "s_branch .Lh13g\n"
    ".Lh13g:\n"
    "ds_read_b64_tr_b16 v[152:153], v2 offset:6144\n"
    "ds_read_b64_tr_b16 v[154:155], v3 offset:6144\n"
    "ds_read_b64_tr_b16 v[156:157], v2 offset:7168\n"
    "ds_read_b64_tr_b16 v[158:159], v3 offset:7168\n"
    "ds_read_b64_tr_b16 v[160:161], v2 offset:8192\n"
    "ds_read_b64_tr_b16 v[162:163], v3 offset:8192\n"
    "ds_read_b64_tr_b16 v[164:165], v2 offset:9216\n"
    "ds_read_b64_tr_b16 v[166:167], v3 offset:9216\n"
    "ds_read_b64_tr_b16 v[168:169], v2 offset:10240\n"
    "ds_read_b64_tr_b16 v[170:171], v3 offset:10240\n"
    "ds_read_b64_tr_b16 v[172:173], v2 offset:11264\n"
    "ds_read_b64_tr_b16 v[174:175], v3 offset:11264\n"
    "ds_read_b32 v252, v1 offset:256\n"
    "s_branch .Lh13j\n"
    ".Lh14:\n"
    "v_mfma_f32_16x16x32_bf16 v[208:211], v[56:59], v[152:155], 0\n"
    "v_mfma_f32_16x16x32_bf16 v[212:215], v[80:83], v[152:155], 0\n"
    "v_mfma_f32_16x16x32_bf16 v[208:211], v[60:63], v[156:159], v[208:211]\n"
    "v_mfma_f32_16x16x32_bf16 v[212:215], v[84:87], v[156:159], v[212:215]\n"
    "v_mul_f32 v200, v200, v252\n"
    "v_mul_f32 v201, v201, v252\n"
    "v_mfma_f32_16x16x32_bf16 v[208:211], v[64:67], v[160:163], v[208:211]\n"
    "v_mul_f32 v202, v202, v252\n"
    "v_mul_f32 v203, v203, v252\n"
    "v_mfma_f32_16x16x32_bf16 v[212:215], v[88:91], v[160:163], v[212:215]\n"
    "ds_read_b32 v228, v224 offset:176\n"
    "v_mul_f32 v204, v204, v252\n"
    "v_mfma_f32_16x16x32_bf16 v[208:211], v[68:71], v[164:167], v[208:211]\n"
    "v_mul_f32 v205, v205, v252\n"
    "v_mul_f32 v206, v206, v252\n"
    "v_mfma_f32_16x16x32_bf16 v[212:215], v[92:95], v[164:167], v[212:215]\n"
    "v_mul_f32 v207, v207, v252\n"
    "v_cvt_pk_bf16_f32 v200, v200, v201\n"
    "v_mfma_f32_16x16x32_bf16 v[208:211], v[72:75], v[168:171], v[208:211]\n"
    "v_cvt_pk_bf16_f32 v201, v202, v203\n"
    "v_cvt_pk_bf16_f32 v202, v204, v205\n"
    "v_mfma_f32_16x16x32_bf16 v[212:215], v[96:99], v[168:171], v[212:215]\n"
    "v_cvt_pk_bf16_f32 v203, v206, v207\n"
    "v_mfma_f32_16x16x32_bf16 v[208:211], v[76:79], v[172:175], v[208:211]\n"
    "buffer_store_dwordx4 v[200:203], v4, s[80:83], 0 offen offset:0 sc1\n"
    "v_mfma_f32_16x16x32_bf16 v[212:215], v[100:103], v[172:175], v[212:215]\n"
    "s_waitcnt lgkmcnt(0)\n"
    "v_cmp_gt_u32 vcc, 3, v228\n"
    "s_cbranch_vccz .Lh14g\n"
    ".Lh14r:\n"
    "s_sub_u32 s52, s52, 1\n"
    "s_cmp_lt_i32 s52, 0\n"
    "s_cbranch_scc1 .Lh14g\n"
    "ds_read_b32 v228, v224 offset:176\n"
    "s_waitcnt lgkmcnt(0)\n"
    "v_cmp_gt_u32 vcc, 3, v228\n"
    "s_cbranch_vccnz .Lh14r\n"
    "s_branch .Lh14g\n"
    ".Lh14g:\n"
    "ds_read_b64_tr_b16 v[176:177], v2 offset:12288\n"
    "ds_read_b64_tr_b16 v[178:179], v3 offset:12288\n"
    "ds_read_b64_tr_b16 v[180:181], v2 offset:13312\n"
    "ds_read_b64_tr_b16 v[182:183], v3 offset:13312\n"
    "ds_read_b64_tr_b16 v[184:185], v2 offset:14336\n"
    "ds_read_b64_tr_b16 v[186:187], v3 offset:14336\n"
    "ds_read_b64_tr_b16 v[188:189], v2 offset:15360\n"
    "ds_read_b64_tr_b16 v[190:191], v3 offset:15360\n"
    "ds_read_b64_tr_b16 v[192:193], v2 offset:16384\n"
    "ds_read_b64_tr_b16 v[194:195], v3 offset:16384\n"
    "ds_read_b64_tr_b16 v[196:197], v2 offset:17408\n"
    "ds_read_b64_tr_b16 v[198:199], v3 offset:17408\n"
    "ds_read_b32 v253, v1 offset:320\n"
    "s_branch .Lh14j\n"
    ".Lh_e12:\n"
    "s_waitcnt lgkmcnt(1)\n"
    "v_mfma_f32_16x16x32_bf16 v[200:203], v[8:11], v[176:179], 0\n"
    "v_mul_f32 v208, v208, v252\n"
    "v_mul_f32 v209, v209, v252\n"
    "v_mfma_f32_16x16x32_bf16 v[204:207], v[32:35], v[176:179], 0\n"
    "v_mul_f32 v210, v210, v252\n"
    "v_mul_f32 v211, v211, v252\n"
    "v_mfma_f32_16x16x32_bf16 v[200:203], v[12:15], v[180:183], v[200:203]\n"
    "v_mul_f32 v212, v212, v252\n"
    "v_mul_f32 v213, v213, v252\n"
    "v_mfma_f32_16x16x32_bf16 v[204:207], v[36:39], v[180:183], v[204:207]\n"
    "v_mul_f32 v214, v214, v252\n"
    "v_mul_f32 v215, v215, v252\n"
    "v_mfma_f32_16x16x32_bf16 v[200:203], v[16:19], v[184:187], v[200:203]\n"
    "v_cvt_pk_bf16_f32 v208, v208, v209\n"
    "v_cvt_pk_bf16_f32 v209, v210, v211\n"
    "v_mfma_f32_16x16x32_bf16 v[204:207], v[40:43], v[184:187], v[204:207]\n"
    "v_cvt_pk_bf16_f32 v210, v212, v213\n"
    "v_cvt_pk_bf16_f32 v211, v214, v215\n"
    "v_mfma_f32_16x16x32_bf16 v[200:203], v[20:23], v[188:191], v[200:203]\n"
    "buffer_store_dwordx4 v[208:211], v4, s[80:83], 0 offen offset:64 sc1\n"
    "v_mfma_f32_16x16x32_bf16 v[204:207], v[44:47], v[188:191], v[204:207]\n"
    "v_mfma_f32_16x16x32_bf16 v[200:203], v[24:27], v[192:195], v[200:203]\n"
    "v_add_u32 v4, v0, v4\n"
    "v_mfma_f32_16x16x32_bf16 v[204:207], v[48:51], v[192:195], v[204:207]\n"
    "v_mfma_f32_16x16x32_bf16 v[200:203], v[28:31], v[196:199], v[200:203]\n"
    "v_mfma_f32_16x16x32_bf16 v[204:207], v[52:55], v[196:199], v[204:207]\n"
    "v_mfma_f32_16x16x32_bf16 v[208:211], v[56:59], v[176:179], 0\n"
    "v_mfma_f32_16x16x32_bf16 v[212:215], v[80:83], v[176:179], 0\n"
    "v_mfma_f32_16x16x32_bf16 v[208:211], v[60:63], v[180:183], v[208:211]\n"
    "v_mfma_f32_16x16x32_bf16 v[212:215], v[84:87], v[180:183], v[212:215]\n"
    "v_mfma_f32_16x16x32_bf16 v[208:211], v[64:67], v[184:187], v[208:211]\n"
    "v_mfma_f32_16x16x32_bf16 v[212:215], v[88:91], v[184:187], v[212:215]\n"
    "s_waitcnt lgkmcnt(0)\n"
    "v_mul_f32 v200, v200, v253\n"
    "v_mfma_f32_16x16x32_bf16 v[208:211], v[68:71], v[188:191], v[208:211]\n"
    "v_mul_f32 v201, v201, v253\n"
    "v_mul_f32 v202, v202, v253\n"
    "v_mfma_f32_16x16x32_bf16 v[212:215], v[92:95], v[188:191], v[212:215]\n"
    "v_mul_f32 v203, v203, v253\n"
    "v_mul_f32 v204, v204, v253\n"
    "v_mfma_f32_16x16x32_bf16 v[208:211], v[72:75], v[192:195], v[208:211]\n"
    "v_mul_f32 v205, v205, v253\n"
    "v_mul_f32 v206, v206, v253\n"
    "v_mfma_f32_16x16x32_bf16 v[212:215], v[96:99], v[192:195], v[212:215]\n"
    "v_mul_f32 v207, v207, v253\n"
    "v_cvt_pk_bf16_f32 v200, v200, v201\n"
    "v_mfma_f32_16x16x32_bf16 v[208:211], v[76:79], v[196:199], v[208:211]\n"
    "v_cvt_pk_bf16_f32 v201, v202, v203\n"
    "v_cvt_pk_bf16_f32 v202, v204, v205\n"
    "v_mfma_f32_16x16x32_bf16 v[212:215], v[100:103], v[196:199], v[212:215]\n"
    "v_cvt_pk_bf16_f32 v203, v206, v207\n"
    "buffer_store_dwordx4 v[200:203], v5, s[80:83], 0 offen offset:0 sc1\n"
    "s_nop 2\n"
    "v_mul_f32 v208, v208, v253\n"
    "v_mul_f32 v209, v209, v253\n"
    "v_mul_f32 v210, v210, v253\n"
    "v_mul_f32 v211, v211, v253\n"
    "v_mul_f32 v212, v212, v253\n"
    "v_mul_f32 v213, v213, v253\n"
    "v_mul_f32 v214, v214, v253\n"
    "v_mul_f32 v215, v215, v253\n"
    "v_cvt_pk_bf16_f32 v208, v208, v209\n"
    "v_cvt_pk_bf16_f32 v209, v210, v211\n"
    "v_cvt_pk_bf16_f32 v210, v212, v213\n"
    "v_cvt_pk_bf16_f32 v211, v214, v215\n"
    "buffer_store_dwordx4 v[208:211], v5, s[80:83], 0 offen offset:64 sc1\n"
    "s_branch .Lh_end\n"
    ".Lh15:\n"
    "v_mfma_f32_16x16x32_bf16 v[208:211], v[56:59], v[176:179], 0\n"
    "v_mfma_f32_16x16x32_bf16 v[212:215], v[80:83], v[176:179], 0\n"
    "v_mfma_f32_16x16x32_bf16 v[208:211], v[60:63], v[180:183], v[208:211]\n"
    "v_mfma_f32_16x16x32_bf16 v[212:215], v[84:87], v[180:183], v[212:215]\n"
    "v_mul_f32 v200, v200, v253\n"
    "v_mul_f32 v201, v201, v253\n"
    "v_mfma_f32_16x16x32_bf16 v[208:211], v[64:67], v[184:187], v[208:211]\n"
    "v_mul_f32 v202, v202, v253\n"
    "v_mul_f32 v203, v203, v253\n"
    "v_mfma_f32_16x16x32_bf16 v[212:215], v[88:91], v[184:187], v[212:215]\n"
    "ds_read_b32 v228, v224 offset:832\n"
    "v_mul_f32 v204, v204, v253\n"
    "v_mfma_f32_16x16x32_bf16 v[208:211], v[68:71], v[188:191], v[208:211]\n"
    "v_mul_f32 v205, v205, v253\n"
    "v_mul_f32 v206, v206, v253\n"
    "v_mfma_f32_16x16x32_bf16 v[212:215], v[92:95], v[188:191], v[212:215]\n"
    "v_mul_f32 v207, v207, v253\n"
    "v_cvt_pk_bf16_f32 v200, v200, v201\n"
    "v_mfma_f32_16x16x32_bf16 v[208:211], v[72:75], v[192:195], v[208:211]\n"
    "v_cvt_pk_bf16_f32 v201, v202, v203\n"
    "v_cvt_pk_bf16_f32 v202, v204, v205\n"
    "v_mfma_f32_16x16x32_bf16 v[212:215], v[96:99], v[192:195], v[212:215]\n"
    "v_cvt_pk_bf16_f32 v203, v206, v207\n"
    "v_mfma_f32_16x16x32_bf16 v[208:211], v[76:79], v[196:199], v[208:211]\n"
    "buffer_store_dwordx4 v[200:203], v5, s[80:83], 0 offen offset:0 sc1\n"
    "v_mfma_f32_16x16x32_bf16 v[212:215], v[100:103], v[196:199], v[212:215]\n"
    "s_waitcnt lgkmcnt(0)\n"
    "v_cmp_gt_u32 vcc, 4, v228\n"
    "s_cbranch_vccz .Lh15g\n"
    ".Lh15r:\n"
    "s_sub_u32 s52, s52, 1\n"
    "s_cmp_lt_i32 s52, 0\n"
    "s_cbranch_scc1 .Lh15g\n"
    "ds_read_b32 v228, v224 offset:832\n"
    "s_waitcnt lgkmcnt(0)\n"
    "v_cmp_gt_u32 vcc, 4, v228\n"
    "s_cbranch_vccnz .Lh15r\n"
    "s_branch .Lh15g\n"
    ".Lh15g:\n"
    "ds_read_b64_tr_b16 v[152:153], v2 offset:0\n"
    "ds_read_b64_tr_b16 v[154:155], v3 offset:0\n"
    "ds_read_b64_tr_b16 v[156:157], v2 offset:1024\n"
    "ds_read_b64_tr_b16 v[158:159], v3 offset:1024\n"
    "ds_read_b64_tr_b16 v[160:161], v2 offset:2048\n"
    "ds_read_b64_tr_b16 v[162:163], v3 offset:2048\n"
    "ds_read_b64_tr_b16 v[164:165], v2 offset:3072\n"
    "ds_read_b64_tr_b16 v[166:167], v3 offset:3072\n"
    "ds_read_b64_tr_b16 v[168:169], v2 offset:4096\n"
    "ds_read_b64_tr_b16 v[170:171], v3 offset:4096\n"
    "ds_read_b64_tr_b16 v[172:173], v2 offset:5120\n"
    "ds_read_b64_tr_b16 v[174:175], v3 offset:5120\n"
    "ds_read_b32 v252, v1 offset:192\n"
    "s_branch .Lh15j\n"
    ".Lh_last:\n"
                :: "{v1}"(vi), "{v[2:3]}"(vz), "{v[4:7]}"(vo), "{v[224:227]}"(fl), "{v235}"(lf[0]), "{v236}"(lf[1]),
                   "{v[238:239]}"(xp), "{s[40:41]}"(ws), "{s[42:43]}"(xb), "{v[230:233]}"(xo), "{v234}"(xo4),
                   "{s84}"(UNI(axis_at(rm, 12) * IROWB)), "{s85}"(UNI(axis_at(rm, 13) * IROWB)),
                   "{s46}"(UNI(2 * rm.d * IROWB)), "{v0}"(d56), "{s48}"(UNI(WS_ACK + (2 * bid + h) * 128)),
                   "{s50}"(h), "{s58}"(UNI(t12)), "{s59}"(UNI(t0 + (bid & 4 ? 4400 : 5200))),
                   "{s87}"(UNI(t0 + 7000 - (bid & 4 ? 5200 - 4400 : 0))), "{v[248:249]}"(uu), "{s[80:83]}"(srd)
                : "memory", "v255", "s51", "s52", "s53", "s54", "s55", "s56", "s57", "s60", "s61", "s76", "s77", "s79", "s88", "s89", "s90", "s91", "s98", "s99", "s92", "s93", "s94", "s95", "vcc", "scc");
        }
    } else {
        const int a = (0x3210 >> (4 * wave - 16)) & 3, H = a & 1, n0 = a, u0 = a >> 1;
        const AttC c = att_const(lane, H, cedge);
        f32x4 cbM, cbF, cbL;
        const float NEG = -__builtin_inff();
#pragma unroll
        for (int r = 0; r < 4; ++r) {
            const bool ok = (c.pk >> (4 + r)) & 1;
            cbM[r] = ok ? 0.f : NEG;
            cbF[r] = ok && e == 0 ? 0.f : NEG;
            cbL[r] = ok && e == 1 ? 0.f : NEG;
        }
        const int sl = n0 >= 3 ? n0 - 3 : n0;
        const u32x4 va = {c.sb, c.v0, c.v1, (unsigned)(LQ + sl * 6144 + lane * 16)};
        const u32x4 vb = {(unsigned)(LZ + sl * 6144 + (16 * g + (i16 ^ (4 * g))) * 16), (c.pk & 15) * 0x1111u, vf, lane < 4 ? vf + 16 * a : OOB};
        const u64 slots = (redge ? 0x975A86420ull : 0x420CA86420ull) >> (4 * u0);
        asm volatile("s_nop 7\n"
    "s_sub_u32 s56, s45, 2\n"
    "s_setprio 0\n"
    "s_cmp_lg_u32 s70, 0\n"
    "s_cbranch_scc1 .La_ld\n"
    ".La_join:\n"
    "s_mov_b32 s52, 0x40000\n"
    "s_bfe_u32 s48, s60, 0x40000\n"
    "s_bfe_u32 s49, s60, 0x40004\n"
    "s_bfe_u32 s50, s60, 0x40008\n"
    "s_bfe_u32 s51, s60, 0x4000c\n"
    "s_mul_i32 s48, s48, 8896\n"
    "s_mul_i32 s49, s49, 8896\n"
    "s_mul_i32 s50, s50, 8896\n"
    "s_mul_i32 s51, s51, 8896\n"
    "s_lshr_b64 s[60:61], s[60:61], 8\n"
    "v_add_u32 v244, s48, v0\n"
    "v_add_u32 v245, s49, v0\n"
    "v_add_u32 v246, s50, v0\n"
    "v_add_u32 v247, s51, v0\n"
    "v_add_u32 v200, s48, v1\n"
    "v_add_u32 v201, s49, v1\n"
    "v_add_u32 v202, s48, v2\n"
    "v_add_u32 v203, s49, v2\n"
    "s_mov_b32 s69, 1\n"
    "s_mov_b32 s53, 0\n"
    "s_add_u32 s55, s45, 1\n"
    "s_lshr_b32 s57, s45, 2\n"
    "v_mov_b32 v248, 0x3f803f80\n"
    "v_mov_b32 v249, 0x3f803f80\n"
    "v_mov_b32 v250, 0x3f803f80\n"
    "v_mov_b32 v251, 0x3f803f80\n"
    "s_mul_i32 s62, s57, 6\n"
    "ds_read_b32 v253, v6 offset:0\n"
    "s_waitcnt lgkmcnt(0)\n"
    "v_cmp_gt_u32 vcc, s54, v253\n"
    "s_cbranch_vccnz .La1\n"
    ".La1b:\n"
    "ds_read_b32 v254, v6 offset:16\n"
    "s_waitcnt lgkmcnt(0)\n"
    "v_cmp_gt_u32 vcc, s55, v254\n"
    "s_cbranch_vccz .La_nf\n"
    "s_mov_b32 s69, 0\n"
    "ds_read_b64_tr_b16 v[104:105], v200 offset:0\n"
    "ds_read_b64_tr_b16 v[106:107], v201 offset:0\n"
    "ds_read_b64_tr_b16 v[108:109], v202 offset:0\n"
    "ds_read_b64_tr_b16 v[110:111], v203 offset:0\n"
    "ds_read_b64_tr_b16 v[112:113], v200 offset:32\n"
    "ds_read_b64_tr_b16 v[114:115], v201 offset:32\n"
    "ds_read_b64_tr_b16 v[116:117], v202 offset:32\n"
    "ds_read_b64_tr_b16 v[118:119], v203 offset:32\n"
    "ds_read_b64_tr_b16 v[120:121], v200 offset:64\n"
    "ds_read_b64_tr_b16 v[122:123], v201 offset:64\n"
    "ds_read_b64_tr_b16 v[124:125], v202 offset:64\n"
    "ds_read_b64_tr_b16 v[126:127], v203 offset:64\n"
    "ds_read_b64_tr_b16 v[128:129], v200 offset:96\n"
    "ds_read_b64_tr_b16 v[130:131], v201 offset:96\n"
    "ds_read_b64_tr_b16 v[132:133], v202 offset:96\n"
    "ds_read_b64_tr_b16 v[134:135], v203 offset:96\n"
    "ds_read_b64_tr_b16 v[136:137], v200 offset:128\n"
    "ds_read_b64_tr_b16 v[138:139], v201 offset:128\n"
    "ds_read_b64_tr_b16 v[140:141], v202 offset:128\n"
    "ds_read_b64_tr_b16 v[142:143], v203 offset:128\n"
    "ds_read_b64_tr_b16 v[144:145], v200 offset:160\n"
    "ds_read_b64_tr_b16 v[146:147], v201 offset:160\n"
    "ds_read_b64_tr_b16 v[148:149], v202 offset:160\n"
    "ds_read_b64_tr_b16 v[150:151], v203 offset:160\n"
    "ds_read_b64_tr_b16 v[152:153], v200 offset:192\n"
    "ds_read_b64_tr_b16 v[154:155], v201 offset:192\n"
    "ds_read_b64_tr_b16 v[156:157], v202 offset:192\n"
    "ds_read_b64_tr_b16 v[158:159], v203 offset:192\n"
    "ds_read_b64_tr_b16 v[160:161], v200 offset:224\n"
    "ds_read_b64_tr_b16 v[162:163], v201 offset:224\n"
    "ds_read_b64_tr_b16 v[164:165], v202 offset:224\n"
    "ds_read_b64_tr_b16 v[166:167], v203 offset:224\n"
    "ds_read_b64_tr_b16 v[168:169], v200 offset:256\n"
    "ds_read_b64_tr_b16 v[170:171], v201 offset:256\n"
    "ds_read_b64_tr_b16 v[172:173], v202 offset:256\n"
    "ds_read_b64_tr_b16 v[174:175], v203 offset:256\n"
    "ds_read_b64_tr_b16 v[176:177], v200 offset:288\n"
    "ds_read_b64_tr_b16 v[178:179], v201 offset:288\n"
    "ds_read_b64_tr_b16 v[180:181], v202 offset:288\n"
    "ds_read_b64_tr_b16 v[182:183], v203 offset:288\n"
    "ds_read_b64_tr_b16 v[184:185], v200 offset:320\n"
    "ds_read_b64_tr_b16 v[186:187], v201 offset:320\n"
    "ds_read_b64_tr_b16 v[188:189], v202 offset:320\n"
    "ds_read_b64_tr_b16 v[190:191], v203 offset:320\n"
    "ds_read_b64_tr_b16 v[192:193], v200 offset:352\n"
    "ds_read_b64_tr_b16 v[194:195], v201 offset:352\n"
    "ds_read_b64_tr_b16 v[196:197], v202 offset:352\n"
    "ds_read_b64_tr_b16 v[198:199], v203 offset:352\n"
    "ds_read_b32 v254, v6 offset:16\n"
    "s_waitcnt lgkmcnt(0)\n"
    "v_cmp_gt_u32 vcc, s55, v254\n"
    "s_cbranch_vccnz .La2\n"
    ".La2b:\n"
    ".La_nf:\n"
    "ds_read_b128 v[8:11], v3 offset:0\n"
    "ds_read_b128 v[56:59], v244 offset:0\n"
    "ds_read_b128 v[12:15], v3 offset:1024\n"
    "ds_read_b128 v[60:63], v244 offset:64\n"
    "ds_read_b128 v[16:19], v3 offset:2048\n"
    "ds_read_b128 v[64:67], v244 offset:128\n"
    "ds_read_b128 v[20:23], v3 offset:3072\n"
    "ds_read_b128 v[68:71], v244 offset:192\n"
    "ds_read_b128 v[24:27], v3 offset:4096\n"
    "ds_read_b128 v[28:31], v3 offset:5120\n"
    "ds_read_b128 v[32:35], v244 offset:256\n"
    "ds_read_b128 v[36:39], v244 offset:320\n"
    "ds_read_b128 v[40:43], v245 offset:0\n"
    "ds_read_b128 v[44:47], v245 offset:64\n"
    ".La_loop:\n"
    "s_setprio 1\n"
    "s_add_u32 s57, s57, 1\n"
    "s_waitcnt lgkmcnt(8)\n"
    "v_mfma_f32_16x16x32_bf16 v[72:75], v[56:59], v[8:11], v[236:239]\n"
    "ds_read_b128 v[48:51], v245 offset:128\n"
    "v_mov_b32 v252, s57\n"
    "v_mfma_f32_16x16x32_bf16 v[72:75], v[60:63], v[12:15], v[72:75]\n"
    "ds_read_b128 v[52:55], v245 offset:192\n"
    "v_add_u32 v4, s53, v4\n"
    "v_mfma_f32_16x16x32_bf16 v[72:75], v[64:67], v[16:19], v[72:75]\n"
    "ds_read_b128 v[56:59], v245 offset:256\n"
    "s_waitcnt lgkmcnt(5)\n"
    "v_mfma_f32_16x16x32_bf16 v[72:75], v[68:71], v[20:23], v[72:75]\n"
    "ds_read_b128 v[60:63], v245 offset:320\n"
    "ds_write_b32 v7, v252 offset:192\n"
    "v_mfma_f32_16x16x32_bf16 v[72:75], v[32:35], v[24:27], v[72:75]\n"
    "ds_read_b128 v[64:67], v244 offset:8896\n"
    "ds_read_b128 v[208:211], v7 offset:32768\n"
    "v_mfma_f32_16x16x32_bf16 v[72:75], v[36:39], v[28:31], v[72:75]\n"
    "ds_read_b128 v[68:71], v244 offset:8960\n"
    "s_waitcnt lgkmcnt(7)\n"
    "v_mfma_f32_16x16x32_bf16 v[76:79], v[40:43], v[8:11], v[232:235]\n"
    "ds_read_b128 v[32:35], v244 offset:9024\n"
    "ds_read_b128 v[212:215], v7 offset:32768\n"
    "v_mfma_f32_16x16x32_bf16 v[76:79], v[44:47], v[12:15], v[76:79]\n"
    "ds_read_b128 v[36:39], v244 offset:9088\n"
    "ds_read_b128 v[216:219], v7 offset:32768\n"
    "v_mfma_f32_16x16x32_bf16 v[76:79], v[48:51], v[16:19], v[76:79]\n"
    "ds_read_b128 v[40:43], v244 offset:9152\n"
    "s_waitcnt lgkmcnt(9)\n"
    "v_mfma_f32_16x16x32_bf16 v[76:79], v[52:55], v[20:23], v[76:79]\n"
    "ds_read_b128 v[44:47], v244 offset:9216\n"
    "v_max_f32 v72, v72, v73\n"
    "v_mfma_f32_16x16x32_bf16 v[76:79], v[56:59], v[24:27], v[76:79]\n"
    "ds_read_b128 v[48:51], v245 offset:8896\n"
    "v_max_f32 v74, v74, v75\n"
    "v_mfma_f32_16x16x32_bf16 v[76:79], v[60:63], v[28:31], v[76:79]\n"
    "ds_read_b128 v[52:55], v245 offset:8960\n"
    "s_waitcnt lgkmcnt(7)\n"
    "v_mfma_f32_16x16x32_bf16 v[80:83], v[64:67], v[8:11], v[232:235]\n"
    "ds_read_b128 v[56:59], v245 offset:9024\n"
    "v_max_f32 v206, v72, v74\n"
    "v_mfma_f32_16x16x32_bf16 v[80:83], v[68:71], v[12:15], v[80:83]\n"
    "ds_read_b128 v[60:63], v245 offset:9088\n"
    "ds_read_b128 v[220:223], v7 offset:32768\n"
    "v_mfma_f32_16x16x32_bf16 v[80:83], v[32:35], v[16:19], v[80:83]\n"
    "ds_read_b128 v[64:67], v245 offset:9152\n"
    "s_waitcnt lgkmcnt(6)\n"
    "v_mfma_f32_16x16x32_bf16 v[80:83], v[36:39], v[20:23], v[80:83]\n"
    "ds_read_b128 v[68:71], v245 offset:9216\n"
    "v_max_f32 v76, v76, v77\n"
    "v_mfma_f32_16x16x32_bf16 v[80:83], v[40:43], v[24:27], v[80:83]\n"
    "ds_read_b128 v[32:35], v246 offset:0\n"
    "v_max_f32 v78, v78, v79\n"
    "v_mfma_f32_16x16x32_bf16 v[80:83], v[44:47], v[28:31], v[80:83]\n"
    "ds_read_b128 v[36:39], v246 offset:64\n"
    "s_waitcnt lgkmcnt(6)\n"
    "v_mfma_f32_16x16x32_bf16 v[84:87], v[48:51], v[8:11], v[232:235]\n"
    "ds_read_b128 v[40:43], v246 offset:128\n"
    "v_max3_f32 v206, v206, v76, v78\n"
    "v_mfma_f32_16x16x32_bf16 v[84:87], v[52:55], v[12:15], v[84:87]\n"
    "ds_read_b128 v[44:47], v246 offset:192\n"
    "ds_read_b128 v[224:227], v7 offset:32768\n"
    "v_mfma_f32_16x16x32_bf16 v[84:87], v[56:59], v[16:19], v[84:87]\n"
    "ds_read_b128 v[48:51], v246 offset:256\n"
    "s_waitcnt lgkmcnt(6)\n"
    "v_mfma_f32_16x16x32_bf16 v[84:87], v[60:63], v[20:23], v[84:87]\n"
    "ds_read_b128 v[52:55], v246 offset:320\n"
    "v_max_f32 v80, v80, v81\n"
    "v_mfma_f32_16x16x32_bf16 v[84:87], v[64:67], v[24:27], v[84:87]\n"
    "ds_read_b128 v[56:59], v247 offset:0\n"
    "v_max_f32 v82, v82, v83\n"
    "v_mfma_f32_16x16x32_bf16 v[84:87], v[68:71], v[28:31], v[84:87]\n"
    "ds_read_b128 v[60:63], v247 offset:64\n"
    "s_waitcnt lgkmcnt(6)\n"
    "v_mfma_f32_16x16x32_bf16 v[88:91], v[32:35], v[8:11], v[232:235]\n"
    "ds_read_b128 v[64:67], v247 offset:128\n"
    "v_max3_f32 v206, v206, v80, v82\n"
    "v_mfma_f32_16x16x32_bf16 v[88:91], v[36:39], v[12:15], v[88:91]\n"
    "ds_read_b128 v[68:71], v247 offset:192\n"
    "ds_read_b128 v[228:231], v7 offset:32768\n"
    "v_mfma_f32_16x16x32_bf16 v[88:91], v[40:43], v[16:19], v[88:91]\n"
    "ds_read_b128 v[32:35], v247 offset:256\n"
    "s_waitcnt lgkmcnt(6)\n"
    "v_mfma_f32_16x16x32_bf16 v[88:91], v[44:47], v[20:23], v[88:91]\n"
    "ds_read_b128 v[36:39], v247 offset:320\n"
    "v_max_f32 v84, v84, v85\n"
    "v_mfma_f32_16x16x32_bf16 v[88:91], v[48:51], v[24:27], v[88:91]\n"
    "ds_read_b128 v[40:43], v246 offset:8896\n"
    "v_max_f32 v86, v86, v87\n"
    "v_mfma_f32_16x16x32_bf16 v[88:91], v[52:55], v[28:31], v[88:91]\n"
    "ds_read_b128 v[44:47], v246 offset:8960\n"
    "s_waitcnt lgkmcnt(6)\n"
    "v_mfma_f32_16x16x32_bf16 v[92:95], v[56:59], v[8:11], v[232:235]\n"
    "ds_read_b128 v[48:51], v246 offset:9024\n"
    "v_max3_f32 v206, v206, v84, v86\n"
    "v_mfma_f32_16x16x32_bf16 v[92:95], v[60:63], v[12:15], v[92:95]\n"
    "ds_read_b128 v[52:55], v246 offset:9088\n"
    "v_mfma_f32_16x16x32_bf16 v[92:95], v[64:67], v[16:19], v[92:95]\n"
    "ds_read_b128 v[56:59], v246 offset:9152\n"
    "s_waitcnt lgkmcnt(5)\n"
    "v_mfma_f32_16x16x32_bf16 v[92:95], v[68:71], v[20:23], v[92:95]\n"
    "ds_read_b128 v[60:63], v246 offset:9216\n"
    "v_max_f32 v88, v88, v89\n"
    "v_mfma_f32_16x16x32_bf16 v[92:95], v[32:35], v[24:27], v[92:95]\n"
    "ds_read_b128 v[64:67], v247 offset:8896\n"
    "v_max_f32 v90, v90, v91\n"
    "v_mfma_f32_16x16x32_bf16 v[92:95], v[36:39], v[28:31], v[92:95]\n"
    "ds_read_b128 v[68:71], v247 offset:8960\n"
    "s_waitcnt lgkmcnt(5)\n"
    "v_mfma_f32_16x16x32_bf16 v[96:99], v[40:43], v[8:11], v[232:235]\n"
    "ds_read_b128 v[32:35], v247 offset:9024\n"
    "v_max3_f32 v206, v206, v88, v90\n"
    "v_mfma_f32_16x16x32_bf16 v[96:99], v[44:47], v[12:15], v[96:99]\n"
    "ds_read_b128 v[36:39], v247 offset:9088\n"
    "v_mfma_f32_16x16x32_bf16 v[96:99], v[48:51], v[16:19], v[96:99]\n"
    "ds_read_b128 v[40:43], v247 offset:9152\n"
    "s_waitcnt lgkmcnt(5)\n"
    "v_mfma_f32_16x16x32_bf16 v[96:99], v[52:55], v[20:23], v[96:99]\n"
    "ds_read_b128 v[44:47], v247 offset:9216\n"
    "v_max_f32 v92, v92, v93\n"
    "v_mfma_f32_16x16x32_bf16 v[96:99], v[56:59], v[24:27], v[96:99]\n"
    "v_add_u32 v244, s50, v1\n"
    "v_add_u32 v245, s51, v1\n"
    "v_mfma_f32_16x16x32_bf16 v[96:99], v[60:63], v[28:31], v[96:99]\n"
    "v_add_u32 v246, s50, v2\n"
    "s_waitcnt lgkmcnt(3)\n"
    "v_mfma_f32_16x16x32_bf16 v[100:103], v[64:67], v[8:11], v[240:243]\n"
    "v_add_u32 v247, s51, v2\n"
    "v_max_f32 v94, v94, v95\n"
    "v_mfma_f32_16x16x32_bf16 v[100:103], v[68:71], v[12:15], v[100:103]\n"
    "ds_read_b64_tr_b16 v[48:49], v244 offset:160\n"
    "v_max3_f32 v206, v206, v92, v94\n"
    "v_mfma_f32_16x16x32_bf16 v[100:103], v[32:35], v[16:19], v[100:103]\n"
    "ds_read_b64_tr_b16 v[50:51], v245 offset:160\n"
    "s_waitcnt lgkmcnt(2)\n"
    "v_mfma_f32_16x16x32_bf16 v[100:103], v[36:39], v[20:23], v[100:103]\n"
    "ds_read_b64_tr_b16 v[52:53], v246 offset:160\n"
    "v_max_f32 v96, v96, v97\n"
    "v_mfma_f32_16x16x32_bf16 v[100:103], v[40:43], v[24:27], v[100:103]\n"
    "ds_read_b64_tr_b16 v[54:55], v247 offset:160\n"
    "v_max_f32 v98, v98, v99\n"
    "v_mfma_f32_16x16x32_bf16 v[100:103], v[44:47], v[28:31], v[100:103]\n"
    "ds_read_b64_tr_b16 v[56:57], v244 offset:192\n"
    "ds_read_b64_tr_b16 v[58:59], v245 offset:192\n"
    "s_cmp_lg_u32 s69, 0\n"
    "s_cbranch_scc1 .La_fill\n"
    ".La_filled:\n"
    "ds_write_b32 v7, v252 offset:64\n"
    "s_setprio 0\n"
    "v_max3_f32 v206, v206, v96, v98\n"
    "ds_read_b64_tr_b16 v[60:61], v246 offset:192\n"
    "v_max_f32 v100, v100, v101\n"
    "v_max_f32 v102, v102, v103\n"
    "v_max3_f32 v206, v206, v100, v102\n"
    "v_mov_b32 v207, v206\n"
    "ds_read_b64_tr_b16 v[62:63], v247 offset:192\n"
    "ds_read_b64_tr_b16 v[64:65], v244 offset:224\n"
    "v_permlane16_swap_b32 v206, v207\n"
    "v_max_f32 v206, v206, v207\n"
    "v_mov_b32 v207, v206\n"
    "ds_read_b64_tr_b16 v[66:67], v245 offset:224\n"
    "ds_read_b64_tr_b16 v[68:69], v246 offset:224\n"
    "v_permlane32_swap_b32 v206, v207\n"
    "v_max_f32 v206, v206, v207\n"
    "v_mul_f32 v207, s46, v206\n"
    "v_fma_f32 v72, v72, s47, v207\n"
    "v_fma_f32 v74, v74, s47, v207\n"
    "v_fma_f32 v76, v76, s47, v207\n"
    "v_fma_f32 v78, v78, s47, v207\n"
    "v_fma_f32 v80, v80, s47, v207\n"
    "v_fma_f32 v82, v82, s47, v207\n"
    "v_fma_f32 v84, v84, s47, v207\n"
    "v_fma_f32 v86, v86, s47, v207\n"
    "v_fma_f32 v88, v88, s47, v207\n"
    "v_fma_f32 v90, v90, s47, v207\n"
    "v_fma_f32 v92, v92, s47, v207\n"
    "v_fma_f32 v94, v94, s47, v207\n"
    "v_fma_f32 v96, v96, s47, v207\n"
    "v_fma_f32 v98, v98, s47, v207\n"
    "v_fma_f32 v100, v100, s47, v207\n"
    "v_fma_f32 v102, v102, s47, v207\n"
    "ds_read_b64_tr_b16 v[70:71], v247 offset:224\n"
    "ds_read_b64_tr_b16 v[32:33], v244 offset:96\n"
    "ds_read_b64_tr_b16 v[34:35], v245 offset:96\n"
    "ds_read_b64_tr_b16 v[36:37], v246 offset:96\n"
    "ds_read_b64_tr_b16 v[38:39], v247 offset:96\n"
    "ds_read_b64_tr_b16 v[40:41], v244 offset:128\n"
    "ds_read_b64_tr_b16 v[42:43], v245 offset:128\n"
    "ds_read_b64_tr_b16 v[44:45], v246 offset:128\n"
    "ds_read_b64_tr_b16 v[46:47], v247 offset:128\n"
    "ds_read_b64_tr_b16 v[8:9], v244 offset:0\n"
    "ds_read_b64_tr_b16 v[10:11], v245 offset:0\n"
    "ds_read_b64_tr_b16 v[12:13], v246 offset:0\n"
    "ds_read_b64_tr_b16 v[14:15], v247 offset:0\n"
    "ds_read_b64_tr_b16 v[16:17], v244 offset:32\n"
    "ds_read_b64_tr_b16 v[18:19], v245 offset:32\n"
    "ds_read_b64_tr_b16 v[20:21], v246 offset:32\n"
    "ds_read_b64_tr_b16 v[22:23], v247 offset:32\n"
    "ds_read_b64_tr_b16 v[24:25], v244 offset:64\n"
    "ds_read_b64_tr_b16 v[26:27], v245 offset:64\n"
    "ds_read_b64_tr_b16 v[28:29], v246 offset:64\n"
    "ds_read_b64_tr_b16 v[30:31], v247 offset:64\n"
    "ds_read_b32 v253, v6 offset:32\n"
    "v_exp_f32 v72, v72\n"
    "s_bfe_u32 s48, s60, 0x40000\n"
    "v_exp_f32 v74, v74\n"
    "s_bfe_u32 s49, s60, 0x40004\n"
    "v_exp_f32 v76, v76\n"
    "s_bfe_u32 s50, s60, 0x40008\n"
    "v_exp_f32 v78, v78\n"
    "s_bfe_u32 s51, s60, 0x4000c\n"
    "v_exp_f32 v80, v80\n"
    "s_mul_i32 s48, s48, 8896\n"
    "v_exp_f32 v82, v82\n"
    "s_mul_i32 s49, s49, 8896\n"
    "v_exp_f32 v84, v84\n"
    "s_mul_i32 s50, s50, 8896\n"
    "v_exp_f32 v86, v86\n"
    "s_mul_i32 s51, s51, 8896\n"
    "v_exp_f32 v88, v88\n"
    "s_lshr_b64 s[60:61], s[60:61], 8\n"
    "v_exp_f32 v90, v90\n"
    "s_add_u32 s45, s45, 4\n"
    "v_exp_f32 v92, v92\n"
    "s_add_u32 s54, s54, 2\n"
    "v_exp_f32 v94, v94\n"
    "s_add_u32 s55, s55, 4\n"
    "v_exp_f32 v96, v96\n"
    "s_cmp_eq_u32 s66, 2\n"
    "s_cselect_b32 s67, -2, 1\n"
    "v_exp_f32 v98, v98\n"
    "s_add_u32 s66, s66, s67\n"
    "v_exp_f32 v100, v100\n"
    "s_mul_i32 s68, s67, 6144\n"
    "v_exp_f32 v102, v102\n"
    "v_cvt_pk_bf16_f32 v200, v72, v74\n"
    "v_cvt_pk_bf16_f32 v201, v76, v78\n"
    "v_cvt_pk_bf16_f32 v202, v80, v82\n"
    "v_cvt_pk_bf16_f32 v203, v84, v86\n"
    "v_cvt_pk_bf16_f32 v204, v88, v90\n"
    "v_cvt_pk_bf16_f32 v205, v92, v94\n"
    "v_cvt_pk_bf16_f32 v206, v96, v98\n"
    "v_cvt_pk_bf16_f32 v207, v100, v102\n"
    "ds_read_b64_tr_b16 v[72:73], v244 offset:256\n"
    "ds_read_b64_tr_b16 v[74:75], v245 offset:256\n"
    "ds_read_b64_tr_b16 v[76:77], v246 offset:256\n"
    "ds_read_b64_tr_b16 v[78:79], v247 offset:256\n"
    "ds_read_b64_tr_b16 v[80:81], v244 offset:288\n"
    "ds_read_b64_tr_b16 v[82:83], v245 offset:288\n"
    "ds_read_b64_tr_b16 v[84:85], v246 offset:288\n"
    "ds_read_b64_tr_b16 v[86:87], v247 offset:288\n"
    "s_waitcnt lgkmcnt(8)\n"
    "s_setprio 1\n"
    "v_mfma_f32_16x16x32_bf16 v[100:103], v[248:251], v[200:203], 0\n"
    "v_cmp_gt_i32 vcc, s56, v253\n"
    "v_mfma_f32_16x16x32_bf16 v[100:103], v[248:251], v[204:207], v[100:103]\n"
    "v_smfmac_f32_16x16x64_bf16 v[220:223], v[200:203], v[128:135], v5\n"
    "ds_read_b128 v[128:131], v7 offset:32768\n"
    "v_smfmac_f32_16x16x64_bf16 v[224:227], v[200:203], v[136:143], v5\n"
    "ds_read_b128 v[132:135], v7 offset:32768\n"
    "v_smfmac_f32_16x16x64_bf16 v[228:231], v[200:203], v[144:151], v5\n"
    "ds_read_b128 v[136:139], v7 offset:32768\n"
    "v_smfmac_f32_16x16x64_bf16 v[208:211], v[200:203], v[104:111], v5\n"
    "ds_read_b128 v[140:143], v7 offset:32768\n"
    "v_smfmac_f32_16x16x64_bf16 v[212:215], v[200:203], v[112:119], v5\n"
    "ds_read_b128 v[144:147], v7 offset:32768\n"
    "v_smfmac_f32_16x16x64_bf16 v[216:219], v[200:203], v[120:127], v5\n"
    "ds_read_b128 v[148:151], v7 offset:32768\n"
    "v_smfmac_f32_16x16x64_bf16 v[208:211], v[204:207], v[8:15], v5\n"
    "ds_read_b64_tr_b16 v[88:89], v244 offset:320\n"
    "v_rcp_f32 v100, v100\n"
    "v_smfmac_f32_16x16x64_bf16 v[212:215], v[204:207], v[16:23], v5\n"
    "ds_read_b64_tr_b16 v[90:91], v245 offset:320\n"
    "v_lshrrev_b32 v101, 2, v3\n"
    "v_smfmac_f32_16x16x64_bf16 v[216:219], v[204:207], v[24:31], v5\n"
    "ds_read_b64_tr_b16 v[92:93], v246 offset:320\n"
    "v_and_or_b32 v101, v101, 60, s63\n"
    "v_smfmac_f32_16x16x64_bf16 v[220:223], v[204:207], v[32:39], v5\n"
    "ds_read_b64_tr_b16 v[94:95], v247 offset:320\n"
    "s_cbranch_vccnz .La3\n"
    ".La3b:\n"
    "v_smfmac_f32_16x16x64_bf16 v[224:227], v[204:207], v[40:47], v5\n"
    "ds_read_b64_tr_b16 v[96:97], v244 offset:352\n"
    "ds_read_b64_tr_b16 v[98:99], v245 offset:352\n"
    "v_smfmac_f32_16x16x64_bf16 v[228:231], v[204:207], v[48:55], v5\n"
    "s_waitcnt lgkmcnt(6)\n"
    "v_smfmac_f32_16x16x64_bf16 v[128:131], v[200:203], v[152:159], v5\n"
    "ds_write_b32 v101, v100 offset:0\n"
    "ds_read_b64_tr_b16 v[100:101], v246 offset:352\n"
    "v_smfmac_f32_16x16x64_bf16 v[132:135], v[200:203], v[160:167], v5\n"
    "ds_read_b64_tr_b16 v[102:103], v247 offset:352\n"
    "v_smfmac_f32_16x16x64_bf16 v[136:139], v[200:203], v[168:175], v5\n"
    "v_cvt_pk_bf16_f32 v208, v208, v209\n"
    "v_cvt_pk_bf16_f32 v209, v210, v211\n"
    "v_smfmac_f32_16x16x64_bf16 v[140:143], v[200:203], v[176:183], v5\n"
    "v_cvt_pk_bf16_f32 v210, v212, v213\n"
    "v_cvt_pk_bf16_f32 v211, v214, v215\n"
    "v_smfmac_f32_16x16x64_bf16 v[144:147], v[200:203], v[184:191], v5\n"
    "ds_write_b128 v4, v[208:211] offset:0\n"
    "v_smfmac_f32_16x16x64_bf16 v[148:151], v[200:203], v[192:199], v5\n"
    "v_cvt_pk_bf16_f32 v216, v216, v217\n"
    "v_cvt_pk_bf16_f32 v217, v218, v219\n"
    "v_smfmac_f32_16x16x64_bf16 v[128:131], v[204:207], v[56:63], v5\n"
    "ds_read_b32 v254, v6 offset:16\n"
    "ds_read_b32 v200, v6 offset:0\n"
    "v_smfmac_f32_16x16x64_bf16 v[132:135], v[204:207], v[64:71], v5\n"
    "v_cvt_pk_bf16_f32 v218, v220, v221\n"
    "s_waitcnt lgkmcnt(8)\n"
    "v_smfmac_f32_16x16x64_bf16 v[136:139], v[204:207], v[72:79], v5\n"
    "v_cvt_pk_bf16_f32 v219, v222, v223\n"
    "v_smfmac_f32_16x16x64_bf16 v[140:143], v[204:207], v[80:87], v5\n"
    "ds_write_b128 v4, v[216:219] offset:1024\n"
    "v_smfmac_f32_16x16x64_bf16 v[144:147], v[204:207], v[88:95], v5\n"
    "v_cvt_pk_bf16_f32 v224, v224, v225\n"
    "s_waitcnt lgkmcnt(4)\n"
    "v_smfmac_f32_16x16x64_bf16 v[148:151], v[204:207], v[96:103], v5\n"
    "v_cvt_pk_bf16_f32 v225, v226, v227\n"
    "v_cvt_pk_bf16_f32 v226, v228, v229\n"
    "s_setprio 0\n"
    "v_add_u32 v244, s48, v0\n"
    "v_add_u32 v245, s49, v0\n"
    "v_add_u32 v246, s50, v0\n"
    "v_add_u32 v247, s51, v0\n"
    "v_add_u32 v3, s68, v3\n"
    "v_cvt_pk_bf16_f32 v227, v230, v231\n"
    "ds_write_b128 v4, v[224:227] offset:2048\n"
    "s_waitcnt lgkmcnt(3)\n"
    "v_cmp_gt_u32 vcc, s55, v254\n"
    "s_waitcnt lgkmcnt(2)\n"
    "v_cmp_gt_u32 s[58:59], s54, v200\n"
    "s_add_u32 s56, s56, 4\n"
    "s_lshl_b32 s67, s67, 6\n"
    "s_add_u32 s63, s63, s67\n"
    "s_sub_u32 s53, s68, 18432\n"
    "s_add_u32 s67, s63, 192\n"
    "s_cmp_ge_u32 s45, 10\n"
    "s_cselect_b32 s53, s53, s68\n"
    "s_cselect_b32 s63, s67, s63\n"
    "s_cselect_b32 s56, -16, s56\n"
    "s_cmp_ge_u32 s45, 12\n"
    "s_cbranch_scc1 .La4\n"
    "s_or_b64 vcc, vcc, s[58:59]\n"
    "s_cbranch_scc1 .La4\n"
    "ds_read_b128 v[104:107], v3 offset:0\n"
    "ds_read_b128 v[152:155], v244 offset:0\n"
    "ds_read_b128 v[108:111], v3 offset:1024\n"
    "ds_read_b128 v[156:159], v244 offset:64\n"
    "ds_read_b128 v[112:115], v3 offset:2048\n"
    "ds_read_b128 v[160:163], v244 offset:128\n"
    "ds_read_b128 v[116:119], v3 offset:3072\n"
    "ds_read_b128 v[164:167], v244 offset:192\n"
    "ds_read_b128 v[120:123], v3 offset:4096\n"
    "ds_read_b128 v[124:127], v3 offset:5120\n"
    "v_cvt_pk_bf16_f32 v128, v128, v129\n"
    "v_cvt_pk_bf16_f32 v129, v130, v131\n"
    "v_cvt_pk_bf16_f32 v130, v132, v133\n"
    "v_cvt_pk_bf16_f32 v131, v134, v135\n"
    "ds_write_b128 v4, v[128:131] offset:3072\n"
    "v_cvt_pk_bf16_f32 v136, v136, v137\n"
    "v_cvt_pk_bf16_f32 v137, v138, v139\n"
    "v_cvt_pk_bf16_f32 v138, v140, v141\n"
    "v_cvt_pk_bf16_f32 v139, v142, v143\n"
    "ds_write_b128 v4, v[136:139] offset:4096\n"
    "v_cvt_pk_bf16_f32 v144, v144, v145\n"
    "v_cvt_pk_bf16_f32 v145, v146, v147\n"
    "v_cvt_pk_bf16_f32 v146, v148, v149\n"
    "v_cvt_pk_bf16_f32 v147, v150, v151\n"
    "ds_write_b128 v4, v[144:147] offset:5120\n"
    "ds_write_b32 v7, v252 offset:128\n"
    "ds_read_b128 v[128:131], v244 offset:256\n"
    "ds_read_b128 v[132:135], v244 offset:320\n"
    "ds_read_b128 v[136:139], v245 offset:0\n"
    "ds_read_b128 v[140:143], v245 offset:64\n"
    ".La4j:\n"
    "s_setprio 1\n"
    "s_add_u32 s57, s57, 1\n"
    "s_waitcnt lgkmcnt(8)\n"
    "v_mfma_f32_16x16x32_bf16 v[168:171], v[152:155], v[104:107], v[236:239]\n"
    "ds_read_b128 v[144:147], v245 offset:128\n"
    "v_mov_b32 v252, s57\n"
    "v_mfma_f32_16x16x32_bf16 v[168:171], v[156:159], v[108:111], v[168:171]\n"
    "ds_read_b128 v[148:151], v245 offset:192\n"
    "v_add_u32 v4, s53, v4\n"
    "v_mfma_f32_16x16x32_bf16 v[168:171], v[160:163], v[112:115], v[168:171]\n"
    "ds_read_b128 v[152:155], v245 offset:256\n"
    "s_waitcnt lgkmcnt(5)\n"
    "v_mfma_f32_16x16x32_bf16 v[168:171], v[164:167], v[116:119], v[168:171]\n"
    "ds_read_b128 v[156:159], v245 offset:320\n"
    "ds_write_b32 v7, v252 offset:192\n"
    "v_mfma_f32_16x16x32_bf16 v[168:171], v[128:131], v[120:123], v[168:171]\n"
    "ds_read_b128 v[160:163], v244 offset:8896\n"
    "ds_read_b128 v[208:211], v7 offset:32768\n"
    "v_mfma_f32_16x16x32_bf16 v[168:171], v[132:135], v[124:127], v[168:171]\n"
    "ds_read_b128 v[164:167], v244 offset:8960\n"
    "s_waitcnt lgkmcnt(7)\n"
    "v_mfma_f32_16x16x32_bf16 v[172:175], v[136:139], v[104:107], v[232:235]\n"
    "ds_read_b128 v[128:131], v244 offset:9024\n"
    "ds_read_b128 v[212:215], v7 offset:32768\n"
    "v_mfma_f32_16x16x32_bf16 v[172:175], v[140:143], v[108:111], v[172:175]\n"
    "ds_read_b128 v[132:135], v244 offset:9088\n"
    "ds_read_b128 v[216:219], v7 offset:32768\n"
    "v_mfma_f32_16x16x32_bf16 v[172:175], v[144:147], v[112:115], v[172:175]\n"
    "ds_read_b128 v[136:139], v244 offset:9152\n"
    "s_waitcnt lgkmcnt(9)\n"
    "v_mfma_f32_16x16x32_bf16 v[172:175], v[148:151], v[116:119], v[172:175]\n"
    "ds_read_b128 v[140:143], v244 offset:9216\n"
    "v_max_f32 v168, v168, v169\n"
    "v_mfma_f32_16x16x32_bf16 v[172:175], v[152:155], v[120:123], v[172:175]\n"
    "ds_read_b128 v[144:147], v245 offset:8896\n"
    "v_max_f32 v170, v170, v171\n"
    "v_mfma_f32_16x16x32_bf16 v[172:175], v[156:159], v[124:127], v[172:175]\n"
    "ds_read_b128 v[148:151], v245 offset:8960\n"
    "s_waitcnt lgkmcnt(7)\n"
    "v_mfma_f32_16x16x32_bf16 v[176:179], v[160:163], v[104:107], v[232:235]\n"
    "ds_read_b128 v[152:155], v245 offset:9024\n"
    "v_max_f32 v206, v168, v170\n"
    "v_mfma_f32_16x16x32_bf16 v[176:179], v[164:167], v[108:111], v[176:179]\n"
    "ds_read_b128 v[156:159], v245 offset:9088\n"
    "ds_read_b128 v[220:223], v7 offset:32768\n"
    "v_mfma_f32_16x16x32_bf16 v[176:179], v[128:131], v[112:115], v[176:179]\n"
    "ds_read_b128 v[160:163], v245 offset:9152\n"
    "s_waitcnt lgkmcnt(6)\n"
    "v_mfma_f32_16x16x32_bf16 v[176:179], v[132:135], v[116:119], v[176:179]\n"
    "ds_read_b128 v[164:167], v245 offset:9216\n"
    "v_max_f32 v172, v172, v173\n"
    "v_mfma_f32_16x16x32_bf16 v[176:179], v[136:139], v[120:123], v[176:179]\n"
    "ds_read_b128 v[128:131], v246 offset:0\n"
    "v_max_f32 v174, v174, v175\n"
    "v_mfma_f32_16x16x32_bf16 v[176:179], v[140:143], v[124:127], v[176:179]\n"
    "ds_read_b128 v[132:135], v246 offset:64\n"
    "s_waitcnt lgkmcnt(6)\n"
    "v_mfma_f32_16x16x32_bf16 v[180:183], v[144:147], v[104:107], v[232:235]\n"
    "ds_read_b128 v[136:139], v246 offset:128\n"
    "v_max3_f32 v206, v206, v172, v174\n"
    "v_mfma_f32_16x16x32_bf16 v[180:183], v[148:151], v[108:111], v[180:183]\n"
    "ds_read_b128 v[140:143], v246 offset:192\n"
    "ds_read_b128 v[224:227], v7 offset:32768\n"
    "v_mfma_f32_16x16x32_bf16 v[180:183], v[152:155], v[112:115], v[180:183]\n"
    "ds_read_b128 v[144:147], v246 offset:256\n"
    "s_waitcnt lgkmcnt(6)\n"
    "v_mfma_f32_16x16x32_bf16 v[180:183], v[156:159], v[116:119], v[180:183]\n"
    "ds_read_b128 v[148:151], v246 offset:320\n"
    "v_max_f32 v176, v176, v177\n"
    "v_mfma_f32_16x16x32_bf16 v[180:183], v[160:163], v[120:123], v[180:183]\n"
    "ds_read_b128 v[152:155], v247 offset:0\n"
    "v_max_f32 v178, v178, v179\n"
    "v_mfma_f32_16x16x32_bf16 v[180:183], v[164:167], v[124:127], v[180:183]\n"
    "ds_read_b128 v[156:159], v247 offset:64\n"
    "s_waitcnt lgkmcnt(6)\n"
    "v_mfma_f32_16x16x32_bf16 v[184:187], v[128:131], v[104:107], v[232:235]\n"
    "ds_read_b128 v[160:163], v247 offset:128\n"
    "v_max3_f32 v206, v206, v176, v178\n"
    "v_mfma_f32_16x16x32_bf16 v[184:187], v[132:135], v[108:111], v[184:187]\n"
    "ds_read_b128 v[164:167], v247 offset:192\n"
    "ds_read_b128 v[228:231], v7 offset:32768\n"
    "v_mfma_f32_16x16x32_bf16 v[184:187], v[136:139], v[112:115], v[184:187]\n"
    "ds_read_b128 v[128:131], v247 offset:256\n"
    "s_waitcnt lgkmcnt(6)\n"
    "v_mfma_f32_16x16x32_bf16 v[184:187], v[140:143], v[116:119], v[184:187]\n"
    "ds_read_b128 v[132:135], v247 offset:320\n"
    "v_max_f32 v180, v180, v181\n"
    "v_mfma_f32_16x16x32_bf16 v[184:187], v[144:147], v[120:123], v[184:187]\n"
    "ds_read_b128 v[136:139], v246 offset:8896\n"
    "v_max_f32 v182, v182, v183\n"
    "v_mfma_f32_16x16x32_bf16 v[184:187], v[148:151], v[124:127], v[184:187]\n"
    "ds_read_b128 v[140:143], v246 offset:8960\n"
    "s_waitcnt lgkmcnt(6)\n"
    "v_mfma_f32_16x16x32_bf16 v[188:191], v[152:155], v[104:107], v[232:235]\n"
    "ds_read_b128 v[144:147], v246 offset:9024\n"
    "v_max3_f32 v206, v206, v180, v182\n"
    "v_mfma_f32_16x16x32_bf16 v[188:191], v[156:159], v[108:111], v[188:191]\n"
    "ds_read_b128 v[148:151], v246 offset:9088\n"
    "v_mfma_f32_16x16x32_bf16 v[188:191], v[160:163], v[112:115], v[188:191]\n"
    "ds_read_b128 v[152:155], v246 offset:9152\n"
    "s_waitcnt lgkmcnt(5)\n"
    "v_mfma_f32_16x16x32_bf16 v[188:191], v[164:167], v[116:119], v[188:191]\n"
    "ds_read_b128 v[156:159], v246 offset:9216\n"
    "v_max_f32 v184, v184, v185\n"
    "v_mfma_f32_16x16x32_bf16 v[188:191], v[128:131], v[120:123], v[188:191]\n"
    "ds_read_b128 v[160:163], v247 offset:8896\n"
    "v_max_f32 v186, v186, v187\n"
    "v_mfma_f32_16x16x32_bf16 v[188:191], v[132:135], v[124:127], v[188:191]\n"
    "ds_read_b128 v[164:167], v247 offset:8960\n"
    "s_waitcnt lgkmcnt(5)\n"
    "v_mfma_f32_16x16x32_bf16 v[192:195], v[136:139], v[104:107], v[232:235]\n"
    "ds_read_b128 v[128:131], v247 offset:9024\n"
    "v_max3_f32 v206, v206, v184, v186\n"
    "v_mfma_f32_16x16x32_bf16 v[192:195], v[140:143], v[108:111], v[192:195]\n"
    "ds_read_b128 v[132:135], v247 offset:9088\n"
    "v_mfma_f32_16x16x32_bf16 v[192:195], v[144:147], v[112:115], v[192:195]\n"
    "ds_read_b128 v[136:139], v247 offset:9152\n"
    "s_waitcnt lgkmcnt(5)\n"
    "v_mfma_f32_16x16x32_bf16 v[192:195], v[148:151], v[116:119], v[192:195]\n"
    "ds_read_b128 v[140:143], v247 offset:9216\n"
    "v_max_f32 v188, v188, v189\n"
    "v_mfma_f32_16x16x32_bf16 v[192:195], v[152:155], v[120:123], v[192:195]\n"
    "v_add_u32 v244, s50, v1\n"
    "v_add_u32 v245, s51, v1\n"
    "v_mfma_f32_16x16x32_bf16 v[192:195], v[156:159], v[124:127], v[192:195]\n"
    "v_add_u32 v246, s50, v2\n"
    "s_waitcnt lgkmcnt(3)\n"
    "v_mfma_f32_16x16x32_bf16 v[196:199], v[160:163], v[104:107], v[240:243]\n"
    "v_add_u32 v247, s51, v2\n"
    "v_max_f32 v190, v190, v191\n"
    "v_mfma_f32_16x16x32_bf16 v[196:199], v[164:167], v[108:111], v[196:199]\n"
    "ds_read_b64_tr_b16 v[144:145], v244 offset:160\n"
    "v_max3_f32 v206, v206, v188, v190\n"
    "v_mfma_f32_16x16x32_bf16 v[196:199], v[128:131], v[112:115], v[196:199]\n"
    "ds_read_b64_tr_b16 v[146:147], v245 offset:160\n"
    "s_waitcnt lgkmcnt(2)\n"
    "v_mfma_f32_16x16x32_bf16 v[196:199], v[132:135], v[116:119], v[196:199]\n"
    "ds_read_b64_tr_b16 v[148:149], v246 offset:160\n"
    "v_max_f32 v192, v192, v193\n"
    "v_mfma_f32_16x16x32_bf16 v[196:199], v[136:139], v[120:123], v[196:199]\n"
    "ds_read_b64_tr_b16 v[150:151], v247 offset:160\n"
    "v_max_f32 v194, v194, v195\n"
    "v_mfma_f32_16x16x32_bf16 v[196:199], v[140:143], v[124:127], v[196:199]\n"
    "ds_read_b64_tr_b16 v[152:153], v244 offset:192\n"
    "ds_read_b64_tr_b16 v[154:155], v245 offset:192\n"
    "ds_write_b32 v7, v252 offset:64\n"
    "s_setprio 0\n"
    "v_max3_f32 v206, v206, v192, v194\n"
    "ds_read_b64_tr_b16 v[156:157], v246 offset:192\n"
    "ds_read_b64_tr_b16 v[158:159], v247 offset:192\n"
    "ds_read_b64_tr_b16 v[160:161], v244 offset:224\n"
    "v_max_f32 v196, v196, v197\n"
    "v_max_f32 v198, v198, v199\n"
    "v_max3_f32 v206, v206, v196, v198\n"
    "v_mov_b32 v207, v206\n"
    "ds_read_b64_tr_b16 v[162:163], v245 offset:224\n"
    "ds_read_b64_tr_b16 v[164:165], v246 offset:224\n"
    "v_permlane16_swap_b32 v206, v207\n"
    "v_max_f32 v206, v206, v207\n"
    "v_mov_b32 v207, v206\n"
    "ds_read_b64_tr_b16 v[166:167], v247 offset:224\n"
    "ds_read_b64_tr_b16 v[128:129], v244 offset:96\n"
    "v_permlane32_swap_b32 v206, v207\n"
    "v_max_f32 v206, v206, v207\n"
    "v_mul_f32 v207, s46, v206\n"
    "v_fma_f32 v168, v168, s47, v207\n"
    "v_fma_f32 v170, v170, s47, v207\n"
    "v_fma_f32 v172, v172, s47, v207\n"
    "v_fma_f32 v174, v174, s47, v207\n"
    "v_fma_f32 v176, v176, s47, v207\n"
    "v_fma_f32 v178, v178, s47, v207\n"
    "v_fma_f32 v180, v180, s47, v207\n"
    "v_fma_f32 v182, v182, s47, v207\n"
    "v_fma_f32 v184, v184, s47, v207\n"
    "v_fma_f32 v186, v186, s47, v207\n"
    "v_fma_f32 v188, v188, s47, v207\n"
    "v_fma_f32 v190, v190, s47, v207\n"
    "v_fma_f32 v192, v192, s47, v207\n"
    "v_fma_f32 v194, v194, s47, v207\n"
    "v_fma_f32 v196, v196, s47, v207\n"
    "v_fma_f32 v198, v198, s47, v207\n"
    "ds_read_b64_tr_b16 v[130:131], v245 offset:96\n"
    "ds_read_b64_tr_b16 v[132:133], v246 offset:96\n"
    "ds_read_b64_tr_b16 v[134:135], v247 offset:96\n"
    "ds_read_b64_tr_b16 v[136:137], v244 offset:128\n"
    "ds_read_b64_tr_b16 v[138:139], v245 offset:128\n"
    "ds_read_b64_tr_b16 v[140:141], v246 offset:128\n"
    "ds_read_b64_tr_b16 v[142:143], v247 offset:128\n"
    "ds_read_b64_tr_b16 v[104:105], v244 offset:0\n"
    "ds_read_b64_tr_b16 v[106:107], v245 offset:0\n"
    "ds_read_b64_tr_b16 v[108:109], v246 offset:0\n"
    "ds_read_b64_tr_b16 v[110:111], v247 offset:0\n"
    "ds_read_b64_tr_b16 v[112:113], v244 offset:32\n"
    "ds_read_b64_tr_b16 v[114:115], v245 offset:32\n"
    "ds_read_b64_tr_b16 v[116:117], v246 offset:32\n"
    "ds_read_b64_tr_b16 v[118:119], v247 offset:32\n"
    "ds_read_b64_tr_b16 v[120:121], v244 offset:64\n"
    "ds_read_b64_tr_b16 v[122:123], v245 offset:64\n"
    "ds_read_b64_tr_b16 v[124:125], v246 offset:64\n"
    "ds_read_b64_tr_b16 v[126:127], v247 offset:64\n"
    "ds_read_b32 v253, v6 offset:32\n"
    "v_exp_f32 v168, v168\n"
    "s_bfe_u32 s48, s60, 0x40000\n"
    "v_exp_f32 v170, v170\n"
    "s_bfe_u32 s49, s60, 0x40004\n"
    "v_exp_f32 v172, v172\n"
    "s_bfe_u32 s50, s60, 0x40008\n"
    "v_exp_f32 v174, v174\n"
    "s_bfe_u32 s51, s60, 0x4000c\n"
    "v_exp_f32 v176, v176\n"
    "s_mul_i32 s48, s48, 8896\n"
    "v_exp_f32 v178, v178\n"
    "s_mul_i32 s49, s49, 8896\n"
    "v_exp_f32 v180, v180\n"
    "s_mul_i32 s50, s50, 8896\n"
    "v_exp_f32 v182, v182\n"
    "s_mul_i32 s51, s51, 8896\n"
    "v_exp_f32 v184, v184\n"
    "s_lshr_b64 s[60:61], s[60:61], 8\n"
    "v_exp_f32 v186, v186\n"
    "s_add_u32 s45, s45, 4\n"
    "v_exp_f32 v188, v188\n"
    "s_add_u32 s54, s54, 2\n"
    "v_exp_f32 v190, v190\n"
    "s_add_u32 s55, s55, 4\n"
    "v_exp_f32 v192, v192\n"
    "s_cmp_eq_u32 s66, 2\n"
    "s_cselect_b32 s67, -2, 1\n"
    "v_exp_f32 v194, v194\n"
    "s_add_u32 s66, s66, s67\n"
    "v_exp_f32 v196, v196\n"
    "s_mul_i32 s68, s67, 6144\n"
    "v_exp_f32 v198, v198\n"
    "v_cvt_pk_bf16_f32 v200, v168, v170\n"
    "v_cvt_pk_bf16_f32 v201, v172, v174\n"
    "v_cvt_pk_bf16_f32 v202, v176, v178\n"
    "v_cvt_pk_bf16_f32 v203, v180, v182\n"
    "v_cvt_pk_bf16_f32 v204, v184, v186\n"
    "v_cvt_pk_bf16_f32 v205, v188, v190\n"
    "v_cvt_pk_bf16_f32 v206, v192, v194\n"
    "v_cvt_pk_bf16_f32 v207, v196, v198\n"
    "ds_read_b64_tr_b16 v[168:169], v244 offset:256\n"
    "ds_read_b64_tr_b16 v[170:171], v245 offset:256\n"
    "ds_read_b64_tr_b16 v[172:173], v246 offset:256\n"
    "ds_read_b64_tr_b16 v[174:175], v247 offset:256\n"
    "ds_read_b64_tr_b16 v[176:177], v244 offset:288\n"
    "ds_read_b64_tr_b16 v[178:179], v245 offset:288\n"
    "ds_read_b64_tr_b16 v[180:181], v246 offset:288\n"
    "ds_read_b64_tr_b16 v[182:183], v247 offset:288\n"
    "s_waitcnt lgkmcnt(8)\n"
    "s_setprio 1\n"
    "v_mfma_f32_16x16x32_bf16 v[196:199], v[248:251], v[200:203], 0\n"
    "v_cmp_gt_i32 vcc, s56, v253\n"
    "v_mfma_f32_16x16x32_bf16 v[196:199], v[248:251], v[204:207], v[196:199]\n"
    "v_smfmac_f32_16x16x64_bf16 v[220:223], v[200:203], v[32:39], v5\n"
    "ds_read_b128 v[32:35], v7 offset:32768\n"
    "v_smfmac_f32_16x16x64_bf16 v[224:227], v[200:203], v[40:47], v5\n"
    "ds_read_b128 v[36:39], v7 offset:32768\n"
    "v_smfmac_f32_16x16x64_bf16 v[228:231], v[200:203], v[48:55], v5\n"
    "ds_read_b128 v[40:43], v7 offset:32768\n"
    "v_smfmac_f32_16x16x64_bf16 v[208:211], v[200:203], v[8:15], v5\n"
    "ds_read_b128 v[44:47], v7 offset:32768\n"
    "v_smfmac_f32_16x16x64_bf16 v[212:215], v[200:203], v[16:23], v5\n"
    "ds_read_b128 v[48:51], v7 offset:32768\n"
    "v_smfmac_f32_16x16x64_bf16 v[216:219], v[200:203], v[24:31], v5\n"
    "ds_read_b128 v[52:55], v7 offset:32768\n"
    "v_smfmac_f32_16x16x64_bf16 v[208:211], v[204:207], v[104:111], v5\n"
    "ds_read_b64_tr_b16 v[184:185], v244 offset:320\n"
    "v_rcp_f32 v196, v196\n"
    "v_smfmac_f32_16x16x64_bf16 v[212:215], v[204:207], v[112:119], v5\n"
    "ds_read_b64_tr_b16 v[186:187], v245 offset:320\n"
    "v_lshrrev_b32 v197, 2, v3\n"
    "v_smfmac_f32_16x16x64_bf16 v[216:219], v[204:207], v[120:127], v5\n"
    "ds_read_b64_tr_b16 v[188:189], v246 offset:320\n"
    "v_and_or_b32 v197, v197, 60, s63\n"
    "v_smfmac_f32_16x16x64_bf16 v[220:223], v[204:207], v[128:135], v5\n"
    "ds_read_b64_tr_b16 v[190:191], v247 offset:320\n"
    "s_cbranch_vccnz .La7\n"
    ".La7b:\n"
    "v_smfmac_f32_16x16x64_bf16 v[224:227], v[204:207], v[136:143], v5\n"
    "ds_read_b64_tr_b16 v[192:193], v244 offset:352\n"
    "ds_read_b64_tr_b16 v[194:195], v245 offset:352\n"
    "v_smfmac_f32_16x16x64_bf16 v[228:231], v[204:207], v[144:151], v5\n"
    "s_waitcnt lgkmcnt(6)\n"
    "v_smfmac_f32_16x16x64_bf16 v[32:35], v[200:203], v[56:63], v5\n"
    "ds_write_b32 v197, v196 offset:0\n"
    "ds_read_b64_tr_b16 v[196:197], v246 offset:352\n"
    "v_smfmac_f32_16x16x64_bf16 v[36:39], v[200:203], v[64:71], v5\n"
    "ds_read_b64_tr_b16 v[198:199], v247 offset:352\n"
    "v_smfmac_f32_16x16x64_bf16 v[40:43], v[200:203], v[72:79], v5\n"
    "v_cvt_pk_bf16_f32 v208, v208, v209\n"
    "v_cvt_pk_bf16_f32 v209, v210, v211\n"
    "v_smfmac_f32_16x16x64_bf16 v[44:47], v[200:203], v[80:87], v5\n"
    "v_cvt_pk_bf16_f32 v210, v212, v213\n"
    "v_cvt_pk_bf16_f32 v211, v214, v215\n"
    "v_smfmac_f32_16x16x64_bf16 v[48:51], v[200:203], v[88:95], v5\n"
    "ds_write_b128 v4, v[208:211] offset:0\n"
    "v_smfmac_f32_16x16x64_bf16 v[52:55], v[200:203], v[96:103], v5\n"
    "v_cvt_pk_bf16_f32 v216, v216, v217\n"
    "v_cvt_pk_bf16_f32 v217, v218, v219\n"
    "v_smfmac_f32_16x16x64_bf16 v[32:35], v[204:207], v[152:159], v5\n"
    "ds_read_b32 v254, v6 offset:16\n"
    "ds_read_b32 v200, v6 offset:0\n"
    "v_smfmac_f32_16x16x64_bf16 v[36:39], v[204:207], v[160:167], v5\n"
    "v_cvt_pk_bf16_f32 v218, v220, v221\n"
    "s_waitcnt lgkmcnt(8)\n"
    "v_smfmac_f32_16x16x64_bf16 v[40:43], v[204:207], v[168:175], v5\n"
    "v_cvt_pk_bf16_f32 v219, v222, v223\n"
    "v_smfmac_f32_16x16x64_bf16 v[44:47], v[204:207], v[176:183], v5\n"
    "ds_write_b128 v4, v[216:219] offset:1024\n"
    "v_smfmac_f32_16x16x64_bf16 v[48:51], v[204:207], v[184:191], v5\n"
    "v_cvt_pk_bf16_f32 v224, v224, v225\n"
    "s_waitcnt lgkmcnt(4)\n"
    "v_smfmac_f32_16x16x64_bf16 v[52:55], v[204:207], v[192:199], v5\n"
    "v_cvt_pk_bf16_f32 v225, v226, v227\n"
    "v_cvt_pk_bf16_f32 v226, v228, v229\n"
    "s_setprio 0\n"
    "v_add_u32 v244, s48, v0\n"
    "v_add_u32 v245, s49, v0\n"
    "v_add_u32 v246, s50, v0\n"
    "v_add_u32 v247, s51, v0\n"
    "v_add_u32 v3, s68, v3\n"
    "v_cvt_pk_bf16_f32 v227, v230, v231\n"
    "ds_write_b128 v4, v[224:227] offset:2048\n"
    "s_waitcnt lgkmcnt(3)\n"
    "v_cmp_gt_u32 vcc, s55, v254\n"
    "s_waitcnt lgkmcnt(2)\n"
    "v_cmp_gt_u32 s[58:59], s54, v200\n"
    "s_add_u32 s56, s56, 4\n"
    "s_lshl_b32 s67, s67, 6\n"
    "s_add_u32 s63, s63, s67\n"
    "s_sub_u32 s53, s68, 18432\n"
    "s_add_u32 s67, s63, 192\n"
    "s_cmp_ge_u32 s45, 10\n"
    "s_cselect_b32 s53, s53, s68\n"
    "s_cselect_b32 s63, s67, s63\n"
    "s_cselect_b32 s56, -16, s56\n"
    "s_cmp_ge_u32 s45, 12\n"
    "s_cbranch_scc1 .La8\n"
    "s_or_b64 vcc, vcc, s[58:59]\n"
    "s_cbranch_scc1 .La8\n"
    "ds_read_b128 v[8:11], v3 offset:0\n"
    "ds_read_b128 v[56:59], v244 offset:0\n"
    "ds_read_b128 v[12:15], v3 offset:1024\n"
    "ds_read_b128 v[60:63], v244 offset:64\n"
    "ds_read_b128 v[16:19], v3 offset:2048\n"
    "ds_read_b128 v[64:67], v244 offset:128\n"
    "ds_read_b128 v[20:23], v3 offset:3072\n"
    "ds_read_b128 v[68:71], v244 offset:192\n"
    "ds_read_b128 v[24:27], v3 offset:4096\n"
    "ds_read_b128 v[28:31], v3 offset:5120\n"
    "v_cvt_pk_bf16_f32 v32, v32, v33\n"
    "v_cvt_pk_bf16_f32 v33, v34, v35\n"
    "v_cvt_pk_bf16_f32 v34, v36, v37\n"
    "v_cvt_pk_bf16_f32 v35, v38, v39\n"
    "ds_write_b128 v4, v[32:35] offset:3072\n"
    "v_cvt_pk_bf16_f32 v40, v40, v41\n"
    "v_cvt_pk_bf16_f32 v41, v42, v43\n"
    "v_cvt_pk_bf16_f32 v42, v44, v45\n"
    "v_cvt_pk_bf16_f32 v43, v46, v47\n"
    "ds_write_b128 v4, v[40:43] offset:4096\n"
    "v_cvt_pk_bf16_f32 v48, v48, v49\n"
    "v_cvt_pk_bf16_f32 v49, v50, v51\n"
    "v_cvt_pk_bf16_f32 v50, v52, v53\n"
    "v_cvt_pk_bf16_f32 v51, v54, v55\n"
    "ds_write_b128 v4, v[48:51] offset:5120\n"
    "ds_write_b32 v7, v252 offset:128\n"
    "ds_read_b128 v[32:35], v244 offset:256\n"
    "ds_read_b128 v[36:39], v244 offset:320\n"
    "ds_read_b128 v[40:43], v245 offset:0\n"
    "ds_read_b128 v[44:47], v245 offset:64\n"
    ".La8j:\n"
    "s_branch .La_loop\n"
    ".La_end:\n"
    "s_waitcnt lgkmcnt(0)\n"
    "s_endpgm\n"
    ".La_ld:\n"
    "s_mov_b32 s52, 0x40000\n"
    "ds_read_b32 v253, v6 offset:256\n"
    "s_waitcnt lgkmcnt(0)\n"
    "v_cmp_gt_u32 vcc, 1, v253\n"
    "s_cbranch_vccnz .Lb1\n"
    ".Lb1b:\n"
    "s_cmp_eq_u32 s70, 2\n"
    "s_cbranch_scc1 .La_l0\n"
    "s_cmp_eq_u32 s70, 3\n"
    "s_cbranch_scc1 .La_l1\n"
    "s_cmp_eq_u32 s70, 1\n"
    "s_cbranch_scc1 .La_l7\n"
    "ds_read_b32 v253, v6 offset:304\n"
    "s_waitcnt lgkmcnt(0)\n"
    "v_cmp_gt_u32 vcc, 1, v253\n"
    "s_cbranch_vccnz .Lb2\n"
    ".Lb2b:\n"
    ".La_l7:\n"
    "v_cmp_gt_u32 vcc, 2, v18\n"
    "v_mov_b32 v19, 0x100000\n"
    "v_mov_b32 v252, 1\n"
    "v_cndmask_b32 v254, v19, v6, vcc\n"
    "s_mul_i32 s48, s75, 9\n"
    "s_add_u32 s48, s48, s74\n"
    "s_add_u32 s50, s72, s48\n"
    "s_addc_u32 s51, s73, 0\n"
    "s_mov_b32 m0, 81088\n"
    "s_nop 0\n"
    "global_load_lds_dwordx4 v13, s[50:51]\n"
    "s_mov_b32 m0, 82112\n"
    "s_nop 0\n"
    "global_load_lds_dwordx4 v8, s[50:51]\n"
    "s_mov_b32 m0, 83136\n"
    "s_nop 0\n"
    "global_load_lds_dwordx4 v14, s[50:51]\n"
    "s_mov_b32 m0, 84160\n"
    "s_nop 0\n"
    "global_load_lds_dwordx4 v9, s[50:51]\n"
    "s_mov_b32 m0, 85184\n"
    "s_nop 0\n"
    "global_load_lds_dwordx4 v15, s[50:51]\n"
    "s_mov_b32 m0, 86208\n"
    "s_nop 0\n"
    "global_load_lds_dwordx4 v10, s[50:51]\n"
    "s_mov_b32 m0, 87232\n"
    "s_nop 0\n"
    "global_load_lds_dwordx4 v16, s[50:51]\n"
    "s_mov_b32 m0, 80064\n"
    "s_nop 0\n"
    "global_load_lds_dwordx4 v17, s[50:51]\n"
    "s_mov_b32 m0, 87840\n"
    "s_nop 0\n"
    "global_load_lds_dwordx4 v12, s[50:51]\n"
    "s_mul_i32 s48, s75, 10\n"
    "s_add_u32 s48, s48, s74\n"
    "s_add_u32 s50, s72, s48\n"
    "s_addc_u32 s51, s73, 0\n"
    "s_mov_b32 m0, 89984\n"
    "s_nop 0\n"
    "global_load_lds_dwordx4 v13, s[50:51]\n"
    "s_mov_b32 m0, 91008\n"
    "s_nop 0\n"
    "global_load_lds_dwordx4 v8, s[50:51]\n"
    "s_mov_b32 m0, 92032\n"
    "s_nop 0\n"
    "global_load_lds_dwordx4 v14, s[50:51]\n"
    "s_mov_b32 m0, 93056\n"
    "s_nop 0\n"
    "global_load_lds_dwordx4 v9, s[50:51]\n"
    "s_mov_b32 m0, 94080\n"
    "s_nop 0\n"
    "global_load_lds_dwordx4 v15, s[50:51]\n"
    "s_mov_b32 m0, 95104\n"
    "s_nop 0\n"
    "global_load_lds_dwordx4 v10, s[50:51]\n"
    "s_mov_b32 m0, 96128\n"
    "s_nop 0\n"
    "global_load_lds_dwordx4 v16, s[50:51]\n"
    "s_mov_b32 m0, 88960\n"
    "s_nop 0\n"
    "global_load_lds_dwordx4 v17, s[50:51]\n"
    "s_mov_b32 m0, 96736\n"
    "s_nop 0\n"
    "global_load_lds_dwordx4 v12, s[50:51]\n"
    "s_mul_i32 s48, s75, 11\n"
    "s_add_u32 s48, s48, s74\n"
    "s_add_u32 s50, s72, s48\n"
    "s_addc_u32 s51, s73, 0\n"
    "s_mov_b32 m0, 98880\n"
    "s_nop 0\n"
    "global_load_lds_dwordx4 v13, s[50:51]\n"
    "s_mov_b32 m0, 99904\n"
    "s_nop 0\n"
    "global_load_lds_dwordx4 v8, s[50:51]\n"
    "s_mov_b32 m0, 100928\n"
    "s_nop 0\n"
    "global_load_lds_dwordx4 v14, s[50:51]\n"
    "s_mov_b32 m0, 101952\n"
    "s_nop 0\n"
    "global_load_lds_dwordx4 v9, s[50:51]\n"
    "s_mov_b32 m0, 102976\n"
    "s_nop 0\n"
    "global_load_lds_dwordx4 v15, s[50:51]\n"
    "s_mov_b32 m0, 104000\n"
    "s_nop 0\n"
    "global_load_lds_dwordx4 v10, s[50:51]\n"
    "s_mov_b32 m0, 105024\n"
    "s_nop 0\n"
    "global_load_lds_dwordx4 v16, s[50:51]\n"
    "s_mov_b32 m0, 97856\n"
    "s_nop 0\n"
    "global_load_lds_dwordx4 v17, s[50:51]\n"
    "s_mov_b32 m0, 105632\n"
    "s_nop 0\n"
    "global_load_lds_dwordx4 v12, s[50:51]\n"
    "ds_write_b32 v6, v252 offset:288\n"
    "ds_read_b32 v253, v6 offset:0\n"
    "s_waitcnt lgkmcnt(0)\n"
    "v_cmp_gt_u32 vcc, 4, v253\n"
    "s_cbranch_vccnz .Lb3\n"
    ".Lb3b:\n"
    "s_waitcnt vmcnt(18)\n"
    "v_mov_b32 v252, 5\n"
    "ds_write_b32 v254, v252 offset:0\n"
    "s_waitcnt vmcnt(0)\n"
    "v_mov_b32 v252, 6\n"
    "ds_write_b32 v254, v252 offset:0\n"
    "s_branch .La_join\n"
    ".Lb1:\n"
    "s_sub_u32 s52, s52, 1\n"
    "s_cmp_lt_i32 s52, 0\n"
    "s_cbranch_scc1 .Lb1b\n"
    "ds_read_b32 v253, v6 offset:256\n"
    "s_waitcnt lgkmcnt(0)\n"
    "v_cmp_gt_u32 vcc, 1, v253\n"
    "s_cbranch_vccnz .Lb1\n"
    "s_branch .Lb1b\n"
    ".La_l0:\n"
    "s_mul_i32 s48, s75, 2\n"
    "s_add_u32 s48, s48, s74\n"
    "s_add_u32 s50, s72, s48\n"
    "s_addc_u32 s51, s73, 0\n"
    "s_mov_b32 m0, 18816\n"
    "s_nop 0\n"
    "global_load_lds_dwordx4 v8, s[50:51]\n"
    "s_mov_b32 m0, 20864\n"
    "s_nop 0\n"
    "global_load_lds_dwordx4 v9, s[50:51]\n"
    "s_mov_b32 m0, 22912\n"
    "s_nop 0\n"
    "global_load_lds_dwordx4 v10, s[50:51]\n"
    "s_mov_b32 m0, 24960\n"
    "s_nop 0\n"
    "global_load_lds_dwordx4 v11, s[50:51]\n"
    "s_mov_b32 m0, 17792\n"
    "s_nop 0\n"
    "global_load_lds_dwordx4 v12, s[50:51]\n"
    "s_mul_i32 s48, s75, 1\n"
    "s_add_u32 s48, s48, s74\n"
    "s_add_u32 s50, s72, s48\n"
    "s_addc_u32 s51, s73, 0\n"
    "s_mov_b32 m0, 9920\n"
    "s_nop 0\n"
    "global_load_lds_dwordx4 v8, s[50:51]\n"
    "s_mov_b32 m0, 11968\n"
    "s_nop 0\n"
    "global_load_lds_dwordx4 v9, s[50:51]\n"
    "s_mov_b32 m0, 14016\n"
    "s_nop 0\n"
    "global_load_lds_dwordx4 v10, s[50:51]\n"
    "s_mov_b32 m0, 16064\n"
    "s_nop 0\n"
    "global_load_lds_dwordx4 v11, s[50:51]\n"
    "s_mov_b32 m0, 8896\n"
    "s_nop 0\n"
    "global_load_lds_dwordx4 v12, s[50:51]\n"
    "s_mul_i32 s48, s75, 0\n"
    "s_add_u32 s48, s48, s74\n"
    "s_add_u32 s50, s72, s48\n"
    "s_addc_u32 s51, s73, 0\n"
    "s_mov_b32 m0, 1024\n"
    "s_nop 0\n"
    "global_load_lds_dwordx4 v8, s[50:51]\n"
    "s_mov_b32 m0, 3072\n"
    "s_nop 0\n"
    "global_load_lds_dwordx4 v9, s[50:51]\n"
    "s_mov_b32 m0, 5120\n"
    "s_nop 0\n"
    "global_load_lds_dwordx4 v10, s[50:51]\n"
    "s_mov_b32 m0, 7168\n"
    "s_nop 0\n"
    "global_load_lds_dwordx4 v11, s[50:51]\n"
    "s_mov_b32 m0, 0\n"
    "s_nop 0\n"
    "global_load_lds_dwordx4 v12, s[50:51]\n"
    "s_mul_i32 s48, s75, 3\n"
    "s_add_u32 s48, s48, s74\n"
    "s_add_u32 s50, s72, s48\n"
    "s_addc_u32 s51, s73, 0\n"
    "s_mov_b32 m0, 26688\n"
    "s_nop 0\n"
    "global_load_lds_dwordx4 v12, s[50:51]\n"
    "s_mul_i32 s48, s75, 4\n"
    "s_add_u32 s48, s48, s74\n"
    "s_add_u32 s50, s72, s48\n"
    "s_addc_u32 s51, s73, 0\n"
    "s_mov_b32 m0, 35584\n"
    "s_nop 0\n"
    "global_load_lds_dwordx4 v12, s[50:51]\n"
    "s_mul_i32 s48, s75, 5\n"
    "s_add_u32 s48, s48, s74\n"
    "s_add_u32 s50, s72, s48\n"
    "s_addc_u32 s51, s73, 0\n"
    "s_mov_b32 m0, 44480\n"
    "s_nop 0\n"
    "global_load_lds_dwordx4 v12, s[50:51]\n"
    "s_mul_i32 s48, s75, 6\n"
    "s_add_u32 s48, s48, s74\n"
    "s_add_u32 s50, s72, s48\n"
    "s_addc_u32 s51, s73, 0\n"
    "s_mov_b32 m0, 53376\n"
    "s_nop 0\n"
    "global_load_lds_dwordx4 v12, s[50:51]\n"
    "s_mul_i32 s48, s75, 7\n"
    "s_add_u32 s48, s48, s74\n"
    "s_add_u32 s50, s72, s48\n"
    "s_addc_u32 s51, s73, 0\n"
    "s_mov_b32 m0, 62272\n"
    "s_nop 0\n"
    "global_load_lds_dwordx4 v12, s[50:51]\n"
    "s_mul_i32 s48, s75, 8\n"
    "s_add_u32 s48, s48, s74\n"
    "s_add_u32 s50, s72, s48\n"
    "s_addc_u32 s51, s73, 0\n"
    "s_mov_b32 m0, 71168\n"
    "s_nop 0\n"
    "global_load_lds_dwordx4 v12, s[50:51]\n"
    "v_lshlrev_b32 v19, 20, v18\n"
    "v_add_u32 v19, 161408, v19\n"
    "v_mov_b32 v252, 1\n"
    "ds_write_b32 v19, v252 offset:304\n"
    "ds_write_b32 v19, v252 offset:312\n"
    "s_waitcnt vmcnt(0)\n"
    "v_mov_b32 v252, 4\n"
    "ds_write_b32 v19, v252 offset:0\n"
    "s_branch .La_join\n"
    ".La_l1:\n"
    "s_mul_i32 s48, s75, 2\n"
    "s_add_u32 s48, s48, s74\n"
    "s_add_u32 s50, s72, s48\n"
    "s_addc_u32 s51, s73, 0\n"
    "s_mov_b32 m0, 19840\n"
    "s_nop 0\n"
    "global_load_lds_dwordx4 v8, s[50:51]\n"
    "s_mov_b32 m0, 21888\n"
    "s_nop 0\n"
    "global_load_lds_dwordx4 v9, s[50:51]\n"
    "s_mov_b32 m0, 23936\n"
    "s_nop 0\n"
    "global_load_lds_dwordx4 v10, s[50:51]\n"
    "s_mov_b32 m0, 25568\n"
    "s_nop 0\n"
    "global_load_lds_dwordx4 v12, s[50:51]\n"
    "s_mul_i32 s48, s75, 1\n"
    "s_add_u32 s48, s48, s74\n"
    "s_add_u32 s50, s72, s48\n"
    "s_addc_u32 s51, s73, 0\n"
    "s_mov_b32 m0, 10944\n"
    "s_nop 0\n"
    "global_load_lds_dwordx4 v8, s[50:51]\n"
    "s_mov_b32 m0, 12992\n"
    "s_nop 0\n"
    "global_load_lds_dwordx4 v9, s[50:51]\n"
    "s_mov_b32 m0, 15040\n"
    "s_nop 0\n"
    "global_load_lds_dwordx4 v10, s[50:51]\n"
    "s_mov_b32 m0, 16672\n"
    "s_nop 0\n"
    "global_load_lds_dwordx4 v12, s[50:51]\n"
    "s_mul_i32 s48, s75, 0\n"
    "s_add_u32 s48, s48, s74\n"
    "s_add_u32 s50, s72, s48\n"
    "s_addc_u32 s51, s73, 0\n"
    "s_mov_b32 m0, 2048\n"
    "s_nop 0\n"
    "global_load_lds_dwordx4 v8, s[50:51]\n"
    "s_mov_b32 m0, 4096\n"
    "s_nop 0\n"
    "global_load_lds_dwordx4 v9, s[50:51]\n"
    "s_mov_b32 m0, 6144\n"
    "s_nop 0\n"
    "global_load_lds_dwordx4 v10, s[50:51]\n"
    "s_mov_b32 m0, 7776\n"
    "s_nop 0\n"
    "global_load_lds_dwordx4 v12, s[50:51]\n"
    "s_mul_i32 s48, s75, 3\n"
    "s_add_u32 s48, s48, s74\n"
    "s_add_u32 s50, s72, s48\n"
    "s_addc_u32 s51, s73, 0\n"
    "s_mov_b32 m0, 34464\n"
    "s_nop 0\n"
    "global_load_lds_dwordx4 v12, s[50:51]\n"
    "s_mul_i32 s48, s75, 4\n"
    "s_add_u32 s48, s48, s74\n"
    "s_add_u32 s50, s72, s48\n"
    "s_addc_u32 s51, s73, 0\n"
    "s_mov_b32 m0, 43360\n"
    "s_nop 0\n"
    "global_load_lds_dwordx4 v12, s[50:51]\n"
    "s_mul_i32 s48, s75, 5\n"
    "s_add_u32 s48, s48, s74\n"
    "s_add_u32 s50, s72, s48\n"
    "s_addc_u32 s51, s73, 0\n"
    "s_mov_b32 m0, 52256\n"
    "s_nop 0\n"
    "global_load_lds_dwordx4 v12, s[50:51]\n"
    "s_mul_i32 s48, s75, 6\n"
    "s_add_u32 s48, s48, s74\n"
    "s_add_u32 s50, s72, s48\n"
    "s_addc_u32 s51, s73, 0\n"
    "s_mov_b32 m0, 61152\n"
    "s_nop 0\n"
    "global_load_lds_dwordx4 v12, s[50:51]\n"
    "s_mul_i32 s48, s75, 7\n"
    "s_add_u32 s48, s48, s74\n"
    "s_add_u32 s50, s72, s48\n"
    "s_addc_u32 s51, s73, 0\n"
    "s_mov_b32 m0, 70048\n"
    "s_nop 0\n"
    "global_load_lds_dwordx4 v12, s[50:51]\n"
    "s_mul_i32 s48, s75, 8\n"
    "s_add_u32 s48, s48, s74\n"
    "s_add_u32 s50, s72, s48\n"
    "s_addc_u32 s51, s73, 0\n"
    "s_mov_b32 m0, 78944\n"
    "s_nop 0\n"
    "global_load_lds_dwordx4 v12, s[50:51]\n"
    "v_lshlrev_b32 v19, 20, v18\n"
    "v_add_u32 v19, 161412, v19\n"
    "v_mov_b32 v252, 1\n"
    "ds_write_b32 v19, v252 offset:304\n"
    "ds_write_b32 v19, v252 offset:312\n"
    "s_waitcnt vmcnt(0)\n"
    "v_mov_b32 v252, 4\n"
    "ds_write_b32 v19, v252 offset:0\n"
    "s_branch .La_join\n"
    ".Lb2:\n"
    "s_sub_u32 s52, s52, 1\n"
    "s_cmp_lt_i32 s52, 0\n"
    "s_cbranch_scc1 .Lb2b\n"
    "ds_read_b32 v253, v6 offset:304\n"
    "s_waitcnt lgkmcnt(0)\n"
    "v_cmp_gt_u32 vcc, 1, v253\n"
    "s_cbranch_vccnz .Lb2\n"
    "s_branch .Lb2b\n"
    ".Lb3:\n"
    "s_sub_u32 s52, s52, 1\n"
    "s_cmp_lt_i32 s52, 0\n"
    "s_cbranch_scc1 .Lb3b\n"
    "ds_read_b32 v253, v6 offset:0\n"
    "s_waitcnt lgkmcnt(0)\n"
    "v_cmp_gt_u32 vcc, 4, v253\n"
    "s_cbranch_vccnz .Lb3\n"
    "s_branch .Lb3b\n"
    ".La1:\n"
    "s_sub_u32 s52, s52, 1\n"
    "s_cmp_lt_i32 s52, 0\n"
    "s_cbranch_scc1 .La1b\n"
    "ds_read_b32 v253, v6 offset:0\n"
    "s_waitcnt lgkmcnt(0)\n"
    "v_cmp_gt_u32 vcc, s54, v253\n"
    "s_cbranch_vccnz .La1\n"
    "s_branch .La1b\n"
    ".La_fill:\n"
    "s_mov_b32 s69, 0\n"
    "ds_read_b64_tr_b16 v[104:105], v200 offset:0\n"
    "ds_read_b64_tr_b16 v[106:107], v201 offset:0\n"
    "ds_read_b64_tr_b16 v[108:109], v202 offset:0\n"
    "ds_read_b64_tr_b16 v[110:111], v203 offset:0\n"
    "ds_read_b64_tr_b16 v[112:113], v200 offset:32\n"
    "ds_read_b64_tr_b16 v[114:115], v201 offset:32\n"
    "ds_read_b64_tr_b16 v[116:117], v202 offset:32\n"
    "ds_read_b64_tr_b16 v[118:119], v203 offset:32\n"
    "ds_read_b64_tr_b16 v[120:121], v200 offset:64\n"
    "ds_read_b64_tr_b16 v[122:123], v201 offset:64\n"
    "ds_read_b64_tr_b16 v[124:125], v202 offset:64\n"
    "ds_read_b64_tr_b16 v[126:127], v203 offset:64\n"
    "ds_read_b64_tr_b16 v[128:129], v200 offset:96\n"
    "ds_read_b64_tr_b16 v[130:131], v201 offset:96\n"
    "ds_read_b64_tr_b16 v[132:133], v202 offset:96\n"
    "ds_read_b64_tr_b16 v[134:135], v203 offset:96\n"
    "ds_read_b64_tr_b16 v[136:137], v200 offset:128\n"
    "ds_read_b64_tr_b16 v[138:139], v201 offset:128\n"
    "ds_read_b64_tr_b16 v[140:141], v202 offset:128\n"
    "ds_read_b64_tr_b16 v[142:143], v203 offset:128\n"
    "ds_read_b64_tr_b16 v[144:145], v200 offset:160\n"
    "ds_read_b64_tr_b16 v[146:147], v201 offset:160\n"
    "ds_read_b64_tr_b16 v[148:149], v202 offset:160\n"
    "ds_read_b64_tr_b16 v[150:151], v203 offset:160\n"
    "ds_read_b64_tr_b16 v[152:153], v200 offset:192\n"
    "ds_read_b64_tr_b16 v[154:155], v201 offset:192\n"
    "ds_read_b64_tr_b16 v[156:157], v202 offset:192\n"
    "ds_read_b64_tr_b16 v[158:159], v203 offset:192\n"
    "ds_read_b64_tr_b16 v[160:161], v200 offset:224\n"
    "ds_read_b64_tr_b16 v[162:163], v201 offset:224\n"
    "ds_read_b64_tr_b16 v[164:165], v202 offset:224\n"
    "ds_read_b64_tr_b16 v[166:167], v203 offset:224\n"
    "ds_read_b64_tr_b16 v[168:169], v200 offset:256\n"
    "ds_read_b64_tr_b16 v[170:171], v201 offset:256\n"
    "ds_read_b64_tr_b16 v[172:173], v202 offset:256\n"
    "ds_read_b64_tr_b16 v[174:175], v203 offset:256\n"
    "ds_read_b64_tr_b16 v[176:177], v200 offset:288\n"
    "ds_read_b64_tr_b16 v[178:179], v201 offset:288\n"
    "ds_read_b64_tr_b16 v[180:181], v202 offset:288\n"
    "ds_read_b64_tr_b16 v[182:183], v203 offset:288\n"
    "ds_read_b64_tr_b16 v[184:185], v200 offset:320\n"
    "ds_read_b64_tr_b16 v[186:187], v201 offset:320\n"
    "ds_read_b64_tr_b16 v[188:189], v202 offset:320\n"
    "ds_read_b64_tr_b16 v[190:191], v203 offset:320\n"
    "ds_read_b64_tr_b16 v[192:193], v200 offset:352\n"
    "ds_read_b64_tr_b16 v[194:195], v201 offset:352\n"
    "ds_read_b64_tr_b16 v[196:197], v202 offset:352\n"
    "ds_read_b64_tr_b16 v[198:199], v203 offset:352\n"
    "s_branch .La_filled\n"
    ".La2:\n"
    "s_sub_u32 s52, s52, 1\n"
    "s_cmp_lt_i32 s52, 0\n"
    "s_cbranch_scc1 .La2b\n"
    "ds_read_b32 v254, v6 offset:16\n"
    "s_waitcnt lgkmcnt(0)\n"
    "v_cmp_gt_u32 vcc, s55, v254\n"
    "s_cbranch_vccnz .La2\n"
    "s_branch .La2b\n"
    ".La3:\n"
    "s_sub_u32 s52, s52, 1\n"
    "s_cmp_lt_i32 s52, 0\n"
    "s_cbranch_scc1 .La3b\n"
    "ds_read_b32 v253, v6 offset:32\n"
    "s_waitcnt lgkmcnt(0)\n"
    "v_cmp_gt_i32 vcc, s56, v253\n"
    "s_cbranch_vccnz .La3\n"
    "s_branch .La3b\n"
    ".La5:\n"
    "s_sub_u32 s52, s52, 1\n"
    "s_cmp_lt_i32 s52, 0\n"
    "s_cbranch_scc1 .La5b\n"
    "ds_read_b32 v200, v6 offset:0\n"
    "s_waitcnt lgkmcnt(0)\n"
    "v_cmp_gt_u32 vcc, s54, v200\n"
    "s_cbranch_vccnz .La5\n"
    "s_branch .La5b\n"
    ".La6:\n"
    "s_sub_u32 s52, s52, 1\n"
    "s_cmp_lt_i32 s52, 0\n"
    "s_cbranch_scc1 .La6b\n"
    "ds_read_b32 v254, v6 offset:16\n"
    "s_waitcnt lgkmcnt(0)\n"
    "v_cmp_gt_u32 vcc, s55, v254\n"
    "s_cbranch_vccnz .La6\n"
    "s_branch .La6b\n"
    ".La4:\n"
    "v_cvt_pk_bf16_f32 v128, v128, v129\n"
    "v_cvt_pk_bf16_f32 v129, v130, v131\n"
    "v_cvt_pk_bf16_f32 v130, v132, v133\n"
    "v_cvt_pk_bf16_f32 v131, v134, v135\n"
    "ds_write_b128 v4, v[128:131] offset:3072\n"
    "v_cvt_pk_bf16_f32 v136, v136, v137\n"
    "v_cvt_pk_bf16_f32 v137, v138, v139\n"
    "v_cvt_pk_bf16_f32 v138, v140, v141\n"
    "v_cvt_pk_bf16_f32 v139, v142, v143\n"
    "ds_write_b128 v4, v[136:139] offset:4096\n"
    "v_cvt_pk_bf16_f32 v144, v144, v145\n"
    "v_cvt_pk_bf16_f32 v145, v146, v147\n"
    "v_cvt_pk_bf16_f32 v146, v148, v149\n"
    "v_cvt_pk_bf16_f32 v147, v150, v151\n"
    "ds_write_b128 v4, v[144:147] offset:5120\n"
    "ds_write_b32 v7, v252 offset:128\n"
    "s_cmp_ge_u32 s45, 12\n"
    "s_cbranch_scc1 .La_end\n"
    "ds_read_b32 v200, v6 offset:0\n"
    "s_waitcnt lgkmcnt(0)\n"
    "v_cmp_gt_u32 vcc, s54, v200\n"
    "s_cbranch_vccnz .La5\n"
    ".La5b:\n"
    "ds_read_b32 v254, v6 offset:16\n"
    "s_waitcnt lgkmcnt(0)\n"
    "v_cmp_gt_u32 vcc, s55, v254\n"
    "s_cbranch_vccnz .La6\n"
    ".La6b:\n"
    "ds_read_b128 v[104:107], v3 offset:0\n"
    "ds_read_b128 v[152:155], v244 offset:0\n"
    "ds_read_b128 v[108:111], v3 offset:1024\n"
    "ds_read_b128 v[156:159], v244 offset:64\n"
    "ds_read_b128 v[112:115], v3 offset:2048\n"
    "ds_read_b128 v[160:163], v244 offset:128\n"
    "ds_read_b128 v[116:119], v3 offset:3072\n"
    "ds_read_b128 v[164:167], v244 offset:192\n"
    "ds_read_b128 v[120:123], v3 offset:4096\n"
    "ds_read_b128 v[124:127], v3 offset:5120\n"
    "ds_read_b128 v[128:131], v244 offset:256\n"
    "ds_read_b128 v[132:135], v244 offset:320\n"
    "ds_read_b128 v[136:139], v245 offset:0\n"
    "ds_read_b128 v[140:143], v245 offset:64\n"
    "s_branch .La4j\n"
    ".La7:\n"
    "s_sub_u32 s52, s52, 1\n"
    "s_cmp_lt_i32 s52, 0\n"
    "s_cbranch_scc1 .La7b\n"
    "ds_read_b32 v253, v6 offset:32\n"
    "s_waitcnt lgkmcnt(0)\n"
    "v_cmp_gt_i32 vcc, s56, v253\n"
    "s_cbranch_vccnz .La7\n"
    "s_branch .La7b\n"
    ".La9:\n"
    "s_sub_u32 s52, s52, 1\n"
    "s_cmp_lt_i32 s52, 0\n"
    "s_cbranch_scc1 .La9b\n"
    "ds_read_b32 v200, v6 offset:0\n"
    "s_waitcnt lgkmcnt(0)\n"
    "v_cmp_gt_u32 vcc, s54, v200\n"
    "s_cbranch_vccnz .La9\n"
    "s_branch .La9b\n"
    ".La10:\n"
    "s_sub_u32 s52, s52, 1\n"
    "s_cmp_lt_i32 s52, 0\n"
    "s_cbranch_scc1 .La10b\n"
    "ds_read_b32 v254, v6 offset:16\n"
    "s_waitcnt lgkmcnt(0)\n"
    "v_cmp_gt_u32 vcc, s55, v254\n"
    "s_cbranch_vccnz .La10\n"
    "s_branch .La10b\n"
    ".La8:\n"
    "v_cvt_pk_bf16_f32 v32, v32, v33\n"
    "v_cvt_pk_bf16_f32 v33, v34, v35\n"
    "v_cvt_pk_bf16_f32 v34, v36, v37\n"
    "v_cvt_pk_bf16_f32 v35, v38, v39\n"
    "ds_write_b128 v4, v[32:35] offset:3072\n"
    "v_cvt_pk_bf16_f32 v40, v40, v41\n"
    "v_cvt_pk_bf16_f32 v41, v42, v43\n"
    "v_cvt_pk_bf16_f32 v42, v44, v45\n"
    "v_cvt_pk_bf16_f32 v43, v46, v47\n"
    "ds_write_b128 v4, v[40:43] offset:4096\n"
    "v_cvt_pk_bf16_f32 v48, v48, v49\n"
    "v_cvt_pk_bf16_f32 v49, v50, v51\n"
    "v_cvt_pk_bf16_f32 v50, v52, v53\n"
    "v_cvt_pk_bf16_f32 v51, v54, v55\n"
    "ds_write_b128 v4, v[48:51] offset:5120\n"
    "ds_write_b32 v7, v252 offset:128\n"
    "s_cmp_ge_u32 s45, 12\n"
    "s_cbranch_scc1 .La_end\n"
    "ds_read_b32 v200, v6 offset:0\n"
    "s_waitcnt lgkmcnt(0)\n"
    "v_cmp_gt_u32 vcc, s54, v200\n"
    "s_cbranch_vccnz .La9\n"
    ".La9b:\n"
    "ds_read_b32 v254, v6 offset:16\n"
    "s_waitcnt lgkmcnt(0)\n"
    "v_cmp_gt_u32 vcc, s55, v254\n"
    "s_cbranch_vccnz .La10\n"
    ".La10b:\n"
    "ds_read_b128 v[8:11], v3 offset:0\n"
    "ds_read_b128 v[56:59], v244 offset:0\n"
    "ds_read_b128 v[12:15], v3 offset:1024\n"
    "ds_read_b128 v[60:63], v244 offset:64\n"
    "ds_read_b128 v[16:19], v3 offset:2048\n"
    "ds_read_b128 v[64:67], v244 offset:128\n"
    "ds_read_b128 v[20:23], v3 offset:3072\n"
    "ds_read_b128 v[68:71], v244 offset:192\n"
    "ds_read_b128 v[24:27], v3 offset:4096\n"
    "ds_read_b128 v[28:31], v3 offset:5120\n"
    "ds_read_b128 v[32:35], v244 offset:256\n"
    "ds_read_b128 v[36:39], v244 offset:320\n"
    "ds_read_b128 v[40:43], v245 offset:0\n"
    "ds_read_b128 v[44:47], v245 offset:64\n"
    "s_branch .La8j\n"
    ".La_last:\n"
            :: "{v[8:11]}"(xo), "{v12}"(xo4), "{v13}"(xq[0]), "{v14}"(xq[1]), "{v15}"(xq[2]), "{v16}"(xq[3]), "{v17}"(xq4), "{v18}"(lane),
               "{s70}"(UNI(wave >= 5 && t12 && (1 == 2 || (1 == 1 && (bid & 4))) ? wave - 3 : wave == 7)), "{s[72:73]}"(xb), "{s74}"(UNI(rm.o * IROWB)), "{s75}"(UNI(rm.d * IROWB)),
               "{v[0:3]}"(va), "{v[4:7]}"(vb), "{v[232:235]}"(cbM), "{v[236:239]}"(cbF), "{v[240:243]}"(cbL),
               "{s60}"(UNI(slots)), "{s61}"(UNI(slots >> 32)), "{s45}"(UNI(n0)), "{s54}"(UNI(u0 + 4)), "{s63}"(UNI((unsigned)(size_t)smem + LF + 320 + 64 * sl)), "{s66}"(UNI(sl)),
               "{s46}"(-0.10411754112863712f), "{s47}"(0.10411754112863712f)
            : "memory", "v254", "s48", "s49", "s50", "s51", "s52", "s53", "s55", "s56", "s57", "s58", "s59", "s62", "s67", "s68", "s69", "s88", "s89", "s90", "s91", "s98", "s99", "s92", "s93", "s94", "s95", "vcc", "scc");
    }
}

extern "C" void kernel_launch(void* const* d_in, const int* in_sizes, int n_in,
                              void* d_out, int out_size, void* d_ws, size_t ws_size,
                              hipStream_t stream) {
    na_main<<<256, 512, 0, stream>>>((const u16*)d_in[0], (const u16*)d_in[1], (const u16*)d_in[2], (u16*)d_out, (char*)d_ws + 0);
}
```

```cpp
#include <hip/hip_runtime.h>
#include <stdint.h>

typedef unsigned short u16;
typedef unsigned long long u64;
typedef __attribute__((ext_vector_type(4))) float f32x4;
typedef __attribute__((ext_vector_type(4))) unsigned u32x4;
typedef __attribute__((ext_vector_type(2))) unsigned u32x2;
typedef __attribute__((ext_vector_type(8))) unsigned u32x8;

#define DEVINL __device__ __forceinline__
#define LAMBDA_INL __attribute__((always_inline))
#define UNI(x) __builtin_amdgcn_readfirstlane((int)(x))

static constexpr int IMW = 224;
static constexpr int PIXB = 384;
static constexpr int IROWB = IMW * PIXB;
static constexpr int RP = 8896;
static constexpr int LQ = 14 * RP, LZ = LQ + 3 * 6144, LF = LZ + 3 * 6144;
static constexpr int LDS_TOT = 163840;
static constexpr unsigned OOB = 0x100000u;
static constexpr int WS_W = 8192, WS_FL = WS_W + 144 * 1024, NREP = 8, WS_ACK = 393216;

DEVINL int pixad(int c) { return 800 * ((c + 1) >> 1) + 384 * ((c + 1) & 1) - 384; }
struct AxisMap { int o, d, x, last; };
DEVINL int axis_at(const AxisMap& m, int v) {
    const int reg = m.o + m.d * v, ext = m.x + 2 * min(v - m.last - 1, 2), sel = (m.last - v) >> 31;
    return (ext & sel) | (reg & ~sel);
}
DEVINL int scol(int i) { return (int)((0xD5B3E6C4A2F79180ull >> (4 * i)) & 15); }

struct AttC { unsigned sb, v0, v1, pk; };
DEVINL AttC att_const(int lane, int H, int cedge) {
    const int i16 = lane & 15, g = lane >> 4, e = (i16 + 4) >> 3 & 1, qj = i16 - 4 * ((i16 + 4) >> 3), sc = scol(i16);
    const int ws0 = (cedge ? min(8 * H + qj, 9) : 8 * H + qj) - 8 * H;
    AttC c;
    c.pk = 8 | e << 8;
#pragma unroll
    for (int r = 0; r < 4; ++r) {
        const unsigned ok = (unsigned)(scol(4 * g + r) - ws0) <= 6u;
        c.pk |= ok << (4 + r) | (r & 1 ? ok << (r - 1) : 0u);
    }
    c.sb = pixad(8 * H + (sc < 14 ? sc : sc - 8)) + 16 * g;
    c.v0 = pixad(8 * H + scol(4 * (g >> 1) + ((lane >> 2) & 3))) + (g & 1) * RP + 8 * (lane & 3);
    c.v1 = pixad(8 * H + scol(8 + 4 * (g >> 1) + ((lane >> 2) & 3))) + (g & 1) * RP + 8 * (lane & 3);
    return c;
}

__global__ __launch_bounds__(512, 2) void na_main(const u16* __restrict__ x_, const u16* __restrict__ wqkv_,
                                                  const u16* __restrict__ wo_, u16* __restrict__ out_, char* ws_) {
    __shared__ __attribute__((aligned(1024))) char smem[LDS_TOT];
    int tid = threadIdx.x, bid = blockIdx.x;
    u32x8 ka;
    u32x2 ka4;
    u32x4 ax, xo, xq;
    unsigned xo4, xq4, t0;
    asm volatile(
    "s_load_dwordx8 s[8:15], s[0:1], 0x0\n"
    "s_load_dwordx2 s[16:17], s[0:1], 0x20\n"
    "s_memtime s[4:5]\n"
    "v_readfirstlane_b32 s7, v0\n"
    "s_lshr_b32 s24, s2, 3\n"
    "v_and_b32 v6, 63, v0\n"
    "s_getpc_b64 s[28:29]\n"
    ".Le_pc:\n"
    "s_lshl_b32 s25, s24, 10\n"
    "s_mov_b32 m0, 162816\n"
    "s_cmp_lg_u32 s7, 128\n"
    "s_cbranch_scc1 .Le_ncp\n"
    "v_lshl_add_u32 v7, v6, 4, s25\n"
    "global_load_lds_dwordx4 v7, s[28:29]\n"
    ".Le_ncp:\n"
    "s_lshr_b32 s27, s2, 6\n"
    "s_and_b32 s26, s24, 7\n"
    "s_lshr_b32 s7, s7, 6\n"
    "s_add_u32 s3, s27, 14\n"
    "s_cmp_eq_u32 s26, 7\n"
    "s_cselect_b32 s3, s3, 99\n"
    "s_cmp_lt_u32 s24, 16\n"
    "s_cselect_b32 s3, s24, s3\n"
    "s_cmp_lg_u32 s7, 7\n"
    "s_cbranch_scc1 .Le_np\n"
    "s_cmp_ge_u32 s3, 18\n"
    "s_cbranch_scc1 .Le_7\n"
    "s_and_b32 s23, s2, 3\n"
    "s_mul_i32 s23, s23, 18\n"
    "s_add_u32 s3, s3, s23\n"
    "s_mul_i32 s44, s3, 43\n"
    "s_lshr_b32 s44, s44, 8\n"
    "s_mul_i32 s25, s44, 6\n"
    "s_sub_u32 s25, s3, s25\n"
    "v_lshrrev_b32 v4, 2, v6\n"
    "v_lshrrev_b32 v5, 4, v6\n"
    "v_sub_u32 v3, 0, v5\n"
    "v_xor_b32 v3, v3, v6\n"
    "v_and_b32 v3, 3, v3\n"
    "v_lshlrev_b32 v3, 4, v3\n"
    "s_mul_i32 s25, s25, 36864\n"
    "s_movk_i32 s23, 1152\n"
    "s_lshr_b32 s42, s44, 1\n"
    "s_and_b32 s43, s44, 1\n"
    "v_mov_b32 v9, 0\n"
    "s_mov_b32 m0, 124544\n"
    "s_bitcmp1_b32 s2, 2\n"
    "s_cbranch_scc1 .Le_r2\n"
    "v_lshl_add_u32 v7, v5, 2, v4\n"
    "v_mad_u32_u24 v7, v7, s23, v3\n"
    "s_lshl3_add_u32 s44, s42, s43\n"
    "s_mul_i32 s44, s44, 4608\n"
    "s_addk_i32 s44, 384\n"
    "s_mov_b32 s23, 6\n"
    "s_waitcnt lgkmcnt(0)\n"
    "s_add_u32 s26, s10, s25\n"
    "s_addc_u32 s27, s11, 0\n"
    "global_load_dwordx2 v[12:13], v9, s[16:17] sc1\n"
    "s_add_u32 s28, s26, 4608\n"
    "s_addc_u32 s29, s27, 0\n"
    "s_add_u32 s30, s10, s44\n"
    "s_addc_u32 s31, s11, 0\n"
    ".Le_l1:\n"
    "global_load_lds_dwordx4 v7, s[26:27]\n"
    "s_addk_i32 m0, 1024\n"
    "s_sub_u32 s23, s23, 1\n"
    "global_load_lds_dwordx4 v7, s[28:29]\n"
    "s_addk_i32 m0, 1024\n"
    "s_nop 0\n"
    "global_load_lds_dwordx4 v7, s[30:31]\n"
    "s_addk_i32 m0, 1024\n"
    "s_cmp_lg_u32 s23, 0\n"
    "v_add_u32 v7, 64, v7\n"
    "s_cbranch_scc1 .Le_l1\n"
    "s_branch .Le_p1\n"
    ".Le_r2:\n"
    "v_bfe_u32 v100, v6, 2, 2\n"
    "v_and_b32 v7, 1, v5\n"
    "v_lshl_add_u32 v7, v7, 4, v100\n"
    "v_lshrrev_b32 v16, 5, v6\n"
    "v_lshl_add_u32 v7, v16, 3, v7\n"
    "v_mad_u32_u24 v7, v7, s23, v3\n"
    "v_and_b32 v19, 3, v6\n"
    "v_mul_u32_u24 v16, 24, v4\n"
    "v_add_lshl_u32 v16, v16, v19, 4\n"
    "s_lshl_b32 s31, s42, 6\n"
    "s_addk_i32 s25, 768\n"
    "s_mov_b32 s23, 6\n"
    "s_waitcnt lgkmcnt(0)\n"
    "s_add_u32 s26, s10, s25\n"
    "s_addc_u32 s27, s11, 0\n"
    "global_load_dwordx2 v[12:13], v9, s[16:17] sc1\n"
    "s_add_u32 s28, s26, 4608\n"
    "s_addc_u32 s29, s27, 0\n"
    "s_add_u32 s30, s12, s31\n"
    "s_addc_u32 s31, s13, 0\n"
    ".Le_l2:\n"
    "global_load_lds_dwordx4 v7, s[26:27]\n"
    "s_addk_i32 m0, 1024\n"
    "s_sub_u32 s23, s23, 1\n"
    "global_load_lds_dwordx4 v7, s[28:29]\n"
    "s_addk_i32 m0, 1024\n"
    "v_add_u32 v7, 64, v7\n"
    "global_load_lds_dwordx4 v16, s[30:31]\n"
    "s_addk_i32 m0, 1024\n"
    "v_add_u32 v16, 6144, v16\n"
    "global_load_lds_dwordx4 v16, s[30:31]\n"
    "s_addk_i32 m0, 1024\n"
    "v_add_u32 v16, 6144, v16\n"
    "s_cmp_lg_u32 s23, 0\n"
    "s_cbranch_scc1 .Le_l2\n"
    "s_branch .Le_p2\n"
    ".Le_np:\n"
    "s_cmp_eq_u32 s7, 4\n"
    "s_cbranch_scc1 .Le_a\n"
    ".Le_xo:\n"
    "s_and_b32 s25, s2, 1\n"
    "s_lshr_b32 s23, 0x14a, s7\n"
    "s_and_b32 s23, s23, 1\n"
    "s_lshl2_add_u32 s29, s25, s27\n"
    "s_mul_i32 s22, s23, 486\n"
    "s_cmp_eq_u32 s26, 7\n"
    "s_cselect_b32 s26, s29, s26\n"
    "s_cselect_b32 s31, 1, s27\n"
    "s_cselect_b32 s18, 1, 0\n"
    "s_cmp_eq_u32 s26, 7\n"
    "s_cbranch_scc1 .Le_sp\n"
    "v_lshl_add_u32 v1, s23, 6, v6\n"
    "v_add_u32 v5, s22, v6\n"
    "s_movk_i32 s22, 1311\n"
    "v_add_u32 v2, 64 + 24 + 128, v1\n"
    "v_add_u32 v3, 64 + 24 + 256, v1\n"
    "v_add_u32 v4, 64 + 24 + 384, v1\n"
    "v_add_u32 v1, 64 + 24, v1\n"
    "v_add_u32 v5, 24, v5\n"
    "v_mul_u32_u24 v7, s22, v1\n"
    "s_lshl_b32 s29, s26, 5\n"
    "v_mul_u32_u24 v8, s22, v2\n"
    "s_sub_u32 s20, 222, s29\n"
    "v_mul_u32_u24 v9, s22, v3\n"
    "s_add_u32 s29, s29, 1\n"
    "v_mul_u32_u24 v10, s22, v4\n"
    "s_bitcmp1_b32 s2, 1\n"
    "v_mul_u32_u24 v11, s22, v5\n"
    "s_cselect_b32 s20, s29, s20\n"
    "v_not_b32 v12, 49\n"
    "v_lshrrev_b32 v7, 16, v7\n"
    "s_cselect_b32 s21, 2, -2\n"
    "v_lshrrev_b32 v8, 16, v8\n"
    "s_cselect_b32 s30, 218, 1\n"
    "v_lshrrev_b32 v9, 16, v9\n"
    "v_lshrrev_b32 v10, 16, v10\n"
    "v_lshrrev_b32 v11, 16, v11\n"
    "s_sub_u32 s29, s20, s21\n"
    "s_mul_i32 s29, s29, 384\n"
    "s_mul_i32 s22, s21, 768\n"
    "v_mad_i32_i24 v1, v7, v12, v1\n"
    "s_lshl_b32 s28, s27, 3\n"
    "v_mad_i32_i24 v2, v8, v12, v2\n"
    "s_lshr_b32 s28, 0x1837321d, s28\n"
    "v_mad_i32_i24 v3, v9, v12, v3\n"
    "s_and_b32 s28, s28, 0xff\n"
    "v_mad_i32_i24 v4, v10, v12, v4\n"
    "v_mad_i32_i24 v5, v11, v12, v5\n"
    "v_mov_b32 v18, s29\n"
    "s_add_u32 s29, s21, -1\n"
    "s_mul_i32 s29, s29, 384\n"
    "v_add_u32 v19, s29, v18\n"
    "v_min_u32 v1, 47, v1\n"
    "s_mul_i32 s29, s25, 50\n"
    "v_min_u32 v2, 47, v2\n"
    "s_add_u32 s28, s28, s29\n"
    "v_min_u32 v3, 47, v3\n"
    "s_cmp_lg_u32 s18, 0\n"
    "v_min_u32 v4, 47, v4\n"
    "s_cselect_b32 s28, 0, s28\n"
    "v_min_u32 v5, 47, v5\n"
    "v_cmp_lt_u32 s[42:43], 23, v1\n"
    "v_cmp_lt_u32 s[44:45], 23, v2\n"
    "v_cmp_lt_u32 s[46:47], 23, v3\n"
    "v_cmp_lt_u32 s[48:49], 23, v4\n"
    "v_cmp_lt_u32 s[50:51], 23, v5\n"
    "s_lshl_b32 s28, s28, 1\n"
    "s_sub_u32 s18, 222, s28\n"
    "s_add_u32 s28, s28, 1\n"
    "s_lshr_b32 s29, s2, 2\n"
    "v_cndmask_b32 v13, v18, v19, s[42:43]\n"
    "v_cndmask_b32 v14, v18, v19, s[44:45]\n"
    "v_cndmask_b32 v15, v18, v19, s[46:47]\n"
    "v_cndmask_b32 v16, v18, v19, s[48:49]\n"
    "v_cndmask_b32 v17, v18, v19, s[50:51]\n"
    "s_xor_b32 s31, s29, s31\n"
    "s_bitcmp1_b32 s2, 2\n"
    "s_cselect_b32 s18, s28, s18\n"
    "v_mad_i32_i24 v13, v7, s22, v13\n"
    "v_mad_i32_i24 v14, v8, s22, v14\n"
    "v_mad_i32_i24 v15, v9, s22, v15\n"
    "v_mad_i32_i24 v16, v10, s22, v16\n"
    "v_mad_i32_i24 v17, v11, s22, v17\n"
    "s_bitcmp1_b32 s31, 0\n"
    "s_cselect_b32 s19, -2, 2\n"
    "s_cmp_eq_u32 s26, 6\n"
    "s_cbranch_scc1 .Le_ce\n"
    ".Le_cd:\n"
    "v_lshl_add_u32 v1, v1, 4, v13\n"
    "v_lshl_add_u32 v2, v2, 4, v14\n"
    "v_lshl_add_u32 v3, v3, 4, v15\n"
    "v_lshl_add_u32 v4, v4, 4, v16\n"
    "v_lshl_add_u32 v5, v5, 4, v17\n"
    "s_cmp_lt_u32 s7, 4\n"
    "s_cbranch_scc1 .Le_b\n"
    "s_cmp_ge_u32 s7, 7\n"
    "s_cbranch_scc1 .Le_72\n"
    "s_mul_i32 s25, s19, 3\n"
    "s_add_u32 s25, s25, s18\n"
    "s_mul_i32 s25, s25, 86016\n"
    "s_mul_i32 s28, s19, 86016\n"
    "s_lshl_b32 s24, s23, 10\n"
    "s_addk_i32 s24, 27712\n"
    "s_mov_b32 s29, 5\n"
    "s_cmp_lt_u32 s3, 18\n"
    "s_cselect_b32 s31, 2, -1\n"
    "s_cselect_b32 s30, 2, 0\n"
    "s_bitcmp1_b32 s2, 2\n"
    "s_cselect_b32 s31, -1, s31\n"
    "s_cselect_b32 s30, s30, 0\n"
    "s_or_b32 s30, s30, s23\n"
    "s_barrier\n"
    "s_waitcnt lgkmcnt(0)\n"
    ".Le_xl:\n"
    "s_cmp_eq_u32 s29, s31\n"
    "s_cbranch_scc1 .Le_h\n"
    ".Le_go:\n"
    "s_mov_b32 m0, s24\n"
    "s_add_u32 s26, s8, s25\n"
    "s_addc_u32 s27, s9, 0\n"
    "global_load_lds_dwordx4 v1, s[26:27]\n"
    "s_addk_i32 m0, 2048\n"
    "s_add_u32 s25, s25, s28\n"
    "global_load_lds_dwordx4 v2, s[26:27]\n"
    "s_addk_i32 m0, 2048\n"
    "s_add_u32 s24, s24, 8896\n"
    "global_load_lds_dwordx4 v3, s[26:27]\n"
    "s_addk_i32 m0, 2048\n"
    "s_cmp_lg_u32 s23, 0\n"
    "s_cbranch_scc1 .Le_x3\n"
    "global_load_lds_dwordx4 v4, s[26:27]\n"
    ".Le_x3:\n"
    "s_sub_u32 s29, s29, 1\n"
    "s_cmp_lg_u32 s29, 0\n"
    "s_cbranch_scc1 .Le_xl\n"
    "s_branch .Le_g2s\n"
    ".Le_h:\n"
    "v_mov_b32 v16, 161456\n"
    "ds_read_b32 v7, v16\n"
    "s_waitcnt lgkmcnt(0)\n"
    "v_cmp_gt_u32 vcc, 1, v7\n"
    "s_cbranch_vccnz .Le_h\n"
    "s_branch .Le_go\n"
    ".Le_a:\n"
    "s_add_u32 s25, s25, 32768\n"
    "s_mov_b32 s23, .Lg_last-.Le_pc\n"
    "s_max_u32 s23, s23, .Lh_last-.Le_pc\n"
    "s_max_u32 s23, s23, .La_last-.Le_pc\n"
    "s_max_u32 s23, s23, .Ls_last-.Le_pc\n"
    "s_min_u32 s25, s25, s23\n"
    "v_lshl_add_u32 v7, v6, 4, s25\n"
    "global_load_lds_dwordx4 v7, s[28:29]\n"
    "s_cmp_lt_u32 s3, 18\n"
    "s_cselect_b32 s23, 0, 8\n"
    "v_lshrrev_b32 v10, 1, v6\n"
    "v_cmp_eq_u32 vcc, 1, v10\n"
    "v_lshrrev_b32 v7, 2, v6\n"
    "v_lshrrev_b32 v8, v7, s23\n"
    "v_mov_b32 v9, 0\n"
    "v_and_b32 v8, 1, v8\n"
    "v_lshlrev_b32 v8, 1, v8\n"
    "v_cndmask_b32 v8, v8, 7, vcc\n"
    "v_cmp_eq_u32 vcc, 5, v10\n"
    "v_lshlrev_b32 v7, 2, v6\n"
    "v_add_u32 v7, 161408, v7\n"
    "v_cndmask_b32 v8, v8, 15, vcc\n"
    "ds_write_b32 v7, v9 offset:64\n"
    "ds_write_b32 v7, v9 offset:768\n"
    "ds_write_b32 v7, v8\n"
    "s_waitcnt lgkmcnt(0)\n"
    "s_branch .Le_b\n"
    ".Le_ce:\n"
    "s_movk_i32 s29, 384\n"
    "v_cndmask_b32 v20, 0, 1, s[42:43]\n"
    "v_lshl_add_u32 v21, v7, 1, v20\n"
    "v_cmp_lt_u32 vcc, 16, v21\n"
    "v_add_u32 v22, -17, v21\n"
    "v_min_u32 v22, 2, v22\n"
    "v_lshl_add_u32 v22, v22, 1, s30\n"
    "v_sub_u32 v22, v22, v20\n"
    "v_mul_i32_i24 v22, s29, v22\n"
    "v_cndmask_b32 v13, v13, v22, vcc\n"
    "v_cndmask_b32 v20, 0, 1, s[44:45]\n"
    "v_lshl_add_u32 v21, v8, 1, v20\n"
    "v_cmp_lt_u32 vcc, 16, v21\n"
    "v_add_u32 v22, -17, v21\n"
    "v_min_u32 v22, 2, v22\n"
    "v_lshl_add_u32 v22, v22, 1, s30\n"
    "v_sub_u32 v22, v22, v20\n"
    "v_mul_i32_i24 v22, s29, v22\n"
    "v_cndmask_b32 v14, v14, v22, vcc\n"
    "v_cndmask_b32 v20, 0, 1, s[46:47]\n"
    "v_lshl_add_u32 v21, v9, 1, v20\n"
    "v_cmp_lt_u32 vcc, 16, v21\n"
    "v_add_u32 v22, -17, v21\n"
    "v_min_u32 v22, 2, v22\n"
    "v_lshl_add_u32 v22, v22, 1, s30\n"
    "v_sub_u32 v22, v22, v20\n"
    "v_mul_i32_i24 v22, s29, v22\n"
    "v_cndmask_b32 v15, v15, v22, vcc\n"
    "v_cndmask_b32 v20, 0, 1, s[48:49]\n"
    "v_lshl_add_u32 v21, v10, 1, v20\n"
    "v_cmp_lt_u32 vcc, 16, v21\n"
    "v_add_u32 v22, -17, v21\n"
    "v_min_u32 v22, 2, v22\n"
    "v_lshl_add_u32 v22, v22, 1, s30\n"
    "v_sub_u32 v22, v22, v20\n"
    "v_mul_i32_i24 v22, s29, v22\n"
    "v_cndmask_b32 v16, v16, v22, vcc\n"
    "v_cndmask_b32 v20, 0, 1, s[50:51]\n"
    "v_lshl_add_u32 v21, v11, 1, v20\n"
    "v_cmp_lt_u32 vcc, 16, v21\n"
    "v_add_u32 v22, -17, v21\n"
    "v_min_u32 v22, 2, v22\n"
    "v_lshl_add_u32 v22, v22, 1, s30\n"
    "v_sub_u32 v22, v22, v20\n"
    "v_mul_i32_i24 v22, s29, v22\n"
    "v_cndmask_b32 v17, v17, v22, vcc\n"
    "s_branch .Le_cd\n"
    ".Le_p1:\n"
    "s_barrier\n"
    "v_bfe_u32 v8, v6, 2, 2\n"
    "v_sub_u32 v8, 0, v8\n"
    "v_xor_b32 v8, v8, v5\n"
    "v_and_b32 v8, 3, v8\n"
    "v_lshlrev_b32 v8, 4, v8\n"
    "v_and_b32 v10, 15, v6\n"
    "v_lshl_add_u32 v8, v10, 6, v8\n"
    "v_add_u32 v8, 124544, v8\n"
    "s_waitcnt vmcnt(9)\n"
    "v_add_u32 v12, 0x7f4a7c15, v12\n"
    "v_add_u32 v13, 0x9e3779b9, v13\n"
    "s_lshl_b32 s23, s3, 10\n"
    "s_add_u32 s23, s23, 8192\n"
    "v_lshl_add_u32 v9, v6, 4, s23\n"
    "s_lshl_b32 s23, s3, 7\n"
    "s_add_u32 s23, s23, 155648\n"
    "v_and_b32 v11, 7, v6\n"
    "v_mov_b32 v17, 2\n"
    "ds_read_b128 v[20:23], v8 offset:0\n"
    "ds_read_b128 v[24:27], v8 offset:1024\n"
    "ds_read_b128 v[28:31], v8 offset:2048\n"
    "ds_read_b128 v[32:35], v8 offset:3072\n"
    "ds_read_b128 v[36:39], v8 offset:4096\n"
    "ds_read_b128 v[40:43], v8 offset:5120\n"
    "ds_read_b128 v[44:47], v8 offset:6144\n"
    "ds_read_b128 v[48:51], v8 offset:7168\n"
    "ds_read_b128 v[52:55], v8 offset:8192\n"
    "s_waitcnt vmcnt(6)\n"
    "ds_read_b128 v[56:59], v8 offset:9216\n"
    "ds_read_b128 v[60:63], v8 offset:10240\n"
    "ds_read_b128 v[64:67], v8 offset:11264\n"
    "s_waitcnt lgkmcnt(9)\n"
    "v_mfma_f32_16x16x32_bf16 v[92:95], v[20:23], v[28:31], 0\n"
    "v_mfma_f32_16x16x32_bf16 v[96:99], v[24:27], v[28:31], 0\n"
    "s_waitcnt lgkmcnt(6)\n"
    "v_mfma_f32_16x16x32_bf16 v[92:95], v[32:35], v[40:43], v[92:95]\n"
    "v_mfma_f32_16x16x32_bf16 v[96:99], v[36:39], v[40:43], v[96:99]\n"
    "s_waitcnt vmcnt(3)\n"
    "ds_read_b128 v[68:71], v8 offset:12288\n"
    "ds_read_b128 v[72:75], v8 offset:13312\n"
    "ds_read_b128 v[76:79], v8 offset:14336\n"
    "s_waitcnt lgkmcnt(6)\n"
    "v_mfma_f32_16x16x32_bf16 v[92:95], v[44:47], v[52:55], v[92:95]\n"
    "v_mfma_f32_16x16x32_bf16 v[96:99], v[48:51], v[52:55], v[96:99]\n"
    "s_waitcnt lgkmcnt(3)\n"
    "v_mfma_f32_16x16x32_bf16 v[92:95], v[56:59], v[64:67], v[92:95]\n"
    "v_mfma_f32_16x16x32_bf16 v[96:99], v[60:63], v[64:67], v[96:99]\n"
    "s_waitcnt vmcnt(0)\n"
    "ds_read_b128 v[80:83], v8 offset:15360\n"
    "ds_read_b128 v[84:87], v8 offset:16384\n"
    "ds_read_b128 v[88:91], v8 offset:17408\n"
    "s_waitcnt lgkmcnt(3)\n"
    "v_mfma_f32_16x16x32_bf16 v[92:95], v[68:71], v[76:79], v[92:95]\n"
    "v_mfma_f32_16x16x32_bf16 v[96:99], v[72:75], v[76:79], v[96:99]\n"
    "s_waitcnt lgkmcnt(0)\n"
    "v_mfma_f32_16x16x32_bf16 v[92:95], v[80:83], v[88:91], v[92:95]\n"
    "v_mfma_f32_16x16x32_bf16 v[96:99], v[84:87], v[88:91], v[96:99]\n"
    "s_nop 7\n"
    "s_branch .Le_pub1\n"
    ".Le_pub:\n"
    "s_lshl_b32 s23, s3, 10\n"
    "s_add_u32 s23, s23, 8192\n"
    "v_lshl_add_u32 v9, v6, 4, s23\n"
    "s_lshl_b32 s23, s3, 7\n"
    "s_add_u32 s23, s23, 155648\n"
    "v_and_b32 v11, 7, v6\n"
    "v_mov_b32 v17, 2\n"
    "s_nop 1\n"
    ".Le_pub1:\n"
    "v_cvt_pk_bf16_f32 v92, v92, v93\n"
    "v_cvt_pk_bf16_f32 v93, v94, v95\n"
    "v_cvt_pk_bf16_f32 v94, v96, v97\n"
    "v_cvt_pk_bf16_f32 v95, v98, v99\n"
    "v_add_u32 v92, v92, v12\n"
    "global_store_dwordx4 v9, v[92:95], s[16:17] sc1\n"
    "v_add_u32 v14, v92, v95\n"
    "v_add_u32 v15, v92, v95\n"
    "v_lshlrev_b32 v18, 2, v11\n"
    "v_and_b32 v18, 12, v18\n"
    "v_add_u32 v18, 161456, v18\n"
    "v_lshrrev_b32 v10, 3, v6\n"
    "ds_write_b32 v18, v17\n"
    "v_mul_u32_u24 v10, 18432, v10\n"
    "v_lshl_add_u32 v10, v11, 4, v10\n"
    "v_add_u32 v10, s23, v10\n"
    "v_permlane32_swap_b32 v14, v15\n"
    "v_add_u32 v14, v14, v15\n"
    "s_nop 1\n"
    "v_add_u32_dpp v14, v14, v14 row_ror:8 row_mask:0xf bank_mask:0xf\n"
    "s_nop 1\n"
    "v_add_u32_dpp v14, v14, v14 row_ror:4 row_mask:0xf bank_mask:0xf\n"
    "s_nop 1\n"
    "v_add_u32_dpp v14, v14, v14 row_ror:2 row_mask:0xf bank_mask:0xf\n"
    "s_nop 1\n"
    "v_add_u32_dpp v14, v14, v14 row_ror:1 row_mask:0xf bank_mask:0xf\n"
    "v_mov_b32 v15, v14\n"
    "s_nop 1\n"
    "v_permlane16_swap_b32 v14, v15\n"
    "v_add_u32 v14, v14, v15\n"
    "global_store_dwordx4 v10, v[12:15], s[16:17] sc1\n"
    "s_branch .Le_71\n"
    ".Le_p2:\n"
    "s_barrier\n"
    "v_bfe_u32 v8, v6, 2, 2\n"
    "v_sub_u32 v8, 0, v8\n"
    "v_xor_b32 v8, v8, v5\n"
    "v_and_b32 v8, 3, v8\n"
    "v_lshlrev_b32 v8, 4, v8\n"
    "v_and_b32 v10, 15, v6\n"
    "v_lshl_add_u32 v8, v10, 6, v8\n"
    "v_add_u32 v8, 124544, v8\n"
    "v_lshl_add_u32 v7, v5, 3, v100\n"
    "v_lshlrev_b32 v7, 6, v7\n"
    "v_lshl_add_u32 v7, v19, 4, v7\n"
    "s_lshl_b32 s23, s43, 3\n"
    "s_add_u32 s23, s23, 126592\n"
    "v_add_u32 v7, s23, v7\n"
    "s_add_u32 s3, s3, 72\n"
    "s_waitcnt vmcnt(0)\n"
    "v_add_u32 v12, 0x7f4a7c15, v12\n"
    "v_add_u32 v13, 0x9e3779b9, v13\n"
    "ds_read_b128 v[20:23], v8 offset:0\n"
    "ds_read_b128 v[24:27], v8 offset:1024\n"
    "ds_read_b64_tr_b16 v[28:29], v7 offset:0\n"
    "ds_read_b64_tr_b16 v[30:31], v7 offset:256\n"
    "ds_read_b128 v[32:35], v8 offset:4096\n"
    "ds_read_b128 v[36:39], v8 offset:5120\n"
    "ds_read_b64_tr_b16 v[40:41], v7 offset:4096\n"
    "ds_read_b64_tr_b16 v[42:43], v7 offset:4352\n"
    "ds_read_b128 v[44:47], v8 offset:8192\n"
    "ds_read_b128 v[48:51], v8 offset:9216\n"
    "ds_read_b64_tr_b16 v[52:53], v7 offset:8192\n"
    "ds_read_b64_tr_b16 v[54:55], v7 offset:8448\n"
    "ds_read_b128 v[56:59], v8 offset:12288\n"
    "ds_read_b128 v[60:63], v8 offset:13312\n"
    "ds_read_b64_tr_b16 v[64:65], v7 offset:12288\n"
    "ds_read_b64_tr_b16 v[66:67], v7 offset:12544\n"
    "ds_read_b128 v[68:71], v8 offset:16384\n"
    "ds_read_b128 v[72:75], v8 offset:17408\n"
    "ds_read_b64_tr_b16 v[76:77], v7 offset:16384\n"
    "ds_read_b64_tr_b16 v[78:79], v7 offset:16640\n"
    "ds_read_b128 v[80:83], v8 offset:20480\n"
    "ds_read_b128 v[84:87], v8 offset:21504\n"
    "ds_read_b64_tr_b16 v[88:89], v7 offset:20480\n"
    "ds_read_b64_tr_b16 v[90:91], v7 offset:20736\n"
    "s_waitcnt lgkmcnt(14)\n"
    "v_mfma_f32_16x16x32_bf16 v[92:95], v[20:23], v[28:31], 0\n"
    "v_mfma_f32_16x16x32_bf16 v[96:99], v[24:27], v[28:31], 0\n"
    "v_mfma_f32_16x16x32_bf16 v[92:95], v[32:35], v[40:43], v[92:95]\n"
    "v_mfma_f32_16x16x32_bf16 v[96:99], v[36:39], v[40:43], v[96:99]\n"
    "s_waitcnt lgkmcnt(12)\n"
    "v_mfma_f32_16x16x32_bf16 v[92:95], v[44:47], v[52:55], v[92:95]\n"
    "v_mfma_f32_16x16x32_bf16 v[96:99], v[48:51], v[52:55], v[96:99]\n"
    "s_waitcnt lgkmcnt(8)\n"
    "v_mfma_f32_16x16x32_bf16 v[92:95], v[56:59], v[64:67], v[92:95]\n"
    "v_mfma_f32_16x16x32_bf16 v[96:99], v[60:63], v[64:67], v[96:99]\n"
    "s_waitcnt lgkmcnt(4)\n"
    "v_mfma_f32_16x16x32_bf16 v[92:95], v[68:71], v[76:79], v[92:95]\n"
    "v_mfma_f32_16x16x32_bf16 v[96:99], v[72:75], v[76:79], v[96:99]\n"
    "s_waitcnt lgkmcnt(0)\n"
    "v_mfma_f32_16x16x32_bf16 v[92:95], v[80:83], v[88:91], v[92:95]\n"
    "v_mfma_f32_16x16x32_bf16 v[96:99], v[84:87], v[88:91], v[96:99]\n"
    "s_branch .Le_pub\n"
    ".Le_7:\n"
    "s_barrier\n"
    ".Le_71:\n"
    "s_lshr_b32 s27, s2, 6\n"
    "s_bfe_u32 s26, s2, 0x30003\n"
    "s_branch .Le_xo\n"
    ".Le_72:\n"
    "s_cmp_eq_u32 s7, 8\n"
    "s_cbranch_scc1 .Le_w\n"
    "s_mov_b32 s7, 8\n"
    "v_mov_b32 v101, v1\n"
    "v_mov_b32 v102, v2\n"
    "v_mov_b32 v103, v3\n"
    "v_mov_b32 v104, v4\n"
    "v_mov_b32 v105, v5\n"
    "s_branch .Le_71\n"
    ".Le_sp:\n"
    "s_cmp_lt_u32 s7, 7\n"
    "s_cbranch_scc1 .Le_b\n"
    "s_branch .Le_w\n"
    ".Le_g2s:\n"
    "s_mul_i32 s44, s18, 86016\n"
    "s_mul_i32 s45, s19, 86016\n"
    "s_lshl_b32 s56, s23, 2\n"
    "s_add_u32 s56, s56, 161408\n"
    "v_lshlrev_b32 v16, 20, v6\n"
    "v_add_u32 v16, s56, v16\n"
    "s_cmp_eq_u32 s30, 1\n"
    "s_cbranch_scc1 .Li_s1\n"
    "s_cmp_eq_u32 s30, 2\n"
    "s_cbranch_scc1 .Li_s2\n"
    "s_cmp_eq_u32 s30, 3\n"
    "s_cbranch_scc1 .Li_s3\n"
    "s_waitcnt vmcnt(12)\n"
    "v_mov_b32 v17, 1\n"
    "ds_write_b32 v16, v17 offset:0\n"
    "s_waitcnt vmcnt(4)\n"
    "v_mov_b32 v17, 2\n"
    "ds_write_b32 v16, v17 offset:0\n"
    "s_mul_i32 s56, s45, 2\n"
    "s_add_u32 s56, s56, s44\n"
    "s_add_u32 s60, s8, s56\n"
    "s_addc_u32 s61, s9, 0\n"
    "s_mov_b32 m0, 18816\n"
    "s_nop 0\n"
    "global_load_lds_dwordx4 v1, s[60:61]\n"
    "s_mov_b32 m0, 20864\n"
    "s_nop 0\n"
    "global_load_lds_dwordx4 v2, s[60:61]\n"
    "s_mov_b32 m0, 22912\n"
    "s_nop 0\n"
    "global_load_lds_dwordx4 v3, s[60:61]\n"
    "s_mov_b32 m0, 24960\n"
    "s_nop 0\n"
    "global_load_lds_dwordx4 v4, s[60:61]\n"
    "s_mov_b32 m0, 17792\n"
    "s_nop 0\n"
    "global_load_lds_dwordx4 v5, s[60:61]\n"
    "s_mul_i32 s56, s45, 1\n"
    "s_add_u32 s56, s56, s44\n"
    "s_add_u32 s60, s8, s56\n"
    "s_addc_u32 s61, s9, 0\n"
    "s_mov_b32 m0, 9920\n"
    "s_nop 0\n"
    "global_load_lds_dwordx4 v1, s[60:61]\n"
    "s_mov_b32 m0, 11968\n"
    "s_nop 0\n"
    "global_load_lds_dwordx4 v2, s[60:61]\n"
    "s_mov_b32 m0, 14016\n"
    "s_nop 0\n"
    "global_load_lds_dwordx4 v3, s[60:61]\n"
    "s_mov_b32 m0, 16064\n"
    "s_nop 0\n"
    "global_load_lds_dwordx4 v4, s[60:61]\n"
    "s_mov_b32 m0, 8896\n"
    "s_nop 0\n"
    "global_load_lds_dwordx4 v5, s[60:61]\n"
    "s_mul_i32 s56, s45, 0\n"
    "s_add_u32 s56, s56, s44\n"
    "s_add_u32 s60, s8, s56\n"
    "s_addc_u32 s61, s9, 0\n"
    "s_mov_b32 m0, 1024\n"
    "s_nop 0\n"
    "global_load_lds_dwordx4 v1, s[60:61]\n"
    "s_mov_b32 m0, 3072\n"
    "s_nop 0\n"
    "global_load_lds_dwordx4 v2, s[60:61]\n"
    "s_mov_b32 m0, 5120\n"
    "s_nop 0\n"
    "global_load_lds_dwordx4 v3, s[60:61]\n"
    "s_mov_b32 m0, 7168\n"
    "s_nop 0\n"
    "global_load_lds_dwordx4 v4, s[60:61]\n"
    "s_mov_b32 m0, 0\n"
    "s_nop 0\n"
    "global_load_lds_dwordx4 v5, s[60:61]\n"
    "s_mul_i32 s56, s45, 3\n"
    "s_add_u32 s56, s56, s44\n"
    "s_add_u32 s60, s8, s56\n"
    "s_addc_u32 s61, s9, 0\n"
    "s_mov_b32 m0, 26688\n"
    "s_nop 0\n"
    "global_load_lds_dwordx4 v5, s[60:61]\n"
    "s_mul_i32 s56, s45, 4\n"
    "s_add_u32 s56, s56, s44\n"
    "s_add_u32 s60, s8, s56\n"
    "s_addc_u32 s61, s9, 0\n"
    "s_mov_b32 m0, 35584\n"
    "s_nop 0\n"
    "global_load_lds_dwordx4 v5, s[60:61]\n"
    "s_mul_i32 s56, s45, 5\n"
    "s_add_u32 s56, s56, s44\n"
    "s_add_u32 s60, s8, s56\n"
    "s_addc_u32 s61, s9, 0\n"
    "s_mov_b32 m0, 44480\n"
    "s_nop 0\n"
    "global_load_lds_dwordx4 v5, s[60:61]\n"
    "s_mul_i32 s56, s45, 6\n"
    "s_add_u32 s56, s56, s44\n"
    "s_add_u32 s60, s8, s56\n"
    "s_addc_u32 s61, s9, 0\n"
    "s_mov_b32 m0, 53376\n"
    "s_nop 0\n"
    "global_load_lds_dwordx4 v5, s[60:61]\n"
    "s_mul_i32 s56, s45, 7\n"
    "s_add_u32 s56, s56, s44\n"
    "s_add_u32 s60, s8, s56\n"
    "s_addc_u32 s61, s9, 0\n"
    "s_mov_b32 m0, 62272\n"
    "s_nop 0\n"
    "global_load_lds_dwordx4 v5, s[60:61]\n"
    "v_mov_b32 v17, 3\n"
    "ds_write_b32 v16, v17 offset:0\n"
    "s_waitcnt vmcnt(20)\n"
    "s_waitcnt vmcnt(0)\n"
    "v_mov_b32 v17, 4\n"
    "ds_write_b32 v16, v17 offset:0\n"
    "s_branch .Li_sd\n"
    ".Li_s1:\n"
    "s_waitcnt vmcnt(9)\n"
    "v_mov_b32 v17, 1\n"
    "ds_write_b32 v16, v17 offset:0\n"
    "s_waitcnt vmcnt(3)\n"
    "v_mov_b32 v17, 2\n"
    "ds_write_b32 v16, v17 offset:0\n"
    "s_mul_i32 s56, s45, 2\n"
    "s_add_u32 s56, s56, s44\n"
    "s_add_u32 s60, s8, s56\n"
    "s_addc_u32 s61, s9, 0\n"
    "s_mov_b32 m0, 19840\n"
    "s_nop 0\n"
    "global_load_lds_dwordx4 v1, s[60:61]\n"
    "s_mov_b32 m0, 21888\n"
    "s_nop 0\n"
    "global_load_lds_dwordx4 v2, s[60:61]\n"
    "s_mov_b32 m0, 23936\n"
    "s_nop 0\n"
    "global_load_lds_dwordx4 v3, s[60:61]\n"
    "s_mov_b32 m0, 25568\n"
    "s_nop 0\n"
    "global_load_lds_dwordx4 v5, s[60:61]\n"
    "s_mul_i32 s56, s45, 1\n"
    "s_add_u32 s56, s56, s44\n"
    "s_add_u32 s60, s8, s56\n"
    "s_addc_u32 s61, s9, 0\n"
    "s_mov_b32 m0, 10944\n"
    "s_nop 0\n"
    "global_load_lds_dwordx4 v1, s[60:61]\n"
    "s_mov_b32 m0, 12992\n"
    "s_nop 0\n"
    "global_load_lds_dwordx4 v2, s[60:61]\n"
    "s_mov_b32 m0, 15040\n"
    "s_nop 0\n"
    "global_load_lds_dwordx4 v3, s[60:61]\n"
    "s_mov_b32 m0, 16672\n"
    "s_nop 0\n"
    "global_load_lds_dwordx4 v5, s[60:61]\n"
    "s_mul_i32 s56, s45, 0\n"
    "s_add_u32 s56, s56, s44\n"
    "s_add_u32 s60, s8, s56\n"
    "s_addc_u32 s61, s9, 0\n"
    "s_mov_b32 m0, 2048\n"
    "s_nop 0\n"
    "global_load_lds_dwordx4 v1, s[60:61]\n"
    "s_mov_b32 m0, 4096\n"
    "s_nop 0\n"
    "global_load_lds_dwordx4 v2, s[60:61]\n"
    "s_mov_b32 m0, 6144\n"
    "s_nop 0\n"
    "global_load_lds_dwordx4 v3, s[60:61]\n"
    "s_mov_b32 m0, 7776\n"
    "s_nop 0\n"
    "global_load_lds_dwordx4 v5, s[60:61]\n"
    "s_mul_i32 s56, s45, 3\n"
    "s_add_u32 s56, s56, s44\n"
    "s_add_u32 s60, s8, s56\n"
    "s_addc_u32 s61, s9, 0\n"
    "s_mov_b32 m0, 34464\n"
    "s_nop 0\n"
    "global_load_lds_dwordx4 v5, s[60:61]\n"
    "s_mul_i32 s56, s45, 4\n"
    "s_add_u32 s56, s56, s44\n"
    "s_add_u32 s60, s8, s56\n"
    "s_addc_u32 s61, s9, 0\n"
    "s_mov_b32 m0, 43360\n"
    "s_nop 0\n"
    "global_load_lds_dwordx4 v5, s[60:61]\n"
    "s_mul_i32 s56, s45, 5\n"
    "s_add_u32 s56, s56, s44\n"
    "s_add_u32 s60, s8, s56\n"
    "s_addc_u32 s61, s9, 0\n"
    "s_mov_b32 m0, 52256\n"
    "s_nop 0\n"
    "global_load_lds_dwordx4 v5, s[60:61]\n"
    "s_mul_i32 s56, s45, 6\n"
    "s_add_u32 s56, s56, s44\n"
    "s_add_u32 s60, s8, s56\n"
    "s_addc_u32 s61, s9, 0\n"
    "s_mov_b32 m0, 61152\n"
    "s_nop 0\n"
    "global_load_lds_dwordx4 v5, s[60:61]\n"
    "s_mul_i32 s56, s45, 7\n"
    "s_add_u32 s56, s56, s44\n"
    "s_add_u32 s60, s8, s56\n"
    "s_addc_u32 s61, s9, 0\n"
    "s_mov_b32 m0, 70048\n"
    "s_nop 0\n"
    "global_load_lds_dwordx4 v5, s[60:61]\n"
    "v_mov_b32 v17, 3\n"
    "ds_write_b32 v16, v17 offset:0\n"
    "s_waitcnt vmcnt(17)\n"
    "s_waitcnt vmcnt(0)\n"
    "v_mov_b32 v17, 4\n"
    "ds_write_b32 v16, v17 offset:0\n"
    "s_branch .Li_sd\n"
    ".Li_s2:\n"
    "s_waitcnt vmcnt(12)\n"
    "v_mov_b32 v17, 1\n"
    "ds_write_b32 v16, v17 offset:0\n"
    "s_waitcnt vmcnt(4)\n"
    "v_mov_b32 v17, 2\n"
    "ds_write_b32 v16, v17 offset:0\n"
    "v_mov_b32 v17, 3\n"
    "ds_write_b32 v16, v17 offset:0\n"
    "s_waitcnt vmcnt(0)\n"
    "s_branch .Li_sd\n"
    ".Li_s3:\n"
    "s_waitcnt vmcnt(9)\n"
    "v_mov_b32 v17, 1\n"
    "ds_write_b32 v16, v17 offset:0\n"
    "s_waitcnt vmcnt(3)\n"
    "v_mov_b32 v17, 2\n"
    "ds_write_b32 v16, v17 offset:0\n"
    "v_mov_b32 v17, 3\n"
    "ds_write_b32 v16, v17 offset:0\n"
    "s_waitcnt vmcnt(0)\n"
    "s_branch .Li_sd\n"
    ".Li_sd:\n"
    "s_setprio 3\n"
    "s_branch .Le_w\n"
    ".Le_b:\n"
    "s_barrier\n"
    ".Le_w:\n"
    "s_waitcnt lgkmcnt(0)\n"
        :
          "={s[8:15]}"(ka), "={s[16:17]}"(ka4), "={s4}"(t0), "={s18}"(ax[0]), "={s19}"(ax[1]), "={s20}"(ax[2]), "={s21}"(ax[3]),
          "={v1}"(xo[0]), "={v2}"(xo[1]), "={v3}"(xo[2]), "={v4}"(xo[3]), "={v5}"(xo4),
          "={v101}"(xq[0]), "={v102}"(xq[1]), "={v103}"(xq[2]), "={v104}"(xq[3]), "={v105}"(xq4), "+{v0}"(tid), "+{s2}"(bid)
        : "{s[0:1]}"(__builtin_amdgcn_kernarg_segment_ptr())
        : "memory", "scc", "vcc", "s3", "s5", "s7", "s22", "s42", "s43", "s44", "s45", "s46", "s47", "s48", "s49", "s50", "s51", "s87", "s56", "s60", "s61", "s23", "s24", "s25", "s26", "s27", "s28", "s29", "s30", "s31",
          "v6", "v7", "v8", "v9", "v10", "v11", "v12", "v13", "v14", "v15", "v16", "v17", "v18", "v19", "v20", "v21", "v22", "v23", "v24", "v25", "v26", "v27", "v28", "v29", "v30", "v31", "v32", "v33", "v34", "v35", "v36", "v37", "v38", "v39", "v40", "v41", "v42", "v43", "v44", "v45", "v46", "v47", "v48", "v49", "v50", "v51", "v52", "v53", "v54", "v55", "v56", "v57", "v58", "v59", "v60", "v61", "v62", "v63", "v64", "v65", "v66", "v67", "v68", "v69", "v70", "v71", "v72", "v73", "v74", "v75", "v76", "v77", "v78", "v79", "v80", "v81", "v82", "v83", "v84", "v85", "v86", "v87", "v88", "v89", "v90", "v91", "v92", "v93", "v94", "v95", "v96", "v97", "v98", "v99", "v100");
    auto mkptr = [](unsigned lo, unsigned hi) LAMBDA_INL { return (char*)(((u64)(unsigned)UNI(hi) << 32) | (unsigned)UNI(lo)); };
    const char* xb = mkptr(ka[0], ka[1]);
    char* ws = mkptr(ka4[0], ka4[1]);
    bid = UNI(bid);
    int lane = tid & 63;
    const int wave = __builtin_amdgcn_readfirstlane(tid >> 6);
    const int tq = bid >> 6, ts = (bid >> 3) & 7, strip = ts == 7 ? tq + 4 * (bid & 1) : ts;
    const int t12 = tq < 2 || ts == 7, hs = tq == 3 && ts != 7, redge = tq == 1 && ts != 7 && (bid & 1), cedge = strip == 6;
    const AxisMap rm = {UNI(ax[0]), UNI(ax[1]), bid & 4 ? 218 : 1, redge ? 11 : 99};
    const AxisMap cm = {UNI(ax[2]), UNI(ax[3]), bid & 2 ? 218 : 1, cedge ? 15 : 99};
    const unsigned vf = LF + 4 * (lane & 3);
    const u32x4 fl = {vf, lane < 2 ? LF + 4 * (wave & 1) + 8 * lane : OOB, lane < 1 ? LF + 4 * wave : OOB, 0};
    const int i16 = lane & 15, g = lane >> 4;
    const int e = (i16 + 4) >> 3 & 1, qj = i16 - 4 * ((i16 + 4) >> 3);

    if (strip == 7) {
        asm volatile("s_nop 7\n"
    "s_cmp_lg_u32 s46, 6\n"
    "s_cbranch_scc1 .Ls_end\n"
    "v_mov_b32 v1, 0\n"
    "global_load_dwordx2 v[12:13], v1, s[40:41] sc1\n"
    "v_and_b32 v2, 15, v0\n"
    "v_lshlrev_b32 v2, 4, v2\n"
    "s_waitcnt vmcnt(0)\n"
    "v_add_u32 v6, 0x7f4a7c15, v12\n"
    "v_add_u32 v7, 0x9e3779b9, v13\n"
    "v_add_u32 v4, 0x7f4a7c15, v12\n"
    "v_add_u32 v5, 0x9e3779b9, v13\n"
    "v_add_u32 v3, s48, v2\n"
    "global_store_dwordx4 v3, v[4:7], s[40:41] sc1\n"
    "s_cmp_eq_u32 s49, 0\n"
    "s_cbranch_scc1 .Ls_end\n"
    "v_and_b32 v2, 0x70, v2\n"
    "v_lshlrev_b32 v3, 10, v0\n"
    "v_add_u32 v3, 393216, v3\n"
    "s_mov_b32 s52, 0x10000\n"
    "s_sleep 110\n"
    ".Ls_p:\n"
    "s_sleep 8\n"
    "global_load_dwordx2 v[8:9], v3, s[40:41] offset:0 sc1\n"
    "global_load_dwordx2 v[10:11], v3, s[40:41] offset:128 sc1\n"
    "global_load_dwordx2 v[12:13], v3, s[40:41] offset:256 sc1\n"
    "global_load_dwordx2 v[14:15], v3, s[40:41] offset:384 sc1\n"
    "global_load_dwordx2 v[16:17], v3, s[40:41] offset:512 sc1\n"
    "global_load_dwordx2 v[18:19], v3, s[40:41] offset:640 sc1\n"
    "global_load_dwordx2 v[20:21], v3, s[40:41] offset:768 sc1\n"
    "global_load_dwordx2 v[22:23], v3, s[40:41] offset:896 sc1\n"
    "s_mov_b64 s[46:47], 0\n"
    "s_waitcnt vmcnt(0)\n"
    "v_cmp_ne_u64 vcc, v[4:5], v[8:9]\n"
    "s_or_b64 s[46:47], s[46:47], vcc\n"
    "v_cmp_ne_u64 vcc, v[4:5], v[10:11]\n"
    "s_or_b64 s[46:47], s[46:47], vcc\n"
    "v_cmp_ne_u64 vcc, v[4:5], v[12:13]\n"
    "s_or_b64 s[46:47], s[46:47], vcc\n"
    "v_cmp_ne_u64 vcc, v[4:5], v[14:15]\n"
    "s_or_b64 s[46:47], s[46:47], vcc\n"
    "v_cmp_ne_u64 vcc, v[4:5], v[16:17]\n"
    "s_or_b64 s[46:47], s[46:47], vcc\n"
    "v_cmp_ne_u64 vcc, v[4:5], v[18:19]\n"
    "s_or_b64 s[46:47], s[46:47], vcc\n"
    "v_cmp_ne_u64 vcc, v[4:5], v[20:21]\n"
    "s_or_b64 s[46:47], s[46:47], vcc\n"
    "v_cmp_ne_u64 vcc, v[4:5], v[22:23]\n"
    "s_or_b64 s[46:47], s[46:47], vcc\n"
    "s_cbranch_scc0 .Ls_c\n"
    "s_sub_u32 s52, s52, 1\n"
    "s_cmp_lg_u32 s52, 0\n"
    "s_cbranch_scc1 .Ls_p\n"
    ".Ls_c:\n"
    "global_store_dwordx4 v2, v[4:7], s[40:41] sc1\n"
    ".Ls_end:\n"
    "s_endpgm\n"
    ".Ls_last:\n"
            :: "{v0}"(lane), "{s[40:41]}"(ws), "{s46}"(wave), "{s48}"(UNI(WS_ACK + 2 * bid * 128)), "{s49}"(UNI(bid == 253))
            : "memory", "v23", "s47", "s52", "vcc", "scc");
    } else if (wave < 4) {
        const int h = wave & 1;
        const unsigned l16 = lane * 16;
        const int nf = min(lane, 35) + 36 * h;
        const unsigned z0 = LQ + (lane & 3) * 256 + (4 * ((2 * (g >> 1)) ^ (lane & 3)) + ((lane >> 2) & 3)) * 16 + (g & 1) * 8;
        const u32x2 vz = {z0, z0 ^ 64};
        const unsigned vi = LF + 320 + 4 * ((i16 + 8) & 15);
        const unsigned c0 = axis_at(cm, 3 + qj) * PIXB + 16 * g, c1 = axis_at(cm, 11 + qj) * PIXB + 16 * g;
        const u32x2 dd = {(unsigned)((axis_at(rm, 11 + e) - axis_at(rm, 9 + e)) * IROWB), (unsigned)((axis_at(rm, 13 + e) - axis_at(rm, 11 + e)) * IROWB)};
        const unsigned d56 = 2 * rm.d * IROWB + (hs ? c1 - c0 : 0);
        const u32x4 srd = {(unsigned)UNI(ka[6]), (unsigned)UNI(ka[7]) & 0xffffu, (unsigned)(IMW * IROWB), 0x00020000u};
        if (wave >= 2) {
            const unsigned b = pixad(3 + qj) + e * RP + 16 * g;
            const u32x2 va = {b, b + 3200};
            const u32x2 vb = {(unsigned)(e ? -9 * RP : 5 * RP), LQ + l16 + h * 3072};
            const u32x2 lf = {l16, (unsigned)(WS_FL + ((bid & (NREP - 1)) * 144 + nf) * 128)};
            constexpr int N0 = 9, UE = (N0 + 1) / 2, UO = N0 / 2;
            u32x4 to;
            if (h != 0) {
                to = (u32x4){axis_at(rm, 3 + 2 * UE + e) * IROWB + c0, axis_at(rm, 3 + 2 * UO + e) * IROWB + c1, dd[0], dd[1]};
            } else {
                const AttC c = att_const(lane, hs, cedge);
                to = (u32x4){c.sb, c.v0, c.v1, c.pk};
            }
            asm volatile("s_nop 7\n"
    "s_mov_b32 s52, 0x40000\n"
    "s_load_dwordx2 s[78:79], s[40:41], 0x0\n"
    "s_mul_i32 s53, s50, 36864\n"
    "s_add_u32 s57, s53, 11264\n"
    "s_add_u32 s64, s40, s57\n"
    "s_addc_u32 s65, s41, 0\n"
    "s_add_u32 s57, s53, 17408\n"
    "s_add_u32 s66, s40, s57\n"
    "s_addc_u32 s67, s41, 0\n"
    "s_add_u32 s57, s53, 23552\n"
    "s_add_u32 s68, s40, s57\n"
    "s_addc_u32 s69, s41, 0\n"
    "s_add_u32 s57, s53, 29696\n"
    "s_add_u32 s70, s40, s57\n"
    "s_addc_u32 s71, s41, 0\n"
    "s_add_u32 s57, s53, 35840\n"
    "s_add_u32 s72, s40, s57\n"
    "s_addc_u32 s73, s41, 0\n"
    "s_add_u32 s57, s53, 41984\n"
    "s_add_u32 s74, s40, s57\n"
    "s_addc_u32 s75, s41, 0\n"
    "v_add_u32 v4, 71168, v2\n"
    "v_add_u32 v5, 71168, v3\n"
    "s_lshl_b32 s54, s50, 10\n"
    "s_add_u32 s54, s54, 1024\n"
    "s_mul_i32 s56, s50, 7776\n"
    "s_cmp_eq_u32 s50, 0\n"
    "s_cbranch_scc1 .Lg_x0\n"
    "v_mov_b32 v233, v234\n"
    ".Lg_x0:\n"
    "s_max_u32 s55, s56, 7168\n"
    "v_add_u32 v254, -8, v226\n"
    ".Lg_dl:\n"
    "s_sleep 1\n"
    "s_memtime s[76:77]\n"
    "s_waitcnt lgkmcnt(0)\n"
    "s_sub_u32 s76, s76, s59\n"
    "s_cmp_lt_i32 s76, 0\n"
    "s_cbranch_scc1 .Lg_dl\n"
    "s_waitcnt lgkmcnt(0)\n"
    "s_add_u32 s78, s78, 0x7f4a7c15\n"
    "s_add_u32 s79, s79, 0x9e3779b9\n"
    "v_mov_b64 v[240:241], s[78:79]\n"
    "s_mov_b32 s51, 0x4000\n"
    ".Lg_retry:\n"
    "v_mov_b64 v[244:245], v[240:241]\n"
    "v_mov_b64 v[246:247], 0\n"
    "v_mov_b64 v[242:243], 0\n"
    "s_mov_b32 exec_hi, 15\n"
    "global_load_dwordx4 v[244:247], v236, s[40:41] sc1\n"
    "s_mov_b64 exec, -1\n"
    "ds_read_b32 v228, v224 offset:48\n"
    "s_waitcnt lgkmcnt(0)\n"
    "v_cmp_gt_u32 vcc, 1, v228\n"
    "s_cbranch_vccnz .Lg1\n"
    ".Lg1b:\n"
    "global_load_dwordx4 v[8:11], v235, s[64:65] offset:-3072 sc1\n"
    "global_load_dwordx4 v[32:35], v235, s[66:67] offset:-3072 sc1\n"
    "global_load_dwordx4 v[12:15], v235, s[64:65] offset:-2048 sc1\n"
    "global_load_dwordx4 v[36:39], v235, s[66:67] offset:-2048 sc1\n"
    "global_load_dwordx4 v[16:19], v235, s[64:65] offset:-1024 sc1\n"
    "global_load_dwordx4 v[40:43], v235, s[66:67] offset:-1024 sc1\n"
    "global_load_dwordx4 v[20:23], v235, s[64:65] offset:0 sc1\n"
    "global_load_dwordx4 v[44:47], v235, s[66:67] offset:0 sc1\n"
    "global_load_dwordx4 v[24:27], v235, s[64:65] offset:1024 sc1\n"
    "global_load_dwordx4 v[48:51], v235, s[66:67] offset:1024 sc1\n"
    "global_load_dwordx4 v[28:31], v235, s[64:65] offset:2048 sc1\n"
    "global_load_dwordx4 v[52:55], v235, s[66:67] offset:2048 sc1\n"
    "global_load_dwordx4 v[56:59], v235, s[68:69] offset:-3072 sc1\n"
    "global_load_dwordx4 v[80:83], v235, s[70:71] offset:-3072 sc1\n"
    "global_load_dwordx4 v[60:63], v235, s[68:69] offset:-2048 sc1\n"
    "global_load_dwordx4 v[84:87], v235, s[70:71] offset:-2048 sc1\n"
    "global_load_dwordx4 v[64:67], v235, s[68:69] offset:-1024 sc1\n"
    "global_load_dwordx4 v[88:91], v235, s[70:71] offset:-1024 sc1\n"
    "global_load_dwordx4 v[68:71], v235, s[68:69] offset:0 sc1\n"
    "global_load_dwordx4 v[92:95], v235, s[70:71] offset:0 sc1\n"
    "s_waitcnt vmcnt(20)\n"
    "v_cmp_ne_u64 vcc, v[240:241], v[244:245]\n"
    "s_cmp_gt_i32 s51, 0\n"
    "s_cbranch_scc0 .Lg_pd\n"
    "s_cbranch_vccnz .Lg_poll\n"
    ".Lg_pd:\n"
    "ds_read_b32 v228, v224 offset:0\n"
    "s_waitcnt lgkmcnt(0)\n"
    "v_cmp_gt_u32 vcc, 1, v228\n"
    "s_cbranch_vccnz .Lg3\n"
    ".Lg3b:\n"
    "ds_read_b128 v[152:155], v2 offset:26688\n"
    "ds_read_b128 v[156:159], v2 offset:26752\n"
    "ds_read_b128 v[160:163], v2 offset:26816\n"
    "ds_read_b128 v[164:167], v2 offset:26880\n"
    "ds_read_b128 v[168:171], v2 offset:26944\n"
    "ds_read_b128 v[172:175], v2 offset:27008\n"
    "s_waitcnt vmcnt(18)\n"
    "v_add3_u32 v242, v242, v8, v11\n"
    "v_sub_u32 v8, v8, v240\n"
    "v_add3_u32 v242, v242, v32, v35\n"
    "v_sub_u32 v32, v32, v240\n"
    "s_waitcnt vmcnt(16)\n"
    "v_add3_u32 v242, v242, v12, v15\n"
    "v_sub_u32 v12, v12, v240\n"
    "s_waitcnt lgkmcnt(5)\n"
    "v_mfma_f32_16x16x32_bf16 v[200:203], v[8:11], v[152:155], 0\n"
    "global_load_dwordx4 v[72:75], v235, s[68:69] offset:1024 sc1\n"
    "v_add3_u32 v242, v242, v36, v39\n"
    "v_sub_u32 v36, v36, v240\n"
    "v_mfma_f32_16x16x32_bf16 v[204:207], v[32:35], v[152:155], 0\n"
    "global_load_dwordx4 v[96:99], v235, s[70:71] offset:1024 sc1\n"
    "s_waitcnt vmcnt(16)\n"
    "v_add3_u32 v242, v242, v16, v19\n"
    "v_sub_u32 v16, v16, v240\n"
    "s_waitcnt lgkmcnt(4)\n"
    "v_mfma_f32_16x16x32_bf16 v[200:203], v[12:15], v[156:159], v[200:203]\n"
    "global_load_dwordx4 v[76:79], v235, s[68:69] offset:2048 sc1\n"
    "v_add3_u32 v242, v242, v40, v43\n"
    "v_sub_u32 v40, v40, v240\n"
    "v_mfma_f32_16x16x32_bf16 v[204:207], v[36:39], v[156:159], v[204:207]\n"
    "global_load_dwordx4 v[100:103], v235, s[70:71] offset:2048 sc1\n"
    "s_waitcnt vmcnt(16)\n"
    "v_add3_u32 v242, v242, v20, v23\n"
    "v_sub_u32 v20, v20, v240\n"
    "s_waitcnt lgkmcnt(3)\n"
    "v_mfma_f32_16x16x32_bf16 v[200:203], v[16:19], v[160:163], v[200:203]\n"
    "global_load_dwordx4 v[104:107], v235, s[72:73] offset:-3072 sc1\n"
    "v_add3_u32 v242, v242, v44, v47\n"
    "v_sub_u32 v44, v44, v240\n"
    "v_mfma_f32_16x16x32_bf16 v[204:207], v[40:43], v[160:163], v[204:207]\n"
    "global_load_dwordx4 v[128:131], v235, s[74:75] offset:-3072 sc1\n"
    "s_waitcnt vmcnt(16)\n"
    "v_add3_u32 v242, v242, v24, v27\n"
    "v_sub_u32 v24, v24, v240\n"
    "s_waitcnt lgkmcnt(2)\n"
    "v_mfma_f32_16x16x32_bf16 v[200:203], v[20:23], v[164:167], v[200:203]\n"
    "global_load_dwordx4 v[108:111], v235, s[72:73] offset:-2048 sc1\n"
    "v_add3_u32 v242, v242, v48, v51\n"
    "v_sub_u32 v48, v48, v240\n"
    "v_mfma_f32_16x16x32_bf16 v[204:207], v[44:47], v[164:167], v[204:207]\n"
    "global_load_dwordx4 v[132:135], v235, s[74:75] offset:-2048 sc1\n"
    "s_waitcnt vmcnt(16)\n"
    "v_add3_u32 v242, v242, v28, v31\n"
    "v_sub_u32 v28, v28, v240\n"
    "s_waitcnt lgkmcnt(1)\n"
    "v_mfma_f32_16x16x32_bf16 v[200:203], v[24:27], v[168:171], v[200:203]\n"
    "global_load_dwordx4 v[112:115], v235, s[72:73] offset:-1024 sc1\n"
    "v_add3_u32 v242, v242, v52, v55\n"
    "v_sub_u32 v52, v52, v240\n"
    "v_mfma_f32_16x16x32_bf16 v[204:207], v[48:51], v[168:171], v[204:207]\n"
    "global_load_dwordx4 v[136:139], v235, s[74:75] offset:-1024 sc1\n"
    "s_waitcnt lgkmcnt(0)\n"
    "v_mfma_f32_16x16x32_bf16 v[200:203], v[28:31], v[172:175], v[200:203]\n"
    "global_load_dwordx4 v[116:119], v235, s[72:73] offset:0 sc1\n"
    "v_mov_b32 v227, 1\n"
    "v_mfma_f32_16x16x32_bf16 v[204:207], v[52:55], v[172:175], v[204:207]\n"
    "global_load_dwordx4 v[140:143], v235, s[74:75] offset:0 sc1\n"
    "ds_write_b32 v225, v227 offset:256\n"
    "s_waitcnt vmcnt(18)\n"
    "v_add3_u32 v242, v242, v56, v59\n"
    "v_sub_u32 v56, v56, v240\n"
    "v_add3_u32 v242, v242, v80, v83\n"
    "v_sub_u32 v80, v80, v240\n"
    "s_waitcnt vmcnt(16)\n"
    "v_add3_u32 v242, v242, v60, v63\n"
    "v_sub_u32 v60, v60, v240\n"
    "v_mfma_f32_16x16x32_bf16 v[208:211], v[56:59], v[152:155], 0\n"
    "global_load_dwordx4 v[120:123], v235, s[72:73] offset:1024 sc1\n"
    "v_add3_u32 v242, v242, v84, v87\n"
    "v_sub_u32 v84, v84, v240\n"
    "v_mfma_f32_16x16x32_bf16 v[212:215], v[80:83], v[152:155], 0\n"
    "global_load_dwordx4 v[144:147], v235, s[74:75] offset:1024 sc1\n"
    "s_waitcnt vmcnt(16)\n"
    "v_add3_u32 v242, v242, v64, v67\n"
    "v_sub_u32 v64, v64, v240\n"
    "v_mfma_f32_16x16x32_bf16 v[208:211], v[60:63], v[156:159], v[208:211]\n"
    "global_load_dwordx4 v[124:127], v235, s[72:73] offset:2048 sc1\n"
    "v_add3_u32 v242, v242, v88, v91\n"
    "v_sub_u32 v88, v88, v240\n"
    "v_mfma_f32_16x16x32_bf16 v[212:215], v[84:87], v[156:159], v[212:215]\n"
    "global_load_dwordx4 v[148:151], v235, s[74:75] offset:2048 sc1\n"
    "s_waitcnt vmcnt(16)\n"
    "v_add3_u32 v242, v242, v68, v71\n"
    "v_sub_u32 v68, v68, v240\n"
    "v_mfma_f32_16x16x32_bf16 v[208:211], v[64:67], v[160:163], v[208:211]\n"
    "v_cvt_pk_bf16_f32 v200, v200, v201\n"
    "v_add3_u32 v242, v242, v92, v95\n"
    "v_sub_u32 v92, v92, v240\n"
    "v_mfma_f32_16x16x32_bf16 v[212:215], v[88:91], v[160:163], v[212:215]\n"
    "v_cvt_pk_bf16_f32 v201, v202, v203\n"
    "s_waitcnt vmcnt(14)\n"
    "v_add3_u32 v242, v242, v72, v75\n"
    "v_sub_u32 v72, v72, v240\n"
    "v_mfma_f32_16x16x32_bf16 v[208:211], v[68:71], v[164:167], v[208:211]\n"
    "v_cvt_pk_bf16_f32 v202, v204, v205\n"
    "v_add3_u32 v242, v242, v96, v99\n"
    "v_sub_u32 v96, v96, v240\n"
    "v_mfma_f32_16x16x32_bf16 v[212:215], v[92:95], v[164:167], v[212:215]\n"
    "v_cvt_pk_bf16_f32 v203, v206, v207\n"
    "s_waitcnt vmcnt(12)\n"
    "v_add3_u32 v242, v242, v76, v79\n"
    "v_sub_u32 v76, v76, v240\n"
    "v_mfma_f32_16x16x32_bf16 v[208:211], v[72:75], v[168:171], v[208:211]\n"
    "v_add3_u32 v242, v242, v100, v103\n"
    "v_sub_u32 v100, v100, v240\n"
    "v_mfma_f32_16x16x32_bf16 v[212:215], v[96:99], v[168:171], v[212:215]\n"
    "v_mfma_f32_16x16x32_bf16 v[208:211], v[76:79], v[172:175], v[208:211]\n"
    "ds_write_b128 v7, v[200:203] offset:0\n"
    "v_mfma_f32_16x16x32_bf16 v[212:215], v[100:103], v[172:175], v[212:215]\n"
    "s_waitcnt vmcnt(10)\n"
    "v_add3_u32 v242, v242, v104, v107\n"
    "v_sub_u32 v104, v104, v240\n"
    "v_add3_u32 v242, v242, v128, v131\n"
    "v_sub_u32 v128, v128, v240\n"
    "s_waitcnt vmcnt(8)\n"
    "v_add3_u32 v242, v242, v108, v111\n"
    "v_sub_u32 v108, v108, v240\n"
    "v_mfma_f32_16x16x32_bf16 v[216:219], v[104:107], v[152:155], 0\n"
    "ds_read_b128 v[176:179], v3 offset:26688\n"
    "v_add3_u32 v242, v242, v132, v135\n"
    "v_sub_u32 v132, v132, v240\n"
    "v_mfma_f32_16x16x32_bf16 v[220:223], v[128:131], v[152:155], 0\n"
    "ds_read_b128 v[180:183], v3 offset:26752\n"
    "s_waitcnt vmcnt(6)\n"
    "v_add3_u32 v242, v242, v112, v115\n"
    "v_sub_u32 v112, v112, v240\n"
    "v_mfma_f32_16x16x32_bf16 v[216:219], v[108:111], v[156:159], v[216:219]\n"
    "ds_read_b128 v[184:187], v3 offset:26816\n"
    "v_add3_u32 v242, v242, v136, v139\n"
    "v_sub_u32 v136, v136, v240\n"
    "v_mfma_f32_16x16x32_bf16 v[220:223], v[132:135], v[156:159], v[220:223]\n"
    "ds_read_b128 v[188:191], v3 offset:26880\n"
    "s_waitcnt vmcnt(4)\n"
    "v_add3_u32 v242, v242, v116, v119\n"
    "v_sub_u32 v116, v116, v240\n"
    "v_mfma_f32_16x16x32_bf16 v[216:219], v[112:115], v[160:163], v[216:219]\n"
    "ds_read_b128 v[192:195], v3 offset:26944\n"
    "v_add3_u32 v242, v242, v140, v143\n"
    "v_sub_u32 v140, v140, v240\n"
    "v_mfma_f32_16x16x32_bf16 v[220:223], v[136:139], v[160:163], v[220:223]\n"
    "ds_read_b128 v[196:199], v3 offset:27008\n"
    "s_waitcnt vmcnt(2)\n"
    "v_add3_u32 v242, v242, v120, v123\n"
    "v_sub_u32 v120, v120, v240\n"
    "v_mfma_f32_16x16x32_bf16 v[216:219], v[116:119], v[164:167], v[216:219]\n"
    "v_cvt_pk_bf16_f32 v208, v208, v209\n"
    "v_add3_u32 v242, v242, v144, v147\n"
    "v_sub_u32 v144, v144, v240\n"
    "v_mfma_f32_16x16x32_bf16 v[220:223], v[140:143], v[164:167], v[220:223]\n"
    "v_cvt_pk_bf16_f32 v209, v210, v211\n"
    "s_waitcnt vmcnt(0)\n"
    "v_add3_u32 v242, v242, v124, v127\n"
    "v_sub_u32 v124, v124, v240\n"
    "v_mfma_f32_16x16x32_bf16 v[216:219], v[120:123], v[168:171], v[216:219]\n"
    "v_cvt_pk_bf16_f32 v210, v212, v213\n"
    "v_add3_u32 v242, v242, v148, v151\n"
    "v_sub_u32 v148, v148, v240\n"
    "v_mfma_f32_16x16x32_bf16 v[220:223], v[144:147], v[168:171], v[220:223]\n"
    "v_cvt_pk_bf16_f32 v211, v214, v215\n"
    "v_mfma_f32_16x16x32_bf16 v[216:219], v[124:127], v[172:175], v[216:219]\n"
    "ds_write_b128 v7, v[208:211] offset:1024\n"
    "v_mfma_f32_16x16x32_bf16 v[220:223], v[148:151], v[172:175], v[220:223]\n"
    "v_cmp_ne_u64 s[76:77], v[240:241], v[244:245]\n"
    "v_sub_u32 v242, v242, v246\n"
    "v_mov_b32 v243, v242\n"
    "s_nop 1\n"
    "v_permlane32_swap_b32 v242, v243\n"
    "v_add_u32 v242, v242, v243\n"
    "v_cvt_pk_bf16_f32 v216, v216, v217\n"
    "v_cvt_pk_bf16_f32 v217, v218, v219\n"
    "v_add_u32_dpp v242, v242, v242 row_ror:8 row_mask:0xf bank_mask:0xf\n"
    "v_cvt_pk_bf16_f32 v218, v220, v221\n"
    "v_cvt_pk_bf16_f32 v219, v222, v223\n"
    "v_add_u32_dpp v242, v242, v242 row_ror:4 row_mask:0xf bank_mask:0xf\n"
    "ds_write_b128 v7, v[216:219] offset:2048\n"
    "s_nop 0\n"
    "v_add_u32_dpp v242, v242, v242 row_ror:2 row_mask:0xf bank_mask:0xf\n"
    "s_nop 1\n"
    "v_add_u32_dpp v242, v242, v242 row_ror:1 row_mask:0xf bank_mask:0xf\n"
    "v_mov_b32 v243, v242\n"
    "s_nop 1\n"
    "v_permlane16_swap_b32 v242, v243\n"
    "v_add_u32 v242, v242, v243\n"
    "v_cmp_ne_u32 vcc, 0, v242\n"
    "v_mov_b32 v227, 1\n"
    "s_cmp_lg_u64 vcc, 0\n"
    "s_cbranch_scc1 .Lg_fail\n"
    "s_cmp_lg_u64 s[76:77], 0\n"
    "s_cbranch_scc1 .Lg_fail\n"
    ".Lg_ok:\n"
    "ds_write_b32 v225, v227 offset:16\n"
    "v_mov_b32 v242, 0\n"
    "s_waitcnt lgkmcnt(3)\n"
    ".Lg_t1:\n"
    "v_mfma_f32_16x16x32_bf16 v[200:203], v[8:11], v[176:179], 0\n"
    "v_mfma_f32_16x16x32_bf16 v[204:207], v[32:35], v[176:179], 0\n"
    "v_mfma_f32_16x16x32_bf16 v[200:203], v[12:15], v[180:183], v[200:203]\n"
    "v_mfma_f32_16x16x32_bf16 v[204:207], v[36:39], v[180:183], v[204:207]\n"
    "v_mfma_f32_16x16x32_bf16 v[200:203], v[16:19], v[184:187], v[200:203]\n"
    "v_mfma_f32_16x16x32_bf16 v[204:207], v[40:43], v[184:187], v[204:207]\n"
    "v_mfma_f32_16x16x32_bf16 v[200:203], v[20:23], v[188:191], v[200:203]\n"
    "v_mfma_f32_16x16x32_bf16 v[204:207], v[44:47], v[188:191], v[204:207]\n"
    "v_mfma_f32_16x16x32_bf16 v[200:203], v[24:27], v[192:195], v[200:203]\n"
    "v_mfma_f32_16x16x32_bf16 v[204:207], v[48:51], v[192:195], v[204:207]\n"
    "v_mfma_f32_16x16x32_bf16 v[200:203], v[28:31], v[196:199], v[200:203]\n"
    "v_mfma_f32_16x16x32_bf16 v[204:207], v[52:55], v[196:199], v[204:207]\n"
    "v_mfma_f32_16x16x32_bf16 v[208:211], v[56:59], v[176:179], 0\n"
    "v_mfma_f32_16x16x32_bf16 v[212:215], v[80:83], v[176:179], 0\n"
    "ds_read_b32 v229, v224 offset:0\n"
    "v_mfma_f32_16x16x32_bf16 v[208:211], v[60:63], v[180:183], v[208:211]\n"
    "v_mfma_f32_16x16x32_bf16 v[212:215], v[84:87], v[180:183], v[212:215]\n"
    "v_mfma_f32_16x16x32_bf16 v[208:211], v[64:67], v[184:187], v[208:211]\n"
    "v_mfma_f32_16x16x32_bf16 v[212:215], v[88:91], v[184:187], v[212:215]\n"
    "v_cvt_pk_bf16_f32 v200, v200, v201\n"
    "v_cvt_pk_bf16_f32 v201, v202, v203\n"
    "v_mfma_f32_16x16x32_bf16 v[208:211], v[68:71], v[188:191], v[208:211]\n"
    "v_cvt_pk_bf16_f32 v202, v204, v205\n"
    "v_cvt_pk_bf16_f32 v203, v206, v207\n"
    "v_mfma_f32_16x16x32_bf16 v[212:215], v[92:95], v[188:191], v[212:215]\n"
    "ds_write_b128 v7, v[200:203] offset:6144\n"
    "v_mfma_f32_16x16x32_bf16 v[208:211], v[72:75], v[192:195], v[208:211]\n"
    "v_mfma_f32_16x16x32_bf16 v[212:215], v[96:99], v[192:195], v[212:215]\n"
    "s_waitcnt lgkmcnt(1)\n"
    "v_cmp_gt_u32 vcc, 2, v229\n"
    "v_mfma_f32_16x16x32_bf16 v[208:211], v[76:79], v[196:199], v[208:211]\n"
    "v_mfma_f32_16x16x32_bf16 v[212:215], v[100:103], v[196:199], v[212:215]\n"
    "s_cbranch_vccnz .Lg4\n"
    ".Lg4b:\n"
    "v_mfma_f32_16x16x32_bf16 v[216:219], v[104:107], v[176:179], 0\n"
    "ds_read_b128 v[152:155], v2 offset:44480\n"
    "ds_read_b128 v[156:159], v2 offset:44544\n"
    "v_mfma_f32_16x16x32_bf16 v[220:223], v[128:131], v[176:179], 0\n"
    "ds_read_b128 v[160:163], v2 offset:44608\n"
    "ds_read_b128 v[164:167], v2 offset:44672\n"
    "v_mfma_f32_16x16x32_bf16 v[216:219], v[108:111], v[180:183], v[216:219]\n"
    "ds_read_b128 v[168:171], v2 offset:44736\n"
    "ds_read_b128 v[172:175], v2 offset:44800\n"
    "v_mfma_f32_16x16x32_bf16 v[220:223], v[132:135], v[180:183], v[220:223]\n"
    "v_cvt_pk_bf16_f32 v208, v208, v209\n"
    "v_cvt_pk_bf16_f32 v209, v210, v211\n"
    "v_mfma_f32_16x16x32_bf16 v[216:219], v[112:115], v[184:187], v[216:219]\n"
    "v_cvt_pk_bf16_f32 v210, v212, v213\n"
    "v_cvt_pk_bf16_f32 v211, v214, v215\n"
    "v_mfma_f32_16x16x32_bf16 v[220:223], v[136:139], v[184:187], v[220:223]\n"
    "ds_write_b128 v7, v[208:211] offset:7168\n"
    "v_mfma_f32_16x16x32_bf16 v[216:219], v[116:119], v[188:191], v[216:219]\n"
    "v_mfma_f32_16x16x32_bf16 v[220:223], v[140:143], v[188:191], v[220:223]\n"
    "v_mfma_f32_16x16x32_bf16 v[216:219], v[120:123], v[192:195], v[216:219]\n"
    "v_mfma_f32_16x16x32_bf16 v[220:223], v[144:147], v[192:195], v[220:223]\n"
    "v_mfma_f32_16x16x32_bf16 v[216:219], v[124:127], v[196:199], v[216:219]\n"
    "v_mfma_f32_16x16x32_bf16 v[220:223], v[148:151], v[196:199], v[220:223]\n"
    "s_waitcnt lgkmcnt(1)\n"
    "v_mfma_f32_16x16x32_bf16 v[200:203], v[8:11], v[152:155], 0\n"
    "v_mov_b32 v227, 2\n"
    "v_mfma_f32_16x16x32_bf16 v[204:207], v[32:35], v[152:155], 0\n"
    "v_mfma_f32_16x16x32_bf16 v[200:203], v[12:15], v[156:159], v[200:203]\n"
    "v_mfma_f32_16x16x32_bf16 v[204:207], v[36:39], v[156:159], v[204:207]\n"
    "v_mfma_f32_16x16x32_bf16 v[200:203], v[16:19], v[160:163], v[200:203]\n"
    "v_cvt_pk_bf16_f32 v216, v216, v217\n"
    "v_cvt_pk_bf16_f32 v217, v218, v219\n"
    "v_mfma_f32_16x16x32_bf16 v[204:207], v[40:43], v[160:163], v[204:207]\n"
    "v_cvt_pk_bf16_f32 v218, v220, v221\n"
    "v_cvt_pk_bf16_f32 v219, v222, v223\n"
    "v_mfma_f32_16x16x32_bf16 v[200:203], v[20:23], v[164:167], v[200:203]\n"
    "ds_write_b128 v7, v[216:219] offset:8192\n"
    "v_mfma_f32_16x16x32_bf16 v[204:207], v[44:47], v[164:167], v[204:207]\n"
    "ds_write_b32 v225, v227 offset:16\n"
    "v_mfma_f32_16x16x32_bf16 v[200:203], v[24:27], v[168:171], v[200:203]\n"
    "v_mfma_f32_16x16x32_bf16 v[204:207], v[48:51], v[168:171], v[204:207]\n"
    "v_mfma_f32_16x16x32_bf16 v[200:203], v[28:31], v[172:175], v[200:203]\n"
    "v_mfma_f32_16x16x32_bf16 v[204:207], v[52:55], v[172:175], v[204:207]\n"
    "v_mfma_f32_16x16x32_bf16 v[208:211], v[56:59], v[152:155], 0\n"
    "v_mfma_f32_16x16x32_bf16 v[212:215], v[80:83], v[152:155], 0\n"
    "v_mfma_f32_16x16x32_bf16 v[208:211], v[60:63], v[156:159], v[208:211]\n"
    "v_mfma_f32_16x16x32_bf16 v[212:215], v[84:87], v[156:159], v[212:215]\n"
    "v_mfma_f32_16x16x32_bf16 v[208:211], v[64:67], v[160:163], v[208:211]\n"
    "v_mfma_f32_16x16x32_bf16 v[212:215], v[88:91], v[160:163], v[212:215]\n"
    "v_mfma_f32_16x16x32_bf16 v[208:211], v[68:71], v[164:167], v[208:211]\n"
    "v_cvt_pk_bf16_f32 v200, v200, v201\n"
    "v_cvt_pk_bf16_f32 v201, v202, v203\n"
    "v_mfma_f32_16x16x32_bf16 v[212:215], v[92:95], v[164:167], v[212:215]\n"
    "v_cvt_pk_bf16_f32 v202, v204, v205\n"
    "v_cvt_pk_bf16_f32 v203, v206, v207\n"
    "v_mfma_f32_16x16x32_bf16 v[208:211], v[72:75], v[168:171], v[208:211]\n"
    "ds_write_b128 v7, v[200:203] offset:12288\n"
    "v_mfma_f32_16x16x32_bf16 v[212:215], v[96:99], v[168:171], v[212:215]\n"
    "v_mfma_f32_16x16x32_bf16 v[208:211], v[76:79], v[172:175], v[208:211]\n"
    "v_mfma_f32_16x16x32_bf16 v[212:215], v[100:103], v[172:175], v[212:215]\n"
    "v_mfma_f32_16x16x32_bf16 v[216:219], v[104:107], v[152:155], 0\n"
    "ds_read_b128 v[176:179], v3 offset:44480\n"
    "ds_read_b128 v[180:183], v3 offset:44544\n"
    "v_mfma_f32_16x16x32_bf16 v[220:223], v[128:131], v[152:155], 0\n"
    "ds_read_b128 v[184:187], v3 offset:44608\n"
    "ds_read_b128 v[188:191], v3 offset:44672\n"
    "v_mfma_f32_16x16x32_bf16 v[216:219], v[108:111], v[156:159], v[216:219]\n"
    "ds_read_b128 v[192:195], v3 offset:44736\n"
    "ds_read_b128 v[196:199], v3 offset:44800\n"
    "v_mfma_f32_16x16x32_bf16 v[220:223], v[132:135], v[156:159], v[220:223]\n"
    "v_cvt_pk_bf16_f32 v208, v208, v209\n"
    "v_cvt_pk_bf16_f32 v209, v210, v211\n"
    "v_mfma_f32_16x16x32_bf16 v[216:219], v[112:115], v[160:163], v[216:219]\n"
    "v_cvt_pk_bf16_f32 v210, v212, v213\n"
    "v_cvt_pk_bf16_f32 v211, v214, v215\n"
    "v_mfma_f32_16x16x32_bf16 v[220:223], v[136:139], v[160:163], v[220:223]\n"
    "ds_write_b128 v7, v[208:211] offset:13312\n"
    "v_mfma_f32_16x16x32_bf16 v[216:219], v[116:119], v[164:167], v[216:219]\n"
    "v_mfma_f32_16x16x32_bf16 v[220:223], v[140:143], v[164:167], v[220:223]\n"
    "v_mfma_f32_16x16x32_bf16 v[216:219], v[120:123], v[168:171], v[216:219]\n"
    "v_mfma_f32_16x16x32_bf16 v[220:223], v[144:147], v[168:171], v[220:223]\n"
    "v_mfma_f32_16x16x32_bf16 v[216:219], v[124:127], v[172:175], v[216:219]\n"
    "v_mfma_f32_16x16x32_bf16 v[220:223], v[148:151], v[172:175], v[220:223]\n"
    "s_waitcnt lgkmcnt(1)\n"
    "v_mfma_f32_16x16x32_bf16 v[200:203], v[8:11], v[176:179], 0\n"
    "v_mov_b32 v227, 3\n"
    "v_mfma_f32_16x16x32_bf16 v[204:207], v[32:35], v[176:179], 0\n"
    "v_mfma_f32_16x16x32_bf16 v[200:203], v[12:15], v[180:183], v[200:203]\n"
    "v_mfma_f32_16x16x32_bf16 v[204:207], v[36:39], v[180:183], v[204:207]\n"
    "v_mfma_f32_16x16x32_bf16 v[200:203], v[16:19], v[184:187], v[200:203]\n"
    "v_cvt_pk_bf16_f32 v216, v216, v217\n"
    "v_cvt_pk_bf16_f32 v217, v218, v219\n"
    "v_mfma_f32_16x16x32_bf16 v[204:207], v[40:43], v[184:187], v[204:207]\n"
    "v_cvt_pk_bf16_f32 v218, v220, v221\n"
    "v_cvt_pk_bf16_f32 v219, v222, v223\n"
    "v_mfma_f32_16x16x32_bf16 v[200:203], v[20:23], v[188:191], v[200:203]\n"
    "ds_write_b128 v7, v[216:219] offset:14336\n"
    "v_mfma_f32_16x16x32_bf16 v[204:207], v[44:47], v[188:191], v[204:207]\n"
    "ds_write_b32 v225, v227 offset:16\n"
    "ds_read_b32 v228, v224 offset:192\n"
    "v_mfma_f32_16x16x32_bf16 v[200:203], v[24:27], v[192:195], v[200:203]\n"
    "v_mfma_f32_16x16x32_bf16 v[204:207], v[48:51], v[192:195], v[204:207]\n"
    "v_mfma_f32_16x16x32_bf16 v[200:203], v[28:31], v[196:199], v[200:203]\n"
    "v_mfma_f32_16x16x32_bf16 v[204:207], v[52:55], v[196:199], v[204:207]\n"
    "v_mfma_f32_16x16x32_bf16 v[208:211], v[56:59], v[176:179], 0\n"
    "v_mfma_f32_16x16x32_bf16 v[212:215], v[80:83], v[176:179], 0\n"
    "ds_read_b32 v229, v224 offset:0\n"
    "v_mfma_f32_16x16x32_bf16 v[208:211], v[60:63], v[180:183], v[208:211]\n"
    "v_mfma_f32_16x16x32_bf16 v[212:215], v[84:87], v[180:183], v[212:215]\n"
    "s_waitcnt lgkmcnt(1)\n"
    "v_cmp_gt_u32 vcc, 1, v228\n"
    "v_mfma_f32_16x16x32_bf16 v[208:211], v[64:67], v[184:187], v[208:211]\n"
    "v_cvt_pk_bf16_f32 v200, v200, v201\n"
    "s_cbranch_vccnz .Lg6\n"
    ".Lg6b:\n"
    "v_mfma_f32_16x16x32_bf16 v[212:215], v[88:91], v[184:187], v[212:215]\n"
    "v_cvt_pk_bf16_f32 v201, v202, v203\n"
    "v_cvt_pk_bf16_f32 v202, v204, v205\n"
    "v_mfma_f32_16x16x32_bf16 v[208:211], v[68:71], v[188:191], v[208:211]\n"
    "v_cvt_pk_bf16_f32 v203, v206, v207\n"
    "v_mfma_f32_16x16x32_bf16 v[212:215], v[92:95], v[188:191], v[212:215]\n"
    "ds_write_b128 v7, v[200:203] offset:0\n"
    "v_mfma_f32_16x16x32_bf16 v[208:211], v[72:75], v[192:195], v[208:211]\n"
    "v_mfma_f32_16x16x32_bf16 v[212:215], v[96:99], v[192:195], v[212:215]\n"
    "s_waitcnt lgkmcnt(1)\n"
    "v_cmp_gt_u32 vcc, 5, v229\n"
    "v_mfma_f32_16x16x32_bf16 v[208:211], v[76:79], v[196:199], v[208:211]\n"
    "v_mfma_f32_16x16x32_bf16 v[212:215], v[100:103], v[196:199], v[212:215]\n"
    "s_cbranch_vccnz .Lg5\n"
    ".Lg5b:\n"
    "v_mfma_f32_16x16x32_bf16 v[216:219], v[104:107], v[176:179], 0\n"
    "ds_read_b128 v[152:155], v2 offset:62272\n"
    "ds_read_b128 v[156:159], v2 offset:62336\n"
    "v_mfma_f32_16x16x32_bf16 v[220:223], v[128:131], v[176:179], 0\n"
    "ds_read_b128 v[160:163], v2 offset:62400\n"
    "ds_read_b128 v[164:167], v2 offset:62464\n"
    "v_mfma_f32_16x16x32_bf16 v[216:219], v[108:111], v[180:183], v[216:219]\n"
    "ds_read_b128 v[168:171], v2 offset:62528\n"
    "ds_read_b128 v[172:175], v2 offset:62592\n"
    "v_mfma_f32_16x16x32_bf16 v[220:223], v[132:135], v[180:183], v[220:223]\n"
    "v_cvt_pk_bf16_f32 v208, v208, v209\n"
    "v_cvt_pk_bf16_f32 v209, v210, v211\n"
    "v_mfma_f32_16x16x32_bf16 v[216:219], v[112:115], v[184:187], v[216:219]\n"
    "v_cvt_pk_bf16_f32 v210, v212, v213\n"
    "v_cvt_pk_bf16_f32 v211, v214, v215\n"
    "v_mfma_f32_16x16x32_bf16 v[220:223], v[136:139], v[184:187], v[220:223]\n"
    "ds_write_b128 v7, v[208:211] offset:1024\n"
    "v_mfma_f32_16x16x32_bf16 v[216:219], v[116:119], v[188:191], v[216:219]\n"
    "v_mfma_f32_16x16x32_bf16 v[220:223], v[140:143], v[188:191], v[220:223]\n"
    "v_mfma_f32_16x16x32_bf16 v[216:219], v[120:123], v[192:195], v[216:219]\n"
    "v_mfma_f32_16x16x32_bf16 v[220:223], v[144:147], v[192:195], v[220:223]\n"
    "v_mfma_f32_16x16x32_bf16 v[216:219], v[124:127], v[196:199], v[216:219]\n"
    "v_mfma_f32_16x16x32_bf16 v[220:223], v[148:151], v[196:199], v[220:223]\n"
    "s_waitcnt lgkmcnt(1)\n"
    "v_mfma_f32_16x16x32_bf16 v[200:203], v[8:11], v[152:155], 0\n"
    "v_mov_b32 v227, 4\n"
    "v_mfma_f32_16x16x32_bf16 v[204:207], v[32:35], v[152:155], 0\n"
    "v_mfma_f32_16x16x32_bf16 v[200:203], v[12:15], v[156:159], v[200:203]\n"
    "v_mfma_f32_16x16x32_bf16 v[204:207], v[36:39], v[156:159], v[204:207]\n"
    "v_mfma_f32_16x16x32_bf16 v[200:203], v[16:19], v[160:163], v[200:203]\n"
    "v_cvt_pk_bf16_f32 v216, v216, v217\n"
    "v_cvt_pk_bf16_f32 v217, v218, v219\n"
    "v_mfma_f32_16x16x32_bf16 v[204:207], v[40:43], v[160:163], v[204:207]\n"
    "v_cvt_pk_bf16_f32 v218, v220, v221\n"
    "v_cvt_pk_bf16_f32 v219, v222, v223\n"
    "v_mfma_f32_16x16x32_bf16 v[200:203], v[20:23], v[164:167], v[200:203]\n"
    "ds_write_b128 v7, v[216:219] offset:2048\n"
    "v_mfma_f32_16x16x32_bf16 v[204:207], v[44:47], v[164:167], v[204:207]\n"
    "ds_write_b32 v225, v227 offset:16\n"
    "ds_read_b32 v228, v224 offset:208\n"
    "v_mfma_f32_16x16x32_bf16 v[200:203], v[24:27], v[168:171], v[200:203]\n"
    "ds_read_b32 v239, v224 offset:64\n"
    "v_mfma_f32_16x16x32_bf16 v[204:207], v[48:51], v[168:171], v[204:207]\n"
    "v_mfma_f32_16x16x32_bf16 v[200:203], v[28:31], v[172:175], v[200:203]\n"
    "v_mfma_f32_16x16x32_bf16 v[204:207], v[52:55], v[172:175], v[204:207]\n"
    "v_mfma_f32_16x16x32_bf16 v[208:211], v[56:59], v[152:155], 0\n"
    "v_mfma_f32_16x16x32_bf16 v[212:215], v[80:83], v[152:155], 0\n"
    "v_mfma_f32_16x16x32_bf16 v[208:211], v[60:63], v[156:159], v[208:211]\n"
    "v_mfma_f32_16x16x32_bf16 v[212:215], v[84:87], v[156:159], v[212:215]\n"
    "s_waitcnt lgkmcnt(1)\n"
    "v_cmp_gt_u32 vcc, 1, v228\n"
    "v_mfma_f32_16x16x32_bf16 v[208:211], v[64:67], v[160:163], v[208:211]\n"
    "v_cvt_pk_bf16_f32 v200, v200, v201\n"
    "s_cbranch_vccnz .Lg7\n"
    ".Lg7b:\n"
    "v_mfma_f32_16x16x32_bf16 v[212:215], v[88:91], v[160:163], v[212:215]\n"
    "v_cvt_pk_bf16_f32 v201, v202, v203\n"
    "v_cvt_pk_bf16_f32 v202, v204, v205\n"
    "v_mfma_f32_16x16x32_bf16 v[208:211], v[68:71], v[164:167], v[208:211]\n"
    "v_cvt_pk_bf16_f32 v203, v206, v207\n"
    "v_mfma_f32_16x16x32_bf16 v[212:215], v[92:95], v[164:167], v[212:215]\n"
    "ds_write_b128 v7, v[200:203] offset:6144\n"
    "v_mfma_f32_16x16x32_bf16 v[208:211], v[72:75], v[168:171], v[208:211]\n"
    "v_mfma_f32_16x16x32_bf16 v[212:215], v[96:99], v[168:171], v[212:215]\n"
    "v_mfma_f32_16x16x32_bf16 v[208:211], v[76:79], v[172:175], v[208:211]\n"
    "v_mfma_f32_16x16x32_bf16 v[212:215], v[100:103], v[172:175], v[212:215]\n"
    "v_mfma_f32_16x16x32_bf16 v[216:219], v[104:107], v[152:155], 0\n"
    "ds_read_b128 v[176:179], v3 offset:62272\n"
    "ds_read_b128 v[180:183], v3 offset:62336\n"
    "v_mfma_f32_16x16x32_bf16 v[220:223], v[128:131], v[152:155], 0\n"
    "ds_read_b128 v[184:187], v3 offset:62400\n"
    "ds_read_b128 v[188:191], v3 offset:62464\n"
    "v_mfma_f32_16x16x32_bf16 v[216:219], v[108:111], v[156:159], v[216:219]\n"
    "ds_read_b128 v[192:195], v3 offset:62528\n"
    "ds_read_b128 v[196:199], v3 offset:62592\n"
    "v_mfma_f32_16x16x32_bf16 v[220:223], v[132:135], v[156:159], v[220:223]\n"
    "v_cvt_pk_bf16_f32 v208, v208, v209\n"
    "v_cvt_pk_bf16_f32 v209, v210, v211\n"
    "v_mfma_f32_16x16x32_bf16 v[216:219], v[112:115], v[160:163], v[216:219]\n"
    "v_cvt_pk_bf16_f32 v210, v212, v213\n"
    "v_cvt_pk_bf16_f32 v211, v214, v215\n"
    "v_mfma_f32_16x16x32_bf16 v[220:223], v[136:139], v[160:163], v[220:223]\n"
    "ds_write_b128 v7, v[208:211] offset:7168\n"
    "v_mfma_f32_16x16x32_bf16 v[216:219], v[116:119], v[164:167], v[216:219]\n"
    "s_waitcnt lgkmcnt(8)\n"
    "v_cmp_gt_u32 vcc, 1, v239\n"
    "v_mfma_f32_16x16x32_bf16 v[220:223], v[140:143], v[164:167], v[220:223]\n"
    "v_mfma_f32_16x16x32_bf16 v[216:219], v[120:123], v[168:171], v[216:219]\n"
    "s_cbranch_vccnz .Lg8\n"
    ".Lg8b:\n"
    "v_mfma_f32_16x16x32_bf16 v[220:223], v[144:147], v[168:171], v[220:223]\n"
    "v_mfma_f32_16x16x32_bf16 v[216:219], v[124:127], v[172:175], v[216:219]\n"
    "v_mfma_f32_16x16x32_bf16 v[220:223], v[148:151], v[172:175], v[220:223]\n"
    "s_waitcnt lgkmcnt(1)\n"
    "v_mfma_f32_16x16x32_bf16 v[200:203], v[8:11], v[176:179], 0\n"
    "v_mov_b32 v227, 5\n"
    "v_mfma_f32_16x16x32_bf16 v[204:207], v[32:35], v[176:179], 0\n"
    "v_mfma_f32_16x16x32_bf16 v[200:203], v[12:15], v[180:183], v[200:203]\n"
    "v_mfma_f32_16x16x32_bf16 v[204:207], v[36:39], v[180:183], v[204:207]\n"
    "v_mfma_f32_16x16x32_bf16 v[200:203], v[16:19], v[184:187], v[200:203]\n"
    "v_cvt_pk_bf16_f32 v216, v216, v217\n"
    "v_cvt_pk_bf16_f32 v217, v218, v219\n"
    "v_mfma_f32_16x16x32_bf16 v[204:207], v[40:43], v[184:187], v[204:207]\n"
    "v_cvt_pk_bf16_f32 v218, v220, v221\n"
    "v_cvt_pk_bf16_f32 v219, v222, v223\n"
    "v_mfma_f32_16x16x32_bf16 v[200:203], v[20:23], v[188:191], v[200:203]\n"
    "ds_write_b128 v7, v[216:219] offset:8192\n"
    "v_mfma_f32_16x16x32_bf16 v[204:207], v[44:47], v[188:191], v[204:207]\n"
    "ds_write_b32 v225, v227 offset:16\n"
    "ds_read_b32 v228, v224 offset:224\n"
    "v_mfma_f32_16x16x32_bf16 v[200:203], v[24:27], v[192:195], v[200:203]\n"
    "ds_read_b32 v239, v224 offset:80\n"
    "v_mfma_f32_16x16x32_bf16 v[204:207], v[48:51], v[192:195], v[204:207]\n"
    "v_mfma_f32_16x16x32_bf16 v[200:203], v[28:31], v[196:199], v[200:203]\n"
    "v_mfma_f32_16x16x32_bf16 v[204:207], v[52:55], v[196:199], v[204:207]\n"
    "v_mfma_f32_16x16x32_bf16 v[208:211], v[56:59], v[176:179], 0\n"
    "v_mfma_f32_16x16x32_bf16 v[212:215], v[80:83], v[176:179], 0\n"
    "ds_read_b32 v229, v224 offset:0\n"
    "v_mfma_f32_16x16x32_bf16 v[208:211], v[60:63], v[180:183], v[208:211]\n"
    "v_mfma_f32_16x16x32_bf16 v[212:215], v[84:87], v[180:183], v[212:215]\n"
    "s_waitcnt lgkmcnt(2)\n"
    "v_cmp_gt_u32 vcc, 1, v228\n"
    "v_mfma_f32_16x16x32_bf16 v[208:211], v[64:67], v[184:187], v[208:211]\n"
    "v_cvt_pk_bf16_f32 v200, v200, v201\n"
    "s_cbranch_vccnz .Lg10\n"
    ".Lg10b:\n"
    "v_mfma_f32_16x16x32_bf16 v[212:215], v[88:91], v[184:187], v[212:215]\n"
    "v_cvt_pk_bf16_f32 v201, v202, v203\n"
    "v_cvt_pk_bf16_f32 v202, v204, v205\n"
    "v_mfma_f32_16x16x32_bf16 v[208:211], v[68:71], v[188:191], v[208:211]\n"
    "v_cvt_pk_bf16_f32 v203, v206, v207\n"
    "v_mfma_f32_16x16x32_bf16 v[212:215], v[92:95], v[188:191], v[212:215]\n"
    "ds_write_b128 v7, v[200:203] offset:12288\n"
    "v_mfma_f32_16x16x32_bf16 v[208:211], v[72:75], v[192:195], v[208:211]\n"
    "v_mfma_f32_16x16x32_bf16 v[212:215], v[96:99], v[192:195], v[212:215]\n"
    "s_waitcnt lgkmcnt(1)\n"
    "v_cmp_gt_u32 vcc, 6, v229\n"
    "v_mfma_f32_16x16x32_bf16 v[208:211], v[76:79], v[196:199], v[208:211]\n"
    "v_mfma_f32_16x16x32_bf16 v[212:215], v[100:103], v[196:199], v[212:215]\n"
    "s_cbranch_vccnz .Lg9\n"
    ".Lg9b:\n"
    "v_mfma_f32_16x16x32_bf16 v[216:219], v[104:107], v[176:179], 0\n"
    "ds_read_b128 v[152:155], v4 offset:8896\n"
    "ds_read_b128 v[156:159], v4 offset:8960\n"
    "v_mfma_f32_16x16x32_bf16 v[220:223], v[128:131], v[176:179], 0\n"
    "ds_read_b128 v[160:163], v4 offset:9024\n"
    "ds_read_b128 v[164:167], v4 offset:9088\n"
    "v_mfma_f32_16x16x32_bf16 v[216:219], v[108:111], v[180:183], v[216:219]\n"
    "ds_read_b128 v[168:171], v4 offset:9152\n"
    "ds_read_b128 v[172:175], v4 offset:9216\n"
    "v_mfma_f32_16x16x32_bf16 v[220:223], v[132:135], v[180:183], v[220:223]\n"
    "v_cvt_pk_bf16_f32 v208, v208, v209\n"
    "v_cvt_pk_bf16_f32 v209, v210, v211\n"
    "v_mfma_f32_16x16x32_bf16 v[216:219], v[112:115], v[184:187], v[216:219]\n"
    "v_cvt_pk_bf16_f32 v210, v212, v213\n"
    "v_cvt_pk_bf16_f32 v211, v214, v215\n"
    "v_mfma_f32_16x16x32_bf16 v[220:223], v[136:139], v[184:187], v[220:223]\n"
    "ds_write_b128 v7, v[208:211] offset:13312\n"
    "v_mfma_f32_16x16x32_bf16 v[216:219], v[116:119], v[188:191], v[216:219]\n"
    "v_cmp_gt_u32 vcc, 1, v239\n"
    "v_mfma_f32_16x16x32_bf16 v[220:223], v[140:143], v[188:191], v[220:223]\n"
    "s_cbranch_vccnz .Lg11\n"
    ".Lg11b:\n"
    "v_mfma_f32_16x16x32_bf16 v[216:219], v[120:123], v[192:195], v[216:219]\n"
    "s_add_u32 s60, s42, s44\n"
    "s_addc_u32 s61, s43, 0\n"
    "v_mfma_f32_16x16x32_bf16 v[220:223], v[144:147], v[192:195], v[220:223]\n"
    "s_add_u32 m0, s54, 0\n"
    "v_mfma_f32_16x16x32_bf16 v[216:219], v[124:127], v[196:199], v[216:219]\n"
    "global_load_lds_dwordx4 v230, s[60:61]\n"
    "s_add_u32 m0, s54, 2048\n"
    "v_mfma_f32_16x16x32_bf16 v[220:223], v[148:151], v[196:199], v[220:223]\n"
    "global_load_lds_dwordx4 v231, s[60:61]\n"
    "s_add_u32 m0, s54, 4096\n"
    "s_waitcnt lgkmcnt(1)\n"
    "v_mfma_f32_16x16x32_bf16 v[200:203], v[8:11], v[152:155], 0\n"
    "v_mov_b32 v227, 6\n"
    "v_cvt_pk_bf16_f32 v216, v216, v217\n"
    "v_mfma_f32_16x16x32_bf16 v[204:207], v[32:35], v[152:155], 0\n"
    "v_cvt_pk_bf16_f32 v217, v218, v219\n"
    "v_cvt_pk_bf16_f32 v218, v220, v221\n"
    "v_mfma_f32_16x16x32_bf16 v[200:203], v[12:15], v[156:159], v[200:203]\n"
    "v_cvt_pk_bf16_f32 v219, v222, v223\n"
    "v_mfma_f32_16x16x32_bf16 v[204:207], v[36:39], v[156:159], v[204:207]\n"
    "ds_write_b128 v7, v[216:219] offset:14336\n"
    "v_mfma_f32_16x16x32_bf16 v[200:203], v[16:19], v[160:163], v[200:203]\n"
    "ds_write_b32 v225, v227 offset:16\n"
    "ds_read_b32 v228, v224 offset:240\n"
    "v_mfma_f32_16x16x32_bf16 v[204:207], v[40:43], v[160:163], v[204:207]\n"
    "ds_read_b32 v239, v224 offset:96\n"
    "global_load_lds_dwordx4 v232, s[60:61]\n"
    "v_mfma_f32_16x16x32_bf16 v[200:203], v[20:23], v[164:167], v[200:203]\n"
    "s_add_u32 m0, s55, 0\n"
    "v_mfma_f32_16x16x32_bf16 v[204:207], v[44:47], v[164:167], v[204:207]\n"
    "global_load_lds_dwordx4 v233, s[60:61]\n"
    "s_add_u32 m0, s56, 0\n"
    "v_mfma_f32_16x16x32_bf16 v[200:203], v[24:27], v[168:171], v[200:203]\n"
    "global_load_lds_dwordx4 v234, s[60:61]\n"
    "v_mfma_f32_16x16x32_bf16 v[204:207], v[48:51], v[168:171], v[204:207]\n"
    "s_add_u32 s60, s42, s45\n"
    "s_addc_u32 s61, s43, 0\n"
    "v_mfma_f32_16x16x32_bf16 v[200:203], v[28:31], v[172:175], v[200:203]\n"
    "s_add_u32 m0, s54, 8896\n"
    "v_mfma_f32_16x16x32_bf16 v[204:207], v[52:55], v[172:175], v[204:207]\n"
    "global_load_lds_dwordx4 v230, s[60:61]\n"
    "s_add_u32 m0, s54, 10944\n"
    "v_mfma_f32_16x16x32_bf16 v[208:211], v[56:59], v[152:155], 0\n"
    "s_waitcnt lgkmcnt(1)\n"
    "v_cmp_gt_u32 vcc, 1, v228\n"
    "v_mfma_f32_16x16x32_bf16 v[212:215], v[80:83], v[152:155], 0\n"
    "v_cvt_pk_bf16_f32 v200, v200, v201\n"
    "s_cbranch_vccnz .Lg12\n"
    ".Lg12b:\n"
    "v_mfma_f32_16x16x32_bf16 v[208:211], v[60:63], v[156:159], v[208:211]\n"
    "v_cvt_pk_bf16_f32 v201, v202, v203\n"
    "v_cvt_pk_bf16_f32 v202, v204, v205\n"
    "v_mfma_f32_16x16x32_bf16 v[212:215], v[84:87], v[156:159], v[212:215]\n"
    "v_cvt_pk_bf16_f32 v203, v206, v207\n"
    "v_mfma_f32_16x16x32_bf16 v[208:211], v[64:67], v[160:163], v[208:211]\n"
    "ds_write_b128 v7, v[200:203] offset:0\n"
    "v_mfma_f32_16x16x32_bf16 v[212:215], v[88:91], v[160:163], v[212:215]\n"
    "global_load_lds_dwordx4 v231, s[60:61]\n"
    "s_add_u32 m0, s54, 12992\n"
    "v_mfma_f32_16x16x32_bf16 v[208:211], v[68:71], v[164:167], v[208:211]\n"
    "global_load_lds_dwordx4 v232, s[60:61]\n"
    "s_add_u32 m0, s55, 8896\n"
    "v_mfma_f32_16x16x32_bf16 v[212:215], v[92:95], v[164:167], v[212:215]\n"
    "global_load_lds_dwordx4 v233, s[60:61]\n"
    "s_add_u32 m0, s56, 8896\n"
    "v_mfma_f32_16x16x32_bf16 v[208:211], v[72:75], v[168:171], v[208:211]\n"
    "global_load_lds_dwordx4 v234, s[60:61]\n"
    "v_mfma_f32_16x16x32_bf16 v[212:215], v[96:99], v[168:171], v[212:215]\n"
    "v_mfma_f32_16x16x32_bf16 v[208:211], v[76:79], v[172:175], v[208:211]\n"
    "v_mfma_f32_16x16x32_bf16 v[212:215], v[100:103], v[172:175], v[212:215]\n"
    "v_mfma_f32_16x16x32_bf16 v[216:219], v[104:107], v[152:155], 0\n"
    "ds_read_b128 v[176:179], v5 offset:8896\n"
    "ds_read_b128 v[180:183], v5 offset:8960\n"
    "v_mfma_f32_16x16x32_bf16 v[220:223], v[128:131], v[152:155], 0\n"
    "ds_read_b128 v[184:187], v5 offset:9024\n"
    "ds_read_b128 v[188:191], v5 offset:9088\n"
    "v_mfma_f32_16x16x32_bf16 v[216:219], v[108:111], v[156:159], v[216:219]\n"
    "ds_read_b128 v[192:195], v5 offset:9152\n"
    "ds_read_b128 v[196:199], v5 offset:9216\n"
    "v_mfma_f32_16x16x32_bf16 v[220:223], v[132:135], v[156:159], v[220:223]\n"
    "v_cvt_pk_bf16_f32 v208, v208, v209\n"
    "v_cvt_pk_bf16_f32 v209, v210, v211\n"
    "v_mfma_f32_16x16x32_bf16 v[216:219], v[112:115], v[160:163], v[216:219]\n"
    "v_cvt_pk_bf16_f32 v210, v212, v213\n"
    "v_cvt_pk_bf16_f32 v211, v214, v215\n"
    "v_mfma_f32_16x16x32_bf16 v[220:223], v[136:139], v[160:163], v[220:223]\n"
    "ds_write_b128 v7, v[208:211] offset:1024\n"
    "v_mfma_f32_16x16x32_bf16 v[216:219], v[116:119], v[164:167], v[216:219]\n"
    "s_waitcnt lgkmcnt(8)\n"
    "v_cmp_gt_u32 vcc, 1, v239\n"
    "v_mfma_f32_16x16x32_bf16 v[220:223], v[140:143], v[164:167], v[220:223]\n"
    "v_mfma_f32_16x16x32_bf16 v[216:219], v[120:123], v[168:171], v[216:219]\n"
    "s_cbranch_vccnz .Lg13\n"
    ".Lg13b:\n"
    "v_mfma_f32_16x16x32_bf16 v[220:223], v[144:147], v[168:171], v[220:223]\n"
    "v_mfma_f32_16x16x32_bf16 v[216:219], v[124:127], v[172:175], v[216:219]\n"
    "v_mfma_f32_16x16x32_bf16 v[220:223], v[148:151], v[172:175], v[220:223]\n"
    "s_waitcnt lgkmcnt(1)\n"
    "v_mfma_f32_16x16x32_bf16 v[200:203], v[8:11], v[176:179], 0\n"
    "v_mov_b32 v227, 7\n"
    "v_mfma_f32_16x16x32_bf16 v[204:207], v[32:35], v[176:179], 0\n"
    "v_mfma_f32_16x16x32_bf16 v[200:203], v[12:15], v[180:183], v[200:203]\n"
    "v_mfma_f32_16x16x32_bf16 v[204:207], v[36:39], v[180:183], v[204:207]\n"
    "v_mfma_f32_16x16x32_bf16 v[200:203], v[16:19], v[184:187], v[200:203]\n"
    "v_cvt_pk_bf16_f32 v216, v216, v217\n"
    "v_cvt_pk_bf16_f32 v217, v218, v219\n"
    "v_mfma_f32_16x16x32_bf16 v[204:207], v[40:43], v[184:187], v[204:207]\n"
    "v_cvt_pk_bf16_f32 v218, v220, v221\n"
    "v_cvt_pk_bf16_f32 v219, v222, v223\n"
    "v_mfma_f32_16x16x32_bf16 v[200:203], v[20:23], v[188:191], v[200:203]\n"
    "ds_write_b128 v7, v[216:219] offset:2048\n"
    "v_mfma_f32_16x16x32_bf16 v[204:207], v[44:47], v[188:191], v[204:207]\n"
    "ds_write_b32 v225, v227 offset:16\n"
    "ds_read_b32 v228, v224 offset:192\n"
    "v_mfma_f32_16x16x32_bf16 v[200:203], v[24:27], v[192:195], v[200:203]\n"
    "ds_read_b32 v239, v224 offset:112\n"
    "v_mfma_f32_16x16x32_bf16 v[204:207], v[48:51], v[192:195], v[204:207]\n"
    "v_mfma_f32_16x16x32_bf16 v[200:203], v[28:31], v[196:199], v[200:203]\n"
    "v_mfma_f32_16x16x32_bf16 v[204:207], v[52:55], v[196:199], v[204:207]\n"
    "v_mfma_f32_16x16x32_bf16 v[208:211], v[56:59], v[176:179], 0\n"
    "v_mfma_f32_16x16x32_bf16 v[212:215], v[80:83], v[176:179], 0\n"
    "ds_read_b32 v229, v224 offset:0\n"
    "v_mfma_f32_16x16x32_bf16 v[208:211], v[60:63], v[180:183], v[208:211]\n"
    "v_mfma_f32_16x16x32_bf16 v[212:215], v[84:87], v[180:183], v[212:215]\n"
    "s_waitcnt lgkmcnt(2)\n"
    "v_cmp_gt_u32 vcc, 2, v228\n"
    "v_mfma_f32_16x16x32_bf16 v[208:211], v[64:67], v[184:187], v[208:211]\n"
    "v_cvt_pk_bf16_f32 v200, v200, v201\n"
    "s_cbranch_vccnz .Lg15\n"
    ".Lg15b:\n"
    "v_mfma_f32_16x16x32_bf16 v[212:215], v[88:91], v[184:187], v[212:215]\n"
    "v_cvt_pk_bf16_f32 v201, v202, v203\n"
    "v_cvt_pk_bf16_f32 v202, v204, v205\n"
    "v_mfma_f32_16x16x32_bf16 v[208:211], v[68:71], v[188:191], v[208:211]\n"
    "v_cvt_pk_bf16_f32 v203, v206, v207\n"
    "v_mfma_f32_16x16x32_bf16 v[212:215], v[92:95], v[188:191], v[212:215]\n"
    "ds_write_b128 v7, v[200:203] offset:6144\n"
    "v_mfma_f32_16x16x32_bf16 v[208:211], v[72:75], v[192:195], v[208:211]\n"
    "v_mfma_f32_16x16x32_bf16 v[212:215], v[96:99], v[192:195], v[212:215]\n"
    "s_waitcnt lgkmcnt(1)\n"
    "v_cmp_gt_u32 vcc, 7, v229\n"
    "v_mfma_f32_16x16x32_bf16 v[208:211], v[76:79], v[196:199], v[208:211]\n"
    "v_mfma_f32_16x16x32_bf16 v[212:215], v[100:103], v[196:199], v[212:215]\n"
    "s_cbranch_vccnz .Lg14\n"
    ".Lg14b:\n"
    "v_mfma_f32_16x16x32_bf16 v[216:219], v[104:107], v[176:179], 0\n"
    "ds_read_b128 v[152:155], v4 offset:26688\n"
    "ds_read_b128 v[156:159], v4 offset:26752\n"
    "v_mfma_f32_16x16x32_bf16 v[220:223], v[128:131], v[176:179], 0\n"
    "ds_read_b128 v[160:163], v4 offset:26816\n"
    "ds_read_b128 v[164:167], v4 offset:26880\n"
    "v_mfma_f32_16x16x32_bf16 v[216:219], v[108:111], v[180:183], v[216:219]\n"
    "ds_read_b128 v[168:171], v4 offset:26944\n"
    "ds_read_b128 v[172:175], v4 offset:27008\n"
    "v_mfma_f32_16x16x32_bf16 v[220:223], v[132:135], v[180:183], v[220:223]\n"
    "v_cvt_pk_bf16_f32 v208, v208, v209\n"
    "v_cvt_pk_bf16_f32 v209, v210, v211\n"
    "v_mfma_f32_16x16x32_bf16 v[216:219], v[112:115], v[184:187], v[216:219]\n"
    "v_cvt_pk_bf16_f32 v210, v212, v213\n"
    "v_cvt_pk_bf16_f32 v211, v214, v215\n"
    "v_mfma_f32_16x16x32_bf16 v[220:223], v[136:139], v[184:187], v[220:223]\n"
    "ds_write_b128 v7, v[208:211] offset:7168\n"
    "v_mfma_f32_16x16x32_bf16 v[216:219], v[116:119], v[188:191], v[216:219]\n"
    "v_cmp_gt_u32 vcc, 1, v239\n"
    "v_mfma_f32_16x16x32_bf16 v[220:223], v[140:143], v[188:191], v[220:223]\n"
    "s_cbranch_vccnz .Lg16\n"
    ".Lg16b:\n"
    "v_mfma_f32_16x16x32_bf16 v[216:219], v[120:123], v[192:195], v[216:219]\n"
    "s_add_u32 s60, s42, s46\n"
    "s_addc_u32 s61, s43, 0\n"
    "v_mfma_f32_16x16x32_bf16 v[220:223], v[144:147], v[192:195], v[220:223]\n"
    "s_add_u32 m0, s54, 17792\n"
    "v_mfma_f32_16x16x32_bf16 v[216:219], v[124:127], v[196:199], v[216:219]\n"
    "global_load_lds_dwordx4 v230, s[60:61]\n"
    "s_add_u32 m0, s54, 19840\n"
    "v_mfma_f32_16x16x32_bf16 v[220:223], v[148:151], v[196:199], v[220:223]\n"
    "global_load_lds_dwordx4 v231, s[60:61]\n"
    "s_add_u32 m0, s54, 21888\n"
    "s_waitcnt lgkmcnt(1)\n"
    "global_load_lds_dwordx4 v232, s[60:61]\n"
    "s_add_u32 m0, s55, 17792\n"
    "s_nop 0\n"
    "global_load_lds_dwordx4 v233, s[60:61]\n"
    "s_add_u32 m0, s56, 17792\n"
    "s_nop 0\n"
    "global_load_lds_dwordx4 v234, s[60:61]\n"
    "s_add_u32 s60, s42, s47\n"
    "s_addc_u32 s61, s43, 0\n"
    "s_add_u32 m0, s54, 26688\n"
    "s_nop 0\n"
    "global_load_lds_dwordx4 v230, s[60:61]\n"
    "s_add_u32 m0, s54, 28736\n"
    "s_nop 0\n"
    "global_load_lds_dwordx4 v231, s[60:61]\n"
    "s_add_u32 m0, s54, 30784\n"
    "s_nop 0\n"
    "global_load_lds_dwordx4 v232, s[60:61]\n"
    "s_add_u32 m0, s55, 26688\n"
    "s_nop 0\n"
    "global_load_lds_dwordx4 v233, s[60:61]\n"
    "s_add_u32 m0, s56, 26688\n"
    "s_nop 0\n"
    "global_load_lds_dwordx4 v234, s[60:61]\n"
    "v_mfma_f32_16x16x32_bf16 v[200:203], v[8:11], v[152:155], 0\n"
    "v_cvt_pk_bf16_f32 v216, v216, v217\n"
    "v_cvt_pk_bf16_f32 v217, v218, v219\n"
    "v_mfma_f32_16x16x32_bf16 v[204:207], v[32:35], v[152:155], 0\n"
    "v_cvt_pk_bf16_f32 v218, v220, v221\n"
    "v_cvt_pk_bf16_f32 v219, v222, v223\n"
    "v_mfma_f32_16x16x32_bf16 v[200:203], v[12:15], v[156:159], v[200:203]\n"
    "ds_write_b128 v7, v[216:219] offset:8192\n"
    "v_mfma_f32_16x16x32_bf16 v[204:207], v[36:39], v[156:159], v[204:207]\n"
    "v_mov_b32 v227, 8\n"
    "v_mfma_f32_16x16x32_bf16 v[200:203], v[16:19], v[160:163], v[200:203]\n"
    "ds_write_b32 v225, v227 offset:16\n"
    "ds_read_b32 v228, v224 offset:208\n"
    "v_mfma_f32_16x16x32_bf16 v[204:207], v[40:43], v[160:163], v[204:207]\n"
    "ds_read_b32 v239, v224 offset:64\n"
    "s_waitcnt vmcnt(10)\n"
    "v_mfma_f32_16x16x32_bf16 v[200:203], v[20:23], v[164:167], v[200:203]\n"
    "v_mov_b32 v227, 8\n"
    "v_mfma_f32_16x16x32_bf16 v[204:207], v[44:47], v[164:167], v[204:207]\n"
    "ds_write_b32 v226, v227 offset:0\n"
    "v_mfma_f32_16x16x32_bf16 v[200:203], v[24:27], v[168:171], v[200:203]\n"
    "v_mfma_f32_16x16x32_bf16 v[204:207], v[48:51], v[168:171], v[204:207]\n"
    "v_mfma_f32_16x16x32_bf16 v[200:203], v[28:31], v[172:175], v[200:203]\n"
    "v_mfma_f32_16x16x32_bf16 v[204:207], v[52:55], v[172:175], v[204:207]\n"
    "v_mfma_f32_16x16x32_bf16 v[208:211], v[56:59], v[152:155], 0\n"
    "s_waitcnt lgkmcnt(2)\n"
    "v_cmp_gt_u32 vcc, 2, v228\n"
    "v_mfma_f32_16x16x32_bf16 v[212:215], v[80:83], v[152:155], 0\n"
    "v_mfma_f32_16x16x32_bf16 v[208:211], v[60:63], v[156:159], v[208:211]\n"
    "s_cbranch_vccnz .Lg17\n"
    ".Lg17b:\n"
    "v_mfma_f32_16x16x32_bf16 v[212:215], v[84:87], v[156:159], v[212:215]\n"
    "v_cvt_pk_bf16_f32 v200, v200, v201\n"
    "v_cvt_pk_bf16_f32 v201, v202, v203\n"
    "v_mfma_f32_16x16x32_bf16 v[208:211], v[64:67], v[160:163], v[208:211]\n"
    "v_cvt_pk_bf16_f32 v202, v204, v205\n"
    "v_cvt_pk_bf16_f32 v203, v206, v207\n"
    "v_mfma_f32_16x16x32_bf16 v[212:215], v[88:91], v[160:163], v[212:215]\n"
    "ds_write_b128 v7, v[200:203] offset:12288\n"
    "v_mfma_f32_16x16x32_bf16 v[208:211], v[68:71], v[164:167], v[208:211]\n"
    "v_mfma_f32_16x16x32_bf16 v[212:215], v[92:95], v[164:167], v[212:215]\n"
    "v_mfma_f32_16x16x32_bf16 v[208:211], v[72:75], v[168:171], v[208:211]\n"
    "v_mfma_f32_16x16x32_bf16 v[212:215], v[96:99], v[168:171], v[212:215]\n"
    "v_mfma_f32_16x16x32_bf16 v[208:211], v[76:79], v[172:175], v[208:211]\n"
    "v_mfma_f32_16x16x32_bf16 v[212:215], v[100:103], v[172:175], v[212:215]\n"
    "v_mfma_f32_16x16x32_bf16 v[216:219], v[104:107], v[152:155], 0\n"
    "ds_read_b128 v[176:179], v5 offset:26688\n"
    "ds_read_b128 v[180:183], v5 offset:26752\n"
    "v_mfma_f32_16x16x32_bf16 v[220:223], v[128:131], v[152:155], 0\n"
    "ds_read_b128 v[184:187], v5 offset:26816\n"
    "ds_read_b128 v[188:191], v5 offset:26880\n"
    "v_mfma_f32_16x16x32_bf16 v[216:219], v[108:111], v[156:159], v[216:219]\n"
    "ds_read_b128 v[192:195], v5 offset:26944\n"
    "ds_read_b128 v[196:199], v5 offset:27008\n"
    "v_mfma_f32_16x16x32_bf16 v[220:223], v[132:135], v[156:159], v[220:223]\n"
    "v_cvt_pk_bf16_f32 v208, v208, v209\n"
    "v_cvt_pk_bf16_f32 v209, v210, v211\n"
    "v_mfma_f32_16x16x32_bf16 v[216:219], v[112:115], v[160:163], v[216:219]\n"
    "v_cvt_pk_bf16_f32 v210, v212, v213\n"
    "v_cvt_pk_bf16_f32 v211, v214, v215\n"
    "v_mfma_f32_16x16x32_bf16 v[220:223], v[136:139], v[160:163], v[220:223]\n"
    "ds_write_b128 v7, v[208:211] offset:13312\n"
    "v_mfma_f32_16x16x32_bf16 v[216:219], v[116:119], v[164:167], v[216:219]\n"
    "s_waitcnt lgkmcnt(9)\n"
    "v_cmp_gt_u32 vcc, 2, v239\n"
    "v_mfma_f32_16x16x32_bf16 v[220:223], v[140:143], v[164:167], v[220:223]\n"
    "v_mfma_f32_16x16x32_bf16 v[216:219], v[120:123], v[168:171], v[216:219]\n"
    "s_cbranch_vccnz .Lg18\n"
    ".Lg18b:\n"
    "v_mfma_f32_16x16x32_bf16 v[220:223], v[144:147], v[168:171], v[220:223]\n"
    "v_mfma_f32_16x16x32_bf16 v[216:219], v[124:127], v[172:175], v[216:219]\n"
    "v_mfma_f32_16x16x32_bf16 v[220:223], v[148:151], v[172:175], v[220:223]\n"
    "s_waitcnt lgkmcnt(1)\n"
    "v_mfma_f32_16x16x32_bf16 v[200:203], v[8:11], v[176:179], 0\n"
    "v_mov_b32 v227, 9\n"
    "v_mfma_f32_16x16x32_bf16 v[204:207], v[32:35], v[176:179], 0\n"
    "v_mfma_f32_16x16x32_bf16 v[200:203], v[12:15], v[180:183], v[200:203]\n"
    "v_mfma_f32_16x16x32_bf16 v[204:207], v[36:39], v[180:183], v[204:207]\n"
    "v_mfma_f32_16x16x32_bf16 v[200:203], v[16:19], v[184:187], v[200:203]\n"
    "v_cvt_pk_bf16_f32 v216, v216, v217\n"
    "v_cvt_pk_bf16_f32 v217, v218, v219\n"
    "v_mfma_f32_16x16x32_bf16 v[204:207], v[40:43], v[184:187], v[204:207]\n"
    "v_cvt_pk_bf16_f32 v218, v220, v221\n"
    "v_cvt_pk_bf16_f32 v219, v222, v223\n"
    "v_mfma_f32_16x16x32_bf16 v[200:203], v[20:23], v[188:191], v[200:203]\n"
    "ds_write_b128 v7, v[216:219] offset:14336\n"
    "v_mfma_f32_16x16x32_bf16 v[204:207], v[44:47], v[188:191], v[204:207]\n"
    "ds_write_b32 v225, v227 offset:16\n"
    "ds_read_b32 v228, v224 offset:224\n"
    "v_mfma_f32_16x16x32_bf16 v[200:203], v[24:27], v[192:195], v[200:203]\n"
    "ds_read_b32 v239, v224 offset:80\n"
    "v_mfma_f32_16x16x32_bf16 v[204:207], v[48:51], v[192:195], v[204:207]\n"
    "v_mfma_f32_16x16x32_bf16 v[200:203], v[28:31], v[196:199], v[200:203]\n"
    "v_mfma_f32_16x16x32_bf16 v[204:207], v[52:55], v[196:199], v[204:207]\n"
    "v_mfma_f32_16x16x32_bf16 v[208:211], v[56:59], v[176:179], 0\n"
    "v_mfma_f32_16x16x32_bf16 v[212:215], v[80:83], v[176:179], 0\n"
    "ds_read_b32 v229, v224 offset:0\n"
    "v_mfma_f32_16x16x32_bf16 v[208:211], v[60:63], v[180:183], v[208:211]\n"
    "v_mfma_f32_16x16x32_bf16 v[212:215], v[84:87], v[180:183], v[212:215]\n"
    "s_waitcnt lgkmcnt(2)\n"
    "v_cmp_gt_u32 vcc, 2, v228\n"
    "v_mfma_f32_16x16x32_bf16 v[208:211], v[64:67], v[184:187], v[208:211]\n"
    "v_cvt_pk_bf16_f32 v200, v200, v201\n"
    "s_cbranch_vccnz .Lg20\n"
    ".Lg20b:\n"
    "v_mfma_f32_16x16x32_bf16 v[212:215], v[88:91], v[184:187], v[212:215]\n"
    "v_cvt_pk_bf16_f32 v201, v202, v203\n"
    "v_cvt_pk_bf16_f32 v202, v204, v205\n"
    "v_mfma_f32_16x16x32_bf16 v[208:211], v[68:71], v[188:191], v[208:211]\n"
    "v_cvt_pk_bf16_f32 v203, v206, v207\n"
    "v_mfma_f32_16x16x32_bf16 v[212:215], v[92:95], v[188:191], v[212:215]\n"
    "ds_write_b128 v7, v[200:203] offset:0\n"
    "v_mfma_f32_16x16x32_bf16 v[208:211], v[72:75], v[192:195], v[208:211]\n"
    "v_mfma_f32_16x16x32_bf16 v[212:215], v[96:99], v[192:195], v[212:215]\n"
    "s_waitcnt lgkmcnt(1)\n"
    "v_cmp_gt_u32 vcc, 8, v229\n"
    "v_mfma_f32_16x16x32_bf16 v[208:211], v[76:79], v[196:199], v[208:211]\n"
    "v_mfma_f32_16x16x32_bf16 v[212:215], v[100:103], v[196:199], v[212:215]\n"
    "s_cbranch_vccnz .Lg19\n"
    ".Lg19b:\n"
    "v_mfma_f32_16x16x32_bf16 v[216:219], v[104:107], v[176:179], 0\n"
    "v_add_u32 v4, v6, v4\n"
    "v_cmp_gt_u32 vcc, 2, v239\n"
    "v_mfma_f32_16x16x32_bf16 v[220:223], v[128:131], v[176:179], 0\n"
    "ds_read_b128 v[152:155], v4 offset:0\n"
    "ds_read_b128 v[156:159], v4 offset:64\n"
    "v_mfma_f32_16x16x32_bf16 v[216:219], v[108:111], v[180:183], v[216:219]\n"
    "ds_read_b128 v[160:163], v4 offset:128\n"
    "ds_read_b128 v[164:167], v4 offset:192\n"
    "v_mfma_f32_16x16x32_bf16 v[220:223], v[132:135], v[180:183], v[220:223]\n"
    "ds_read_b128 v[168:171], v4 offset:256\n"
    "ds_read_b128 v[172:175], v4 offset:320\n"
    "v_mfma_f32_16x16x32_bf16 v[216:219], v[112:115], v[184:187], v[216:219]\n"
    "v_cvt_pk_bf16_f32 v208, v208, v209\n"
    "v_cvt_pk_bf16_f32 v209, v210, v211\n"
    "v_mfma_f32_16x16x32_bf16 v[220:223], v[136:139], v[184:187], v[220:223]\n"
    "v_cvt_pk_bf16_f32 v210, v212, v213\n"
    "v_cvt_pk_bf16_f32 v211, v214, v215\n"
    "v_mfma_f32_16x16x32_bf16 v[216:219], v[116:119], v[188:191], v[216:219]\n"
    "ds_write_b128 v7, v[208:211] offset:1024\n"
    "v_mfma_f32_16x16x32_bf16 v[220:223], v[140:143], v[188:191], v[220:223]\n"
    "s_cbranch_vccnz .Lg21\n"
    ".Lg21b:\n"
    "v_mfma_f32_16x16x32_bf16 v[216:219], v[120:123], v[192:195], v[216:219]\n"
    "s_add_u32 s60, s42, s48\n"
    "s_addc_u32 s61, s43, 0\n"
    "v_mfma_f32_16x16x32_bf16 v[220:223], v[144:147], v[192:195], v[220:223]\n"
    "s_add_u32 m0, s54, 35584\n"
    "v_mfma_f32_16x16x32_bf16 v[216:219], v[124:127], v[196:199], v[216:219]\n"
    "global_load_lds_dwordx4 v230, s[60:61]\n"
    "s_add_u32 m0, s54, 37632\n"
    "v_mfma_f32_16x16x32_bf16 v[220:223], v[148:151], v[196:199], v[220:223]\n"
    "global_load_lds_dwordx4 v231, s[60:61]\n"
    "s_add_u32 m0, s54, 39680\n"
    "s_waitcnt lgkmcnt(1)\n"
    "global_load_lds_dwordx4 v232, s[60:61]\n"
    "s_add_u32 m0, s55, 35584\n"
    "s_nop 0\n"
    "global_load_lds_dwordx4 v233, s[60:61]\n"
    "s_add_u32 m0, s56, 35584\n"
    "s_nop 0\n"
    "global_load_lds_dwordx4 v234, s[60:61]\n"
    "s_add_u32 s60, s42, s49\n"
    "s_addc_u32 s61, s43, 0\n"
    "s_add_u32 m0, s54, 44480\n"
    "s_nop 0\n"
    "global_load_lds_dwordx4 v230, s[60:61]\n"
    "s_add_u32 m0, s54, 46528\n"
    "s_nop 0\n"
    "global_load_lds_dwordx4 v231, s[60:61]\n"
    "s_add_u32 m0, s54, 48576\n"
    "s_nop 0\n"
    "global_load_lds_dwordx4 v232, s[60:61]\n"
    "s_add_u32 m0, s55, 44480\n"
    "s_nop 0\n"
    "global_load_lds_dwordx4 v233, s[60:61]\n"
    "s_add_u32 m0, s56, 44480\n"
    "s_nop 0\n"
    "global_load_lds_dwordx4 v234, s[60:61]\n"
    "v_mfma_f32_16x16x32_bf16 v[200:203], v[8:11], v[152:155], 0\n"
    "v_cvt_pk_bf16_f32 v216, v216, v217\n"
    "v_cvt_pk_bf16_f32 v217, v218, v219\n"
    "v_mfma_f32_16x16x32_bf16 v[204:207], v[32:35], v[152:155], 0\n"
    "v_cvt_pk_bf16_f32 v218, v220, v221\n"
    "v_cvt_pk_bf16_f32 v219, v222, v223\n"
    "v_mfma_f32_16x16x32_bf16 v[200:203], v[12:15], v[156:159], v[200:203]\n"
    "ds_write_b128 v7, v[216:219] offset:2048\n"
    "v_mfma_f32_16x16x32_bf16 v[204:207], v[36:39], v[156:159], v[204:207]\n"
    "v_mov_b32 v227, 10\n"
    "v_mfma_f32_16x16x32_bf16 v[200:203], v[16:19], v[160:163], v[200:203]\n"
    "ds_write_b32 v225, v227 offset:16\n"
    "ds_read_b32 v228, v224 offset:240\n"
    "v_mfma_f32_16x16x32_bf16 v[204:207], v[40:43], v[160:163], v[204:207]\n"
    "s_waitcnt vmcnt(10)\n"
    "v_mov_b32 v227, 9\n"
    "v_mfma_f32_16x16x32_bf16 v[200:203], v[20:23], v[164:167], v[200:203]\n"
    "ds_write_b32 v226, v227 offset:0\n"
    "v_mfma_f32_16x16x32_bf16 v[204:207], v[44:47], v[164:167], v[204:207]\n"
    "v_mfma_f32_16x16x32_bf16 v[200:203], v[24:27], v[168:171], v[200:203]\n"
    "v_mfma_f32_16x16x32_bf16 v[204:207], v[48:51], v[168:171], v[204:207]\n"
    "v_mfma_f32_16x16x32_bf16 v[200:203], v[28:31], v[172:175], v[200:203]\n"
    "v_mfma_f32_16x16x32_bf16 v[204:207], v[52:55], v[172:175], v[204:207]\n"
    "v_mfma_f32_16x16x32_bf16 v[208:211], v[56:59], v[152:155], 0\n"
    "s_waitcnt lgkmcnt(1)\n"
    "v_cmp_gt_u32 vcc, 2, v228\n"
    "v_mfma_f32_16x16x32_bf16 v[212:215], v[80:83], v[152:155], 0\n"
    "v_mfma_f32_16x16x32_bf16 v[208:211], v[60:63], v[156:159], v[208:211]\n"
    "s_cbranch_vccnz .Lg22\n"
    ".Lg22b:\n"
    "v_mfma_f32_16x16x32_bf16 v[212:215], v[84:87], v[156:159], v[212:215]\n"
    "v_cvt_pk_bf16_f32 v200, v200, v201\n"
    "v_cvt_pk_bf16_f32 v201, v202, v203\n"
    "v_mfma_f32_16x16x32_bf16 v[208:211], v[64:67], v[160:163], v[208:211]\n"
    "v_cvt_pk_bf16_f32 v202, v204, v205\n"
    "v_cvt_pk_bf16_f32 v203, v206, v207\n"
    "v_mfma_f32_16x16x32_bf16 v[212:215], v[88:91], v[160:163], v[212:215]\n"
    "ds_write_b128 v7, v[200:203] offset:6144\n"
    "v_mfma_f32_16x16x32_bf16 v[208:211], v[68:71], v[164:167], v[208:211]\n"
    "v_mfma_f32_16x16x32_bf16 v[212:215], v[92:95], v[164:167], v[212:215]\n"
    "v_mfma_f32_16x16x32_bf16 v[208:211], v[72:75], v[168:171], v[208:211]\n"
    "v_mfma_f32_16x16x32_bf16 v[212:215], v[96:99], v[168:171], v[212:215]\n"
    "v_mfma_f32_16x16x32_bf16 v[208:211], v[76:79], v[172:175], v[208:211]\n"
    "v_mfma_f32_16x16x32_bf16 v[212:215], v[100:103], v[172:175], v[212:215]\n"
    "v_mfma_f32_16x16x32_bf16 v[216:219], v[104:107], v[152:155], 0\n"
    "v_add_u32 v5, v6, v5\n"
    "v_mfma_f32_16x16x32_bf16 v[220:223], v[128:131], v[152:155], 0\n"
    "ds_read_b128 v[176:179], v5 offset:0\n"
    "ds_read_b128 v[180:183], v5 offset:64\n"
    "v_mfma_f32_16x16x32_bf16 v[216:219], v[108:111], v[156:159], v[216:219]\n"
    "ds_read_b128 v[184:187], v5 offset:128\n"
    "ds_read_b128 v[188:191], v5 offset:192\n"
    "v_mfma_f32_16x16x32_bf16 v[220:223], v[132:135], v[156:159], v[220:223]\n"
    "ds_read_b128 v[192:195], v5 offset:256\n"
    "ds_read_b128 v[196:199], v5 offset:320\n"
    "v_mfma_f32_16x16x32_bf16 v[216:219], v[112:115], v[160:163], v[216:219]\n"
    "v_cvt_pk_bf16_f32 v208, v208, v209\n"
    "v_cvt_pk_bf16_f32 v209, v210, v211\n"
    "v_mfma_f32_16x16x32_bf16 v[220:223], v[136:139], v[160:163], v[220:223]\n"
    "v_cvt_pk_bf16_f32 v210, v212, v213\n"
    "v_cvt_pk_bf16_f32 v211, v214, v215\n"
    "v_mfma_f32_16x16x32_bf16 v[216:219], v[116:119], v[164:167], v[216:219]\n"
    "ds_write_b128 v7, v[208:211] offset:7168\n"
    "v_mfma_f32_16x16x32_bf16 v[220:223], v[140:143], v[164:167], v[220:223]\n"
    "v_mfma_f32_16x16x32_bf16 v[216:219], v[120:123], v[168:171], v[216:219]\n"
    "v_mfma_f32_16x16x32_bf16 v[220:223], v[144:147], v[168:171], v[220:223]\n"
    "v_mfma_f32_16x16x32_bf16 v[216:219], v[124:127], v[172:175], v[216:219]\n"
    "v_mfma_f32_16x16x32_bf16 v[220:223], v[148:151], v[172:175], v[220:223]\n"
    "s_waitcnt lgkmcnt(1)\n"
    "s_lshl1_add_u32 s53, s58, s50\n"
    "s_cmp_eq_u32 s53, 2\n"
    "s_cbranch_scc1 .Lg_e12a\n"
    "s_cmp_eq_u32 s53, 3\n"
    "s_cbranch_scc1 .Lg_e12b\n"
    "v_mfma_f32_16x16x32_bf16 v[200:203], v[8:11], v[176:179], 0\n"
    "v_cvt_pk_bf16_f32 v216, v216, v217\n"
    "v_cvt_pk_bf16_f32 v217, v218, v219\n"
    "v_mfma_f32_16x16x32_bf16 v[204:207], v[32:35], v[176:179], 0\n"
    "v_cvt_pk_bf16_f32 v218, v220, v221\n"
    "v_cvt_pk_bf16_f32 v219, v222, v223\n"
    "v_mfma_f32_16x16x32_bf16 v[200:203], v[12:15], v[180:183], v[200:203]\n"
    "ds_write_b128 v7, v[216:219] offset:8192\n"
    "v_mfma_f32_16x16x32_bf16 v[204:207], v[36:39], v[180:183], v[204:207]\n"
    "v_mov_b32 v227, 11\n"
    "v_mfma_f32_16x16x32_bf16 v[200:203], v[16:19], v[184:187], v[200:203]\n"
    "ds_write_b32 v225, v227 offset:16\n"
    "ds_read_b32 v228, v224 offset:192\n"
    "v_mfma_f32_16x16x32_bf16 v[204:207], v[40:43], v[184:187], v[204:207]\n"
    "v_mfma_f32_16x16x32_bf16 v[200:203], v[20:23], v[188:191], v[200:203]\n"
    "v_mfma_f32_16x16x32_bf16 v[204:207], v[44:47], v[188:191], v[204:207]\n"
    "v_mfma_f32_16x16x32_bf16 v[200:203], v[24:27], v[192:195], v[200:203]\n"
    "v_mfma_f32_16x16x32_bf16 v[204:207], v[48:51], v[192:195], v[204:207]\n"
    "v_mfma_f32_16x16x32_bf16 v[200:203], v[28:31], v[196:199], v[200:203]\n"
    "v_mfma_f32_16x16x32_bf16 v[204:207], v[52:55], v[196:199], v[204:207]\n"
    "v_mfma_f32_16x16x32_bf16 v[208:211], v[56:59], v[176:179], 0\n"
    "s_waitcnt lgkmcnt(0)\n"
    "v_cmp_gt_u32 vcc, 3, v228\n"
    "v_mfma_f32_16x16x32_bf16 v[212:215], v[80:83], v[176:179], 0\n"
    "ds_read_b32 v229, v224 offset:0\n"
    "s_cbranch_vccnz .Lg27\n"
    ".Lg27b:\n"
    "v_mfma_f32_16x16x32_bf16 v[208:211], v[60:63], v[180:183], v[208:211]\n"
    "v_cvt_pk_bf16_f32 v200, v200, v201\n"
    "v_cvt_pk_bf16_f32 v201, v202, v203\n"
    "v_mfma_f32_16x16x32_bf16 v[212:215], v[84:87], v[180:183], v[212:215]\n"
    "v_cvt_pk_bf16_f32 v202, v204, v205\n"
    "v_cvt_pk_bf16_f32 v203, v206, v207\n"
    "v_mfma_f32_16x16x32_bf16 v[208:211], v[64:67], v[184:187], v[208:211]\n"
    "ds_write_b128 v7, v[200:203] offset:12288\n"
    "v_mfma_f32_16x16x32_bf16 v[212:215], v[88:91], v[184:187], v[212:215]\n"
    "v_mfma_f32_16x16x32_bf16 v[208:211], v[68:71], v[188:191], v[208:211]\n"
    "v_mfma_f32_16x16x32_bf16 v[212:215], v[92:95], v[188:191], v[212:215]\n"
    "v_mfma_f32_16x16x32_bf16 v[208:211], v[72:75], v[192:195], v[208:211]\n"
    "v_mfma_f32_16x16x32_bf16 v[212:215], v[96:99], v[192:195], v[212:215]\n"
    "s_waitcnt lgkmcnt(1)\n"
    "v_cmp_gt_u32 vcc, 9, v229\n"
    "v_mfma_f32_16x16x32_bf16 v[208:211], v[76:79], v[196:199], v[208:211]\n"
    "v_mfma_f32_16x16x32_bf16 v[212:215], v[100:103], v[196:199], v[212:215]\n"
    "s_cbranch_vccnz .Lg26\n"
    ".Lg26b:\n"
    "v_mfma_f32_16x16x32_bf16 v[216:219], v[104:107], v[176:179], 0\n"
    "v_add_u32 v2, s84, v2\n"
    "v_mfma_f32_16x16x32_bf16 v[220:223], v[128:131], v[176:179], 0\n"
    "ds_read_b128 v[152:155], v2 offset:8896\n"
    "ds_read_b128 v[156:159], v2 offset:8960\n"
    "v_mfma_f32_16x16x32_bf16 v[216:219], v[108:111], v[180:183], v[216:219]\n"
    "ds_read_b128 v[160:163], v2 offset:9024\n"
    "ds_read_b128 v[164:167], v2 offset:9088\n"
    "v_mfma_f32_16x16x32_bf16 v[220:223], v[132:135], v[180:183], v[220:223]\n"
    "ds_read_b128 v[168:171], v2 offset:9152\n"
    "ds_read_b128 v[172:175], v2 offset:9216\n"
    "v_mfma_f32_16x16x32_bf16 v[216:219], v[112:115], v[184:187], v[216:219]\n"
    "v_cvt_pk_bf16_f32 v208, v208, v209\n"
    "v_cvt_pk_bf16_f32 v209, v210, v211\n"
    "v_mfma_f32_16x16x32_bf16 v[220:223], v[136:139], v[184:187], v[220:223]\n"
    "v_cvt_pk_bf16_f32 v210, v212, v213\n"
    "v_cvt_pk_bf16_f32 v211, v214, v215\n"
    "v_mfma_f32_16x16x32_bf16 v[216:219], v[116:119], v[188:191], v[216:219]\n"
    "ds_write_b128 v7, v[208:211] offset:13312\n"
    "v_mfma_f32_16x16x32_bf16 v[220:223], v[140:143], v[188:191], v[220:223]\n"
    "v_mfma_f32_16x16x32_bf16 v[216:219], v[120:123], v[192:195], v[216:219]\n"
    "v_mfma_f32_16x16x32_bf16 v[220:223], v[144:147], v[192:195], v[220:223]\n"
    "v_mfma_f32_16x16x32_bf16 v[216:219], v[124:127], v[196:199], v[216:219]\n"
    "v_mfma_f32_16x16x32_bf16 v[220:223], v[148:151], v[196:199], v[220:223]\n"
    "s_waitcnt lgkmcnt(1)\n"
    "s_cmp_eq_u32 s50, 0\n"
    "s_cbranch_scc1 .Lg_e13a\n"
    "v_mfma_f32_16x16x32_bf16 v[200:203], v[8:11], v[152:155], 0\n"
    "v_mov_b32 v227, 12\n"
    "v_mfma_f32_16x16x32_bf16 v[204:207], v[32:35], v[152:155], 0\n"
    "v_mfma_f32_16x16x32_bf16 v[200:203], v[12:15], v[156:159], v[200:203]\n"
    "v_cvt_pk_bf16_f32 v216, v216, v217\n"
    "v_cvt_pk_bf16_f32 v217, v218, v219\n"
    "v_mfma_f32_16x16x32_bf16 v[204:207], v[36:39], v[156:159], v[204:207]\n"
    "v_cvt_pk_bf16_f32 v218, v220, v221\n"
    "v_cvt_pk_bf16_f32 v219, v222, v223\n"
    "v_mfma_f32_16x16x32_bf16 v[200:203], v[16:19], v[160:163], v[200:203]\n"
    "ds_write_b128 v7, v[216:219] offset:14336\n"
    "v_mfma_f32_16x16x32_bf16 v[204:207], v[40:43], v[160:163], v[204:207]\n"
    "ds_write_b32 v225, v227 offset:16\n"
    "ds_read_b32 v228, v224 offset:208\n"
    "v_mfma_f32_16x16x32_bf16 v[200:203], v[20:23], v[164:167], v[200:203]\n"
    "s_waitcnt vmcnt(0)\n"
    "v_mov_b32 v227, 10\n"
    "v_mfma_f32_16x16x32_bf16 v[204:207], v[44:47], v[164:167], v[204:207]\n"
    "ds_write_b32 v226, v227 offset:0\n"
    "v_mfma_f32_16x16x32_bf16 v[200:203], v[24:27], v[168:171], v[200:203]\n"
    "v_mfma_f32_16x16x32_bf16 v[204:207], v[48:51], v[168:171], v[204:207]\n"
    "v_mfma_f32_16x16x32_bf16 v[200:203], v[28:31], v[172:175], v[200:203]\n"
    "v_mfma_f32_16x16x32_bf16 v[204:207], v[52:55], v[172:175], v[204:207]\n"
    "ds_read_b32 v239, v224 offset:272\n"
    "s_waitcnt lgkmcnt(0)\n"
    "v_cmp_gt_u32 vcc, 1, v239\n"
    "s_cbranch_vccnz .Lg30\n"
    ".Lg30b:\n"
    "v_mfma_f32_16x16x32_bf16 v[208:211], v[56:59], v[152:155], 0\n"
    "v_cmp_gt_u32 vcc, 3, v228\n"
    "v_mfma_f32_16x16x32_bf16 v[212:215], v[80:83], v[152:155], 0\n"
    "s_cbranch_vccnz .Lg29\n"
    ".Lg29b:\n"
    "v_cvt_pk_bf16_f32 v200, v200, v201\n"
    "v_mfma_f32_16x16x32_bf16 v[208:211], v[60:63], v[156:159], v[208:211]\n"
    "v_cvt_pk_bf16_f32 v201, v202, v203\n"
    "v_cvt_pk_bf16_f32 v202, v204, v205\n"
    "v_mfma_f32_16x16x32_bf16 v[212:215], v[84:87], v[156:159], v[212:215]\n"
    "v_cvt_pk_bf16_f32 v203, v206, v207\n"
    "v_mfma_f32_16x16x32_bf16 v[208:211], v[64:67], v[160:163], v[208:211]\n"
    "ds_write_b128 v7, v[200:203] offset:0\n"
    "v_mfma_f32_16x16x32_bf16 v[212:215], v[88:91], v[160:163], v[212:215]\n"
    "s_add_u32 s60, s40, 109568\n"
    "s_addc_u32 s61, s41, 0\n"
    "v_mfma_f32_16x16x32_bf16 v[208:211], v[68:71], v[164:167], v[208:211]\n"
    "s_add_u32 s66, s60, 6144\n"
    "s_addc_u32 s67, s61, 0\n"
    "v_mfma_f32_16x16x32_bf16 v[212:215], v[92:95], v[164:167], v[212:215]\n"
    "global_load_dwordx4 v[8:11], v235, s[60:61] offset:-3072 sc1\n"
    "global_load_dwordx4 v[32:35], v235, s[66:67] offset:-3072 sc1\n"
    "v_mfma_f32_16x16x32_bf16 v[208:211], v[72:75], v[168:171], v[208:211]\n"
    "global_load_dwordx4 v[12:15], v235, s[60:61] offset:-2048 sc1\n"
    "global_load_dwordx4 v[36:39], v235, s[66:67] offset:-2048 sc1\n"
    "v_mfma_f32_16x16x32_bf16 v[212:215], v[96:99], v[168:171], v[212:215]\n"
    "global_load_dwordx4 v[16:19], v235, s[60:61] offset:-1024 sc1\n"
    "global_load_dwordx4 v[40:43], v235, s[66:67] offset:-1024 sc1\n"
    "v_mfma_f32_16x16x32_bf16 v[208:211], v[76:79], v[172:175], v[208:211]\n"
    "global_load_dwordx4 v[20:23], v235, s[60:61] offset:0 sc1\n"
    "global_load_dwordx4 v[44:47], v235, s[66:67] offset:0 sc1\n"
    "v_mfma_f32_16x16x32_bf16 v[212:215], v[100:103], v[172:175], v[212:215]\n"
    "global_load_dwordx4 v[24:27], v235, s[60:61] offset:1024 sc1\n"
    "global_load_dwordx4 v[48:51], v235, s[66:67] offset:1024 sc1\n"
    "v_mfma_f32_16x16x32_bf16 v[216:219], v[104:107], v[152:155], 0\n"
    "global_load_dwordx4 v[28:31], v235, s[60:61] offset:2048 sc1\n"
    "global_load_dwordx4 v[52:55], v235, s[66:67] offset:2048 sc1\n"
    "v_mfma_f32_16x16x32_bf16 v[220:223], v[128:131], v[152:155], 0\n"
    "v_cvt_pk_bf16_f32 v208, v208, v209\n"
    "v_cvt_pk_bf16_f32 v209, v210, v211\n"
    "v_mfma_f32_16x16x32_bf16 v[216:219], v[108:111], v[156:159], v[216:219]\n"
    "v_cvt_pk_bf16_f32 v210, v212, v213\n"
    "v_cvt_pk_bf16_f32 v211, v214, v215\n"
    "v_mfma_f32_16x16x32_bf16 v[220:223], v[132:135], v[156:159], v[220:223]\n"
    "ds_write_b128 v7, v[208:211] offset:1024\n"
    "v_mfma_f32_16x16x32_bf16 v[216:219], v[112:115], v[160:163], v[216:219]\n"
    "s_add_u32 s60, s40, 146432\n"
    "s_addc_u32 s61, s41, 0\n"
    "v_mfma_f32_16x16x32_bf16 v[220:223], v[136:139], v[160:163], v[220:223]\n"
    "s_add_u32 s66, s60, 6144\n"
    "s_addc_u32 s67, s61, 0\n"
    "v_mfma_f32_16x16x32_bf16 v[216:219], v[116:119], v[164:167], v[216:219]\n"
    "global_load_dwordx4 v[56:59], v235, s[60:61] offset:-3072 sc1\n"
    "global_load_dwordx4 v[80:83], v235, s[66:67] offset:-3072 sc1\n"
    "v_mfma_f32_16x16x32_bf16 v[220:223], v[140:143], v[164:167], v[220:223]\n"
    "global_load_dwordx4 v[60:63], v235, s[60:61] offset:-2048 sc1\n"
    "global_load_dwordx4 v[84:87], v235, s[66:67] offset:-2048 sc1\n"
    "v_mfma_f32_16x16x32_bf16 v[216:219], v[120:123], v[168:171], v[216:219]\n"
    "global_load_dwordx4 v[64:67], v235, s[60:61] offset:-1024 sc1\n"
    "global_load_dwordx4 v[88:91], v235, s[66:67] offset:-1024 sc1\n"
    "v_mfma_f32_16x16x32_bf16 v[220:223], v[144:147], v[168:171], v[220:223]\n"
    "global_load_dwordx4 v[68:71], v235, s[60:61] offset:0 sc1\n"
    "global_load_dwordx4 v[92:95], v235, s[66:67] offset:0 sc1\n"
    "v_mfma_f32_16x16x32_bf16 v[216:219], v[124:127], v[172:175], v[216:219]\n"
    "global_load_dwordx4 v[72:75], v235, s[60:61] offset:1024 sc1\n"
    "global_load_dwordx4 v[96:99], v235, s[66:67] offset:1024 sc1\n"
    "v_mfma_f32_16x16x32_bf16 v[220:223], v[148:151], v[172:175], v[220:223]\n"
    "global_load_dwordx4 v[76:79], v235, s[60:61] offset:2048 sc1\n"
    "global_load_dwordx4 v[100:103], v235, s[66:67] offset:2048 sc1\n"
    "s_nop 2\n"
    "v_cvt_pk_bf16_f32 v216, v216, v217\n"
    "v_cvt_pk_bf16_f32 v217, v218, v219\n"
    "s_nop 0\n"
    "v_cvt_pk_bf16_f32 v218, v220, v221\n"
    "v_cvt_pk_bf16_f32 v219, v222, v223\n"
    "ds_write_b128 v7, v[216:219] offset:2048\n"
    "v_mov_b32 v227, 13\n"
    "ds_write_b32 v225, v227 offset:16\n"
    ".Lg_to:\n"
    "s_waitcnt vmcnt(12)\n"
    "v_sub_u32 v8, v8, v240\n"
    "v_sub_u32 v12, v12, v240\n"
    "v_sub_u32 v16, v16, v240\n"
    "v_sub_u32 v20, v20, v240\n"
    "v_sub_u32 v24, v24, v240\n"
    "v_sub_u32 v28, v28, v240\n"
    "v_sub_u32 v32, v32, v240\n"
    "v_sub_u32 v36, v36, v240\n"
    "v_sub_u32 v40, v40, v240\n"
    "v_sub_u32 v44, v44, v240\n"
    "v_sub_u32 v48, v48, v240\n"
    "v_sub_u32 v52, v52, v240\n"
    "s_waitcnt vmcnt(0)\n"
    "v_sub_u32 v56, v56, v240\n"
    "v_sub_u32 v60, v60, v240\n"
    "v_sub_u32 v64, v64, v240\n"
    "v_sub_u32 v68, v68, v240\n"
    "v_sub_u32 v72, v72, v240\n"
    "v_sub_u32 v76, v76, v240\n"
    "v_sub_u32 v80, v80, v240\n"
    "v_sub_u32 v84, v84, v240\n"
    "v_sub_u32 v88, v88, v240\n"
    "v_sub_u32 v92, v92, v240\n"
    "v_sub_u32 v96, v96, v240\n"
    "v_sub_u32 v100, v100, v240\n"
    "ds_read_b32 v228, v224 offset:144\n"
    "s_waitcnt lgkmcnt(0)\n"
    "v_cmp_gt_u32 vcc, 3, v228\n"
    "s_cbranch_vccnz .Lg31\n"
    ".Lg31b:\n"
    "ds_read_b64_tr_b16 v[176:177], v0 offset:18432\n"
    "ds_read_b64_tr_b16 v[178:179], v1 offset:18432\n"
    "ds_read_b64_tr_b16 v[180:181], v0 offset:19456\n"
    "ds_read_b64_tr_b16 v[182:183], v1 offset:19456\n"
    "ds_read_b64_tr_b16 v[184:185], v0 offset:20480\n"
    "ds_read_b64_tr_b16 v[186:187], v1 offset:20480\n"
    "ds_read_b64_tr_b16 v[188:189], v0 offset:21504\n"
    "ds_read_b64_tr_b16 v[190:191], v1 offset:21504\n"
    "ds_read_b64_tr_b16 v[192:193], v0 offset:22528\n"
    "ds_read_b64_tr_b16 v[194:195], v1 offset:22528\n"
    "ds_read_b64_tr_b16 v[196:197], v0 offset:23552\n"
    "ds_read_b64_tr_b16 v[198:199], v1 offset:23552\n"
    "ds_read_b32 v253, v238 offset:0\n"
    "s_waitcnt lgkmcnt(9)\n"
    "v_mfma_f32_16x16x32_bf16 v[200:203], v[8:11], v[176:179], 0\n"
    "v_mfma_f32_16x16x32_bf16 v[204:207], v[32:35], v[176:179], 0\n"
    "ds_read_b32 v228, v224 offset:160\n"
    "v_mfma_f32_16x16x32_bf16 v[200:203], v[12:15], v[180:183], v[200:203]\n"
    "v_mfma_f32_16x16x32_bf16 v[204:207], v[36:39], v[180:183], v[204:207]\n"
    "s_waitcnt lgkmcnt(8)\n"
    "v_mfma_f32_16x16x32_bf16 v[200:203], v[16:19], v[184:187], v[200:203]\n"
    "v_mfma_f32_16x16x32_bf16 v[204:207], v[40:43], v[184:187], v[204:207]\n"
    "s_waitcnt lgkmcnt(6)\n"
    "v_mfma_f32_16x16x32_bf16 v[200:203], v[20:23], v[188:191], v[200:203]\n"
    "v_mfma_f32_16x16x32_bf16 v[204:207], v[44:47], v[188:191], v[204:207]\n"
    "s_waitcnt lgkmcnt(4)\n"
    "v_mfma_f32_16x16x32_bf16 v[200:203], v[24:27], v[192:195], v[200:203]\n"
    "v_mfma_f32_16x16x32_bf16 v[204:207], v[48:51], v[192:195], v[204:207]\n"
    "s_waitcnt lgkmcnt(2)\n"
    "v_mfma_f32_16x16x32_bf16 v[200:203], v[28:31], v[196:199], v[200:203]\n"
    "s_waitcnt lgkmcnt(0)\n"
    "v_cmp_gt_u32 vcc, 3, v228\n"
    "v_mfma_f32_16x16x32_bf16 v[204:207], v[52:55], v[196:199], v[204:207]\n"
    "s_cbranch_vccnz .Lg32\n"
    "v_mfma_f32_16x16x32_bf16 v[208:211], v[56:59], v[176:179], 0\n"
    "ds_read_b64_tr_b16 v[152:153], v0 offset:6144\n"
    "ds_read_b64_tr_b16 v[154:155], v1 offset:6144\n"
    "v_mfma_f32_16x16x32_bf16 v[212:215], v[80:83], v[176:179], 0\n"
    "ds_read_b64_tr_b16 v[156:157], v0 offset:7168\n"
    "ds_read_b64_tr_b16 v[158:159], v1 offset:7168\n"
    "v_mfma_f32_16x16x32_bf16 v[208:211], v[60:63], v[180:183], v[208:211]\n"
    "ds_read_b64_tr_b16 v[160:161], v0 offset:8192\n"
    "ds_read_b64_tr_b16 v[162:163], v1 offset:8192\n"
    "v_mfma_f32_16x16x32_bf16 v[212:215], v[84:87], v[180:183], v[212:215]\n"
    "ds_read_b64_tr_b16 v[164:165], v0 offset:9216\n"
    "ds_read_b64_tr_b16 v[166:167], v1 offset:9216\n"
    "v_mfma_f32_16x16x32_bf16 v[208:211], v[64:67], v[184:187], v[208:211]\n"
    "v_mul_f32 v200, v200, v253\n"
    "v_mul_f32 v201, v201, v253\n"
    "v_mfma_f32_16x16x32_bf16 v[212:215], v[88:91], v[184:187], v[212:215]\n"
    "ds_read_b64_tr_b16 v[168:169], v0 offset:10240\n"
    "ds_read_b64_tr_b16 v[170:171], v1 offset:10240\n"
    "v_mfma_f32_16x16x32_bf16 v[208:211], v[68:71], v[188:191], v[208:211]\n"
    "v_mul_f32 v202, v202, v253\n"
    "v_mul_f32 v203, v203, v253\n"
    "v_mfma_f32_16x16x32_bf16 v[212:215], v[92:95], v[188:191], v[212:215]\n"
    "ds_read_b64_tr_b16 v[172:173], v0 offset:11264\n"
    "ds_read_b64_tr_b16 v[174:175], v1 offset:11264\n"
    "v_mfma_f32_16x16x32_bf16 v[208:211], v[72:75], v[192:195], v[208:211]\n"
    "v_mul_f32 v204, v204, v253\n"
    "v_mul_f32 v205, v205, v253\n"
    "v_mfma_f32_16x16x32_bf16 v[212:215], v[96:99], v[192:195], v[212:215]\n"
    "ds_read_b32 v252, v238 offset:256\n"
    "v_mul_f32 v206, v206, v253\n"
    "v_mfma_f32_16x16x32_bf16 v[208:211], v[76:79], v[196:199], v[208:211]\n"
    "v_mul_f32 v207, v207, v253\n"
    "v_cvt_pk_bf16_f32 v200, v200, v201\n"
    "v_mfma_f32_16x16x32_bf16 v[212:215], v[100:103], v[196:199], v[212:215]\n"
    "v_cvt_pk_bf16_f32 v201, v202, v203\n"
    "v_cvt_pk_bf16_f32 v202, v204, v205\n"
    "v_cvt_pk_bf16_f32 v203, v206, v207\n"
    "buffer_store_dwordx4 v[200:203], v249, s[80:83], 0 offen offset:128 sc1\n"
    ".Lg32j:\n"
    "s_waitcnt lgkmcnt(1)\n"
    "s_nop 0\n"
    "v_mfma_f32_16x16x32_bf16 v[200:203], v[8:11], v[152:155], 0\n"
    "v_mul_f32 v208, v208, v253\n"
    "v_mul_f32 v209, v209, v253\n"
    "v_mfma_f32_16x16x32_bf16 v[204:207], v[32:35], v[152:155], 0\n"
    "ds_read_b32 v228, v224 offset:176\n"
    "v_mul_f32 v210, v210, v253\n"
    "v_mfma_f32_16x16x32_bf16 v[200:203], v[12:15], v[156:159], v[200:203]\n"
    "v_mul_f32 v211, v211, v253\n"
    "v_mul_f32 v212, v212, v253\n"
    "v_mfma_f32_16x16x32_bf16 v[204:207], v[36:39], v[156:159], v[204:207]\n"
    "v_mul_f32 v213, v213, v253\n"
    "v_mul_f32 v214, v214, v253\n"
    "v_mfma_f32_16x16x32_bf16 v[200:203], v[16:19], v[160:163], v[200:203]\n"
    "v_mul_f32 v215, v215, v253\n"
    "v_cvt_pk_bf16_f32 v208, v208, v209\n"
    "v_mfma_f32_16x16x32_bf16 v[204:207], v[40:43], v[160:163], v[204:207]\n"
    "v_cvt_pk_bf16_f32 v209, v210, v211\n"
    "v_cvt_pk_bf16_f32 v210, v212, v213\n"
    "v_mfma_f32_16x16x32_bf16 v[200:203], v[20:23], v[164:167], v[200:203]\n"
    "v_cvt_pk_bf16_f32 v211, v214, v215\n"
    "v_mfma_f32_16x16x32_bf16 v[204:207], v[44:47], v[164:167], v[204:207]\n"
    "buffer_store_dwordx4 v[208:211], v249, s[80:83], 0 offen offset:320 sc1\n"
    "v_mfma_f32_16x16x32_bf16 v[200:203], v[24:27], v[168:171], v[200:203]\n"
    "v_mfma_f32_16x16x32_bf16 v[204:207], v[48:51], v[168:171], v[204:207]\n"
    "v_add_u32 v249, v251, v249\n"
    "v_mfma_f32_16x16x32_bf16 v[200:203], v[28:31], v[172:175], v[200:203]\n"
    "s_waitcnt lgkmcnt(0)\n"
    "v_cmp_gt_u32 vcc, 3, v228\n"
    "v_mfma_f32_16x16x32_bf16 v[204:207], v[52:55], v[172:175], v[204:207]\n"
    "s_cbranch_vccnz .Lg33\n"
    "v_mfma_f32_16x16x32_bf16 v[208:211], v[56:59], v[152:155], 0\n"
    "ds_read_b64_tr_b16 v[176:177], v0 offset:12288\n"
    "ds_read_b64_tr_b16 v[178:179], v1 offset:12288\n"
    "v_mfma_f32_16x16x32_bf16 v[212:215], v[80:83], v[152:155], 0\n"
    "ds_read_b64_tr_b16 v[180:181], v0 offset:13312\n"
    "ds_read_b64_tr_b16 v[182:183], v1 offset:13312\n"
    "v_mfma_f32_16x16x32_bf16 v[208:211], v[60:63], v[156:159], v[208:211]\n"
    "ds_read_b64_tr_b16 v[184:185], v0 offset:14336\n"
    "ds_read_b64_tr_b16 v[186:187], v1 offset:14336\n"
    "v_mfma_f32_16x16x32_bf16 v[212:215], v[84:87], v[156:159], v[212:215]\n"
    "ds_read_b64_tr_b16 v[188:189], v0 offset:15360\n"
    "ds_read_b64_tr_b16 v[190:191], v1 offset:15360\n"
    "v_mfma_f32_16x16x32_bf16 v[208:211], v[64:67], v[160:163], v[208:211]\n"
    "v_mul_f32 v200, v200, v252\n"
    "v_mul_f32 v201, v201, v252\n"
    "v_mfma_f32_16x16x32_bf16 v[212:215], v[88:91], v[160:163], v[212:215]\n"
    "ds_read_b64_tr_b16 v[192:193], v0 offset:16384\n"
    "ds_read_b64_tr_b16 v[194:195], v1 offset:16384\n"
    "v_mfma_f32_16x16x32_bf16 v[208:211], v[68:71], v[164:167], v[208:211]\n"
    "v_mul_f32 v202, v202, v252\n"
    "v_mul_f32 v203, v203, v252\n"
    "v_mfma_f32_16x16x32_bf16 v[212:215], v[92:95], v[164:167], v[212:215]\n"
    "ds_read_b64_tr_b16 v[196:197], v0 offset:17408\n"
    "ds_read_b64_tr_b16 v[198:199], v1 offset:17408\n"
    "v_mfma_f32_16x16x32_bf16 v[208:211], v[72:75], v[168:171], v[208:211]\n"
    "v_mul_f32 v204, v204, v252\n"
    "v_mul_f32 v205, v205, v252\n"
    "v_mfma_f32_16x16x32_bf16 v[212:215], v[96:99], v[168:171], v[212:215]\n"
    "ds_read_b32 v253, v238 offset:320\n"
    "v_mul_f32 v206, v206, v252\n"
    "v_mfma_f32_16x16x32_bf16 v[208:211], v[76:79], v[172:175], v[208:211]\n"
    "v_mul_f32 v207, v207, v252\n"
    "v_cvt_pk_bf16_f32 v200, v200, v201\n"
    "v_mfma_f32_16x16x32_bf16 v[212:215], v[100:103], v[172:175], v[212:215]\n"
    "v_cvt_pk_bf16_f32 v201, v202, v203\n"
    "v_cvt_pk_bf16_f32 v202, v204, v205\n"
    "v_cvt_pk_bf16_f32 v203, v206, v207\n"
    "buffer_store_dwordx4 v[200:203], v248, s[80:83], 0 offen offset:128 sc1\n"
    ".Lg33j:\n"
    "s_cmp_lg_u32 s58, 0\n"
    "s_cbranch_scc1 .Lg_t12\n"
    "s_waitcnt lgkmcnt(1)\n"
    "v_mfma_f32_16x16x32_bf16 v[200:203], v[8:11], v[176:179], 0\n"
    "v_mul_f32 v208, v208, v252\n"
    "v_mul_f32 v209, v209, v252\n"
    "v_mfma_f32_16x16x32_bf16 v[204:207], v[32:35], v[176:179], 0\n"
    "ds_read_b32 v228, v224 offset:832\n"
    "v_mul_f32 v210, v210, v252\n"
    "v_mfma_f32_16x16x32_bf16 v[200:203], v[12:15], v[180:183], v[200:203]\n"
    "v_mul_f32 v211, v211, v252\n"
    "v_mul_f32 v212, v212, v252\n"
    "v_mfma_f32_16x16x32_bf16 v[204:207], v[36:39], v[180:183], v[204:207]\n"
    "v_mul_f32 v213, v213, v252\n"
    "v_mul_f32 v214, v214, v252\n"
    "v_mfma_f32_16x16x32_bf16 v[200:203], v[16:19], v[184:187], v[200:203]\n"
    "v_mul_f32 v215, v215, v252\n"
    "v_cvt_pk_bf16_f32 v208, v208, v209\n"
    "v_mfma_f32_16x16x32_bf16 v[204:207], v[40:43], v[184:187], v[204:207]\n"
    "v_cvt_pk_bf16_f32 v209, v210, v211\n"
    "v_cvt_pk_bf16_f32 v210, v212, v213\n"
    "v_mfma_f32_16x16x32_bf16 v[200:203], v[20:23], v[188:191], v[200:203]\n"
    "v_cvt_pk_bf16_f32 v211, v214, v215\n"
    "v_mfma_f32_16x16x32_bf16 v[204:207], v[44:47], v[188:191], v[204:207]\n"
    "buffer_store_dwordx4 v[208:211], v248, s[80:83], 0 offen offset:320 sc1\n"
    "v_mfma_f32_16x16x32_bf16 v[200:203], v[24:27], v[192:195], v[200:203]\n"
    "v_mfma_f32_16x16x32_bf16 v[204:207], v[48:51], v[192:195], v[204:207]\n"
    "v_add_u32 v248, v255, v248\n"
    "v_mfma_f32_16x16x32_bf16 v[200:203], v[28:31], v[196:199], v[200:203]\n"
    "s_waitcnt lgkmcnt(0)\n"
    "v_cmp_gt_u32 vcc, 4, v228\n"
    "v_mfma_f32_16x16x32_bf16 v[204:207], v[52:55], v[196:199], v[204:207]\n"
    "s_cbranch_vccnz .Lg34\n"
    "v_mfma_f32_16x16x32_bf16 v[208:211], v[56:59], v[176:179], 0\n"
    "ds_read_b64_tr_b16 v[152:153], v0 offset:0\n"
    "ds_read_b64_tr_b16 v[154:155], v1 offset:0\n"
    "v_mfma_f32_16x16x32_bf16 v[212:215], v[80:83], v[176:179], 0\n"
    "ds_read_b64_tr_b16 v[156:157], v0 offset:1024\n"
    "ds_read_b64_tr_b16 v[158:159], v1 offset:1024\n"
    "v_mfma_f32_16x16x32_bf16 v[208:211], v[60:63], v[180:183], v[208:211]\n"
    "ds_read_b64_tr_b16 v[160:161], v0 offset:2048\n"
    "ds_read_b64_tr_b16 v[162:163], v1 offset:2048\n"
    "v_mfma_f32_16x16x32_bf16 v[212:215], v[84:87], v[180:183], v[212:215]\n"
    "ds_read_b64_tr_b16 v[164:165], v0 offset:3072\n"
    "ds_read_b64_tr_b16 v[166:167], v1 offset:3072\n"
    "v_mfma_f32_16x16x32_bf16 v[208:211], v[64:67], v[184:187], v[208:211]\n"
    "v_mul_f32 v200, v200, v253\n"
    "v_mul_f32 v201, v201, v253\n"
    "v_mfma_f32_16x16x32_bf16 v[212:215], v[88:91], v[184:187], v[212:215]\n"
    "ds_read_b64_tr_b16 v[168:169], v0 offset:4096\n"
    "ds_read_b64_tr_b16 v[170:171], v1 offset:4096\n"
    "v_mfma_f32_16x16x32_bf16 v[208:211], v[68:71], v[188:191], v[208:211]\n"
    "v_mul_f32 v202, v202, v253\n"
    "v_mul_f32 v203, v203, v253\n"
    "v_mfma_f32_16x16x32_bf16 v[212:215], v[92:95], v[188:191], v[212:215]\n"
    "ds_read_b64_tr_b16 v[172:173], v0 offset:5120\n"
    "ds_read_b64_tr_b16 v[174:175], v1 offset:5120\n"
    "v_mfma_f32_16x16x32_bf16 v[208:211], v[72:75], v[192:195], v[208:211]\n"
    "v_mul_f32 v204, v204, v253\n"
    "v_mul_f32 v205, v205, v253\n"
    "v_mfma_f32_16x16x32_bf16 v[212:215], v[96:99], v[192:195], v[212:215]\n"
    "ds_read_b32 v252, v238 offset:192\n"
    "v_mul_f32 v206, v206, v253\n"
    "v_mfma_f32_16x16x32_bf16 v[208:211], v[76:79], v[196:199], v[208:211]\n"
    "v_mul_f32 v207, v207, v253\n"
    "v_cvt_pk_bf16_f32 v200, v200, v201\n"
    "v_mfma_f32_16x16x32_bf16 v[212:215], v[100:103], v[196:199], v[212:215]\n"
    "v_cvt_pk_bf16_f32 v201, v202, v203\n"
    "v_cvt_pk_bf16_f32 v202, v204, v205\n"
    "v_cvt_pk_bf16_f32 v203, v206, v207\n"
    "buffer_store_dwordx4 v[200:203], v249, s[80:83], 0 offen offset:128 sc1\n"
    ".Lg34j:\n"
    "s_waitcnt lgkmcnt(1)\n"
    "s_nop 0\n"
    "v_mfma_f32_16x16x32_bf16 v[200:203], v[8:11], v[152:155], 0\n"
    "v_mul_f32 v208, v208, v253\n"
    "v_mul_f32 v209, v209, v253\n"
    "v_mfma_f32_16x16x32_bf16 v[204:207], v[32:35], v[152:155], 0\n"
    "v_mul_f32 v210, v210, v253\n"
    "v_mul_f32 v211, v211, v253\n"
    "v_mfma_f32_16x16x32_bf16 v[200:203], v[12:15], v[156:159], v[200:203]\n"
    "v_mul_f32 v212, v212, v253\n"
    "v_mul_f32 v213, v213, v253\n"
    "v_mfma_f32_16x16x32_bf16 v[204:207], v[36:39], v[156:159], v[204:207]\n"
    "v_mul_f32 v214, v214, v253\n"
    "v_mul_f32 v215, v215, v253\n"
    "v_mfma_f32_16x16x32_bf16 v[200:203], v[16:19], v[160:163], v[200:203]\n"
    "v_cvt_pk_bf16_f32 v208, v208, v209\n"
    "v_cvt_pk_bf16_f32 v209, v210, v211\n"
    "v_mfma_f32_16x16x32_bf16 v[204:207], v[40:43], v[160:163], v[204:207]\n"
    "v_cvt_pk_bf16_f32 v210, v212, v213\n"
    "v_cvt_pk_bf16_f32 v211, v214, v215\n"
    "v_mfma_f32_16x16x32_bf16 v[200:203], v[20:23], v[164:167], v[200:203]\n"
    "buffer_store_dwordx4 v[208:211], v249, s[80:83], 0 offen offset:320 sc1\n"
    "v_mfma_f32_16x16x32_bf16 v[204:207], v[44:47], v[164:167], v[204:207]\n"
    "v_mfma_f32_16x16x32_bf16 v[200:203], v[24:27], v[168:171], v[200:203]\n"
    "v_mfma_f32_16x16x32_bf16 v[204:207], v[48:51], v[168:171], v[204:207]\n"
    "v_mfma_f32_16x16x32_bf16 v[200:203], v[28:31], v[172:175], v[200:203]\n"
    "v_mfma_f32_16x16x32_bf16 v[204:207], v[52:55], v[172:175], v[204:207]\n"
    "v_mfma_f32_16x16x32_bf16 v[208:211], v[56:59], v[152:155], 0\n"
    "v_mfma_f32_16x16x32_bf16 v[212:215], v[80:83], v[152:155], 0\n"
    "v_mfma_f32_16x16x32_bf16 v[208:211], v[60:63], v[156:159], v[208:211]\n"
    "v_mfma_f32_16x16x32_bf16 v[212:215], v[84:87], v[156:159], v[212:215]\n"
    "v_mfma_f32_16x16x32_bf16 v[208:211], v[64:67], v[160:163], v[208:211]\n"
    "v_mfma_f32_16x16x32_bf16 v[212:215], v[88:91], v[160:163], v[212:215]\n"
    "s_waitcnt lgkmcnt(0)\n"
    "v_mul_f32 v200, v200, v252\n"
    "v_mfma_f32_16x16x32_bf16 v[208:211], v[68:71], v[164:167], v[208:211]\n"
    "v_mul_f32 v201, v201, v252\n"
    "v_mul_f32 v202, v202, v252\n"
    "v_mfma_f32_16x16x32_bf16 v[212:215], v[92:95], v[164:167], v[212:215]\n"
    "v_mul_f32 v203, v203, v252\n"
    "v_mul_f32 v204, v204, v252\n"
    "v_mfma_f32_16x16x32_bf16 v[208:211], v[72:75], v[168:171], v[208:211]\n"
    "v_mul_f32 v205, v205, v252\n"
    "v_mul_f32 v206, v206, v252\n"
    "v_mfma_f32_16x16x32_bf16 v[212:215], v[96:99], v[168:171], v[212:215]\n"
    "v_mul_f32 v207, v207, v252\n"
    "v_cvt_pk_bf16_f32 v200, v200, v201\n"
    "v_mfma_f32_16x16x32_bf16 v[208:211], v[76:79], v[172:175], v[208:211]\n"
    "v_cvt_pk_bf16_f32 v201, v202, v203\n"
    "v_cvt_pk_bf16_f32 v202, v204, v205\n"
    "v_mfma_f32_16x16x32_bf16 v[212:215], v[100:103], v[172:175], v[212:215]\n"
    "v_cvt_pk_bf16_f32 v203, v206, v207\n"
    "buffer_store_dwordx4 v[200:203], v248, s[80:83], 0 offen offset:128 sc1\n"
    "s_nop 2\n"
    "v_mul_f32 v208, v208, v252\n"
    "v_mul_f32 v209, v209, v252\n"
    "v_mul_f32 v210, v210, v252\n"
    "v_mul_f32 v211, v211, v252\n"
    "v_mul_f32 v212, v212, v252\n"
    "v_mul_f32 v213, v213, v252\n"
    "v_mul_f32 v214, v214, v252\n"
    "v_mul_f32 v215, v215, v252\n"
    "v_cvt_pk_bf16_f32 v208, v208, v209\n"
    "v_cvt_pk_bf16_f32 v209, v210, v211\n"
    "v_cvt_pk_bf16_f32 v210, v212, v213\n"
    "v_cvt_pk_bf16_f32 v211, v214, v215\n"
    "buffer_store_dwordx4 v[208:211], v248, s[80:83], 0 offen offset:320 sc1\n"
    ".Lg_end:\n"
    "s_waitcnt lgkmcnt(0)\n"
    "s_endpgm\n"
    ".Lg1:\n"
    "s_sub_u32 s52, s52, 1\n"
    "s_cmp_lt_i32 s52, 0\n"
    "s_cbranch_scc1 .Lg1b\n"
    "ds_read_b32 v228, v224 offset:48\n"
    "s_waitcnt lgkmcnt(0)\n"
    "v_cmp_gt_u32 vcc, 1, v228\n"
    "s_cbranch_vccnz .Lg1\n"
    "s_branch .Lg1b\n"
    ".Lg_poll:\n"
    "global_load_dwordx4 v[72:75], v235, s[68:69] offset:1024 sc1\n"
    "global_load_dwordx4 v[96:99], v235, s[70:71] offset:1024 sc1\n"
    "global_load_dwordx4 v[76:79], v235, s[68:69] offset:2048 sc1\n"
    "global_load_dwordx4 v[100:103], v235, s[70:71] offset:2048 sc1\n"
    "global_load_dwordx4 v[104:107], v235, s[72:73] offset:-3072 sc1\n"
    "global_load_dwordx4 v[128:131], v235, s[74:75] offset:-3072 sc1\n"
    "global_load_dwordx4 v[108:111], v235, s[72:73] offset:-2048 sc1\n"
    "global_load_dwordx4 v[132:135], v235, s[74:75] offset:-2048 sc1\n"
    "global_load_dwordx4 v[112:115], v235, s[72:73] offset:-1024 sc1\n"
    "global_load_dwordx4 v[136:139], v235, s[74:75] offset:-1024 sc1\n"
    "global_load_dwordx4 v[116:119], v235, s[72:73] offset:0 sc1\n"
    "global_load_dwordx4 v[140:143], v235, s[74:75] offset:0 sc1\n"
    "global_load_dwordx4 v[120:123], v235, s[72:73] offset:1024 sc1\n"
    "global_load_dwordx4 v[144:147], v235, s[74:75] offset:1024 sc1\n"
    "global_load_dwordx4 v[124:127], v235, s[72:73] offset:2048 sc1\n"
    "global_load_dwordx4 v[148:151], v235, s[74:75] offset:2048 sc1\n"
    "v_mov_b32 v227, 1\n"
    "ds_write_b32 v225, v227 offset:256\n"
    ".Lg_pl2:\n"
    "s_mov_b64 s[76:77], vcc\n"
    "v_mov_b64 v[244:245], v[240:241]\n"
    "v_mov_b64 v[246:247], 0\n"
    "v_mov_b64 v[242:243], 0\n"
    "s_mov_b32 exec_hi, 15\n"
    "global_load_dwordx4 v[244:247], v236, s[40:41] sc1\n"
    "s_mov_b64 exec, -1\n"
    "s_bitcmp1_b64 s[76:77], 0\n"
    "s_cbranch_scc0 2\n"
    "global_load_dwordx4 v[8:11], v235, s[64:65] offset:-3072 sc1\n"
    "s_bitcmp1_b64 s[76:77], 6\n"
    "s_cbranch_scc0 2\n"
    "global_load_dwordx4 v[32:35], v235, s[66:67] offset:-3072 sc1\n"
    "s_bitcmp1_b64 s[76:77], 1\n"
    "s_cbranch_scc0 2\n"
    "global_load_dwordx4 v[12:15], v235, s[64:65] offset:-2048 sc1\n"
    "s_bitcmp1_b64 s[76:77], 7\n"
    "s_cbranch_scc0 2\n"
    "global_load_dwordx4 v[36:39], v235, s[66:67] offset:-2048 sc1\n"
    "s_bitcmp1_b64 s[76:77], 2\n"
    "s_cbranch_scc0 2\n"
    "global_load_dwordx4 v[16:19], v235, s[64:65] offset:-1024 sc1\n"
    "s_bitcmp1_b64 s[76:77], 8\n"
    "s_cbranch_scc0 2\n"
    "global_load_dwordx4 v[40:43], v235, s[66:67] offset:-1024 sc1\n"
    "s_bitcmp1_b64 s[76:77], 3\n"
    "s_cbranch_scc0 2\n"
    "global_load_dwordx4 v[20:23], v235, s[64:65] offset:0 sc1\n"
    "s_bitcmp1_b64 s[76:77], 9\n"
    "s_cbranch_scc0 2\n"
    "global_load_dwordx4 v[44:47], v235, s[66:67] offset:0 sc1\n"
    "s_bitcmp1_b64 s[76:77], 4\n"
    "s_cbranch_scc0 2\n"
    "global_load_dwordx4 v[24:27], v235, s[64:65] offset:1024 sc1\n"
    "s_bitcmp1_b64 s[76:77], 10\n"
    "s_cbranch_scc0 2\n"
    "global_load_dwordx4 v[48:51], v235, s[66:67] offset:1024 sc1\n"
    "s_bitcmp1_b64 s[76:77], 5\n"
    "s_cbranch_scc0 2\n"
    "global_load_dwordx4 v[28:31], v235, s[64:65] offset:2048 sc1\n"
    "s_bitcmp1_b64 s[76:77], 11\n"
    "s_cbranch_scc0 2\n"
    "global_load_dwordx4 v[52:55], v235, s[66:67] offset:2048 sc1\n"
    "s_bitcmp1_b64 s[76:77], 12\n"
    "s_cbranch_scc0 2\n"
    "global_load_dwordx4 v[56:59], v235, s[68:69] offset:-3072 sc1\n"
    "s_bitcmp1_b64 s[76:77], 18\n"
    "s_cbranch_scc0 2\n"
    "global_load_dwordx4 v[80:83], v235, s[70:71] offset:-3072 sc1\n"
    "s_bitcmp1_b64 s[76:77], 13\n"
    "s_cbranch_scc0 2\n"
    "global_load_dwordx4 v[60:63], v235, s[68:69] offset:-2048 sc1\n"
    "s_bitcmp1_b64 s[76:77], 19\n"
    "s_cbranch_scc0 2\n"
    "global_load_dwordx4 v[84:87], v235, s[70:71] offset:-2048 sc1\n"
    "s_bitcmp1_b64 s[76:77], 14\n"
    "s_cbranch_scc0 2\n"
    "global_load_dwordx4 v[64:67], v235, s[68:69] offset:-1024 sc1\n"
    "s_bitcmp1_b64 s[76:77], 20\n"
    "s_cbranch_scc0 2\n"
    "global_load_dwordx4 v[88:91], v235, s[70:71] offset:-1024 sc1\n"
    "s_bitcmp1_b64 s[76:77], 15\n"
    "s_cbranch_scc0 2\n"
    "global_load_dwordx4 v[68:71], v235, s[68:69] offset:0 sc1\n"
    "s_bitcmp1_b64 s[76:77], 21\n"
    "s_cbranch_scc0 2\n"
    "global_load_dwordx4 v[92:95], v235, s[70:71] offset:0 sc1\n"
    "s_bitcmp1_b64 s[76:77], 16\n"
    "s_cbranch_scc0 2\n"
    "global_load_dwordx4 v[72:75], v235, s[68:69] offset:1024 sc1\n"
    "s_bitcmp1_b64 s[76:77], 22\n"
    "s_cbranch_scc0 2\n"
    "global_load_dwordx4 v[96:99], v235, s[70:71] offset:1024 sc1\n"
    "s_bitcmp1_b64 s[76:77], 17\n"
    "s_cbranch_scc0 2\n"
    "global_load_dwordx4 v[76:79], v235, s[68:69] offset:2048 sc1\n"
    "s_bitcmp1_b64 s[76:77], 23\n"
    "s_cbranch_scc0 2\n"
    "global_load_dwordx4 v[100:103], v235, s[70:71] offset:2048 sc1\n"
    "s_bitcmp1_b64 s[76:77], 24\n"
    "s_cbranch_scc0 2\n"
    "global_load_dwordx4 v[104:107], v235, s[72:73] offset:-3072 sc1\n"
    "s_bitcmp1_b64 s[76:77], 30\n"
    "s_cbranch_scc0 2\n"
    "global_load_dwordx4 v[128:131], v235, s[74:75] offset:-3072 sc1\n"
    "s_bitcmp1_b64 s[76:77], 25\n"
    "s_cbranch_scc0 2\n"
    "global_load_dwordx4 v[108:111], v235, s[72:73] offset:-2048 sc1\n"
    "s_bitcmp1_b64 s[76:77], 31\n"
    "s_cbranch_scc0 2\n"
    "global_load_dwordx4 v[132:135], v235, s[74:75] offset:-2048 sc1\n"
    "s_bitcmp1_b64 s[76:77], 26\n"
    "s_cbranch_scc0 2\n"
    "global_load_dwordx4 v[112:115], v235, s[72:73] offset:-1024 sc1\n"
    "s_bitcmp1_b64 s[76:77], 32\n"
    "s_cbranch_scc0 2\n"
    "global_load_dwordx4 v[136:139], v235, s[74:75] offset:-1024 sc1\n"
    "s_bitcmp1_b64 s[76:77], 27\n"
    "s_cbranch_scc0 2\n"
    "global_load_dwordx4 v[116:119], v235, s[72:73] offset:0 sc1\n"
    "s_bitcmp1_b64 s[76:77], 33\n"
    "s_cbranch_scc0 2\n"
    "global_load_dwordx4 v[140:143], v235, s[74:75] offset:0 sc1\n"
    "s_bitcmp1_b64 s[76:77], 28\n"
    "s_cbranch_scc0 2\n"
    "global_load_dwordx4 v[120:123], v235, s[72:73] offset:1024 sc1\n"
    "s_bitcmp1_b64 s[76:77], 34\n"
    "s_cbranch_scc0 2\n"
    "global_load_dwordx4 v[144:147], v235, s[74:75] offset:1024 sc1\n"
    "s_bitcmp1_b64 s[76:77], 29\n"
    "s_cbranch_scc0 2\n"
    "global_load_dwordx4 v[124:127], v235, s[72:73] offset:2048 sc1\n"
    "s_bitcmp1_b64 s[76:77], 35\n"
    "s_cbranch_scc0 2\n"
    "global_load_dwordx4 v[148:151], v235, s[74:75] offset:2048 sc1\n"
    "s_sub_u32 s51, s51, 1\n"
    "s_waitcnt vmcnt(0)\n"
    "v_cmp_ne_u64 vcc, v[240:241], v[244:245]\n"
    "s_cmp_gt_i32 s51, 0\n"
    "s_cbranch_scc0 .Lg_tb\n"
    "s_cbranch_vccnz .Lg_pl2\n"
    "s_branch .Lg_tb\n"
    ".Lg_fail:\n"
    "s_sub_u32 s51, s51, 1\n"
    "s_sleep 2\n"
    "s_cmp_gt_i32 s51, 0\n"
    "s_cbranch_scc1 .Lg_retry\n"
    "s_branch .Lg_ok\n"
    ".Lg2:\n"
    "s_sub_u32 s52, s52, 1\n"
    "s_cmp_lt_i32 s52, 0\n"
    "s_cbranch_scc1 .Lg2b\n"
    "ds_read_b32 v228, v224 offset:0\n"
    "s_waitcnt lgkmcnt(0)\n"
    "v_cmp_gt_u32 vcc, 1, v228\n"
    "s_cbranch_vccnz .Lg2\n"
    "s_branch .Lg2b\n"
    ".Lg_tb:\n"
    "ds_read_b32 v228, v224 offset:0\n"
    "s_waitcnt lgkmcnt(0)\n"
    "v_cmp_gt_u32 vcc, 1, v228\n"
    "s_cbranch_vccnz .Lg2\n"
    ".Lg2b:\n"
    "ds_read_b128 v[152:155], v2 offset:26688\n"
    "ds_read_b128 v[156:159], v2 offset:26752\n"
    "ds_read_b128 v[160:163], v2 offset:26816\n"
    "ds_read_b128 v[164:167], v2 offset:26880\n"
    "ds_read_b128 v[168:171], v2 offset:26944\n"
    "ds_read_b128 v[172:175], v2 offset:27008\n"
    "v_add3_u32 v242, v242, v8, v11\n"
    "v_sub_u32 v8, v8, v240\n"
    "v_add3_u32 v242, v242, v32, v35\n"
    "v_sub_u32 v32, v32, v240\n"
    "v_add3_u32 v242, v242, v12, v15\n"
    "v_sub_u32 v12, v12, v240\n"
    "s_waitcnt lgkmcnt(5)\n"
    "v_mfma_f32_16x16x32_bf16 v[200:203], v[8:11], v[152:155], 0\n"
    "v_add3_u32 v242, v242, v36, v39\n"
    "v_sub_u32 v36, v36, v240\n"
    "v_mfma_f32_16x16x32_bf16 v[204:207], v[32:35], v[152:155], 0\n"
    "v_add3_u32 v242, v242, v16, v19\n"
    "v_sub_u32 v16, v16, v240\n"
    "s_waitcnt lgkmcnt(4)\n"
    "v_mfma_f32_16x16x32_bf16 v[200:203], v[12:15], v[156:159], v[200:203]\n"
    "v_add3_u32 v242, v242, v40, v43\n"
    "v_sub_u32 v40, v40, v240\n"
    "v_mfma_f32_16x16x32_bf16 v[204:207], v[36:39], v[156:159], v[204:207]\n"
    "v_add3_u32 v242, v242, v20, v23\n"
    "v_sub_u32 v20, v20, v240\n"
    "s_waitcnt lgkmcnt(3)\n"
    "v_mfma_f32_16x16x32_bf16 v[200:203], v[16:19], v[160:163], v[200:203]\n"
    "v_add3_u32 v242, v242, v44, v47\n"
    "v_sub_u32 v44, v44, v240\n"
    "v_mfma_f32_16x16x32_bf16 v[204:207], v[40:43], v[160:163], v[204:207]\n"
    "v_add3_u32 v242, v242, v24, v27\n"
    "v_sub_u32 v24, v24, v240\n"
    "s_waitcnt lgkmcnt(2)\n"
    "v_mfma_f32_16x16x32_bf16 v[200:203], v[20:23], v[164:167], v[200:203]\n"
    "v_add3_u32 v242, v242, v48, v51\n"
    "v_sub_u32 v48, v48, v240\n"
    "v_mfma_f32_16x16x32_bf16 v[204:207], v[44:47], v[164:167], v[204:207]\n"
    "v_add3_u32 v242, v242, v28, v31\n"
    "v_sub_u32 v28, v28, v240\n"
    "s_waitcnt lgkmcnt(1)\n"
    "v_mfma_f32_16x16x32_bf16 v[200:203], v[24:27], v[168:171], v[200:203]\n"
    "v_add3_u32 v242, v242, v52, v55\n"
    "v_sub_u32 v52, v52, v240\n"
    "v_mfma_f32_16x16x32_bf16 v[204:207], v[48:51], v[168:171], v[204:207]\n"
    "s_waitcnt lgkmcnt(0)\n"
    "v_mfma_f32_16x16x32_bf16 v[200:203], v[28:31], v[172:175], v[200:203]\n"
    "v_mfma_f32_16x16x32_bf16 v[204:207], v[52:55], v[172:175], v[204:207]\n"
    "v_add3_u32 v242, v242, v56, v59\n"
    "v_sub_u32 v56, v56, v240\n"
    "v_add3_u32 v242, v242, v80, v83\n"
    "v_sub_u32 v80, v80, v240\n"
    "v_add3_u32 v242, v242, v60, v63\n"
    "v_sub_u32 v60, v60, v240\n"
    "v_mfma_f32_16x16x32_bf16 v[208:211], v[56:59], v[152:155], 0\n"
    "v_cvt_pk_bf16_f32 v200, v200, v201\n"
    "v_add3_u32 v242, v242, v84, v87\n"
    "v_sub_u32 v84, v84, v240\n"
    "v_mfma_f32_16x16x32_bf16 v[212:215], v[80:83], v[152:155], 0\n"
    "v_cvt_pk_bf16_f32 v201, v202, v203\n"
    "v_add3_u32 v242, v242, v64, v67\n"
    "v_sub_u32 v64, v64, v240\n"
    "v_mfma_f32_16x16x32_bf16 v[208:211], v[60:63], v[156:159], v[208:211]\n"
    "v_cvt_pk_bf16_f32 v202, v204, v205\n"
    "v_add3_u32 v242, v242, v88, v91\n"
    "v_sub_u32 v88, v88, v240\n"
    "v_mfma_f32_16x16x32_bf16 v[212:215], v[84:87], v[156:159], v[212:215]\n"
    "v_cvt_pk_bf16_f32 v203, v206, v207\n"
    "v_add3_u32 v242, v242, v68, v71\n"
    "v_sub_u32 v68, v68, v240\n"
    "v_mfma_f32_16x16x32_bf16 v[208:211], v[64:67], v[160:163], v[208:211]\n"
    "v_add3_u32 v242, v242, v92, v95\n"
    "v_sub_u32 v92, v92, v240\n"
    "v_mfma_f32_16x16x32_bf16 v[212:215], v[88:91], v[160:163], v[212:215]\n"
    "v_add3_u32 v242, v242, v72, v75\n"
    "v_sub_u32 v72, v72, v240\n"
    "v_mfma_f32_16x16x32_bf16 v[208:211], v[68:71], v[164:167], v[208:211]\n"
    "v_add3_u32 v242, v242, v96, v99\n"
    "v_sub_u32 v96, v96, v240\n"
    "v_mfma_f32_16x16x32_bf16 v[212:215], v[92:95], v[164:167], v[212:215]\n"
    "v_add3_u32 v242, v242, v76, v79\n"
    "v_sub_u32 v76, v76, v240\n"
    "v_mfma_f32_16x16x32_bf16 v[208:211], v[72:75], v[168:171], v[208:211]\n"
    "v_add3_u32 v242, v242, v100, v103\n"
    "v_sub_u32 v100, v100, v240\n"
    "v_mfma_f32_16x16x32_bf16 v[212:215], v[96:99], v[168:171], v[212:215]\n"
    "v_mfma_f32_16x16x32_bf16 v[208:211], v[76:79], v[172:175], v[208:211]\n"
    "ds_write_b128 v7, v[200:203] offset:0\n"
    "v_mfma_f32_16x16x32_bf16 v[212:215], v[100:103], v[172:175], v[212:215]\n"
    "v_add3_u32 v242, v242, v104, v107\n"
    "v_sub_u32 v104, v104, v240\n"
    "v_add3_u32 v242, v242, v128, v131\n"
    "v_sub_u32 v128, v128, v240\n"
    "v_add3_u32 v242, v242, v108, v111\n"
    "v_sub_u32 v108, v108, v240\n"
    "v_mfma_f32_16x16x32_bf16 v[216:219], v[104:107], v[152:155], 0\n"
    "ds_read_b128 v[176:179], v3 offset:26688\n"
    "v_add3_u32 v242, v242, v132, v135\n"
    "v_sub_u32 v132, v132, v240\n"
    "v_mfma_f32_16x16x32_bf16 v[220:223], v[128:131], v[152:155], 0\n"
    "ds_read_b128 v[180:183], v3 offset:26752\n"
    "v_add3_u32 v242, v242, v112, v115\n"
    "v_sub_u32 v112, v112, v240\n"
    "v_mfma_f32_16x16x32_bf16 v[216:219], v[108:111], v[156:159], v[216:219]\n"
    "ds_read_b128 v[184:187], v3 offset:26816\n"
    "v_add3_u32 v242, v242, v136, v139\n"
    "v_sub_u32 v136, v136, v240\n"
    "v_mfma_f32_16x16x32_bf16 v[220:223], v[132:135], v[156:159], v[220:223]\n"
    "ds_read_b128 v[188:191], v3 offset:26880\n"
    "v_add3_u32 v242, v242, v116, v119\n"
    "v_sub_u32 v116, v116, v240\n"
    "v_mfma_f32_16x16x32_bf16 v[216:219], v[112:115], v[160:163], v[216:219]\n"
    "ds_read_b128 v[192:195], v3 offset:26944\n"
    "v_add3_u32 v242, v242, v140, v143\n"
    "v_sub_u32 v140, v140, v240\n"
    "v_mfma_f32_16x16x32_bf16 v[220:223], v[136:139], v[160:163], v[220:223]\n"
    "ds_read_b128 v[196:199], v3 offset:27008\n"
    "v_add3_u32 v242, v242, v120, v123\n"
    "v_sub_u32 v120, v120, v240\n"
    "v_mfma_f32_16x16x32_bf16 v[216:219], v[116:119], v[164:167], v[216:219]\n"
    "v_cvt_pk_bf16_f32 v208, v208, v209\n"
    "v_add3_u32 v242, v242, v144, v147\n"
    "v_sub_u32 v144, v144, v240\n"
    "v_mfma_f32_16x16x32_bf16 v[220:223], v[140:143], v[164:167], v[220:223]\n"
    "v_cvt_pk_bf16_f32 v209, v210, v211\n"
    "v_add3_u32 v242, v242, v124, v127\n"
    "v_sub_u32 v124, v124, v240\n"
    "v_mfma_f32_16x16x32_bf16 v[216:219], v[120:123], v[168:171], v[216:219]\n"
    "v_cvt_pk_bf16_f32 v210, v212, v213\n"
    "v_add3_u32 v242, v242, v148, v151\n"
    "v_sub_u32 v148, v148, v240\n"
    "v_mfma_f32_16x16x32_bf16 v[220:223], v[144:147], v[168:171], v[220:223]\n"
    "v_cvt_pk_bf16_f32 v211, v214, v215\n"
    "v_mfma_f32_16x16x32_bf16 v[216:219], v[124:127], v[172:175], v[216:219]\n"
    "ds_write_b128 v7, v[208:211] offset:1024\n"
    "v_mfma_f32_16x16x32_bf16 v[220:223], v[148:151], v[172:175], v[220:223]\n"
    "v_cmp_ne_u64 s[76:77], v[240:241], v[244:245]\n"
    "v_sub_u32 v242, v242, v246\n"
    "v_mov_b32 v243, v242\n"
    "s_nop 1\n"
    "v_permlane32_swap_b32 v242, v243\n"
    "v_add_u32 v242, v242, v243\n"
    "v_cvt_pk_bf16_f32 v216, v216, v217\n"
    "v_cvt_pk_bf16_f32 v217, v218, v219\n"
    "v_add_u32_dpp v242, v242, v242 row_ror:8 row_mask:0xf bank_mask:0xf\n"
    "v_cvt_pk_bf16_f32 v218, v220, v221\n"
    "v_cvt_pk_bf16_f32 v219, v222, v223\n"
    "v_add_u32_dpp v242, v242, v242 row_ror:4 row_mask:0xf bank_mask:0xf\n"
    "ds_write_b128 v7, v[216:219] offset:2048\n"
    "s_nop 0\n"
    "v_add_u32_dpp v242, v242, v242 row_ror:2 row_mask:0xf bank_mask:0xf\n"
    "s_nop 1\n"
    "v_add_u32_dpp v242, v242, v242 row_ror:1 row_mask:0xf bank_mask:0xf\n"
    "v_mov_b32 v243, v242\n"
    "s_nop 1\n"
    "v_permlane16_swap_b32 v242, v243\n"
    "v_add_u32 v242, v242, v243\n"
    "v_cmp_ne_u32 vcc, 0, v242\n"
    "v_mov_b32 v227, 1\n"
    "s_cmp_lg_u64 vcc, 0\n"
    "s_cbranch_scc1 .Lg_fail\n"
    "s_cmp_lg_u64 s[76:77], 0\n"
    "s_cbranch_scc1 .Lg_fail\n"
    ".Lg_okb:\n"
    "ds_write_b32 v225, v227 offset:16\n"
    "v_mov_b32 v242, 0\n"
    "s_waitcnt lgkmcnt(3)\n"
    "s_branch .Lg_t1\n"
    ".Lg3:\n"
    "s_sub_u32 s52, s52, 1\n"
    "s_cmp_lt_i32 s52, 0\n"
    "s_cbranch_scc1 .Lg3b\n"
    "ds_read_b32 v228, v224 offset:0\n"
    "s_waitcnt lgkmcnt(0)\n"
    "v_cmp_gt_u32 vcc, 1, v228\n"
    "s_cbranch_vccnz .Lg3\n"
    "s_branch .Lg3b\n"
    ".Lg4:\n"
    "s_sub_u32 s52, s52, 1\n"
    "s_cmp_lt_i32 s52, 0\n"
    "s_cbranch_scc1 .Lg4b\n"
    "ds_read_b32 v229, v224 offset:0\n"
    "s_waitcnt lgkmcnt(0)\n"
    "v_cmp_gt_u32 vcc, 2, v229\n"
    "s_cbranch_vccnz .Lg4\n"
    "s_branch .Lg4b\n"
    ".Lg5:\n"
    "s_sub_u32 s52, s52, 1\n"
    "s_cmp_lt_i32 s52, 0\n"
    "s_cbranch_scc1 .Lg5b\n"
    "ds_read_b32 v229, v224 offset:0\n"
    "s_waitcnt lgkmcnt(0)\n"
    "v_cmp_gt_u32 vcc, 5, v229\n"
    "s_cbranch_vccnz .Lg5\n"
    "s_branch .Lg5b\n"
    ".Lg6:\n"
    "s_sub_u32 s52, s52, 1\n"
    "s_cmp_lt_i32 s52, 0\n"
    "s_cbranch_scc1 .Lg6b\n"
    "ds_read_b32 v228, v224 offset:192\n"
    "s_waitcnt lgkmcnt(0)\n"
    "v_cmp_gt_u32 vcc, 1, v228\n"
    "s_cbranch_vccnz .Lg6\n"
    "s_branch .Lg6b\n"
    ".Lg7:\n"
    "s_sub_u32 s52, s52, 1\n"
    "s_cmp_lt_i32 s52, 0\n"
    "s_cbranch_scc1 .Lg7b\n"
    "ds_read_b32 v228, v224 offset:208\n"
    "s_waitcnt lgkmcnt(0)\n"
    "v_cmp_gt_u32 vcc, 1, v228\n"
    "s_cbranch_vccnz .Lg7\n"
    "s_branch .Lg7b\n"
    ".Lg8:\n"
    "s_sub_u32 s52, s52, 1\n"
    "s_cmp_lt_i32 s52, 0\n"
    "s_cbranch_scc1 .Lg8b\n"
    "ds_read_b32 v239, v224 offset:64\n"
    "s_waitcnt lgkmcnt(0)\n"
    "v_cmp_gt_u32 vcc, 1, v239\n"
    "s_cbranch_vccnz .Lg8\n"
    "s_branch .Lg8b\n"
    ".Lg9:\n"
    "s_sub_u32 s52, s52, 1\n"
    "s_cmp_lt_i32 s52, 0\n"
    "s_cbranch_scc1 .Lg9b\n"
    "ds_read_b32 v229, v224 offset:0\n"
    "s_waitcnt lgkmcnt(0)\n"
    "v_cmp_gt_u32 vcc, 6, v229\n"
    "s_cbranch_vccnz .Lg9\n"
    "s_branch .Lg9b\n"
    ".Lg10:\n"
    "s_sub_u32 s52, s52, 1\n"
    "s_cmp_lt_i32 s52, 0\n"
    "s_cbranch_scc1 .Lg10b\n"
    "ds_read_b32 v228, v224 offset:224\n"
    "s_waitcnt lgkmcnt(0)\n"
    "v_cmp_gt_u32 vcc, 1, v228\n"
    "s_cbranch_vccnz .Lg10\n"
    "s_branch .Lg10b\n"
    ".Lg11:\n"
    "s_sub_u32 s52, s52, 1\n"
    "s_cmp_lt_i32 s52, 0\n"
    "s_cbranch_scc1 .Lg11b\n"
    "ds_read_b32 v239, v224 offset:80\n"
    "s_waitcnt lgkmcnt(0)\n"
    "v_cmp_gt_u32 vcc, 1, v239\n"
    "s_cbranch_vccnz .Lg11\n"
    "s_branch .Lg11b\n"
    ".Lg12:\n"
    "s_sub_u32 s52, s52, 1\n"
    "s_cmp_lt_i32 s52, 0\n"
    "s_cbranch_scc1 .Lg12b\n"
    "ds_read_b32 v228, v224 offset:240\n"
    "s_waitcnt lgkmcnt(0)\n"
    "v_cmp_gt_u32 vcc, 1, v228\n"
    "s_cbranch_vccnz .Lg12\n"
    "s_branch .Lg12b\n"
    ".Lg13:\n"
    "s_sub_u32 s52, s52, 1\n"
    "s_cmp_lt_i32 s52, 0\n"
    "s_cbranch_scc1 .Lg13b\n"
    "ds_read_b32 v239, v224 offset:96\n"
    "s_waitcnt lgkmcnt(0)\n"
    "v_cmp_gt_u32 vcc, 1, v239\n"
    "s_cbranch_vccnz .Lg13\n"
    "s_branch .Lg13b\n"
    ".Lg14:\n"
    "s_sub_u32 s52, s52, 1\n"
    "s_cmp_lt_i32 s52, 0\n"
    "s_cbranch_scc1 .Lg14b\n"
    "ds_read_b32 v229, v224 offset:0\n"
    "s_waitcnt lgkmcnt(0)\n"
    "v_cmp_gt_u32 vcc, 7, v229\n"
    "s_cbranch_vccnz .Lg14\n"
    "s_branch .Lg14b\n"
    ".Lg15:\n"
    "s_sub_u32 s52, s52, 1\n"
    "s_cmp_lt_i32 s52, 0\n"
    "s_cbranch_scc1 .Lg15b\n"
    "ds_read_b32 v228, v224 offset:192\n"
    "s_waitcnt lgkmcnt(0)\n"
    "v_cmp_gt_u32 vcc, 2, v228\n"
    "s_cbranch_vccnz .Lg15\n"
    "s_branch .Lg15b\n"
    ".Lg16:\n"
    "s_sub_u32 s52, s52, 1\n"
    "s_cmp_lt_i32 s52, 0\n"
    "s_cbranch_scc1 .Lg16b\n"
    "ds_read_b32 v239, v224 offset:112\n"
    "s_waitcnt lgkmcnt(0)\n"
    "v_cmp_gt_u32 vcc, 1, v239\n"
    "s_cbranch_vccnz .Lg16\n"
    "s_branch .Lg16b\n"
    ".Lg17:\n"
    "s_sub_u32 s52, s52, 1\n"
    "s_cmp_lt_i32 s52, 0\n"
    "s_cbranch_scc1 .Lg17b\n"
    "ds_read_b32 v228, v224 offset:208\n"
    "s_waitcnt lgkmcnt(0)\n"
    "v_cmp_gt_u32 vcc, 2, v228\n"
    "s_cbranch_vccnz .Lg17\n"
    "s_branch .Lg17b\n"
    ".Lg18:\n"
    "s_sub_u32 s52, s52, 1\n"
    "s_cmp_lt_i32 s52, 0\n"
    "s_cbranch_scc1 .Lg18b\n"
    "ds_read_b32 v239, v224 offset:64\n"
    "s_waitcnt lgkmcnt(0)\n"
    "v_cmp_gt_u32 vcc, 2, v239\n"
    "s_cbranch_vccnz .Lg18\n"
    "s_branch .Lg18b\n"
    ".Lg19:\n"
    "s_sub_u32 s52, s52, 1\n"
    "s_cmp_lt_i32 s52, 0\n"
    "s_cbranch_scc1 .Lg19b\n"
    "ds_read_b32 v229, v224 offset:0\n"
    "s_waitcnt lgkmcnt(0)\n"
    "v_cmp_gt_u32 vcc, 8, v229\n"
    "s_cbranch_vccnz .Lg19\n"
    "s_branch .Lg19b\n"
    ".Lg20:\n"
    "s_sub_u32 s52, s52, 1\n"
    "s_cmp_lt_i32 s52, 0\n"
    "s_cbranch_scc1 .Lg20b\n"
    "ds_read_b32 v228, v224 offset:224\n"
    "s_waitcnt lgkmcnt(0)\n"
    "v_cmp_gt_u32 vcc, 2, v228\n"
    "s_cbranch_vccnz .Lg20\n"
    "s_branch .Lg20b\n"
    ".Lg21:\n"
    "s_sub_u32 s52, s52, 1\n"
    "s_cmp_lt_i32 s52, 0\n"
    "s_cbranch_scc1 .Lg21b\n"
    "ds_read_b32 v239, v224 offset:80\n"
    "s_waitcnt lgkmcnt(0)\n"
    "v_cmp_gt_u32 vcc, 2, v239\n"
    "s_cbranch_vccnz .Lg21\n"
    "s_branch .Lg21b\n"
    ".Lg22:\n"
    "s_sub_u32 s52, s52, 1\n"
    "s_cmp_lt_i32 s52, 0\n"
    "s_cbranch_scc1 .Lg22b\n"
    "ds_read_b32 v228, v224 offset:240\n"
    "s_waitcnt lgkmcnt(0)\n"
    "v_cmp_gt_u32 vcc, 2, v228\n"
    "s_cbranch_vccnz .Lg22\n"
    "s_branch .Lg22b\n"
    ".Lg23:\n"
    "s_sub_u32 s52, s52, 1\n"
    "s_cmp_lt_i32 s52, 0\n"
    "s_cbranch_scc1 .Lg23b\n"
    "ds_read_b32 v228, v224 offset:192\n"
    "s_waitcnt lgkmcnt(0)\n"
    "v_cmp_gt_u32 vcc, 3, v228\n"
    "s_cbranch_vccnz .Lg23\n"
    "s_branch .Lg23b\n"
    ".Lg_e12a:\n"
    "v_mfma_f32_16x16x32_bf16 v[200:203], v[8:11], v[176:179], 0\n"
    "v_cvt_pk_bf16_f32 v216, v216, v217\n"
    "v_cvt_pk_bf16_f32 v217, v218, v219\n"
    "v_mfma_f32_16x16x32_bf16 v[204:207], v[32:35], v[176:179], 0\n"
    "v_cvt_pk_bf16_f32 v218, v220, v221\n"
    "v_cvt_pk_bf16_f32 v219, v222, v223\n"
    "v_mfma_f32_16x16x32_bf16 v[200:203], v[12:15], v[180:183], v[200:203]\n"
    "ds_write_b128 v7, v[216:219] offset:8192\n"
    "v_mfma_f32_16x16x32_bf16 v[204:207], v[36:39], v[180:183], v[204:207]\n"
    "v_mov_b32 v227, 11\n"
    "v_mfma_f32_16x16x32_bf16 v[200:203], v[16:19], v[184:187], v[200:203]\n"
    "ds_write_b32 v225, v227 offset:16\n"
    "ds_read_b32 v228, v224 offset:192\n"
    "v_mfma_f32_16x16x32_bf16 v[204:207], v[40:43], v[184:187], v[204:207]\n"
    "v_mfma_f32_16x16x32_bf16 v[200:203], v[20:23], v[188:191], v[200:203]\n"
    "v_mfma_f32_16x16x32_bf16 v[204:207], v[44:47], v[188:191], v[204:207]\n"
    "v_mfma_f32_16x16x32_bf16 v[200:203], v[24:27], v[192:195], v[200:203]\n"
    "v_mfma_f32_16x16x32_bf16 v[204:207], v[48:51], v[192:195], v[204:207]\n"
    "v_mfma_f32_16x16x32_bf16 v[200:203], v[28:31], v[196:199], v[200:203]\n"
    "v_mfma_f32_16x16x32_bf16 v[204:207], v[52:55], v[196:199], v[204:207]\n"
    "v_mfma_f32_16x16x32_bf16 v[208:211], v[56:59], v[176:179], 0\n"
    "s_waitcnt lgkmcnt(0)\n"
    "v_cmp_gt_u32 vcc, 3, v228\n"
    "v_mfma_f32_16x16x32_bf16 v[212:215], v[80:83], v[176:179], 0\n"
    "v_mfma_f32_16x16x32_bf16 v[208:211], v[60:63], v[180:183], v[208:211]\n"
    "s_cbranch_vccnz .Lg23\n"
    ".Lg23b:\n"
    "v_mfma_f32_16x16x32_bf16 v[212:215], v[84:87], v[180:183], v[212:215]\n"
    "v_cvt_pk_bf16_f32 v200, v200, v201\n"
    "v_cvt_pk_bf16_f32 v201, v202, v203\n"
    "v_mfma_f32_16x16x32_bf16 v[208:211], v[64:67], v[184:187], v[208:211]\n"
    "v_cvt_pk_bf16_f32 v202, v204, v205\n"
    "v_cvt_pk_bf16_f32 v203, v206, v207\n"
    "v_mfma_f32_16x16x32_bf16 v[212:215], v[88:91], v[184:187], v[212:215]\n"
    "ds_write_b128 v7, v[200:203] offset:12288\n"
    "v_mfma_f32_16x16x32_bf16 v[208:211], v[68:71], v[188:191], v[208:211]\n"
    "v_mfma_f32_16x16x32_bf16 v[212:215], v[92:95], v[188:191], v[212:215]\n"
    "v_mfma_f32_16x16x32_bf16 v[208:211], v[72:75], v[192:195], v[208:211]\n"
    "v_mfma_f32_16x16x32_bf16 v[212:215], v[96:99], v[192:195], v[212:215]\n"
    "v_mfma_f32_16x16x32_bf16 v[208:211], v[76:79], v[196:199], v[208:211]\n"
    "v_mfma_f32_16x16x32_bf16 v[212:215], v[100:103], v[196:199], v[212:215]\n"
    "v_mfma_f32_16x16x32_bf16 v[216:219], v[104:107], v[176:179], 0\n"
    "v_mfma_f32_16x16x32_bf16 v[220:223], v[128:131], v[176:179], 0\n"
    "v_mfma_f32_16x16x32_bf16 v[216:219], v[108:111], v[180:183], v[216:219]\n"
    "v_mfma_f32_16x16x32_bf16 v[220:223], v[132:135], v[180:183], v[220:223]\n"
    "v_mfma_f32_16x16x32_bf16 v[216:219], v[112:115], v[184:187], v[216:219]\n"
    "v_mfma_f32_16x16x32_bf16 v[220:223], v[136:139], v[184:187], v[220:223]\n"
    "v_mfma_f32_16x16x32_bf16 v[216:219], v[116:119], v[188:191], v[216:219]\n"
    "v_cvt_pk_bf16_f32 v208, v208, v209\n"
    "v_cvt_pk_bf16_f32 v209, v210, v211\n"
    "v_mfma_f32_16x16x32_bf16 v[220:223], v[140:143], v[188:191], v[220:223]\n"
    "v_cvt_pk_bf16_f32 v210, v212, v213\n"
    "v_cvt_pk_bf16_f32 v211, v214, v215\n"
    "v_mfma_f32_16x16x32_bf16 v[216:219], v[120:123], v[192:195], v[216:219]\n"
    "ds_write_b128 v7, v[208:211] offset:13312\n"
    "v_mfma_f32_16x16x32_bf16 v[220:223], v[144:147], v[192:195], v[220:223]\n"
    "v_mfma_f32_16x16x32_bf16 v[216:219], v[124:127], v[196:199], v[216:219]\n"
    "v_mfma_f32_16x16x32_bf16 v[220:223], v[148:151], v[196:199], v[220:223]\n"
    "s_nop 6\n"
    "v_cvt_pk_bf16_f32 v216, v216, v217\n"
    "v_cvt_pk_bf16_f32 v217, v218, v219\n"
    "v_cvt_pk_bf16_f32 v218, v220, v221\n"
    "v_cvt_pk_bf16_f32 v219, v222, v223\n"
    "ds_write_b128 v7, v[216:219] offset:14336\n"
    "v_mov_b32 v227, 12\n"
    "ds_write_b32 v225, v227 offset:16\n"
    "s_branch .Lg_end\n"
    ".Lg24:\n"
    "s_sub_u32 s52, s52, 1\n"
    "s_cmp_lt_i32 s52, 0\n"
    "s_cbranch_scc1 .Lg24b\n"
    "ds_read_b32 v228, v224 offset:192\n"
    "s_waitcnt lgkmcnt(0)\n"
    "v_cmp_gt_u32 vcc, 3, v228\n"
    "s_cbranch_vccnz .Lg24\n"
    "s_branch .Lg24b\n"
    ".Lg25:\n"
    "s_sub_u32 s52, s52, 1\n"
    "s_cmp_lt_i32 s52, 0\n"
    "s_cbranch_scc1 .Lg25b\n"
    "ds_read_b32 v239, v224 offset:272\n"
    "s_waitcnt lgkmcnt(0)\n"
    "v_cmp_gt_u32 vcc, 1, v239\n"
    "s_cbranch_vccnz .Lg25\n"
    "s_branch .Lg25b\n"
    ".Lg_e12b:\n"
    "v_mfma_f32_16x16x32_bf16 v[200:203], v[8:11], v[176:179], 0\n"
    "v_cvt_pk_bf16_f32 v216, v216, v217\n"
    "v_cvt_pk_bf16_f32 v217, v218, v219\n"
    "v_mfma_f32_16x16x32_bf16 v[204:207], v[32:35], v[176:179], 0\n"
    "v_cvt_pk_bf16_f32 v218, v220, v221\n"
    "v_cvt_pk_bf16_f32 v219, v222, v223\n"
    "v_mfma_f32_16x16x32_bf16 v[200:203], v[12:15], v[180:183], v[200:203]\n"
    "ds_write_b128 v7, v[216:219] offset:8192\n"
    "v_mfma_f32_16x16x32_bf16 v[204:207], v[36:39], v[180:183], v[204:207]\n"
    "v_mov_b32 v227, 11\n"
    "v_mfma_f32_16x16x32_bf16 v[200:203], v[16:19], v[184:187], v[200:203]\n"
    "ds_write_b32 v225, v227 offset:16\n"
    "ds_read_b32 v228, v224 offset:192\n"
    "v_mfma_f32_16x16x32_bf16 v[204:207], v[40:43], v[184:187], v[204:207]\n"
    "v_mfma_f32_16x16x32_bf16 v[200:203], v[20:23], v[188:191], v[200:203]\n"
    "v_mfma_f32_16x16x32_bf16 v[204:207], v[44:47], v[188:191], v[204:207]\n"
    "v_mfma_f32_16x16x32_bf16 v[200:203], v[24:27], v[192:195], v[200:203]\n"
    "v_mfma_f32_16x16x32_bf16 v[204:207], v[48:51], v[192:195], v[204:207]\n"
    "v_mfma_f32_16x16x32_bf16 v[200:203], v[28:31], v[196:199], v[200:203]\n"
    "v_mfma_f32_16x16x32_bf16 v[204:207], v[52:55], v[196:199], v[204:207]\n"
    "ds_read_b32 v239, v224 offset:272\n"
    "s_waitcnt lgkmcnt(0)\n"
    "v_cmp_gt_u32 vcc, 1, v239\n"
    "s_cbranch_vccnz .Lg25\n"
    ".Lg25b:\n"
    "v_mfma_f32_16x16x32_bf16 v[208:211], v[56:59], v[176:179], 0\n"
    "v_cmp_gt_u32 vcc, 3, v228\n"
    "v_mfma_f32_16x16x32_bf16 v[212:215], v[80:83], v[176:179], 0\n"
    "s_cbranch_vccnz .Lg24\n"
    ".Lg24b:\n"
    "v_cvt_pk_bf16_f32 v200, v200, v201\n"
    "v_mfma_f32_16x16x32_bf16 v[208:211], v[60:63], v[180:183], v[208:211]\n"
    "v_cvt_pk_bf16_f32 v201, v202, v203\n"
    "v_cvt_pk_bf16_f32 v202, v204, v205\n"
    "v_mfma_f32_16x16x32_bf16 v[212:215], v[84:87], v[180:183], v[212:215]\n"
    "v_cvt_pk_bf16_f32 v203, v206, v207\n"
    "v_mfma_f32_16x16x32_bf16 v[208:211], v[64:67], v[184:187], v[208:211]\n"
    "ds_write_b128 v7, v[200:203] offset:12288\n"
    "v_mfma_f32_16x16x32_bf16 v[212:215], v[88:91], v[184:187], v[212:215]\n"
    "s_add_u32 s60, s40, 109568\n"
    "s_addc_u32 s61, s41, 0\n"
    "v_mfma_f32_16x16x32_bf16 v[208:211], v[68:71], v[188:191], v[208:211]\n"
    "s_add_u32 s66, s60, 6144\n"
    "s_addc_u32 s67, s61, 0\n"
    "v_mfma_f32_16x16x32_bf16 v[212:215], v[92:95], v[188:191], v[212:215]\n"
    "global_load_dwordx4 v[8:11], v235, s[60:61] offset:-3072 sc1\n"
    "global_load_dwordx4 v[32:35], v235, s[66:67] offset:-3072 sc1\n"
    "v_mfma_f32_16x16x32_bf16 v[208:211], v[72:75], v[192:195], v[208:211]\n"
    "global_load_dwordx4 v[12:15], v235, s[60:61] offset:-2048 sc1\n"
    "global_load_dwordx4 v[36:39], v235, s[66:67] offset:-2048 sc1\n"
    "v_mfma_f32_16x16x32_bf16 v[212:215], v[96:99], v[192:195], v[212:215]\n"
    "global_load_dwordx4 v[16:19], v235, s[60:61] offset:-1024 sc1\n"
    "global_load_dwordx4 v[40:43], v235, s[66:67] offset:-1024 sc1\n"
    "v_mfma_f32_16x16x32_bf16 v[208:211], v[76:79], v[196:199], v[208:211]\n"
    "global_load_dwordx4 v[20:23], v235, s[60:61] offset:0 sc1\n"
    "global_load_dwordx4 v[44:47], v235, s[66:67] offset:0 sc1\n"
    "v_mfma_f32_16x16x32_bf16 v[212:215], v[100:103], v[196:199], v[212:215]\n"
    "global_load_dwordx4 v[24:27], v235, s[60:61] offset:1024 sc1\n"
    "global_load_dwordx4 v[48:51], v235, s[66:67] offset:1024 sc1\n"
    "v_mfma_f32_16x16x32_bf16 v[216:219], v[104:107], v[176:179], 0\n"
    "global_load_dwordx4 v[28:31], v235, s[60:61] offset:2048 sc1\n"
    "global_load_dwordx4 v[52:55], v235, s[66:67] offset:2048 sc1\n"
    "v_mfma_f32_16x16x32_bf16 v[220:223], v[128:131], v[176:179], 0\n"
    "v_cvt_pk_bf16_f32 v208, v208, v209\n"
    "v_cvt_pk_bf16_f32 v209, v210, v211\n"
    "v_mfma_f32_16x16x32_bf16 v[216:219], v[108:111], v[180:183], v[216:219]\n"
    "v_cvt_pk_bf16_f32 v210, v212, v213\n"
    "v_cvt_pk_bf16_f32 v211, v214, v215\n"
    "v_mfma_f32_16x16x32_bf16 v[220:223], v[132:135], v[180:183], v[220:223]\n"
    "ds_write_b128 v7, v[208:211] offset:13312\n"
    "v_mfma_f32_16x16x32_bf16 v[216:219], v[112:115], v[184:187], v[216:219]\n"
    "s_add_u32 s60, s40, 146432\n"
    "s_addc_u32 s61, s41, 0\n"
    "v_mfma_f32_16x16x32_bf16 v[220:223], v[136:139], v[184:187], v[220:223]\n"
    "s_add_u32 s66, s60, 6144\n"
    "s_addc_u32 s67, s61, 0\n"
    "v_mfma_f32_16x16x32_bf16 v[216:219], v[116:119], v[188:191], v[216:219]\n"
    "global_load_dwordx4 v[56:59], v235, s[60:61] offset:-3072 sc1\n"
    "global_load_dwordx4 v[80:83], v235, s[66:67] offset:-3072 sc1\n"
    "v_mfma_f32_16x16x32_bf16 v[220:223], v[140:143], v[188:191], v[220:223]\n"
    "global_load_dwordx4 v[60:63], v235, s[60:61] offset:-2048 sc1\n"
    "global_load_dwordx4 v[84:87], v235, s[66:67] offset:-2048 sc1\n"
    "v_mfma_f32_16x16x32_bf16 v[216:219], v[120:123], v[192:195], v[216:219]\n"
    "global_load_dwordx4 v[64:67], v235, s[60:61] offset:-1024 sc1\n"
    "global_load_dwordx4 v[88:91], v235, s[66:67] offset:-1024 sc1\n"
    "v_mfma_f32_16x16x32_bf16 v[220:223], v[144:147], v[192:195], v[220:223]\n"
    "global_load_dwordx4 v[68:71], v235, s[60:61] offset:0 sc1\n"
    "global_load_dwordx4 v[92:95], v235, s[66:67] offset:0 sc1\n"
    "v_mfma_f32_16x16x32_bf16 v[216:219], v[124:127], v[196:199], v[216:219]\n"
    "global_load_dwordx4 v[72:75], v235, s[60:61] offset:1024 sc1\n"
    "global_load_dwordx4 v[96:99], v235, s[66:67] offset:1024 sc1\n"
    "v_mfma_f32_16x16x32_bf16 v[220:223], v[148:151], v[196:199], v[220:223]\n"
    "global_load_dwordx4 v[76:79], v235, s[60:61] offset:2048 sc1\n"
    "global_load_dwordx4 v[100:103], v235, s[66:67] offset:2048 sc1\n"
    "s_nop 2\n"
    "v_cvt_pk_bf16_f32 v216, v216, v217\n"
    "v_cvt_pk_bf16_f32 v217, v218, v219\n"
    "s_nop 0\n"
    "v_cvt_pk_bf16_f32 v218, v220, v221\n"
    "v_cvt_pk_bf16_f32 v219, v222, v223\n"
    "ds_write_b128 v7, v[216:219] offset:14336\n"
    "v_mov_b32 v227, 12\n"
    "ds_write_b32 v225, v227 offset:16\n"
    "s_branch .Lg_to\n"
    ".Lg26:\n"
    "s_sub_u32 s52, s52, 1\n"
    "s_cmp_lt_i32 s52, 0\n"
    "s_cbranch_scc1 .Lg26b\n"
    "ds_read_b32 v229, v224 offset:0\n"
    "s_waitcnt lgkmcnt(0)\n"
    "v_cmp_gt_u32 vcc, 9, v229\n"
    "s_cbranch_vccnz .Lg26\n"
    "s_branch .Lg26b\n"
    ".Lg27:\n"
    "s_sub_u32 s52, s52, 1\n"
    "s_cmp_lt_i32 s52, 0\n"
    "s_cbranch_scc1 .Lg27b\n"
    "ds_read_b32 v228, v224 offset:192\n"
    "s_waitcnt lgkmcnt(0)\n"
    "v_cmp_gt_u32 vcc, 3, v228\n"
    "s_cbranch_vccnz .Lg27\n"
    "s_branch .Lg27b\n"
    ".Lg28:\n"
    "s_sub_u32 s52, s52, 1\n"
    "s_cmp_lt_i32 s52, 0\n"
    "s_cbranch_scc1 .Lg28b\n"
    "ds_read_b32 v228, v224 offset:208\n"
    "s_waitcnt lgkmcnt(0)\n"
    "v_cmp_gt_u32 vcc, 3, v228\n"
    "s_cbranch_vccnz .Lg28\n"
    "s_branch .Lg28b\n"
    ".Lg_e13a:\n"
    "s_setprio 2\n"
    "v_mfma_f32_16x16x32_bf16 v[200:203], v[8:11], v[152:155], 0\n"
    "v_mov_b32 v227, 12\n"
    "v_mfma_f32_16x16x32_bf16 v[204:207], v[32:35], v[152:155], 0\n"
    "v_cvt_pk_bf16_f32 v216, v216, v217\n"
    "v_cvt_pk_bf16_f32 v217, v218, v219\n"
    "v_mfma_f32_16x16x32_bf16 v[200:203], v[12:15], v[156:159], v[200:203]\n"
    "v_cvt_pk_bf16_f32 v218, v220, v221\n"
    "v_cvt_pk_bf16_f32 v219, v222, v223\n"
    "v_mfma_f32_16x16x32_bf16 v[204:207], v[36:39], v[156:159], v[204:207]\n"
    "ds_write_b128 v7, v[216:219] offset:14336\n"
    "v_mfma_f32_16x16x32_bf16 v[200:203], v[16:19], v[160:163], v[200:203]\n"
    "ds_write_b32 v225, v227 offset:16\n"
    "ds_read_b32 v228, v224 offset:208\n"
    "v_mfma_f32_16x16x32_bf16 v[204:207], v[40:43], v[160:163], v[204:207]\n"
    "s_waitcnt vmcnt(0)\n"
    "v_mov_b32 v227, 10\n"
    "v_mfma_f32_16x16x32_bf16 v[200:203], v[20:23], v[164:167], v[200:203]\n"
    "ds_write_b32 v226, v227 offset:0\n"
    "v_mfma_f32_16x16x32_bf16 v[204:207], v[44:47], v[164:167], v[204:207]\n"
    "v_mfma_f32_16x16x32_bf16 v[200:203], v[24:27], v[168:171], v[200:203]\n"
    "v_mfma_f32_16x16x32_bf16 v[204:207], v[48:51], v[168:171], v[204:207]\n"
    "v_mfma_f32_16x16x32_bf16 v[200:203], v[28:31], v[172:175], v[200:203]\n"
    "v_mfma_f32_16x16x32_bf16 v[204:207], v[52:55], v[172:175], v[204:207]\n"
    "v_mfma_f32_16x16x32_bf16 v[208:211], v[56:59], v[152:155], 0\n"
    "s_waitcnt lgkmcnt(1)\n"
    "v_cmp_gt_u32 vcc, 3, v228\n"
    "v_mfma_f32_16x16x32_bf16 v[212:215], v[80:83], v[152:155], 0\n"
    "v_mfma_f32_16x16x32_bf16 v[208:211], v[60:63], v[156:159], v[208:211]\n"
    "s_cbranch_vccnz .Lg28\n"
    ".Lg28b:\n"
    "v_mfma_f32_16x16x32_bf16 v[212:215], v[84:87], v[156:159], v[212:215]\n"
    "v_cvt_pk_bf16_f32 v200, v200, v201\n"
    "v_cvt_pk_bf16_f32 v201, v202, v203\n"
    "v_mfma_f32_16x16x32_bf16 v[208:211], v[64:67], v[160:163], v[208:211]\n"
    "v_cvt_pk_bf16_f32 v202, v204, v205\n"
    "v_cvt_pk_bf16_f32 v203, v206, v207\n"
    "v_mfma_f32_16x16x32_bf16 v[212:215], v[88:91], v[160:163], v[212:215]\n"
    "ds_write_b128 v7, v[200:203] offset:0\n"
    "v_mfma_f32_16x16x32_bf16 v[208:211], v[68:71], v[164:167], v[208:211]\n"
    "v_mfma_f32_16x16x32_bf16 v[212:215], v[92:95], v[164:167], v[212:215]\n"
    "v_mfma_f32_16x16x32_bf16 v[208:211], v[72:75], v[168:171], v[208:211]\n"
    "v_mfma_f32_16x16x32_bf16 v[212:215], v[96:99], v[168:171], v[212:215]\n"
    "v_mfma_f32_16x16x32_bf16 v[208:211], v[76:79], v[172:175], v[208:211]\n"
    "v_mfma_f32_16x16x32_bf16 v[212:215], v[100:103], v[172:175], v[212:215]\n"
    "v_mfma_f32_16x16x32_bf16 v[216:219], v[104:107], v[152:155], 0\n"
    "v_mfma_f32_16x16x32_bf16 v[220:223], v[128:131], v[152:155], 0\n"
    "v_mfma_f32_16x16x32_bf16 v[216:219], v[108:111], v[156:159], v[216:219]\n"
    "v_mfma_f32_16x16x32_bf16 v[220:223], v[132:135], v[156:159], v[220:223]\n"
    "v_mfma_f32_16x16x32_bf16 v[216:219], v[112:115], v[160:163], v[216:219]\n"
    "v_mfma_f32_16x16x32_bf16 v[220:223], v[136:139], v[160:163], v[220:223]\n"
    "v_mfma_f32_16x16x32_bf16 v[216:219], v[116:119], v[164:167], v[216:219]\n"
    "v_cvt_pk_bf16_f32 v208, v208, v209\n"
    "v_cvt_pk_bf16_f32 v209, v210, v211\n"
    "v_mfma_f32_16x16x32_bf16 v[220:223], v[140:143], v[164:167], v[220:223]\n"
    "v_cvt_pk_bf16_f32 v210, v212, v213\n"
    "v_cvt_pk_bf16_f32 v211, v214, v215\n"
    "v_mfma_f32_16x16x32_bf16 v[216:219], v[120:123], v[168:171], v[216:219]\n"
    "ds_write_b128 v7, v[208:211] offset:1024\n"
    "v_mfma_f32_16x16x32_bf16 v[220:223], v[144:147], v[168:171], v[220:223]\n"
    "v_mfma_f32_16x16x32_bf16 v[216:219], v[124:127], v[172:175], v[216:219]\n"
    "v_mfma_f32_16x16x32_bf16 v[220:223], v[148:151], v[172:175], v[220:223]\n"
    "s_nop 6\n"
    "v_cvt_pk_bf16_f32 v216, v216, v217\n"
    "v_cvt_pk_bf16_f32 v217, v218, v219\n"
    "v_cvt_pk_bf16_f32 v218, v220, v221\n"
    "v_cvt_pk_bf16_f32 v219, v222, v223\n"
    "ds_write_b128 v7, v[216:219] offset:2048\n"
    "v_mov_b32 v227, 13\n"
    "ds_write_b32 v225, v227 offset:16\n"
    "s_setprio 0\n"
    "s_branch .Lg_att\n"
    ".Lg29:\n"
    "s_sub_u32 s52, s52, 1\n"
    "s_cmp_lt_i32 s52, 0\n"
    "s_cbranch_scc1 .Lg29b\n"
    "ds_read_b32 v228, v224 offset:208\n"
    "s_waitcnt lgkmcnt(0)\n"
    "v_cmp_gt_u32 vcc, 3, v228\n"
    "s_cbranch_vccnz .Lg29\n"
    "s_branch .Lg29b\n"
    ".Lg30:\n"
    "s_sub_u32 s52, s52, 1\n"
    "s_cmp_lt_i32 s52, 0\n"
    "s_cbranch_scc1 .Lg30b\n"
    "ds_read_b32 v239, v224 offset:272\n"
    "s_waitcnt lgkmcnt(0)\n"
    "v_cmp_gt_u32 vcc, 1, v239\n"
    "s_cbranch_vccnz .Lg30\n"
    "s_branch .Lg30b\n"
    ".Lg_att:\n"
    "s_waitcnt vmcnt(0)\n"
    "v_mov_b32 v8, 0xff800000\n"
    "v_add_u32 v3, 124544, v235\n"
    "v_mov_b32 v6, v224\n"
    "v_lshrrev_b32 v9, 6, v235\n"
    "v_and_b32 v9, 12, v9\n"
    "v_lshlrev_b32 v9, 4, v9\n"
    "v_xor_b32 v4, v9, v235\n"
    "v_add_u32 v4, 124544, v4\n"
    "v_cmp_gt_u32 vcc, 64, v235\n"
    "v_add_u32 v7, 704, v224\n"
    "v_mov_b32 v9, 0x100000\n"
    "v_mov_b32 v0, v248\n"
    "v_cndmask_b32 v7, v9, v7, vcc\n"
    "v_mov_b32 v1, v249\n"
    "v_mov_b32 v2, v250\n"
    "v_and_b32 v5, 15, v251\n"
    "v_mul_u32_u24 v5, 0x1111, v5\n"
    "v_bfe_i32 v10, v251, 8, 1\n"
    "v_bfe_i32 v9, v251, 4, 1\n"
    "v_bfi_b32 v232, v9, 0, v8\n"
    "v_bfi_b32 v236, v10, v8, v232\n"
    "v_bfi_b32 v240, v10, v232, v8\n"
    "v_bfe_i32 v9, v251, 5, 1\n"
    "v_bfi_b32 v233, v9, 0, v8\n"
    "v_bfi_b32 v237, v10, v8, v233\n"
    "v_bfi_b32 v241, v10, v233, v8\n"
    "v_bfe_i32 v9, v251, 6, 1\n"
    "v_bfi_b32 v234, v9, 0, v8\n"
    "v_bfi_b32 v238, v10, v8, v234\n"
    "v_bfi_b32 v242, v10, v234, v8\n"
    "v_bfe_i32 v9, v251, 7, 1\n"
    "v_bfi_b32 v235, v9, 0, v8\n"
    "v_bfi_b32 v239, v10, v8, v235\n"
    "v_bfi_b32 v243, v10, v235, v8\n"
    "s_mov_b32 s45, 12\n"
    "s_mov_b32 s54, 10\n"
    "s_movk_i32 s60, 0x420c\n"
    "s_mov_b32 s61, 0\n"
    "s_mov_b32 s46, 0xbdd53b94\n"
    "s_mov_b32 s47, 0x3dd53b94\n"
    "s_mov_b32 s63, 161920\n"
    "s_mov_b32 s66, 0\n"
    "s_mov_b32 s56, -16\n"
    "s_branch .La_join\n"
    ".Lg31:\n"
    "s_sub_u32 s52, s52, 1\n"
    "s_cmp_lt_i32 s52, 0\n"
    "s_cbranch_scc1 .Lg31b\n"
    "ds_read_b32 v228, v224 offset:144\n"
    "s_waitcnt lgkmcnt(0)\n"
    "v_cmp_gt_u32 vcc, 3, v228\n"
    "s_cbranch_vccnz .Lg31\n"
    "s_branch .Lg31b\n"
    ".Lg32:\n"
    "v_mfma_f32_16x16x32_bf16 v[208:211], v[56:59], v[176:179], 0\n"
    "v_mfma_f32_16x16x32_bf16 v[212:215], v[80:83], v[176:179], 0\n"
    "v_mfma_f32_16x16x32_bf16 v[208:211], v[60:63], v[180:183], v[208:211]\n"
    "v_mfma_f32_16x16x32_bf16 v[212:215], v[84:87], v[180:183], v[212:215]\n"
    "v_mul_f32 v200, v200, v253\n"
    "v_mul_f32 v201, v201, v253\n"
    "v_mfma_f32_16x16x32_bf16 v[208:211], v[64:67], v[184:187], v[208:211]\n"
    "v_mul_f32 v202, v202, v253\n"
    "v_mul_f32 v203, v203, v253\n"
    "v_mfma_f32_16x16x32_bf16 v[212:215], v[88:91], v[184:187], v[212:215]\n"
    "ds_read_b32 v228, v224 offset:160\n"
    "v_mul_f32 v204, v204, v253\n"
    "v_mfma_f32_16x16x32_bf16 v[208:211], v[68:71], v[188:191], v[208:211]\n"
    "v_mul_f32 v205, v205, v253\n"
    "v_mul_f32 v206, v206, v253\n"
    "v_mfma_f32_16x16x32_bf16 v[212:215], v[92:95], v[188:191], v[212:215]\n"
    "v_mul_f32 v207, v207, v253\n"
    "v_cvt_pk_bf16_f32 v200, v200, v201\n"
    "v_mfma_f32_16x16x32_bf16 v[208:211], v[72:75], v[192:195], v[208:211]\n"
    "v_cvt_pk_bf16_f32 v201, v202, v203\n"
    "v_cvt_pk_bf16_f32 v202, v204, v205\n"
    "v_mfma_f32_16x16x32_bf16 v[212:215], v[96:99], v[192:195], v[212:215]\n"
    "v_cvt_pk_bf16_f32 v203, v206, v207\n"
    "v_mfma_f32_16x16x32_bf16 v[208:211], v[76:79], v[196:199], v[208:211]\n"
    "buffer_store_dwordx4 v[200:203], v249, s[80:83], 0 offen offset:128 sc1\n"
    "v_mfma_f32_16x16x32_bf16 v[212:215], v[100:103], v[196:199], v[212:215]\n"
    "s_waitcnt lgkmcnt(0)\n"
    "v_cmp_gt_u32 vcc, 3, v228\n"
    "s_cbranch_vccz .Lg32g\n"
    ".Lg32r:\n"
    "s_sub_u32 s52, s52, 1\n"
    "s_cmp_lt_i32 s52, 0\n"
    "s_cbranch_scc1 .Lg32g\n"
    "ds_read_b32 v228, v224 offset:160\n"
    "s_waitcnt lgkmcnt(0)\n"
    "v_cmp_gt_u32 vcc, 3, v228\n"
    "s_cbranch_vccnz .Lg32r\n"
    "s_branch .Lg32g\n"
    ".Lg32g:\n"
    "ds_read_b64_tr_b16 v[152:153], v0 offset:6144\n"
    "ds_read_b64_tr_b16 v[154:155], v1 offset:6144\n"
    "ds_read_b64_tr_b16 v[156:157], v0 offset:7168\n"
    "ds_read_b64_tr_b16 v[158:159], v1 offset:7168\n"
    "ds_read_b64_tr_b16 v[160:161], v0 offset:8192\n"
    "ds_read_b64_tr_b16 v[162:163], v1 offset:8192\n"
    "ds_read_b64_tr_b16 v[164:165], v0 offset:9216\n"
    "ds_read_b64_tr_b16 v[166:167], v1 offset:9216\n"
    "ds_read_b64_tr_b16 v[168:169], v0 offset:10240\n"
    "ds_read_b64_tr_b16 v[170:171], v1 offset:10240\n"
    "ds_read_b64_tr_b16 v[172:173], v0 offset:11264\n"
    "ds_read_b64_tr_b16 v[174:175], v1 offset:11264\n"
    "ds_read_b32 v252, v238 offset:256\n"
    "s_branch .Lg32j\n"
    ".Lg33:\n"
    "v_mfma_f32_16x16x32_bf16 v[208:211], v[56:59], v[152:155], 0\n"
    "v_mfma_f32_16x16x32_bf16 v[212:215], v[80:83], v[152:155], 0\n"
    "v_mfma_f32_16x16x32_bf16 v[208:211], v[60:63], v[156:159], v[208:211]\n"
    "v_mfma_f32_16x16x32_bf16 v[212:215], v[84:87], v[156:159], v[212:215]\n"
    "v_mul_f32 v200, v200, v252\n"
    "v_mul_f32 v201, v201, v252\n"
    "v_mfma_f32_16x16x32_bf16 v[208:211], v[64:67], v[160:163], v[208:211]\n"
    "v_mul_f32 v202, v202, v252\n"
    "v_mul_f32 v203, v203, v252\n"
    "v_mfma_f32_16x16x32_bf16 v[212:215], v[88:91], v[160:163], v[212:215]\n"
    "ds_read_b32 v228, v224 offset:176\n"
    "v_mul_f32 v204, v204, v252\n"
    "v_mfma_f32_16x16x32_bf16 v[208:211], v[68:71], v[164:167], v[208:211]\n"
    "v_mul_f32 v205, v205, v252\n"
    "v_mul_f32 v206, v206, v252\n"
    "v_mfma_f32_16x16x32_bf16 v[212:215], v[92:95], v[164:167], v[212:215]\n"
    "v_mul_f32 v207, v207, v252\n"
    "v_cvt_pk_bf16_f32 v200, v200, v201\n"
    "v_mfma_f32_16x16x32_bf16 v[208:211], v[72:75], v[168:171], v[208:211]\n"
    "v_cvt_pk_bf16_f32 v201, v202, v203\n"
    "v_cvt_pk_bf16_f32 v202, v204, v205\n"
    "v_mfma_f32_16x16x32_bf16 v[212:215], v[96:99], v[168:171], v[212:215]\n"
    "v_cvt_pk_bf16_f32 v203, v206, v207\n"
    "v_mfma_f32_16x16x32_bf16 v[208:211], v[76:79], v[172:175], v[208:211]\n"
    "buffer_store_dwordx4 v[200:203], v248, s[80:83], 0 offen offset:128 sc1\n"
    "v_mfma_f32_16x16x32_bf16 v[212:215], v[100:103], v[172:175], v[212:215]\n"
    "s_waitcnt lgkmcnt(0)\n"
    "v_cmp_gt_u32 vcc, 3, v228\n"
    "s_cbranch_vccz .Lg33g\n"
    ".Lg33r:\n"
    "s_sub_u32 s52, s52, 1\n"
    "s_cmp_lt_i32 s52, 0\n"
    "s_cbranch_scc1 .Lg33g\n"
    "ds_read_b32 v228, v224 offset:176\n"
    "s_waitcnt lgkmcnt(0)\n"
    "v_cmp_gt_u32 vcc, 3, v228\n"
    "s_cbranch_vccnz .Lg33r\n"
    "s_branch .Lg33g\n"
    ".Lg33g:\n"
    "ds_read_b64_tr_b16 v[176:177], v0 offset:12288\n"
    "ds_read_b64_tr_b16 v[178:179], v1 offset:12288\n"
    "ds_read_b64_tr_b16 v[180:181], v0 offset:13312\n"
    "ds_read_b64_tr_b16 v[182:183], v1 offset:13312\n"
    "ds_read_b64_tr_b16 v[184:185], v0 offset:14336\n"
    "ds_read_b64_tr_b16 v[186:187], v1 offset:14336\n"
    "ds_read_b64_tr_b16 v[188:189], v0 offset:15360\n"
    "ds_read_b64_tr_b16 v[190:191], v1 offset:15360\n"
    "ds_read_b64_tr_b16 v[192:193], v0 offset:16384\n"
    "ds_read_b64_tr_b16 v[194:195], v1 offset:16384\n"
    "ds_read_b64_tr_b16 v[196:197], v0 offset:17408\n"
    "ds_read_b64_tr_b16 v[198:199], v1 offset:17408\n"
    "ds_read_b32 v253, v238 offset:320\n"
    "s_branch .Lg33j\n"
    ".Lg_t12:\n"
    "s_waitcnt lgkmcnt(1)\n"
    "v_mfma_f32_16x16x32_bf16 v[200:203], v[8:11], v[176:179], 0\n"
    "v_mul_f32 v208, v208, v252\n"
    "v_mul_f32 v209, v209, v252\n"
    "v_mfma_f32_16x16x32_bf16 v[204:207], v[32:35], v[176:179], 0\n"
    "v_mul_f32 v210, v210, v252\n"
    "v_mul_f32 v211, v211, v252\n"
    "v_mfma_f32_16x16x32_bf16 v[200:203], v[12:15], v[180:183], v[200:203]\n"
    "v_mul_f32 v212, v212, v252\n"
    "v_mul_f32 v213, v213, v252\n"
    "v_mfma_f32_16x16x32_bf16 v[204:207], v[36:39], v[180:183], v[204:207]\n"
    "v_mul_f32 v214, v214, v252\n"
    "v_mul_f32 v215, v215, v252\n"
    "v_mfma_f32_16x16x32_bf16 v[200:203], v[16:19], v[184:187], v[200:203]\n"
    "v_cvt_pk_bf16_f32 v208, v208, v209\n"
    "v_cvt_pk_bf16_f32 v209, v210, v211\n"
    "v_mfma_f32_16x16x32_bf16 v[204:207], v[40:43], v[184:187], v[204:207]\n"
    "v_cvt_pk_bf16_f32 v210, v212, v213\n"
    "v_cvt_pk_bf16_f32 v211, v214, v215\n"
    "v_mfma_f32_16x16x32_bf16 v[200:203], v[20:23], v[188:191], v[200:203]\n"
    "buffer_store_dwordx4 v[208:211], v248, s[80:83], 0 offen offset:320 sc1\n"
    "v_mfma_f32_16x16x32_bf16 v[204:207], v[44:47], v[188:191], v[204:207]\n"
    "v_mfma_f32_16x16x32_bf16 v[200:203], v[24:27], v[192:195], v[200:203]\n"
    "v_add_u32 v248, v255, v248\n"
    "v_mfma_f32_16x16x32_bf16 v[204:207], v[48:51], v[192:195], v[204:207]\n"
    "v_mfma_f32_16x16x32_bf16 v[200:203], v[28:31], v[196:199], v[200:203]\n"
    "v_mfma_f32_16x16x32_bf16 v[204:207], v[52:55], v[196:199], v[204:207]\n"
    "v_mfma_f32_16x16x32_bf16 v[208:211], v[56:59], v[176:179], 0\n"
    "v_mfma_f32_16x16x32_bf16 v[212:215], v[80:83], v[176:179], 0\n"
    "v_mfma_f32_16x16x32_bf16 v[208:211], v[60:63], v[180:183], v[208:211]\n"
    "v_mfma_f32_16x16x32_bf16 v[212:215], v[84:87], v[180:183], v[212:215]\n"
    "v_mfma_f32_16x16x32_bf16 v[208:211], v[64:67], v[184:187], v[208:211]\n"
    "v_mfma_f32_16x16x32_bf16 v[212:215], v[88:91], v[184:187], v[212:215]\n"
    "s_waitcnt lgkmcnt(0)\n"
    "v_mul_f32 v200, v200, v253\n"
    "v_mfma_f32_16x16x32_bf16 v[208:211], v[68:71], v[188:191], v[208:211]\n"
    "v_mul_f32 v201, v201, v253\n"
    "v_mul_f32 v202, v202, v253\n"
    "v_mfma_f32_16x16x32_bf16 v[212:215], v[92:95], v[188:191], v[212:215]\n"
    "v_mul_f32 v203, v203, v253\n"
    "v_mul_f32 v204, v204, v253\n"
    "v_mfma_f32_16x16x32_bf16 v[208:211], v[72:75], v[192:195], v[208:211]\n"
    "v_mul_f32 v205, v205, v253\n"
    "v_mul_f32 v206, v206, v253\n"
    "v_mfma_f32_16x16x32_bf16 v[212:215], v[96:99], v[192:195], v[212:215]\n"
    "v_mul_f32 v207, v207, v253\n"
    "v_cvt_pk_bf16_f32 v200, v200, v201\n"
    "v_mfma_f32_16x16x32_bf16 v[208:211], v[76:79], v[196:199], v[208:211]\n"
    "v_cvt_pk_bf16_f32 v201, v202, v203\n"
    "v_cvt_pk_bf16_f32 v202, v204, v205\n"
    "v_mfma_f32_16x16x32_bf16 v[212:215], v[100:103], v[196:199], v[212:215]\n"
    "v_cvt_pk_bf16_f32 v203, v206, v207\n"
    "buffer_store_dwordx4 v[200:203], v249, s[80:83], 0 offen offset:128 sc1\n"
    "s_nop 2\n"
    "v_mul_f32 v208, v208, v253\n"
    "v_mul_f32 v209, v209, v253\n"
    "v_mul_f32 v210, v210, v253\n"
    "v_mul_f32 v211, v211, v253\n"
    "v_mul_f32 v212, v212, v253\n"
    "v_mul_f32 v213, v213, v253\n"
    "v_mul_f32 v214, v214, v253\n"
    "v_mul_f32 v215, v215, v253\n"
    "v_cvt_pk_bf16_f32 v208, v208, v209\n"
    "v_cvt_pk_bf16_f32 v209, v210, v211\n"
    "v_cvt_pk_bf16_f32 v210, v212, v213\n"
    "v_cvt_pk_bf16_f32 v211, v214, v215\n"
    "buffer_store_dwordx4 v[208:211], v249, s[80:83], 0 offen offset:320 sc1\n"
    "s_branch .Lg_end\n"
    ".Lg34:\n"
    "v_mfma_f32_16x16x32_bf16 v[208:211], v[56:59], v[176:179], 0\n"
    "v_mfma_f32_16x16x32_bf16 v[212:215], v[80:83], v[176:179], 0\n"
    "v_mfma_f32_16x16x32_bf16 v[208:211], v[60:63], v[180:183], v[208:211]\n"
    "v_mfma_f32_16x16x32_bf16 v[212:215], v[84:87], v[180:183], v[212:215]\n"
    "v_mul_f32 v200, v200, v253\n"
    "v_mul_f32 v201, v201, v253\n"
    "v_mfma_f32_16x16x32_bf16 v[208:211], v[64:67], v[184:187], v[208:211]\n"
    "v_mul_f32 v202, v202, v253\n"
    "v_mul_f32 v203, v203, v253\n"
    "v_mfma_f32_16x16x32_bf16 v[212:215], v[88:91], v[184:187], v[212:215]\n"
    "ds_read_b32 v228, v224 offset:832\n"
    "v_mul_f32 v204, v204, v253\n"
    "v_mfma_f32_16x16x32_bf16 v[208:211], v[68:71], v[188:191], v[208:211]\n"
    "v_mul_f32 v205, v205, v253\n"
    "v_mul_f32 v206, v206, v253\n"
    "v_mfma_f32_16x16x32_bf16 v[212:215], v[92:95], v[188:191], v[212:215]\n"
    "v_mul_f32 v207, v207, v253\n"
    "v_cvt_pk_bf16_f32 v200, v200, v201\n"
    "v_mfma_f32_16x16x32_bf16 v[208:211], v[72:75], v[192:195], v[208:211]\n"
    "v_cvt_pk_bf16_f32 v201, v202, v203\n"
    "v_cvt_pk_bf16_f32 v202, v204, v205\n"
    "v_mfma_f32_16x16x32_bf16 v[212:215], v[96:99], v[192:195], v[212:215]\n"
    "v_cvt_pk_bf16_f32 v203, v206, v207\n"
    "v_mfma_f32_16x16x32_bf16 v[208:211], v[76:79], v[196:199], v[208:211]\n"
    "buffer_store_dwordx4 v[200:203], v249, s[80:83], 0 offen offset:128 sc1\n"
    "v_mfma_f32_16x16x32_bf16 v[212:215], v[100:103], v[196:199], v[212:215]\n"
    "s_waitcnt lgkmcnt(0)\n"
    "v_cmp_gt_u32 vcc, 4, v228\n"
    "s_cbranch_vccz .Lg34g\n"
    ".Lg34r:\n"
    "s_sub_u32 s52, s52, 1\n"
    "s_cmp_lt_i32 s52, 0\n"
    "s_cbranch_scc1 .Lg34g\n"
    "ds_read_b32 v228, v224 offset:832\n"
    "s_waitcnt lgkmcnt(0)\n"
    "v_cmp_gt_u32 vcc, 4, v228\n"
    "s_cbranch_vccnz .Lg34r\n"
    "s_branch .Lg34g\n"
    ".Lg34g:\n"
    "ds_read_b64_tr_b16 v[152:153], v0 offset:0\n"
    "ds_read_b64_tr_b16 v[154:155], v1 offset:0\n"
    "ds_read_b64_tr_b16 v[156:157], v0 offset:1024\n"
    "ds_read_b64_tr_b16 v[158:159], v1 offset:1024\n"
    "ds_read_b64_tr_b16 v[160:161], v0 offset:2048\n"
    "ds_read_b64_tr_b16 v[162:163], v1 offset:2048\n"
    "ds_read_b64_tr_b16 v[164:165], v0 offset:3072\n"
    "ds_read_b64_tr_b16 v[166:167], v1 offset:3072\n"
    "ds_read_b64_tr_b16 v[168:169], v0 offset:4096\n"
    "ds_read_b64_tr_b16 v[170:171], v1 offset:4096\n"
    "ds_read_b64_tr_b16 v[172:173], v0 offset:5120\n"
    "ds_read_b64_tr_b16 v[174:175], v1 offset:5120\n"
    "ds_read_b32 v252, v238 offset:192\n"
    "s_branch .Lg34j\n"
    ".Lg_last:\n"
                :: "{v[0:1]}"(vz), "{v238}"(vi), "{v[248:251]}"(to), "{s[80:83]}"(srd), "{v[2:3]}"(va), "{v[6:7]}"(vb), "{v[224:227]}"(fl), "{v[230:233]}"(xo), "{v234}"(xo4), "{v235}"(lf[0]), "{v236}"(lf[1]),
                   "{s[40:41]}"(ws), "{s[42:43]}"(xb), "{s44}"(UNI(axis_at(rm, 14) * IROWB)), "{s45}"(UNI(axis_at(rm, 15) * IROWB)),
                   "{s46}"(UNI(axis_at(rm, 16) * IROWB)), "{s47}"(UNI(axis_at(rm, 17) * IROWB)), "{s48}"(UNI(axis_at(rm, t12 ? 4 : 18) * IROWB)),
                   "{s49}"(UNI(axis_at(rm, t12 ? 5 : 19) * IROWB)), "{s84}"(UNI(3200 * hs)), "{s85}"(UNI(2 * rm.d * IROWB)), "{v255}"(d56), "{s50}"(h), "{s58}"(UNI(t12)), "{s59}"(UNI(t0 + (bid & 4 ? 4200 : 5000)))
                : "memory", "v253", "s51", "s52", "s53", "s54", "s55", "s56", "s57", "s60", "s61", "s79", "s90", "s91", "s98", "s99", "s92", "s93", "s94", "s95", "vcc", "scc");
        } else {
            const unsigned r3 = axis_at(rm, 3 + e) * IROWB + 192 * h;
            const u32x4 vo = {r3 + c0, r3 + c1, dd[0], dd[1]};
            const u32x2 uu = {(unsigned)(pixad(3 + qj) + (3 + e) * RP + 16 * g), LQ + l16 + h * 3072 + 2048};
            u32x2 xp;
#pragma unroll
            for (int k = 0; k < 2; ++k) {
                const int i = 72 * h + min(lane + 64 * k, 71), r = i / 48, c = (i - 48 * r) / 3;
                xp[k] = axis_at(rm, 14 + r) * IROWB + axis_at(cm, 3 + c) * PIXB + 128 * (i - 48 * r - 3 * c);
            }
            u32x2 lf = {l16, (unsigned)(WS_FL + ((bid & (NREP - 1)) * 144 + 72 + nf) * 128)};
            asm volatile("s_nop 7\n"
    "s_mov_b32 s52, 0x40000\n"
    "s_load_dwordx2 s[78:79], s[40:41], 0x0\n"
    "s_mul_i32 s53, s50, 36864\n"
    "s_add_u32 s57, s53, 84992\n"
    "s_add_u32 s64, s40, s57\n"
    "s_addc_u32 s65, s41, 0\n"
    "s_add_u32 s57, s53, 91136\n"
    "s_add_u32 s66, s40, s57\n"
    "s_addc_u32 s67, s41, 0\n"
    "s_add_u32 s57, s53, 97280\n"
    "s_add_u32 s68, s40, s57\n"
    "s_addc_u32 s69, s41, 0\n"
    "s_add_u32 s57, s53, 103424\n"
    "s_add_u32 s70, s40, s57\n"
    "s_addc_u32 s71, s41, 0\n"
    "s_add_u32 s57, s53, 109568\n"
    "s_add_u32 s72, s40, s57\n"
    "s_addc_u32 s73, s41, 0\n"
    "s_add_u32 s57, s53, 115712\n"
    "s_add_u32 s74, s40, s57\n"
    "s_addc_u32 s75, s41, 0\n"
    "s_sub_u32 s88, s72, 73728\n"
    "s_subb_u32 s89, s73, 0\n"
    "s_sub_u32 s90, s74, 73728\n"
    "s_subb_u32 s91, s75, 0\n"
    "v_add_u32 v251, -6144, v236\n"
    "s_cmp_eq_u32 s50, 0\n"
    "s_cbranch_scc1 .Lh_x0\n"
    "v_mov_b32 v233, v234\n"
    ".Lh_x0:\n"
    "s_waitcnt lgkmcnt(0)\n"
    "s_add_u32 s78, s78, 0x7f4a7c15\n"
    "s_add_u32 s79, s79, 0x9e3779b9\n"
    "v_mov_b64 v[240:241], s[78:79]\n"
    "s_mov_b32 s51, 0x4000\n"
    "ds_read_b32 v228, v224 offset:288\n"
    "s_waitcnt lgkmcnt(0)\n"
    "v_cmp_gt_u32 vcc, 1, v228\n"
    "s_cbranch_vccnz .Lh1\n"
    ".Lh1b:\n"
    ".Lh_d2:\n"
    "s_sleep 1\n"
    "s_memtime s[76:77]\n"
    "s_waitcnt lgkmcnt(0)\n"
    "s_sub_u32 s76, s76, s87\n"
    "s_cmp_lt_i32 s76, 0\n"
    "s_cbranch_scc1 .Lh_d2\n"
    "s_mov_b32 s51, 0x4000\n"
    "v_mov_b64 v[244:245], v[240:241]\n"
    "v_mov_b64 v[246:247], 0\n"
    "v_mov_b64 v[242:243], 0\n"
    "s_mov_b32 exec_hi, 15\n"
    "global_load_dwordx4 v[244:247], v236, s[40:41] sc1\n"
    "s_mov_b64 exec, -1\n"
    "global_load_dwordx4 v[8:11], v235, s[64:65] offset:-3072 sc1\n"
    "global_load_dwordx4 v[32:35], v235, s[66:67] offset:-3072 sc1\n"
    "global_load_dwordx4 v[12:15], v235, s[64:65] offset:-2048 sc1\n"
    "global_load_dwordx4 v[36:39], v235, s[66:67] offset:-2048 sc1\n"
    "global_load_dwordx4 v[16:19], v235, s[64:65] offset:-1024 sc1\n"
    "global_load_dwordx4 v[40:43], v235, s[66:67] offset:-1024 sc1\n"
    "global_load_dwordx4 v[20:23], v235, s[64:65] offset:0 sc1\n"
    "global_load_dwordx4 v[44:47], v235, s[66:67] offset:0 sc1\n"
    "global_load_dwordx4 v[24:27], v235, s[64:65] offset:1024 sc1\n"
    "global_load_dwordx4 v[48:51], v235, s[66:67] offset:1024 sc1\n"
    "global_load_dwordx4 v[28:31], v235, s[64:65] offset:2048 sc1\n"
    "global_load_dwordx4 v[52:55], v235, s[66:67] offset:2048 sc1\n"
    "global_load_dwordx4 v[56:59], v235, s[68:69] offset:-3072 sc1\n"
    "global_load_dwordx4 v[80:83], v235, s[70:71] offset:-3072 sc1\n"
    "global_load_dwordx4 v[60:63], v235, s[68:69] offset:-2048 sc1\n"
    "global_load_dwordx4 v[84:87], v235, s[70:71] offset:-2048 sc1\n"
    "global_load_dwordx4 v[64:67], v235, s[68:69] offset:-1024 sc1\n"
    "global_load_dwordx4 v[88:91], v235, s[70:71] offset:-1024 sc1\n"
    "global_load_dwordx4 v[68:71], v235, s[68:69] offset:0 sc1\n"
    "global_load_dwordx4 v[92:95], v235, s[70:71] offset:0 sc1\n"
    "s_waitcnt vmcnt(20)\n"
    "v_cmp_ne_u64 vcc, v[240:241], v[244:245]\n"
    "s_cbranch_vccnz .Lh_pl\n"
    "global_load_dwordx4 v[72:75], v235, s[68:69] offset:1024 sc1\n"
    "global_load_dwordx4 v[96:99], v235, s[70:71] offset:1024 sc1\n"
    "global_load_dwordx4 v[76:79], v235, s[68:69] offset:2048 sc1\n"
    "global_load_dwordx4 v[100:103], v235, s[70:71] offset:2048 sc1\n"
    "global_load_dwordx4 v[104:107], v235, s[72:73] offset:-3072 sc1\n"
    "global_load_dwordx4 v[128:131], v235, s[74:75] offset:-3072 sc1\n"
    "global_load_dwordx4 v[108:111], v235, s[72:73] offset:-2048 sc1\n"
    "global_load_dwordx4 v[132:135], v235, s[74:75] offset:-2048 sc1\n"
    "global_load_dwordx4 v[112:115], v235, s[72:73] offset:-1024 sc1\n"
    "global_load_dwordx4 v[136:139], v235, s[74:75] offset:-1024 sc1\n"
    "global_load_dwordx4 v[116:119], v235, s[72:73] offset:0 sc1\n"
    "global_load_dwordx4 v[140:143], v235, s[74:75] offset:0 sc1\n"
    "global_load_dwordx4 v[120:123], v235, s[72:73] offset:1024 sc1\n"
    "global_load_dwordx4 v[144:147], v235, s[74:75] offset:1024 sc1\n"
    "global_load_dwordx4 v[124:127], v235, s[72:73] offset:2048 sc1\n"
    "global_load_dwordx4 v[148:151], v235, s[74:75] offset:2048 sc1\n"
    ".Lh_rs:\n"
    "s_lshl_b32 s54, s50, 10\n"
    "s_add_u32 s54, s54, 1024\n"
    "s_mul_i32 s56, s50, 7776\n"
    "s_max_u32 s55, s56, 7168\n"
    "s_add_u32 s60, s42, s84\n"
    "s_addc_u32 s61, s43, 0\n"
    "s_add_u32 m0, s54, 106752\n"
    "s_nop 0\n"
    "global_load_lds_dwordx4 v230, s[60:61]\n"
    "s_add_u32 m0, s54, 108800\n"
    "s_nop 0\n"
    "global_load_lds_dwordx4 v231, s[60:61]\n"
    "s_add_u32 m0, s54, 110848\n"
    "s_nop 0\n"
    "global_load_lds_dwordx4 v232, s[60:61]\n"
    "s_add_u32 m0, s55, 106752\n"
    "s_nop 0\n"
    "global_load_lds_dwordx4 v233, s[60:61]\n"
    "s_add_u32 m0, s56, 106752\n"
    "s_nop 0\n"
    "global_load_lds_dwordx4 v234, s[60:61]\n"
    "s_add_u32 s60, s42, s85\n"
    "s_addc_u32 s61, s43, 0\n"
    "s_add_u32 m0, s54, 115648\n"
    "s_nop 0\n"
    "global_load_lds_dwordx4 v230, s[60:61]\n"
    "s_add_u32 m0, s54, 117696\n"
    "s_nop 0\n"
    "global_load_lds_dwordx4 v231, s[60:61]\n"
    "s_add_u32 m0, s54, 119744\n"
    "s_nop 0\n"
    "global_load_lds_dwordx4 v232, s[60:61]\n"
    "s_add_u32 m0, s55, 115648\n"
    "s_nop 0\n"
    "global_load_lds_dwordx4 v233, s[60:61]\n"
    "s_add_u32 m0, s56, 115648\n"
    "s_nop 0\n"
    "global_load_lds_dwordx4 v234, s[60:61]\n"
    "global_load_dword v250, v238, s[42:43]\n"
    "global_load_dword v250, v239, s[42:43]\n"
    "s_waitcnt vmcnt(44)\n"
    "v_add3_u32 v242, v242, v8, v11\n"
    "v_sub_u32 v8, v8, v240\n"
    "v_add3_u32 v242, v242, v32, v35\n"
    "v_sub_u32 v32, v32, v240\n"
    "v_add3_u32 v242, v242, v12, v15\n"
    "v_sub_u32 v12, v12, v240\n"
    "v_add3_u32 v242, v242, v36, v39\n"
    "v_sub_u32 v36, v36, v240\n"
    "s_waitcnt vmcnt(40)\n"
    "v_add3_u32 v242, v242, v16, v19\n"
    "v_sub_u32 v16, v16, v240\n"
    "v_add3_u32 v242, v242, v40, v43\n"
    "v_sub_u32 v40, v40, v240\n"
    "v_add3_u32 v242, v242, v20, v23\n"
    "v_sub_u32 v20, v20, v240\n"
    "v_add3_u32 v242, v242, v44, v47\n"
    "v_sub_u32 v44, v44, v240\n"
    "s_waitcnt vmcnt(36)\n"
    "v_add3_u32 v242, v242, v24, v27\n"
    "v_sub_u32 v24, v24, v240\n"
    "v_add3_u32 v242, v242, v48, v51\n"
    "v_sub_u32 v48, v48, v240\n"
    "v_add3_u32 v242, v242, v28, v31\n"
    "v_sub_u32 v28, v28, v240\n"
    "v_add3_u32 v242, v242, v52, v55\n"
    "v_sub_u32 v52, v52, v240\n"
    "s_waitcnt vmcnt(32)\n"
    "v_add3_u32 v242, v242, v56, v59\n"
    "v_sub_u32 v56, v56, v240\n"
    "v_add3_u32 v242, v242, v80, v83\n"
    "v_sub_u32 v80, v80, v240\n"
    "v_add3_u32 v242, v242, v60, v63\n"
    "v_sub_u32 v60, v60, v240\n"
    "v_add3_u32 v242, v242, v84, v87\n"
    "v_sub_u32 v84, v84, v240\n"
    "s_waitcnt vmcnt(28)\n"
    "v_add3_u32 v242, v242, v64, v67\n"
    "v_sub_u32 v64, v64, v240\n"
    "v_add3_u32 v242, v242, v88, v91\n"
    "v_sub_u32 v88, v88, v240\n"
    "v_add3_u32 v242, v242, v68, v71\n"
    "v_sub_u32 v68, v68, v240\n"
    "v_add3_u32 v242, v242, v92, v95\n"
    "v_sub_u32 v92, v92, v240\n"
    "s_waitcnt vmcnt(24)\n"
    "v_add3_u32 v242, v242, v72, v75\n"
    "v_sub_u32 v72, v72, v240\n"
    "v_add3_u32 v242, v242, v96, v99\n"
    "v_sub_u32 v96, v96, v240\n"
    "v_add3_u32 v242, v242, v76, v79\n"
    "v_sub_u32 v76, v76, v240\n"
    "v_add3_u32 v242, v242, v100, v103\n"
    "v_sub_u32 v100, v100, v240\n"
    "s_waitcnt vmcnt(20)\n"
    "v_add3_u32 v242, v242, v104, v107\n"
    "v_sub_u32 v104, v104, v240\n"
    "v_add3_u32 v242, v242, v128, v131\n"
    "v_sub_u32 v128, v128, v240\n"
    "v_add3_u32 v242, v242, v108, v111\n"
    "v_sub_u32 v108, v108, v240\n"
    "v_add3_u32 v242, v242, v132, v135\n"
    "v_sub_u32 v132, v132, v240\n"
    "s_waitcnt vmcnt(16)\n"
    "v_add3_u32 v242, v242, v112, v115\n"
    "v_sub_u32 v112, v112, v240\n"
    "v_add3_u32 v242, v242, v136, v139\n"
    "v_sub_u32 v136, v136, v240\n"
    "v_add3_u32 v242, v242, v116, v119\n"
    "v_sub_u32 v116, v116, v240\n"
    "v_add3_u32 v242, v242, v140, v143\n"
    "v_sub_u32 v140, v140, v240\n"
    "s_waitcnt vmcnt(12)\n"
    "v_add3_u32 v242, v242, v120, v123\n"
    "v_sub_u32 v120, v120, v240\n"
    "v_add3_u32 v242, v242, v144, v147\n"
    "v_sub_u32 v144, v144, v240\n"
    "v_add3_u32 v242, v242, v124, v127\n"
    "v_sub_u32 v124, v124, v240\n"
    "v_add3_u32 v242, v242, v148, v151\n"
    "v_sub_u32 v148, v148, v240\n"
    ".Lh_ce:\n"
    "v_cmp_ne_u64 s[76:77], v[240:241], v[244:245]\n"
    "v_sub_u32 v242, v242, v246\n"
    "v_mov_b32 v243, v242\n"
    "s_nop 1\n"
    "v_permlane32_swap_b32 v242, v243\n"
    "v_add_u32 v242, v242, v243\n"
    "s_nop 1\n"
    "v_add_u32_dpp v242, v242, v242 row_ror:8 row_mask:0xf bank_mask:0xf\n"
    "s_nop 1\n"
    "v_add_u32_dpp v242, v242, v242 row_ror:4 row_mask:0xf bank_mask:0xf\n"
    "s_nop 1\n"
    "v_add_u32_dpp v242, v242, v242 row_ror:2 row_mask:0xf bank_mask:0xf\n"
    "s_nop 1\n"
    "v_add_u32_dpp v242, v242, v242 row_ror:1 row_mask:0xf bank_mask:0xf\n"
    "v_mov_b32 v243, v242\n"
    "s_nop 1\n"
    "v_permlane16_swap_b32 v242, v243\n"
    "v_add_u32 v242, v242, v243\n"
    "v_cmp_ne_u32 vcc, 0, v242\n"
    "s_nop 1\n"
    "s_or_b64 vcc, vcc, s[76:77]\n"
    "s_cbranch_scc1 .Lh_re\n"
    ".Lh_ok:\n"
    "v_mov_b32 v227, 1\n"
    "ds_write_b32 v225, v227 offset:272\n"
    "ds_read_b32 v228, v224 offset:0\n"
    "s_waitcnt lgkmcnt(0)\n"
    "v_cmp_gt_u32 vcc, 6, v228\n"
    "s_cbranch_vccnz .Lh2\n"
    ".Lh2b:\n"
    "s_waitcnt vmcnt(2)\n"
    "v_mov_b32 v227, 99\n"
    "ds_write_b32 v226, v227 offset:0\n"
    "s_setprio 0\n"
    "ds_read_b32 v228, v224 offset:128\n"
    "s_waitcnt lgkmcnt(0)\n"
    "v_cmp_gt_u32 vcc, 1, v228\n"
    "s_cbranch_vccnz .Lh3\n"
    ".Lh3b:\n"
    "ds_read_b64_tr_b16 v[152:153], v2 offset:18432\n"
    "ds_read_b64_tr_b16 v[154:155], v3 offset:18432\n"
    "ds_read_b64_tr_b16 v[156:157], v2 offset:19456\n"
    "ds_read_b64_tr_b16 v[158:159], v3 offset:19456\n"
    "ds_read_b64_tr_b16 v[160:161], v2 offset:20480\n"
    "ds_read_b64_tr_b16 v[162:163], v3 offset:20480\n"
    "ds_read_b64_tr_b16 v[164:165], v2 offset:21504\n"
    "ds_read_b64_tr_b16 v[166:167], v3 offset:21504\n"
    "ds_read_b64_tr_b16 v[168:169], v2 offset:22528\n"
    "ds_read_b64_tr_b16 v[170:171], v3 offset:22528\n"
    "ds_read_b64_tr_b16 v[172:173], v2 offset:23552\n"
    "ds_read_b64_tr_b16 v[174:175], v3 offset:23552\n"
    "ds_read_b32 v252, v1 offset:0\n"
    "v_mov_b32 v227, 1\n"
    "ds_write_b32 v226, v227 offset:32\n"
    "v_mov_b32 v242, v240\n"
    "v_mov_b32 v243, v241\n"
    "v_and_b32 v229, 0x70, v235\n"
    "v_add_u32 v229, s48, v229\n"
    "global_store_dwordx4 v229, v[240:243], s[40:41] sc1\n"
    "s_waitcnt lgkmcnt(10)\n"
    "v_mfma_f32_16x16x32_bf16 v[200:203], v[8:11], v[152:155], 0\n"
    "v_mfma_f32_16x16x32_bf16 v[204:207], v[32:35], v[152:155], 0\n"
    "v_mfma_f32_16x16x32_bf16 v[200:203], v[12:15], v[156:159], v[200:203]\n"
    "v_mfma_f32_16x16x32_bf16 v[204:207], v[36:39], v[156:159], v[204:207]\n"
    "s_waitcnt lgkmcnt(8)\n"
    "v_mfma_f32_16x16x32_bf16 v[200:203], v[16:19], v[160:163], v[200:203]\n"
    "v_mfma_f32_16x16x32_bf16 v[204:207], v[40:43], v[160:163], v[204:207]\n"
    "s_waitcnt lgkmcnt(6)\n"
    "v_mfma_f32_16x16x32_bf16 v[200:203], v[20:23], v[164:167], v[200:203]\n"
    "v_mfma_f32_16x16x32_bf16 v[204:207], v[44:47], v[164:167], v[204:207]\n"
    "s_waitcnt lgkmcnt(4)\n"
    "v_mfma_f32_16x16x32_bf16 v[200:203], v[24:27], v[168:171], v[200:203]\n"
    "v_mfma_f32_16x16x32_bf16 v[204:207], v[48:51], v[168:171], v[204:207]\n"
    "s_waitcnt lgkmcnt(2)\n"
    "v_mfma_f32_16x16x32_bf16 v[200:203], v[28:31], v[172:175], v[200:203]\n"
    "v_mfma_f32_16x16x32_bf16 v[204:207], v[52:55], v[172:175], v[204:207]\n"
    "v_mfma_f32_16x16x32_bf16 v[208:211], v[56:59], v[152:155], 0\n"
    "v_mfma_f32_16x16x32_bf16 v[212:215], v[80:83], v[152:155], 0\n"
    "ds_read_b32 v228, v224 offset:144\n"
    "v_mfma_f32_16x16x32_bf16 v[208:211], v[60:63], v[156:159], v[208:211]\n"
    "v_mfma_f32_16x16x32_bf16 v[212:215], v[84:87], v[156:159], v[212:215]\n"
    "v_mfma_f32_16x16x32_bf16 v[208:211], v[64:67], v[160:163], v[208:211]\n"
    "s_waitcnt lgkmcnt(2)\n"
    "v_mul_f32 v200, v200, v252\n"
    "v_mfma_f32_16x16x32_bf16 v[212:215], v[88:91], v[160:163], v[212:215]\n"
    "v_mul_f32 v201, v201, v252\n"
    "v_mul_f32 v202, v202, v252\n"
    "v_mfma_f32_16x16x32_bf16 v[208:211], v[68:71], v[164:167], v[208:211]\n"
    "v_mul_f32 v203, v203, v252\n"
    "v_mul_f32 v204, v204, v252\n"
    "v_mfma_f32_16x16x32_bf16 v[212:215], v[92:95], v[164:167], v[212:215]\n"
    "v_mul_f32 v205, v205, v252\n"
    "v_mul_f32 v206, v206, v252\n"
    "v_mfma_f32_16x16x32_bf16 v[208:211], v[72:75], v[168:171], v[208:211]\n"
    "v_mul_f32 v207, v207, v252\n"
    "v_cvt_pk_bf16_f32 v200, v200, v201\n"
    "v_mfma_f32_16x16x32_bf16 v[212:215], v[96:99], v[168:171], v[212:215]\n"
    "v_cvt_pk_bf16_f32 v201, v202, v203\n"
    "v_cvt_pk_bf16_f32 v202, v204, v205\n"
    "v_mfma_f32_16x16x32_bf16 v[208:211], v[76:79], v[172:175], v[208:211]\n"
    "s_waitcnt lgkmcnt(0)\n"
    "v_cmp_gt_u32 vcc, 1, v228\n"
    "v_mfma_f32_16x16x32_bf16 v[212:215], v[100:103], v[172:175], v[212:215]\n"
    "v_cvt_pk_bf16_f32 v203, v206, v207\n"
    "s_cbranch_vccnz .Lh4\n"
    "v_mfma_f32_16x16x32_bf16 v[216:219], v[104:107], v[152:155], 0\n"
    "ds_read_b64_tr_b16 v[176:177], v2 offset:24576\n"
    "ds_read_b64_tr_b16 v[178:179], v3 offset:24576\n"
    "v_mfma_f32_16x16x32_bf16 v[220:223], v[128:131], v[152:155], 0\n"
    "ds_read_b64_tr_b16 v[180:181], v2 offset:25600\n"
    "ds_read_b64_tr_b16 v[182:183], v3 offset:25600\n"
    "v_mfma_f32_16x16x32_bf16 v[216:219], v[108:111], v[156:159], v[216:219]\n"
    "ds_read_b64_tr_b16 v[184:185], v2 offset:26624\n"
    "ds_read_b64_tr_b16 v[186:187], v3 offset:26624\n"
    "v_mfma_f32_16x16x32_bf16 v[220:223], v[132:135], v[156:159], v[220:223]\n"
    "ds_read_b64_tr_b16 v[188:189], v2 offset:27648\n"
    "ds_read_b64_tr_b16 v[190:191], v3 offset:27648\n"
    "v_mfma_f32_16x16x32_bf16 v[216:219], v[112:115], v[160:163], v[216:219]\n"
    "buffer_store_dwordx4 v[200:203], v4, s[80:83], 0 offen offset:0 sc1\n"
    "v_mfma_f32_16x16x32_bf16 v[220:223], v[136:139], v[160:163], v[220:223]\n"
    "v_mul_f32 v208, v208, v252\n"
    "v_mul_f32 v209, v209, v252\n"
    "v_mfma_f32_16x16x32_bf16 v[216:219], v[116:119], v[164:167], v[216:219]\n"
    "ds_read_b64_tr_b16 v[192:193], v2 offset:28672\n"
    "ds_read_b64_tr_b16 v[194:195], v3 offset:28672\n"
    "v_mfma_f32_16x16x32_bf16 v[220:223], v[140:143], v[164:167], v[220:223]\n"
    "v_mul_f32 v210, v210, v252\n"
    "v_mul_f32 v211, v211, v252\n"
    "v_mfma_f32_16x16x32_bf16 v[216:219], v[120:123], v[168:171], v[216:219]\n"
    "ds_read_b64_tr_b16 v[196:197], v2 offset:29696\n"
    "ds_read_b64_tr_b16 v[198:199], v3 offset:29696\n"
    "v_mfma_f32_16x16x32_bf16 v[220:223], v[144:147], v[168:171], v[220:223]\n"
    "v_mul_f32 v212, v212, v252\n"
    "v_mov_b32 v227, 2\n"
    "v_mfma_f32_16x16x32_bf16 v[216:219], v[124:127], v[172:175], v[216:219]\n"
    "ds_read_b32 v253, v1 offset:64\n"
    "v_mul_f32 v213, v213, v252\n"
    "v_mfma_f32_16x16x32_bf16 v[220:223], v[148:151], v[172:175], v[220:223]\n"
    "v_mul_f32 v214, v214, v252\n"
    "v_mul_f32 v215, v215, v252\n"
    "ds_write_b32 v226, v227 offset:32\n"
    "v_cvt_pk_bf16_f32 v208, v208, v209\n"
    "v_cvt_pk_bf16_f32 v209, v210, v211\n"
    "v_cvt_pk_bf16_f32 v210, v212, v213\n"
    "v_cvt_pk_bf16_f32 v211, v214, v215\n"
    "buffer_store_dwordx4 v[208:211], v4, s[80:83], 0 offen offset:64 sc1\n"
    ".Lh4j:\n"
    "s_waitcnt lgkmcnt(2)\n"
    "v_mfma_f32_16x16x32_bf16 v[200:203], v[8:11], v[176:179], 0\n"
    "v_mul_f32 v216, v216, v252\n"
    "v_mul_f32 v217, v217, v252\n"
    "v_mfma_f32_16x16x32_bf16 v[204:207], v[32:35], v[176:179], 0\n"
    "v_mul_f32 v218, v218, v252\n"
    "v_mul_f32 v219, v219, v252\n"
    "v_mfma_f32_16x16x32_bf16 v[200:203], v[12:15], v[180:183], v[200:203]\n"
    "v_mul_f32 v220, v220, v252\n"
    "v_mul_f32 v221, v221, v252\n"
    "v_mfma_f32_16x16x32_bf16 v[204:207], v[36:39], v[180:183], v[204:207]\n"
    "v_mul_f32 v222, v222, v252\n"
    "v_mul_f32 v223, v223, v252\n"
    "v_mfma_f32_16x16x32_bf16 v[200:203], v[16:19], v[184:187], v[200:203]\n"
    "v_cvt_pk_bf16_f32 v216, v216, v217\n"
    "v_cvt_pk_bf16_f32 v217, v218, v219\n"
    "v_mfma_f32_16x16x32_bf16 v[204:207], v[40:43], v[184:187], v[204:207]\n"
    "v_cvt_pk_bf16_f32 v218, v220, v221\n"
    "v_cvt_pk_bf16_f32 v219, v222, v223\n"
    "v_mfma_f32_16x16x32_bf16 v[200:203], v[20:23], v[188:191], v[200:203]\n"
    "buffer_store_dwordx4 v[216:219], v4, s[80:83], 0 offen offset:128 sc1\n"
    "v_mfma_f32_16x16x32_bf16 v[204:207], v[44:47], v[188:191], v[204:207]\n"
    "v_mfma_f32_16x16x32_bf16 v[200:203], v[24:27], v[192:195], v[200:203]\n"
    "v_add_u32 v4, s46, v4\n"
    "v_mfma_f32_16x16x32_bf16 v[204:207], v[48:51], v[192:195], v[204:207]\n"
    "v_mfma_f32_16x16x32_bf16 v[200:203], v[28:31], v[196:199], v[200:203]\n"
    "v_mfma_f32_16x16x32_bf16 v[204:207], v[52:55], v[196:199], v[204:207]\n"
    "v_mfma_f32_16x16x32_bf16 v[208:211], v[56:59], v[176:179], 0\n"
    "v_mfma_f32_16x16x32_bf16 v[212:215], v[80:83], v[176:179], 0\n"
    "ds_read_b32 v228, v224 offset:160\n"
    "v_mfma_f32_16x16x32_bf16 v[208:211], v[60:63], v[180:183], v[208:211]\n"
    "v_mfma_f32_16x16x32_bf16 v[212:215], v[84:87], v[180:183], v[212:215]\n"
    "v_mfma_f32_16x16x32_bf16 v[208:211], v[64:67], v[184:187], v[208:211]\n"
    "s_waitcnt lgkmcnt(2)\n"
    "v_mul_f32 v200, v200, v253\n"
    "v_mfma_f32_16x16x32_bf16 v[212:215], v[88:91], v[184:187], v[212:215]\n"
    "v_mul_f32 v201, v201, v253\n"
    "v_mul_f32 v202, v202, v253\n"
    "v_mfma_f32_16x16x32_bf16 v[208:211], v[68:71], v[188:191], v[208:211]\n"
    "v_mul_f32 v203, v203, v253\n"
    "v_mul_f32 v204, v204, v253\n"
    "v_mfma_f32_16x16x32_bf16 v[212:215], v[92:95], v[188:191], v[212:215]\n"
    "v_mul_f32 v205, v205, v253\n"
    "v_mul_f32 v206, v206, v253\n"
    "v_mfma_f32_16x16x32_bf16 v[208:211], v[72:75], v[192:195], v[208:211]\n"
    "v_mul_f32 v207, v207, v253\n"
    "v_cvt_pk_bf16_f32 v200, v200, v201\n"
    "v_mfma_f32_16x16x32_bf16 v[212:215], v[96:99], v[192:195], v[212:215]\n"
    "v_cvt_pk_bf16_f32 v201, v202, v203\n"
    "v_cvt_pk_bf16_f32 v202, v204, v205\n"
    "v_mfma_f32_16x16x32_bf16 v[208:211], v[76:79], v[196:199], v[208:211]\n"
    "s_waitcnt lgkmcnt(0)\n"
    "v_cmp_gt_u32 vcc, 1, v228\n"
    "v_mfma_f32_16x16x32_bf16 v[212:215], v[100:103], v[196:199], v[212:215]\n"
    "v_cvt_pk_bf16_f32 v203, v206, v207\n"
    "s_cbranch_vccnz .Lh5\n"
    "v_mfma_f32_16x16x32_bf16 v[216:219], v[104:107], v[176:179], 0\n"
    "ds_read_b64_tr_b16 v[152:153], v2 offset:30720\n"
    "ds_read_b64_tr_b16 v[154:155], v3 offset:30720\n"
    "v_mfma_f32_16x16x32_bf16 v[220:223], v[128:131], v[176:179], 0\n"
    "ds_read_b64_tr_b16 v[156:157], v2 offset:31744\n"
    "ds_read_b64_tr_b16 v[158:159], v3 offset:31744\n"
    "v_mfma_f32_16x16x32_bf16 v[216:219], v[108:111], v[180:183], v[216:219]\n"
    "ds_read_b64_tr_b16 v[160:161], v2 offset:32768\n"
    "ds_read_b64_tr_b16 v[162:163], v3 offset:32768\n"
    "v_mfma_f32_16x16x32_bf16 v[220:223], v[132:135], v[180:183], v[220:223]\n"
    "ds_read_b64_tr_b16 v[164:165], v2 offset:33792\n"
    "ds_read_b64_tr_b16 v[166:167], v3 offset:33792\n"
    "v_mfma_f32_16x16x32_bf16 v[216:219], v[112:115], v[184:187], v[216:219]\n"
    "buffer_store_dwordx4 v[200:203], v5, s[80:83], 0 offen offset:0 sc1\n"
    "v_mfma_f32_16x16x32_bf16 v[220:223], v[136:139], v[184:187], v[220:223]\n"
    "v_mul_f32 v208, v208, v253\n"
    "v_mul_f32 v209, v209, v253\n"
    "v_mfma_f32_16x16x32_bf16 v[216:219], v[116:119], v[188:191], v[216:219]\n"
    "ds_read_b64_tr_b16 v[168:169], v2 offset:34816\n"
    "ds_read_b64_tr_b16 v[170:171], v3 offset:34816\n"
    "v_mfma_f32_16x16x32_bf16 v[220:223], v[140:143], v[188:191], v[220:223]\n"
    "v_mul_f32 v210, v210, v253\n"
    "v_mul_f32 v211, v211, v253\n"
    "v_mfma_f32_16x16x32_bf16 v[216:219], v[120:123], v[192:195], v[216:219]\n"
    "ds_read_b64_tr_b16 v[172:173], v2 offset:35840\n"
    "ds_read_b64_tr_b16 v[174:175], v3 offset:35840\n"
    "v_mfma_f32_16x16x32_bf16 v[220:223], v[144:147], v[192:195], v[220:223]\n"
    "v_mul_f32 v212, v212, v253\n"
    "v_mov_b32 v227, 3\n"
    "v_mfma_f32_16x16x32_bf16 v[216:219], v[124:127], v[196:199], v[216:219]\n"
    "ds_read_b32 v252, v1 offset:128\n"
    "v_mul_f32 v213, v213, v253\n"
    "v_mfma_f32_16x16x32_bf16 v[220:223], v[148:151], v[196:199], v[220:223]\n"
    "v_mul_f32 v214, v214, v253\n"
    "v_mul_f32 v215, v215, v253\n"
    "ds_write_b32 v226, v227 offset:32\n"
    "v_cvt_pk_bf16_f32 v208, v208, v209\n"
    "v_cvt_pk_bf16_f32 v209, v210, v211\n"
    "v_cvt_pk_bf16_f32 v210, v212, v213\n"
    "v_cvt_pk_bf16_f32 v211, v214, v215\n"
    "buffer_store_dwordx4 v[208:211], v5, s[80:83], 0 offen offset:64 sc1\n"
    ".Lh5j:\n"
    "s_waitcnt lgkmcnt(2)\n"
    "v_mfma_f32_16x16x32_bf16 v[200:203], v[8:11], v[152:155], 0\n"
    "v_mul_f32 v216, v216, v253\n"
    "v_mul_f32 v217, v217, v253\n"
    "v_mfma_f32_16x16x32_bf16 v[204:207], v[32:35], v[152:155], 0\n"
    "v_mul_f32 v218, v218, v253\n"
    "v_mul_f32 v219, v219, v253\n"
    "v_mfma_f32_16x16x32_bf16 v[200:203], v[12:15], v[156:159], v[200:203]\n"
    "v_mul_f32 v220, v220, v253\n"
    "v_mul_f32 v221, v221, v253\n"
    "v_mfma_f32_16x16x32_bf16 v[204:207], v[36:39], v[156:159], v[204:207]\n"
    "v_mul_f32 v222, v222, v253\n"
    "v_mul_f32 v223, v223, v253\n"
    "v_mfma_f32_16x16x32_bf16 v[200:203], v[16:19], v[160:163], v[200:203]\n"
    "v_cvt_pk_bf16_f32 v216, v216, v217\n"
    "v_cvt_pk_bf16_f32 v217, v218, v219\n"
    "v_mfma_f32_16x16x32_bf16 v[204:207], v[40:43], v[160:163], v[204:207]\n"
    "v_cvt_pk_bf16_f32 v218, v220, v221\n"
    "v_cvt_pk_bf16_f32 v219, v222, v223\n"
    "v_mfma_f32_16x16x32_bf16 v[200:203], v[20:23], v[164:167], v[200:203]\n"
    "buffer_store_dwordx4 v[216:219], v5, s[80:83], 0 offen offset:128 sc1\n"
    "v_mfma_f32_16x16x32_bf16 v[204:207], v[44:47], v[164:167], v[204:207]\n"
    "v_mfma_f32_16x16x32_bf16 v[200:203], v[24:27], v[168:171], v[200:203]\n"
    "v_add_u32 v5, s46, v5\n"
    "v_mfma_f32_16x16x32_bf16 v[204:207], v[48:51], v[168:171], v[204:207]\n"
    "v_mfma_f32_16x16x32_bf16 v[200:203], v[28:31], v[172:175], v[200:203]\n"
    "v_mfma_f32_16x16x32_bf16 v[204:207], v[52:55], v[172:175], v[204:207]\n"
    "v_mfma_f32_16x16x32_bf16 v[208:211], v[56:59], v[152:155], 0\n"
    "v_mfma_f32_16x16x32_bf16 v[212:215], v[80:83], v[152:155], 0\n"
    "ds_read_b32 v228, v224 offset:176\n"
    "v_mfma_f32_16x16x32_bf16 v[208:211], v[60:63], v[156:159], v[208:211]\n"
    "v_mfma_f32_16x16x32_bf16 v[212:215], v[84:87], v[156:159], v[212:215]\n"
    "v_mfma_f32_16x16x32_bf16 v[208:211], v[64:67], v[160:163], v[208:211]\n"
    "s_waitcnt lgkmcnt(2)\n"
    "v_mul_f32 v200, v200, v252\n"
    "v_mfma_f32_16x16x32_bf16 v[212:215], v[88:91], v[160:163], v[212:215]\n"
    "v_mul_f32 v201, v201, v252\n"
    "v_mul_f32 v202, v202, v252\n"
    "v_mfma_f32_16x16x32_bf16 v[208:211], v[68:71], v[164:167], v[208:211]\n"
    "v_mul_f32 v203, v203, v252\n"
    "v_mul_f32 v204, v204, v252\n"
    "v_mfma_f32_16x16x32_bf16 v[212:215], v[92:95], v[164:167], v[212:215]\n"
    "v_mul_f32 v205, v205, v252\n"
    "v_mul_f32 v206, v206, v252\n"
    "v_mfma_f32_16x16x32_bf16 v[208:211], v[72:75], v[168:171], v[208:211]\n"
    "v_mul_f32 v207, v207, v252\n"
    "v_cvt_pk_bf16_f32 v200, v200, v201\n"
    "v_mfma_f32_16x16x32_bf16 v[212:215], v[96:99], v[168:171], v[212:215]\n"
    "v_cvt_pk_bf16_f32 v201, v202, v203\n"
    "v_cvt_pk_bf16_f32 v202, v204, v205\n"
    "v_mfma_f32_16x16x32_bf16 v[208:211], v[76:79], v[172:175], v[208:211]\n"
    "s_waitcnt lgkmcnt(0)\n"
    "v_cmp_gt_u32 vcc, 1, v228\n"
    "v_mfma_f32_16x16x32_bf16 v[212:215], v[100:103], v[172:175], v[212:215]\n"
    "v_cvt_pk_bf16_f32 v203, v206, v207\n"
    "s_cbranch_vccnz .Lh6\n"
    "v_mfma_f32_16x16x32_bf16 v[216:219], v[104:107], v[152:155], 0\n"
    "ds_read_b64_tr_b16 v[176:177], v2 offset:18432\n"
    "ds_read_b64_tr_b16 v[178:179], v3 offset:18432\n"
    "v_mfma_f32_16x16x32_bf16 v[220:223], v[128:131], v[152:155], 0\n"
    "ds_read_b64_tr_b16 v[180:181], v2 offset:19456\n"
    "ds_read_b64_tr_b16 v[182:183], v3 offset:19456\n"
    "v_mfma_f32_16x16x32_bf16 v[216:219], v[108:111], v[156:159], v[216:219]\n"
    "ds_read_b64_tr_b16 v[184:185], v2 offset:20480\n"
    "ds_read_b64_tr_b16 v[186:187], v3 offset:20480\n"
    "v_mfma_f32_16x16x32_bf16 v[220:223], v[132:135], v[156:159], v[220:223]\n"
    "ds_read_b64_tr_b16 v[188:189], v2 offset:21504\n"
    "ds_read_b64_tr_b16 v[190:191], v3 offset:21504\n"
    "v_mfma_f32_16x16x32_bf16 v[216:219], v[112:115], v[160:163], v[216:219]\n"
    "buffer_store_dwordx4 v[200:203], v4, s[80:83], 0 offen offset:0 sc1\n"
    "v_mfma_f32_16x16x32_bf16 v[220:223], v[136:139], v[160:163], v[220:223]\n"
    "v_mul_f32 v208, v208, v252\n"
    "v_mul_f32 v209, v209, v252\n"
    "v_mfma_f32_16x16x32_bf16 v[216:219], v[116:119], v[164:167], v[216:219]\n"
    "ds_read_b64_tr_b16 v[192:193], v2 offset:22528\n"
    "ds_read_b64_tr_b16 v[194:195], v3 offset:22528\n"
    "v_mfma_f32_16x16x32_bf16 v[220:223], v[140:143], v[164:167], v[220:223]\n"
    "v_mul_f32 v210, v210, v252\n"
    "v_mul_f32 v211, v211, v252\n"
    "v_mfma_f32_16x16x32_bf16 v[216:219], v[120:123], v[168:171], v[216:219]\n"
    "ds_read_b64_tr_b16 v[196:197], v2 offset:23552\n"
    "ds_read_b64_tr_b16 v[198:199], v3 offset:23552\n"
    "v_mfma_f32_16x16x32_bf16 v[220:223], v[144:147], v[168:171], v[220:223]\n"
    "v_mul_f32 v212, v212, v252\n"
    "v_mov_b32 v227, 4\n"
    "v_mfma_f32_16x16x32_bf16 v[216:219], v[124:127], v[172:175], v[216:219]\n"
    "ds_read_b32 v253, v1 offset:0\n"
    "v_mul_f32 v213, v213, v252\n"
    "v_mfma_f32_16x16x32_bf16 v[220:223], v[148:151], v[172:175], v[220:223]\n"
    "v_mul_f32 v214, v214, v252\n"
    "v_mul_f32 v215, v215, v252\n"
    "ds_write_b32 v226, v227 offset:32\n"
    "v_cvt_pk_bf16_f32 v208, v208, v209\n"
    "v_cvt_pk_bf16_f32 v209, v210, v211\n"
    "v_cvt_pk_bf16_f32 v210, v212, v213\n"
    "v_cvt_pk_bf16_f32 v211, v214, v215\n"
    "buffer_store_dwordx4 v[208:211], v4, s[80:83], 0 offen offset:64 sc1\n"
    ".Lh6j:\n"
    "s_waitcnt lgkmcnt(2)\n"
    "v_mfma_f32_16x16x32_bf16 v[200:203], v[8:11], v[176:179], 0\n"
    "v_mul_f32 v216, v216, v252\n"
    "v_mul_f32 v217, v217, v252\n"
    "v_mfma_f32_16x16x32_bf16 v[204:207], v[32:35], v[176:179], 0\n"
    "v_mul_f32 v218, v218, v252\n"
    "v_mul_f32 v219, v219, v252\n"
    "v_mfma_f32_16x16x32_bf16 v[200:203], v[12:15], v[180:183], v[200:203]\n"
    "v_mul_f32 v220, v220, v252\n"
    "v_mul_f32 v221, v221, v252\n"
    "v_mfma_f32_16x16x32_bf16 v[204:207], v[36:39], v[180:183], v[204:207]\n"
    "v_mul_f32 v222, v222, v252\n"
    "v_mul_f32 v223, v223, v252\n"
    "v_mfma_f32_16x16x32_bf16 v[200:203], v[16:19], v[184:187], v[200:203]\n"
    "v_cvt_pk_bf16_f32 v216, v216, v217\n"
    "v_cvt_pk_bf16_f32 v217, v218, v219\n"
    "v_mfma_f32_16x16x32_bf16 v[204:207], v[40:43], v[184:187], v[204:207]\n"
    "v_cvt_pk_bf16_f32 v218, v220, v221\n"
    "v_cvt_pk_bf16_f32 v219, v222, v223\n"
    "v_mfma_f32_16x16x32_bf16 v[200:203], v[20:23], v[188:191], v[200:203]\n"
    "buffer_store_dwordx4 v[216:219], v4, s[80:83], 0 offen offset:128 sc1\n"
    "v_mfma_f32_16x16x32_bf16 v[204:207], v[44:47], v[188:191], v[204:207]\n"
    "v_mfma_f32_16x16x32_bf16 v[200:203], v[24:27], v[192:195], v[200:203]\n"
    "v_add_u32 v4, s46, v4\n"
    "v_mfma_f32_16x16x32_bf16 v[204:207], v[48:51], v[192:195], v[204:207]\n"
    "v_mfma_f32_16x16x32_bf16 v[200:203], v[28:31], v[196:199], v[200:203]\n"
    "v_mfma_f32_16x16x32_bf16 v[204:207], v[52:55], v[196:199], v[204:207]\n"
    "v_mfma_f32_16x16x32_bf16 v[208:211], v[56:59], v[176:179], 0\n"
    "v_mfma_f32_16x16x32_bf16 v[212:215], v[80:83], v[176:179], 0\n"
    "ds_read_b32 v228, v224 offset:128\n"
    "v_mfma_f32_16x16x32_bf16 v[208:211], v[60:63], v[180:183], v[208:211]\n"
    "v_mfma_f32_16x16x32_bf16 v[212:215], v[84:87], v[180:183], v[212:215]\n"
    "v_mfma_f32_16x16x32_bf16 v[208:211], v[64:67], v[184:187], v[208:211]\n"
    "s_waitcnt lgkmcnt(2)\n"
    "v_mul_f32 v200, v200, v253\n"
    "v_mfma_f32_16x16x32_bf16 v[212:215], v[88:91], v[184:187], v[212:215]\n"
    "v_mul_f32 v201, v201, v253\n"
    "v_mul_f32 v202, v202, v253\n"
    "v_mfma_f32_16x16x32_bf16 v[208:211], v[68:71], v[188:191], v[208:211]\n"
    "v_mul_f32 v203, v203, v253\n"
    "v_mul_f32 v204, v204, v253\n"
    "v_mfma_f32_16x16x32_bf16 v[212:215], v[92:95], v[188:191], v[212:215]\n"
    "v_mul_f32 v205, v205, v253\n"
    "v_mul_f32 v206, v206, v253\n"
    "v_mfma_f32_16x16x32_bf16 v[208:211], v[72:75], v[192:195], v[208:211]\n"
    "v_mul_f32 v207, v207, v253\n"
    "v_cvt_pk_bf16_f32 v200, v200, v201\n"
    "v_mfma_f32_16x16x32_bf16 v[212:215], v[96:99], v[192:195], v[212:215]\n"
    "v_cvt_pk_bf16_f32 v201, v202, v203\n"
    "v_cvt_pk_bf16_f32 v202, v204, v205\n"
    "v_mfma_f32_16x16x32_bf16 v[208:211], v[76:79], v[196:199], v[208:211]\n"
    "s_waitcnt lgkmcnt(0)\n"
    "v_cmp_gt_u32 vcc, 2, v228\n"
    "v_mfma_f32_16x16x32_bf16 v[212:215], v[100:103], v[196:199], v[212:215]\n"
    "v_cvt_pk_bf16_f32 v203, v206, v207\n"
    "s_cbranch_vccnz .Lh7\n"
    "v_mfma_f32_16x16x32_bf16 v[216:219], v[104:107], v[176:179], 0\n"
    "ds_read_b64_tr_b16 v[152:153], v2 offset:24576\n"
    "ds_read_b64_tr_b16 v[154:155], v3 offset:24576\n"
    "v_mfma_f32_16x16x32_bf16 v[220:223], v[128:131], v[176:179], 0\n"
    "ds_read_b64_tr_b16 v[156:157], v2 offset:25600\n"
    "ds_read_b64_tr_b16 v[158:159], v3 offset:25600\n"
    "v_mfma_f32_16x16x32_bf16 v[216:219], v[108:111], v[180:183], v[216:219]\n"
    "ds_read_b64_tr_b16 v[160:161], v2 offset:26624\n"
    "ds_read_b64_tr_b16 v[162:163], v3 offset:26624\n"
    "v_mfma_f32_16x16x32_bf16 v[220:223], v[132:135], v[180:183], v[220:223]\n"
    "ds_read_b64_tr_b16 v[164:165], v2 offset:27648\n"
    "ds_read_b64_tr_b16 v[166:167], v3 offset:27648\n"
    "v_mfma_f32_16x16x32_bf16 v[216:219], v[112:115], v[184:187], v[216:219]\n"
    "buffer_store_dwordx4 v[200:203], v5, s[80:83], 0 offen offset:0 sc1\n"
    "v_mfma_f32_16x16x32_bf16 v[220:223], v[136:139], v[184:187], v[220:223]\n"
    "v_mul_f32 v208, v208, v253\n"
    "v_mul_f32 v209, v209, v253\n"
    "v_mfma_f32_16x16x32_bf16 v[216:219], v[116:119], v[188:191], v[216:219]\n"
    "ds_read_b64_tr_b16 v[168:169], v2 offset:28672\n"
    "ds_read_b64_tr_b16 v[170:171], v3 offset:28672\n"
    "v_mfma_f32_16x16x32_bf16 v[220:223], v[140:143], v[188:191], v[220:223]\n"
    "v_mul_f32 v210, v210, v253\n"
    "v_mul_f32 v211, v211, v253\n"
    "v_mfma_f32_16x16x32_bf16 v[216:219], v[120:123], v[192:195], v[216:219]\n"
    "ds_read_b64_tr_b16 v[172:173], v2 offset:29696\n"
    "ds_read_b64_tr_b16 v[174:175], v3 offset:29696\n"
    "v_mfma_f32_16x16x32_bf16 v[220:223], v[144:147], v[192:195], v[220:223]\n"
    "v_mul_f32 v212, v212, v253\n"
    "v_mov_b32 v227, 5\n"
    "v_mfma_f32_16x16x32_bf16 v[216:219], v[124:127], v[196:199], v[216:219]\n"
    "ds_read_b32 v252, v1 offset:64\n"
    "v_mul_f32 v213, v213, v253\n"
    "v_mfma_f32_16x16x32_bf16 v[220:223], v[148:151], v[196:199], v[220:223]\n"
    "v_mul_f32 v214, v214, v253\n"
    "v_mul_f32 v215, v215, v253\n"
    "ds_write_b32 v226, v227 offset:32\n"
    "v_cvt_pk_bf16_f32 v208, v208, v209\n"
    "v_cvt_pk_bf16_f32 v209, v210, v211\n"
    "v_cvt_pk_bf16_f32 v210, v212, v213\n"
    "v_cvt_pk_bf16_f32 v211, v214, v215\n"
    "buffer_store_dwordx4 v[208:211], v5, s[80:83], 0 offen offset:64 sc1\n"
    ".Lh7j:\n"
    "s_waitcnt lgkmcnt(2)\n"
    "v_mfma_f32_16x16x32_bf16 v[200:203], v[8:11], v[152:155], 0\n"
    "v_mul_f32 v216, v216, v253\n"
    "v_mul_f32 v217, v217, v253\n"
    "v_mfma_f32_16x16x32_bf16 v[204:207], v[32:35], v[152:155], 0\n"
    "v_mul_f32 v218, v218, v253\n"
    "v_mul_f32 v219, v219, v253\n"
    "v_mfma_f32_16x16x32_bf16 v[200:203], v[12:15], v[156:159], v[200:203]\n"
    "v_mul_f32 v220, v220, v253\n"
    "v_mul_f32 v221, v221, v253\n"
    "v_mfma_f32_16x16x32_bf16 v[204:207], v[36:39], v[156:159], v[204:207]\n"
    "v_mul_f32 v222, v222, v253\n"
    "v_mul_f32 v223, v223, v253\n"
    "v_mfma_f32_16x16x32_bf16 v[200:203], v[16:19], v[160:163], v[200:203]\n"
    "v_cvt_pk_bf16_f32 v216, v216, v217\n"
    "v_cvt_pk_bf16_f32 v217, v218, v219\n"
    "v_mfma_f32_16x16x32_bf16 v[204:207], v[40:43], v[160:163], v[204:207]\n"
    "v_cvt_pk_bf16_f32 v218, v220, v221\n"
    "v_cvt_pk_bf16_f32 v219, v222, v223\n"
    "v_mfma_f32_16x16x32_bf16 v[200:203], v[20:23], v[164:167], v[200:203]\n"
    "buffer_store_dwordx4 v[216:219], v5, s[80:83], 0 offen offset:128 sc1\n"
    "v_mfma_f32_16x16x32_bf16 v[204:207], v[44:47], v[164:167], v[204:207]\n"
    "v_mfma_f32_16x16x32_bf16 v[200:203], v[24:27], v[168:171], v[200:203]\n"
    "v_add_u32 v5, s46, v5\n"
    "v_mfma_f32_16x16x32_bf16 v[204:207], v[48:51], v[168:171], v[204:207]\n"
    "v_mfma_f32_16x16x32_bf16 v[200:203], v[28:31], v[172:175], v[200:203]\n"
    "v_mfma_f32_16x16x32_bf16 v[204:207], v[52:55], v[172:175], v[204:207]\n"
    "v_mfma_f32_16x16x32_bf16 v[208:211], v[56:59], v[152:155], 0\n"
    "v_mfma_f32_16x16x32_bf16 v[212:215], v[80:83], v[152:155], 0\n"
    "ds_read_b32 v228, v224 offset:144\n"
    "v_mfma_f32_16x16x32_bf16 v[208:211], v[60:63], v[156:159], v[208:211]\n"
    "v_mfma_f32_16x16x32_bf16 v[212:215], v[84:87], v[156:159], v[212:215]\n"
    "v_mfma_f32_16x16x32_bf16 v[208:211], v[64:67], v[160:163], v[208:211]\n"
    "s_waitcnt lgkmcnt(2)\n"
    "v_mul_f32 v200, v200, v252\n"
    "v_mfma_f32_16x16x32_bf16 v[212:215], v[88:91], v[160:163], v[212:215]\n"
    "v_mul_f32 v201, v201, v252\n"
    "v_mul_f32 v202, v202, v252\n"
    "v_mfma_f32_16x16x32_bf16 v[208:211], v[68:71], v[164:167], v[208:211]\n"
    "v_mul_f32 v203, v203, v252\n"
    "v_mul_f32 v204, v204, v252\n"
    "v_mfma_f32_16x16x32_bf16 v[212:215], v[92:95], v[164:167], v[212:215]\n"
    "v_mul_f32 v205, v205, v252\n"
    "v_mul_f32 v206, v206, v252\n"
    "v_mfma_f32_16x16x32_bf16 v[208:211], v[72:75], v[168:171], v[208:211]\n"
    "v_mul_f32 v207, v207, v252\n"
    "v_cvt_pk_bf16_f32 v200, v200, v201\n"
    "v_mfma_f32_16x16x32_bf16 v[212:215], v[96:99], v[168:171], v[212:215]\n"
    "v_cvt_pk_bf16_f32 v201, v202, v203\n"
    "v_cvt_pk_bf16_f32 v202, v204, v205\n"
    "v_mfma_f32_16x16x32_bf16 v[208:211], v[76:79], v[172:175], v[208:211]\n"
    "s_waitcnt lgkmcnt(0)\n"
    "v_cmp_gt_u32 vcc, 2, v228\n"
    "v_mfma_f32_16x16x32_bf16 v[212:215], v[100:103], v[172:175], v[212:215]\n"
    "v_cvt_pk_bf16_f32 v203, v206, v207\n"
    "s_cbranch_vccnz .Lh8\n"
    "v_mfma_f32_16x16x32_bf16 v[216:219], v[104:107], v[152:155], 0\n"
    "ds_read_b64_tr_b16 v[176:177], v2 offset:30720\n"
    "ds_read_b64_tr_b16 v[178:179], v3 offset:30720\n"
    "v_mfma_f32_16x16x32_bf16 v[220:223], v[128:131], v[152:155], 0\n"
    "ds_read_b64_tr_b16 v[180:181], v2 offset:31744\n"
    "ds_read_b64_tr_b16 v[182:183], v3 offset:31744\n"
    "v_mfma_f32_16x16x32_bf16 v[216:219], v[108:111], v[156:159], v[216:219]\n"
    "ds_read_b64_tr_b16 v[184:185], v2 offset:32768\n"
    "ds_read_b64_tr_b16 v[186:187], v3 offset:32768\n"
    "v_mfma_f32_16x16x32_bf16 v[220:223], v[132:135], v[156:159], v[220:223]\n"
    "ds_read_b64_tr_b16 v[188:189], v2 offset:33792\n"
    "ds_read_b64_tr_b16 v[190:191], v3 offset:33792\n"
    "v_mfma_f32_16x16x32_bf16 v[216:219], v[112:115], v[160:163], v[216:219]\n"
    "buffer_store_dwordx4 v[200:203], v4, s[80:83], 0 offen offset:0 sc1\n"
    "v_mfma_f32_16x16x32_bf16 v[220:223], v[136:139], v[160:163], v[220:223]\n"
    "v_mul_f32 v208, v208, v252\n"
    "v_mul_f32 v209, v209, v252\n"
    "v_mfma_f32_16x16x32_bf16 v[216:219], v[116:119], v[164:167], v[216:219]\n"
    "ds_read_b64_tr_b16 v[192:193], v2 offset:34816\n"
    "ds_read_b64_tr_b16 v[194:195], v3 offset:34816\n"
    "v_mfma_f32_16x16x32_bf16 v[220:223], v[140:143], v[164:167], v[220:223]\n"
    "v_mul_f32 v210, v210, v252\n"
    "v_mul_f32 v211, v211, v252\n"
    "v_mfma_f32_16x16x32_bf16 v[216:219], v[120:123], v[168:171], v[216:219]\n"
    "ds_read_b64_tr_b16 v[196:197], v2 offset:35840\n"
    "ds_read_b64_tr_b16 v[198:199], v3 offset:35840\n"
    "v_mfma_f32_16x16x32_bf16 v[220:223], v[144:147], v[168:171], v[220:223]\n"
    "v_mul_f32 v212, v212, v252\n"
    "v_mov_b32 v227, 6\n"
    "v_mfma_f32_16x16x32_bf16 v[216:219], v[124:127], v[172:175], v[216:219]\n"
    "ds_read_b32 v253, v1 offset:128\n"
    "v_mul_f32 v213, v213, v252\n"
    "v_mfma_f32_16x16x32_bf16 v[220:223], v[148:151], v[172:175], v[220:223]\n"
    "v_mul_f32 v214, v214, v252\n"
    "v_mul_f32 v215, v215, v252\n"
    "ds_write_b32 v226, v227 offset:32\n"
    "v_cvt_pk_bf16_f32 v208, v208, v209\n"
    "v_cvt_pk_bf16_f32 v209, v210, v211\n"
    "v_cvt_pk_bf16_f32 v210, v212, v213\n"
    "v_cvt_pk_bf16_f32 v211, v214, v215\n"
    "buffer_store_dwordx4 v[208:211], v4, s[80:83], 0 offen offset:64 sc1\n"
    ".Lh8j:\n"
    "s_waitcnt lgkmcnt(2)\n"
    "v_mfma_f32_16x16x32_bf16 v[200:203], v[8:11], v[176:179], 0\n"
    "v_mul_f32 v216, v216, v252\n"
    "v_mul_f32 v217, v217, v252\n"
    "v_mfma_f32_16x16x32_bf16 v[204:207], v[32:35], v[176:179], 0\n"
    "v_mul_f32 v218, v218, v252\n"
    "v_mul_f32 v219, v219, v252\n"
    "v_mfma_f32_16x16x32_bf16 v[200:203], v[12:15], v[180:183], v[200:203]\n"
    "v_mul_f32 v220, v220, v252\n"
    "v_mul_f32 v221, v221, v252\n"
    "v_mfma_f32_16x16x32_bf16 v[204:207], v[36:39], v[180:183], v[204:207]\n"
    "v_mul_f32 v222, v222, v252\n"
    "v_mul_f32 v223, v223, v252\n"
    "v_mfma_f32_16x16x32_bf16 v[200:203], v[16:19], v[184:187], v[200:203]\n"
    "v_cvt_pk_bf16_f32 v216, v216, v217\n"
    "v_cvt_pk_bf16_f32 v217, v218, v219\n"
    "v_mfma_f32_16x16x32_bf16 v[204:207], v[40:43], v[184:187], v[204:207]\n"
    "v_cvt_pk_bf16_f32 v218, v220, v221\n"
    "v_cvt_pk_bf16_f32 v219, v222, v223\n"
    "v_mfma_f32_16x16x32_bf16 v[200:203], v[20:23], v[188:191], v[200:203]\n"
    "buffer_store_dwordx4 v[216:219], v4, s[80:83], 0 offen offset:128 sc1\n"
    "v_mfma_f32_16x16x32_bf16 v[204:207], v[44:47], v[188:191], v[204:207]\n"
    "v_mfma_f32_16x16x32_bf16 v[200:203], v[24:27], v[192:195], v[200:203]\n"
    "v_add_u32 v4, s46, v4\n"
    "v_mfma_f32_16x16x32_bf16 v[204:207], v[48:51], v[192:195], v[204:207]\n"
    "v_mfma_f32_16x16x32_bf16 v[200:203], v[28:31], v[196:199], v[200:203]\n"
    "v_mfma_f32_16x16x32_bf16 v[204:207], v[52:55], v[196:199], v[204:207]\n"
    "v_mfma_f32_16x16x32_bf16 v[208:211], v[56:59], v[176:179], 0\n"
    "v_mfma_f32_16x16x32_bf16 v[212:215], v[80:83], v[176:179], 0\n"
    "ds_read_b32 v228, v224 offset:160\n"
    "v_mfma_f32_16x16x32_bf16 v[208:211], v[60:63], v[180:183], v[208:211]\n"
    "v_mfma_f32_16x16x32_bf16 v[212:215], v[84:87], v[180:183], v[212:215]\n"
    "v_mfma_f32_16x16x32_bf16 v[208:211], v[64:67], v[184:187], v[208:211]\n"
    "s_waitcnt lgkmcnt(2)\n"
    "v_mul_f32 v200, v200, v253\n"
    "v_mfma_f32_16x16x32_bf16 v[212:215], v[88:91], v[184:187], v[212:215]\n"
    "v_mul_f32 v201, v201, v253\n"
    "v_mul_f32 v202, v202, v253\n"
    "v_mfma_f32_16x16x32_bf16 v[208:211], v[68:71], v[188:191], v[208:211]\n"
    "v_mul_f32 v203, v203, v253\n"
    "v_mul_f32 v204, v204, v253\n"
    "v_mfma_f32_16x16x32_bf16 v[212:215], v[92:95], v[188:191], v[212:215]\n"
    "v_mul_f32 v205, v205, v253\n"
    "v_mul_f32 v206, v206, v253\n"
    "v_mfma_f32_16x16x32_bf16 v[208:211], v[72:75], v[192:195], v[208:211]\n"
    "v_mul_f32 v207, v207, v253\n"
    "v_cvt_pk_bf16_f32 v200, v200, v201\n"
    "v_mfma_f32_16x16x32_bf16 v[212:215], v[96:99], v[192:195], v[212:215]\n"
    "v_cvt_pk_bf16_f32 v201, v202, v203\n"
    "v_cvt_pk_bf16_f32 v202, v204, v205\n"
    "v_mfma_f32_16x16x32_bf16 v[208:211], v[76:79], v[196:199], v[208:211]\n"
    "s_waitcnt lgkmcnt(0)\n"
    "v_cmp_gt_u32 vcc, 2, v228\n"
    "v_mfma_f32_16x16x32_bf16 v[212:215], v[100:103], v[196:199], v[212:215]\n"
    "v_cvt_pk_bf16_f32 v203, v206, v207\n"
    "s_cbranch_vccnz .Lh9\n"
    "v_mfma_f32_16x16x32_bf16 v[216:219], v[104:107], v[176:179], 0\n"
    "ds_read_b64_tr_b16 v[152:153], v2 offset:18432\n"
    "ds_read_b64_tr_b16 v[154:155], v3 offset:18432\n"
    "v_mfma_f32_16x16x32_bf16 v[220:223], v[128:131], v[176:179], 0\n"
    "ds_read_b64_tr_b16 v[156:157], v2 offset:19456\n"
    "ds_read_b64_tr_b16 v[158:159], v3 offset:19456\n"
    "v_mfma_f32_16x16x32_bf16 v[216:219], v[108:111], v[180:183], v[216:219]\n"
    "ds_read_b64_tr_b16 v[160:161], v2 offset:20480\n"
    "ds_read_b64_tr_b16 v[162:163], v3 offset:20480\n"
    "v_mfma_f32_16x16x32_bf16 v[220:223], v[132:135], v[180:183], v[220:223]\n"
    "ds_read_b64_tr_b16 v[164:165], v2 offset:21504\n"
    "ds_read_b64_tr_b16 v[166:167], v3 offset:21504\n"
    "v_mfma_f32_16x16x32_bf16 v[216:219], v[112:115], v[184:187], v[216:219]\n"
    "buffer_store_dwordx4 v[200:203], v5, s[80:83], 0 offen offset:0 sc1\n"
    "v_mfma_f32_16x16x32_bf16 v[220:223], v[136:139], v[184:187], v[220:223]\n"
    "v_mul_f32 v208, v208, v253\n"
    "v_mul_f32 v209, v209, v253\n"
    "v_mfma_f32_16x16x32_bf16 v[216:219], v[116:119], v[188:191], v[216:219]\n"
    "ds_read_b64_tr_b16 v[168:169], v2 offset:22528\n"
    "ds_read_b64_tr_b16 v[170:171], v3 offset:22528\n"
    "v_mfma_f32_16x16x32_bf16 v[220:223], v[140:143], v[188:191], v[220:223]\n"
    "v_mul_f32 v210, v210, v253\n"
    "v_mul_f32 v211, v211, v253\n"
    "v_mfma_f32_16x16x32_bf16 v[216:219], v[120:123], v[192:195], v[216:219]\n"
    "ds_read_b64_tr_b16 v[172:173], v2 offset:23552\n"
    "ds_read_b64_tr_b16 v[174:175], v3 offset:23552\n"
    "v_mfma_f32_16x16x32_bf16 v[220:223], v[144:147], v[192:195], v[220:223]\n"
    "v_mul_f32 v212, v212, v253\n"
    "v_mov_b32 v227, 7\n"
    "v_mfma_f32_16x16x32_bf16 v[216:219], v[124:127], v[196:199], v[216:219]\n"
    "ds_read_b32 v252, v1 offset:0\n"
    "v_mul_f32 v213, v213, v253\n"
    "v_mfma_f32_16x16x32_bf16 v[220:223], v[148:151], v[196:199], v[220:223]\n"
    "v_mul_f32 v214, v214, v253\n"
    "v_mul_f32 v215, v215, v253\n"
    "ds_write_b32 v226, v227 offset:32\n"
    "v_cvt_pk_bf16_f32 v208, v208, v209\n"
    "v_cvt_pk_bf16_f32 v209, v210, v211\n"
    "v_cvt_pk_bf16_f32 v210, v212, v213\n"
    "v_cvt_pk_bf16_f32 v211, v214, v215\n"
    "buffer_store_dwordx4 v[208:211], v5, s[80:83], 0 offen offset:64 sc1\n"
    ".Lh9j:\n"
    "s_waitcnt lgkmcnt(2)\n"
    "v_mfma_f32_16x16x32_bf16 v[200:203], v[8:11], v[152:155], 0\n"
    "v_mul_f32 v216, v216, v253\n"
    "v_mul_f32 v217, v217, v253\n"
    "v_mfma_f32_16x16x32_bf16 v[204:207], v[32:35], v[152:155], 0\n"
    "v_mul_f32 v218, v218, v253\n"
    "v_mul_f32 v219, v219, v253\n"
    "v_mfma_f32_16x16x32_bf16 v[200:203], v[12:15], v[156:159], v[200:203]\n"
    "v_mul_f32 v220, v220, v253\n"
    "v_mul_f32 v221, v221, v253\n"
    "v_mfma_f32_16x16x32_bf16 v[204:207], v[36:39], v[156:159], v[204:207]\n"
    "v_mul_f32 v222, v222, v253\n"
    "v_mul_f32 v223, v223, v253\n"
    "v_mfma_f32_16x16x32_bf16 v[200:203], v[16:19], v[160:163], v[200:203]\n"
    "v_cvt_pk_bf16_f32 v216, v216, v217\n"
    "v_cvt_pk_bf16_f32 v217, v218, v219\n"
    "v_mfma_f32_16x16x32_bf16 v[204:207], v[40:43], v[160:163], v[204:207]\n"
    "v_cvt_pk_bf16_f32 v218, v220, v221\n"
    "v_cvt_pk_bf16_f32 v219, v222, v223\n"
    "v_mfma_f32_16x16x32_bf16 v[200:203], v[20:23], v[164:167], v[200:203]\n"
    "buffer_store_dwordx4 v[216:219], v5, s[80:83], 0 offen offset:128 sc1\n"
    "v_mfma_f32_16x16x32_bf16 v[204:207], v[44:47], v[164:167], v[204:207]\n"
    "v_mfma_f32_16x16x32_bf16 v[200:203], v[24:27], v[168:171], v[200:203]\n"
    "v_add_u32 v5, s46, v5\n"
    "v_mfma_f32_16x16x32_bf16 v[204:207], v[48:51], v[168:171], v[204:207]\n"
    "v_mfma_f32_16x16x32_bf16 v[200:203], v[28:31], v[172:175], v[200:203]\n"
    "v_mfma_f32_16x16x32_bf16 v[204:207], v[52:55], v[172:175], v[204:207]\n"
    "v_mfma_f32_16x16x32_bf16 v[208:211], v[56:59], v[152:155], 0\n"
    "v_mfma_f32_16x16x32_bf16 v[212:215], v[80:83], v[152:155], 0\n"
    "ds_read_b32 v228, v224 offset:176\n"
    "v_mfma_f32_16x16x32_bf16 v[208:211], v[60:63], v[156:159], v[208:211]\n"
    "v_mfma_f32_16x16x32_bf16 v[212:215], v[84:87], v[156:159], v[212:215]\n"
    "v_mfma_f32_16x16x32_bf16 v[208:211], v[64:67], v[160:163], v[208:211]\n"
    "s_waitcnt lgkmcnt(2)\n"
    "v_mul_f32 v200, v200, v252\n"
    "v_mfma_f32_16x16x32_bf16 v[212:215], v[88:91], v[160:163], v[212:215]\n"
    "v_mul_f32 v201, v201, v252\n"
    "v_mul_f32 v202, v202, v252\n"
    "v_mfma_f32_16x16x32_bf16 v[208:211], v[68:71], v[164:167], v[208:211]\n"
    "v_mul_f32 v203, v203, v252\n"
    "v_mul_f32 v204, v204, v252\n"
    "v_mfma_f32_16x16x32_bf16 v[212:215], v[92:95], v[164:167], v[212:215]\n"
    "v_mul_f32 v205, v205, v252\n"
    "v_mul_f32 v206, v206, v252\n"
    "v_mfma_f32_16x16x32_bf16 v[208:211], v[72:75], v[168:171], v[208:211]\n"
    "v_mul_f32 v207, v207, v252\n"
    "v_cvt_pk_bf16_f32 v200, v200, v201\n"
    "v_mfma_f32_16x16x32_bf16 v[212:215], v[96:99], v[168:171], v[212:215]\n"
    "v_cvt_pk_bf16_f32 v201, v202, v203\n"
    "v_cvt_pk_bf16_f32 v202, v204, v205\n"
    "v_mfma_f32_16x16x32_bf16 v[208:211], v[76:79], v[172:175], v[208:211]\n"
    "s_waitcnt lgkmcnt(0)\n"
    "v_cmp_gt_u32 vcc, 2, v228\n"
    "v_mfma_f32_16x16x32_bf16 v[212:215], v[100:103], v[172:175], v[212:215]\n"
    "v_cvt_pk_bf16_f32 v203, v206, v207\n"
    "s_cbranch_vccnz .Lh10\n"
    "v_mfma_f32_16x16x32_bf16 v[216:219], v[104:107], v[152:155], 0\n"
    "ds_read_b64_tr_b16 v[176:177], v2 offset:24576\n"
    "ds_read_b64_tr_b16 v[178:179], v3 offset:24576\n"
    "v_mfma_f32_16x16x32_bf16 v[220:223], v[128:131], v[152:155], 0\n"
    "ds_read_b64_tr_b16 v[180:181], v2 offset:25600\n"
    "ds_read_b64_tr_b16 v[182:183], v3 offset:25600\n"
    "v_mfma_f32_16x16x32_bf16 v[216:219], v[108:111], v[156:159], v[216:219]\n"
    "ds_read_b64_tr_b16 v[184:185], v2 offset:26624\n"
    "ds_read_b64_tr_b16 v[186:187], v3 offset:26624\n"
    "v_mfma_f32_16x16x32_bf16 v[220:223], v[132:135], v[156:159], v[220:223]\n"
    "ds_read_b64_tr_b16 v[188:189], v2 offset:27648\n"
    "ds_read_b64_tr_b16 v[190:191], v3 offset:27648\n"
    "v_mfma_f32_16x16x32_bf16 v[216:219], v[112:115], v[160:163], v[216:219]\n"
    "buffer_store_dwordx4 v[200:203], v4, s[80:83], 0 offen offset:0 sc1\n"
    "v_mfma_f32_16x16x32_bf16 v[220:223], v[136:139], v[160:163], v[220:223]\n"
    "v_mul_f32 v208, v208, v252\n"
    "v_mul_f32 v209, v209, v252\n"
    "v_mfma_f32_16x16x32_bf16 v[216:219], v[116:119], v[164:167], v[216:219]\n"
    "ds_read_b64_tr_b16 v[192:193], v2 offset:28672\n"
    "ds_read_b64_tr_b16 v[194:195], v3 offset:28672\n"
    "v_mfma_f32_16x16x32_bf16 v[220:223], v[140:143], v[164:167], v[220:223]\n"
    "v_mul_f32 v210, v210, v252\n"
    "v_mul_f32 v211, v211, v252\n"
    "v_mfma_f32_16x16x32_bf16 v[216:219], v[120:123], v[168:171], v[216:219]\n"
    "ds_read_b64_tr_b16 v[196:197], v2 offset:29696\n"
    "ds_read_b64_tr_b16 v[198:199], v3 offset:29696\n"
    "v_mfma_f32_16x16x32_bf16 v[220:223], v[144:147], v[168:171], v[220:223]\n"
    "v_mul_f32 v212, v212, v252\n"
    "v_mul_f32 v213, v213, v252\n"
    "v_mfma_f32_16x16x32_bf16 v[216:219], v[124:127], v[172:175], v[216:219]\n"
    "ds_read_b32 v253, v1 offset:64\n"
    "v_mul_f32 v214, v214, v252\n"
    "v_mfma_f32_16x16x32_bf16 v[220:223], v[148:151], v[172:175], v[220:223]\n"
    "v_mul_f32 v215, v215, v252\n"
    "v_cvt_pk_bf16_f32 v208, v208, v209\n"
    "v_cvt_pk_bf16_f32 v209, v210, v211\n"
    "v_cvt_pk_bf16_f32 v210, v212, v213\n"
    "v_cvt_pk_bf16_f32 v211, v214, v215\n"
    "buffer_store_dwordx4 v[208:211], v4, s[80:83], 0 offen offset:64 sc1\n"
    ".Lh10j:\n"
    "s_waitcnt lgkmcnt(1)\n"
    "v_mfma_f32_16x16x32_bf16 v[200:203], v[8:11], v[176:179], 0\n"
    "v_mul_f32 v216, v216, v252\n"
    "v_mul_f32 v217, v217, v252\n"
    "v_mfma_f32_16x16x32_bf16 v[204:207], v[32:35], v[176:179], 0\n"
    "v_mul_f32 v218, v218, v252\n"
    "v_mul_f32 v219, v219, v252\n"
    "v_mfma_f32_16x16x32_bf16 v[200:203], v[12:15], v[180:183], v[200:203]\n"
    "v_mul_f32 v220, v220, v252\n"
    "v_mul_f32 v221, v221, v252\n"
    "v_mfma_f32_16x16x32_bf16 v[204:207], v[36:39], v[180:183], v[204:207]\n"
    "v_mul_f32 v222, v222, v252\n"
    "v_mul_f32 v223, v223, v252\n"
    "v_mfma_f32_16x16x32_bf16 v[200:203], v[16:19], v[184:187], v[200:203]\n"
    "v_cvt_pk_bf16_f32 v216, v216, v217\n"
    "v_cvt_pk_bf16_f32 v217, v218, v219\n"
    "v_mfma_f32_16x16x32_bf16 v[204:207], v[40:43], v[184:187], v[204:207]\n"
    "v_cvt_pk_bf16_f32 v218, v220, v221\n"
    "v_cvt_pk_bf16_f32 v219, v222, v223\n"
    "v_mfma_f32_16x16x32_bf16 v[200:203], v[20:23], v[188:191], v[200:203]\n"
    "buffer_store_dwordx4 v[216:219], v4, s[80:83], 0 offen offset:128 sc1\n"
    "v_mfma_f32_16x16x32_bf16 v[204:207], v[44:47], v[188:191], v[204:207]\n"
    "v_mfma_f32_16x16x32_bf16 v[200:203], v[24:27], v[192:195], v[200:203]\n"
    "v_add_u32 v4, v6, v4\n"
    "v_mfma_f32_16x16x32_bf16 v[204:207], v[48:51], v[192:195], v[204:207]\n"
    "v_mfma_f32_16x16x32_bf16 v[200:203], v[28:31], v[196:199], v[200:203]\n"
    "v_mfma_f32_16x16x32_bf16 v[204:207], v[52:55], v[196:199], v[204:207]\n"
    "v_mfma_f32_16x16x32_bf16 v[208:211], v[56:59], v[176:179], 0\n"
    "v_mfma_f32_16x16x32_bf16 v[212:215], v[80:83], v[176:179], 0\n"
    "ds_read_b32 v228, v224 offset:128\n"
    "v_mfma_f32_16x16x32_bf16 v[208:211], v[60:63], v[180:183], v[208:211]\n"
    "v_mfma_f32_16x16x32_bf16 v[212:215], v[84:87], v[180:183], v[212:215]\n"
    "v_mfma_f32_16x16x32_bf16 v[208:211], v[64:67], v[184:187], v[208:211]\n"
    "s_waitcnt lgkmcnt(1)\n"
    "v_mul_f32 v200, v200, v253\n"
    "v_mfma_f32_16x16x32_bf16 v[212:215], v[88:91], v[184:187], v[212:215]\n"
    "v_mul_f32 v201, v201, v253\n"
    "v_mul_f32 v202, v202, v253\n"
    "v_mfma_f32_16x16x32_bf16 v[208:211], v[68:71], v[188:191], v[208:211]\n"
    "v_mul_f32 v203, v203, v253\n"
    "v_mul_f32 v204, v204, v253\n"
    "v_mfma_f32_16x16x32_bf16 v[212:215], v[92:95], v[188:191], v[212:215]\n"
    "v_mul_f32 v205, v205, v253\n"
    "v_mul_f32 v206, v206, v253\n"
    "v_mfma_f32_16x16x32_bf16 v[208:211], v[72:75], v[192:195], v[208:211]\n"
    "v_mul_f32 v207, v207, v253\n"
    "v_cvt_pk_bf16_f32 v200, v200, v201\n"
    "v_mfma_f32_16x16x32_bf16 v[212:215], v[96:99], v[192:195], v[212:215]\n"
    "v_cvt_pk_bf16_f32 v201, v202, v203\n"
    "v_cvt_pk_bf16_f32 v202, v204, v205\n"
    "v_mfma_f32_16x16x32_bf16 v[208:211], v[76:79], v[196:199], v[208:211]\n"
    "s_waitcnt lgkmcnt(0)\n"
    "v_cmp_gt_u32 vcc, 3, v228\n"
    "v_mfma_f32_16x16x32_bf16 v[212:215], v[100:103], v[196:199], v[212:215]\n"
    "v_cvt_pk_bf16_f32 v203, v206, v207\n"
    "s_cbranch_vccnz .Lh11\n"
    "v_mfma_f32_16x16x32_bf16 v[216:219], v[104:107], v[176:179], 0\n"
    "ds_read_b64_tr_b16 v[152:153], v2 offset:30720\n"
    "ds_read_b64_tr_b16 v[154:155], v3 offset:30720\n"
    "v_mfma_f32_16x16x32_bf16 v[220:223], v[128:131], v[176:179], 0\n"
    "ds_read_b64_tr_b16 v[156:157], v2 offset:31744\n"
    "ds_read_b64_tr_b16 v[158:159], v3 offset:31744\n"
    "v_mfma_f32_16x16x32_bf16 v[216:219], v[108:111], v[180:183], v[216:219]\n"
    "ds_read_b64_tr_b16 v[160:161], v2 offset:32768\n"
    "ds_read_b64_tr_b16 v[162:163], v3 offset:32768\n"
    "v_mfma_f32_16x16x32_bf16 v[220:223], v[132:135], v[180:183], v[220:223]\n"
    "ds_read_b64_tr_b16 v[164:165], v2 offset:33792\n"
    "ds_read_b64_tr_b16 v[166:167], v3 offset:33792\n"
    "v_mfma_f32_16x16x32_bf16 v[216:219], v[112:115], v[184:187], v[216:219]\n"
    "buffer_store_dwordx4 v[200:203], v5, s[80:83], 0 offen offset:0 sc1\n"
    "v_mfma_f32_16x16x32_bf16 v[220:223], v[136:139], v[184:187], v[220:223]\n"
    "v_mul_f32 v208, v208, v253\n"
    "v_mul_f32 v209, v209, v253\n"
    "v_mfma_f32_16x16x32_bf16 v[216:219], v[116:119], v[188:191], v[216:219]\n"
    "ds_read_b64_tr_b16 v[168:169], v2 offset:34816\n"
    "ds_read_b64_tr_b16 v[170:171], v3 offset:34816\n"
    "v_mfma_f32_16x16x32_bf16 v[220:223], v[140:143], v[188:191], v[220:223]\n"
    "v_mul_f32 v210, v210, v253\n"
    "v_mul_f32 v211, v211, v253\n"
    "v_mfma_f32_16x16x32_bf16 v[216:219], v[120:123], v[192:195], v[216:219]\n"
    "ds_read_b64_tr_b16 v[172:173], v2 offset:35840\n"
    "ds_read_b64_tr_b16 v[174:175], v3 offset:35840\n"
    "v_mfma_f32_16x16x32_bf16 v[220:223], v[144:147], v[192:195], v[220:223]\n"
    "v_mul_f32 v212, v212, v253\n"
    "v_mul_f32 v213, v213, v253\n"
    "v_mfma_f32_16x16x32_bf16 v[216:219], v[124:127], v[196:199], v[216:219]\n"
    "ds_read_b32 v252, v1 offset:128\n"
    "v_mul_f32 v214, v214, v253\n"
    "v_mfma_f32_16x16x32_bf16 v[220:223], v[148:151], v[196:199], v[220:223]\n"
    "v_mul_f32 v215, v215, v253\n"
    "v_cvt_pk_bf16_f32 v208, v208, v209\n"
    "v_cvt_pk_bf16_f32 v209, v210, v211\n"
    "v_cvt_pk_bf16_f32 v210, v212, v213\n"
    "v_cvt_pk_bf16_f32 v211, v214, v215\n"
    "buffer_store_dwordx4 v[208:211], v5, s[80:83], 0 offen offset:64 sc1\n"
    ".Lh11j:\n"
    "s_waitcnt lgkmcnt(1)\n"
    "v_mfma_f32_16x16x32_bf16 v[200:203], v[8:11], v[152:155], 0\n"
    "v_mul_f32 v216, v216, v253\n"
    "v_mul_f32 v217, v217, v253\n"
    "v_mfma_f32_16x16x32_bf16 v[204:207], v[32:35], v[152:155], 0\n"
    "v_mul_f32 v218, v218, v253\n"
    "v_mul_f32 v219, v219, v253\n"
    "v_mfma_f32_16x16x32_bf16 v[200:203], v[12:15], v[156:159], v[200:203]\n"
    "v_mul_f32 v220, v220, v253\n"
    "v_mul_f32 v221, v221, v253\n"
    "v_mfma_f32_16x16x32_bf16 v[204:207], v[36:39], v[156:159], v[204:207]\n"
    "v_mul_f32 v222, v222, v253\n"
    "v_mul_f32 v223, v223, v253\n"
    "v_mfma_f32_16x16x32_bf16 v[200:203], v[16:19], v[160:163], v[200:203]\n"
    "v_cvt_pk_bf16_f32 v216, v216, v217\n"
    "v_cvt_pk_bf16_f32 v217, v218, v219\n"
    "v_mfma_f32_16x16x32_bf16 v[204:207], v[40:43], v[160:163], v[204:207]\n"
    "v_cvt_pk_bf16_f32 v218, v220, v221\n"
    "v_cvt_pk_bf16_f32 v219, v222, v223\n"
    "v_mfma_f32_16x16x32_bf16 v[200:203], v[20:23], v[164:167], v[200:203]\n"
    "buffer_store_dwordx4 v[216:219], v5, s[80:83], 0 offen offset:128 sc1\n"
    "v_mfma_f32_16x16x32_bf16 v[204:207], v[44:47], v[164:167], v[204:207]\n"
    "v_mfma_f32_16x16x32_bf16 v[200:203], v[24:27], v[168:171], v[200:203]\n"
    "v_add_u32 v5, v6, v5\n"
    "v_mfma_f32_16x16x32_bf16 v[204:207], v[48:51], v[168:171], v[204:207]\n"
    "v_mfma_f32_16x16x32_bf16 v[200:203], v[28:31], v[172:175], v[200:203]\n"
    "v_mfma_f32_16x16x32_bf16 v[204:207], v[52:55], v[172:175], v[204:207]\n"
    "v_mfma_f32_16x16x32_bf16 v[208:211], v[56:59], v[152:155], 0\n"
    "v_mfma_f32_16x16x32_bf16 v[212:215], v[80:83], v[152:155], 0\n"
    "ds_read_b32 v228, v224 offset:144\n"
    "v_mfma_f32_16x16x32_bf16 v[208:211], v[60:63], v[156:159], v[208:211]\n"
    "v_mfma_f32_16x16x32_bf16 v[212:215], v[84:87], v[156:159], v[212:215]\n"
    "v_mfma_f32_16x16x32_bf16 v[208:211], v[64:67], v[160:163], v[208:211]\n"
    "s_waitcnt lgkmcnt(1)\n"
    "v_mul_f32 v200, v200, v252\n"
    "v_mfma_f32_16x16x32_bf16 v[212:215], v[88:91], v[160:163], v[212:215]\n"
    "v_mul_f32 v201, v201, v252\n"
    "v_mul_f32 v202, v202, v252\n"
    "v_mfma_f32_16x16x32_bf16 v[208:211], v[68:71], v[164:167], v[208:211]\n"
    "v_mul_f32 v203, v203, v252\n"
    "v_mul_f32 v204, v204, v252\n"
    "v_mfma_f32_16x16x32_bf16 v[212:215], v[92:95], v[164:167], v[212:215]\n"
    "v_mul_f32 v205, v205, v252\n"
    "v_mul_f32 v206, v206, v252\n"
    "v_mfma_f32_16x16x32_bf16 v[208:211], v[72:75], v[168:171], v[208:211]\n"
    "v_mul_f32 v207, v207, v252\n"
    "v_cvt_pk_bf16_f32 v200, v200, v201\n"
    "v_mfma_f32_16x16x32_bf16 v[212:215], v[96:99], v[168:171], v[212:215]\n"
    "v_cvt_pk_bf16_f32 v201, v202, v203\n"
    "v_cvt_pk_bf16_f32 v202, v204, v205\n"
    "v_mfma_f32_16x16x32_bf16 v[208:211], v[76:79], v[172:175], v[208:211]\n"
    "s_waitcnt lgkmcnt(0)\n"
    "v_cmp_gt_u32 vcc, 3, v228\n"
    "v_mfma_f32_16x16x32_bf16 v[212:215], v[100:103], v[172:175], v[212:215]\n"
    "v_cvt_pk_bf16_f32 v203, v206, v207\n"
    "s_cbranch_vccnz .Lh12\n"
    "v_mfma_f32_16x16x32_bf16 v[216:219], v[104:107], v[152:155], 0\n"
    "ds_read_b64_tr_b16 v[176:177], v2 offset:18432\n"
    "ds_read_b64_tr_b16 v[178:179], v3 offset:18432\n"
    "v_mfma_f32_16x16x32_bf16 v[220:223], v[128:131], v[152:155], 0\n"
    "ds_read_b64_tr_b16 v[180:181], v2 offset:19456\n"
    "ds_read_b64_tr_b16 v[182:183], v3 offset:19456\n"
    "v_mfma_f32_16x16x32_bf16 v[216:219], v[108:111], v[156:159], v[216:219]\n"
    "ds_read_b64_tr_b16 v[184:185], v2 offset:20480\n"
    "ds_read_b64_tr_b16 v[186:187], v3 offset:20480\n"
    "v_mfma_f32_16x16x32_bf16 v[220:223], v[132:135], v[156:159], v[220:223]\n"
    "ds_read_b64_tr_b16 v[188:189], v2 offset:21504\n"
    "ds_read_b64_tr_b16 v[190:191], v3 offset:21504\n"
    "v_mfma_f32_16x16x32_bf16 v[216:219], v[112:115], v[160:163], v[216:219]\n"
    "buffer_store_dwordx4 v[200:203], v4, s[80:83], 0 offen offset:0 sc1\n"
    "v_mfma_f32_16x16x32_bf16 v[220:223], v[136:139], v[160:163], v[220:223]\n"
    "v_mul_f32 v208, v208, v252\n"
    "v_mul_f32 v209, v209, v252\n"
    "v_mfma_f32_16x16x32_bf16 v[216:219], v[116:119], v[164:167], v[216:219]\n"
    "ds_read_b64_tr_b16 v[192:193], v2 offset:22528\n"
    "ds_read_b64_tr_b16 v[194:195], v3 offset:22528\n"
    "v_mfma_f32_16x16x32_bf16 v[220:223], v[140:143], v[164:167], v[220:223]\n"
    "v_mul_f32 v210, v210, v252\n"
    "v_mul_f32 v211, v211, v252\n"
    "v_mfma_f32_16x16x32_bf16 v[216:219], v[120:123], v[168:171], v[216:219]\n"
    "ds_read_b64_tr_b16 v[196:197], v2 offset:23552\n"
    "ds_read_b64_tr_b16 v[198:199], v3 offset:23552\n"
    "v_mfma_f32_16x16x32_bf16 v[220:223], v[144:147], v[168:171], v[220:223]\n"
    "v_mul_f32 v212, v212, v252\n"
    "v_mul_f32 v213, v213, v252\n"
    "v_mfma_f32_16x16x32_bf16 v[216:219], v[124:127], v[172:175], v[216:219]\n"
    "ds_read_b32 v253, v1 offset:0\n"
    "v_mul_f32 v214, v214, v252\n"
    "v_mfma_f32_16x16x32_bf16 v[220:223], v[148:151], v[172:175], v[220:223]\n"
    "v_mul_f32 v215, v215, v252\n"
    "v_cvt_pk_bf16_f32 v208, v208, v209\n"
    "v_cvt_pk_bf16_f32 v209, v210, v211\n"
    "v_cvt_pk_bf16_f32 v210, v212, v213\n"
    "v_cvt_pk_bf16_f32 v211, v214, v215\n"
    "buffer_store_dwordx4 v[208:211], v4, s[80:83], 0 offen offset:64 sc1\n"
    ".Lh12j:\n"
    "s_waitcnt lgkmcnt(1)\n"
    "v_mfma_f32_16x16x32_bf16 v[200:203], v[8:11], v[176:179], 0\n"
    "v_mul_f32 v216, v216, v252\n"
    "v_mul_f32 v217, v217, v252\n"
    "v_mfma_f32_16x16x32_bf16 v[204:207], v[32:35], v[176:179], 0\n"
    "ds_read_b32 v228, v224 offset:160\n"
    "v_mul_f32 v218, v218, v252\n"
    "v_mfma_f32_16x16x32_bf16 v[200:203], v[12:15], v[180:183], v[200:203]\n"
    "v_mul_f32 v219, v219, v252\n"
    "v_mul_f32 v220, v220, v252\n"
    "v_mfma_f32_16x16x32_bf16 v[204:207], v[36:39], v[180:183], v[204:207]\n"
    "v_mul_f32 v221, v221, v252\n"
    "v_mul_f32 v222, v222, v252\n"
    "v_mfma_f32_16x16x32_bf16 v[200:203], v[16:19], v[184:187], v[200:203]\n"
    "v_mul_f32 v223, v223, v252\n"
    "v_cvt_pk_bf16_f32 v216, v216, v217\n"
    "v_mfma_f32_16x16x32_bf16 v[204:207], v[40:43], v[184:187], v[204:207]\n"
    "v_cvt_pk_bf16_f32 v217, v218, v219\n"
    "v_cvt_pk_bf16_f32 v218, v220, v221\n"
    "v_mfma_f32_16x16x32_bf16 v[200:203], v[20:23], v[188:191], v[200:203]\n"
    "v_cvt_pk_bf16_f32 v219, v222, v223\n"
    "v_mfma_f32_16x16x32_bf16 v[204:207], v[44:47], v[188:191], v[204:207]\n"
    "buffer_store_dwordx4 v[216:219], v4, s[80:83], 0 offen offset:128 sc1\n"
    "v_mfma_f32_16x16x32_bf16 v[200:203], v[24:27], v[192:195], v[200:203]\n"
    "v_mfma_f32_16x16x32_bf16 v[204:207], v[48:51], v[192:195], v[204:207]\n"
    "v_add_u32 v4, v7, v4\n"
    "v_mfma_f32_16x16x32_bf16 v[200:203], v[28:31], v[196:199], v[200:203]\n"
    "s_waitcnt lgkmcnt(0)\n"
    "v_cmp_gt_u32 vcc, 3, v228\n"
    "v_mfma_f32_16x16x32_bf16 v[204:207], v[52:55], v[196:199], v[204:207]\n"
    "s_cbranch_vccnz .Lh13\n"
    "v_mfma_f32_16x16x32_bf16 v[208:211], v[56:59], v[176:179], 0\n"
    "ds_read_b64_tr_b16 v[152:153], v2 offset:6144\n"
    "ds_read_b64_tr_b16 v[154:155], v3 offset:6144\n"
    "v_mfma_f32_16x16x32_bf16 v[212:215], v[80:83], v[176:179], 0\n"
    "ds_read_b64_tr_b16 v[156:157], v2 offset:7168\n"
    "ds_read_b64_tr_b16 v[158:159], v3 offset:7168\n"
    "v_mfma_f32_16x16x32_bf16 v[208:211], v[60:63], v[180:183], v[208:211]\n"
    "ds_read_b64_tr_b16 v[160:161], v2 offset:8192\n"
    "ds_read_b64_tr_b16 v[162:163], v3 offset:8192\n"
    "v_mfma_f32_16x16x32_bf16 v[212:215], v[84:87], v[180:183], v[212:215]\n"
    "ds_read_b64_tr_b16 v[164:165], v2 offset:9216\n"
    "ds_read_b64_tr_b16 v[166:167], v3 offset:9216\n"
    "v_mfma_f32_16x16x32_bf16 v[208:211], v[64:67], v[184:187], v[208:211]\n"
    "v_mul_f32 v200, v200, v253\n"
    "v_mul_f32 v201, v201, v253\n"
    "v_mfma_f32_16x16x32_bf16 v[212:215], v[88:91], v[184:187], v[212:215]\n"
    "ds_read_b64_tr_b16 v[168:169], v2 offset:10240\n"
    "ds_read_b64_tr_b16 v[170:171], v3 offset:10240\n"
    "v_mfma_f32_16x16x32_bf16 v[208:211], v[68:71], v[188:191], v[208:211]\n"
    "v_mul_f32 v202, v202, v253\n"
    "v_mul_f32 v203, v203, v253\n"
    "v_mfma_f32_16x16x32_bf16 v[212:215], v[92:95], v[188:191], v[212:215]\n"
    "ds_read_b64_tr_b16 v[172:173], v2 offset:11264\n"
    "ds_read_b64_tr_b16 v[174:175], v3 offset:11264\n"
    "v_mfma_f32_16x16x32_bf16 v[208:211], v[72:75], v[192:195], v[208:211]\n"
    "v_mul_f32 v204, v204, v253\n"
    "v_mul_f32 v205, v205, v253\n"
    "v_mfma_f32_16x16x32_bf16 v[212:215], v[96:99], v[192:195], v[212:215]\n"
    "ds_read_b32 v252, v1 offset:256\n"
    "v_mul_f32 v206, v206, v253\n"
    "v_mfma_f32_16x16x32_bf16 v[208:211], v[76:79], v[196:199], v[208:211]\n"
    "v_mul_f32 v207, v207, v253\n"
    "v_cvt_pk_bf16_f32 v200, v200, v201\n"
    "v_mfma_f32_16x16x32_bf16 v[212:215], v[100:103], v[196:199], v[212:215]\n"
    "v_cvt_pk_bf16_f32 v201, v202, v203\n"
    "v_cvt_pk_bf16_f32 v202, v204, v205\n"
    "v_cvt_pk_bf16_f32 v203, v206, v207\n"
    "buffer_store_dwordx4 v[200:203], v5, s[80:83], 0 offen offset:0 sc1\n"
    ".Lh13j:\n"
    "s_waitcnt lgkmcnt(1)\n"
    "s_nop 0\n"
    "v_mfma_f32_16x16x32_bf16 v[200:203], v[8:11], v[152:155], 0\n"
    "v_mul_f32 v208, v208, v253\n"
    "v_mul_f32 v209, v209, v253\n"
    "v_mfma_f32_16x16x32_bf16 v[204:207], v[32:35], v[152:155], 0\n"
    "ds_read_b32 v228, v224 offset:176\n"
    "v_mul_f32 v210, v210, v253\n"
    "v_mfma_f32_16x16x32_bf16 v[200:203], v[12:15], v[156:159], v[200:203]\n"
    "v_mul_f32 v211, v211, v253\n"
    "v_mul_f32 v212, v212, v253\n"
    "v_mfma_f32_16x16x32_bf16 v[204:207], v[36:39], v[156:159], v[204:207]\n"
    "v_mul_f32 v213, v213, v253\n"
    "v_mul_f32 v214, v214, v253\n"
    "v_mfma_f32_16x16x32_bf16 v[200:203], v[16:19], v[160:163], v[200:203]\n"
    "v_mul_f32 v215, v215, v253\n"
    "v_cvt_pk_bf16_f32 v208, v208, v209\n"
    "v_mfma_f32_16x16x32_bf16 v[204:207], v[40:43], v[160:163], v[204:207]\n"
    "v_cvt_pk_bf16_f32 v209, v210, v211\n"
    "v_cvt_pk_bf16_f32 v210, v212, v213\n"
    "v_mfma_f32_16x16x32_bf16 v[200:203], v[20:23], v[164:167], v[200:203]\n"
    "v_cvt_pk_bf16_f32 v211, v214, v215\n"
    "v_mfma_f32_16x16x32_bf16 v[204:207], v[44:47], v[164:167], v[204:207]\n"
    "buffer_store_dwordx4 v[208:211], v5, s[80:83], 0 offen offset:64 sc1\n"
    "v_mfma_f32_16x16x32_bf16 v[200:203], v[24:27], v[168:171], v[200:203]\n"
    "v_mfma_f32_16x16x32_bf16 v[204:207], v[48:51], v[168:171], v[204:207]\n"
    "v_add_u32 v5, v7, v5\n"
    "v_mfma_f32_16x16x32_bf16 v[200:203], v[28:31], v[172:175], v[200:203]\n"
    "s_waitcnt lgkmcnt(0)\n"
    "v_cmp_gt_u32 vcc, 3, v228\n"
    "v_mfma_f32_16x16x32_bf16 v[204:207], v[52:55], v[172:175], v[204:207]\n"
    "s_cbranch_vccnz .Lh14\n"
    "v_mfma_f32_16x16x32_bf16 v[208:211], v[56:59], v[152:155], 0\n"
    "ds_read_b64_tr_b16 v[176:177], v2 offset:12288\n"
    "ds_read_b64_tr_b16 v[178:179], v3 offset:12288\n"
    "v_mfma_f32_16x16x32_bf16 v[212:215], v[80:83], v[152:155], 0\n"
    "ds_read_b64_tr_b16 v[180:181], v2 offset:13312\n"
    "ds_read_b64_tr_b16 v[182:183], v3 offset:13312\n"
    "v_mfma_f32_16x16x32_bf16 v[208:211], v[60:63], v[156:159], v[208:211]\n"
    "ds_read_b64_tr_b16 v[184:185], v2 offset:14336\n"
    "ds_read_b64_tr_b16 v[186:187], v3 offset:14336\n"
    "v_mfma_f32_16x16x32_bf16 v[212:215], v[84:87], v[156:159], v[212:215]\n"
    "ds_read_b64_tr_b16 v[188:189], v2 offset:15360\n"
    "ds_read_b64_tr_b16 v[190:191], v3 offset:15360\n"
    "v_mfma_f32_16x16x32_bf16 v[208:211], v[64:67], v[160:163], v[208:211]\n"
    "v_mul_f32 v200, v200, v252\n"
    "v_mul_f32 v201, v201, v252\n"
    "v_mfma_f32_16x16x32_bf16 v[212:215], v[88:91], v[160:163], v[212:215]\n"
    "ds_read_b64_tr_b16 v[192:193], v2 offset:16384\n"
    "ds_read_b64_tr_b16 v[194:195], v3 offset:16384\n"
    "v_mfma_f32_16x16x32_bf16 v[208:211], v[68:71], v[164:167], v[208:211]\n"
    "v_mul_f32 v202, v202, v252\n"
    "v_mul_f32 v203, v203, v252\n"
    "v_mfma_f32_16x16x32_bf16 v[212:215], v[92:95], v[164:167], v[212:215]\n"
    "ds_read_b64_tr_b16 v[196:197], v2 offset:17408\n"
    "ds_read_b64_tr_b16 v[198:199], v3 offset:17408\n"
    "v_mfma_f32_16x16x32_bf16 v[208:211], v[72:75], v[168:171], v[208:211]\n"
    "v_mul_f32 v204, v204, v252\n"
    "v_mul_f32 v205, v205, v252\n"
    "v_mfma_f32_16x16x32_bf16 v[212:215], v[96:99], v[168:171], v[212:215]\n"
    "ds_read_b32 v253, v1 offset:320\n"
    "v_mul_f32 v206, v206, v252\n"
    "v_mfma_f32_16x16x32_bf16 v[208:211], v[76:79], v[172:175], v[208:211]\n"
    "v_mul_f32 v207, v207, v252\n"
    "v_cvt_pk_bf16_f32 v200, v200, v201\n"
    "v_mfma_f32_16x16x32_bf16 v[212:215], v[100:103], v[172:175], v[212:215]\n"
    "v_cvt_pk_bf16_f32 v201, v202, v203\n"
    "v_cvt_pk_bf16_f32 v202, v204, v205\n"
    "v_cvt_pk_bf16_f32 v203, v206, v207\n"
    "buffer_store_dwordx4 v[200:203], v4, s[80:83], 0 offen offset:0 sc1\n"
    ".Lh14j:\n"
    "s_cmp_lg_u32 s58, 0\n"
    "s_cbranch_scc1 .Lh_e12\n"
    "s_waitcnt lgkmcnt(1)\n"
    "v_mfma_f32_16x16x32_bf16 v[200:203], v[8:11], v[176:179], 0\n"
    "v_mul_f32 v208, v208, v252\n"
    "v_mul_f32 v209, v209, v252\n"
    "v_mfma_f32_16x16x32_bf16 v[204:207], v[32:35], v[176:179], 0\n"
    "ds_read_b32 v228, v224 offset:832\n"
    "v_mul_f32 v210, v210, v252\n"
    "v_mfma_f32_16x16x32_bf16 v[200:203], v[12:15], v[180:183], v[200:203]\n"
    "v_mul_f32 v211, v211, v252\n"
    "v_mul_f32 v212, v212, v252\n"
    "v_mfma_f32_16x16x32_bf16 v[204:207], v[36:39], v[180:183], v[204:207]\n"
    "v_mul_f32 v213, v213, v252\n"
    "v_mul_f32 v214, v214, v252\n"
    "v_mfma_f32_16x16x32_bf16 v[200:203], v[16:19], v[184:187], v[200:203]\n"
    "v_mul_f32 v215, v215, v252\n"
    "v_cvt_pk_bf16_f32 v208, v208, v209\n"
    "v_mfma_f32_16x16x32_bf16 v[204:207], v[40:43], v[184:187], v[204:207]\n"
    "v_cvt_pk_bf16_f32 v209, v210, v211\n"
    "v_cvt_pk_bf16_f32 v210, v212, v213\n"
    "v_mfma_f32_16x16x32_bf16 v[200:203], v[20:23], v[188:191], v[200:203]\n"
    "v_cvt_pk_bf16_f32 v211, v214, v215\n"
    "v_mfma_f32_16x16x32_bf16 v[204:207], v[44:47], v[188:191], v[204:207]\n"
    "buffer_store_dwordx4 v[208:211], v4, s[80:83], 0 offen offset:64 sc1\n"
    "v_mfma_f32_16x16x32_bf16 v[200:203], v[24:27], v[192:195], v[200:203]\n"
    "v_mfma_f32_16x16x32_bf16 v[204:207], v[48:51], v[192:195], v[204:207]\n"
    "v_add_u32 v4, v0, v4\n"
    "v_mfma_f32_16x16x32_bf16 v[200:203], v[28:31], v[196:199], v[200:203]\n"
    "s_waitcnt lgkmcnt(0)\n"
    "v_cmp_gt_u32 vcc, 4, v228\n"
    "v_mfma_f32_16x16x32_bf16 v[204:207], v[52:55], v[196:199], v[204:207]\n"
    "s_cbranch_vccnz .Lh15\n"
    "v_mfma_f32_16x16x32_bf16 v[208:211], v[56:59], v[176:179], 0\n"
    "ds_read_b64_tr_b16 v[152:153], v2 offset:0\n"
    "ds_read_b64_tr_b16 v[154:155], v3 offset:0\n"
    "v_mfma_f32_16x16x32_bf16 v[212:215], v[80:83], v[176:179], 0\n"
    "ds_read_b64_tr_b16 v[156:157], v2 offset:1024\n"
    "ds_read_b64_tr_b16 v[158:159], v3 offset:1024\n"
    "v_mfma_f32_16x16x32_bf16 v[208:211], v[60:63], v[180:183], v[208:211]\n"
    "ds_read_b64_tr_b16 v[160:161], v2 offset:2048\n"
    "ds_read_b64_tr_b16 v[162:163], v3 offset:2048\n"
    "v_mfma_f32_16x16x32_bf16 v[212:215], v[84:87], v[180:183], v[212:215]\n"
    "ds_read_b64_tr_b16 v[164:165], v2 offset:3072\n"
    "ds_read_b64_tr_b16 v[166:167], v3 offset:3072\n"
    "v_mfma_f32_16x16x32_bf16 v[208:211], v[64:67], v[184:187], v[208:211]\n"
    "v_mul_f32 v200, v200, v253\n"
    "v_mul_f32 v201, v201, v253\n"
    "v_mfma_f32_16x16x32_bf16 v[212:215], v[88:91], v[184:187], v[212:215]\n"
    "ds_read_b64_tr_b16 v[168:169], v2 offset:4096\n"
    "ds_read_b64_tr_b16 v[170:171], v3 offset:4096\n"
    "v_mfma_f32_16x16x32_bf16 v[208:211], v[68:71], v[188:191], v[208:211]\n"
    "v_mul_f32 v202, v202, v253\n"
    "v_mul_f32 v203, v203, v253\n"
    "v_mfma_f32_16x16x32_bf16 v[212:215], v[92:95], v[188:191], v[212:215]\n"
    "ds_read_b64_tr_b16 v[172:173], v2 offset:5120\n"
    "ds_read_b64_tr_b16 v[174:175], v3 offset:5120\n"
    "v_mfma_f32_16x16x32_bf16 v[208:211], v[72:75], v[192:195], v[208:211]\n"
    "v_mul_f32 v204, v204, v253\n"
    "v_mul_f32 v205, v205, v253\n"
    "v_mfma_f32_16x16x32_bf16 v[212:215], v[96:99], v[192:195], v[212:215]\n"
    "ds_read_b32 v252, v1 offset:192\n"
    "v_mul_f32 v206, v206, v253\n"
    "v_mfma_f32_16x16x32_bf16 v[208:211], v[76:79], v[196:199], v[208:211]\n"
    "v_mul_f32 v207, v207, v253\n"
    "v_cvt_pk_bf16_f32 v200, v200, v201\n"
    "v_mfma_f32_16x16x32_bf16 v[212:215], v[100:103], v[196:199], v[212:215]\n"
    "v_cvt_pk_bf16_f32 v201, v202, v203\n"
    "v_cvt_pk_bf16_f32 v202, v204, v205\n"
    "v_cvt_pk_bf16_f32 v203, v206, v207\n"
    "buffer_store_dwordx4 v[200:203], v5, s[80:83], 0 offen offset:0 sc1\n"
    ".Lh15j:\n"
    "s_waitcnt lgkmcnt(1)\n"
    "s_nop 0\n"
    "v_mfma_f32_16x16x32_bf16 v[200:203], v[8:11], v[152:155], 0\n"
    "v_mul_f32 v208, v208, v253\n"
    "v_mul_f32 v209, v209, v253\n"
    "v_mfma_f32_16x16x32_bf16 v[204:207], v[32:35], v[152:155], 0\n"
    "v_mul_f32 v210, v210, v253\n"
    "v_mul_f32 v211, v211, v253\n"
    "v_mfma_f32_16x16x32_bf16 v[200:203], v[12:15], v[156:159], v[200:203]\n"
    "v_mul_f32 v212, v212, v253\n"
    "v_mul_f32 v213, v213, v253\n"
    "v_mfma_f32_16x16x32_bf16 v[204:207], v[36:39], v[156:159], v[204:207]\n"
    "v_mul_f32 v214, v214, v253\n"
    "v_mul_f32 v215, v215, v253\n"
    "v_mfma_f32_16x16x32_bf16 v[200:203], v[16:19], v[160:163], v[200:203]\n"
    "v_cvt_pk_bf16_f32 v208, v208, v209\n"
    "v_cvt_pk_bf16_f32 v209, v210, v211\n"
    "v_mfma_f32_16x16x32_bf16 v[204:207], v[40:43], v[160:163], v[204:207]\n"
    "v_cvt_pk_bf16_f32 v210, v212, v213\n"
    "v_cvt_pk_bf16_f32 v211, v214, v215\n"
    "v_mfma_f32_16x16x32_bf16 v[200:203], v[20:23], v[164:167], v[200:203]\n"
    "buffer_store_dwordx4 v[208:211], v5, s[80:83], 0 offen offset:64 sc1\n"
    "v_mfma_f32_16x16x32_bf16 v[204:207], v[44:47], v[164:167], v[204:207]\n"
    "v_mfma_f32_16x16x32_bf16 v[200:203], v[24:27], v[168:171], v[200:203]\n"
    "v_mfma_f32_16x16x32_bf16 v[204:207], v[48:51], v[168:171], v[204:207]\n"
    "v_mfma_f32_16x16x32_bf16 v[200:203], v[28:31], v[172:175], v[200:203]\n"
    "v_mfma_f32_16x16x32_bf16 v[204:207], v[52:55], v[172:175], v[204:207]\n"
    "v_mfma_f32_16x16x32_bf16 v[208:211], v[56:59], v[152:155], 0\n"
    "v_mfma_f32_16x16x32_bf16 v[212:215], v[80:83], v[152:155], 0\n"
    "v_mfma_f32_16x16x32_bf16 v[208:211], v[60:63], v[156:159], v[208:211]\n"
    "v_mfma_f32_16x16x32_bf16 v[212:215], v[84:87], v[156:159], v[212:215]\n"
    "v_mfma_f32_16x16x32_bf16 v[208:211], v[64:67], v[160:163], v[208:211]\n"
    "v_mfma_f32_16x16x32_bf16 v[212:215], v[88:91], v[160:163], v[212:215]\n"
    "s_waitcnt lgkmcnt(0)\n"
    "v_mul_f32 v200, v200, v252\n"
    "v_mfma_f32_16x16x32_bf16 v[208:211], v[68:71], v[164:167], v[208:211]\n"
    "v_mul_f32 v201, v201, v252\n"
    "v_mul_f32 v202, v202, v252\n"
    "v_mfma_f32_16x16x32_bf16 v[212:215], v[92:95], v[164:167], v[212:215]\n"
    "v_mul_f32 v203, v203, v252\n"
    "v_mul_f32 v204, v204, v252\n"
    "v_mfma_f32_16x16x32_bf16 v[208:211], v[72:75], v[168:171], v[208:211]\n"
    "v_mul_f32 v205, v205, v252\n"
    "v_mul_f32 v206, v206, v252\n"
    "v_mfma_f32_16x16x32_bf16 v[212:215], v[96:99], v[168:171], v[212:215]\n"
    "v_mul_f32 v207, v207, v252\n"
    "v_cvt_pk_bf16_f32 v200, v200, v201\n"
    "v_mfma_f32_16x16x32_bf16 v[208:211], v[76:79], v[172:175], v[208:211]\n"
    "v_cvt_pk_bf16_f32 v201, v202, v203\n"
    "v_cvt_pk_bf16_f32 v202, v204, v205\n"
    "v_mfma_f32_16x16x32_bf16 v[212:215], v[100:103], v[172:175], v[212:215]\n"
    "v_cvt_pk_bf16_f32 v203, v206, v207\n"
    "buffer_store_dwordx4 v[200:203], v4, s[80:83], 0 offen offset:0 sc1\n"
    "s_nop 2\n"
    "v_mul_f32 v208, v208, v252\n"
    "v_mul_f32 v209, v209, v252\n"
    "v_mul_f32 v210, v210, v252\n"
    "v_mul_f32 v211, v211, v252\n"
    "v_mul_f32 v212, v212, v252\n"
    "v_mul_f32 v213, v213, v252\n"
    "v_mul_f32 v214, v214, v252\n"
    "v_mul_f32 v215, v215, v252\n"
    "v_cvt_pk_bf16_f32 v208, v208, v209\n"
    "v_cvt_pk_bf16_f32 v209, v210, v211\n"
    "v_cvt_pk_bf16_f32 v210, v212, v213\n"
    "v_cvt_pk_bf16_f32 v211, v214, v215\n"
    "buffer_store_dwordx4 v[208:211], v4, s[80:83], 0 offen offset:64 sc1\n"
    ".Lh_end:\n"
    "s_endpgm\n"
    ".Lh1:\n"
    "s_sub_u32 s52, s52, 1\n"
    "s_cmp_lt_i32 s52, 0\n"
    "s_cbranch_scc1 .Lh1b\n"
    "ds_read_b32 v228, v224 offset:288\n"
    "s_waitcnt lgkmcnt(0)\n"
    "v_cmp_gt_u32 vcc, 1, v228\n"
    "s_cbranch_vccnz .Lh1\n"
    "s_branch .Lh1b\n"
    ".Lh2:\n"
    "s_sub_u32 s52, s52, 1\n"
    "s_cmp_lt_i32 s52, 0\n"
    "s_cbranch_scc1 .Lh2b\n"
    "ds_read_b32 v228, v224 offset:0\n"
    "s_waitcnt lgkmcnt(0)\n"
    "v_cmp_gt_u32 vcc, 6, v228\n"
    "s_cbranch_vccnz .Lh2\n"
    "s_branch .Lh2b\n"
    ".Lh_re:\n"
    "s_sub_u32 s51, s51, 1\n"
    "s_cmp_le_i32 s51, 0\n"
    "s_cbranch_scc1 .Lh_ok\n"
    "s_sleep 2\n"
    "v_mov_b64 v[244:245], v[240:241]\n"
    "v_mov_b64 v[246:247], 0\n"
    "v_mov_b64 v[242:243], 0\n"
    "s_mov_b32 exec_hi, 15\n"
    "global_load_dwordx4 v[244:247], v236, s[40:41] sc1\n"
    "s_mov_b64 exec, -1\n"
    "global_load_dwordx4 v[8:11], v235, s[64:65] offset:-3072 sc1\n"
    "global_load_dwordx4 v[32:35], v235, s[66:67] offset:-3072 sc1\n"
    "global_load_dwordx4 v[12:15], v235, s[64:65] offset:-2048 sc1\n"
    "global_load_dwordx4 v[36:39], v235, s[66:67] offset:-2048 sc1\n"
    "global_load_dwordx4 v[16:19], v235, s[64:65] offset:-1024 sc1\n"
    "global_load_dwordx4 v[40:43], v235, s[66:67] offset:-1024 sc1\n"
    "global_load_dwordx4 v[20:23], v235, s[64:65] offset:0 sc1\n"
    "global_load_dwordx4 v[44:47], v235, s[66:67] offset:0 sc1\n"
    "global_load_dwordx4 v[24:27], v235, s[64:65] offset:1024 sc1\n"
    "global_load_dwordx4 v[48:51], v235, s[66:67] offset:1024 sc1\n"
    "global_load_dwordx4 v[28:31], v235, s[64:65] offset:2048 sc1\n"
    "global_load_dwordx4 v[52:55], v235, s[66:67] offset:2048 sc1\n"
    "global_load_dwordx4 v[56:59], v235, s[68:69] offset:-3072 sc1\n"
    "global_load_dwordx4 v[80:83], v235, s[70:71] offset:-3072 sc1\n"
    "global_load_dwordx4 v[60:63], v235, s[68:69] offset:-2048 sc1\n"
    "global_load_dwordx4 v[84:87], v235, s[70:71] offset:-2048 sc1\n"
    "global_load_dwordx4 v[64:67], v235, s[68:69] offset:-1024 sc1\n"
    "global_load_dwordx4 v[88:91], v235, s[70:71] offset:-1024 sc1\n"
    "global_load_dwordx4 v[68:71], v235, s[68:69] offset:0 sc1\n"
    "global_load_dwordx4 v[92:95], v235, s[70:71] offset:0 sc1\n"
    "global_load_dwordx4 v[72:75], v235, s[68:69] offset:1024 sc1\n"
    "global_load_dwordx4 v[96:99], v235, s[70:71] offset:1024 sc1\n"
    "global_load_dwordx4 v[76:79], v235, s[68:69] offset:2048 sc1\n"
    "global_load_dwordx4 v[100:103], v235, s[70:71] offset:2048 sc1\n"
    "global_load_dwordx4 v[104:107], v235, s[72:73] offset:-3072 sc1\n"
    "global_load_dwordx4 v[128:131], v235, s[74:75] offset:-3072 sc1\n"
    "global_load_dwordx4 v[108:111], v235, s[72:73] offset:-2048 sc1\n"
    "global_load_dwordx4 v[132:135], v235, s[74:75] offset:-2048 sc1\n"
    "global_load_dwordx4 v[112:115], v235, s[72:73] offset:-1024 sc1\n"
    "global_load_dwordx4 v[136:139], v235, s[74:75] offset:-1024 sc1\n"
    "global_load_dwordx4 v[116:119], v235, s[72:73] offset:0 sc1\n"
    "global_load_dwordx4 v[140:143], v235, s[74:75] offset:0 sc1\n"
    "global_load_dwordx4 v[120:123], v235, s[72:73] offset:1024 sc1\n"
    "global_load_dwordx4 v[144:147], v235, s[74:75] offset:1024 sc1\n"
    "global_load_dwordx4 v[124:127], v235, s[72:73] offset:2048 sc1\n"
    "global_load_dwordx4 v[148:151], v235, s[74:75] offset:2048 sc1\n"
    "s_waitcnt vmcnt(0)\n"
    "v_add3_u32 v242, v242, v8, v11\n"
    "v_sub_u32 v8, v8, v240\n"
    "v_add3_u32 v242, v242, v32, v35\n"
    "v_sub_u32 v32, v32, v240\n"
    "v_add3_u32 v242, v242, v12, v15\n"
    "v_sub_u32 v12, v12, v240\n"
    "v_add3_u32 v242, v242, v36, v39\n"
    "v_sub_u32 v36, v36, v240\n"
    "v_add3_u32 v242, v242, v16, v19\n"
    "v_sub_u32 v16, v16, v240\n"
    "v_add3_u32 v242, v242, v40, v43\n"
    "v_sub_u32 v40, v40, v240\n"
    "v_add3_u32 v242, v242, v20, v23\n"
    "v_sub_u32 v20, v20, v240\n"
    "v_add3_u32 v242, v242, v44, v47\n"
    "v_sub_u32 v44, v44, v240\n"
    "v_add3_u32 v242, v242, v24, v27\n"
    "v_sub_u32 v24, v24, v240\n"
    "v_add3_u32 v242, v242, v48, v51\n"
    "v_sub_u32 v48, v48, v240\n"
    "v_add3_u32 v242, v242, v28, v31\n"
    "v_sub_u32 v28, v28, v240\n"
    "v_add3_u32 v242, v242, v52, v55\n"
    "v_sub_u32 v52, v52, v240\n"
    "v_add3_u32 v242, v242, v56, v59\n"
    "v_sub_u32 v56, v56, v240\n"
    "v_add3_u32 v242, v242, v80, v83\n"
    "v_sub_u32 v80, v80, v240\n"
    "v_add3_u32 v242, v242, v60, v63\n"
    "v_sub_u32 v60, v60, v240\n"
    "v_add3_u32 v242, v242, v84, v87\n"
    "v_sub_u32 v84, v84, v240\n"
    "v_add3_u32 v242, v242, v64, v67\n"
    "v_sub_u32 v64, v64, v240\n"
    "v_add3_u32 v242, v242, v88, v91\n"
    "v_sub_u32 v88, v88, v240\n"
    "v_add3_u32 v242, v242, v68, v71\n"
    "v_sub_u32 v68, v68, v240\n"
    "v_add3_u32 v242, v242, v92, v95\n"
    "v_sub_u32 v92, v92, v240\n"
    "v_add3_u32 v242, v242, v72, v75\n"
    "v_sub_u32 v72, v72, v240\n"
    "v_add3_u32 v242, v242, v96, v99\n"
    "v_sub_u32 v96, v96, v240\n"
    "v_add3_u32 v242, v242, v76, v79\n"
    "v_sub_u32 v76, v76, v240\n"
    "v_add3_u32 v242, v242, v100, v103\n"
    "v_sub_u32 v100, v100, v240\n"
    "v_add3_u32 v242, v242, v104, v107\n"
    "v_sub_u32 v104, v104, v240\n"
    "v_add3_u32 v242, v242, v128, v131\n"
    "v_sub_u32 v128, v128, v240\n"
    "v_add3_u32 v242, v242, v108, v111\n"
    "v_sub_u32 v108, v108, v240\n"
    "v_add3_u32 v242, v242, v132, v135\n"
    "v_sub_u32 v132, v132, v240\n"
    "v_add3_u32 v242, v242, v112, v115\n"
    "v_sub_u32 v112, v112, v240\n"
    "v_add3_u32 v242, v242, v136, v139\n"
    "v_sub_u32 v136, v136, v240\n"
    "v_add3_u32 v242, v242, v116, v119\n"
    "v_sub_u32 v116, v116, v240\n"
    "v_add3_u32 v242, v242, v140, v143\n"
    "v_sub_u32 v140, v140, v240\n"
    "v_add3_u32 v242, v242, v120, v123\n"
    "v_sub_u32 v120, v120, v240\n"
    "v_add3_u32 v242, v242, v144, v147\n"
    "v_sub_u32 v144, v144, v240\n"
    "v_add3_u32 v242, v242, v124, v127\n"
    "v_sub_u32 v124, v124, v240\n"
    "v_add3_u32 v242, v242, v148, v151\n"
    "v_sub_u32 v148, v148, v240\n"
    "s_branch .Lh_ce\n"
    ".Lh_pl:\n"
    "s_sub_u32 s51, s51, 1\n"
    "s_cmp_le_i32 s51, 0\n"
    "s_cbranch_scc1 .Lh_ld\n"
    "s_sleep 4\n"
    "s_mov_b32 exec_hi, 15\n"
    "global_load_dwordx2 v[244:245], v236, s[40:41] sc1\n"
    "s_mov_b64 exec, -1\n"
    "s_waitcnt vmcnt(0)\n"
    "v_cmp_ne_u64 vcc, v[240:241], v[244:245]\n"
    "s_cbranch_vccnz .Lh_pl\n"
    "s_branch .Lh_ld\n"
    ".Lh_ld:\n"
    "v_mov_b64 v[244:245], v[240:241]\n"
    "v_mov_b64 v[246:247], 0\n"
    "v_mov_b64 v[242:243], 0\n"
    "s_mov_b32 exec_hi, 15\n"
    "global_load_dwordx4 v[244:247], v236, s[40:41] sc1\n"
    "s_mov_b64 exec, -1\n"
    "global_load_dwordx4 v[8:11], v235, s[64:65] offset:-3072 sc1\n"
    "global_load_dwordx4 v[32:35], v235, s[66:67] offset:-3072 sc1\n"
    "global_load_dwordx4 v[12:15], v235, s[64:65] offset:-2048 sc1\n"
    "global_load_dwordx4 v[36:39], v235, s[66:67] offset:-2048 sc1\n"
    "global_load_dwordx4 v[16:19], v235, s[64:65] offset:-1024 sc1\n"
    "global_load_dwordx4 v[40:43], v235, s[66:67] offset:-1024 sc1\n"
    "global_load_dwordx4 v[20:23], v235, s[64:65] offset:0 sc1\n"
    "global_load_dwordx4 v[44:47], v235, s[66:67] offset:0 sc1\n"
    "global_load_dwordx4 v[24:27], v235, s[64:65] offset:1024 sc1\n"
    "global_load_dwordx4 v[48:51], v235, s[66:67] offset:1024 sc1\n"
    "global_load_dwordx4 v[28:31], v235, s[64:65] offset:2048 sc1\n"
    "global_load_dwordx4 v[52:55], v235, s[66:67] offset:2048 sc1\n"
    "global_load_dwordx4 v[56:59], v235, s[68:69] offset:-3072 sc1\n"
    "global_load_dwordx4 v[80:83], v235, s[70:71] offset:-3072 sc1\n"
    "global_load_dwordx4 v[60:63], v235, s[68:69] offset:-2048 sc1\n"
    "global_load_dwordx4 v[84:87], v235, s[70:71] offset:-2048 sc1\n"
    "global_load_dwordx4 v[64:67], v235, s[68:69] offset:-1024 sc1\n"
    "global_load_dwordx4 v[88:91], v235, s[70:71] offset:-1024 sc1\n"
    "global_load_dwordx4 v[68:71], v235, s[68:69] offset:0 sc1\n"
    "global_load_dwordx4 v[92:95], v235, s[70:71] offset:0 sc1\n"
    "global_load_dwordx4 v[72:75], v235, s[68:69] offset:1024 sc1\n"
    "global_load_dwordx4 v[96:99], v235, s[70:71] offset:1024 sc1\n"
    "global_load_dwordx4 v[76:79], v235, s[68:69] offset:2048 sc1\n"
    "global_load_dwordx4 v[100:103], v235, s[70:71] offset:2048 sc1\n"
    "global_load_dwordx4 v[104:107], v235, s[72:73] offset:-3072 sc1\n"
    "global_load_dwordx4 v[128:131], v235, s[74:75] offset:-3072 sc1\n"
    "global_load_dwordx4 v[108:111], v235, s[72:73] offset:-2048 sc1\n"
    "global_load_dwordx4 v[132:135], v235, s[74:75] offset:-2048 sc1\n"
    "global_load_dwordx4 v[112:115], v235, s[72:73] offset:-1024 sc1\n"
    "global_load_dwordx4 v[136:139], v235, s[74:75] offset:-1024 sc1\n"
    "global_load_dwordx4 v[116:119], v235, s[72:73] offset:0 sc1\n"
    "global_load_dwordx4 v[140:143], v235, s[74:75] offset:0 sc1\n"
    "global_load_dwordx4 v[120:123], v235, s[72:73] offset:1024 sc1\n"
    "global_load_dwordx4 v[144:147], v235, s[74:75] offset:1024 sc1\n"
    "global_load_dwordx4 v[124:127], v235, s[72:73] offset:2048 sc1\n"
    "global_load_dwordx4 v[148:151], v235, s[74:75] offset:2048 sc1\n"
    "s_branch .Lh_rs\n"
    ".Lh3:\n"
    "s_sub_u32 s52, s52, 1\n"
    "s_cmp_lt_i32 s52, 0\n"
    "s_cbranch_scc1 .Lh3b\n"
    "ds_read_b32 v228, v224 offset:128\n"
    "s_waitcnt lgkmcnt(0)\n"
    "v_cmp_gt_u32 vcc, 1, v228\n"
    "s_cbranch_vccnz .Lh3\n"
    "s_branch .Lh3b\n"
    ".Lh4:\n"
    "v_mfma_f32_16x16x32_bf16 v[216:219], v[104:107], v[152:155], 0\n"
    "buffer_store_dwordx4 v[200:203], v4, s[80:83], 0 offen offset:0 sc1\n"
    "v_mfma_f32_16x16x32_bf16 v[220:223], v[128:131], v[152:155], 0\n"
    "v_mul_f32 v208, v208, v252\n"
    "v_mul_f32 v209, v209, v252\n"
    "v_mfma_f32_16x16x32_bf16 v[216:219], v[108:111], v[156:159], v[216:219]\n"
    "v_mul_f32 v210, v210, v252\n"
    "v_mul_f32 v211, v211, v252\n"
    "v_mfma_f32_16x16x32_bf16 v[220:223], v[132:135], v[156:159], v[220:223]\n"
    "v_mul_f32 v212, v212, v252\n"
    "v_mul_f32 v213, v213, v252\n"
    "v_mfma_f32_16x16x32_bf16 v[216:219], v[112:115], v[160:163], v[216:219]\n"
    "v_mul_f32 v214, v214, v252\n"
    "v_mul_f32 v215, v215, v252\n"
    "v_mfma_f32_16x16x32_bf16 v[220:223], v[136:139], v[160:163], v[220:223]\n"
    "ds_read_b32 v228, v224 offset:144\n"
    "v_cvt_pk_bf16_f32 v208, v208, v209\n"
    "v_mfma_f32_16x16x32_bf16 v[216:219], v[116:119], v[164:167], v[216:219]\n"
    "v_cvt_pk_bf16_f32 v209, v210, v211\n"
    "v_cvt_pk_bf16_f32 v210, v212, v213\n"
    "v_mfma_f32_16x16x32_bf16 v[220:223], v[140:143], v[164:167], v[220:223]\n"
    "v_cvt_pk_bf16_f32 v211, v214, v215\n"
    "v_mfma_f32_16x16x32_bf16 v[216:219], v[120:123], v[168:171], v[216:219]\n"
    "buffer_store_dwordx4 v[208:211], v4, s[80:83], 0 offen offset:64 sc1\n"
    "v_mfma_f32_16x16x32_bf16 v[220:223], v[144:147], v[168:171], v[220:223]\n"
    "v_mfma_f32_16x16x32_bf16 v[216:219], v[124:127], v[172:175], v[216:219]\n"
    "v_mfma_f32_16x16x32_bf16 v[220:223], v[148:151], v[172:175], v[220:223]\n"
    "s_waitcnt lgkmcnt(0)\n"
    "v_cmp_gt_u32 vcc, 1, v228\n"
    "s_cbranch_vccz .Lh4g\n"
    ".Lh4r:\n"
    "s_sub_u32 s52, s52, 1\n"
    "s_cmp_lt_i32 s52, 0\n"
    "s_cbranch_scc1 .Lh4g\n"
    "ds_read_b32 v228, v224 offset:144\n"
    "s_waitcnt lgkmcnt(0)\n"
    "v_cmp_gt_u32 vcc, 1, v228\n"
    "s_cbranch_vccnz .Lh4r\n"
    "s_branch .Lh4g\n"
    ".Lh4g:\n"
    "ds_read_b64_tr_b16 v[176:177], v2 offset:24576\n"
    "ds_read_b64_tr_b16 v[178:179], v3 offset:24576\n"
    "ds_read_b64_tr_b16 v[180:181], v2 offset:25600\n"
    "ds_read_b64_tr_b16 v[182:183], v3 offset:25600\n"
    "ds_read_b64_tr_b16 v[184:185], v2 offset:26624\n"
    "ds_read_b64_tr_b16 v[186:187], v3 offset:26624\n"
    "ds_read_b64_tr_b16 v[188:189], v2 offset:27648\n"
    "ds_read_b64_tr_b16 v[190:191], v3 offset:27648\n"
    "ds_read_b64_tr_b16 v[192:193], v2 offset:28672\n"
    "ds_read_b64_tr_b16 v[194:195], v3 offset:28672\n"
    "ds_read_b64_tr_b16 v[196:197], v2 offset:29696\n"
    "ds_read_b64_tr_b16 v[198:199], v3 offset:29696\n"
    "ds_read_b32 v253, v1 offset:64\n"
    "v_mov_b32 v227, 2\n"
    "ds_write_b32 v226, v227 offset:32\n"
    "s_branch .Lh4j\n"
    ".Lh5:\n"
    "v_mfma_f32_16x16x32_bf16 v[216:219], v[104:107], v[176:179], 0\n"
    "buffer_store_dwordx4 v[200:203], v5, s[80:83], 0 offen offset:0 sc1\n"
    "v_mfma_f32_16x16x32_bf16 v[220:223], v[128:131], v[176:179], 0\n"
    "v_mul_f32 v208, v208, v253\n"
    "v_mul_f32 v209, v209, v253\n"
    "v_mfma_f32_16x16x32_bf16 v[216:219], v[108:111], v[180:183], v[216:219]\n"
    "v_mul_f32 v210, v210, v253\n"
    "v_mul_f32 v211, v211, v253\n"
    "v_mfma_f32_16x16x32_bf16 v[220:223], v[132:135], v[180:183], v[220:223]\n"
    "v_mul_f32 v212, v212, v253\n"
    "v_mul_f32 v213, v213, v253\n"
    "v_mfma_f32_16x16x32_bf16 v[216:219], v[112:115], v[184:187], v[216:219]\n"
    "v_mul_f32 v214, v214, v253\n"
    "v_mul_f32 v215, v215, v253\n"
    "v_mfma_f32_16x16x32_bf16 v[220:223], v[136:139], v[184:187], v[220:223]\n"
    "ds_read_b32 v228, v224 offset:160\n"
    "v_cvt_pk_bf16_f32 v208, v208, v209\n"
    "v_mfma_f32_16x16x32_bf16 v[216:219], v[116:119], v[188:191], v[216:219]\n"
    "v_cvt_pk_bf16_f32 v209, v210, v211\n"
    "v_cvt_pk_bf16_f32 v210, v212, v213\n"
    "v_mfma_f32_16x16x32_bf16 v[220:223], v[140:143], v[188:191], v[220:223]\n"
    "v_cvt_pk_bf16_f32 v211, v214, v215\n"
    "v_mfma_f32_16x16x32_bf16 v[216:219], v[120:123], v[192:195], v[216:219]\n"
    "buffer_store_dwordx4 v[208:211], v5, s[80:83], 0 offen offset:64 sc1\n"
    "v_mfma_f32_16x16x32_bf16 v[220:223], v[144:147], v[192:195], v[220:223]\n"
    "v_mfma_f32_16x16x32_bf16 v[216:219], v[124:127], v[196:199], v[216:219]\n"
    "v_mfma_f32_16x16x32_bf16 v[220:223], v[148:151], v[196:199], v[220:223]\n"
    "s_waitcnt lgkmcnt(0)\n"
    "v_cmp_gt_u32 vcc, 1, v228\n"
    "s_cbranch_vccz .Lh5g\n"
    ".Lh5r:\n"
    "s_sub_u32 s52, s52, 1\n"
    "s_cmp_lt_i32 s52, 0\n"
    "s_cbranch_scc1 .Lh5g\n"
    "ds_read_b32 v228, v224 offset:160\n"
    "s_waitcnt lgkmcnt(0)\n"
    "v_cmp_gt_u32 vcc, 1, v228\n"
    "s_cbranch_vccnz .Lh5r\n"
    "s_branch .Lh5g\n"
    ".Lh5g:\n"
    "ds_read_b64_tr_b16 v[152:153], v2 offset:30720\n"
    "ds_read_b64_tr_b16 v[154:155], v3 offset:30720\n"
    "ds_read_b64_tr_b16 v[156:157], v2 offset:31744\n"
    "ds_read_b64_tr_b16 v[158:159], v3 offset:31744\n"
    "ds_read_b64_tr_b16 v[160:161], v2 offset:32768\n"
    "ds_read_b64_tr_b16 v[162:163], v3 offset:32768\n"
    "ds_read_b64_tr_b16 v[164:165], v2 offset:33792\n"
    "ds_read_b64_tr_b16 v[166:167], v3 offset:33792\n"
    "ds_read_b64_tr_b16 v[168:169], v2 offset:34816\n"
    "ds_read_b64_tr_b16 v[170:171], v3 offset:34816\n"
    "ds_read_b64_tr_b16 v[172:173], v2 offset:35840\n"
    "ds_read_b64_tr_b16 v[174:175], v3 offset:35840\n"
    "ds_read_b32 v252, v1 offset:128\n"
    "v_mov_b32 v227, 3\n"
    "ds_write_b32 v226, v227 offset:32\n"
    "s_branch .Lh5j\n"
    ".Lh6:\n"
    "v_mfma_f32_16x16x32_bf16 v[216:219], v[104:107], v[152:155], 0\n"
    "buffer_store_dwordx4 v[200:203], v4, s[80:83], 0 offen offset:0 sc1\n"
    "v_mfma_f32_16x16x32_bf16 v[220:223], v[128:131], v[152:155], 0\n"
    "v_mul_f32 v208, v208, v252\n"
    "v_mul_f32 v209, v209, v252\n"
    "v_mfma_f32_16x16x32_bf16 v[216:219], v[108:111], v[156:159], v[216:219]\n"
    "v_mul_f32 v210, v210, v252\n"
    "v_mul_f32 v211, v211, v252\n"
    "v_mfma_f32_16x16x32_bf16 v[220:223], v[132:135], v[156:159], v[220:223]\n"
    "v_mul_f32 v212, v212, v252\n"
    "v_mul_f32 v213, v213, v252\n"
    "v_mfma_f32_16x16x32_bf16 v[216:219], v[112:115], v[160:163], v[216:219]\n"
    "v_mul_f32 v214, v214, v252\n"
    "v_mul_f32 v215, v215, v252\n"
    "v_mfma_f32_16x16x32_bf16 v[220:223], v[136:139], v[160:163], v[220:223]\n"
    "ds_read_b32 v228, v224 offset:176\n"
    "v_cvt_pk_bf16_f32 v208, v208, v209\n"
    "v_mfma_f32_16x16x32_bf16 v[216:219], v[116:119], v[164:167], v[216:219]\n"
    "v_cvt_pk_bf16_f32 v209, v210, v211\n"
    "v_cvt_pk_bf16_f32 v210, v212, v213\n"
    "v_mfma_f32_16x16x32_bf16 v[220:223], v[140:143], v[164:167], v[220:223]\n"
    "v_cvt_pk_bf16_f32 v211, v214, v215\n"
    "v_mfma_f32_16x16x32_bf16 v[216:219], v[120:123], v[168:171], v[216:219]\n"
    "buffer_store_dwordx4 v[208:211], v4, s[80:83], 0 offen offset:64 sc1\n"
    "v_mfma_f32_16x16x32_bf16 v[220:223], v[144:147], v[168:171], v[220:223]\n"
    "v_mfma_f32_16x16x32_bf16 v[216:219], v[124:127], v[172:175], v[216:219]\n"
    "v_mfma_f32_16x16x32_bf16 v[220:223], v[148:151], v[172:175], v[220:223]\n"
    "s_waitcnt lgkmcnt(0)\n"
    "v_cmp_gt_u32 vcc, 1, v228\n"
    "s_cbranch_vccz .Lh6g\n"
    ".Lh6r:\n"
    "s_sub_u32 s52, s52, 1\n"
    "s_cmp_lt_i32 s52, 0\n"
    "s_cbranch_scc1 .Lh6g\n"
    "ds_read_b32 v228, v224 offset:176\n"
    "s_waitcnt lgkmcnt(0)\n"
    "v_cmp_gt_u32 vcc, 1, v228\n"
    "s_cbranch_vccnz .Lh6r\n"
    "s_branch .Lh6g\n"
    ".Lh6g:\n"
    "ds_read_b64_tr_b16 v[176:177], v2 offset:18432\n"
    "ds_read_b64_tr_b16 v[178:179], v3 offset:18432\n"
    "ds_read_b64_tr_b16 v[180:181], v2 offset:19456\n"
    "ds_read_b64_tr_b16 v[182:183], v3 offset:19456\n"
    "ds_read_b64_tr_b16 v[184:185], v2 offset:20480\n"
    "ds_read_b64_tr_b16 v[186:187], v3 offset:20480\n"
    "ds_read_b64_tr_b16 v[188:189], v2 offset:21504\n"
    "ds_read_b64_tr_b16 v[190:191], v3 offset:21504\n"
    "ds_read_b64_tr_b16 v[192:193], v2 offset:22528\n"
    "ds_read_b64_tr_b16 v[194:195], v3 offset:22528\n"
    "ds_read_b64_tr_b16 v[196:197], v2 offset:23552\n"
    "ds_read_b64_tr_b16 v[198:199], v3 offset:23552\n"
    "ds_read_b32 v253, v1 offset:0\n"
    "v_mov_b32 v227, 4\n"
    "ds_write_b32 v226, v227 offset:32\n"
    "s_branch .Lh6j\n"
    ".Lh7:\n"
    "v_mfma_f32_16x16x32_bf16 v[216:219], v[104:107], v[176:179], 0\n"
    "buffer_store_dwordx4 v[200:203], v5, s[80:83], 0 offen offset:0 sc1\n"
    "v_mfma_f32_16x16x32_bf16 v[220:223], v[128:131], v[176:179], 0\n"
    "v_mul_f32 v208, v208, v253\n"
    "v_mul_f32 v209, v209, v253\n"
    "v_mfma_f32_16x16x32_bf16 v[216:219], v[108:111], v[180:183], v[216:219]\n"
    "v_mul_f32 v210, v210, v253\n"
    "v_mul_f32 v211, v211, v253\n"
    "v_mfma_f32_16x16x32_bf16 v[220:223], v[132:135], v[180:183], v[220:223]\n"
    "v_mul_f32 v212, v212, v253\n"
    "v_mul_f32 v213, v213, v253\n"
    "v_mfma_f32_16x16x32_bf16 v[216:219], v[112:115], v[184:187], v[216:219]\n"
    "v_mul_f32 v214, v214, v253\n"
    "v_mul_f32 v215, v215, v253\n"
    "v_mfma_f32_16x16x32_bf16 v[220:223], v[136:139], v[184:187], v[220:223]\n"
    "ds_read_b32 v228, v224 offset:128\n"
    "v_cvt_pk_bf16_f32 v208, v208, v209\n"
    "v_mfma_f32_16x16x32_bf16 v[216:219], v[116:119], v[188:191], v[216:219]\n"
    "v_cvt_pk_bf16_f32 v209, v210, v211\n"
    "v_cvt_pk_bf16_f32 v210, v212, v213\n"
    "v_mfma_f32_16x16x32_bf16 v[220:223], v[140:143], v[188:191], v[220:223]\n"
    "v_cvt_pk_bf16_f32 v211, v214, v215\n"
    "v_mfma_f32_16x16x32_bf16 v[216:219], v[120:123], v[192:195], v[216:219]\n"
    "buffer_store_dwordx4 v[208:211], v5, s[80:83], 0 offen offset:64 sc1\n"
    "v_mfma_f32_16x16x32_bf16 v[220:223], v[144:147], v[192:195], v[220:223]\n"
    "v_mfma_f32_16x16x32_bf16 v[216:219], v[124:127], v[196:199], v[216:219]\n"
    "v_mfma_f32_16x16x32_bf16 v[220:223], v[148:151], v[196:199], v[220:223]\n"
    "s_waitcnt lgkmcnt(0)\n"
    "v_cmp_gt_u32 vcc, 2, v228\n"
    "s_cbranch_vccz .Lh7g\n"
    ".Lh7r:\n"
    "s_sub_u32 s52, s52, 1\n"
    "s_cmp_lt_i32 s52, 0\n"
    "s_cbranch_scc1 .Lh7g\n"
    "ds_read_b32 v228, v224 offset:128\n"
    "s_waitcnt lgkmcnt(0)\n"
    "v_cmp_gt_u32 vcc, 2, v228\n"
    "s_cbranch_vccnz .Lh7r\n"
    "s_branch .Lh7g\n"
    ".Lh7g:\n"
    "ds_read_b64_tr_b16 v[152:153], v2 offset:24576\n"
    "ds_read_b64_tr_b16 v[154:155], v3 offset:24576\n"
    "ds_read_b64_tr_b16 v[156:157], v2 offset:25600\n"
    "ds_read_b64_tr_b16 v[158:159], v3 offset:25600\n"
    "ds_read_b64_tr_b16 v[160:161], v2 offset:26624\n"
    "ds_read_b64_tr_b16 v[162:163], v3 offset:26624\n"
    "ds_read_b64_tr_b16 v[164:165], v2 offset:27648\n"
    "ds_read_b64_tr_b16 v[166:167], v3 offset:27648\n"
    "ds_read_b64_tr_b16 v[168:169], v2 offset:28672\n"
    "ds_read_b64_tr_b16 v[170:171], v3 offset:28672\n"
    "ds_read_b64_tr_b16 v[172:173], v2 offset:29696\n"
    "ds_read_b64_tr_b16 v[174:175], v3 offset:29696\n"
    "ds_read_b32 v252, v1 offset:64\n"
    "v_mov_b32 v227, 5\n"
    "ds_write_b32 v226, v227 offset:32\n"
    "s_branch .Lh7j\n"
    ".Lh8:\n"
    "v_mfma_f32_16x16x32_bf16 v[216:219], v[104:107], v[152:155], 0\n"
    "buffer_store_dwordx4 v[200:203], v4, s[80:83], 0 offen offset:0 sc1\n"
    "v_mfma_f32_16x16x32_bf16 v[220:223], v[128:131], v[152:155], 0\n"
    "v_mul_f32 v208, v208, v252\n"
    "v_mul_f32 v209, v209, v252\n"
    "v_mfma_f32_16x16x32_bf16 v[216:219], v[108:111], v[156:159], v[216:219]\n"
    "v_mul_f32 v210, v210, v252\n"
    "v_mul_f32 v211, v211, v252\n"
    "v_mfma_f32_16x16x32_bf16 v[220:223], v[132:135], v[156:159], v[220:223]\n"
    "v_mul_f32 v212, v212, v252\n"
    "v_mul_f32 v213, v213, v252\n"
    "v_mfma_f32_16x16x32_bf16 v[216:219], v[112:115], v[160:163], v[216:219]\n"
    "v_mul_f32 v214, v214, v252\n"
    "v_mul_f32 v215, v215, v252\n"
    "v_mfma_f32_16x16x32_bf16 v[220:223], v[136:139], v[160:163], v[220:223]\n"
    "ds_read_b32 v228, v224 offset:144\n"
    "v_cvt_pk_bf16_f32 v208, v208, v209\n"
    "v_mfma_f32_16x16x32_bf16 v[216:219], v[116:119], v[164:167], v[216:219]\n"
    "v_cvt_pk_bf16_f32 v209, v210, v211\n"
    "v_cvt_pk_bf16_f32 v210, v212, v213\n"
    "v_mfma_f32_16x16x32_bf16 v[220:223], v[140:143], v[164:167], v[220:223]\n"
    "v_cvt_pk_bf16_f32 v211, v214, v215\n"
    "v_mfma_f32_16x16x32_bf16 v[216:219], v[120:123], v[168:171], v[216:219]\n"
    "buffer_store_dwordx4 v[208:211], v4, s[80:83], 0 offen offset:64 sc1\n"
    "v_mfma_f32_16x16x32_bf16 v[220:223], v[144:147], v[168:171], v[220:223]\n"
    "v_mfma_f32_16x16x32_bf16 v[216:219], v[124:127], v[172:175], v[216:219]\n"
    "v_mfma_f32_16x16x32_bf16 v[220:223], v[148:151], v[172:175], v[220:223]\n"
    "s_waitcnt lgkmcnt(0)\n"
    "v_cmp_gt_u32 vcc, 2, v228\n"
    "s_cbranch_vccz .Lh8g\n"
    ".Lh8r:\n"
    "s_sub_u32 s52, s52, 1\n"
    "s_cmp_lt_i32 s52, 0\n"
    "s_cbranch_scc1 .Lh8g\n"
    "ds_read_b32 v228, v224 offset:144\n"
    "s_waitcnt lgkmcnt(0)\n"
    "v_cmp_gt_u32 vcc, 2, v228\n"
    "s_cbranch_vccnz .Lh8r\n"
    "s_branch .Lh8g\n"
    ".Lh8g:\n"
    "ds_read_b64_tr_b16 v[176:177], v2 offset:30720\n"
    "ds_read_b64_tr_b16 v[178:179], v3 offset:30720\n"
    "ds_read_b64_tr_b16 v[180:181], v2 offset:31744\n"
    "ds_read_b64_tr_b16 v[182:183], v3 offset:31744\n"
    "ds_read_b64_tr_b16 v[184:185], v2 offset:32768\n"
    "ds_read_b64_tr_b16 v[186:187], v3 offset:32768\n"
    "ds_read_b64_tr_b16 v[188:189], v2 offset:33792\n"
    "ds_read_b64_tr_b16 v[190:191], v3 offset:33792\n"
    "ds_read_b64_tr_b16 v[192:193], v2 offset:34816\n"
    "ds_read_b64_tr_b16 v[194:195], v3 offset:34816\n"
    "ds_read_b64_tr_b16 v[196:197], v2 offset:35840\n"
    "ds_read_b64_tr_b16 v[198:199], v3 offset:35840\n"
    "ds_read_b32 v253, v1 offset:128\n"
    "v_mov_b32 v227, 6\n"
    "ds_write_b32 v226, v227 offset:32\n"
    "s_branch .Lh8j\n"
    ".Lh9:\n"
    "v_mfma_f32_16x16x32_bf16 v[216:219], v[104:107], v[176:179], 0\n"
    "buffer_store_dwordx4 v[200:203], v5, s[80:83], 0 offen offset:0 sc1\n"
    "v_mfma_f32_16x16x32_bf16 v[220:223], v[128:131], v[176:179], 0\n"
    "v_mul_f32 v208, v208, v253\n"
    "v_mul_f32 v209, v209, v253\n"
    "v_mfma_f32_16x16x32_bf16 v[216:219], v[108:111], v[180:183], v[216:219]\n"
    "v_mul_f32 v210, v210, v253\n"
    "v_mul_f32 v211, v211, v253\n"
    "v_mfma_f32_16x16x32_bf16 v[220:223], v[132:135], v[180:183], v[220:223]\n"
    "v_mul_f32 v212, v212, v253\n"
    "v_mul_f32 v213, v213, v253\n"
    "v_mfma_f32_16x16x32_bf16 v[216:219], v[112:115], v[184:187], v[216:219]\n"
    "v_mul_f32 v214, v214, v253\n"
    "v_mul_f32 v215, v215, v253\n"
    "v_mfma_f32_16x16x32_bf16 v[220:223], v[136:139], v[184:187], v[220:223]\n"
    "ds_read_b32 v228, v224 offset:160\n"
    "v_cvt_pk_bf16_f32 v208, v208, v209\n"
    "v_mfma_f32_16x16x32_bf16 v[216:219], v[116:119], v[188:191], v[216:219]\n"
    "v_cvt_pk_bf16_f32 v209, v210, v211\n"
    "v_cvt_pk_bf16_f32 v210, v212, v213\n"
    "v_mfma_f32_16x16x32_bf16 v[220:223], v[140:143], v[188:191], v[220:223]\n"
    "v_cvt_pk_bf16_f32 v211, v214, v215\n"
    "v_mfma_f32_16x16x32_bf16 v[216:219], v[120:123], v[192:195], v[216:219]\n"
    "buffer_store_dwordx4 v[208:211], v5, s[80:83], 0 offen offset:64 sc1\n"
    "v_mfma_f32_16x16x32_bf16 v[220:223], v[144:147], v[192:195], v[220:223]\n"
    "v_mfma_f32_16x16x32_bf16 v[216:219], v[124:127], v[196:199], v[216:219]\n"
    "v_mfma_f32_16x16x32_bf16 v[220:223], v[148:151], v[196:199], v[220:223]\n"
    "s_waitcnt lgkmcnt(0)\n"
    "v_cmp_gt_u32 vcc, 2, v228\n"
    "s_cbranch_vccz .Lh9g\n"
    ".Lh9r:\n"
    "s_sub_u32 s52, s52, 1\n"
    "s_cmp_lt_i32 s52, 0\n"
    "s_cbranch_scc1 .Lh9g\n"
    "ds_read_b32 v228, v224 offset:160\n"
    "s_waitcnt lgkmcnt(0)\n"
    "v_cmp_gt_u32 vcc, 2, v228\n"
    "s_cbranch_vccnz .Lh9r\n"
    "s_branch .Lh9g\n"
    ".Lh9g:\n"
    "ds_read_b64_tr_b16 v[152:153], v2 offset:18432\n"
    "ds_read_b64_tr_b16 v[154:155], v3 offset:18432\n"
    "ds_read_b64_tr_b16 v[156:157], v2 offset:19456\n"
    "ds_read_b64_tr_b16 v[158:159], v3 offset:19456\n"
    "ds_read_b64_tr_b16 v[160:161], v2 offset:20480\n"
    "ds_read_b64_tr_b16 v[162:163], v3 offset:20480\n"
    "ds_read_b64_tr_b16 v[164:165], v2 offset:21504\n"
    "ds_read_b64_tr_b16 v[166:167], v3 offset:21504\n"
    "ds_read_b64_tr_b16 v[168:169], v2 offset:22528\n"
    "ds_read_b64_tr_b16 v[170:171], v3 offset:22528\n"
    "ds_read_b64_tr_b16 v[172:173], v2 offset:23552\n"
    "ds_read_b64_tr_b16 v[174:175], v3 offset:23552\n"
    "ds_read_b32 v252, v1 offset:0\n"
    "v_mov_b32 v227, 7\n"
    "ds_write_b32 v226, v227 offset:32\n"
    "s_branch .Lh9j\n"
    ".Lh10:\n"
    "v_mfma_f32_16x16x32_bf16 v[216:219], v[104:107], v[152:155], 0\n"
    "buffer_store_dwordx4 v[200:203], v4, s[80:83], 0 offen offset:0 sc1\n"
    "v_mfma_f32_16x16x32_bf16 v[220:223], v[128:131], v[152:155], 0\n"
    "v_mul_f32 v208, v208, v252\n"
    "v_mul_f32 v209, v209, v252\n"
    "v_mfma_f32_16x16x32_bf16 v[216:219], v[108:111], v[156:159], v[216:219]\n"
    "v_mul_f32 v210, v210, v252\n"
    "v_mul_f32 v211, v211, v252\n"
    "v_mfma_f32_16x16x32_bf16 v[220:223], v[132:135], v[156:159], v[220:223]\n"
    "v_mul_f32 v212, v212, v252\n"
    "v_mul_f32 v213, v213, v252\n"
    "v_mfma_f32_16x16x32_bf16 v[216:219], v[112:115], v[160:163], v[216:219]\n"
    "v_mul_f32 v214, v214, v252\n"
    "v_mul_f32 v215, v215, v252\n"
    "v_mfma_f32_16x16x32_bf16 v[220:223], v[136:139], v[160:163], v[220:223]\n"
    "ds_read_b32 v228, v224 offset:176\n"
    "v_cvt_pk_bf16_f32 v208, v208, v209\n"
    "v_mfma_f32_16x16x32_bf16 v[216:219], v[116:119], v[164:167], v[216:219]\n"
    "v_cvt_pk_bf16_f32 v209, v210, v211\n"
    "v_cvt_pk_bf16_f32 v210, v212, v213\n"
    "v_mfma_f32_16x16x32_bf16 v[220:223], v[140:143], v[164:167], v[220:223]\n"
    "v_cvt_pk_bf16_f32 v211, v214, v215\n"
    "v_mfma_f32_16x16x32_bf16 v[216:219], v[120:123], v[168:171], v[216:219]\n"
    "buffer_store_dwordx4 v[208:211], v4, s[80:83], 0 offen offset:64 sc1\n"
    "v_mfma_f32_16x16x32_bf16 v[220:223], v[144:147], v[168:171], v[220:223]\n"
    "v_mfma_f32_16x16x32_bf16 v[216:219], v[124:127], v[172:175], v[216:219]\n"
    "v_mfma_f32_16x16x32_bf16 v[220:223], v[148:151], v[172:175], v[220:223]\n"
    "s_waitcnt lgkmcnt(0)\n"
    "v_cmp_gt_u32 vcc, 2, v228\n"
    "s_cbranch_vccz .Lh10g\n"
    ".Lh10r:\n"
    "s_sub_u32 s52, s52, 1\n"
    "s_cmp_lt_i32 s52, 0\n"
    "s_cbranch_scc1 .Lh10g\n"
    "ds_read_b32 v228, v224 offset:176\n"
    "s_waitcnt lgkmcnt(0)\n"
    "v_cmp_gt_u32 vcc, 2, v228\n"
    "s_cbranch_vccnz .Lh10r\n"
    "s_branch .Lh10g\n"
    ".Lh10g:\n"
    "ds_read_b64_tr_b16 v[176:177], v2 offset:24576\n"
    "ds_read_b64_tr_b16 v[178:179], v3 offset:24576\n"
    "ds_read_b64_tr_b16 v[180:181], v2 offset:25600\n"
    "ds_read_b64_tr_b16 v[182:183], v3 offset:25600\n"
    "ds_read_b64_tr_b16 v[184:185], v2 offset:26624\n"
    "ds_read_b64_tr_b16 v[186:187], v3 offset:26624\n"
    "ds_read_b64_tr_b16 v[188:189], v2 offset:27648\n"
    "ds_read_b64_tr_b16 v[190:191], v3 offset:27648\n"
    "ds_read_b64_tr_b16 v[192:193], v2 offset:28672\n"
    "ds_read_b64_tr_b16 v[194:195], v3 offset:28672\n"
    "ds_read_b64_tr_b16 v[196:197], v2 offset:29696\n"
    "ds_read_b64_tr_b16 v[198:199], v3 offset:29696\n"
    "ds_read_b32 v253, v1 offset:64\n"
    "s_branch .Lh10j\n"
    ".Lh11:\n"
    "v_mfma_f32_16x16x32_bf16 v[216:219], v[104:107], v[176:179], 0\n"
    "buffer_store_dwordx4 v[200:203], v5, s[80:83], 0 offen offset:0 sc1\n"
    "v_mfma_f32_16x16x32_bf16 v[220:223], v[128:131], v[176:179], 0\n"
    "v_mul_f32 v208, v208, v253\n"
    "v_mul_f32 v209, v209, v253\n"
    "v_mfma_f32_16x16x32_bf16 v[216:219], v[108:111], v[180:183], v[216:219]\n"
    "v_mul_f32 v210, v210, v253\n"
    "v_mul_f32 v211, v211, v253\n"
    "v_mfma_f32_16x16x32_bf16 v[220:223], v[132:135], v[180:183], v[220:223]\n"
    "v_mul_f32 v212, v212, v253\n"
    "v_mul_f32 v213, v213, v253\n"
    "v_mfma_f32_16x16x32_bf16 v[216:219], v[112:115], v[184:187], v[216:219]\n"
    "v_mul_f32 v214, v214, v253\n"
    "v_mul_f32 v215, v215, v253\n"
    "v_mfma_f32_16x16x32_bf16 v[220:223], v[136:139], v[184:187], v[220:223]\n"
    "ds_read_b32 v228, v224 offset:128\n"
    "v_cvt_pk_bf16_f32 v208, v208, v209\n"
    "v_mfma_f32_16x16x32_bf16 v[216:219], v[116:119], v[188:191], v[216:219]\n"
    "v_cvt_pk_bf16_f32 v209, v210, v211\n"
    "v_cvt_pk_bf16_f32 v210, v212, v213\n"
    "v_mfma_f32_16x16x32_bf16 v[220:223], v[140:143], v[188:191], v[220:223]\n"
    "v_cvt_pk_bf16_f32 v211, v214, v215\n"
    "v_mfma_f32_16x16x32_bf16 v[216:219], v[120:123], v[192:195], v[216:219]\n"
    "buffer_store_dwordx4 v[208:211], v5, s[80:83], 0 offen offset:64 sc1\n"
    "v_mfma_f32_16x16x32_bf16 v[220:223], v[144:147], v[192:195], v[220:223]\n"
    "v_mfma_f32_16x16x32_bf16 v[216:219], v[124:127], v[196:199], v[216:219]\n"
    "v_mfma_f32_16x16x32_bf16 v[220:223], v[148:151], v[196:199], v[220:223]\n"
    "s_waitcnt lgkmcnt(0)\n"
    "v_cmp_gt_u32 vcc, 3, v228\n"
    "s_cbranch_vccz .Lh11g\n"
    ".Lh11r:\n"
    "s_sub_u32 s52, s52, 1\n"
    "s_cmp_lt_i32 s52, 0\n"
    "s_cbranch_scc1 .Lh11g\n"
    "ds_read_b32 v228, v224 offset:128\n"
    "s_waitcnt lgkmcnt(0)\n"
    "v_cmp_gt_u32 vcc, 3, v228\n"
    "s_cbranch_vccnz .Lh11r\n"
    "s_branch .Lh11g\n"
    ".Lh11g:\n"
    "ds_read_b64_tr_b16 v[152:153], v2 offset:30720\n"
    "ds_read_b64_tr_b16 v[154:155], v3 offset:30720\n"
    "ds_read_b64_tr_b16 v[156:157], v2 offset:31744\n"
    "ds_read_b64_tr_b16 v[158:159], v3 offset:31744\n"
    "ds_read_b64_tr_b16 v[160:161], v2 offset:32768\n"
    "ds_read_b64_tr_b16 v[162:163], v3 offset:32768\n"
    "ds_read_b64_tr_b16 v[164:165], v2 offset:33792\n"
    "ds_read_b64_tr_b16 v[166:167], v3 offset:33792\n"
    "ds_read_b64_tr_b16 v[168:169], v2 offset:34816\n"
    "ds_read_b64_tr_b16 v[170:171], v3 offset:34816\n"
    "ds_read_b64_tr_b16 v[172:173], v2 offset:35840\n"
    "ds_read_b64_tr_b16 v[174:175], v3 offset:35840\n"
    "ds_read_b32 v252, v1 offset:128\n"
    "s_branch .Lh11j\n"
    ".Lh12:\n"
    "v_mfma_f32_16x16x32_bf16 v[216:219], v[104:107], v[152:155], 0\n"
    "buffer_store_dwordx4 v[200:203], v4, s[80:83], 0 offen offset:0 sc1\n"
    "v_mfma_f32_16x16x32_bf16 v[220:223], v[128:131], v[152:155], 0\n"
    "v_mul_f32 v208, v208, v252\n"
    "v_mul_f32 v209, v209, v252\n"
    "v_mfma_f32_16x16x32_bf16 v[216:219], v[108:111], v[156:159], v[216:219]\n"
    "v_mul_f32 v210, v210, v252\n"
    "v_mul_f32 v211, v211, v252\n"
    "v_mfma_f32_16x16x32_bf16 v[220:223], v[132:135], v[156:159], v[220:223]\n"
    "v_mul_f32 v212, v212, v252\n"
    "v_mul_f32 v213, v213, v252\n"
    "v_mfma_f32_16x16x32_bf16 v[216:219], v[112:115], v[160:163], v[216:219]\n"
    "v_mul_f32 v214, v214, v252\n"
    "v_mul_f32 v215, v215, v252\n"
    "v_mfma_f32_16x16x32_bf16 v[220:223], v[136:139], v[160:163], v[220:223]\n"
    "ds_read_b32 v228, v224 offset:144\n"
    "v_cvt_pk_bf16_f32 v208, v208, v209\n"
    "v_mfma_f32_16x16x32_bf16 v[216:219], v[116:119], v[164:167], v[216:219]\n"
    "v_cvt_pk_bf16_f32 v209, v210, v211\n"
    "v_cvt_pk_bf16_f32 v210, v212, v213\n"
    "v_mfma_f32_16x16x32_bf16 v[220:223], v[140:143], v[164:167], v[220:223]\n"
    "v_cvt_pk_bf16_f32 v211, v214, v215\n"
    "v_mfma_f32_16x16x32_bf16 v[216:219], v[120:123], v[168:171], v[216:219]\n"
    "buffer_store_dwordx4 v[208:211], v4, s[80:83], 0 offen offset:64 sc1\n"
    "v_mfma_f32_16x16x32_bf16 v[220:223], v[144:147], v[168:171], v[220:223]\n"
    "v_mfma_f32_16x16x32_bf16 v[216:219], v[124:127], v[172:175], v[216:219]\n"
    "v_mfma_f32_16x16x32_bf16 v[220:223], v[148:151], v[172:175], v[220:223]\n"
    "s_waitcnt lgkmcnt(0)\n"
    "v_cmp_gt_u32 vcc, 3, v228\n"
    "s_cbranch_vccz .Lh12g\n"
    ".Lh12r:\n"
    "s_sub_u32 s52, s52, 1\n"
    "s_cmp_lt_i32 s52, 0\n"
    "s_cbranch_scc1 .Lh12g\n"
    "ds_read_b32 v228, v224 offset:144\n"
    "s_waitcnt lgkmcnt(0)\n"
    "v_cmp_gt_u32 vcc, 3, v228\n"
    "s_cbranch_vccnz .Lh12r\n"
    "s_branch .Lh12g\n"
    ".Lh12g:\n"
    "ds_read_b64_tr_b16 v[176:177], v2 offset:18432\n"
    "ds_read_b64_tr_b16 v[178:179], v3 offset:18432\n"
    "ds_read_b64_tr_b16 v[180:181], v2 offset:19456\n"
    "ds_read_b64_tr_b16 v[182:183], v3 offset:19456\n"
    "ds_read_b64_tr_b16 v[184:185], v2 offset:20480\n"
    "ds_read_b64_tr_b16 v[186:187], v3 offset:20480\n"
    "ds_read_b64_tr_b16 v[188:189], v2 offset:21504\n"
    "ds_read_b64_tr_b16 v[190:191], v3 offset:21504\n"
    "ds_read_b64_tr_b16 v[192:193], v2 offset:22528\n"
    "ds_read_b64_tr_b16 v[194:195], v3 offset:22528\n"
    "ds_read_b64_tr_b16 v[196:197], v2 offset:23552\n"
    "ds_read_b64_tr_b16 v[198:199], v3 offset:23552\n"
    "ds_read_b32 v253, v1 offset:0\n"
    "s_branch .Lh12j\n"
    ".Lh13:\n"
    "v_mfma_f32_16x16x32_bf16 v[208:211], v[56:59], v[176:179], 0\n"
    "v_mfma_f32_16x16x32_bf16 v[212:215], v[80:83], v[176:179], 0\n"
    "v_mfma_f32_16x16x32_bf16 v[208:211], v[60:63], v[180:183], v[208:211]\n"
    "v_mfma_f32_16x16x32_bf16 v[212:215], v[84:87], v[180:183], v[212:215]\n"
    "v_mul_f32 v200, v200, v253\n"
    "v_mul_f32 v201, v201, v253\n"
    "v_mfma_f32_16x16x32_bf16 v[208:211], v[64:67], v[184:187], v[208:211]\n"
    "v_mul_f32 v202, v202, v253\n"
    "v_mul_f32 v203, v203, v253\n"
    "v_mfma_f32_16x16x32_bf16 v[212:215], v[88:91], v[184:187], v[212:215]\n"
    "ds_read_b32 v228, v224 offset:160\n"
    "v_mul_f32 v204, v204, v253\n"
    "v_mfma_f32_16x16x32_bf16 v[208:211], v[68:71], v[188:191], v[208:211]\n"
    "v_mul_f32 v205, v205, v253\n"
    "v_mul_f32 v206, v206, v253\n"
    "v_mfma_f32_16x16x32_bf16 v[212:215], v[92:95], v[188:191], v[212:215]\n"
    "v_mul_f32 v207, v207, v253\n"
    "v_cvt_pk_bf16_f32 v200, v200, v201\n"
    "v_mfma_f32_16x16x32_bf16 v[208:211], v[72:75], v[192:195], v[208:211]\n"
    "v_cvt_pk_bf16_f32 v201, v202, v203\n"
    "v_cvt_pk_bf16_f32 v202, v204, v205\n"
    "v_mfma_f32_16x16x32_bf16 v[212:215], v[96:99], v[192:195], v[212:215]\n"
    "v_cvt_pk_bf16_f32 v203, v206, v207\n"
    "v_mfma_f32_16x16x32_bf16 v[208:211], v[76:79], v[196:199], v[208:211]\n"
    "buffer_store_dwordx4 v[200:203], v5, s[80:83], 0 offen offset:0 sc1\n"
    "v_mfma_f32_16x16x32_bf16 v[212:215], v[100:103], v[196:199], v[212:215]\n"
    "s_waitcnt lgkmcnt(0)\n"
    "v_cmp_gt_u32 vcc, 3, v228\n"
    "s_cbranch_vccz .Lh13g\n"
    ".Lh13r:\n"
    "s_sub_u32 s52, s52, 1\n"
    "s_cmp_lt_i32 s52, 0\n"
    "s_cbranch_scc1 .Lh13g\n"
    "ds_read_b32 v228, v224 offset:160\n"
    "s_waitcnt lgkmcnt(0)\n"
    "v_cmp_gt_u32 vcc, 3, v228\n"
    "s_cbranch_vccnz .Lh13r\n"
    "s_branch .Lh13g\n"
    ".Lh13g:\n"
    "ds_read_b64_tr_b16 v[152:153], v2 offset:6144\n"
    "ds_read_b64_tr_b16 v[154:155], v3 offset:6144\n"
    "ds_read_b64_tr_b16 v[156:157], v2 offset:7168\n"
    "ds_read_b64_tr_b16 v[158:159], v3 offset:7168\n"
    "ds_read_b64_tr_b16 v[160:161], v2 offset:8192\n"
    "ds_read_b64_tr_b16 v[162:163], v3 offset:8192\n"
    "ds_read_b64_tr_b16 v[164:165], v2 offset:9216\n"
    "ds_read_b64_tr_b16 v[166:167], v3 offset:9216\n"
    "ds_read_b64_tr_b16 v[168:169], v2 offset:10240\n"
    "ds_read_b64_tr_b16 v[170:171], v3 offset:10240\n"
    "ds_read_b64_tr_b16 v[172:173], v2 offset:11264\n"
    "ds_read_b64_tr_b16 v[174:175], v3 offset:11264\n"
    "ds_read_b32 v252, v1 offset:256\n"
    "s_branch .Lh13j\n"
    ".Lh14:\n"
    "v_mfma_f32_16x16x32_bf16 v[208:211], v[56:59], v[152:155], 0\n"
    "v_mfma_f32_16x16x32_bf16 v[212:215], v[80:83], v[152:155], 0\n"
    "v_mfma_f32_16x16x32_bf16 v[208:211], v[60:63], v[156:159], v[208:211]\n"
    "v_mfma_f32_16x16x32_bf16 v[212:215], v[84:87], v[156:159], v[212:215]\n"
    "v_mul_f32 v200, v200, v252\n"
    "v_mul_f32 v201, v201, v252\n"
    "v_mfma_f32_16x16x32_bf16 v[208:211], v[64:67], v[160:163], v[208:211]\n"
    "v_mul_f32 v202, v202, v252\n"
    "v_mul_f32 v203, v203, v252\n"
    "v_mfma_f32_16x16x32_bf16 v[212:215], v[88:91], v[160:163], v[212:215]\n"
    "ds_read_b32 v228, v224 offset:176\n"
    "v_mul_f32 v204, v204, v252\n"
    "v_mfma_f32_16x16x32_bf16 v[208:211], v[68:71], v[164:167], v[208:211]\n"
    "v_mul_f32 v205, v205, v252\n"
    "v_mul_f32 v206, v206, v252\n"
    "v_mfma_f32_16x16x32_bf16 v[212:215], v[92:95], v[164:167], v[212:215]\n"
    "v_mul_f32 v207, v207, v252\n"
    "v_cvt_pk_bf16_f32 v200, v200, v201\n"
    "v_mfma_f32_16x16x32_bf16 v[208:211], v[72:75], v[168:171], v[208:211]\n"
    "v_cvt_pk_bf16_f32 v201, v202, v203\n"
    "v_cvt_pk_bf16_f32 v202, v204, v205\n"
    "v_mfma_f32_16x16x32_bf16 v[212:215], v[96:99], v[168:171], v[212:215]\n"
    "v_cvt_pk_bf16_f32 v203, v206, v207\n"
    "v_mfma_f32_16x16x32_bf16 v[208:211], v[76:79], v[172:175], v[208:211]\n"
    "buffer_store_dwordx4 v[200:203], v4, s[80:83], 0 offen offset:0 sc1\n"
    "v_mfma_f32_16x16x32_bf16 v[212:215], v[100:103], v[172:175], v[212:215]\n"
    "s_waitcnt lgkmcnt(0)\n"
    "v_cmp_gt_u32 vcc, 3, v228\n"
    "s_cbranch_vccz .Lh14g\n"
    ".Lh14r:\n"
    "s_sub_u32 s52, s52, 1\n"
    "s_cmp_lt_i32 s52, 0\n"
    "s_cbranch_scc1 .Lh14g\n"
    "ds_read_b32 v228, v224 offset:176\n"
    "s_waitcnt lgkmcnt(0)\n"
    "v_cmp_gt_u32 vcc, 3, v228\n"
    "s_cbranch_vccnz .Lh14r\n"
    "s_branch .Lh14g\n"
    ".Lh14g:\n"
    "ds_read_b64_tr_b16 v[176:177], v2 offset:12288\n"
    "ds_read_b64_tr_b16 v[178:179], v3 offset:12288\n"
    "ds_read_b64_tr_b16 v[180:181], v2 offset:13312\n"
    "ds_read_b64_tr_b16 v[182:183], v3 offset:13312\n"
    "ds_read_b64_tr_b16 v[184:185], v2 offset:14336\n"
    "ds_read_b64_tr_b16 v[186:187], v3 offset:14336\n"
    "ds_read_b64_tr_b16 v[188:189], v2 offset:15360\n"
    "ds_read_b64_tr_b16 v[190:191], v3 offset:15360\n"
    "ds_read_b64_tr_b16 v[192:193], v2 offset:16384\n"
    "ds_read_b64_tr_b16 v[194:195], v3 offset:16384\n"
    "ds_read_b64_tr_b16 v[196:197], v2 offset:17408\n"
    "ds_read_b64_tr_b16 v[198:199], v3 offset:17408\n"
    "ds_read_b32 v253, v1 offset:320\n"
    "s_branch .Lh14j\n"
    ".Lh_e12:\n"
    "s_waitcnt lgkmcnt(1)\n"
    "v_mfma_f32_16x16x32_bf16 v[200:203], v[8:11], v[176:179], 0\n"
    "v_mul_f32 v208, v208, v252\n"
    "v_mul_f32 v209, v209, v252\n"
    "v_mfma_f32_16x16x32_bf16 v[204:207], v[32:35], v[176:179], 0\n"
    "v_mul_f32 v210, v210, v252\n"
    "v_mul_f32 v211, v211, v252\n"
    "v_mfma_f32_16x16x32_bf16 v[200:203], v[12:15], v[180:183], v[200:203]\n"
    "v_mul_f32 v212, v212, v252\n"
    "v_mul_f32 v213, v213, v252\n"
    "v_mfma_f32_16x16x32_bf16 v[204:207], v[36:39], v[180:183], v[204:207]\n"
    "v_mul_f32 v214, v214, v252\n"
    "v_mul_f32 v215, v215, v252\n"
    "v_mfma_f32_16x16x32_bf16 v[200:203], v[16:19], v[184:187], v[200:203]\n"
    "v_cvt_pk_bf16_f32 v208, v208, v209\n"
    "v_cvt_pk_bf16_f32 v209, v210, v211\n"
    "v_mfma_f32_16x16x32_bf16 v[204:207], v[40:43], v[184:187], v[204:207]\n"
    "v_cvt_pk_bf16_f32 v210, v212, v213\n"
    "v_cvt_pk_bf16_f32 v211, v214, v215\n"
    "v_mfma_f32_16x16x32_bf16 v[200:203], v[20:23], v[188:191], v[200:203]\n"
    "buffer_store_dwordx4 v[208:211], v4, s[80:83], 0 offen offset:64 sc1\n"
    "v_mfma_f32_16x16x32_bf16 v[204:207], v[44:47], v[188:191], v[204:207]\n"
    "v_mfma_f32_16x16x32_bf16 v[200:203], v[24:27], v[192:195], v[200:203]\n"
    "v_add_u32 v4, v0, v4\n"
    "v_mfma_f32_16x16x32_bf16 v[204:207], v[48:51], v[192:195], v[204:207]\n"
    "v_mfma_f32_16x16x32_bf16 v[200:203], v[28:31], v[196:199], v[200:203]\n"
    "v_mfma_f32_16x16x32_bf16 v[204:207], v[52:55], v[196:199], v[204:207]\n"
    "v_mfma_f32_16x16x32_bf16 v[208:211], v[56:59], v[176:179], 0\n"
    "v_mfma_f32_16x16x32_bf16 v[212:215], v[80:83], v[176:179], 0\n"
    "v_mfma_f32_16x16x32_bf16 v[208:211], v[60:63], v[180:183], v[208:211]\n"
    "v_mfma_f32_16x16x32_bf16 v[212:215], v[84:87], v[180:183], v[212:215]\n"
    "v_mfma_f32_16x16x32_bf16 v[208:211], v[64:67], v[184:187], v[208:211]\n"
    "v_mfma_f32_16x16x32_bf16 v[212:215], v[88:91], v[184:187], v[212:215]\n"
    "s_waitcnt lgkmcnt(0)\n"
    "v_mul_f32 v200, v200, v253\n"
    "v_mfma_f32_16x16x32_bf16 v[208:211], v[68:71], v[188:191], v[208:211]\n"
    "v_mul_f32 v201, v201, v253\n"
    "v_mul_f32 v202, v202, v253\n"
    "v_mfma_f32_16x16x32_bf16 v[212:215], v[92:95], v[188:191], v[212:215]\n"
    "v_mul_f32 v203, v203, v253\n"
    "v_mul_f32 v204, v204, v253\n"
    "v_mfma_f32_16x16x32_bf16 v[208:211], v[72:75], v[192:195], v[208:211]\n"
    "v_mul_f32 v205, v205, v253\n"
    "v_mul_f32 v206, v206, v253\n"
    "v_mfma_f32_16x16x32_bf16 v[212:215], v[96:99], v[192:195], v[212:215]\n"
    "v_mul_f32 v207, v207, v253\n"
    "v_cvt_pk_bf16_f32 v200, v200, v201\n"
    "v_mfma_f32_16x16x32_bf16 v[208:211], v[76:79], v[196:199], v[208:211]\n"
    "v_cvt_pk_bf16_f32 v201, v202, v203\n"
    "v_cvt_pk_bf16_f32 v202, v204, v205\n"
    "v_mfma_f32_16x16x32_bf16 v[212:215], v[100:103], v[196:199], v[212:215]\n"
    "v_cvt_pk_bf16_f32 v203, v206, v207\n"
    "buffer_store_dwordx4 v[200:203], v5, s[80:83], 0 offen offset:0 sc1\n"
    "s_nop 2\n"
    "v_mul_f32 v208, v208, v253\n"
    "v_mul_f32 v209, v209, v253\n"
    "v_mul_f32 v210, v210, v253\n"
    "v_mul_f32 v211, v211, v253\n"
    "v_mul_f32 v212, v212, v253\n"
    "v_mul_f32 v213, v213, v253\n"
    "v_mul_f32 v214, v214, v253\n"
    "v_mul_f32 v215, v215, v253\n"
    "v_cvt_pk_bf16_f32 v208, v208, v209\n"
    "v_cvt_pk_bf16_f32 v209, v210, v211\n"
    "v_cvt_pk_bf16_f32 v210, v212, v213\n"
    "v_cvt_pk_bf16_f32 v211, v214, v215\n"
    "buffer_store_dwordx4 v[208:211], v5, s[80:83], 0 offen offset:64 sc1\n"
    "s_branch .Lh_end\n"
    ".Lh15:\n"
    "v_mfma_f32_16x16x32_bf16 v[208:211], v[56:59], v[176:179], 0\n"
    "v_mfma_f32_16x16x32_bf16 v[212:215], v[80:83], v[176:179], 0\n"
    "v_mfma_f32_16x16x32_bf16 v[208:211], v[60:63], v[180:183], v[208:211]\n"
    "v_mfma_f32_16x16x32_bf16 v[212:215], v[84:87], v[180:183], v[212:215]\n"
    "v_mul_f32 v200, v200, v253\n"
    "v_mul_f32 v201, v201, v253\n"
    "v_mfma_f32_16x16x32_bf16 v[208:211], v[64:67], v[184:187], v[208:211]\n"
    "v_mul_f32 v202, v202, v253\n"
    "v_mul_f32 v203, v203, v253\n"
    "v_mfma_f32_16x16x32_bf16 v[212:215], v[88:91], v[184:187], v[212:215]\n"
    "ds_read_b32 v228, v224 offset:832\n"
    "v_mul_f32 v204, v204, v253\n"
    "v_mfma_f32_16x16x32_bf16 v[208:211], v[68:71], v[188:191], v[208:211]\n"
    "v_mul_f32 v205, v205, v253\n"
    "v_mul_f32 v206, v206, v253\n"
    "v_mfma_f32_16x16x32_bf16 v[212:215], v[92:95], v[188:191], v[212:215]\n"
    "v_mul_f32 v207, v207, v253\n"
    "v_cvt_pk_bf16_f32 v200, v200, v201\n"
    "v_mfma_f32_16x16x32_bf16 v[208:211], v[72:75], v[192:195], v[208:211]\n"
    "v_cvt_pk_bf16_f32 v201, v202, v203\n"
    "v_cvt_pk_bf16_f32 v202, v204, v205\n"
    "v_mfma_f32_16x16x32_bf16 v[212:215], v[96:99], v[192:195], v[212:215]\n"
    "v_cvt_pk_bf16_f32 v203, v206, v207\n"
    "v_mfma_f32_16x16x32_bf16 v[208:211], v[76:79], v[196:199], v[208:211]\n"
    "buffer_store_dwordx4 v[200:203], v5, s[80:83], 0 offen offset:0 sc1\n"
    "v_mfma_f32_16x16x32_bf16 v[212:215], v[100:103], v[196:199], v[212:215]\n"
    "s_waitcnt lgkmcnt(0)\n"
    "v_cmp_gt_u32 vcc, 4, v228\n"
    "s_cbranch_vccz .Lh15g\n"
    ".Lh15r:\n"
    "s_sub_u32 s52, s52, 1\n"
    "s_cmp_lt_i32 s52, 0\n"
    "s_cbranch_scc1 .Lh15g\n"
    "ds_read_b32 v228, v224 offset:832\n"
    "s_waitcnt lgkmcnt(0)\n"
    "v_cmp_gt_u32 vcc, 4, v228\n"
    "s_cbranch_vccnz .Lh15r\n"
    "s_branch .Lh15g\n"
    ".Lh15g:\n"
    "ds_read_b64_tr_b16 v[152:153], v2 offset:0\n"
    "ds_read_b64_tr_b16 v[154:155], v3 offset:0\n"
    "ds_read_b64_tr_b16 v[156:157], v2 offset:1024\n"
    "ds_read_b64_tr_b16 v[158:159], v3 offset:1024\n"
    "ds_read_b64_tr_b16 v[160:161], v2 offset:2048\n"
    "ds_read_b64_tr_b16 v[162:163], v3 offset:2048\n"
    "ds_read_b64_tr_b16 v[164:165], v2 offset:3072\n"
    "ds_read_b64_tr_b16 v[166:167], v3 offset:3072\n"
    "ds_read_b64_tr_b16 v[168:169], v2 offset:4096\n"
    "ds_read_b64_tr_b16 v[170:171], v3 offset:4096\n"
    "ds_read_b64_tr_b16 v[172:173], v2 offset:5120\n"
    "ds_read_b64_tr_b16 v[174:175], v3 offset:5120\n"
    "ds_read_b32 v252, v1 offset:192\n"
    "s_branch .Lh15j\n"
    ".Lh_last:\n"
                :: "{v1}"(vi), "{v[2:3]}"(vz), "{v[4:7]}"(vo), "{v[224:227]}"(fl), "{v235}"(lf[0]), "{v236}"(lf[1]),
                   "{v[238:239]}"(xp), "{s[40:41]}"(ws), "{s[42:43]}"(xb), "{v[230:233]}"(xo), "{v234}"(xo4),
                   "{s84}"(UNI(axis_at(rm, 12) * IROWB)), "{s85}"(UNI(axis_at(rm, 13) * IROWB)),
                   "{s46}"(UNI(2 * rm.d * IROWB)), "{v0}"(d56), "{s48}"(UNI(WS_ACK + (2 * bid + h) * 128)),
                   "{s50}"(h), "{s58}"(UNI(t12)), "{s59}"(UNI(t0 + (bid & 4 ? 4200 : 5000))),
                   "{s87}"(UNI(t0 + 7000 - (bid & 4 ? 5000 - 4200 : 0))), "{v[248:249]}"(uu), "{s[80:83]}"(srd)
                : "memory", "v255", "s51", "s52", "s53", "s54", "s55", "s56", "s57", "s60", "s61", "s76", "s77", "s79", "s88", "s89", "s90", "s91", "s98", "s99", "s92", "s93", "s94", "s95", "vcc", "scc");
        }
    } else {
        const int a = (0x3210 >> (4 * wave - 16)) & 3, H = a & 1, n0 = a, u0 = a >> 1;
        const AttC c = att_const(lane, H, cedge);
        f32x4 cbM, cbF, cbL;
        const float NEG = -__builtin_inff();
#pragma unroll
        for (int r = 0; r < 4; ++r) {
            const bool ok = (c.pk >> (4 + r)) & 1;
            cbM[r] = ok ? 0.f : NEG;
            cbF[r] = ok && e == 0 ? 0.f : NEG;
            cbL[r] = ok && e == 1 ? 0.f : NEG;
        }
        const int sl = n0 >= 3 ? n0 - 3 : n0;
        const u32x4 va = {c.sb, c.v0, c.v1, (unsigned)(LQ + sl * 6144 + lane * 16)};
        const u32x4 vb = {(unsigned)(LZ + sl * 6144 + (16 * g + (i16 ^ (4 * g))) * 16), (c.pk & 15) * 0x1111u, vf, lane < 4 ? vf + 16 * a : OOB};
        const u64 slots = (redge ? 0x975A86420ull : 0x420CA86420ull) >> (4 * u0);
        asm volatile("s_nop 7\n"
    "s_sub_u32 s56, s45, 2\n"
    "s_setprio 0\n"
    "s_cmp_lg_u32 s70, 0\n"
    "s_cbranch_scc1 .La_ld\n"
    ".La_join:\n"
    "s_mov_b32 s52, 0x40000\n"
    "s_bfe_u32 s48, s60, 0x40000\n"
    "s_bfe_u32 s49, s60, 0x40004\n"
    "s_bfe_u32 s50, s60, 0x40008\n"
    "s_bfe_u32 s51, s60, 0x4000c\n"
    "s_mul_i32 s48, s48, 8896\n"
    "s_mul_i32 s49, s49, 8896\n"
    "s_mul_i32 s50, s50, 8896\n"
    "s_mul_i32 s51, s51, 8896\n"
    "s_lshr_b64 s[60:61], s[60:61], 8\n"
    "v_add_u32 v244, s48, v0\n"
    "v_add_u32 v245, s49, v0\n"
    "v_add_u32 v246, s50, v0\n"
    "v_add_u32 v247, s51, v0\n"
    "v_add_u32 v200, s48, v1\n"
    "v_add_u32 v201, s49, v1\n"
    "v_add_u32 v202, s48, v2\n"
    "v_add_u32 v203, s49, v2\n"
    "s_mov_b32 s69, 1\n"
    "s_mov_b32 s53, 0\n"
    "s_add_u32 s55, s45, 1\n"
    "s_lshr_b32 s57, s45, 2\n"
    "v_mov_b32 v248, 0x3f803f80\n"
    "v_mov_b32 v249, 0x3f803f80\n"
    "v_mov_b32 v250, 0x3f803f80\n"
    "v_mov_b32 v251, 0x3f803f80\n"
    "s_mul_i32 s62, s57, 6\n"
    "ds_read_b32 v253, v6 offset:0\n"
    "s_waitcnt lgkmcnt(0)\n"
    "v_cmp_gt_u32 vcc, s54, v253\n"
    "s_cbranch_vccnz .La1\n"
    ".La1b:\n"
    "ds_read_b32 v254, v6 offset:16\n"
    "s_waitcnt lgkmcnt(0)\n"
    "v_cmp_gt_u32 vcc, s55, v254\n"
    "s_cbranch_vccz .La_nf\n"
    "s_mov_b32 s69, 0\n"
    "ds_read_b64_tr_b16 v[104:105], v200 offset:0\n"
    "ds_read_b64_tr_b16 v[106:107], v201 offset:0\n"
    "ds_read_b64_tr_b16 v[108:109], v202 offset:0\n"
    "ds_read_b64_tr_b16 v[110:111], v203 offset:0\n"
    "ds_read_b64_tr_b16 v[112:113], v200 offset:32\n"
    "ds_read_b64_tr_b16 v[114:115], v201 offset:32\n"
    "ds_read_b64_tr_b16 v[116:117], v202 offset:32\n"
    "ds_read_b64_tr_b16 v[118:119], v203 offset:32\n"
    "ds_read_b64_tr_b16 v[120:121], v200 offset:64\n"
    "ds_read_b64_tr_b16 v[122:123], v201 offset:64\n"
    "ds_read_b64_tr_b16 v[124:125], v202 offset:64\n"
    "ds_read_b64_tr_b16 v[126:127], v203 offset:64\n"
    "ds_read_b64_tr_b16 v[128:129], v200 offset:96\n"
    "ds_read_b64_tr_b16 v[130:131], v201 offset:96\n"
    "ds_read_b64_tr_b16 v[132:133], v202 offset:96\n"
    "ds_read_b64_tr_b16 v[134:135], v203 offset:96\n"
    "ds_read_b64_tr_b16 v[136:137], v200 offset:128\n"
    "ds_read_b64_tr_b16 v[138:139], v201 offset:128\n"
    "ds_read_b64_tr_b16 v[140:141], v202 offset:128\n"
    "ds_read_b64_tr_b16 v[142:143], v203 offset:128\n"
    "ds_read_b64_tr_b16 v[144:145], v200 offset:160\n"
    "ds_read_b64_tr_b16 v[146:147], v201 offset:160\n"
    "ds_read_b64_tr_b16 v[148:149], v202 offset:160\n"
    "ds_read_b64_tr_b16 v[150:151], v203 offset:160\n"
    "ds_read_b64_tr_b16 v[152:153], v200 offset:192\n"
    "ds_read_b64_tr_b16 v[154:155], v201 offset:192\n"
    "ds_read_b64_tr_b16 v[156:157], v202 offset:192\n"
    "ds_read_b64_tr_b16 v[158:159], v203 offset:192\n"
    "ds_read_b64_tr_b16 v[160:161], v200 offset:224\n"
    "ds_read_b64_tr_b16 v[162:163], v201 offset:224\n"
    "ds_read_b64_tr_b16 v[164:165], v202 offset:224\n"
    "ds_read_b64_tr_b16 v[166:167], v203 offset:224\n"
    "ds_read_b64_tr_b16 v[168:169], v200 offset:256\n"
    "ds_read_b64_tr_b16 v[170:171], v201 offset:256\n"
    "ds_read_b64_tr_b16 v[172:173], v202 offset:256\n"
    "ds_read_b64_tr_b16 v[174:175], v203 offset:256\n"
    "ds_read_b64_tr_b16 v[176:177], v200 offset:288\n"
    "ds_read_b64_tr_b16 v[178:179], v201 offset:288\n"
    "ds_read_b64_tr_b16 v[180:181], v202 offset:288\n"
    "ds_read_b64_tr_b16 v[182:183], v203 offset:288\n"
    "ds_read_b64_tr_b16 v[184:185], v200 offset:320\n"
    "ds_read_b64_tr_b16 v[186:187], v201 offset:320\n"
    "ds_read_b64_tr_b16 v[188:189], v202 offset:320\n"
    "ds_read_b64_tr_b16 v[190:191], v203 offset:320\n"
    "ds_read_b64_tr_b16 v[192:193], v200 offset:352\n"
    "ds_read_b64_tr_b16 v[194:195], v201 offset:352\n"
    "ds_read_b64_tr_b16 v[196:197], v202 offset:352\n"
    "ds_read_b64_tr_b16 v[198:199], v203 offset:352\n"
    "ds_read_b32 v254, v6 offset:16\n"
    "s_waitcnt lgkmcnt(0)\n"
    "v_cmp_gt_u32 vcc, s55, v254\n"
    "s_cbranch_vccnz .La2\n"
    ".La2b:\n"
    ".La_nf:\n"
    "ds_read_b128 v[8:11], v3 offset:0\n"
    "ds_read_b128 v[56:59], v244 offset:0\n"
    "ds_read_b128 v[12:15], v3 offset:1024\n"
    "ds_read_b128 v[60:63], v244 offset:64\n"
    "ds_read_b128 v[16:19], v3 offset:2048\n"
    "ds_read_b128 v[64:67], v244 offset:128\n"
    "ds_read_b128 v[20:23], v3 offset:3072\n"
    "ds_read_b128 v[68:71], v244 offset:192\n"
    "ds_read_b128 v[24:27], v3 offset:4096\n"
    "ds_read_b128 v[28:31], v3 offset:5120\n"
    "ds_read_b128 v[32:35], v244 offset:256\n"
    "ds_read_b128 v[36:39], v244 offset:320\n"
    "ds_read_b128 v[40:43], v245 offset:0\n"
    "ds_read_b128 v[44:47], v245 offset:64\n"
    ".La_loop:\n"
    "s_setprio 1\n"
    "s_add_u32 s57, s57, 1\n"
    "s_waitcnt lgkmcnt(8)\n"
    "v_mfma_f32_16x16x32_bf16 v[72:75], v[56:59], v[8:11], v[236:239]\n"
    "ds_read_b128 v[48:51], v245 offset:128\n"
    "v_mov_b32 v252, s57\n"
    "v_mfma_f32_16x16x32_bf16 v[72:75], v[60:63], v[12:15], v[72:75]\n"
    "ds_read_b128 v[52:55], v245 offset:192\n"
    "v_add_u32 v4, s53, v4\n"
    "v_mfma_f32_16x16x32_bf16 v[72:75], v[64:67], v[16:19], v[72:75]\n"
    "ds_read_b128 v[56:59], v245 offset:256\n"
    "s_waitcnt lgkmcnt(5)\n"
    "v_mfma_f32_16x16x32_bf16 v[72:75], v[68:71], v[20:23], v[72:75]\n"
    "ds_read_b128 v[60:63], v245 offset:320\n"
    "ds_write_b32 v7, v252 offset:192\n"
    "v_mfma_f32_16x16x32_bf16 v[72:75], v[32:35], v[24:27], v[72:75]\n"
    "ds_read_b128 v[64:67], v244 offset:8896\n"
    "ds_read_b128 v[208:211], v7 offset:32768\n"
    "v_mfma_f32_16x16x32_bf16 v[72:75], v[36:39], v[28:31], v[72:75]\n"
    "ds_read_b128 v[68:71], v244 offset:8960\n"
    "s_waitcnt lgkmcnt(7)\n"
    "v_mfma_f32_16x16x32_bf16 v[76:79], v[40:43], v[8:11], v[232:235]\n"
    "ds_read_b128 v[32:35], v244 offset:9024\n"
    "ds_read_b128 v[212:215], v7 offset:32768\n"
    "v_mfma_f32_16x16x32_bf16 v[76:79], v[44:47], v[12:15], v[76:79]\n"
    "ds_read_b128 v[36:39], v244 offset:9088\n"
    "ds_read_b128 v[216:219], v7 offset:32768\n"
    "v_mfma_f32_16x16x32_bf16 v[76:79], v[48:51], v[16:19], v[76:79]\n"
    "ds_read_b128 v[40:43], v244 offset:9152\n"
    "s_waitcnt lgkmcnt(9)\n"
    "v_mfma_f32_16x16x32_bf16 v[76:79], v[52:55], v[20:23], v[76:79]\n"
    "ds_read_b128 v[44:47], v244 offset:9216\n"
    "v_max_f32 v72, v72, v73\n"
    "v_mfma_f32_16x16x32_bf16 v[76:79], v[56:59], v[24:27], v[76:79]\n"
    "ds_read_b128 v[48:51], v245 offset:8896\n"
    "v_max_f32 v74, v74, v75\n"
    "v_mfma_f32_16x16x32_bf16 v[76:79], v[60:63], v[28:31], v[76:79]\n"
    "ds_read_b128 v[52:55], v245 offset:8960\n"
    "s_waitcnt lgkmcnt(7)\n"
    "v_mfma_f32_16x16x32_bf16 v[80:83], v[64:67], v[8:11], v[232:235]\n"
    "ds_read_b128 v[56:59], v245 offset:9024\n"
    "v_max_f32 v206, v72, v74\n"
    "v_mfma_f32_16x16x32_bf16 v[80:83], v[68:71], v[12:15], v[80:83]\n"
    "ds_read_b128 v[60:63], v245 offset:9088\n"
    "ds_read_b128 v[220:223], v7 offset:32768\n"
    "v_mfma_f32_16x16x32_bf16 v[80:83], v[32:35], v[16:19], v[80:83]\n"
    "ds_read_b128 v[64:67], v245 offset:9152\n"
    "s_waitcnt lgkmcnt(6)\n"
    "v_mfma_f32_16x16x32_bf16 v[80:83], v[36:39], v[20:23], v[80:83]\n"
    "ds_read_b128 v[68:71], v245 offset:9216\n"
    "v_max_f32 v76, v76, v77\n"
    "v_mfma_f32_16x16x32_bf16 v[80:83], v[40:43], v[24:27], v[80:83]\n"
    "ds_read_b128 v[32:35], v246 offset:0\n"
    "v_max_f32 v78, v78, v79\n"
    "v_mfma_f32_16x16x32_bf16 v[80:83], v[44:47], v[28:31], v[80:83]\n"
    "ds_read_b128 v[36:39], v246 offset:64\n"
    "s_waitcnt lgkmcnt(6)\n"
    "v_mfma_f32_16x16x32_bf16 v[84:87], v[48:51], v[8:11], v[232:235]\n"
    "ds_read_b128 v[40:43], v246 offset:128\n"
    "v_max3_f32 v206, v206, v76, v78\n"
    "v_mfma_f32_16x16x32_bf16 v[84:87], v[52:55], v[12:15], v[84:87]\n"
    "ds_read_b128 v[44:47], v246 offset:192\n"
    "ds_read_b128 v[224:227], v7 offset:32768\n"
    "v_mfma_f32_16x16x32_bf16 v[84:87], v[56:59], v[16:19], v[84:87]\n"
    "ds_read_b128 v[48:51], v246 offset:256\n"
    "s_waitcnt lgkmcnt(6)\n"
    "v_mfma_f32_16x16x32_bf16 v[84:87], v[60:63], v[20:23], v[84:87]\n"
    "ds_read_b128 v[52:55], v246 offset:320\n"
    "v_max_f32 v80, v80, v81\n"
    "v_mfma_f32_16x16x32_bf16 v[84:87], v[64:67], v[24:27], v[84:87]\n"
    "ds_read_b128 v[56:59], v247 offset:0\n"
    "v_max_f32 v82, v82, v83\n"
    "v_mfma_f32_16x16x32_bf16 v[84:87], v[68:71], v[28:31], v[84:87]\n"
    "ds_read_b128 v[60:63], v247 offset:64\n"
    "s_waitcnt lgkmcnt(6)\n"
    "v_mfma_f32_16x16x32_bf16 v[88:91], v[32:35], v[8:11], v[232:235]\n"
    "ds_read_b128 v[64:67], v247 offset:128\n"
    "v_max3_f32 v206, v206, v80, v82\n"
    "v_mfma_f32_16x16x32_bf16 v[88:91], v[36:39], v[12:15], v[88:91]\n"
    "ds_read_b128 v[68:71], v247 offset:192\n"
    "ds_read_b128 v[228:231], v7 offset:32768\n"
    "v_mfma_f32_16x16x32_bf16 v[88:91], v[40:43], v[16:19], v[88:91]\n"
    "ds_read_b128 v[32:35], v247 offset:256\n"
    "s_waitcnt lgkmcnt(6)\n"
    "v_mfma_f32_16x16x32_bf16 v[88:91], v[44:47], v[20:23], v[88:91]\n"
    "ds_read_b128 v[36:39], v247 offset:320\n"
    "v_max_f32 v84, v84, v85\n"
    "v_mfma_f32_16x16x32_bf16 v[88:91], v[48:51], v[24:27], v[88:91]\n"
    "ds_read_b128 v[40:43], v246 offset:8896\n"
    "v_max_f32 v86, v86, v87\n"
    "v_mfma_f32_16x16x32_bf16 v[88:91], v[52:55], v[28:31], v[88:91]\n"
    "ds_read_b128 v[44:47], v246 offset:8960\n"
    "s_waitcnt lgkmcnt(6)\n"
    "v_mfma_f32_16x16x32_bf16 v[92:95], v[56:59], v[8:11], v[232:235]\n"
    "ds_read_b128 v[48:51], v246 offset:9024\n"
    "v_max3_f32 v206, v206, v84, v86\n"
    "v_mfma_f32_16x16x32_bf16 v[92:95], v[60:63], v[12:15], v[92:95]\n"
    "ds_read_b128 v[52:55], v246 offset:9088\n"
    "v_mfma_f32_16x16x32_bf16 v[92:95], v[64:67], v[16:19], v[92:95]\n"
    "ds_read_b128 v[56:59], v246 offset:9152\n"
    "s_waitcnt lgkmcnt(5)\n"
    "v_mfma_f32_16x16x32_bf16 v[92:95], v[68:71], v[20:23], v[92:95]\n"
    "ds_read_b128 v[60:63], v246 offset:9216\n"
    "v_max_f32 v88, v88, v89\n"
    "v_mfma_f32_16x16x32_bf16 v[92:95], v[32:35], v[24:27], v[92:95]\n"
    "ds_read_b128 v[64:67], v247 offset:8896\n"
    "v_max_f32 v90, v90, v91\n"
    "v_mfma_f32_16x16x32_bf16 v[92:95], v[36:39], v[28:31], v[92:95]\n"
    "ds_read_b128 v[68:71], v247 offset:8960\n"
    "s_waitcnt lgkmcnt(5)\n"
    "v_mfma_f32_16x16x32_bf16 v[96:99], v[40:43], v[8:11], v[232:235]\n"
    "ds_read_b128 v[32:35], v247 offset:9024\n"
    "v_max3_f32 v206, v206, v88, v90\n"
    "v_mfma_f32_16x16x32_bf16 v[96:99], v[44:47], v[12:15], v[96:99]\n"
    "ds_read_b128 v[36:39], v247 offset:9088\n"
    "v_mfma_f32_16x16x32_bf16 v[96:99], v[48:51], v[16:19], v[96:99]\n"
    "ds_read_b128 v[40:43], v247 offset:9152\n"
    "s_waitcnt lgkmcnt(5)\n"
    "v_mfma_f32_16x16x32_bf16 v[96:99], v[52:55], v[20:23], v[96:99]\n"
    "ds_read_b128 v[44:47], v247 offset:9216\n"
    "v_max_f32 v92, v92, v93\n"
    "v_mfma_f32_16x16x32_bf16 v[96:99], v[56:59], v[24:27], v[96:99]\n"
    "v_add_u32 v244, s50, v1\n"
    "v_add_u32 v245, s51, v1\n"
    "v_mfma_f32_16x16x32_bf16 v[96:99], v[60:63], v[28:31], v[96:99]\n"
    "v_add_u32 v246, s50, v2\n"
    "s_waitcnt lgkmcnt(3)\n"
    "v_mfma_f32_16x16x32_bf16 v[100:103], v[64:67], v[8:11], v[240:243]\n"
    "v_add_u32 v247, s51, v2\n"
    "v_max_f32 v94, v94, v95\n"
    "v_mfma_f32_16x16x32_bf16 v[100:103], v[68:71], v[12:15], v[100:103]\n"
    "ds_read_b64_tr_b16 v[48:49], v244 offset:160\n"
    "v_max3_f32 v206, v206, v92, v94\n"
    "v_mfma_f32_16x16x32_bf16 v[100:103], v[32:35], v[16:19], v[100:103]\n"
    "ds_read_b64_tr_b16 v[50:51], v245 offset:160\n"
    "s_waitcnt lgkmcnt(2)\n"
    "v_mfma_f32_16x16x32_bf16 v[100:103], v[36:39], v[20:23], v[100:103]\n"
    "ds_read_b64_tr_b16 v[52:53], v246 offset:160\n"
    "v_max_f32 v96, v96, v97\n"
    "v_mfma_f32_16x16x32_bf16 v[100:103], v[40:43], v[24:27], v[100:103]\n"
    "ds_read_b64_tr_b16 v[54:55], v247 offset:160\n"
    "v_max_f32 v98, v98, v99\n"
    "v_mfma_f32_16x16x32_bf16 v[100:103], v[44:47], v[28:31], v[100:103]\n"
    "ds_read_b64_tr_b16 v[56:57], v244 offset:192\n"
    "ds_read_b64_tr_b16 v[58:59], v245 offset:192\n"
    "s_cmp_lg_u32 s69, 0\n"
    "s_cbranch_scc1 .La_fill\n"
    ".La_filled:\n"
    "ds_write_b32 v7, v252 offset:64\n"
    "s_setprio 0\n"
    "v_max3_f32 v206, v206, v96, v98\n"
    "ds_read_b64_tr_b16 v[60:61], v246 offset:192\n"
    "v_max_f32 v100, v100, v101\n"
    "v_max_f32 v102, v102, v103\n"
    "v_max3_f32 v206, v206, v100, v102\n"
    "v_mov_b32 v207, v206\n"
    "ds_read_b64_tr_b16 v[62:63], v247 offset:192\n"
    "ds_read_b64_tr_b16 v[64:65], v244 offset:224\n"
    "v_permlane16_swap_b32 v206, v207\n"
    "v_max_f32 v206, v206, v207\n"
    "v_mov_b32 v207, v206\n"
    "ds_read_b64_tr_b16 v[66:67], v245 offset:224\n"
    "ds_read_b64_tr_b16 v[68:69], v246 offset:224\n"
    "v_permlane32_swap_b32 v206, v207\n"
    "v_max_f32 v206, v206, v207\n"
    "v_mul_f32 v207, s46, v206\n"
    "v_fma_f32 v72, v72, s47, v207\n"
    "v_fma_f32 v74, v74, s47, v207\n"
    "v_fma_f32 v76, v76, s47, v207\n"
    "v_fma_f32 v78, v78, s47, v207\n"
    "v_fma_f32 v80, v80, s47, v207\n"
    "v_fma_f32 v82, v82, s47, v207\n"
    "v_fma_f32 v84, v84, s47, v207\n"
    "v_fma_f32 v86, v86, s47, v207\n"
    "v_fma_f32 v88, v88, s47, v207\n"
    "v_fma_f32 v90, v90, s47, v207\n"
    "v_fma_f32 v92, v92, s47, v207\n"
    "v_fma_f32 v94, v94, s47, v207\n"
    "v_fma_f32 v96, v96, s47, v207\n"
    "v_fma_f32 v98, v98, s47, v207\n"
    "v_fma_f32 v100, v100, s47, v207\n"
    "v_fma_f32 v102, v102, s47, v207\n"
    "ds_read_b64_tr_b16 v[70:71], v247 offset:224\n"
    "ds_read_b64_tr_b16 v[32:33], v244 offset:96\n"
    "ds_read_b64_tr_b16 v[34:35], v245 offset:96\n"
    "ds_read_b64_tr_b16 v[36:37], v246 offset:96\n"
    "ds_read_b64_tr_b16 v[38:39], v247 offset:96\n"
    "ds_read_b64_tr_b16 v[40:41], v244 offset:128\n"
    "ds_read_b64_tr_b16 v[42:43], v245 offset:128\n"
    "ds_read_b64_tr_b16 v[44:45], v246 offset:128\n"
    "ds_read_b64_tr_b16 v[46:47], v247 offset:128\n"
    "ds_read_b64_tr_b16 v[8:9], v244 offset:0\n"
    "ds_read_b64_tr_b16 v[10:11], v245 offset:0\n"
    "ds_read_b64_tr_b16 v[12:13], v246 offset:0\n"
    "ds_read_b64_tr_b16 v[14:15], v247 offset:0\n"
    "ds_read_b64_tr_b16 v[16:17], v244 offset:32\n"
    "ds_read_b64_tr_b16 v[18:19], v245 offset:32\n"
    "ds_read_b64_tr_b16 v[20:21], v246 offset:32\n"
    "ds_read_b64_tr_b16 v[22:23], v247 offset:32\n"
    "ds_read_b64_tr_b16 v[24:25], v244 offset:64\n"
    "ds_read_b64_tr_b16 v[26:27], v245 offset:64\n"
    "ds_read_b64_tr_b16 v[28:29], v246 offset:64\n"
    "ds_read_b64_tr_b16 v[30:31], v247 offset:64\n"
    "ds_read_b32 v253, v6 offset:32\n"
    "v_exp_f32 v72, v72\n"
    "s_bfe_u32 s48, s60, 0x40000\n"
    "v_exp_f32 v74, v74\n"
    "s_bfe_u32 s49, s60, 0x40004\n"
    "v_exp_f32 v76, v76\n"
    "s_bfe_u32 s50, s60, 0x40008\n"
    "v_exp_f32 v78, v78\n"
    "s_bfe_u32 s51, s60, 0x4000c\n"
    "v_exp_f32 v80, v80\n"
    "s_mul_i32 s48, s48, 8896\n"
    "v_exp_f32 v82, v82\n"
    "s_mul_i32 s49, s49, 8896\n"
    "v_exp_f32 v84, v84\n"
    "s_mul_i32 s50, s50, 8896\n"
    "v_exp_f32 v86, v86\n"
    "s_mul_i32 s51, s51, 8896\n"
    "v_exp_f32 v88, v88\n"
    "s_lshr_b64 s[60:61], s[60:61], 8\n"
    "v_exp_f32 v90, v90\n"
    "s_add_u32 s45, s45, 4\n"
    "v_exp_f32 v92, v92\n"
    "s_add_u32 s54, s54, 2\n"
    "v_exp_f32 v94, v94\n"
    "s_add_u32 s55, s55, 4\n"
    "v_exp_f32 v96, v96\n"
    "s_cmp_eq_u32 s66, 2\n"
    "s_cselect_b32 s67, -2, 1\n"
    "v_exp_f32 v98, v98\n"
    "s_add_u32 s66, s66, s67\n"
    "v_exp_f32 v100, v100\n"
    "s_mul_i32 s68, s67, 6144\n"
    "v_exp_f32 v102, v102\n"
    "v_cvt_pk_bf16_f32 v200, v72, v74\n"
    "v_cvt_pk_bf16_f32 v201, v76, v78\n"
    "v_cvt_pk_bf16_f32 v202, v80, v82\n"
    "v_cvt_pk_bf16_f32 v203, v84, v86\n"
    "v_cvt_pk_bf16_f32 v204, v88, v90\n"
    "v_cvt_pk_bf16_f32 v205, v92, v94\n"
    "v_cvt_pk_bf16_f32 v206, v96, v98\n"
    "v_cvt_pk_bf16_f32 v207, v100, v102\n"
    "ds_read_b64_tr_b16 v[72:73], v244 offset:256\n"
    "ds_read_b64_tr_b16 v[74:75], v245 offset:256\n"
    "ds_read_b64_tr_b16 v[76:77], v246 offset:256\n"
    "ds_read_b64_tr_b16 v[78:79], v247 offset:256\n"
    "ds_read_b64_tr_b16 v[80:81], v244 offset:288\n"
    "ds_read_b64_tr_b16 v[82:83], v245 offset:288\n"
    "ds_read_b64_tr_b16 v[84:85], v246 offset:288\n"
    "ds_read_b64_tr_b16 v[86:87], v247 offset:288\n"
    "s_waitcnt lgkmcnt(8)\n"
    "s_setprio 1\n"
    "v_mfma_f32_16x16x32_bf16 v[100:103], v[248:251], v[200:203], 0\n"
    "v_cmp_gt_i32 vcc, s56, v253\n"
    "v_mfma_f32_16x16x32_bf16 v[100:103], v[248:251], v[204:207], v[100:103]\n"
    "v_smfmac_f32_16x16x64_bf16 v[220:223], v[200:203], v[128:135], v5\n"
    "ds_read_b128 v[128:131], v7 offset:32768\n"
    "v_smfmac_f32_16x16x64_bf16 v[224:227], v[200:203], v[136:143], v5\n"
    "ds_read_b128 v[132:135], v7 offset:32768\n"
    "v_smfmac_f32_16x16x64_bf16 v[228:231], v[200:203], v[144:151], v5\n"
    "ds_read_b128 v[136:139], v7 offset:32768\n"
    "v_smfmac_f32_16x16x64_bf16 v[208:211], v[200:203], v[104:111], v5\n"
    "ds_read_b128 v[140:143], v7 offset:32768\n"
    "v_smfmac_f32_16x16x64_bf16 v[212:215], v[200:203], v[112:119], v5\n"
    "ds_read_b128 v[144:147], v7 offset:32768\n"
    "v_smfmac_f32_16x16x64_bf16 v[216:219], v[200:203], v[120:127], v5\n"
    "ds_read_b128 v[148:151], v7 offset:32768\n"
    "v_smfmac_f32_16x16x64_bf16 v[208:211], v[204:207], v[8:15], v5\n"
    "ds_read_b64_tr_b16 v[88:89], v244 offset:320\n"
    "v_rcp_f32 v100, v100\n"
    "v_smfmac_f32_16x16x64_bf16 v[212:215], v[204:207], v[16:23], v5\n"
    "ds_read_b64_tr_b16 v[90:91], v245 offset:320\n"
    "v_lshrrev_b32 v101, 2, v3\n"
    "v_smfmac_f32_16x16x64_bf16 v[216:219], v[204:207], v[24:31], v5\n"
    "ds_read_b64_tr_b16 v[92:93], v246 offset:320\n"
    "v_and_or_b32 v101, v101, 60, s63\n"
    "v_smfmac_f32_16x16x64_bf16 v[220:223], v[204:207], v[32:39], v5\n"
    "ds_read_b64_tr_b16 v[94:95], v247 offset:320\n"
    "s_cbranch_vccnz .La3\n"
    ".La3b:\n"
    "v_smfmac_f32_16x16x64_bf16 v[224:227], v[204:207], v[40:47], v5\n"
    "ds_read_b64_tr_b16 v[96:97], v244 offset:352\n"
    "ds_read_b64_tr_b16 v[98:99], v245 offset:352\n"
    "v_smfmac_f32_16x16x64_bf16 v[228:231], v[204:207], v[48:55], v5\n"
    "s_waitcnt lgkmcnt(6)\n"
    "v_smfmac_f32_16x16x64_bf16 v[128:131], v[200:203], v[152:159], v5\n"
    "ds_write_b32 v101, v100 offset:0\n"
    "ds_read_b64_tr_b16 v[100:101], v246 offset:352\n"
    "v_smfmac_f32_16x16x64_bf16 v[132:135], v[200:203], v[160:167], v5\n"
    "ds_read_b64_tr_b16 v[102:103], v247 offset:352\n"
    "v_smfmac_f32_16x16x64_bf16 v[136:139], v[200:203], v[168:175], v5\n"
    "v_cvt_pk_bf16_f32 v208, v208, v209\n"
    "v_cvt_pk_bf16_f32 v209, v210, v211\n"
    "v_smfmac_f32_16x16x64_bf16 v[140:143], v[200:203], v[176:183], v5\n"
    "v_cvt_pk_bf16_f32 v210, v212, v213\n"
    "v_cvt_pk_bf16_f32 v211, v214, v215\n"
    "v_smfmac_f32_16x16x64_bf16 v[144:147], v[200:203], v[184:191], v5\n"
    "ds_write_b128 v4, v[208:211] offset:0\n"
    "v_smfmac_f32_16x16x64_bf16 v[148:151], v[200:203], v[192:199], v5\n"
    "v_cvt_pk_bf16_f32 v216, v216, v217\n"
    "v_cvt_pk_bf16_f32 v217, v218, v219\n"
    "v_smfmac_f32_16x16x64_bf16 v[128:131], v[204:207], v[56:63], v5\n"
    "ds_read_b32 v254, v6 offset:16\n"
    "ds_read_b32 v200, v6 offset:0\n"
    "v_smfmac_f32_16x16x64_bf16 v[132:135], v[204:207], v[64:71], v5\n"
    "v_cvt_pk_bf16_f32 v218, v220, v221\n"
    "s_waitcnt lgkmcnt(8)\n"
    "v_smfmac_f32_16x16x64_bf16 v[136:139], v[204:207], v[72:79], v5\n"
    "v_cvt_pk_bf16_f32 v219, v222, v223\n"
    "v_smfmac_f32_16x16x64_bf16 v[140:143], v[204:207], v[80:87], v5\n"
    "ds_write_b128 v4, v[216:219] offset:1024\n"
    "v_smfmac_f32_16x16x64_bf16 v[144:147], v[204:207], v[88:95], v5\n"
    "v_cvt_pk_bf16_f32 v224, v224, v225\n"
    "s_waitcnt lgkmcnt(4)\n"
    "v_smfmac_f32_16x16x64_bf16 v[148:151], v[204:207], v[96:103], v5\n"
    "v_cvt_pk_bf16_f32 v225, v226, v227\n"
    "v_cvt_pk_bf16_f32 v226, v228, v229\n"
    "s_setprio 0\n"
    "v_add_u32 v244, s48, v0\n"
    "v_add_u32 v245, s49, v0\n"
    "v_add_u32 v246, s50, v0\n"
    "v_add_u32 v247, s51, v0\n"
    "v_add_u32 v3, s68, v3\n"
    "v_cvt_pk_bf16_f32 v227, v230, v231\n"
    "ds_write_b128 v4, v[224:227] offset:2048\n"
    "s_waitcnt lgkmcnt(3)\n"
    "v_cmp_gt_u32 vcc, s55, v254\n"
    "s_waitcnt lgkmcnt(2)\n"
    "v_cmp_gt_u32 s[58:59], s54, v200\n"
    "s_add_u32 s56, s56, 4\n"
    "s_lshl_b32 s67, s67, 6\n"
    "s_add_u32 s63, s63, s67\n"
    "s_sub_u32 s53, s68, 18432\n"
    "s_add_u32 s67, s63, 192\n"
    "s_cmp_ge_u32 s45, 10\n"
    "s_cselect_b32 s53, s53, s68\n"
    "s_cselect_b32 s63, s67, s63\n"
    "s_cselect_b32 s56, -16, s56\n"
    "s_cmp_ge_u32 s45, 12\n"
    "s_cbranch_scc1 .La4\n"
    "s_or_b64 vcc, vcc, s[58:59]\n"
    "s_cbranch_scc1 .La4\n"
    "ds_read_b128 v[104:107], v3 offset:0\n"
    "ds_read_b128 v[152:155], v244 offset:0\n"
    "ds_read_b128 v[108:111], v3 offset:1024\n"
    "ds_read_b128 v[156:159], v244 offset:64\n"
    "ds_read_b128 v[112:115], v3 offset:2048\n"
    "ds_read_b128 v[160:163], v244 offset:128\n"
    "ds_read_b128 v[116:119], v3 offset:3072\n"
    "ds_read_b128 v[164:167], v244 offset:192\n"
    "ds_read_b128 v[120:123], v3 offset:4096\n"
    "ds_read_b128 v[124:127], v3 offset:5120\n"
    "v_cvt_pk_bf16_f32 v128, v128, v129\n"
    "v_cvt_pk_bf16_f32 v129, v130, v131\n"
    "v_cvt_pk_bf16_f32 v130, v132, v133\n"
    "v_cvt_pk_bf16_f32 v131, v134, v135\n"
    "ds_write_b128 v4, v[128:131] offset:3072\n"
    "v_cvt_pk_bf16_f32 v136, v136, v137\n"
    "v_cvt_pk_bf16_f32 v137, v138, v139\n"
    "v_cvt_pk_bf16_f32 v138, v140, v141\n"
    "v_cvt_pk_bf16_f32 v139, v142, v143\n"
    "ds_write_b128 v4, v[136:139] offset:4096\n"
    "v_cvt_pk_bf16_f32 v144, v144, v145\n"
    "v_cvt_pk_bf16_f32 v145, v146, v147\n"
    "v_cvt_pk_bf16_f32 v146, v148, v149\n"
    "v_cvt_pk_bf16_f32 v147, v150, v151\n"
    "ds_write_b128 v4, v[144:147] offset:5120\n"
    "ds_write_b32 v7, v252 offset:128\n"
    "ds_read_b128 v[128:131], v244 offset:256\n"
    "ds_read_b128 v[132:135], v244 offset:320\n"
    "ds_read_b128 v[136:139], v245 offset:0\n"
    "ds_read_b128 v[140:143], v245 offset:64\n"
    ".La4j:\n"
    "s_setprio 1\n"
    "s_add_u32 s57, s57, 1\n"
    "s_waitcnt lgkmcnt(8)\n"
    "v_mfma_f32_16x16x32_bf16 v[168:171], v[152:155], v[104:107], v[236:239]\n"
    "ds_read_b128 v[144:147], v245 offset:128\n"
    "v_mov_b32 v252, s57\n"
    "v_mfma_f32_16x16x32_bf16 v[168:171], v[156:159], v[108:111], v[168:171]\n"
    "ds_read_b128 v[148:151], v245 offset:192\n"
    "v_add_u32 v4, s53, v4\n"
    "v_mfma_f32_16x16x32_bf16 v[168:171], v[160:163], v[112:115], v[168:171]\n"
    "ds_read_b128 v[152:155], v245 offset:256\n"
    "s_waitcnt lgkmcnt(5)\n"
    "v_mfma_f32_16x16x32_bf16 v[168:171], v[164:167], v[116:119], v[168:171]\n"
    "ds_read_b128 v[156:159], v245 offset:320\n"
    "ds_write_b32 v7, v252 offset:192\n"
    "v_mfma_f32_16x16x32_bf16 v[168:171], v[128:131], v[120:123], v[168:171]\n"
    "ds_read_b128 v[160:163], v244 offset:8896\n"
    "ds_read_b128 v[208:211], v7 offset:32768\n"
    "v_mfma_f32_16x16x32_bf16 v[168:171], v[132:135], v[124:127], v[168:171]\n"
    "ds_read_b128 v[164:167], v244 offset:8960\n"
    "s_waitcnt lgkmcnt(7)\n"
    "v_mfma_f32_16x16x32_bf16 v[172:175], v[136:139], v[104:107], v[232:235]\n"
    "ds_read_b128 v[128:131], v244 offset:9024\n"
    "ds_read_b128 v[212:215], v7 offset:32768\n"
    "v_mfma_f32_16x16x32_bf16 v[172:175], v[140:143], v[108:111], v[172:175]\n"
    "ds_read_b128 v[132:135], v244 offset:9088\n"
    "ds_read_b128 v[216:219], v7 offset:32768\n"
    "v_mfma_f32_16x16x32_bf16 v[172:175], v[144:147], v[112:115], v[172:175]\n"
    "ds_read_b128 v[136:139], v244 offset:9152\n"
    "s_waitcnt lgkmcnt(9)\n"
    "v_mfma_f32_16x16x32_bf16 v[172:175], v[148:151], v[116:119], v[172:175]\n"
    "ds_read_b128 v[140:143], v244 offset:9216\n"
    "v_max_f32 v168, v168, v169\n"
    "v_mfma_f32_16x16x32_bf16 v[172:175], v[152:155], v[120:123], v[172:175]\n"
    "ds_read_b128 v[144:147], v245 offset:8896\n"
    "v_max_f32 v170, v170, v171\n"
    "v_mfma_f32_16x16x32_bf16 v[172:175], v[156:159], v[124:127], v[172:175]\n"
    "ds_read_b128 v[148:151], v245 offset:8960\n"
    "s_waitcnt lgkmcnt(7)\n"
    "v_mfma_f32_16x16x32_bf16 v[176:179], v[160:163], v[104:107], v[232:235]\n"
    "ds_read_b128 v[152:155], v245 offset:9024\n"
    "v_max_f32 v206, v168, v170\n"
    "v_mfma_f32_16x16x32_bf16 v[176:179], v[164:167], v[108:111], v[176:179]\n"
    "ds_read_b128 v[156:159], v245 offset:9088\n"
    "ds_read_b128 v[220:223], v7 offset:32768\n"
    "v_mfma_f32_16x16x32_bf16 v[176:179], v[128:131], v[112:115], v[176:179]\n"
    "ds_read_b128 v[160:163], v245 offset:9152\n"
    "s_waitcnt lgkmcnt(6)\n"
    "v_mfma_f32_16x16x32_bf16 v[176:179], v[132:135], v[116:119], v[176:179]\n"
    "ds_read_b128 v[164:167], v245 offset:9216\n"
    "v_max_f32 v172, v172, v173\n"
    "v_mfma_f32_16x16x32_bf16 v[176:179], v[136:139], v[120:123], v[176:179]\n"
    "ds_read_b128 v[128:131], v246 offset:0\n"
    "v_max_f32 v174, v174, v175\n"
    "v_mfma_f32_16x16x32_bf16 v[176:179], v[140:143], v[124:127], v[176:179]\n"
    "ds_read_b128 v[132:135], v246 offset:64\n"
    "s_waitcnt lgkmcnt(6)\n"
    "v_mfma_f32_16x16x32_bf16 v[180:183], v[144:147], v[104:107], v[232:235]\n"
    "ds_read_b128 v[136:139], v246 offset:128\n"
    "v_max3_f32 v206, v206, v172, v174\n"
    "v_mfma_f32_16x16x32_bf16 v[180:183], v[148:151], v[108:111], v[180:183]\n"
    "ds_read_b128 v[140:143], v246 offset:192\n"
    "ds_read_b128 v[224:227], v7 offset:32768\n"
    "v_mfma_f32_16x16x32_bf16 v[180:183], v[152:155], v[112:115], v[180:183]\n"
    "ds_read_b128 v[144:147], v246 offset:256\n"
    "s_waitcnt lgkmcnt(6)\n"
    "v_mfma_f32_16x16x32_bf16 v[180:183], v[156:159], v[116:119], v[180:183]\n"
    "ds_read_b128 v[148:151], v246 offset:320\n"
    "v_max_f32 v176, v176, v177\n"
    "v_mfma_f32_16x16x32_bf16 v[180:183], v[160:163], v[120:123], v[180:183]\n"
    "ds_read_b128 v[152:155], v247 offset:0\n"
    "v_max_f32 v178, v178, v179\n"
    "v_mfma_f32_16x16x32_bf16 v[180:183], v[164:167], v[124:127], v[180:183]\n"
    "ds_read_b128 v[156:159], v247 offset:64\n"
    "s_waitcnt lgkmcnt(6)\n"
    "v_mfma_f32_16x16x32_bf16 v[184:187], v[128:131], v[104:107], v[232:235]\n"
    "ds_read_b128 v[160:163], v247 offset:128\n"
    "v_max3_f32 v206, v206, v176, v178\n"
    "v_mfma_f32_16x16x32_bf16 v[184:187], v[132:135], v[108:111], v[184:187]\n"
    "ds_read_b128 v[164:167], v247 offset:192\n"
    "ds_read_b128 v[228:231], v7 offset:32768\n"
    "v_mfma_f32_16x16x32_bf16 v[184:187], v[136:139], v[112:115], v[184:187]\n"
    "ds_read_b128 v[128:131], v247 offset:256\n"
    "s_waitcnt lgkmcnt(6)\n"
    "v_mfma_f32_16x16x32_bf16 v[184:187], v[140:143], v[116:119], v[184:187]\n"
    "ds_read_b128 v[132:135], v247 offset:320\n"
    "v_max_f32 v180, v180, v181\n"
    "v_mfma_f32_16x16x32_bf16 v[184:187], v[144:147], v[120:123], v[184:187]\n"
    "ds_read_b128 v[136:139], v246 offset:8896\n"
    "v_max_f32 v182, v182, v183\n"
    "v_mfma_f32_16x16x32_bf16 v[184:187], v[148:151], v[124:127], v[184:187]\n"
    "ds_read_b128 v[140:143], v246 offset:8960\n"
    "s_waitcnt lgkmcnt(6)\n"
    "v_mfma_f32_16x16x32_bf16 v[188:191], v[152:155], v[104:107], v[232:235]\n"
    "ds_read_b128 v[144:147], v246 offset:9024\n"
    "v_max3_f32 v206, v206, v180, v182\n"
    "v_mfma_f32_16x16x32_bf16 v[188:191], v[156:159], v[108:111], v[188:191]\n"
    "ds_read_b128 v[148:151], v246 offset:9088\n"
    "v_mfma_f32_16x16x32_bf16 v[188:191], v[160:163], v[112:115], v[188:191]\n"
    "ds_read_b128 v[152:155], v246 offset:9152\n"
    "s_waitcnt lgkmcnt(5)\n"
    "v_mfma_f32_16x16x32_bf16 v[188:191], v[164:167], v[116:119], v[188:191]\n"
    "ds_read_b128 v[156:159], v246 offset:9216\n"
    "v_max_f32 v184, v184, v185\n"
    "v_mfma_f32_16x16x32_bf16 v[188:191], v[128:131], v[120:123], v[188:191]\n"
    "ds_read_b128 v[160:163], v247 offset:8896\n"
    "v_max_f32 v186, v186, v187\n"
    "v_mfma_f32_16x16x32_bf16 v[188:191], v[132:135], v[124:127], v[188:191]\n"
    "ds_read_b128 v[164:167], v247 offset:8960\n"
    "s_waitcnt lgkmcnt(5)\n"
    "v_mfma_f32_16x16x32_bf16 v[192:195], v[136:139], v[104:107], v[232:235]\n"
    "ds_read_b128 v[128:131], v247 offset:9024\n"
    "v_max3_f32 v206, v206, v184, v186\n"
    "v_mfma_f32_16x16x32_bf16 v[192:195], v[140:143], v[108:111], v[192:195]\n"
    "ds_read_b128 v[132:135], v247 offset:9088\n"
    "v_mfma_f32_16x16x32_bf16 v[192:195], v[144:147], v[112:115], v[192:195]\n"
    "ds_read_b128 v[136:139], v247 offset:9152\n"
    "s_waitcnt lgkmcnt(5)\n"
    "v_mfma_f32_16x16x32_bf16 v[192:195], v[148:151], v[116:119], v[192:195]\n"
    "ds_read_b128 v[140:143], v247 offset:9216\n"
    "v_max_f32 v188, v188, v189\n"
    "v_mfma_f32_16x16x32_bf16 v[192:195], v[152:155], v[120:123], v[192:195]\n"
    "v_add_u32 v244, s50, v1\n"
    "v_add_u32 v245, s51, v1\n"
    "v_mfma_f32_16x16x32_bf16 v[192:195], v[156:159], v[124:127], v[192:195]\n"
    "v_add_u32 v246, s50, v2\n"
    "s_waitcnt lgkmcnt(3)\n"
    "v_mfma_f32_16x16x32_bf16 v[196:199], v[160:163], v[104:107], v[240:243]\n"
    "v_add_u32 v247, s51, v2\n"
    "v_max_f32 v190, v190, v191\n"
    "v_mfma_f32_16x16x32_bf16 v[196:199], v[164:167], v[108:111], v[196:199]\n"
    "ds_read_b64_tr_b16 v[144:145], v244 offset:160\n"
    "v_max3_f32 v206, v206, v188, v190\n"
    "v_mfma_f32_16x16x32_bf16 v[196:199], v[128:131], v[112:115], v[196:199]\n"
    "ds_read_b64_tr_b16 v[146:147], v245 offset:160\n"
    "s_waitcnt lgkmcnt(2)\n"
    "v_mfma_f32_16x16x32_bf16 v[196:199], v[132:135], v[116:119], v[196:199]\n"
    "ds_read_b64_tr_b16 v[148:149], v246 offset:160\n"
    "v_max_f32 v192, v192, v193\n"
    "v_mfma_f32_16x16x32_bf16 v[196:199], v[136:139], v[120:123], v[196:199]\n"
    "ds_read_b64_tr_b16 v[150:151], v247 offset:160\n"
    "v_max_f32 v194, v194, v195\n"
    "v_mfma_f32_16x16x32_bf16 v[196:199], v[140:143], v[124:127], v[196:199]\n"
    "ds_read_b64_tr_b16 v[152:153], v244 offset:192\n"
    "ds_read_b64_tr_b16 v[154:155], v245 offset:192\n"
    "ds_write_b32 v7, v252 offset:64\n"
    "s_setprio 0\n"
    "v_max3_f32 v206, v206, v192, v194\n"
    "ds_read_b64_tr_b16 v[156:157], v246 offset:192\n"
    "ds_read_b64_tr_b16 v[158:159], v247 offset:192\n"
    "ds_read_b64_tr_b16 v[160:161], v244 offset:224\n"
    "v_max_f32 v196, v196, v197\n"
    "v_max_f32 v198, v198, v199\n"
    "v_max3_f32 v206, v206, v196, v198\n"
    "v_mov_b32 v207, v206\n"
    "ds_read_b64_tr_b16 v[162:163], v245 offset:224\n"
    "ds_read_b64_tr_b16 v[164:165], v246 offset:224\n"
    "v_permlane16_swap_b32 v206, v207\n"
    "v_max_f32 v206, v206, v207\n"
    "v_mov_b32 v207, v206\n"
    "ds_read_b64_tr_b16 v[166:167], v247 offset:224\n"
    "ds_read_b64_tr_b16 v[128:129], v244 offset:96\n"
    "v_permlane32_swap_b32 v206, v207\n"
    "v_max_f32 v206, v206, v207\n"
    "v_mul_f32 v207, s46, v206\n"
    "v_fma_f32 v168, v168, s47, v207\n"
    "v_fma_f32 v170, v170, s47, v207\n"
    "v_fma_f32 v172, v172, s47, v207\n"
    "v_fma_f32 v174, v174, s47, v207\n"
    "v_fma_f32 v176, v176, s47, v207\n"
    "v_fma_f32 v178, v178, s47, v207\n"
    "v_fma_f32 v180, v180, s47, v207\n"
    "v_fma_f32 v182, v182, s47, v207\n"
    "v_fma_f32 v184, v184, s47, v207\n"
    "v_fma_f32 v186, v186, s47, v207\n"
    "v_fma_f32 v188, v188, s47, v207\n"
    "v_fma_f32 v190, v190, s47, v207\n"
    "v_fma_f32 v192, v192, s47, v207\n"
    "v_fma_f32 v194, v194, s47, v207\n"
    "v_fma_f32 v196, v196, s47, v207\n"
    "v_fma_f32 v198, v198, s47, v207\n"
    "ds_read_b64_tr_b16 v[130:131], v245 offset:96\n"
    "ds_read_b64_tr_b16 v[132:133], v246 offset:96\n"
    "ds_read_b64_tr_b16 v[134:135], v247 offset:96\n"
    "ds_read_b64_tr_b16 v[136:137], v244 offset:128\n"
    "ds_read_b64_tr_b16 v[138:139], v245 offset:128\n"
    "ds_read_b64_tr_b16 v[140:141], v246 offset:128\n"
    "ds_read_b64_tr_b16 v[142:143], v247 offset:128\n"
    "ds_read_b64_tr_b16 v[104:105], v244 offset:0\n"
    "ds_read_b64_tr_b16 v[106:107], v245 offset:0\n"
    "ds_read_b64_tr_b16 v[108:109], v246 offset:0\n"
    "ds_read_b64_tr_b16 v[110:111], v247 offset:0\n"
    "ds_read_b64_tr_b16 v[112:113], v244 offset:32\n"
    "ds_read_b64_tr_b16 v[114:115], v245 offset:32\n"
    "ds_read_b64_tr_b16 v[116:117], v246 offset:32\n"
    "ds_read_b64_tr_b16 v[118:119], v247 offset:32\n"
    "ds_read_b64_tr_b16 v[120:121], v244 offset:64\n"
    "ds_read_b64_tr_b16 v[122:123], v245 offset:64\n"
    "ds_read_b64_tr_b16 v[124:125], v246 offset:64\n"
    "ds_read_b64_tr_b16 v[126:127], v247 offset:64\n"
    "ds_read_b32 v253, v6 offset:32\n"
    "v_exp_f32 v168, v168\n"
    "s_bfe_u32 s48, s60, 0x40000\n"
    "v_exp_f32 v170, v170\n"
    "s_bfe_u32 s49, s60, 0x40004\n"
    "v_exp_f32 v172, v172\n"
    "s_bfe_u32 s50, s60, 0x40008\n"
    "v_exp_f32 v174, v174\n"
    "s_bfe_u32 s51, s60, 0x4000c\n"
    "v_exp_f32 v176, v176\n"
    "s_mul_i32 s48, s48, 8896\n"
    "v_exp_f32 v178, v178\n"
    "s_mul_i32 s49, s49, 8896\n"
    "v_exp_f32 v180, v180\n"
    "s_mul_i32 s50, s50, 8896\n"
    "v_exp_f32 v182, v182\n"
    "s_mul_i32 s51, s51, 8896\n"
    "v_exp_f32 v184, v184\n"
    "s_lshr_b64 s[60:61], s[60:61], 8\n"
    "v_exp_f32 v186, v186\n"
    "s_add_u32 s45, s45, 4\n"
    "v_exp_f32 v188, v188\n"
    "s_add_u32 s54, s54, 2\n"
    "v_exp_f32 v190, v190\n"
    "s_add_u32 s55, s55, 4\n"
    "v_exp_f32 v192, v192\n"
    "s_cmp_eq_u32 s66, 2\n"
    "s_cselect_b32 s67, -2, 1\n"
    "v_exp_f32 v194, v194\n"
    "s_add_u32 s66, s66, s67\n"
    "v_exp_f32 v196, v196\n"
    "s_mul_i32 s68, s67, 6144\n"
    "v_exp_f32 v198, v198\n"
    "v_cvt_pk_bf16_f32 v200, v168, v170\n"
    "v_cvt_pk_bf16_f32 v201, v172, v174\n"
    "v_cvt_pk_bf16_f32 v202, v176, v178\n"
    "v_cvt_pk_bf16_f32 v203, v180, v182\n"
    "v_cvt_pk_bf16_f32 v204, v184, v186\n"
    "v_cvt_pk_bf16_f32 v205, v188, v190\n"
    "v_cvt_pk_bf16_f32 v206, v192, v194\n"
    "v_cvt_pk_bf16_f32 v207, v196, v198\n"
    "ds_read_b64_tr_b16 v[168:169], v244 offset:256\n"
    "ds_read_b64_tr_b16 v[170:171], v245 offset:256\n"
    "ds_read_b64_tr_b16 v[172:173], v246 offset:256\n"
    "ds_read_b64_tr_b16 v[174:175], v247 offset:256\n"
    "ds_read_b64_tr_b16 v[176:177], v244 offset:288\n"
    "ds_read_b64_tr_b16 v[178:179], v245 offset:288\n"
    "ds_read_b64_tr_b16 v[180:181], v246 offset:288\n"
    "ds_read_b64_tr_b16 v[182:183], v247 offset:288\n"
    "s_waitcnt lgkmcnt(8)\n"
    "s_setprio 1\n"
    "v_mfma_f32_16x16x32_bf16 v[196:199], v[248:251], v[200:203], 0\n"
    "v_cmp_gt_i32 vcc, s56, v253\n"
    "v_mfma_f32_16x16x32_bf16 v[196:199], v[248:251], v[204:207], v[196:199]\n"
    "v_smfmac_f32_16x16x64_bf16 v[220:223], v[200:203], v[32:39], v5\n"
    "ds_read_b128 v[32:35], v7 offset:32768\n"
    "v_smfmac_f32_16x16x64_bf16 v[224:227], v[200:203], v[40:47], v5\n"
    "ds_read_b128 v[36:39], v7 offset:32768\n"
    "v_smfmac_f32_16x16x64_bf16 v[228:231], v[200:203], v[48:55], v5\n"
    "ds_read_b128 v[40:43], v7 offset:32768\n"
    "v_smfmac_f32_16x16x64_bf16 v[208:211], v[200:203], v[8:15], v5\n"
    "ds_read_b128 v[44:47], v7 offset:32768\n"
    "v_smfmac_f32_16x16x64_bf16 v[212:215], v[200:203], v[16:23], v5\n"
    "ds_read_b128 v[48:51], v7 offset:32768\n"
    "v_smfmac_f32_16x16x64_bf16 v[216:219], v[200:203], v[24:31], v5\n"
    "ds_read_b128 v[52:55], v7 offset:32768\n"
    "v_smfmac_f32_16x16x64_bf16 v[208:211], v[204:207], v[104:111], v5\n"
    "ds_read_b64_tr_b16 v[184:185], v244 offset:320\n"
    "v_rcp_f32 v196, v196\n"
    "v_smfmac_f32_16x16x64_bf16 v[212:215], v[204:207], v[112:119], v5\n"
    "ds_read_b64_tr_b16 v[186:187], v245 offset:320\n"
    "v_lshrrev_b32 v197, 2, v3\n"
    "v_smfmac_f32_16x16x64_bf16 v[216:219], v[204:207], v[120:127], v5\n"
    "ds_read_b64_tr_b16 v[188:189], v246 offset:320\n"
    "v_and_or_b32 v197, v197, 60, s63\n"
    "v_smfmac_f32_16x16x64_bf16 v[220:223], v[204:207], v[128:135], v5\n"
    "ds_read_b64_tr_b16 v[190:191], v247 offset:320\n"
    "s_cbranch_vccnz .La7\n"
    ".La7b:\n"
    "v_smfmac_f32_16x16x64_bf16 v[224:227], v[204:207], v[136:143], v5\n"
    "ds_read_b64_tr_b16 v[192:193], v244 offset:352\n"
    "ds_read_b64_tr_b16 v[194:195], v245 offset:352\n"
    "v_smfmac_f32_16x16x64_bf16 v[228:231], v[204:207], v[144:151], v5\n"
    "s_waitcnt lgkmcnt(6)\n"
    "v_smfmac_f32_16x16x64_bf16 v[32:35], v[200:203], v[56:63], v5\n"
    "ds_write_b32 v197, v196 offset:0\n"
    "ds_read_b64_tr_b16 v[196:197], v246 offset:352\n"
    "v_smfmac_f32_16x16x64_bf16 v[36:39], v[200:203], v[64:71], v5\n"
    "ds_read_b64_tr_b16 v[198:199], v247 offset:352\n"
    "v_smfmac_f32_16x16x64_bf16 v[40:43], v[200:203], v[72:79], v5\n"
    "v_cvt_pk_bf16_f32 v208, v208, v209\n"
    "v_cvt_pk_bf16_f32 v209, v210, v211\n"
    "v_smfmac_f32_16x16x64_bf16 v[44:47], v[200:203], v[80:87], v5\n"
    "v_cvt_pk_bf16_f32 v210, v212, v213\n"
    "v_cvt_pk_bf16_f32 v211, v214, v215\n"
    "v_smfmac_f32_16x16x64_bf16 v[48:51], v[200:203], v[88:95], v5\n"
    "ds_write_b128 v4, v[208:211] offset:0\n"
    "v_smfmac_f32_16x16x64_bf16 v[52:55], v[200:203], v[96:103], v5\n"
    "v_cvt_pk_bf16_f32 v216, v216, v217\n"
    "v_cvt_pk_bf16_f32 v217, v218, v219\n"
    "v_smfmac_f32_16x16x64_bf16 v[32:35], v[204:207], v[152:159], v5\n"
    "ds_read_b32 v254, v6 offset:16\n"
    "ds_read_b32 v200, v6 offset:0\n"
    "v_smfmac_f32_16x16x64_bf16 v[36:39], v[204:207], v[160:167], v5\n"
    "v_cvt_pk_bf16_f32 v218, v220, v221\n"
    "s_waitcnt lgkmcnt(8)\n"
    "v_smfmac_f32_16x16x64_bf16 v[40:43], v[204:207], v[168:175], v5\n"
    "v_cvt_pk_bf16_f32 v219, v222, v223\n"
    "v_smfmac_f32_16x16x64_bf16 v[44:47], v[204:207], v[176:183], v5\n"
    "ds_write_b128 v4, v[216:219] offset:1024\n"
    "v_smfmac_f32_16x16x64_bf16 v[48:51], v[204:207], v[184:191], v5\n"
    "v_cvt_pk_bf16_f32 v224, v224, v225\n"
    "s_waitcnt lgkmcnt(4)\n"
    "v_smfmac_f32_16x16x64_bf16 v[52:55], v[204:207], v[192:199], v5\n"
    "v_cvt_pk_bf16_f32 v225, v226, v227\n"
    "v_cvt_pk_bf16_f32 v226, v228, v229\n"
    "s_setprio 0\n"
    "v_add_u32 v244, s48, v0\n"
    "v_add_u32 v245, s49, v0\n"
    "v_add_u32 v246, s50, v0\n"
    "v_add_u32 v247, s51, v0\n"
    "v_add_u32 v3, s68, v3\n"
    "v_cvt_pk_bf16_f32 v227, v230, v231\n"
    "ds_write_b128 v4, v[224:227] offset:2048\n"
    "s_waitcnt lgkmcnt(3)\n"
    "v_cmp_gt_u32 vcc, s55, v254\n"
    "s_waitcnt lgkmcnt(2)\n"
    "v_cmp_gt_u32 s[58:59], s54, v200\n"
    "s_add_u32 s56, s56, 4\n"
    "s_lshl_b32 s67, s67, 6\n"
    "s_add_u32 s63, s63, s67\n"
    "s_sub_u32 s53, s68, 18432\n"
    "s_add_u32 s67, s63, 192\n"
    "s_cmp_ge_u32 s45, 10\n"
    "s_cselect_b32 s53, s53, s68\n"
    "s_cselect_b32 s63, s67, s63\n"
    "s_cselect_b32 s56, -16, s56\n"
    "s_cmp_ge_u32 s45, 12\n"
    "s_cbranch_scc1 .La8\n"
    "s_or_b64 vcc, vcc, s[58:59]\n"
    "s_cbranch_scc1 .La8\n"
    "ds_read_b128 v[8:11], v3 offset:0\n"
    "ds_read_b128 v[56:59], v244 offset:0\n"
    "ds_read_b128 v[12:15], v3 offset:1024\n"
    "ds_read_b128 v[60:63], v244 offset:64\n"
    "ds_read_b128 v[16:19], v3 offset:2048\n"
    "ds_read_b128 v[64:67], v244 offset:128\n"
    "ds_read_b128 v[20:23], v3 offset:3072\n"
    "ds_read_b128 v[68:71], v244 offset:192\n"
    "ds_read_b128 v[24:27], v3 offset:4096\n"
    "ds_read_b128 v[28:31], v3 offset:5120\n"
    "v_cvt_pk_bf16_f32 v32, v32, v33\n"
    "v_cvt_pk_bf16_f32 v33, v34, v35\n"
    "v_cvt_pk_bf16_f32 v34, v36, v37\n"
    "v_cvt_pk_bf16_f32 v35, v38, v39\n"
    "ds_write_b128 v4, v[32:35] offset:3072\n"
    "v_cvt_pk_bf16_f32 v40, v40, v41\n"
    "v_cvt_pk_bf16_f32 v41, v42, v43\n"
    "v_cvt_pk_bf16_f32 v42, v44, v45\n"
    "v_cvt_pk_bf16_f32 v43, v46, v47\n"
    "ds_write_b128 v4, v[40:43] offset:4096\n"
    "v_cvt_pk_bf16_f32 v48, v48, v49\n"
    "v_cvt_pk_bf16_f32 v49, v50, v51\n"
    "v_cvt_pk_bf16_f32 v50, v52, v53\n"
    "v_cvt_pk_bf16_f32 v51, v54, v55\n"
    "ds_write_b128 v4, v[48:51] offset:5120\n"
    "ds_write_b32 v7, v252 offset:128\n"
    "ds_read_b128 v[32:35], v244 offset:256\n"
    "ds_read_b128 v[36:39], v244 offset:320\n"
    "ds_read_b128 v[40:43], v245 offset:0\n"
    "ds_read_b128 v[44:47], v245 offset:64\n"
    ".La8j:\n"
    "s_branch .La_loop\n"
    ".La_end:\n"
    "s_waitcnt lgkmcnt(0)\n"
    "s_endpgm\n"
    ".La_ld:\n"
    "s_mov_b32 s52, 0x40000\n"
    "ds_read_b32 v253, v6 offset:256\n"
    "s_waitcnt lgkmcnt(0)\n"
    "v_cmp_gt_u32 vcc, 1, v253\n"
    "s_cbranch_vccnz .Lb1\n"
    ".Lb1b:\n"
    "s_cmp_eq_u32 s70, 2\n"
    "s_cbranch_scc1 .La_l0\n"
    "s_cmp_eq_u32 s70, 3\n"
    "s_cbranch_scc1 .La_l1\n"
    "s_cmp_eq_u32 s70, 1\n"
    "s_cbranch_scc1 .La_l7\n"
    "ds_read_b32 v253, v6 offset:304\n"
    "s_waitcnt lgkmcnt(0)\n"
    "v_cmp_gt_u32 vcc, 1, v253\n"
    "s_cbranch_vccnz .Lb2\n"
    ".Lb2b:\n"
    ".La_l7:\n"
    "v_cmp_gt_u32 vcc, 2, v18\n"
    "v_mov_b32 v19, 0x100000\n"
    "v_mov_b32 v252, 1\n"
    "v_cndmask_b32 v254, v19, v6, vcc\n"
    "s_mul_i32 s48, s75, 8\n"
    "s_add_u32 s48, s48, s74\n"
    "s_add_u32 s50, s72, s48\n"
    "s_addc_u32 s51, s73, 0\n"
    "s_mov_b32 m0, 72192\n"
    "s_nop 0\n"
    "global_load_lds_dwordx4 v13, s[50:51]\n"
    "s_mov_b32 m0, 73216\n"
    "s_nop 0\n"
    "global_load_lds_dwordx4 v8, s[50:51]\n"
    "s_mov_b32 m0, 74240\n"
    "s_nop 0\n"
    "global_load_lds_dwordx4 v14, s[50:51]\n"
    "s_mov_b32 m0, 75264\n"
    "s_nop 0\n"
    "global_load_lds_dwordx4 v9, s[50:51]\n"
    "s_mov_b32 m0, 76288\n"
    "s_nop 0\n"
    "global_load_lds_dwordx4 v15, s[50:51]\n"
    "s_mov_b32 m0, 77312\n"
    "s_nop 0\n"
    "global_load_lds_dwordx4 v10, s[50:51]\n"
    "s_mov_b32 m0, 78336\n"
    "s_nop 0\n"
    "global_load_lds_dwordx4 v16, s[50:51]\n"
    "s_mov_b32 m0, 71168\n"
    "s_nop 0\n"
    "global_load_lds_dwordx4 v17, s[50:51]\n"
    "s_mov_b32 m0, 78944\n"
    "s_nop 0\n"
    "global_load_lds_dwordx4 v12, s[50:51]\n"
    "s_mul_i32 s48, s75, 9\n"
    "s_add_u32 s48, s48, s74\n"
    "s_add_u32 s50, s72, s48\n"
    "s_addc_u32 s51, s73, 0\n"
    "s_mov_b32 m0, 81088\n"
    "s_nop 0\n"
    "global_load_lds_dwordx4 v13, s[50:51]\n"
    "s_mov_b32 m0, 82112\n"
    "s_nop 0\n"
    "global_load_lds_dwordx4 v8, s[50:51]\n"
    "s_mov_b32 m0, 83136\n"
    "s_nop 0\n"
    "global_load_lds_dwordx4 v14, s[50:51]\n"
    "s_mov_b32 m0, 84160\n"
    "s_nop 0\n"
    "global_load_lds_dwordx4 v9, s[50:51]\n"
    "s_mov_b32 m0, 85184\n"
    "s_nop 0\n"
    "global_load_lds_dwordx4 v15, s[50:51]\n"
    "s_mov_b32 m0, 86208\n"
    "s_nop 0\n"
    "global_load_lds_dwordx4 v10, s[50:51]\n"
    "s_mov_b32 m0, 87232\n"
    "s_nop 0\n"
    "global_load_lds_dwordx4 v16, s[50:51]\n"
    "s_mov_b32 m0, 80064\n"
    "s_nop 0\n"
    "global_load_lds_dwordx4 v17, s[50:51]\n"
    "s_mov_b32 m0, 87840\n"
    "s_nop 0\n"
    "global_load_lds_dwordx4 v12, s[50:51]\n"
    "s_mul_i32 s48, s75, 10\n"
    "s_add_u32 s48, s48, s74\n"
    "s_add_u32 s50, s72, s48\n"
    "s_addc_u32 s51, s73, 0\n"
    "s_mov_b32 m0, 89984\n"
    "s_nop 0\n"
    "global_load_lds_dwordx4 v13, s[50:51]\n"
    "s_mov_b32 m0, 91008\n"
    "s_nop 0\n"
    "global_load_lds_dwordx4 v8, s[50:51]\n"
    "s_mov_b32 m0, 92032\n"
    "s_nop 0\n"
    "global_load_lds_dwordx4 v14, s[50:51]\n"
    "s_mov_b32 m0, 93056\n"
    "s_nop 0\n"
    "global_load_lds_dwordx4 v9, s[50:51]\n"
    "s_mov_b32 m0, 94080\n"
    "s_nop 0\n"
    "global_load_lds_dwordx4 v15, s[50:51]\n"
    "s_mov_b32 m0, 95104\n"
    "s_nop 0\n"
    "global_load_lds_dwordx4 v10, s[50:51]\n"
    "s_mov_b32 m0, 96128\n"
    "s_nop 0\n"
    "global_load_lds_dwordx4 v16, s[50:51]\n"
    "s_mov_b32 m0, 88960\n"
    "s_nop 0\n"
    "global_load_lds_dwordx4 v17, s[50:51]\n"
    "s_mov_b32 m0, 96736\n"
    "s_nop 0\n"
    "global_load_lds_dwordx4 v12, s[50:51]\n"
    "s_mul_i32 s48, s75, 11\n"
    "s_add_u32 s48, s48, s74\n"
    "s_add_u32 s50, s72, s48\n"
    "s_addc_u32 s51, s73, 0\n"
    "s_mov_b32 m0, 98880\n"
    "s_nop 0\n"
    "global_load_lds_dwordx4 v13, s[50:51]\n"
    "s_mov_b32 m0, 99904\n"
    "s_nop 0\n"
    "global_load_lds_dwordx4 v8, s[50:51]\n"
    "s_mov_b32 m0, 100928\n"
    "s_nop 0\n"
    "global_load_lds_dwordx4 v14, s[50:51]\n"
    "s_mov_b32 m0, 101952\n"
    "s_nop 0\n"
    "global_load_lds_dwordx4 v9, s[50:51]\n"
    "s_mov_b32 m0, 102976\n"
    "s_nop 0\n"
    "global_load_lds_dwordx4 v15, s[50:51]\n"
    "s_mov_b32 m0, 104000\n"
    "s_nop 0\n"
    "global_load_lds_dwordx4 v10, s[50:51]\n"
    "s_mov_b32 m0, 105024\n"
    "s_nop 0\n"
    "global_load_lds_dwordx4 v16, s[50:51]\n"
    "s_mov_b32 m0, 97856\n"
    "s_nop 0\n"
    "global_load_lds_dwordx4 v17, s[50:51]\n"
    "s_mov_b32 m0, 105632\n"
    "s_nop 0\n"
    "global_load_lds_dwordx4 v12, s[50:51]\n"
    "ds_write_b32 v6, v252 offset:288\n"
    "ds_read_b32 v253, v6 offset:0\n"
    "s_waitcnt lgkmcnt(0)\n"
    "v_cmp_gt_u32 vcc, 4, v253\n"
    "s_cbranch_vccnz .Lb3\n"
    ".Lb3b:\n"
    "s_waitcnt vmcnt(18)\n"
    "v_mov_b32 v252, 5\n"
    "ds_write_b32 v254, v252 offset:0\n"
    "s_waitcnt vmcnt(0)\n"
    "v_mov_b32 v252, 6\n"
    "ds_write_b32 v254, v252 offset:0\n"
    "s_branch .La_join\n"
    ".Lb1:\n"
    "s_sub_u32 s52, s52, 1\n"
    "s_cmp_lt_i32 s52, 0\n"
    "s_cbranch_scc1 .Lb1b\n"
    "ds_read_b32 v253, v6 offset:256\n"
    "s_waitcnt lgkmcnt(0)\n"
    "v_cmp_gt_u32 vcc, 1, v253\n"
    "s_cbranch_vccnz .Lb1\n"
    "s_branch .Lb1b\n"
    ".La_l0:\n"
    "s_mul_i32 s48, s75, 2\n"
    "s_add_u32 s48, s48, s74\n"
    "s_add_u32 s50, s72, s48\n"
    "s_addc_u32 s51, s73, 0\n"
    "s_mov_b32 m0, 18816\n"
    "s_nop 0\n"
    "global_load_lds_dwordx4 v8, s[50:51]\n"
    "s_mov_b32 m0, 20864\n"
    "s_nop 0\n"
    "global_load_lds_dwordx4 v9, s[50:51]\n"
    "s_mov_b32 m0, 22912\n"
    "s_nop 0\n"
    "global_load_lds_dwordx4 v10, s[50:51]\n"
    "s_mov_b32 m0, 24960\n"
    "s_nop 0\n"
    "global_load_lds_dwordx4 v11, s[50:51]\n"
    "s_mov_b32 m0, 17792\n"
    "s_nop 0\n"
    "global_load_lds_dwordx4 v12, s[50:51]\n"
    "s_mul_i32 s48, s75, 1\n"
    "s_add_u32 s48, s48, s74\n"
    "s_add_u32 s50, s72, s48\n"
    "s_addc_u32 s51, s73, 0\n"
    "s_mov_b32 m0, 9920\n"
    "s_nop 0\n"
    "global_load_lds_dwordx4 v8, s[50:51]\n"
    "s_mov_b32 m0, 11968\n"
    "s_nop 0\n"
    "global_load_lds_dwordx4 v9, s[50:51]\n"
    "s_mov_b32 m0, 14016\n"
    "s_nop 0\n"
    "global_load_lds_dwordx4 v10, s[50:51]\n"
    "s_mov_b32 m0, 16064\n"
    "s_nop 0\n"
    "global_load_lds_dwordx4 v11, s[50:51]\n"
    "s_mov_b32 m0, 8896\n"
    "s_nop 0\n"
    "global_load_lds_dwordx4 v12, s[50:51]\n"
    "s_mul_i32 s48, s75, 0\n"
    "s_add_u32 s48, s48, s74\n"
    "s_add_u32 s50, s72, s48\n"
    "s_addc_u32 s51, s73, 0\n"
    "s_mov_b32 m0, 1024\n"
    "s_nop 0\n"
    "global_load_lds_dwordx4 v8, s[50:51]\n"
    "s_mov_b32 m0, 3072\n"
    "s_nop 0\n"
    "global_load_lds_dwordx4 v9, s[50:51]\n"
    "s_mov_b32 m0, 5120\n"
    "s_nop 0\n"
    "global_load_lds_dwordx4 v10, s[50:51]\n"
    "s_mov_b32 m0, 7168\n"
    "s_nop 0\n"
    "global_load_lds_dwordx4 v11, s[50:51]\n"
    "s_mov_b32 m0, 0\n"
    "s_nop 0\n"
    "global_load_lds_dwordx4 v12, s[50:51]\n"
    "s_mul_i32 s48, s75, 3\n"
    "s_add_u32 s48, s48, s74\n"
    "s_add_u32 s50, s72, s48\n"
    "s_addc_u32 s51, s73, 0\n"
    "s_mov_b32 m0, 26688\n"
    "s_nop 0\n"
    "global_load_lds_dwordx4 v12, s[50:51]\n"
    "s_mul_i32 s48, s75, 4\n"
    "s_add_u32 s48, s48, s74\n"
    "s_add_u32 s50, s72, s48\n"
    "s_addc_u32 s51, s73, 0\n"
    "s_mov_b32 m0, 35584\n"
    "s_nop 0\n"
    "global_load_lds_dwordx4 v12, s[50:51]\n"
    "s_mul_i32 s48, s75, 5\n"
    "s_add_u32 s48, s48, s74\n"
    "s_add_u32 s50, s72, s48\n"
    "s_addc_u32 s51, s73, 0\n"
    "s_mov_b32 m0, 44480\n"
    "s_nop 0\n"
    "global_load_lds_dwordx4 v12, s[50:51]\n"
    "s_mul_i32 s48, s75, 6\n"
    "s_add_u32 s48, s48, s74\n"
    "s_add_u32 s50, s72, s48\n"
    "s_addc_u32 s51, s73, 0\n"
    "s_mov_b32 m0, 53376\n"
    "s_nop 0\n"
    "global_load_lds_dwordx4 v12, s[50:51]\n"
    "s_mul_i32 s48, s75, 7\n"
    "s_add_u32 s48, s48, s74\n"
    "s_add_u32 s50, s72, s48\n"
    "s_addc_u32 s51, s73, 0\n"
    "s_mov_b32 m0, 62272\n"
    "s_nop 0\n"
    "global_load_lds_dwordx4 v12, s[50:51]\n"
    "v_lshlrev_b32 v19, 20, v18\n"
    "v_add_u32 v19, 161408, v19\n"
    "v_mov_b32 v252, 1\n"
    "ds_write_b32 v19, v252 offset:304\n"
    "ds_write_b32 v19, v252 offset:312\n"
    "s_waitcnt vmcnt(0)\n"
    "v_mov_b32 v252, 4\n"
    "ds_write_b32 v19, v252 offset:0\n"
    "s_branch .La_join\n"
    ".La_l1:\n"
    "s_mul_i32 s48, s75, 2\n"
    "s_add_u32 s48, s48, s74\n"
    "s_add_u32 s50, s72, s48\n"
    "s_addc_u32 s51, s73, 0\n"
    "s_mov_b32 m0, 19840\n"
    "s_nop 0\n"
    "global_load_lds_dwordx4 v8, s[50:51]\n"
    "s_mov_b32 m0, 21888\n"
    "s_nop 0\n"
    "global_load_lds_dwordx4 v9, s[50:51]\n"
    "s_mov_b32 m0, 23936\n"
    "s_nop 0\n"
    "global_load_lds_dwordx4 v10, s[50:51]\n"
    "s_mov_b32 m0, 25568\n"
    "s_nop 0\n"
    "global_load_lds_dwordx4 v12, s[50:51]\n"
    "s_mul_i32 s48, s75, 1\n"
    "s_add_u32 s48, s48, s74\n"
    "s_add_u32 s50, s72, s48\n"
    "s_addc_u32 s51, s73, 0\n"
    "s_mov_b32 m0, 10944\n"
    "s_nop 0\n"
    "global_load_lds_dwordx4 v8, s[50:51]\n"
    "s_mov_b32 m0, 12992\n"
    "s_nop 0\n"
    "global_load_lds_dwordx4 v9, s[50:51]\n"
    "s_mov_b32 m0, 15040\n"
    "s_nop 0\n"
    "global_load_lds_dwordx4 v10, s[50:51]\n"
    "s_mov_b32 m0, 16672\n"
    "s_nop 0\n"
    "global_load_lds_dwordx4 v12, s[50:51]\n"
    "s_mul_i32 s48, s75, 0\n"
    "s_add_u32 s48, s48, s74\n"
    "s_add_u32 s50, s72, s48\n"
    "s_addc_u32 s51, s73, 0\n"
    "s_mov_b32 m0, 2048\n"
    "s_nop 0\n"
    "global_load_lds_dwordx4 v8, s[50:51]\n"
    "s_mov_b32 m0, 4096\n"
    "s_nop 0\n"
    "global_load_lds_dwordx4 v9, s[50:51]\n"
    "s_mov_b32 m0, 6144\n"
    "s_nop 0\n"
    "global_load_lds_dwordx4 v10, s[50:51]\n"
    "s_mov_b32 m0, 7776\n"
    "s_nop 0\n"
    "global_load_lds_dwordx4 v12, s[50:51]\n"
    "s_mul_i32 s48, s75, 3\n"
    "s_add_u32 s48, s48, s74\n"
    "s_add_u32 s50, s72, s48\n"
    "s_addc_u32 s51, s73, 0\n"
    "s_mov_b32 m0, 34464\n"
    "s_nop 0\n"
    "global_load_lds_dwordx4 v12, s[50:51]\n"
    "s_mul_i32 s48, s75, 4\n"
    "s_add_u32 s48, s48, s74\n"
    "s_add_u32 s50, s72, s48\n"
    "s_addc_u32 s51, s73, 0\n"
    "s_mov_b32 m0, 43360\n"
    "s_nop 0\n"
    "global_load_lds_dwordx4 v12, s[50:51]\n"
    "s_mul_i32 s48, s75, 5\n"
    "s_add_u32 s48, s48, s74\n"
    "s_add_u32 s50, s72, s48\n"
    "s_addc_u32 s51, s73, 0\n"
    "s_mov_b32 m0, 52256\n"
    "s_nop 0\n"
    "global_load_lds_dwordx4 v12, s[50:51]\n"
    "s_mul_i32 s48, s75, 6\n"
    "s_add_u32 s48, s48, s74\n"
    "s_add_u32 s50, s72, s48\n"
    "s_addc_u32 s51, s73, 0\n"
    "s_mov_b32 m0, 61152\n"
    "s_nop 0\n"
    "global_load_lds_dwordx4 v12, s[50:51]\n"
    "s_mul_i32 s48, s75, 7\n"
    "s_add_u32 s48, s48, s74\n"
    "s_add_u32 s50, s72, s48\n"
    "s_addc_u32 s51, s73, 0\n"
    "s_mov_b32 m0, 70048\n"
    "s_nop 0\n"
    "global_load_lds_dwordx4 v12, s[50:51]\n"
    "v_lshlrev_b32 v19, 20, v18\n"
    "v_add_u32 v19, 161412, v19\n"
    "v_mov_b32 v252, 1\n"
    "ds_write_b32 v19, v252 offset:304\n"
    "ds_write_b32 v19, v252 offset:312\n"
    "s_waitcnt vmcnt(0)\n"
    "v_mov_b32 v252, 4\n"
    "ds_write_b32 v19, v252 offset:0\n"
    "s_branch .La_join\n"
    ".Lb2:\n"
    "s_sub_u32 s52, s52, 1\n"
    "s_cmp_lt_i32 s52, 0\n"
    "s_cbranch_scc1 .Lb2b\n"
    "ds_read_b32 v253, v6 offset:304\n"
    "s_waitcnt lgkmcnt(0)\n"
    "v_cmp_gt_u32 vcc, 1, v253\n"
    "s_cbranch_vccnz .Lb2\n"
    "s_branch .Lb2b\n"
    ".Lb3:\n"
    "s_sub_u32 s52, s52, 1\n"
    "s_cmp_lt_i32 s52, 0\n"
    "s_cbranch_scc1 .Lb3b\n"
    "ds_read_b32 v253, v6 offset:0\n"
    "s_waitcnt lgkmcnt(0)\n"
    "v_cmp_gt_u32 vcc, 4, v253\n"
    "s_cbranch_vccnz .Lb3\n"
    "s_branch .Lb3b\n"
    ".La1:\n"
    "s_sub_u32 s52, s52, 1\n"
    "s_cmp_lt_i32 s52, 0\n"
    "s_cbranch_scc1 .La1b\n"
    "ds_read_b32 v253, v6 offset:0\n"
    "s_waitcnt lgkmcnt(0)\n"
    "v_cmp_gt_u32 vcc, s54, v253\n"
    "s_cbranch_vccnz .La1\n"
    "s_branch .La1b\n"
    ".La_fill:\n"
    "s_mov_b32 s69, 0\n"
    "ds_read_b64_tr_b16 v[104:105], v200 offset:0\n"
    "ds_read_b64_tr_b16 v[106:107], v201 offset:0\n"
    "ds_read_b64_tr_b16 v[108:109], v202 offset:0\n"
    "ds_read_b64_tr_b16 v[110:111], v203 offset:0\n"
    "ds_read_b64_tr_b16 v[112:113], v200 offset:32\n"
    "ds_read_b64_tr_b16 v[114:115], v201 offset:32\n"
    "ds_read_b64_tr_b16 v[116:117], v202 offset:32\n"
    "ds_read_b64_tr_b16 v[118:119], v203 offset:32\n"
    "ds_read_b64_tr_b16 v[120:121], v200 offset:64\n"
    "ds_read_b64_tr_b16 v[122:123], v201 offset:64\n"
    "ds_read_b64_tr_b16 v[124:125], v202 offset:64\n"
    "ds_read_b64_tr_b16 v[126:127], v203 offset:64\n"
    "ds_read_b64_tr_b16 v[128:129], v200 offset:96\n"
    "ds_read_b64_tr_b16 v[130:131], v201 offset:96\n"
    "ds_read_b64_tr_b16 v[132:133], v202 offset:96\n"
    "ds_read_b64_tr_b16 v[134:135], v203 offset:96\n"
    "ds_read_b64_tr_b16 v[136:137], v200 offset:128\n"
    "ds_read_b64_tr_b16 v[138:139], v201 offset:128\n"
    "ds_read_b64_tr_b16 v[140:141], v202 offset:128\n"
    "ds_read_b64_tr_b16 v[142:143], v203 offset:128\n"
    "ds_read_b64_tr_b16 v[144:145], v200 offset:160\n"
    "ds_read_b64_tr_b16 v[146:147], v201 offset:160\n"
    "ds_read_b64_tr_b16 v[148:149], v202 offset:160\n"
    "ds_read_b64_tr_b16 v[150:151], v203 offset:160\n"
    "ds_read_b64_tr_b16 v[152:153], v200 offset:192\n"
    "ds_read_b64_tr_b16 v[154:155], v201 offset:192\n"
    "ds_read_b64_tr_b16 v[156:157], v202 offset:192\n"
    "ds_read_b64_tr_b16 v[158:159], v203 offset:192\n"
    "ds_read_b64_tr_b16 v[160:161], v200 offset:224\n"
    "ds_read_b64_tr_b16 v[162:163], v201 offset:224\n"
    "ds_read_b64_tr_b16 v[164:165], v202 offset:224\n"
    "ds_read_b64_tr_b16 v[166:167], v203 offset:224\n"
    "ds_read_b64_tr_b16 v[168:169], v200 offset:256\n"
    "ds_read_b64_tr_b16 v[170:171], v201 offset:256\n"
    "ds_read_b64_tr_b16 v[172:173], v202 offset:256\n"
    "ds_read_b64_tr_b16 v[174:175], v203 offset:256\n"
    "ds_read_b64_tr_b16 v[176:177], v200 offset:288\n"
    "ds_read_b64_tr_b16 v[178:179], v201 offset:288\n"
    "ds_read_b64_tr_b16 v[180:181], v202 offset:288\n"
    "ds_read_b64_tr_b16 v[182:183], v203 offset:288\n"
    "ds_read_b64_tr_b16 v[184:185], v200 offset:320\n"
    "ds_read_b64_tr_b16 v[186:187], v201 offset:320\n"
    "ds_read_b64_tr_b16 v[188:189], v202 offset:320\n"
    "ds_read_b64_tr_b16 v[190:191], v203 offset:320\n"
    "ds_read_b64_tr_b16 v[192:193], v200 offset:352\n"
    "ds_read_b64_tr_b16 v[194:195], v201 offset:352\n"
    "ds_read_b64_tr_b16 v[196:197], v202 offset:352\n"
    "ds_read_b64_tr_b16 v[198:199], v203 offset:352\n"
    "s_branch .La_filled\n"
    ".La2:\n"
    "s_sub_u32 s52, s52, 1\n"
    "s_cmp_lt_i32 s52, 0\n"
    "s_cbranch_scc1 .La2b\n"
    "ds_read_b32 v254, v6 offset:16\n"
    "s_waitcnt lgkmcnt(0)\n"
    "v_cmp_gt_u32 vcc, s55, v254\n"
    "s_cbranch_vccnz .La2\n"
    "s_branch .La2b\n"
    ".La3:\n"
    "s_sub_u32 s52, s52, 1\n"
    "s_cmp_lt_i32 s52, 0\n"
    "s_cbranch_scc1 .La3b\n"
    "ds_read_b32 v253, v6 offset:32\n"
    "s_waitcnt lgkmcnt(0)\n"
    "v_cmp_gt_i32 vcc, s56, v253\n"
    "s_cbranch_vccnz .La3\n"
    "s_branch .La3b\n"
    ".La5:\n"
    "s_sub_u32 s52, s52, 1\n"
    "s_cmp_lt_i32 s52, 0\n"
    "s_cbranch_scc1 .La5b\n"
    "ds_read_b32 v200, v6 offset:0\n"
    "s_waitcnt lgkmcnt(0)\n"
    "v_cmp_gt_u32 vcc, s54, v200\n"
    "s_cbranch_vccnz .La5\n"
    "s_branch .La5b\n"
    ".La6:\n"
    "s_sub_u32 s52, s52, 1\n"
    "s_cmp_lt_i32 s52, 0\n"
    "s_cbranch_scc1 .La6b\n"
    "ds_read_b32 v254, v6 offset:16\n"
    "s_waitcnt lgkmcnt(0)\n"
    "v_cmp_gt_u32 vcc, s55, v254\n"
    "s_cbranch_vccnz .La6\n"
    "s_branch .La6b\n"
    ".La4:\n"
    "v_cvt_pk_bf16_f32 v128, v128, v129\n"
    "v_cvt_pk_bf16_f32 v129, v130, v131\n"
    "v_cvt_pk_bf16_f32 v130, v132, v133\n"
    "v_cvt_pk_bf16_f32 v131, v134, v135\n"
    "ds_write_b128 v4, v[128:131] offset:3072\n"
    "v_cvt_pk_bf16_f32 v136, v136, v137\n"
    "v_cvt_pk_bf16_f32 v137, v138, v139\n"
    "v_cvt_pk_bf16_f32 v138, v140, v141\n"
    "v_cvt_pk_bf16_f32 v139, v142, v143\n"
    "ds_write_b128 v4, v[136:139] offset:4096\n"
    "v_cvt_pk_bf16_f32 v144, v144, v145\n"
    "v_cvt_pk_bf16_f32 v145, v146, v147\n"
    "v_cvt_pk_bf16_f32 v146, v148, v149\n"
    "v_cvt_pk_bf16_f32 v147, v150, v151\n"
    "ds_write_b128 v4, v[144:147] offset:5120\n"
    "ds_write_b32 v7, v252 offset:128\n"
    "s_cmp_ge_u32 s45, 12\n"
    "s_cbranch_scc1 .La_end\n"
    "ds_read_b32 v200, v6 offset:0\n"
    "s_waitcnt lgkmcnt(0)\n"
    "v_cmp_gt_u32 vcc, s54, v200\n"
    "s_cbranch_vccnz .La5\n"
    ".La5b:\n"
    "ds_read_b32 v254, v6 offset:16\n"
    "s_waitcnt lgkmcnt(0)\n"
    "v_cmp_gt_u32 vcc, s55, v254\n"
    "s_cbranch_vccnz .La6\n"
    ".La6b:\n"
    "ds_read_b128 v[104:107], v3 offset:0\n"
    "ds_read_b128 v[152:155], v244 offset:0\n"
    "ds_read_b128 v[108:111], v3 offset:1024\n"
    "ds_read_b128 v[156:159], v244 offset:64\n"
    "ds_read_b128 v[112:115], v3 offset:2048\n"
    "ds_read_b128 v[160:163], v244 offset:128\n"
    "ds_read_b128 v[116:119], v3 offset:3072\n"
    "ds_read_b128 v[164:167], v244 offset:192\n"
    "ds_read_b128 v[120:123], v3 offset:4096\n"
    "ds_read_b128 v[124:127], v3 offset:5120\n"
    "ds_read_b128 v[128:131], v244 offset:256\n"
    "ds_read_b128 v[132:135], v244 offset:320\n"
    "ds_read_b128 v[136:139], v245 offset:0\n"
    "ds_read_b128 v[140:143], v245 offset:64\n"
    "s_branch .La4j\n"
    ".La7:\n"
    "s_sub_u32 s52, s52, 1\n"
    "s_cmp_lt_i32 s52, 0\n"
    "s_cbranch_scc1 .La7b\n"
    "ds_read_b32 v253, v6 offset:32\n"
    "s_waitcnt lgkmcnt(0)\n"
    "v_cmp_gt_i32 vcc, s56, v253\n"
    "s_cbranch_vccnz .La7\n"
    "s_branch .La7b\n"
    ".La9:\n"
    "s_sub_u32 s52, s52, 1\n"
    "s_cmp_lt_i32 s52, 0\n"
    "s_cbranch_scc1 .La9b\n"
    "ds_read_b32 v200, v6 offset:0\n"
    "s_waitcnt lgkmcnt(0)\n"
    "v_cmp_gt_u32 vcc, s54, v200\n"
    "s_cbranch_vccnz .La9\n"
    "s_branch .La9b\n"
    ".La10:\n"
    "s_sub_u32 s52, s52, 1\n"
    "s_cmp_lt_i32 s52, 0\n"
    "s_cbranch_scc1 .La10b\n"
    "ds_read_b32 v254, v6 offset:16\n"
    "s_waitcnt lgkmcnt(0)\n"
    "v_cmp_gt_u32 vcc, s55, v254\n"
    "s_cbranch_vccnz .La10\n"
    "s_branch .La10b\n"
    ".La8:\n"
    "v_cvt_pk_bf16_f32 v32, v32, v33\n"
    "v_cvt_pk_bf16_f32 v33, v34, v35\n"
    "v_cvt_pk_bf16_f32 v34, v36, v37\n"
    "v_cvt_pk_bf16_f32 v35, v38, v39\n"
    "ds_write_b128 v4, v[32:35] offset:3072\n"
    "v_cvt_pk_bf16_f32 v40, v40, v41\n"
    "v_cvt_pk_bf16_f32 v41, v42, v43\n"
    "v_cvt_pk_bf16_f32 v42, v44, v45\n"
    "v_cvt_pk_bf16_f32 v43, v46, v47\n"
    "ds_write_b128 v4, v[40:43] offset:4096\n"
    "v_cvt_pk_bf16_f32 v48, v48, v49\n"
    "v_cvt_pk_bf16_f32 v49, v50, v51\n"
    "v_cvt_pk_bf16_f32 v50, v52, v53\n"
    "v_cvt_pk_bf16_f32 v51, v54, v55\n"
    "ds_write_b128 v4, v[48:51] offset:5120\n"
    "ds_write_b32 v7, v252 offset:128\n"
    "s_cmp_ge_u32 s45, 12\n"
    "s_cbranch_scc1 .La_end\n"
    "ds_read_b32 v200, v6 offset:0\n"
    "s_waitcnt lgkmcnt(0)\n"
    "v_cmp_gt_u32 vcc, s54, v200\n"
    "s_cbranch_vccnz .La9\n"
    ".La9b:\n"
    "ds_read_b32 v254, v6 offset:16\n"
    "s_waitcnt lgkmcnt(0)\n"
    "v_cmp_gt_u32 vcc, s55, v254\n"
    "s_cbranch_vccnz .La10\n"
    ".La10b:\n"
    "ds_read_b128 v[8:11], v3 offset:0\n"
    "ds_read_b128 v[56:59], v244 offset:0\n"
    "ds_read_b128 v[12:15], v3 offset:1024\n"
    "ds_read_b128 v[60:63], v244 offset:64\n"
    "ds_read_b128 v[16:19], v3 offset:2048\n"
    "ds_read_b128 v[64:67], v244 offset:128\n"
    "ds_read_b128 v[20:23], v3 offset:3072\n"
    "ds_read_b128 v[68:71], v244 offset:192\n"
    "ds_read_b128 v[24:27], v3 offset:4096\n"
    "ds_read_b128 v[28:31], v3 offset:5120\n"
    "ds_read_b128 v[32:35], v244 offset:256\n"
    "ds_read_b128 v[36:39], v244 offset:320\n"
    "ds_read_b128 v[40:43], v245 offset:0\n"
    "ds_read_b128 v[44:47], v245 offset:64\n"
    "s_branch .La8j\n"
    ".La_last:\n"
            :: "{v[8:11]}"(xo), "{v12}"(xo4), "{v13}"(xq[0]), "{v14}"(xq[1]), "{v15}"(xq[2]), "{v16}"(xq[3]), "{v17}"(xq4), "{v18}"(lane),
               "{s70}"(UNI(wave >= 5 && t12 && (1 == 2 || (1 == 1 && (bid & 4))) ? wave - 3 : wave == 7)), "{s[72:73]}"(xb), "{s74}"(UNI(rm.o * IROWB)), "{s75}"(UNI(rm.d * IROWB)),
               "{v[0:3]}"(va), "{v[4:7]}"(vb), "{v[232:235]}"(cbM), "{v[236:239]}"(cbF), "{v[240:243]}"(cbL),
               "{s60}"(UNI(slots)), "{s61}"(UNI(slots >> 32)), "{s45}"(UNI(n0)), "{s54}"(UNI(u0 + 4)), "{s63}"(UNI((unsigned)(size_t)smem + LF + 320 + 64 * sl)), "{s66}"(UNI(sl)),
               "{s46}"(-0.10411754112863712f), "{s47}"(0.10411754112863712f)
            : "memory", "v254", "s48", "s49", "s50", "s51", "s52", "s53", "s55", "s56", "s57", "s58", "s59", "s62", "s67", "s68", "s69", "s88", "s89", "s90", "s91", "s98", "s99", "s92", "s93", "s94", "s95", "vcc", "scc");
    }
}

extern "C" void kernel_launch(void* const* d_in, const int* in_sizes, int n_in,
                              void* d_out, int out_size, void* d_ws, size_t ws_size,
                              hipStream_t stream) {
    na_main<<<256, 512, 0, stream>>>((const u16*)d_in[0], (const u16*)d_in[1], (const u16*)d_in[2], (u16*)d_out, (char*)d_ws + 0);
}
```

```cpp
#include <hip/hip_runtime.h>
#include <stdint.h>

typedef unsigned short u16;
typedef unsigned long long u64;
typedef __attribute__((ext_vector_type(4))) float f32x4;
typedef __attribute__((ext_vector_type(4))) unsigned u32x4;
typedef __attribute__((ext_vector_type(2))) unsigned u32x2;
typedef __attribute__((ext_vector_type(8))) unsigned u32x8;

#define DEVINL __device__ __forceinline__
#define LAMBDA_INL __attribute__((always_inline))
#define UNI(x) __builtin_amdgcn_readfirstlane((int)(x))

static constexpr int IMW = 224;
static constexpr int PIXB = 384;
static constexpr int IROWB = IMW * PIXB;
static constexpr int RP = 8896;
static constexpr int LQ = 14 * RP, LZ = LQ + 3 * 6144, LF = LZ + 3 * 6144;
static constexpr int LDS_TOT = 163840;
static constexpr unsigned OOB = 0x100000u;
static constexpr int WS_W = 8192, WS_FL = WS_W + 144 * 1024, NREP = 8, WS_ACK = 393216;

DEVINL int pixad(int c) { return 800 * ((c + 1) >> 1) + 384 * ((c + 1) & 1) - 384; }
struct AxisMap { int o, d, x, last; };
DEVINL int axis_at(const AxisMap& m, int v) {
    const int reg = m.o + m.d * v, ext = m.x + 2 * min(v - m.last - 1, 2), sel = (m.last - v) >> 31;
    return (ext & sel) | (reg & ~sel);
}
DEVINL int scol(int i) { return (int)((0xD5B3E6C4A2F79180ull >> (4 * i)) & 15); }

struct AttC { unsigned sb, v0, v1, pk; };
DEVINL AttC att_const(int lane, int H, int cedge) {
    const int i16 = lane & 15, g = lane >> 4, e = (i16 + 4) >> 3 & 1, qj = i16 - 4 * ((i16 + 4) >> 3), sc = scol(i16);
    const int ws0 = (cedge ? min(8 * H + qj, 9) : 8 * H + qj) - 8 * H;
    AttC c;
    c.pk = 8 | e << 8;
#pragma unroll
    for (int r = 0; r < 4; ++r) {
        const unsigned ok = (unsigned)(scol(4 * g + r) - ws0) <= 6u;
        c.pk |= ok << (4 + r) | (r & 1 ? ok << (r - 1) : 0u);
    }
    c.sb = pixad(8 * H + (sc < 14 ? sc : sc - 8)) + 16 * g;
    c.v0 = pixad(8 * H + scol(4 * (g >> 1) + ((lane >> 2) & 3))) + (g & 1) * RP + 8 * (lane & 3);
    c.v1 = pixad(8 * H + scol(8 + 4 * (g >> 1) + ((lane >> 2) & 3))) + (g & 1) * RP + 8 * (lane & 3);
    return c;
}

__global__ __launch_bounds__(512, 2) void na_main(const u16* __restrict__ x_, const u16* __restrict__ wqkv_,
                                                  const u16* __restrict__ wo_, u16* __restrict__ out_, char* ws_) {
    __shared__ __attribute__((aligned(1024))) char smem[LDS_TOT];
    int tid = threadIdx.x, bid = blockIdx.x;
    u32x8 ka;
    u32x2 ka4;
    u32x4 ax, xo, xq;
    unsigned xo4, xq4, t0;
    asm volatile(
    "s_load_dwordx8 s[8:15], s[0:1], 0x0\n"
    "s_load_dwordx2 s[16:17], s[0:1], 0x20\n"
    "s_memtime s[4:5]\n"
    "v_readfirstlane_b32 s7, v0\n"
    "s_lshr_b32 s24, s2, 3\n"
    "v_and_b32 v6, 63, v0\n"
    "s_getpc_b64 s[28:29]\n"
    ".Le_pc:\n"
    "s_lshl_b32 s25, s24, 10\n"
    "s_mov_b32 m0, 162816\n"
    "s_cmp_lg_u32 s7, 128\n"
    "s_cbranch_scc1 .Le_ncp\n"
    "v_lshl_add_u32 v7, v6, 4, s25\n"
    "global_load_lds_dwordx4 v7, s[28:29]\n"
    ".Le_ncp:\n"
    "s_lshr_b32 s27, s2, 6\n"
    "s_and_b32 s26, s24, 7\n"
    "s_lshr_b32 s7, s7, 6\n"
    "s_add_u32 s3, s27, 14\n"
    "s_cmp_eq_u32 s26, 7\n"
    "s_cselect_b32 s3, s3, 99\n"
    "s_cmp_lt_u32 s24, 16\n"
    "s_cselect_b32 s3, s24, s3\n"
    "s_cmp_lg_u32 s7, 7\n"
    "s_cbranch_scc1 .Le_np\n"
    "s_cmp_ge_u32 s3, 18\n"
    "s_cbranch_scc1 .Le_7\n"
    "s_and_b32 s23, s2, 3\n"
    "s_mul_i32 s23, s23, 18\n"
    "s_add_u32 s3, s3, s23\n"
    "s_mul_i32 s44, s3, 43\n"
    "s_lshr_b32 s44, s44, 8\n"
    "s_mul_i32 s25, s44, 6\n"
    "s_sub_u32 s25, s3, s25\n"
    "v_lshrrev_b32 v4, 2, v6\n"
    "v_lshrrev_b32 v5, 4, v6\n"
    "v_sub_u32 v3, 0, v5\n"
    "v_xor_b32 v3, v3, v6\n"
    "v_and_b32 v3, 3, v3\n"
    "v_lshlrev_b32 v3, 4, v3\n"
    "s_mul_i32 s25, s25, 36864\n"
    "s_movk_i32 s23, 1152\n"
    "s_lshr_b32 s42, s44, 1\n"
    "s_and_b32 s43, s44, 1\n"
    "v_mov_b32 v9, 0\n"
    "s_mov_b32 m0, 124544\n"
    "s_bitcmp1_b32 s2, 2\n"
    "s_cbranch_scc1 .Le_r2\n"
    "v_lshl_add_u32 v7, v5, 2, v4\n"
    "v_mad_u32_u24 v7, v7, s23, v3\n"
    "s_lshl3_add_u32 s44, s42, s43\n"
    "s_mul_i32 s44, s44, 4608\n"
    "s_addk_i32 s44, 384\n"
    "s_mov_b32 s23, 6\n"
    "s_waitcnt lgkmcnt(0)\n"
    "s_add_u32 s26, s10, s25\n"
    "s_addc_u32 s27, s11, 0\n"
    "global_load_dwordx2 v[12:13], v9, s[16:17] sc1\n"
    "s_add_u32 s28, s26, 4608\n"
    "s_addc_u32 s29, s27, 0\n"
    "s_add_u32 s30, s10, s44\n"
    "s_addc_u32 s31, s11, 0\n"
    ".Le_l1:\n"
    "global_load_lds_dwordx4 v7, s[26:27]\n"
    "s_addk_i32 m0, 1024\n"
    "s_sub_u32 s23, s23, 1\n"
    "global_load_lds_dwordx4 v7, s[28:29]\n"
    "s_addk_i32 m0, 1024\n"
    "s_nop 0\n"
    "global_load_lds_dwordx4 v7, s[30:31]\n"
    "s_addk_i32 m0, 1024\n"
    "s_cmp_lg_u32 s23, 0\n"
    "v_add_u32 v7, 64, v7\n"
    "s_cbranch_scc1 .Le_l1\n"
    "s_branch .Le_p1\n"
    ".Le_r2:\n"
    "v_bfe_u32 v100, v6, 2, 2\n"
    "v_and_b32 v7, 1, v5\n"
    "v_lshl_add_u32 v7, v7, 4, v100\n"
    "v_lshrrev_b32 v16, 5, v6\n"
    "v_lshl_add_u32 v7, v16, 3, v7\n"
    "v_mad_u32_u24 v7, v7, s23, v3\n"
    "v_and_b32 v19, 3, v6\n"
    "v_mul_u32_u24 v16, 24, v4\n"
    "v_add_lshl_u32 v16, v16, v19, 4\n"
    "s_lshl_b32 s31, s42, 6\n"
    "s_addk_i32 s25, 768\n"
    "s_mov_b32 s23, 6\n"
    "s_waitcnt lgkmcnt(0)\n"
    "s_add_u32 s26, s10, s25\n"
    "s_addc_u32 s27, s11, 0\n"
    "global_load_dwordx2 v[12:13], v9, s[16:17] sc1\n"
    "s_add_u32 s28, s26, 4608\n"
    "s_addc_u32 s29, s27, 0\n"
    "s_add_u32 s30, s12, s31\n"
    "s_addc_u32 s31, s13, 0\n"
    ".Le_l2:\n"
    "global_load_lds_dwordx4 v7, s[26:27]\n"
    "s_addk_i32 m0, 1024\n"
    "s_sub_u32 s23, s23, 1\n"
    "global_load_lds_dwordx4 v7, s[28:29]\n"
    "s_addk_i32 m0, 1024\n"
    "v_add_u32 v7, 64, v7\n"
    "global_load_lds_dwordx4 v16, s[30:31]\n"
    "s_addk_i32 m0, 1024\n"
    "v_add_u32 v16, 6144, v16\n"
    "global_load_lds_dwordx4 v16, s[30:31]\n"
    "s_addk_i32 m0, 1024\n"
    "v_add_u32 v16, 6144, v16\n"
    "s_cmp_lg_u32 s23, 0\n"
    "s_cbranch_scc1 .Le_l2\n"
    "s_branch .Le_p2\n"
    ".Le_np:\n"
    "s_cmp_eq_u32 s7, 4\n"
    "s_cbranch_scc1 .Le_a\n"
    ".Le_xo:\n"
    "s_and_b32 s25, s2, 1\n"
    "s_lshr_b32 s23, 0x14a, s7\n"
    "s_and_b32 s23, s23, 1\n"
    "s_lshl2_add_u32 s29, s25, s27\n"
    "s_mul_i32 s22, s23, 486\n"
    "s_cmp_eq_u32 s26, 7\n"
    "s_cselect_b32 s26, s29, s26\n"
    "s_cselect_b32 s31, 1, s27\n"
    "s_cselect_b32 s18, 1, 0\n"
    "s_cmp_eq_u32 s26, 7\n"
    "s_cbranch_scc1 .Le_sp\n"
    "v_lshl_add_u32 v1, s23, 6, v6\n"
    "v_add_u32 v5, s22, v6\n"
    "s_movk_i32 s22, 1311\n"
    "v_add_u32 v2, 64 + 24 + 128, v1\n"
    "v_add_u32 v3, 64 + 24 + 256, v1\n"
    "v_add_u32 v4, 64 + 24 + 384, v1\n"
    "v_add_u32 v1, 64 + 24, v1\n"
    "v_add_u32 v5, 24, v5\n"
    "v_mul_u32_u24 v7, s22, v1\n"
    "s_lshl_b32 s29, s26, 5\n"
    "v_mul_u32_u24 v8, s22, v2\n"
    "s_sub_u32 s20, 222, s29\n"
    "v_mul_u32_u24 v9, s22, v3\n"
    "s_add_u32 s29, s29, 1\n"
    "v_mul_u32_u24 v10, s22, v4\n"
    "s_bitcmp1_b32 s2, 1\n"
    "v_mul_u32_u24 v11, s22, v5\n"
    "s_cselect_b32 s20, s29, s20\n"
    "v_not_b32 v12, 49\n"
    "v_lshrrev_b32 v7, 16, v7\n"
    "s_cselect_b32 s21, 2, -2\n"
    "v_lshrrev_b32 v8, 16, v8\n"
    "s_cselect_b32 s30, 218, 1\n"
    "v_lshrrev_b32 v9, 16, v9\n"
    "v_lshrrev_b32 v10, 16, v10\n"
    "v_lshrrev_b32 v11, 16, v11\n"
    "s_sub_u32 s29, s20, s21\n"
    "s_mul_i32 s29, s29, 384\n"
    "s_mul_i32 s22, s21, 768\n"
    "v_mad_i32_i24 v1, v7, v12, v1\n"
    "s_lshl_b32 s28, s27, 3\n"
    "v_mad_i32_i24 v2, v8, v12, v2\n"
    "s_lshr_b32 s28, 0x1837321d, s28\n"
    "v_mad_i32_i24 v3, v9, v12, v3\n"
    "s_and_b32 s28, s28, 0xff\n"
    "v_mad_i32_i24 v4, v10, v12, v4\n"
    "v_mad_i32_i24 v5, v11, v12, v5\n"
    "v_mov_b32 v18, s29\n"
    "s_add_u32 s29, s21, -1\n"
    "s_mul_i32 s29, s29, 384\n"
    "v_add_u32 v19, s29, v18\n"
    "v_min_u32 v1, 47, v1\n"
    "s_mul_i32 s29, s25, 50\n"
    "v_min_u32 v2, 47, v2\n"
    "s_add_u32 s28, s28, s29\n"
    "v_min_u32 v3, 47, v3\n"
    "s_cmp_lg_u32 s18, 0\n"
    "v_min_u32 v4, 47, v4\n"
    "s_cselect_b32 s28, 0, s28\n"
    "v_min_u32 v5, 47, v5\n"
    "v_cmp_lt_u32 s[42:43], 23, v1\n"
    "v_cmp_lt_u32 s[44:45], 23, v2\n"
    "v_cmp_lt_u32 s[46:47], 23, v3\n"
    "v_cmp_lt_u32 s[48:49], 23, v4\n"
    "v_cmp_lt_u32 s[50:51], 23, v5\n"
    "s_lshl_b32 s28, s28, 1\n"
    "s_sub_u32 s18, 222, s28\n"
    "s_add_u32 s28, s28, 1\n"
    "s_lshr_b32 s29, s2, 2\n"
    "v_cndmask_b32 v13, v18, v19, s[42:43]\n"
    "v_cndmask_b32 v14, v18, v19, s[44:45]\n"
    "v_cndmask_b32 v15, v18, v19, s[46:47]\n"
    "v_cndmask_b32 v16, v18, v19, s[48:49]\n"
    "v_cndmask_b32 v17, v18, v19, s[50:51]\n"
    "s_xor_b32 s31, s29, s31\n"
    "s_bitcmp1_b32 s2, 2\n"
    "s_cselect_b32 s18, s28, s18\n"
    "v_mad_i32_i24 v13, v7, s22, v13\n"
    "v_mad_i32_i24 v14, v8, s22, v14\n"
    "v_mad_i32_i24 v15, v9, s22, v15\n"
    "v_mad_i32_i24 v16, v10, s22, v16\n"
    "v_mad_i32_i24 v17, v11, s22, v17\n"
    "s_bitcmp1_b32 s31, 0\n"
    "s_cselect_b32 s19, -2, 2\n"
    "s_cmp_eq_u32 s26, 6\n"
    "s_cbranch_scc1 .Le_ce\n"
    ".Le_cd:\n"
    "v_lshl_add_u32 v1, v1, 4, v13\n"
    "v_lshl_add_u32 v2, v2, 4, v14\n"
    "v_lshl_add_u32 v3, v3, 4, v15\n"
    "v_lshl_add_u32 v4, v4, 4, v16\n"
    "v_lshl_add_u32 v5, v5, 4, v17\n"
    "s_cmp_lt_u32 s7, 4\n"
    "s_cbranch_scc1 .Le_b\n"
    "s_cmp_ge_u32 s7, 7\n"
    "s_cbranch_scc1 .Le_72\n"
    "s_mul_i32 s25, s19, 3\n"
    "s_add_u32 s25, s25, s18\n"
    "s_mul_i32 s25, s25, 86016\n"
    "s_mul_i32 s28, s19, 86016\n"
    "s_lshl_b32 s24, s23, 10\n"
    "s_addk_i32 s24, 27712\n"
    "s_mov_b32 s29, 6\n"
    "s_cmp_lt_u32 s3, 18\n"
    "s_cselect_b32 s31, 3, -1\n"
    "s_cselect_b32 s30, 2, 0\n"
    "s_bitcmp1_b32 s2, 2\n"
    "s_cselect_b32 s31, -1, s31\n"
    "s_cselect_b32 s30, s30, 0\n"
    "s_or_b32 s30, s30, s23\n"
    "s_barrier\n"
    "s_waitcnt lgkmcnt(0)\n"
    ".Le_xl:\n"
    "s_cmp_eq_u32 s29, s31\n"
    "s_cbranch_scc1 .Le_h\n"
    ".Le_go:\n"
    "s_mov_b32 m0, s24\n"
    "s_add_u32 s26, s8, s25\n"
    "s_addc_u32 s27, s9, 0\n"
    "global_load_lds_dwordx4 v1, s[26:27]\n"
    "s_addk_i32 m0, 2048\n"
    "s_add_u32 s25, s25, s28\n"
    "global_load_lds_dwordx4 v2, s[26:27]\n"
    "s_addk_i32 m0, 2048\n"
    "s_add_u32 s24, s24, 8896\n"
    "global_load_lds_dwordx4 v3, s[26:27]\n"
    "s_addk_i32 m0, 2048\n"
    "s_cmp_lg_u32 s23, 0\n"
    "s_cbranch_scc1 .Le_x3\n"
    "global_load_lds_dwordx4 v4, s[26:27]\n"
    ".Le_x3:\n"
    "s_sub_u32 s29, s29, 1\n"
    "s_cmp_lg_u32 s29, 0\n"
    "s_cbranch_scc1 .Le_xl\n"
    "s_branch .Le_g2s\n"
    ".Le_h:\n"
    "v_mov_b32 v16, 161456\n"
    "ds_read_b32 v7, v16\n"
    "s_waitcnt lgkmcnt(0)\n"
    "v_cmp_gt_u32 vcc, 1, v7\n"
    "s_cbranch_vccnz .Le_h\n"
    "s_branch .Le_go\n"
    ".Le_a:\n"
    "s_add_u32 s25, s25, 32768\n"
    "s_mov_b32 s23, .Lg_last-.Le_pc\n"
    "s_max_u32 s23, s23, .Lh_last-.Le_pc\n"
    "s_max_u32 s23, s23, .La_last-.Le_pc\n"
    "s_max_u32 s23, s23, .Ls_last-.Le_pc\n"
    "s_min_u32 s25, s25, s23\n"
    "v_lshl_add_u32 v7, v6, 4, s25\n"
    "global_load_lds_dwordx4 v7, s[28:29]\n"
    "s_cmp_lt_u32 s3, 18\n"
    "s_cselect_b32 s23, 0, 8\n"
    "v_lshrrev_b32 v10, 1, v6\n"
    "v_cmp_eq_u32 vcc, 1, v10\n"
    "v_lshrrev_b32 v7, 2, v6\n"
    "v_lshrrev_b32 v8, v7, s23\n"
    "v_mov_b32 v9, 0\n"
    "v_and_b32 v8, 1, v8\n"
    "v_lshlrev_b32 v8, 1, v8\n"
    "v_cndmask_b32 v8, v8, 7, vcc\n"
    "v_cmp_eq_u32 vcc, 5, v10\n"
    "v_lshlrev_b32 v7, 2, v6\n"
    "v_add_u32 v7, 161408, v7\n"
    "v_cndmask_b32 v8, v8, 15, vcc\n"
    "ds_write_b32 v7, v9 offset:64\n"
    "ds_write_b32 v7, v9 offset:768\n"
    "ds_write_b32 v7, v8\n"
    "s_waitcnt lgkmcnt(0)\n"
    "s_branch .Le_b\n"
    ".Le_ce:\n"
    "s_movk_i32 s29, 384\n"
    "v_cndmask_b32 v20, 0, 1, s[42:43]\n"
    "v_lshl_add_u32 v21, v7, 1, v20\n"
    "v_cmp_lt_u32 vcc, 16, v21\n"
    "v_add_u32 v22, -17, v21\n"
    "v_min_u32 v22, 2, v22\n"
    "v_lshl_add_u32 v22, v22, 1, s30\n"
    "v_sub_u32 v22, v22, v20\n"
    "v_mul_i32_i24 v22, s29, v22\n"
    "v_cndmask_b32 v13, v13, v22, vcc\n"
    "v_cndmask_b32 v20, 0, 1, s[44:45]\n"
    "v_lshl_add_u32 v21, v8, 1, v20\n"
    "v_cmp_lt_u32 vcc, 16, v21\n"
    "v_add_u32 v22, -17, v21\n"
    "v_min_u32 v22, 2, v22\n"
    "v_lshl_add_u32 v22, v22, 1, s30\n"
    "v_sub_u32 v22, v22, v20\n"
    "v_mul_i32_i24 v22, s29, v22\n"
    "v_cndmask_b32 v14, v14, v22, vcc\n"
    "v_cndmask_b32 v20, 0, 1, s[46:47]\n"
    "v_lshl_add_u32 v21, v9, 1, v20\n"
    "v_cmp_lt_u32 vcc, 16, v21\n"
    "v_add_u32 v22, -17, v21\n"
    "v_min_u32 v22, 2, v22\n"
    "v_lshl_add_u32 v22, v22, 1, s30\n"
    "v_sub_u32 v22, v22, v20\n"
    "v_mul_i32_i24 v22, s29, v22\n"
    "v_cndmask_b32 v15, v15, v22, vcc\n"
    "v_cndmask_b32 v20, 0, 1, s[48:49]\n"
    "v_lshl_add_u32 v21, v10, 1, v20\n"
    "v_cmp_lt_u32 vcc, 16, v21\n"
    "v_add_u32 v22, -17, v21\n"
    "v_min_u32 v22, 2, v22\n"
    "v_lshl_add_u32 v22, v22, 1, s30\n"
    "v_sub_u32 v22, v22, v20\n"
    "v_mul_i32_i24 v22, s29, v22\n"
    "v_cndmask_b32 v16, v16, v22, vcc\n"
    "v_cndmask_b32 v20, 0, 1, s[50:51]\n"
    "v_lshl_add_u32 v21, v11, 1, v20\n"
    "v_cmp_lt_u32 vcc, 16, v21\n"
    "v_add_u32 v22, -17, v21\n"
    "v_min_u32 v22, 2, v22\n"
    "v_lshl_add_u32 v22, v22, 1, s30\n"
    "v_sub_u32 v22, v22, v20\n"
    "v_mul_i32_i24 v22, s29, v22\n"
    "v_cndmask_b32 v17, v17, v22, vcc\n"
    "s_branch .Le_cd\n"
    ".Le_p1:\n"
    "s_barrier\n"
    "v_bfe_u32 v8, v6, 2, 2\n"
    "v_sub_u32 v8, 0, v8\n"
    "v_xor_b32 v8, v8, v5\n"
    "v_and_b32 v8, 3, v8\n"
    "v_lshlrev_b32 v8, 4, v8\n"
    "v_and_b32 v10, 15, v6\n"
    "v_lshl_add_u32 v8, v10, 6, v8\n"
    "v_add_u32 v8, 124544, v8\n"
    "s_waitcnt vmcnt(9)\n"
    "v_add_u32 v12, 0x7f4a7c15, v12\n"
    "v_add_u32 v13, 0x9e3779b9, v13\n"
    "s_lshl_b32 s23, s3, 10\n"
    "s_add_u32 s23, s23, 8192\n"
    "v_lshl_add_u32 v9, v6, 4, s23\n"
    "s_lshl_b32 s23, s3, 7\n"
    "s_add_u32 s23, s23, 155648\n"
    "v_and_b32 v11, 7, v6\n"
    "v_mov_b32 v17, 2\n"
    "ds_read_b128 v[20:23], v8 offset:0\n"
    "ds_read_b128 v[24:27], v8 offset:1024\n"
    "ds_read_b128 v[28:31], v8 offset:2048\n"
    "ds_read_b128 v[32:35], v8 offset:3072\n"
    "ds_read_b128 v[36:39], v8 offset:4096\n"
    "ds_read_b128 v[40:43], v8 offset:5120\n"
    "ds_read_b128 v[44:47], v8 offset:6144\n"
    "ds_read_b128 v[48:51], v8 offset:7168\n"
    "ds_read_b128 v[52:55], v8 offset:8192\n"
    "s_waitcnt vmcnt(6)\n"
    "ds_read_b128 v[56:59], v8 offset:9216\n"
    "ds_read_b128 v[60:63], v8 offset:10240\n"
    "ds_read_b128 v[64:67], v8 offset:11264\n"
    "s_waitcnt lgkmcnt(9)\n"
    "v_mfma_f32_16x16x32_bf16 v[92:95], v[20:23], v[28:31], 0\n"
    "v_mfma_f32_16x16x32_bf16 v[96:99], v[24:27], v[28:31], 0\n"
    "s_waitcnt lgkmcnt(6)\n"
    "v_mfma_f32_16x16x32_bf16 v[92:95], v[32:35], v[40:43], v[92:95]\n"
    "v_mfma_f32_16x16x32_bf16 v[96:99], v[36:39], v[40:43], v[96:99]\n"
    "s_waitcnt vmcnt(3)\n"
    "ds_read_b128 v[68:71], v8 offset:12288\n"
    "ds_read_b128 v[72:75], v8 offset:13312\n"
    "ds_read_b128 v[76:79], v8 offset:14336\n"
    "s_waitcnt lgkmcnt(6)\n"
    "v_mfma_f32_16x16x32_bf16 v[92:95], v[44:47], v[52:55], v[92:95]\n"
    "v_mfma_f32_16x16x32_bf16 v[96:99], v[48:51], v[52:55], v[96:99]\n"
    "s_waitcnt lgkmcnt(3)\n"
    "v_mfma_f32_16x16x32_bf16 v[92:95], v[56:59], v[64:67], v[92:95]\n"
    "v_mfma_f32_16x16x32_bf16 v[96:99], v[60:63], v[64:67], v[96:99]\n"
    "s_waitcnt vmcnt(0)\n"
    "ds_read_b128 v[80:83], v8 offset:15360\n"
    "ds_read_b128 v[84:87], v8 offset:16384\n"
    "ds_read_b128 v[88:91], v8 offset:17408\n"
    "s_waitcnt lgkmcnt(3)\n"
    "v_mfma_f32_16x16x32_bf16 v[92:95], v[68:71], v[76:79], v[92:95]\n"
    "v_mfma_f32_16x16x32_bf16 v[96:99], v[72:75], v[76:79], v[96:99]\n"
    "s_waitcnt lgkmcnt(0)\n"
    "v_mfma_f32_16x16x32_bf16 v[92:95], v[80:83], v[88:91], v[92:95]\n"
    "v_mfma_f32_16x16x32_bf16 v[96:99], v[84:87], v[88:91], v[96:99]\n"
    "s_nop 7\n"
    "s_branch .Le_pub1\n"
    ".Le_pub:\n"
    "s_lshl_b32 s23, s3, 10\n"
    "s_add_u32 s23, s23, 8192\n"
    "v_lshl_add_u32 v9, v6, 4, s23\n"
    "s_lshl_b32 s23, s3, 7\n"
    "s_add_u32 s23, s23, 155648\n"
    "v_and_b32 v11, 7, v6\n"
    "v_mov_b32 v17, 2\n"
    "s_nop 1\n"
    ".Le_pub1:\n"
    "v_cvt_pk_bf16_f32 v92, v92, v93\n"
    "v_cvt_pk_bf16_f32 v93, v94, v95\n"
    "v_cvt_pk_bf16_f32 v94, v96, v97\n"
    "v_cvt_pk_bf16_f32 v95, v98, v99\n"
    "v_add_u32 v92, v92, v12\n"
    "global_store_dwordx4 v9, v[92:95], s[16:17] sc1\n"
    "v_add_u32 v14, v92, v95\n"
    "v_add_u32 v15, v92, v95\n"
    "v_lshlrev_b32 v18, 2, v11\n"
    "v_and_b32 v18, 12, v18\n"
    "v_add_u32 v18, 161456, v18\n"
    "v_lshrrev_b32 v10, 3, v6\n"
    "ds_write_b32 v18, v17\n"
    "v_mul_u32_u24 v10, 18432, v10\n"
    "v_lshl_add_u32 v10, v11, 4, v10\n"
    "v_add_u32 v10, s23, v10\n"
    "v_permlane32_swap_b32 v14, v15\n"
    "v_add_u32 v14, v14, v15\n"
    "s_nop 1\n"
    "v_add_u32_dpp v14, v14, v14 row_ror:8 row_mask:0xf bank_mask:0xf\n"
    "s_nop 1\n"
    "v_add_u32_dpp v14, v14, v14 row_ror:4 row_mask:0xf bank_mask:0xf\n"
    "s_nop 1\n"
    "v_add_u32_dpp v14, v14, v14 row_ror:2 row_mask:0xf bank_mask:0xf\n"
    "s_nop 1\n"
    "v_add_u32_dpp v14, v14, v14 row_ror:1 row_mask:0xf bank_mask:0xf\n"
    "v_mov_b32 v15, v14\n"
    "s_nop 1\n"
    "v_permlane16_swap_b32 v14, v15\n"
    "v_add_u32 v14, v14, v15\n"
    "global_store_dwordx4 v10, v[12:15], s[16:17] sc1\n"
    "s_branch .Le_71\n"
    ".Le_p2:\n"
    "s_barrier\n"
    "v_bfe_u32 v8, v6, 2, 2\n"
    "v_sub_u32 v8, 0, v8\n"
    "v_xor_b32 v8, v8, v5\n"
    "v_and_b32 v8, 3, v8\n"
    "v_lshlrev_b32 v8, 4, v8\n"
    "v_and_b32 v10, 15, v6\n"
    "v_lshl_add_u32 v8, v10, 6, v8\n"
    "v_add_u32 v8, 124544, v8\n"
    "v_lshl_add_u32 v7, v5, 3, v100\n"
    "v_lshlrev_b32 v7, 6, v7\n"
    "v_lshl_add_u32 v7, v19, 4, v7\n"
    "s_lshl_b32 s23, s43, 3\n"
    "s_add_u32 s23, s23, 126592\n"
    "v_add_u32 v7, s23, v7\n"
    "s_add_u32 s3, s3, 72\n"
    "s_waitcnt vmcnt(0)\n"
    "v_add_u32 v12, 0x7f4a7c15, v12\n"
    "v_add_u32 v13, 0x9e3779b9, v13\n"
    "ds_read_b128 v[20:23], v8 offset:0\n"
    "ds_read_b128 v[24:27], v8 offset:1024\n"
    "ds_read_b64_tr_b16 v[28:29], v7 offset:0\n"
    "ds_read_b64_tr_b16 v[30:31], v7 offset:256\n"
    "ds_read_b128 v[32:35], v8 offset:4096\n"
    "ds_read_b128 v[36:39], v8 offset:5120\n"
    "ds_read_b64_tr_b16 v[40:41], v7 offset:4096\n"
    "ds_read_b64_tr_b16 v[42:43], v7 offset:4352\n"
    "ds_read_b128 v[44:47], v8 offset:8192\n"
    "ds_read_b128 v[48:51], v8 offset:9216\n"
    "ds_read_b64_tr_b16 v[52:53], v7 offset:8192\n"
    "ds_read_b64_tr_b16 v[54:55], v7 offset:8448\n"
    "ds_read_b128 v[56:59], v8 offset:12288\n"
    "ds_read_b128 v[60:63], v8 offset:13312\n"
    "ds_read_b64_tr_b16 v[64:65], v7 offset:12288\n"
    "ds_read_b64_tr_b16 v[66:67], v7 offset:12544\n"
    "ds_read_b128 v[68:71], v8 offset:16384\n"
    "ds_read_b128 v[72:75], v8 offset:17408\n"
    "ds_read_b64_tr_b16 v[76:77], v7 offset:16384\n"
    "ds_read_b64_tr_b16 v[78:79], v7 offset:16640\n"
    "ds_read_b128 v[80:83], v8 offset:20480\n"
    "ds_read_b128 v[84:87], v8 offset:21504\n"
    "ds_read_b64_tr_b16 v[88:89], v7 offset:20480\n"
    "ds_read_b64_tr_b16 v[90:91], v7 offset:20736\n"
    "s_waitcnt lgkmcnt(14)\n"
    "v_mfma_f32_16x16x32_bf16 v[92:95], v[20:23], v[28:31], 0\n"
    "v_mfma_f32_16x16x32_bf16 v[96:99], v[24:27], v[28:31], 0\n"
    "v_mfma_f32_16x16x32_bf16 v[92:95], v[32:35], v[40:43], v[92:95]\n"
    "v_mfma_f32_16x16x32_bf16 v[96:99], v[36:39], v[40:43], v[96:99]\n"
    "s_waitcnt lgkmcnt(12)\n"
    "v_mfma_f32_16x16x32_bf16 v[92:95], v[44:47], v[52:55], v[92:95]\n"
    "v_mfma_f32_16x16x32_bf16 v[96:99], v[48:51], v[52:55], v[96:99]\n"
    "s_waitcnt lgkmcnt(8)\n"
    "v_mfma_f32_16x16x32_bf16 v[92:95], v[56:59], v[64:67], v[92:95]\n"
    "v_mfma_f32_16x16x32_bf16 v[96:99], v[60:63], v[64:67], v[96:99]\n"
    "s_waitcnt lgkmcnt(4)\n"
    "v_mfma_f32_16x16x32_bf16 v[92:95], v[68:71], v[76:79], v[92:95]\n"
    "v_mfma_f32_16x16x32_bf16 v[96:99], v[72:75], v[76:79], v[96:99]\n"
    "s_waitcnt lgkmcnt(0)\n"
    "v_mfma_f32_16x16x32_bf16 v[92:95], v[80:83], v[88:91], v[92:95]\n"
    "v_mfma_f32_16x16x32_bf16 v[96:99], v[84:87], v[88:91], v[96:99]\n"
    "s_branch .Le_pub\n"
    ".Le_7:\n"
    "s_barrier\n"
    ".Le_71:\n"
    "s_lshr_b32 s27, s2, 6\n"
    "s_bfe_u32 s26, s2, 0x30003\n"
    "s_branch .Le_xo\n"
    ".Le_72:\n"
    "s_cmp_eq_u32 s7, 8\n"
    "s_cbranch_scc1 .Le_w\n"
    "s_mov_b32 s7, 8\n"
    "v_mov_b32 v101, v1\n"
    "v_mov_b32 v102, v2\n"
    "v_mov_b32 v103, v3\n"
    "v_mov_b32 v104, v4\n"
    "v_mov_b32 v105, v5\n"
    "s_branch .Le_71\n"
    ".Le_sp:\n"
    "s_cmp_lt_u32 s7, 7\n"
    "s_cbranch_scc1 .Le_b\n"
    "s_branch .Le_w\n"
    ".Le_g2s:\n"
    "s_mul_i32 s44, s18, 86016\n"
    "s_mul_i32 s45, s19, 86016\n"
    "s_lshl_b32 s56, s23, 2\n"
    "s_add_u32 s56, s56, 161408\n"
    "v_lshlrev_b32 v16, 20, v6\n"
    "v_add_u32 v16, s56, v16\n"
    "s_cmp_eq_u32 s30, 1\n"
    "s_cbranch_scc1 .Li_s1\n"
    "s_cmp_eq_u32 s30, 2\n"
    "s_cbranch_scc1 .Li_s2\n"
    "s_cmp_eq_u32 s30, 3\n"
    "s_cbranch_scc1 .Li_s3\n"
    "s_waitcnt vmcnt(16)\n"
    "v_mov_b32 v17, 1\n"
    "ds_write_b32 v16, v17 offset:0\n"
    "s_waitcnt vmcnt(8)\n"
    "v_mov_b32 v17, 2\n"
    "ds_write_b32 v16, v17 offset:0\n"
    "s_mul_i32 s56, s45, 2\n"
    "s_add_u32 s56, s56, s44\n"
    "s_add_u32 s60, s8, s56\n"
    "s_addc_u32 s61, s9, 0\n"
    "s_mov_b32 m0, 18816\n"
    "s_nop 0\n"
    "global_load_lds_dwordx4 v1, s[60:61]\n"
    "s_mov_b32 m0, 20864\n"
    "s_nop 0\n"
    "global_load_lds_dwordx4 v2, s[60:61]\n"
    "s_mov_b32 m0, 22912\n"
    "s_nop 0\n"
    "global_load_lds_dwordx4 v3, s[60:61]\n"
    "s_mov_b32 m0, 24960\n"
    "s_nop 0\n"
    "global_load_lds_dwordx4 v4, s[60:61]\n"
    "s_mov_b32 m0, 17792\n"
    "s_nop 0\n"
    "global_load_lds_dwordx4 v5, s[60:61]\n"
    "s_mul_i32 s56, s45, 1\n"
    "s_add_u32 s56, s56, s44\n"
    "s_add_u32 s60, s8, s56\n"
    "s_addc_u32 s61, s9, 0\n"
    "s_mov_b32 m0, 9920\n"
    "s_nop 0\n"
    "global_load_lds_dwordx4 v1, s[60:61]\n"
    "s_mov_b32 m0, 11968\n"
    "s_nop 0\n"
    "global_load_lds_dwordx4 v2, s[60:61]\n"
    "s_mov_b32 m0, 14016\n"
    "s_nop 0\n"
    "global_load_lds_dwordx4 v3, s[60:61]\n"
    "s_mov_b32 m0, 16064\n"
    "s_nop 0\n"
    "global_load_lds_dwordx4 v4, s[60:61]\n"
    "s_mov_b32 m0, 8896\n"
    "s_nop 0\n"
    "global_load_lds_dwordx4 v5, s[60:61]\n"
    "s_mul_i32 s56, s45, 0\n"
    "s_add_u32 s56, s56, s44\n"
    "s_add_u32 s60, s8, s56\n"
    "s_addc_u32 s61, s9, 0\n"
    "s_mov_b32 m0, 1024\n"
    "s_nop 0\n"
    "global_load_lds_dwordx4 v1, s[60:61]\n"
    "s_mov_b32 m0, 3072\n"
    "s_nop 0\n"
    "global_load_lds_dwordx4 v2, s[60:61]\n"
    "s_mov_b32 m0, 5120\n"
    "s_nop 0\n"
    "global_load_lds_dwordx4 v3, s[60:61]\n"
    "s_mov_b32 m0, 7168\n"
    "s_nop 0\n"
    "global_load_lds_dwordx4 v4, s[60:61]\n"
    "s_mov_b32 m0, 0\n"
    "s_nop 0\n"
    "global_load_lds_dwordx4 v5, s[60:61]\n"
    "s_mul_i32 s56, s45, 3\n"
    "s_add_u32 s56, s56, s44\n"
    "s_add_u32 s60, s8, s56\n"
    "s_addc_u32 s61, s9, 0\n"
    "s_mov_b32 m0, 26688\n"
    "s_nop 0\n"
    "global_load_lds_dwordx4 v5, s[60:61]\n"
    "s_mul_i32 s56, s45, 4\n"
    "s_add_u32 s56, s56, s44\n"
    "s_add_u32 s60, s8, s56\n"
    "s_addc_u32 s61, s9, 0\n"
    "s_mov_b32 m0, 35584\n"
    "s_nop 0\n"
    "global_load_lds_dwordx4 v5, s[60:61]\n"
    "s_mul_i32 s56, s45, 5\n"
    "s_add_u32 s56, s56, s44\n"
    "s_add_u32 s60, s8, s56\n"
    "s_addc_u32 s61, s9, 0\n"
    "s_mov_b32 m0, 44480\n"
    "s_nop 0\n"
    "global_load_lds_dwordx4 v5, s[60:61]\n"
    "s_mul_i32 s56, s45, 6\n"
    "s_add_u32 s56, s56, s44\n"
    "s_add_u32 s60, s8, s56\n"
    "s_addc_u32 s61, s9, 0\n"
    "s_mov_b32 m0, 53376\n"
    "s_nop 0\n"
    "global_load_lds_dwordx4 v5, s[60:61]\n"
    "s_mul_i32 s56, s45, 7\n"
    "s_add_u32 s56, s56, s44\n"
    "s_add_u32 s60, s8, s56\n"
    "s_addc_u32 s61, s9, 0\n"
    "s_mov_b32 m0, 62272\n"
    "s_nop 0\n"
    "global_load_lds_dwordx4 v5, s[60:61]\n"
    "s_mul_i32 s56, s45, 8\n"
    "s_add_u32 s56, s56, s44\n"
    "s_add_u32 s60, s8, s56\n"
    "s_addc_u32 s61, s9, 0\n"
    "s_mov_b32 m0, 71168\n"
    "s_nop 0\n"
    "global_load_lds_dwordx4 v5, s[60:61]\n"
    "s_waitcnt vmcnt(21)\n"
    "v_mov_b32 v17, 3\n"
    "ds_write_b32 v16, v17 offset:0\n"
    "s_waitcnt vmcnt(0)\n"
    "v_mov_b32 v17, 4\n"
    "ds_write_b32 v16, v17 offset:0\n"
    "s_branch .Li_sd\n"
    ".Li_s1:\n"
    "s_waitcnt vmcnt(12)\n"
    "v_mov_b32 v17, 1\n"
    "ds_write_b32 v16, v17 offset:0\n"
    "s_waitcnt vmcnt(6)\n"
    "v_mov_b32 v17, 2\n"
    "ds_write_b32 v16, v17 offset:0\n"
    "s_mul_i32 s56, s45, 2\n"
    "s_add_u32 s56, s56, s44\n"
    "s_add_u32 s60, s8, s56\n"
    "s_addc_u32 s61, s9, 0\n"
    "s_mov_b32 m0, 19840\n"
    "s_nop 0\n"
    "global_load_lds_dwordx4 v1, s[60:61]\n"
    "s_mov_b32 m0, 21888\n"
    "s_nop 0\n"
    "global_load_lds_dwordx4 v2, s[60:61]\n"
    "s_mov_b32 m0, 23936\n"
    "s_nop 0\n"
    "global_load_lds_dwordx4 v3, s[60:61]\n"
    "s_mov_b32 m0, 25568\n"
    "s_nop 0\n"
    "global_load_lds_dwordx4 v5, s[60:61]\n"
    "s_mul_i32 s56, s45, 1\n"
    "s_add_u32 s56, s56, s44\n"
    "s_add_u32 s60, s8, s56\n"
    "s_addc_u32 s61, s9, 0\n"
    "s_mov_b32 m0, 10944\n"
    "s_nop 0\n"
    "global_load_lds_dwordx4 v1, s[60:61]\n"
    "s_mov_b32 m0, 12992\n"
    "s_nop 0\n"
    "global_load_lds_dwordx4 v2, s[60:61]\n"
    "s_mov_b32 m0, 15040\n"
    "s_nop 0\n"
    "global_load_lds_dwordx4 v3, s[60:61]\n"
    "s_mov_b32 m0, 16672\n"
    "s_nop 0\n"
    "global_load_lds_dwordx4 v5, s[60:61]\n"
    "s_mul_i32 s56, s45, 0\n"
    "s_add_u32 s56, s56, s44\n"
    "s_add_u32 s60, s8, s56\n"
    "s_addc_u32 s61, s9, 0\n"
    "s_mov_b32 m0, 2048\n"
    "s_nop 0\n"
    "global_load_lds_dwordx4 v1, s[60:61]\n"
    "s_mov_b32 m0, 4096\n"
    "s_nop 0\n"
    "global_load_lds_dwordx4 v2, s[60:61]\n"
    "s_mov_b32 m0, 6144\n"
    "s_nop 0\n"
    "global_load_lds_dwordx4 v3, s[60:61]\n"
    "s_mov_b32 m0, 7776\n"
    "s_nop 0\n"
    "global_load_lds_dwordx4 v5, s[60:61]\n"
    "s_mul_i32 s56, s45, 3\n"
    "s_add_u32 s56, s56, s44\n"
    "s_add_u32 s60, s8, s56\n"
    "s_addc_u32 s61, s9, 0\n"
    "s_mov_b32 m0, 34464\n"
    "s_nop 0\n"
    "global_load_lds_dwordx4 v5, s[60:61]\n"
    "s_mul_i32 s56, s45, 4\n"
    "s_add_u32 s56, s56, s44\n"
    "s_add_u32 s60, s8, s56\n"
    "s_addc_u32 s61, s9, 0\n"
    "s_mov_b32 m0, 43360\n"
    "s_nop 0\n"
    "global_load_lds_dwordx4 v5, s[60:61]\n"
    "s_mul_i32 s56, s45, 5\n"
    "s_add_u32 s56, s56, s44\n"
    "s_add_u32 s60, s8, s56\n"
    "s_addc_u32 s61, s9, 0\n"
    "s_mov_b32 m0, 52256\n"
    "s_nop 0\n"
    "global_load_lds_dwordx4 v5, s[60:61]\n"
    "s_mul_i32 s56, s45, 6\n"
    "s_add_u32 s56, s56, s44\n"
    "s_add_u32 s60, s8, s56\n"
    "s_addc_u32 s61, s9, 0\n"
    "s_mov_b32 m0, 61152\n"
    "s_nop 0\n"
    "global_load_lds_dwordx4 v5, s[60:61]\n"
    "s_mul_i32 s56, s45, 7\n"
    "s_add_u32 s56, s56, s44\n"
    "s_add_u32 s60, s8, s56\n"
    "s_addc_u32 s61, s9, 0\n"
    "s_mov_b32 m0, 70048\n"
    "s_nop 0\n"
    "global_load_lds_dwordx4 v5, s[60:61]\n"
    "s_mul_i32 s56, s45, 8\n"
    "s_add_u32 s56, s56, s44\n"
    "s_add_u32 s60, s8, s56\n"
    "s_addc_u32 s61, s9, 0\n"
    "s_mov_b32 m0, 78944\n"
    "s_nop 0\n"
    "global_load_lds_dwordx4 v5, s[60:61]\n"
    "s_waitcnt vmcnt(18)\n"
    "v_mov_b32 v17, 3\n"
    "ds_write_b32 v16, v17 offset:0\n"
    "s_waitcnt vmcnt(0)\n"
    "v_mov_b32 v17, 4\n"
    "ds_write_b32 v16, v17 offset:0\n"
    "s_branch .Li_sd\n"
    ".Li_s2:\n"
    "s_waitcnt vmcnt(16)\n"
    "v_mov_b32 v17, 1\n"
    "ds_write_b32 v16, v17 offset:0\n"
    "s_waitcnt vmcnt(8)\n"
    "v_mov_b32 v17, 2\n"
    "ds_write_b32 v16, v17 offset:0\n"
    "s_waitcnt vmcnt(0)\n"
    "v_mov_b32 v17, 3\n"
    "ds_write_b32 v16, v17 offset:0\n"
    "s_branch .Li_sd\n"
    ".Li_s3:\n"
    "s_waitcnt vmcnt(12)\n"
    "v_mov_b32 v17, 1\n"
    "ds_write_b32 v16, v17 offset:0\n"
    "s_waitcnt vmcnt(6)\n"
    "v_mov_b32 v17, 2\n"
    "ds_write_b32 v16, v17 offset:0\n"
    "s_waitcnt vmcnt(0)\n"
    "v_mov_b32 v17, 3\n"
    "ds_write_b32 v16, v17 offset:0\n"
    "s_branch .Li_sd\n"
    ".Li_sd:\n"
    "s_setprio 3\n"
    "s_branch .Le_w\n"
    ".Le_b:\n"
    "s_barrier\n"
    ".Le_w:\n"
    "s_waitcnt lgkmcnt(0)\n"
        :
          "={s[8:15]}"(ka), "={s[16:17]}"(ka4), "={s4}"(t0), "={s18}"(ax[0]), "={s19}"(ax[1]), "={s20}"(ax[2]), "={s21}"(ax[3]),
          "={v1}"(xo[0]), "={v2}"(xo[1]), "={v3}"(xo[2]), "={v4}"(xo[3]), "={v5}"(xo4),
          "={v101}"(xq[0]), "={v102}"(xq[1]), "={v103}"(xq[2]), "={v104}"(xq[3]), "={v105}"(xq4), "+{v0}"(tid), "+{s2}"(bid)
        : "{s[0:1]}"(__builtin_amdgcn_kernarg_segment_ptr())
        : "memory", "scc", "vcc", "s3", "s5", "s7", "s22", "s42", "s43", "s44", "s45", "s46", "s47", "s48", "s49", "s50", "s51", "s87", "s56", "s60", "s61", "s23", "s24", "s25", "s26", "s27", "s28", "s29", "s30", "s31",
          "v6", "v7", "v8", "v9", "v10", "v11", "v12", "v13", "v14", "v15", "v16", "v17", "v18", "v19", "v20", "v21", "v22", "v23", "v24", "v25", "v26", "v27", "v28", "v29", "v30", "v31", "v32", "v33", "v34", "v35", "v36", "v37", "v38", "v39", "v40", "v41", "v42", "v43", "v44", "v45", "v46", "v47", "v48", "v49", "v50", "v51", "v52", "v53", "v54", "v55", "v56", "v57", "v58", "v59", "v60", "v61", "v62", "v63", "v64", "v65", "v66", "v67", "v68", "v69", "v70", "v71", "v72", "v73", "v74", "v75", "v76", "v77", "v78", "v79", "v80", "v81", "v82", "v83", "v84", "v85", "v86", "v87", "v88", "v89", "v90", "v91", "v92", "v93", "v94", "v95", "v96", "v97", "v98", "v99", "v100");
    auto mkptr = [](unsigned lo, unsigned hi) LAMBDA_INL { return (char*)(((u64)(unsigned)UNI(hi) << 32) | (unsigned)UNI(lo)); };
    const char* xb = mkptr(ka[0], ka[1]);
    char* ws = mkptr(ka4[0], ka4[1]);
    bid = UNI(bid);
    int lane = tid & 63;
    const int wave = __builtin_amdgcn_readfirstlane(tid >> 6);
    const int tq = bid >> 6, ts = (bid >> 3) & 7, strip = ts == 7 ? tq + 4 * (bid & 1) : ts;
    const int t12 = tq < 2 || ts == 7, hs = tq == 3 && ts != 7, redge = tq == 1 && ts != 7 && (bid & 1), cedge = strip == 6;
    const AxisMap rm = {UNI(ax[0]), UNI(ax[1]), bid & 4 ? 218 : 1, redge ? 11 : 99};
    const AxisMap cm = {UNI(ax[2]), UNI(ax[3]), bid & 2 ? 218 : 1, cedge ? 15 : 99};
    const unsigned vf = LF + 4 * (lane & 3);
    const u32x4 fl = {vf, lane < 2 ? LF + 4 * (wave & 1) + 8 * lane : OOB, lane < 1 ? LF + 4 * wave : OOB, 0};
    const int i16 = lane & 15, g = lane >> 4;
    const int e = (i16 + 4) >> 3 & 1, qj = i16 - 4 * ((i16 + 4) >> 3);

    if (strip == 7) {
        asm volatile("s_nop 7\n"
    "s_cmp_lg_u32 s46, 6\n"
    "s_cbranch_scc1 .Ls_end\n"
    "v_mov_b32 v1, 0\n"
    "global_load_dwordx2 v[12:13], v1, s[40:41] sc1\n"
    "v_and_b32 v2, 15, v0\n"
    "v_lshlrev_b32 v2, 4, v2\n"
    "s_waitcnt vmcnt(0)\n"
    "v_add_u32 v6, 0x7f4a7c15, v12\n"
    "v_add_u32 v7, 0x9e3779b9, v13\n"
    "v_add_u32 v4, 0x7f4a7c15, v12\n"
    "v_add_u32 v5, 0x9e3779b9, v13\n"
    "v_add_u32 v3, s48, v2\n"
    "global_store_dwordx4 v3, v[4:7], s[40:41] sc1\n"
    "s_cmp_eq_u32 s49, 0\n"
    "s_cbranch_scc1 .Ls_end\n"
    "v_and_b32 v2, 0x70, v2\n"
    "v_lshlrev_b32 v3, 10, v0\n"
    "v_add_u32 v3, 393216, v3\n"
    "s_mov_b32 s52, 0x10000\n"
    "s_sleep 110\n"
    ".Ls_p:\n"
    "s_sleep 8\n"
    "global_load_dwordx2 v[8:9], v3, s[40:41] offset:0 sc1\n"
    "global_load_dwordx2 v[10:11], v3, s[40:41] offset:128 sc1\n"
    "global_load_dwordx2 v[12:13], v3, s[40:41] offset:256 sc1\n"
    "global_load_dwordx2 v[14:15], v3, s[40:41] offset:384 sc1\n"
    "global_load_dwordx2 v[16:17], v3, s[40:41] offset:512 sc1\n"
    "global_load_dwordx2 v[18:19], v3, s[40:41] offset:640 sc1\n"
    "global_load_dwordx2 v[20:21], v3, s[40:41] offset:768 sc1\n"
    "global_load_dwordx2 v[22:23], v3, s[40:41] offset:896 sc1\n"
    "s_mov_b64 s[46:47], 0\n"
    "s_waitcnt vmcnt(0)\n"
    "v_cmp_ne_u64 vcc, v[4:5], v[8:9]\n"
    "s_or_b64 s[46:47], s[46:47], vcc\n"
    "v_cmp_ne_u64 vcc, v[4:5], v[10:11]\n"
    "s_or_b64 s[46:47], s[46:47], vcc\n"
    "v_cmp_ne_u64 vcc, v[4:5], v[12:13]\n"
    "s_or_b64 s[46:47], s[46:47], vcc\n"
    "v_cmp_ne_u64 vcc, v[4:5], v[14:15]\n"
    "s_or_b64 s[46:47], s[46:47], vcc\n"
    "v_cmp_ne_u64 vcc, v[4:5], v[16:17]\n"
    "s_or_b64 s[46:47], s[46:47], vcc\n"
    "v_cmp_ne_u64 vcc, v[4:5], v[18:19]\n"
    "s_or_b64 s[46:47], s[46:47], vcc\n"
    "v_cmp_ne_u64 vcc, v[4:5], v[20:21]\n"
    "s_or_b64 s[46:47], s[46:47], vcc\n"
    "v_cmp_ne_u64 vcc, v[4:5], v[22:23]\n"
    "s_or_b64 s[46:47], s[46:47], vcc\n"
    "s_cbranch_scc0 .Ls_c\n"
    "s_sub_u32 s52, s52, 1\n"
    "s_cmp_lg_u32 s52, 0\n"
    "s_cbranch_scc1 .Ls_p\n"
    ".Ls_c:\n"
    "global_store_dwordx4 v2, v[4:7], s[40:41] sc1\n"
    ".Ls_end:\n"
    "s_endpgm\n"
    ".Ls_last:\n"
            :: "{v0}"(lane), "{s[40:41]}"(ws), "{s46}"(wave), "{s48}"(UNI(WS_ACK + 2 * bid * 128)), "{s49}"(UNI(bid == 253))
            : "memory", "v23", "s47", "s52", "vcc", "scc");
    } else if (wave < 4) {
        const int h = wave & 1;
        const unsigned l16 = lane * 16;
        const int nf = min(lane, 35) + 36 * h;
        const unsigned z0 = LQ + (lane & 3) * 256 + (4 * ((2 * (g >> 1)) ^ (lane & 3)) + ((lane >> 2) & 3)) * 16 + (g & 1) * 8;
        const u32x2 vz = {z0, z0 ^ 64};
        const unsigned vi = LF + 320 + 4 * ((i16 + 8) & 15);
        const unsigned c0 = axis_at(cm, 3 + qj) * PIXB + 16 * g, c1 = axis_at(cm, 11 + qj) * PIXB + 16 * g;
        const u32x2 dd = {(unsigned)((axis_at(rm, 11 + e) - axis_at(rm, 9 + e)) * IROWB), (unsigned)((axis_at(rm, 13 + e) - axis_at(rm, 11 + e)) * IROWB)};
        const unsigned d56 = 2 * rm.d * IROWB + (hs ? c1 - c0 : 0);
        const u32x4 srd = {(unsigned)UNI(ka[6]), (unsigned)UNI(ka[7]) & 0xffffu, (unsigned)(IMW * IROWB), 0x00020000u};
        if (wave >= 2) {
            const unsigned b = pixad(3 + qj) + e * RP + 16 * g;
            const u32x2 va = {b, b + 3200};
            const u32x2 vb = {(unsigned)(e ? -9 * RP : 5 * RP), LQ + l16 + h * 3072};
            const u32x2 lf = {l16, (unsigned)(WS_FL + ((bid & (NREP - 1)) * 144 + nf) * 128)};
            constexpr int N0 = 9, UE = (N0 + 1) / 2, UO = N0 / 2;
            u32x4 to;
            if (h != 0) {
                to = (u32x4){axis_at(rm, 3 + 2 * UE + e) * IROWB + c0, axis_at(rm, 3 + 2 * UO + e) * IROWB + c1, dd[0], dd[1]};
            } else {
                const AttC c = att_const(lane, hs, cedge);
                to = (u32x4){c.sb, c.v0, c.v1, c.pk};
            }
            asm volatile("s_nop 7\n"
    "s_mov_b32 s52, 0x40000\n"
    "s_load_dwordx2 s[78:79], s[40:41], 0x0\n"
    "v_mov_b32 v227, 0\n"
    "global_load_dword v254, v227, s[40:41]\n"
    "s_mul_i32 s53, s50, 36864\n"
    "s_add_u32 s57, s53, 11264\n"
    "s_add_u32 s64, s40, s57\n"
    "s_addc_u32 s65, s41, 0\n"
    "s_add_u32 s57, s53, 17408\n"
    "s_add_u32 s66, s40, s57\n"
    "s_addc_u32 s67, s41, 0\n"
    "s_add_u32 s57, s53, 23552\n"
    "s_add_u32 s68, s40, s57\n"
    "s_addc_u32 s69, s41, 0\n"
    "s_add_u32 s57, s53, 29696\n"
    "s_add_u32 s70, s40, s57\n"
    "s_addc_u32 s71, s41, 0\n"
    "s_add_u32 s57, s53, 35840\n"
    "s_add_u32 s72, s40, s57\n"
    "s_addc_u32 s73, s41, 0\n"
    "s_add_u32 s57, s53, 41984\n"
    "s_add_u32 s74, s40, s57\n"
    "s_addc_u32 s75, s41, 0\n"
    "v_add_u32 v4, 71168, v2\n"
    "v_add_u32 v5, 71168, v3\n"
    "s_lshl_b32 s54, s50, 10\n"
    "s_add_u32 s54, s54, 1024\n"
    "s_mul_i32 s56, s50, 7776\n"
    "s_cmp_eq_u32 s50, 0\n"
    "s_cbranch_scc1 .Lg_x0\n"
    "v_mov_b32 v233, v234\n"
    ".Lg_x0:\n"
    "s_max_u32 s55, s56, 7168\n"
    "v_add_u32 v254, -8, v226\n"
    ".Lg_dl:\n"
    "s_sleep 1\n"
    "s_memtime s[76:77]\n"
    "s_waitcnt lgkmcnt(0)\n"
    "s_sub_u32 s76, s76, s59\n"
    "s_cmp_lt_i32 s76, 0\n"
    "s_cbranch_scc1 .Lg_dl\n"
    "s_waitcnt lgkmcnt(0)\n"
    "s_add_u32 s78, s78, 0x7f4a7c15\n"
    "s_add_u32 s79, s79, 0x9e3779b9\n"
    "v_mov_b64 v[240:241], s[78:79]\n"
    "s_mov_b32 s51, 0x4000\n"
    ".Lg_retry:\n"
    "v_mov_b64 v[244:245], v[240:241]\n"
    "v_mov_b64 v[246:247], 0\n"
    "v_mov_b64 v[242:243], 0\n"
    "s_mov_b32 exec_hi, 15\n"
    "global_load_dwordx4 v[244:247], v236, s[40:41] sc1\n"
    "s_mov_b64 exec, -1\n"
    "ds_read_b32 v228, v224 offset:48\n"
    "s_waitcnt lgkmcnt(0)\n"
    "v_cmp_gt_u32 vcc, 1, v228\n"
    "s_cbranch_vccnz .Lg1\n"
    ".Lg1b:\n"
    "global_load_dwordx4 v[8:11], v235, s[64:65] offset:-3072 sc1\n"
    "global_load_dwordx4 v[32:35], v235, s[66:67] offset:-3072 sc1\n"
    "global_load_dwordx4 v[12:15], v235, s[64:65] offset:-2048 sc1\n"
    "global_load_dwordx4 v[36:39], v235, s[66:67] offset:-2048 sc1\n"
    "global_load_dwordx4 v[16:19], v235, s[64:65] offset:-1024 sc1\n"
    "global_load_dwordx4 v[40:43], v235, s[66:67] offset:-1024 sc1\n"
    "global_load_dwordx4 v[20:23], v235, s[64:65] offset:0 sc1\n"
    "global_load_dwordx4 v[44:47], v235, s[66:67] offset:0 sc1\n"
    "global_load_dwordx4 v[24:27], v235, s[64:65] offset:1024 sc1\n"
    "global_load_dwordx4 v[48:51], v235, s[66:67] offset:1024 sc1\n"
    "global_load_dwordx4 v[28:31], v235, s[64:65] offset:2048 sc1\n"
    "global_load_dwordx4 v[52:55], v235, s[66:67] offset:2048 sc1\n"
    "global_load_dwordx4 v[56:59], v235, s[68:69] offset:-3072 sc1\n"
    "global_load_dwordx4 v[80:83], v235, s[70:71] offset:-3072 sc1\n"
    "global_load_dwordx4 v[60:63], v235, s[68:69] offset:-2048 sc1\n"
    "global_load_dwordx4 v[84:87], v235, s[70:71] offset:-2048 sc1\n"
    "global_load_dwordx4 v[64:67], v235, s[68:69] offset:-1024 sc1\n"
    "global_load_dwordx4 v[88:91], v235, s[70:71] offset:-1024 sc1\n"
    "global_load_dwordx4 v[68:71], v235, s[68:69] offset:0 sc1\n"
    "global_load_dwordx4 v[92:95], v235, s[70:71] offset:0 sc1\n"
    "s_waitcnt vmcnt(20)\n"
    "v_cmp_ne_u64 vcc, v[240:241], v[244:245]\n"
    "s_cmp_gt_i32 s51, 0\n"
    "s_cbranch_scc0 .Lg_pd\n"
    "s_cbranch_vccnz .Lg_poll\n"
    ".Lg_pd:\n"
    "ds_read_b32 v228, v224 offset:0\n"
    "s_waitcnt lgkmcnt(0)\n"
    "v_cmp_gt_u32 vcc, 1, v228\n"
    "s_cbranch_vccnz .Lg3\n"
    ".Lg3b:\n"
    "ds_read_b128 v[152:155], v2 offset:26688\n"
    "ds_read_b128 v[156:159], v2 offset:26752\n"
    "ds_read_b128 v[160:163], v2 offset:26816\n"
    "ds_read_b128 v[164:167], v2 offset:26880\n"
    "ds_read_b128 v[168:171], v2 offset:26944\n"
    "ds_read_b128 v[172:175], v2 offset:27008\n"
    "s_waitcnt vmcnt(18)\n"
    "v_add3_u32 v242, v242, v8, v11\n"
    "v_sub_u32 v8, v8, v240\n"
    "v_add3_u32 v242, v242, v32, v35\n"
    "v_sub_u32 v32, v32, v240\n"
    "s_waitcnt vmcnt(16)\n"
    "v_add3_u32 v242, v242, v12, v15\n"
    "v_sub_u32 v12, v12, v240\n"
    "s_waitcnt lgkmcnt(5)\n"
    "v_mfma_f32_16x16x32_bf16 v[200:203], v[8:11], v[152:155], 0\n"
    "global_load_dwordx4 v[72:75], v235, s[68:69] offset:1024 sc1\n"
    "v_add3_u32 v242, v242, v36, v39\n"
    "v_sub_u32 v36, v36, v240\n"
    "v_mfma_f32_16x16x32_bf16 v[204:207], v[32:35], v[152:155], 0\n"
    "global_load_dwordx4 v[96:99], v235, s[70:71] offset:1024 sc1\n"
    "s_waitcnt vmcnt(16)\n"
    "v_add3_u32 v242, v242, v16, v19\n"
    "v_sub_u32 v16, v16, v240\n"
    "s_waitcnt lgkmcnt(4)\n"
    "v_mfma_f32_16x16x32_bf16 v[200:203], v[12:15], v[156:159], v[200:203]\n"
    "global_load_dwordx4 v[76:79], v235, s[68:69] offset:2048 sc1\n"
    "v_add3_u32 v242, v242, v40, v43\n"
    "v_sub_u32 v40, v40, v240\n"
    "v_mfma_f32_16x16x32_bf16 v[204:207], v[36:39], v[156:159], v[204:207]\n"
    "global_load_dwordx4 v[100:103], v235, s[70:71] offset:2048 sc1\n"
    "s_waitcnt vmcnt(16)\n"
    "v_add3_u32 v242, v242, v20, v23\n"
    "v_sub_u32 v20, v20, v240\n"
    "s_waitcnt lgkmcnt(3)\n"
    "v_mfma_f32_16x16x32_bf16 v[200:203], v[16:19], v[160:163], v[200:203]\n"
    "global_load_dwordx4 v[104:107], v235, s[72:73] offset:-3072 sc1\n"
    "v_add3_u32 v242, v242, v44, v47\n"
    "v_sub_u32 v44, v44, v240\n"
    "v_mfma_f32_16x16x32_bf16 v[204:207], v[40:43], v[160:163], v[204:207]\n"
    "global_load_dwordx4 v[128:131], v235, s[74:75] offset:-3072 sc1\n"
    "s_waitcnt vmcnt(16)\n"
    "v_add3_u32 v242, v242, v24, v27\n"
    "v_sub_u32 v24, v24, v240\n"
    "s_waitcnt lgkmcnt(2)\n"
    "v_mfma_f32_16x16x32_bf16 v[200:203], v[20:23], v[164:167], v[200:203]\n"
    "global_load_dwordx4 v[108:111], v235, s[72:73] offset:-2048 sc1\n"
    "v_add3_u32 v242, v242, v48, v51\n"
    "v_sub_u32 v48, v48, v240\n"
    "v_mfma_f32_16x16x32_bf16 v[204:207], v[44:47], v[164:167], v[204:207]\n"
    "global_load_dwordx4 v[132:135], v235, s[74:75] offset:-2048 sc1\n"
    "s_waitcnt vmcnt(16)\n"
    "v_add3_u32 v242, v242, v28, v31\n"
    "v_sub_u32 v28, v28, v240\n"
    "s_waitcnt lgkmcnt(1)\n"
    "v_mfma_f32_16x16x32_bf16 v[200:203], v[24:27], v[168:171], v[200:203]\n"
    "global_load_dwordx4 v[112:115], v235, s[72:73] offset:-1024 sc1\n"
    "v_add3_u32 v242, v242, v52, v55\n"
    "v_sub_u32 v52, v52, v240\n"
    "v_mfma_f32_16x16x32_bf16 v[204:207], v[48:51], v[168:171], v[204:207]\n"
    "global_load_dwordx4 v[136:139], v235, s[74:75] offset:-1024 sc1\n"
    "s_waitcnt lgkmcnt(0)\n"
    "v_mfma_f32_16x16x32_bf16 v[200:203], v[28:31], v[172:175], v[200:203]\n"
    "global_load_dwordx4 v[116:119], v235, s[72:73] offset:0 sc1\n"
    "v_mov_b32 v227, 1\n"
    "v_mfma_f32_16x16x32_bf16 v[204:207], v[52:55], v[172:175], v[204:207]\n"
    "global_load_dwordx4 v[140:143], v235, s[74:75] offset:0 sc1\n"
    "ds_write_b32 v225, v227 offset:256\n"
    "s_waitcnt vmcnt(18)\n"
    "v_add3_u32 v242, v242, v56, v59\n"
    "v_sub_u32 v56, v56, v240\n"
    "v_add3_u32 v242, v242, v80, v83\n"
    "v_sub_u32 v80, v80, v240\n"
    "s_waitcnt vmcnt(16)\n"
    "v_add3_u32 v242, v242, v60, v63\n"
    "v_sub_u32 v60, v60, v240\n"
    "v_mfma_f32_16x16x32_bf16 v[208:211], v[56:59], v[152:155], 0\n"
    "global_load_dwordx4 v[120:123], v235, s[72:73] offset:1024 sc1\n"
    "v_add3_u32 v242, v242, v84, v87\n"
    "v_sub_u32 v84, v84, v240\n"
    "v_mfma_f32_16x16x32_bf16 v[212:215], v[80:83], v[152:155], 0\n"
    "global_load_dwordx4 v[144:147], v235, s[74:75] offset:1024 sc1\n"
    "s_waitcnt vmcnt(16)\n"
    "v_add3_u32 v242, v242, v64, v67\n"
    "v_sub_u32 v64, v64, v240\n"
    "v_mfma_f32_16x16x32_bf16 v[208:211], v[60:63], v[156:159], v[208:211]\n"
    "global_load_dwordx4 v[124:127], v235, s[72:73] offset:2048 sc1\n"
    "v_add3_u32 v242, v242, v88, v91\n"
    "v_sub_u32 v88, v88, v240\n"
    "v_mfma_f32_16x16x32_bf16 v[212:215], v[84:87], v[156:159], v[212:215]\n"
    "global_load_dwordx4 v[148:151], v235, s[74:75] offset:2048 sc1\n"
    "s_waitcnt vmcnt(16)\n"
    "v_add3_u32 v242, v242, v68, v71\n"
    "v_sub_u32 v68, v68, v240\n"
    "v_mfma_f32_16x16x32_bf16 v[208:211], v[64:67], v[160:163], v[208:211]\n"
    "v_cvt_pk_bf16_f32 v200, v200, v201\n"
    "v_add3_u32 v242, v242, v92, v95\n"
    "v_sub_u32 v92, v92, v240\n"
    "v_mfma_f32_16x16x32_bf16 v[212:215], v[88:91], v[160:163], v[212:215]\n"
    "v_cvt_pk_bf16_f32 v201, v202, v203\n"
    "s_waitcnt vmcnt(14)\n"
    "v_add3_u32 v242, v242, v72, v75\n"
    "v_sub_u32 v72, v72, v240\n"
    "v_mfma_f32_16x16x32_bf16 v[208:211], v[68:71], v[164:167], v[208:211]\n"
    "v_cvt_pk_bf16_f32 v202, v204, v205\n"
    "v_add3_u32 v242, v242, v96, v99\n"
    "v_sub_u32 v96, v96, v240\n"
    "v_mfma_f32_16x16x32_bf16 v[212:215], v[92:95], v[164:167], v[212:215]\n"
    "v_cvt_pk_bf16_f32 v203, v206, v207\n"
    "s_waitcnt vmcnt(12)\n"
    "v_add3_u32 v242, v242, v76, v79\n"
    "v_sub_u32 v76, v76, v240\n"
    "v_mfma_f32_16x16x32_bf16 v[208:211], v[72:75], v[168:171], v[208:211]\n"
    "v_add3_u32 v242, v242, v100, v103\n"
    "v_sub_u32 v100, v100, v240\n"
    "v_mfma_f32_16x16x32_bf16 v[212:215], v[96:99], v[168:171], v[212:215]\n"
    "v_mfma_f32_16x16x32_bf16 v[208:211], v[76:79], v[172:175], v[208:211]\n"
    "ds_write_b128 v7, v[200:203] offset:0\n"
    "v_mfma_f32_16x16x32_bf16 v[212:215], v[100:103], v[172:175], v[212:215]\n"
    "s_waitcnt vmcnt(10)\n"
    "v_add3_u32 v242, v242, v104, v107\n"
    "v_sub_u32 v104, v104, v240\n"
    "v_add3_u32 v242, v242, v128, v131\n"
    "v_sub_u32 v128, v128, v240\n"
    "s_waitcnt vmcnt(8)\n"
    "v_add3_u32 v242, v242, v108, v111\n"
    "v_sub_u32 v108, v108, v240\n"
    "v_mfma_f32_16x16x32_bf16 v[216:219], v[104:107], v[152:155], 0\n"
    "ds_read_b128 v[176:179], v3 offset:26688\n"
    "v_add3_u32 v242, v242, v132, v135\n"
    "v_sub_u32 v132, v132, v240\n"
    "v_mfma_f32_16x16x32_bf16 v[220:223], v[128:131], v[152:155], 0\n"
    "ds_read_b128 v[180:183], v3 offset:26752\n"
    "s_waitcnt vmcnt(6)\n"
    "v_add3_u32 v242, v242, v112, v115\n"
    "v_sub_u32 v112, v112, v240\n"
    "v_mfma_f32_16x16x32_bf16 v[216:219], v[108:111], v[156:159], v[216:219]\n"
    "ds_read_b128 v[184:187], v3 offset:26816\n"
    "v_add3_u32 v242, v242, v136, v139\n"
    "v_sub_u32 v136, v136, v240\n"
    "v_mfma_f32_16x16x32_bf16 v[220:223], v[132:135], v[156:159], v[220:223]\n"
    "ds_read_b128 v[188:191], v3 offset:26880\n"
    "s_waitcnt vmcnt(4)\n"
    "v_add3_u32 v242, v242, v116, v119\n"
    "v_sub_u32 v116, v116, v240\n"
    "v_mfma_f32_16x16x32_bf16 v[216:219], v[112:115], v[160:163], v[216:219]\n"
    "ds_read_b128 v[192:195], v3 offset:26944\n"
    "v_add3_u32 v242, v242, v140, v143\n"
    "v_sub_u32 v140, v140, v240\n"
    "v_mfma_f32_16x16x32_bf16 v[220:223], v[136:139], v[160:163], v[220:223]\n"
    "ds_read_b128 v[196:199], v3 offset:27008\n"
    "s_waitcnt vmcnt(2)\n"
    "v_add3_u32 v242, v242, v120, v123\n"
    "v_sub_u32 v120, v120, v240\n"
    "v_mfma_f32_16x16x32_bf16 v[216:219], v[116:119], v[164:167], v[216:219]\n"
    "v_cvt_pk_bf16_f32 v208, v208, v209\n"
    "v_add3_u32 v242, v242, v144, v147\n"
    "v_sub_u32 v144, v144, v240\n"
    "v_mfma_f32_16x16x32_bf16 v[220:223], v[140:143], v[164:167], v[220:223]\n"
    "v_cvt_pk_bf16_f32 v209, v210, v211\n"
    "s_waitcnt vmcnt(0)\n"
    "v_add3_u32 v242, v242, v124, v127\n"
    "v_sub_u32 v124, v124, v240\n"
    "v_mfma_f32_16x16x32_bf16 v[216:219], v[120:123], v[168:171], v[216:219]\n"
    "v_cvt_pk_bf16_f32 v210, v212, v213\n"
    "v_add3_u32 v242, v242, v148, v151\n"
    "v_sub_u32 v148, v148, v240\n"
    "v_mfma_f32_16x16x32_bf16 v[220:223], v[144:147], v[168:171], v[220:223]\n"
    "v_cvt_pk_bf16_f32 v211, v214, v215\n"
    "v_mfma_f32_16x16x32_bf16 v[216:219], v[124:127], v[172:175], v[216:219]\n"
    "ds_write_b128 v7, v[208:211] offset:1024\n"
    "v_mfma_f32_16x16x32_bf16 v[220:223], v[148:151], v[172:175], v[220:223]\n"
    "v_cmp_ne_u64 s[76:77], v[240:241], v[244:245]\n"
    "v_sub_u32 v242, v242, v246\n"
    "v_mov_b32 v243, v242\n"
    "s_nop 1\n"
    "v_permlane32_swap_b32 v242, v243\n"
    "v_add_u32 v242, v242, v243\n"
    "v_cvt_pk_bf16_f32 v216, v216, v217\n"
    "v_cvt_pk_bf16_f32 v217, v218, v219\n"
    "v_add_u32_dpp v242, v242, v242 row_ror:8 row_mask:0xf bank_mask:0xf\n"
    "v_cvt_pk_bf16_f32 v218, v220, v221\n"
    "v_cvt_pk_bf16_f32 v219, v222, v223\n"
    "v_add_u32_dpp v242, v242, v242 row_ror:4 row_mask:0xf bank_mask:0xf\n"
    "ds_write_b128 v7, v[216:219] offset:2048\n"
    "s_nop 0\n"
    "v_add_u32_dpp v242, v242, v242 row_ror:2 row_mask:0xf bank_mask:0xf\n"
    "s_nop 1\n"
    "v_add_u32_dpp v242, v242, v242 row_ror:1 row_mask:0xf bank_mask:0xf\n"
    "v_mov_b32 v243, v242\n"
    "s_nop 1\n"
    "v_permlane16_swap_b32 v242, v243\n"
    "v_add_u32 v242, v242, v243\n"
    "v_cmp_ne_u32 vcc, 0, v242\n"
    "v_mov_b32 v227, 1\n"
    "s_cmp_lg_u64 vcc, 0\n"
    "s_cbranch_scc1 .Lg_fail\n"
    "s_cmp_lg_u64 s[76:77], 0\n"
    "s_cbranch_scc1 .Lg_fail\n"
    ".Lg_ok:\n"
    "ds_write_b32 v225, v227 offset:16\n"
    "v_mov_b32 v242, 0\n"
    "s_waitcnt lgkmcnt(3)\n"
    ".Lg_t1:\n"
    "v_mfma_f32_16x16x32_bf16 v[200:203], v[8:11], v[176:179], 0\n"
    "v_mfma_f32_16x16x32_bf16 v[204:207], v[32:35], v[176:179], 0\n"
    "v_mfma_f32_16x16x32_bf16 v[200:203], v[12:15], v[180:183], v[200:203]\n"
    "v_mfma_f32_16x16x32_bf16 v[204:207], v[36:39], v[180:183], v[204:207]\n"
    "v_mfma_f32_16x16x32_bf16 v[200:203], v[16:19], v[184:187], v[200:203]\n"
    "v_mfma_f32_16x16x32_bf16 v[204:207], v[40:43], v[184:187], v[204:207]\n"
    "v_mfma_f32_16x16x32_bf16 v[200:203], v[20:23], v[188:191], v[200:203]\n"
    "v_mfma_f32_16x16x32_bf16 v[204:207], v[44:47], v[188:191], v[204:207]\n"
    "v_mfma_f32_16x16x32_bf16 v[200:203], v[24:27], v[192:195], v[200:203]\n"
    "v_mfma_f32_16x16x32_bf16 v[204:207], v[48:51], v[192:195], v[204:207]\n"
    "v_mfma_f32_16x16x32_bf16 v[200:203], v[28:31], v[196:199], v[200:203]\n"
    "v_mfma_f32_16x16x32_bf16 v[204:207], v[52:55], v[196:199], v[204:207]\n"
    "v_mfma_f32_16x16x32_bf16 v[208:211], v[56:59], v[176:179], 0\n"
    "v_mfma_f32_16x16x32_bf16 v[212:215], v[80:83], v[176:179], 0\n"
    "ds_read_b32 v229, v224 offset:0\n"
    "v_mfma_f32_16x16x32_bf16 v[208:211], v[60:63], v[180:183], v[208:211]\n"
    "v_mfma_f32_16x16x32_bf16 v[212:215], v[84:87], v[180:183], v[212:215]\n"
    "v_mfma_f32_16x16x32_bf16 v[208:211], v[64:67], v[184:187], v[208:211]\n"
    "v_mfma_f32_16x16x32_bf16 v[212:215], v[88:91], v[184:187], v[212:215]\n"
    "v_cvt_pk_bf16_f32 v200, v200, v201\n"
    "v_cvt_pk_bf16_f32 v201, v202, v203\n"
    "v_mfma_f32_16x16x32_bf16 v[208:211], v[68:71], v[188:191], v[208:211]\n"
    "v_cvt_pk_bf16_f32 v202, v204, v205\n"
    "v_cvt_pk_bf16_f32 v203, v206, v207\n"
    "v_mfma_f32_16x16x32_bf16 v[212:215], v[92:95], v[188:191], v[212:215]\n"
    "ds_write_b128 v7, v[200:203] offset:6144\n"
    "v_mfma_f32_16x16x32_bf16 v[208:211], v[72:75], v[192:195], v[208:211]\n"
    "v_mfma_f32_16x16x32_bf16 v[212:215], v[96:99], v[192:195], v[212:215]\n"
    "s_waitcnt lgkmcnt(1)\n"
    "v_cmp_gt_u32 vcc, 2, v229\n"
    "v_mfma_f32_16x16x32_bf16 v[208:211], v[76:79], v[196:199], v[208:211]\n"
    "v_mfma_f32_16x16x32_bf16 v[212:215], v[100:103], v[196:199], v[212:215]\n"
    "s_cbranch_vccnz .Lg4\n"
    ".Lg4b:\n"
    "v_mfma_f32_16x16x32_bf16 v[216:219], v[104:107], v[176:179], 0\n"
    "ds_read_b128 v[152:155], v2 offset:44480\n"
    "ds_read_b128 v[156:159], v2 offset:44544\n"
    "v_mfma_f32_16x16x32_bf16 v[220:223], v[128:131], v[176:179], 0\n"
    "ds_read_b128 v[160:163], v2 offset:44608\n"
    "ds_read_b128 v[164:167], v2 offset:44672\n"
    "v_mfma_f32_16x16x32_bf16 v[216:219], v[108:111], v[180:183], v[216:219]\n"
    "ds_read_b128 v[168:171], v2 offset:44736\n"
    "ds_read_b128 v[172:175], v2 offset:44800\n"
    "v_mfma_f32_16x16x32_bf16 v[220:223], v[132:135], v[180:183], v[220:223]\n"
    "v_cvt_pk_bf16_f32 v208, v208, v209\n"
    "v_cvt_pk_bf16_f32 v209, v210, v211\n"
    "v_mfma_f32_16x16x32_bf16 v[216:219], v[112:115], v[184:187], v[216:219]\n"
    "v_cvt_pk_bf16_f32 v210, v212, v213\n"
    "v_cvt_pk_bf16_f32 v211, v214, v215\n"
    "v_mfma_f32_16x16x32_bf16 v[220:223], v[136:139], v[184:187], v[220:223]\n"
    "ds_write_b128 v7, v[208:211] offset:7168\n"
    "v_mfma_f32_16x16x32_bf16 v[216:219], v[116:119], v[188:191], v[216:219]\n"
    "v_mfma_f32_16x16x32_bf16 v[220:223], v[140:143], v[188:191], v[220:223]\n"
    "v_mfma_f32_16x16x32_bf16 v[216:219], v[120:123], v[192:195], v[216:219]\n"
    "v_mfma_f32_16x16x32_bf16 v[220:223], v[144:147], v[192:195], v[220:223]\n"
    "v_mfma_f32_16x16x32_bf16 v[216:219], v[124:127], v[196:199], v[216:219]\n"
    "v_mfma_f32_16x16x32_bf16 v[220:223], v[148:151], v[196:199], v[220:223]\n"
    "s_waitcnt lgkmcnt(1)\n"
    "v_mfma_f32_16x16x32_bf16 v[200:203], v[8:11], v[152:155], 0\n"
    "v_mov_b32 v227, 2\n"
    "v_mfma_f32_16x16x32_bf16 v[204:207], v[32:35], v[152:155], 0\n"
    "v_mfma_f32_16x16x32_bf16 v[200:203], v[12:15], v[156:159], v[200:203]\n"
    "v_mfma_f32_16x16x32_bf16 v[204:207], v[36:39], v[156:159], v[204:207]\n"
    "v_mfma_f32_16x16x32_bf16 v[200:203], v[16:19], v[160:163], v[200:203]\n"
    "v_cvt_pk_bf16_f32 v216, v216, v217\n"
    "v_cvt_pk_bf16_f32 v217, v218, v219\n"
    "v_mfma_f32_16x16x32_bf16 v[204:207], v[40:43], v[160:163], v[204:207]\n"
    "v_cvt_pk_bf16_f32 v218, v220, v221\n"
    "v_cvt_pk_bf16_f32 v219, v222, v223\n"
    "v_mfma_f32_16x16x32_bf16 v[200:203], v[20:23], v[164:167], v[200:203]\n"
    "ds_write_b128 v7, v[216:219] offset:8192\n"
    "v_mfma_f32_16x16x32_bf16 v[204:207], v[44:47], v[164:167], v[204:207]\n"
    "ds_write_b32 v225, v227 offset:16\n"
    "v_mfma_f32_16x16x32_bf16 v[200:203], v[24:27], v[168:171], v[200:203]\n"
    "v_mfma_f32_16x16x32_bf16 v[204:207], v[48:51], v[168:171], v[204:207]\n"
    "v_mfma_f32_16x16x32_bf16 v[200:203], v[28:31], v[172:175], v[200:203]\n"
    "v_mfma_f32_16x16x32_bf16 v[204:207], v[52:55], v[172:175], v[204:207]\n"
    "v_mfma_f32_16x16x32_bf16 v[208:211], v[56:59], v[152:155], 0\n"
    "v_mfma_f32_16x16x32_bf16 v[212:215], v[80:83], v[152:155], 0\n"
    "v_mfma_f32_16x16x32_bf16 v[208:211], v[60:63], v[156:159], v[208:211]\n"
    "v_mfma_f32_16x16x32_bf16 v[212:215], v[84:87], v[156:159], v[212:215]\n"
    "v_mfma_f32_16x16x32_bf16 v[208:211], v[64:67], v[160:163], v[208:211]\n"
    "v_mfma_f32_16x16x32_bf16 v[212:215], v[88:91], v[160:163], v[212:215]\n"
    "v_mfma_f32_16x16x32_bf16 v[208:211], v[68:71], v[164:167], v[208:211]\n"
    "v_cvt_pk_bf16_f32 v200, v200, v201\n"
    "v_cvt_pk_bf16_f32 v201, v202, v203\n"
    "v_mfma_f32_16x16x32_bf16 v[212:215], v[92:95], v[164:167], v[212:215]\n"
    "v_cvt_pk_bf16_f32 v202, v204, v205\n"
    "v_cvt_pk_bf16_f32 v203, v206, v207\n"
    "v_mfma_f32_16x16x32_bf16 v[208:211], v[72:75], v[168:171], v[208:211]\n"
    "ds_write_b128 v7, v[200:203] offset:12288\n"
    "v_mfma_f32_16x16x32_bf16 v[212:215], v[96:99], v[168:171], v[212:215]\n"
    "v_mfma_f32_16x16x32_bf16 v[208:211], v[76:79], v[172:175], v[208:211]\n"
    "v_mfma_f32_16x16x32_bf16 v[212:215], v[100:103], v[172:175], v[212:215]\n"
    "v_mfma_f32_16x16x32_bf16 v[216:219], v[104:107], v[152:155], 0\n"
    "ds_read_b128 v[176:179], v3 offset:44480\n"
    "ds_read_b128 v[180:183], v3 offset:44544\n"
    "v_mfma_f32_16x16x32_bf16 v[220:223], v[128:131], v[152:155], 0\n"
    "ds_read_b128 v[184:187], v3 offset:44608\n"
    "ds_read_b128 v[188:191], v3 offset:44672\n"
    "v_mfma_f32_16x16x32_bf16 v[216:219], v[108:111], v[156:159], v[216:219]\n"
    "ds_read_b128 v[192:195], v3 offset:44736\n"
    "ds_read_b128 v[196:199], v3 offset:44800\n"
    "v_mfma_f32_16x16x32_bf16 v[220:223], v[132:135], v[156:159], v[220:223]\n"
    "v_cvt_pk_bf16_f32 v208, v208, v209\n"
    "v_cvt_pk_bf16_f32 v209, v210, v211\n"
    "v_mfma_f32_16x16x32_bf16 v[216:219], v[112:115], v[160:163], v[216:219]\n"
    "v_cvt_pk_bf16_f32 v210, v212, v213\n"
    "v_cvt_pk_bf16_f32 v211, v214, v215\n"
    "v_mfma_f32_16x16x32_bf16 v[220:223], v[136:139], v[160:163], v[220:223]\n"
    "ds_write_b128 v7, v[208:211] offset:13312\n"
    "v_mfma_f32_16x16x32_bf16 v[216:219], v[116:119], v[164:167], v[216:219]\n"
    "v_mfma_f32_16x16x32_bf16 v[220:223], v[140:143], v[164:167], v[220:223]\n"
    "v_mfma_f32_16x16x32_bf16 v[216:219], v[120:123], v[168:171], v[216:219]\n"
    "v_mfma_f32_16x16x32_bf16 v[220:223], v[144:147], v[168:171], v[220:223]\n"
    "v_mfma_f32_16x16x32_bf16 v[216:219], v[124:127], v[172:175], v[216:219]\n"
    "v_mfma_f32_16x16x32_bf16 v[220:223], v[148:151], v[172:175], v[220:223]\n"
    "s_waitcnt lgkmcnt(1)\n"
    "v_mfma_f32_16x16x32_bf16 v[200:203], v[8:11], v[176:179], 0\n"
    "v_mov_b32 v227, 3\n"
    "v_mfma_f32_16x16x32_bf16 v[204:207], v[32:35], v[176:179], 0\n"
    "v_mfma_f32_16x16x32_bf16 v[200:203], v[12:15], v[180:183], v[200:203]\n"
    "v_mfma_f32_16x16x32_bf16 v[204:207], v[36:39], v[180:183], v[204:207]\n"
    "v_mfma_f32_16x16x32_bf16 v[200:203], v[16:19], v[184:187], v[200:203]\n"
    "v_cvt_pk_bf16_f32 v216, v216, v217\n"
    "v_cvt_pk_bf16_f32 v217, v218, v219\n"
    "v_mfma_f32_16x16x32_bf16 v[204:207], v[40:43], v[184:187], v[204:207]\n"
    "v_cvt_pk_bf16_f32 v218, v220, v221\n"
    "v_cvt_pk_bf16_f32 v219, v222, v223\n"
    "v_mfma_f32_16x16x32_bf16 v[200:203], v[20:23], v[188:191], v[200:203]\n"
    "ds_write_b128 v7, v[216:219] offset:14336\n"
    "v_mfma_f32_16x16x32_bf16 v[204:207], v[44:47], v[188:191], v[204:207]\n"
    "ds_write_b32 v225, v227 offset:16\n"
    "ds_read_b32 v228, v224 offset:192\n"
    "v_mfma_f32_16x16x32_bf16 v[200:203], v[24:27], v[192:195], v[200:203]\n"
    "v_mfma_f32_16x16x32_bf16 v[204:207], v[48:51], v[192:195], v[204:207]\n"
    "v_mfma_f32_16x16x32_bf16 v[200:203], v[28:31], v[196:199], v[200:203]\n"
    "v_mfma_f32_16x16x32_bf16 v[204:207], v[52:55], v[196:199], v[204:207]\n"
    "v_mfma_f32_16x16x32_bf16 v[208:211], v[56:59], v[176:179], 0\n"
    "v_mfma_f32_16x16x32_bf16 v[212:215], v[80:83], v[176:179], 0\n"
    "ds_read_b32 v229, v224 offset:0\n"
    "v_mfma_f32_16x16x32_bf16 v[208:211], v[60:63], v[180:183], v[208:211]\n"
    "v_mfma_f32_16x16x32_bf16 v[212:215], v[84:87], v[180:183], v[212:215]\n"
    "s_waitcnt lgkmcnt(1)\n"
    "v_cmp_gt_u32 vcc, 1, v228\n"
    "v_mfma_f32_16x16x32_bf16 v[208:211], v[64:67], v[184:187], v[208:211]\n"
    "v_cvt_pk_bf16_f32 v200, v200, v201\n"
    "s_cbranch_vccnz .Lg6\n"
    ".Lg6b:\n"
    "v_mfma_f32_16x16x32_bf16 v[212:215], v[88:91], v[184:187], v[212:215]\n"
    "v_cvt_pk_bf16_f32 v201, v202, v203\n"
    "v_cvt_pk_bf16_f32 v202, v204, v205\n"
    "v_mfma_f32_16x16x32_bf16 v[208:211], v[68:71], v[188:191], v[208:211]\n"
    "v_cvt_pk_bf16_f32 v203, v206, v207\n"
    "v_mfma_f32_16x16x32_bf16 v[212:215], v[92:95], v[188:191], v[212:215]\n"
    "ds_write_b128 v7, v[200:203] offset:0\n"
    "v_mfma_f32_16x16x32_bf16 v[208:211], v[72:75], v[192:195], v[208:211]\n"
    "v_mfma_f32_16x16x32_bf16 v[212:215], v[96:99], v[192:195], v[212:215]\n"
    "s_waitcnt lgkmcnt(1)\n"
    "v_cmp_gt_u32 vcc, 3, v229\n"
    "v_mfma_f32_16x16x32_bf16 v[208:211], v[76:79], v[196:199], v[208:211]\n"
    "v_mfma_f32_16x16x32_bf16 v[212:215], v[100:103], v[196:199], v[212:215]\n"
    "s_cbranch_vccnz .Lg5\n"
    ".Lg5b:\n"
    "v_mfma_f32_16x16x32_bf16 v[216:219], v[104:107], v[176:179], 0\n"
    "ds_read_b128 v[152:155], v2 offset:62272\n"
    "ds_read_b128 v[156:159], v2 offset:62336\n"
    "v_mfma_f32_16x16x32_bf16 v[220:223], v[128:131], v[176:179], 0\n"
    "ds_read_b128 v[160:163], v2 offset:62400\n"
    "ds_read_b128 v[164:167], v2 offset:62464\n"
    "v_mfma_f32_16x16x32_bf16 v[216:219], v[108:111], v[180:183], v[216:219]\n"
    "ds_read_b128 v[168:171], v2 offset:62528\n"
    "ds_read_b128 v[172:175], v2 offset:62592\n"
    "v_mfma_f32_16x16x32_bf16 v[220:223], v[132:135], v[180:183], v[220:223]\n"
    "v_cvt_pk_bf16_f32 v208, v208, v209\n"
    "v_cvt_pk_bf16_f32 v209, v210, v211\n"
    "v_mfma_f32_16x16x32_bf16 v[216:219], v[112:115], v[184:187], v[216:219]\n"
    "v_cvt_pk_bf16_f32 v210, v212, v213\n"
    "v_cvt_pk_bf16_f32 v211, v214, v215\n"
    "v_mfma_f32_16x16x32_bf16 v[220:223], v[136:139], v[184:187], v[220:223]\n"
    "ds_write_b128 v7, v[208:211] offset:1024\n"
    "v_mfma_f32_16x16x32_bf16 v[216:219], v[116:119], v[188:191], v[216:219]\n"
    "v_mfma_f32_16x16x32_bf16 v[220:223], v[140:143], v[188:191], v[220:223]\n"
    "v_mfma_f32_16x16x32_bf16 v[216:219], v[120:123], v[192:195], v[216:219]\n"
    "v_mfma_f32_16x16x32_bf16 v[220:223], v[144:147], v[192:195], v[220:223]\n"
    "v_mfma_f32_16x16x32_bf16 v[216:219], v[124:127], v[196:199], v[216:219]\n"
    "v_mfma_f32_16x16x32_bf16 v[220:223], v[148:151], v[196:199], v[220:223]\n"
    "s_waitcnt lgkmcnt(1)\n"
    "v_mfma_f32_16x16x32_bf16 v[200:203], v[8:11], v[152:155], 0\n"
    "v_mov_b32 v227, 4\n"
    "v_mfma_f32_16x16x32_bf16 v[204:207], v[32:35], v[152:155], 0\n"
    "v_mfma_f32_16x16x32_bf16 v[200:203], v[12:15], v[156:159], v[200:203]\n"
    "v_mfma_f32_16x16x32_bf16 v[204:207], v[36:39], v[156:159], v[204:207]\n"
    "v_mfma_f32_16x16x32_bf16 v[200:203], v[16:19], v[160:163], v[200:203]\n"
    "v_cvt_pk_bf16_f32 v216, v216, v217\n"
    "v_cvt_pk_bf16_f32 v217, v218, v219\n"
    "v_mfma_f32_16x16x32_bf16 v[204:207], v[40:43], v[160:163], v[204:207]\n"
    "v_cvt_pk_bf16_f32 v218, v220, v221\n"
    "v_cvt_pk_bf16_f32 v219, v222, v223\n"
    "v_mfma_f32_16x16x32_bf16 v[200:203], v[20:23], v[164:167], v[200:203]\n"
    "ds_write_b128 v7, v[216:219] offset:2048\n"
    "v_mfma_f32_16x16x32_bf16 v[204:207], v[44:47], v[164:167], v[204:207]\n"
    "ds_write_b32 v225, v227 offset:16\n"
    "ds_read_b32 v228, v224 offset:208\n"
    "v_mfma_f32_16x16x32_bf16 v[200:203], v[24:27], v[168:171], v[200:203]\n"
    "ds_read_b32 v239, v224 offset:64\n"
    "v_mfma_f32_16x16x32_bf16 v[204:207], v[48:51], v[168:171], v[204:207]\n"
    "v_mfma_f32_16x16x32_bf16 v[200:203], v[28:31], v[172:175], v[200:203]\n"
    "v_mfma_f32_16x16x32_bf16 v[204:207], v[52:55], v[172:175], v[204:207]\n"
    "v_mfma_f32_16x16x32_bf16 v[208:211], v[56:59], v[152:155], 0\n"
    "v_mfma_f32_16x16x32_bf16 v[212:215], v[80:83], v[152:155], 0\n"
    "v_mfma_f32_16x16x32_bf16 v[208:211], v[60:63], v[156:159], v[208:211]\n"
    "v_mfma_f32_16x16x32_bf16 v[212:215], v[84:87], v[156:159], v[212:215]\n"
    "s_waitcnt lgkmcnt(1)\n"
    "v_cmp_gt_u32 vcc, 1, v228\n"
    "v_mfma_f32_16x16x32_bf16 v[208:211], v[64:67], v[160:163], v[208:211]\n"
    "v_cvt_pk_bf16_f32 v200, v200, v201\n"
    "s_cbranch_vccnz .Lg7\n"
    ".Lg7b:\n"
    "v_mfma_f32_16x16x32_bf16 v[212:215], v[88:91], v[160:163], v[212:215]\n"
    "v_cvt_pk_bf16_f32 v201, v202, v203\n"
    "v_cvt_pk_bf16_f32 v202, v204, v205\n"
    "v_mfma_f32_16x16x32_bf16 v[208:211], v[68:71], v[164:167], v[208:211]\n"
    "v_cvt_pk_bf16_f32 v203, v206, v207\n"
    "v_mfma_f32_16x16x32_bf16 v[212:215], v[92:95], v[164:167], v[212:215]\n"
    "ds_write_b128 v7, v[200:203] offset:6144\n"
    "v_mfma_f32_16x16x32_bf16 v[208:211], v[72:75], v[168:171], v[208:211]\n"
    "v_mfma_f32_16x16x32_bf16 v[212:215], v[96:99], v[168:171], v[212:215]\n"
    "v_mfma_f32_16x16x32_bf16 v[208:211], v[76:79], v[172:175], v[208:211]\n"
    "v_mfma_f32_16x16x32_bf16 v[212:215], v[100:103], v[172:175], v[212:215]\n"
    "v_mfma_f32_16x16x32_bf16 v[216:219], v[104:107], v[152:155], 0\n"
    "ds_read_b128 v[176:179], v3 offset:62272\n"
    "ds_read_b128 v[180:183], v3 offset:62336\n"
    "v_mfma_f32_16x16x32_bf16 v[220:223], v[128:131], v[152:155], 0\n"
    "ds_read_b128 v[184:187], v3 offset:62400\n"
    "ds_read_b128 v[188:191], v3 offset:62464\n"
    "v_mfma_f32_16x16x32_bf16 v[216:219], v[108:111], v[156:159], v[216:219]\n"
    "ds_read_b128 v[192:195], v3 offset:62528\n"
    "ds_read_b128 v[196:199], v3 offset:62592\n"
    "v_mfma_f32_16x16x32_bf16 v[220:223], v[132:135], v[156:159], v[220:223]\n"
    "v_cvt_pk_bf16_f32 v208, v208, v209\n"
    "v_cvt_pk_bf16_f32 v209, v210, v211\n"
    "v_mfma_f32_16x16x32_bf16 v[216:219], v[112:115], v[160:163], v[216:219]\n"
    "v_cvt_pk_bf16_f32 v210, v212, v213\n"
    "v_cvt_pk_bf16_f32 v211, v214, v215\n"
    "v_mfma_f32_16x16x32_bf16 v[220:223], v[136:139], v[160:163], v[220:223]\n"
    "ds_write_b128 v7, v[208:211] offset:7168\n"
    "v_mfma_f32_16x16x32_bf16 v[216:219], v[116:119], v[164:167], v[216:219]\n"
    "s_waitcnt lgkmcnt(8)\n"
    "v_cmp_gt_u32 vcc, 1, v239\n"
    "v_mfma_f32_16x16x32_bf16 v[220:223], v[140:143], v[164:167], v[220:223]\n"
    "v_mfma_f32_16x16x32_bf16 v[216:219], v[120:123], v[168:171], v[216:219]\n"
    "s_cbranch_vccnz .Lg8\n"
    ".Lg8b:\n"
    "v_mfma_f32_16x16x32_bf16 v[220:223], v[144:147], v[168:171], v[220:223]\n"
    "v_mfma_f32_16x16x32_bf16 v[216:219], v[124:127], v[172:175], v[216:219]\n"
    "v_mfma_f32_16x16x32_bf16 v[220:223], v[148:151], v[172:175], v[220:223]\n"
    "s_waitcnt lgkmcnt(1)\n"
    "v_mfma_f32_16x16x32_bf16 v[200:203], v[8:11], v[176:179], 0\n"
    "v_mov_b32 v227, 5\n"
    "v_mfma_f32_16x16x32_bf16 v[204:207], v[32:35], v[176:179], 0\n"
    "v_mfma_f32_16x16x32_bf16 v[200:203], v[12:15], v[180:183], v[200:203]\n"
    "v_mfma_f32_16x16x32_bf16 v[204:207], v[36:39], v[180:183], v[204:207]\n"
    "v_mfma_f32_16x16x32_bf16 v[200:203], v[16:19], v[184:187], v[200:203]\n"
    "v_cvt_pk_bf16_f32 v216, v216, v217\n"
    "v_cvt_pk_bf16_f32 v217, v218, v219\n"
    "v_mfma_f32_16x16x32_bf16 v[204:207], v[40:43], v[184:187], v[204:207]\n"
    "v_cvt_pk_bf16_f32 v218, v220, v221\n"
    "v_cvt_pk_bf16_f32 v219, v222, v223\n"
    "v_mfma_f32_16x16x32_bf16 v[200:203], v[20:23], v[188:191], v[200:203]\n"
    "ds_write_b128 v7, v[216:219] offset:8192\n"
    "v_mfma_f32_16x16x32_bf16 v[204:207], v[44:47], v[188:191], v[204:207]\n"
    "ds_write_b32 v225, v227 offset:16\n"
    "ds_read_b32 v228, v224 offset:224\n"
    "v_mfma_f32_16x16x32_bf16 v[200:203], v[24:27], v[192:195], v[200:203]\n"
    "ds_read_b32 v239, v224 offset:80\n"
    "v_mfma_f32_16x16x32_bf16 v[204:207], v[48:51], v[192:195], v[204:207]\n"
    "v_mfma_f32_16x16x32_bf16 v[200:203], v[28:31], v[196:199], v[200:203]\n"
    "v_mfma_f32_16x16x32_bf16 v[204:207], v[52:55], v[196:199], v[204:207]\n"
    "v_mfma_f32_16x16x32_bf16 v[208:211], v[56:59], v[176:179], 0\n"
    "v_mfma_f32_16x16x32_bf16 v[212:215], v[80:83], v[176:179], 0\n"
    "ds_read_b32 v229, v224 offset:0\n"
    "v_mfma_f32_16x16x32_bf16 v[208:211], v[60:63], v[180:183], v[208:211]\n"
    "v_mfma_f32_16x16x32_bf16 v[212:215], v[84:87], v[180:183], v[212:215]\n"
    "s_waitcnt lgkmcnt(2)\n"
    "v_cmp_gt_u32 vcc, 1, v228\n"
    "v_mfma_f32_16x16x32_bf16 v[208:211], v[64:67], v[184:187], v[208:211]\n"
    "v_cvt_pk_bf16_f32 v200, v200, v201\n"
    "s_cbranch_vccnz .Lg10\n"
    ".Lg10b:\n"
    "v_mfma_f32_16x16x32_bf16 v[212:215], v[88:91], v[184:187], v[212:215]\n"
    "v_cvt_pk_bf16_f32 v201, v202, v203\n"
    "v_cvt_pk_bf16_f32 v202, v204, v205\n"
    "v_mfma_f32_16x16x32_bf16 v[208:211], v[68:71], v[188:191], v[208:211]\n"
    "v_cvt_pk_bf16_f32 v203, v206, v207\n"
    "v_mfma_f32_16x16x32_bf16 v[212:215], v[92:95], v[188:191], v[212:215]\n"
    "ds_write_b128 v7, v[200:203] offset:12288\n"
    "v_mfma_f32_16x16x32_bf16 v[208:211], v[72:75], v[192:195], v[208:211]\n"
    "v_mfma_f32_16x16x32_bf16 v[212:215], v[96:99], v[192:195], v[212:215]\n"
    "s_waitcnt lgkmcnt(1)\n"
    "v_cmp_gt_u32 vcc, 6, v229\n"
    "v_mfma_f32_16x16x32_bf16 v[208:211], v[76:79], v[196:199], v[208:211]\n"
    "v_mfma_f32_16x16x32_bf16 v[212:215], v[100:103], v[196:199], v[212:215]\n"
    "s_cbranch_vccnz .Lg9\n"
    ".Lg9b:\n"
    "v_mfma_f32_16x16x32_bf16 v[216:219], v[104:107], v[176:179], 0\n"
    "ds_read_b128 v[152:155], v4 offset:8896\n"
    "ds_read_b128 v[156:159], v4 offset:8960\n"
    "v_mfma_f32_16x16x32_bf16 v[220:223], v[128:131], v[176:179], 0\n"
    "ds_read_b128 v[160:163], v4 offset:9024\n"
    "ds_read_b128 v[164:167], v4 offset:9088\n"
    "v_mfma_f32_16x16x32_bf16 v[216:219], v[108:111], v[180:183], v[216:219]\n"
    "ds_read_b128 v[168:171], v4 offset:9152\n"
    "ds_read_b128 v[172:175], v4 offset:9216\n"
    "v_mfma_f32_16x16x32_bf16 v[220:223], v[132:135], v[180:183], v[220:223]\n"
    "v_cvt_pk_bf16_f32 v208, v208, v209\n"
    "v_cvt_pk_bf16_f32 v209, v210, v211\n"
    "v_mfma_f32_16x16x32_bf16 v[216:219], v[112:115], v[184:187], v[216:219]\n"
    "v_cvt_pk_bf16_f32 v210, v212, v213\n"
    "v_cvt_pk_bf16_f32 v211, v214, v215\n"
    "v_mfma_f32_16x16x32_bf16 v[220:223], v[136:139], v[184:187], v[220:223]\n"
    "ds_write_b128 v7, v[208:211] offset:13312\n"
    "v_mfma_f32_16x16x32_bf16 v[216:219], v[116:119], v[188:191], v[216:219]\n"
    "v_cmp_gt_u32 vcc, 1, v239\n"
    "v_mfma_f32_16x16x32_bf16 v[220:223], v[140:143], v[188:191], v[220:223]\n"
    "s_cbranch_vccnz .Lg11\n"
    ".Lg11b:\n"
    "v_mfma_f32_16x16x32_bf16 v[216:219], v[120:123], v[192:195], v[216:219]\n"
    "s_add_u32 s60, s42, s44\n"
    "s_addc_u32 s61, s43, 0\n"
    "v_mfma_f32_16x16x32_bf16 v[220:223], v[144:147], v[192:195], v[220:223]\n"
    "s_add_u32 m0, s54, 0\n"
    "v_mfma_f32_16x16x32_bf16 v[216:219], v[124:127], v[196:199], v[216:219]\n"
    "global_load_lds_dwordx4 v230, s[60:61]\n"
    "s_add_u32 m0, s54, 2048\n"
    "v_mfma_f32_16x16x32_bf16 v[220:223], v[148:151], v[196:199], v[220:223]\n"
    "global_load_lds_dwordx4 v231, s[60:61]\n"
    "s_add_u32 m0, s54, 4096\n"
    "s_waitcnt lgkmcnt(1)\n"
    "v_mfma_f32_16x16x32_bf16 v[200:203], v[8:11], v[152:155], 0\n"
    "v_mov_b32 v227, 6\n"
    "v_cvt_pk_bf16_f32 v216, v216, v217\n"
    "v_mfma_f32_16x16x32_bf16 v[204:207], v[32:35], v[152:155], 0\n"
    "v_cvt_pk_bf16_f32 v217, v218, v219\n"
    "v_cvt_pk_bf16_f32 v218, v220, v221\n"
    "v_mfma_f32_16x16x32_bf16 v[200:203], v[12:15], v[156:159], v[200:203]\n"
    "v_cvt_pk_bf16_f32 v219, v222, v223\n"
    "v_mfma_f32_16x16x32_bf16 v[204:207], v[36:39], v[156:159], v[204:207]\n"
    "ds_write_b128 v7, v[216:219] offset:14336\n"
    "v_mfma_f32_16x16x32_bf16 v[200:203], v[16:19], v[160:163], v[200:203]\n"
    "ds_write_b32 v225, v227 offset:16\n"
    "ds_read_b32 v228, v224 offset:240\n"
    "v_mfma_f32_16x16x32_bf16 v[204:207], v[40:43], v[160:163], v[204:207]\n"
    "ds_read_b32 v239, v224 offset:96\n"
    "global_load_lds_dwordx4 v232, s[60:61]\n"
    "v_mfma_f32_16x16x32_bf16 v[200:203], v[20:23], v[164:167], v[200:203]\n"
    "s_add_u32 m0, s55, 0\n"
    "v_mfma_f32_16x16x32_bf16 v[204:207], v[44:47], v[164:167], v[204:207]\n"
    "global_load_lds_dwordx4 v233, s[60:61]\n"
    "s_add_u32 m0, s56, 0\n"
    "v_mfma_f32_16x16x32_bf16 v[200:203], v[24:27], v[168:171], v[200:203]\n"
    "global_load_lds_dwordx4 v234, s[60:61]\n"
    "v_mfma_f32_16x16x32_bf16 v[204:207], v[48:51], v[168:171], v[204:207]\n"
    "s_add_u32 s60, s42, s45\n"
    "s_addc_u32 s61, s43, 0\n"
    "v_mfma_f32_16x16x32_bf16 v[200:203], v[28:31], v[172:175], v[200:203]\n"
    "s_add_u32 m0, s54, 8896\n"
    "v_mfma_f32_16x16x32_bf16 v[204:207], v[52:55], v[172:175], v[204:207]\n"
    "global_load_lds_dwordx4 v230, s[60:61]\n"
    "s_add_u32 m0, s54, 10944\n"
    "v_mfma_f32_16x16x32_bf16 v[208:211], v[56:59], v[152:155], 0\n"
    "s_waitcnt lgkmcnt(1)\n"
    "v_cmp_gt_u32 vcc, 1, v228\n"
    "v_mfma_f32_16x16x32_bf16 v[212:215], v[80:83], v[152:155], 0\n"
    "v_cvt_pk_bf16_f32 v200, v200, v201\n"
    "s_cbranch_vccnz .Lg12\n"
    ".Lg12b:\n"
    "v_mfma_f32_16x16x32_bf16 v[208:211], v[60:63], v[156:159], v[208:211]\n"
    "v_cvt_pk_bf16_f32 v201, v202, v203\n"
    "v_cvt_pk_bf16_f32 v202, v204, v205\n"
    "v_mfma_f32_16x16x32_bf16 v[212:215], v[84:87], v[156:159], v[212:215]\n"
    "v_cvt_pk_bf16_f32 v203, v206, v207\n"
    "v_mfma_f32_16x16x32_bf16 v[208:211], v[64:67], v[160:163], v[208:211]\n"
    "ds_write_b128 v7, v[200:203] offset:0\n"
    "v_mfma_f32_16x16x32_bf16 v[212:215], v[88:91], v[160:163], v[212:215]\n"
    "global_load_lds_dwordx4 v231, s[60:61]\n"
    "s_add_u32 m0, s54, 12992\n"
    "v_mfma_f32_16x16x32_bf16 v[208:211], v[68:71], v[164:167], v[208:211]\n"
    "global_load_lds_dwordx4 v232, s[60:61]\n"
    "s_add_u32 m0, s55, 8896\n"
    "v_mfma_f32_16x16x32_bf16 v[212:215], v[92:95], v[164:167], v[212:215]\n"
    "global_load_lds_dwordx4 v233, s[60:61]\n"
    "s_add_u32 m0, s56, 8896\n"
    "v_mfma_f32_16x16x32_bf16 v[208:211], v[72:75], v[168:171], v[208:211]\n"
    "global_load_lds_dwordx4 v234, s[60:61]\n"
    "v_mfma_f32_16x16x32_bf16 v[212:215], v[96:99], v[168:171], v[212:215]\n"
    "v_mfma_f32_16x16x32_bf16 v[208:211], v[76:79], v[172:175], v[208:211]\n"
    "v_mfma_f32_16x16x32_bf16 v[212:215], v[100:103], v[172:175], v[212:215]\n"
    "v_mfma_f32_16x16x32_bf16 v[216:219], v[104:107], v[152:155], 0\n"
    "ds_read_b128 v[176:179], v5 offset:8896\n"
    "ds_read_b128 v[180:183], v5 offset:8960\n"
    "v_mfma_f32_16x16x32_bf16 v[220:223], v[128:131], v[152:155], 0\n"
    "ds_read_b128 v[184:187], v5 offset:9024\n"
    "ds_read_b128 v[188:191], v5 offset:9088\n"
    "v_mfma_f32_16x16x32_bf16 v[216:219], v[108:111], v[156:159], v[216:219]\n"
    "ds_read_b128 v[192:195], v5 offset:9152\n"
    "ds_read_b128 v[196:199], v5 offset:9216\n"
    "v_mfma_f32_16x16x32_bf16 v[220:223], v[132:135], v[156:159], v[220:223]\n"
    "v_cvt_pk_bf16_f32 v208, v208, v209\n"
    "v_cvt_pk_bf16_f32 v209, v210, v211\n"
    "v_mfma_f32_16x16x32_bf16 v[216:219], v[112:115], v[160:163], v[216:219]\n"
    "v_cvt_pk_bf16_f32 v210, v212, v213\n"
    "v_cvt_pk_bf16_f32 v211, v214, v215\n"
    "v_mfma_f32_16x16x32_bf16 v[220:223], v[136:139], v[160:163], v[220:223]\n"
    "ds_write_b128 v7, v[208:211] offset:1024\n"
    "v_mfma_f32_16x16x32_bf16 v[216:219], v[116:119], v[164:167], v[216:219]\n"
    "s_waitcnt lgkmcnt(8)\n"
    "v_cmp_gt_u32 vcc, 1, v239\n"
    "v_mfma_f32_16x16x32_bf16 v[220:223], v[140:143], v[164:167], v[220:223]\n"
    "v_mfma_f32_16x16x32_bf16 v[216:219], v[120:123], v[168:171], v[216:219]\n"
    "s_cbranch_vccnz .Lg13\n"
    ".Lg13b:\n"
    "v_mfma_f32_16x16x32_bf16 v[220:223], v[144:147], v[168:171], v[220:223]\n"
    "v_mfma_f32_16x16x32_bf16 v[216:219], v[124:127], v[172:175], v[216:219]\n"
    "v_mfma_f32_16x16x32_bf16 v[220:223], v[148:151], v[172:175], v[220:223]\n"
    "s_waitcnt lgkmcnt(1)\n"
    "v_mfma_f32_16x16x32_bf16 v[200:203], v[8:11], v[176:179], 0\n"
    "v_mov_b32 v227, 7\n"
    "v_mfma_f32_16x16x32_bf16 v[204:207], v[32:35], v[176:179], 0\n"
    "v_mfma_f32_16x16x32_bf16 v[200:203], v[12:15], v[180:183], v[200:203]\n"
    "v_mfma_f32_16x16x32_bf16 v[204:207], v[36:39], v[180:183], v[204:207]\n"
    "v_mfma_f32_16x16x32_bf16 v[200:203], v[16:19], v[184:187], v[200:203]\n"
    "v_cvt_pk_bf16_f32 v216, v216, v217\n"
    "v_cvt_pk_bf16_f32 v217, v218, v219\n"
    "v_mfma_f32_16x16x32_bf16 v[204:207], v[40:43], v[184:187], v[204:207]\n"
    "v_cvt_pk_bf16_f32 v218, v220, v221\n"
    "v_cvt_pk_bf16_f32 v219, v222, v223\n"
    "v_mfma_f32_16x16x32_bf16 v[200:203], v[20:23], v[188:191], v[200:203]\n"
    "ds_write_b128 v7, v[216:219] offset:2048\n"
    "v_mfma_f32_16x16x32_bf16 v[204:207], v[44:47], v[188:191], v[204:207]\n"
    "ds_write_b32 v225, v227 offset:16\n"
    "ds_read_b32 v228, v224 offset:192\n"
    "v_mfma_f32_16x16x32_bf16 v[200:203], v[24:27], v[192:195], v[200:203]\n"
    "ds_read_b32 v239, v224 offset:112\n"
    "v_mfma_f32_16x16x32_bf16 v[204:207], v[48:51], v[192:195], v[204:207]\n"
    "v_mfma_f32_16x16x32_bf16 v[200:203], v[28:31], v[196:199], v[200:203]\n"
    "v_mfma_f32_16x16x32_bf16 v[204:207], v[52:55], v[196:199], v[204:207]\n"
    "v_mfma_f32_16x16x32_bf16 v[208:211], v[56:59], v[176:179], 0\n"
    "v_mfma_f32_16x16x32_bf16 v[212:215], v[80:83], v[176:179], 0\n"
    "ds_read_b32 v229, v224 offset:0\n"
    "v_mfma_f32_16x16x32_bf16 v[208:211], v[60:63], v[180:183], v[208:211]\n"
    "v_mfma_f32_16x16x32_bf16 v[212:215], v[84:87], v[180:183], v[212:215]\n"
    "s_waitcnt lgkmcnt(2)\n"
    "v_cmp_gt_u32 vcc, 2, v228\n"
    "v_mfma_f32_16x16x32_bf16 v[208:211], v[64:67], v[184:187], v[208:211]\n"
    "v_cvt_pk_bf16_f32 v200, v200, v201\n"
    "s_cbranch_vccnz .Lg15\n"
    ".Lg15b:\n"
    "v_mfma_f32_16x16x32_bf16 v[212:215], v[88:91], v[184:187], v[212:215]\n"
    "v_cvt_pk_bf16_f32 v201, v202, v203\n"
    "v_cvt_pk_bf16_f32 v202, v204, v205\n"
    "v_mfma_f32_16x16x32_bf16 v[208:211], v[68:71], v[188:191], v[208:211]\n"
    "v_cvt_pk_bf16_f32 v203, v206, v207\n"
    "v_mfma_f32_16x16x32_bf16 v[212:215], v[92:95], v[188:191], v[212:215]\n"
    "ds_write_b128 v7, v[200:203] offset:6144\n"
    "v_mfma_f32_16x16x32_bf16 v[208:211], v[72:75], v[192:195], v[208:211]\n"
    "v_mfma_f32_16x16x32_bf16 v[212:215], v[96:99], v[192:195], v[212:215]\n"
    "s_waitcnt lgkmcnt(1)\n"
    "v_cmp_gt_u32 vcc, 7, v229\n"
    "v_mfma_f32_16x16x32_bf16 v[208:211], v[76:79], v[196:199], v[208:211]\n"
    "v_mfma_f32_16x16x32_bf16 v[212:215], v[100:103], v[196:199], v[212:215]\n"
    "s_cbranch_vccnz .Lg14\n"
    ".Lg14b:\n"
    "v_mfma_f32_16x16x32_bf16 v[216:219], v[104:107], v[176:179], 0\n"
    "ds_read_b128 v[152:155], v4 offset:26688\n"
    "ds_read_b128 v[156:159], v4 offset:26752\n"
    "v_mfma_f32_16x16x32_bf16 v[220:223], v[128:131], v[176:179], 0\n"
    "ds_read_b128 v[160:163], v4 offset:26816\n"
    "ds_read_b128 v[164:167], v4 offset:26880\n"
    "v_mfma_f32_16x16x32_bf16 v[216:219], v[108:111], v[180:183], v[216:219]\n"
    "ds_read_b128 v[168:171], v4 offset:26944\n"
    "ds_read_b128 v[172:175], v4 offset:27008\n"
    "v_mfma_f32_16x16x32_bf16 v[220:223], v[132:135], v[180:183], v[220:223]\n"
    "v_cvt_pk_bf16_f32 v208, v208, v209\n"
    "v_cvt_pk_bf16_f32 v209, v210, v211\n"
    "v_mfma_f32_16x16x32_bf16 v[216:219], v[112:115], v[184:187], v[216:219]\n"
    "v_cvt_pk_bf16_f32 v210, v212, v213\n"
    "v_cvt_pk_bf16_f32 v211, v214, v215\n"
    "v_mfma_f32_16x16x32_bf16 v[220:223], v[136:139], v[184:187], v[220:223]\n"
    "ds_write_b128 v7, v[208:211] offset:7168\n"
    "v_mfma_f32_16x16x32_bf16 v[216:219], v[116:119], v[188:191], v[216:219]\n"
    "v_cmp_gt_u32 vcc, 1, v239\n"
    "v_mfma_f32_16x16x32_bf16 v[220:223], v[140:143], v[188:191], v[220:223]\n"
    "s_cbranch_vccnz .Lg16\n"
    ".Lg16b:\n"
    "v_mfma_f32_16x16x32_bf16 v[216:219], v[120:123], v[192:195], v[216:219]\n"
    "s_add_u32 s60, s42, s46\n"
    "s_addc_u32 s61, s43, 0\n"
    "v_mfma_f32_16x16x32_bf16 v[220:223], v[144:147], v[192:195], v[220:223]\n"
    "s_add_u32 m0, s54, 17792\n"
    "v_mfma_f32_16x16x32_bf16 v[216:219], v[124:127], v[196:199], v[216:219]\n"
    "global_load_lds_dwordx4 v230, s[60:61]\n"
    "s_add_u32 m0, s54, 19840\n"
    "v_mfma_f32_16x16x32_bf16 v[220:223], v[148:151], v[196:199], v[220:223]\n"
    "global_load_lds_dwordx4 v231, s[60:61]\n"
    "s_add_u32 m0, s54, 21888\n"
    "s_waitcnt lgkmcnt(1)\n"
    "global_load_lds_dwordx4 v232, s[60:61]\n"
    "s_add_u32 m0, s55, 17792\n"
    "s_nop 0\n"
    "global_load_lds_dwordx4 v233, s[60:61]\n"
    "s_add_u32 m0, s56, 17792\n"
    "s_nop 0\n"
    "global_load_lds_dwordx4 v234, s[60:61]\n"
    "s_add_u32 s60, s42, s47\n"
    "s_addc_u32 s61, s43, 0\n"
    "s_add_u32 m0, s54, 26688\n"
    "s_nop 0\n"
    "global_load_lds_dwordx4 v230, s[60:61]\n"
    "s_add_u32 m0, s54, 28736\n"
    "s_nop 0\n"
    "global_load_lds_dwordx4 v231, s[60:61]\n"
    "s_add_u32 m0, s54, 30784\n"
    "s_nop 0\n"
    "global_load_lds_dwordx4 v232, s[60:61]\n"
    "s_add_u32 m0, s55, 26688\n"
    "s_nop 0\n"
    "global_load_lds_dwordx4 v233, s[60:61]\n"
    "s_add_u32 m0, s56, 26688\n"
    "s_nop 0\n"
    "global_load_lds_dwordx4 v234, s[60:61]\n"
    "v_mfma_f32_16x16x32_bf16 v[200:203], v[8:11], v[152:155], 0\n"
    "v_cvt_pk_bf16_f32 v216, v216, v217\n"
    "v_cvt_pk_bf16_f32 v217, v218, v219\n"
    "v_mfma_f32_16x16x32_bf16 v[204:207], v[32:35], v[152:155], 0\n"
    "v_cvt_pk_bf16_f32 v218, v220, v221\n"
    "v_cvt_pk_bf16_f32 v219, v222, v223\n"
    "v_mfma_f32_16x16x32_bf16 v[200:203], v[12:15], v[156:159], v[200:203]\n"
    "ds_write_b128 v7, v[216:219] offset:8192\n"
    "v_mfma_f32_16x16x32_bf16 v[204:207], v[36:39], v[156:159], v[204:207]\n"
    "v_mov_b32 v227, 8\n"
    "v_mfma_f32_16x16x32_bf16 v[200:203], v[16:19], v[160:163], v[200:203]\n"
    "ds_write_b32 v225, v227 offset:16\n"
    "ds_read_b32 v228, v224 offset:208\n"
    "v_mfma_f32_16x16x32_bf16 v[204:207], v[40:43], v[160:163], v[204:207]\n"
    "ds_read_b32 v239, v224 offset:64\n"
    "s_waitcnt vmcnt(10)\n"
    "v_mfma_f32_16x16x32_bf16 v[200:203], v[20:23], v[164:167], v[200:203]\n"
    "v_mov_b32 v227, 8\n"
    "v_mfma_f32_16x16x32_bf16 v[204:207], v[44:47], v[164:167], v[204:207]\n"
    "ds_write_b32 v226, v227 offset:0\n"
    "v_mfma_f32_16x16x32_bf16 v[200:203], v[24:27], v[168:171], v[200:203]\n"
    "v_mfma_f32_16x16x32_bf16 v[204:207], v[48:51], v[168:171], v[204:207]\n"
    "v_mfma_f32_16x16x32_bf16 v[200:203], v[28:31], v[172:175], v[200:203]\n"
    "v_mfma_f32_16x16x32_bf16 v[204:207], v[52:55], v[172:175], v[204:207]\n"
    "v_mfma_f32_16x16x32_bf16 v[208:211], v[56:59], v[152:155], 0\n"
    "s_waitcnt lgkmcnt(2)\n"
    "v_cmp_gt_u32 vcc, 2, v228\n"
    "v_mfma_f32_16x16x32_bf16 v[212:215], v[80:83], v[152:155], 0\n"
    "v_mfma_f32_16x16x32_bf16 v[208:211], v[60:63], v[156:159], v[208:211]\n"
    "s_cbranch_vccnz .Lg17\n"
    ".Lg17b:\n"
    "v_mfma_f32_16x16x32_bf16 v[212:215], v[84:87], v[156:159], v[212:215]\n"
    "v_cvt_pk_bf16_f32 v200, v200, v201\n"
    "v_cvt_pk_bf16_f32 v201, v202, v203\n"
    "v_mfma_f32_16x16x32_bf16 v[208:211], v[64:67], v[160:163], v[208:211]\n"
    "v_cvt_pk_bf16_f32 v202, v204, v205\n"
    "v_cvt_pk_bf16_f32 v203, v206, v207\n"
    "v_mfma_f32_16x16x32_bf16 v[212:215], v[88:91], v[160:163], v[212:215]\n"
    "ds_write_b128 v7, v[200:203] offset:12288\n"
    "v_mfma_f32_16x16x32_bf16 v[208:211], v[68:71], v[164:167], v[208:211]\n"
    "v_mfma_f32_16x16x32_bf16 v[212:215], v[92:95], v[164:167], v[212:215]\n"
    "v_mfma_f32_16x16x32_bf16 v[208:211], v[72:75], v[168:171], v[208:211]\n"
    "v_mfma_f32_16x16x32_bf16 v[212:215], v[96:99], v[168:171], v[212:215]\n"
    "v_mfma_f32_16x16x32_bf16 v[208:211], v[76:79], v[172:175], v[208:211]\n"
    "v_mfma_f32_16x16x32_bf16 v[212:215], v[100:103], v[172:175], v[212:215]\n"
    "v_mfma_f32_16x16x32_bf16 v[216:219], v[104:107], v[152:155], 0\n"
    "ds_read_b128 v[176:179], v5 offset:26688\n"
    "ds_read_b128 v[180:183], v5 offset:26752\n"
    "v_mfma_f32_16x16x32_bf16 v[220:223], v[128:131], v[152:155], 0\n"
    "ds_read_b128 v[184:187], v5 offset:26816\n"
    "ds_read_b128 v[188:191], v5 offset:26880\n"
    "v_mfma_f32_16x16x32_bf16 v[216:219], v[108:111], v[156:159], v[216:219]\n"
    "ds_read_b128 v[192:195], v5 offset:26944\n"
    "ds_read_b128 v[196:199], v5 offset:27008\n"
    "v_mfma_f32_16x16x32_bf16 v[220:223], v[132:135], v[156:159], v[220:223]\n"
    "v_cvt_pk_bf16_f32 v208, v208, v209\n"
    "v_cvt_pk_bf16_f32 v209, v210, v211\n"
    "v_mfma_f32_16x16x32_bf16 v[216:219], v[112:115], v[160:163], v[216:219]\n"
    "v_cvt_pk_bf16_f32 v210, v212, v213\n"
    "v_cvt_pk_bf16_f32 v211, v214, v215\n"
    "v_mfma_f32_16x16x32_bf16 v[220:223], v[136:139], v[160:163], v[220:223]\n"
    "ds_write_b128 v7, v[208:211] offset:13312\n"
    "v_mfma_f32_16x16x32_bf16 v[216:219], v[116:119], v[164:167], v[216:219]\n"
    "s_waitcnt lgkmcnt(9)\n"
    "v_cmp_gt_u32 vcc, 2, v239\n"
    "v_mfma_f32_16x16x32_bf16 v[220:223], v[140:143], v[164:167], v[220:223]\n"
    "v_mfma_f32_16x16x32_bf16 v[216:219], v[120:123], v[168:171], v[216:219]\n"
    "s_cbranch_vccnz .Lg18\n"
    ".Lg18b:\n"
    "v_mfma_f32_16x16x32_bf16 v[220:223], v[144:147], v[168:171], v[220:223]\n"
    "v_mfma_f32_16x16x32_bf16 v[216:219], v[124:127], v[172:175], v[216:219]\n"
    "v_mfma_f32_16x16x32_bf16 v[220:223], v[148:151], v[172:175], v[220:223]\n"
    "s_waitcnt lgkmcnt(1)\n"
    "v_mfma_f32_16x16x32_bf16 v[200:203], v[8:11], v[176:179], 0\n"
    "v_mov_b32 v227, 9\n"
    "v_mfma_f32_16x16x32_bf16 v[204:207], v[32:35], v[176:179], 0\n"
    "v_mfma_f32_16x16x32_bf16 v[200:203], v[12:15], v[180:183], v[200:203]\n"
    "v_mfma_f32_16x16x32_bf16 v[204:207], v[36:39], v[180:183], v[204:207]\n"
    "v_mfma_f32_16x16x32_bf16 v[200:203], v[16:19], v[184:187], v[200:203]\n"
    "v_cvt_pk_bf16_f32 v216, v216, v217\n"
    "v_cvt_pk_bf16_f32 v217, v218, v219\n"
    "v_mfma_f32_16x16x32_bf16 v[204:207], v[40:43], v[184:187], v[204:207]\n"
    "v_cvt_pk_bf16_f32 v218, v220, v221\n"
    "v_cvt_pk_bf16_f32 v219, v222, v223\n"
    "v_mfma_f32_16x16x32_bf16 v[200:203], v[20:23], v[188:191], v[200:203]\n"
    "ds_write_b128 v7, v[216:219] offset:14336\n"
    "v_mfma_f32_16x16x32_bf16 v[204:207], v[44:47], v[188:191], v[204:207]\n"
    "ds_write_b32 v225, v227 offset:16\n"
    "ds_read_b32 v228, v224 offset:224\n"
    "v_mfma_f32_16x16x32_bf16 v[200:203], v[24:27], v[192:195], v[200:203]\n"
    "ds_read_b32 v239, v224 offset:80\n"
    "v_mfma_f32_16x16x32_bf16 v[204:207], v[48:51], v[192:195], v[204:207]\n"
    "v_mfma_f32_16x16x32_bf16 v[200:203], v[28:31], v[196:199], v[200:203]\n"
    "v_mfma_f32_16x16x32_bf16 v[204:207], v[52:55], v[196:199], v[204:207]\n"
    "v_mfma_f32_16x16x32_bf16 v[208:211], v[56:59], v[176:179], 0\n"
    "v_mfma_f32_16x16x32_bf16 v[212:215], v[80:83], v[176:179], 0\n"
    "ds_read_b32 v229, v224 offset:0\n"
    "v_mfma_f32_16x16x32_bf16 v[208:211], v[60:63], v[180:183], v[208:211]\n"
    "v_mfma_f32_16x16x32_bf16 v[212:215], v[84:87], v[180:183], v[212:215]\n"
    "s_waitcnt lgkmcnt(2)\n"
    "v_cmp_gt_u32 vcc, 2, v228\n"
    "v_mfma_f32_16x16x32_bf16 v[208:211], v[64:67], v[184:187], v[208:211]\n"
    "v_cvt_pk_bf16_f32 v200, v200, v201\n"
    "s_cbranch_vccnz .Lg20\n"
    ".Lg20b:\n"
    "v_mfma_f32_16x16x32_bf16 v[212:215], v[88:91], v[184:187], v[212:215]\n"
    "v_cvt_pk_bf16_f32 v201, v202, v203\n"
    "v_cvt_pk_bf16_f32 v202, v204, v205\n"
    "v_mfma_f32_16x16x32_bf16 v[208:211], v[68:71], v[188:191], v[208:211]\n"
    "v_cvt_pk_bf16_f32 v203, v206, v207\n"
    "v_mfma_f32_16x16x32_bf16 v[212:215], v[92:95], v[188:191], v[212:215]\n"
    "ds_write_b128 v7, v[200:203] offset:0\n"
    "v_mfma_f32_16x16x32_bf16 v[208:211], v[72:75], v[192:195], v[208:211]\n"
    "v_mfma_f32_16x16x32_bf16 v[212:215], v[96:99], v[192:195], v[212:215]\n"
    "s_waitcnt lgkmcnt(1)\n"
    "v_cmp_gt_u32 vcc, 8, v229\n"
    "v_mfma_f32_16x16x32_bf16 v[208:211], v[76:79], v[196:199], v[208:211]\n"
    "v_mfma_f32_16x16x32_bf16 v[212:215], v[100:103], v[196:199], v[212:215]\n"
    "s_cbranch_vccnz .Lg19\n"
    ".Lg19b:\n"
    "v_mfma_f32_16x16x32_bf16 v[216:219], v[104:107], v[176:179], 0\n"
    "v_add_u32 v4, v6, v4\n"
    "v_cmp_gt_u32 vcc, 2, v239\n"
    "v_mfma_f32_16x16x32_bf16 v[220:223], v[128:131], v[176:179], 0\n"
    "ds_read_b128 v[152:155], v4 offset:0\n"
    "ds_read_b128 v[156:159], v4 offset:64\n"
    "v_mfma_f32_16x16x32_bf16 v[216:219], v[108:111], v[180:183], v[216:219]\n"
    "ds_read_b128 v[160:163], v4 offset:128\n"
    "ds_read_b128 v[164:167], v4 offset:192\n"
    "v_mfma_f32_16x16x32_bf16 v[220:223], v[132:135], v[180:183], v[220:223]\n"
    "ds_read_b128 v[168:171], v4 offset:256\n"
    "ds_read_b128 v[172:175], v4 offset:320\n"
    "v_mfma_f32_16x16x32_bf16 v[216:219], v[112:115], v[184:187], v[216:219]\n"
    "v_cvt_pk_bf16_f32 v208, v208, v209\n"
    "v_cvt_pk_bf16_f32 v209, v210, v211\n"
    "v_mfma_f32_16x16x32_bf16 v[220:223], v[136:139], v[184:187], v[220:223]\n"
    "v_cvt_pk_bf16_f32 v210, v212, v213\n"
    "v_cvt_pk_bf16_f32 v211, v214, v215\n"
    "v_mfma_f32_16x16x32_bf16 v[216:219], v[116:119], v[188:191], v[216:219]\n"
    "ds_write_b128 v7, v[208:211] offset:1024\n"
    "v_mfma_f32_16x16x32_bf16 v[220:223], v[140:143], v[188:191], v[220:223]\n"
    "s_cbranch_vccnz .Lg21\n"
    ".Lg21b:\n"
    "v_mfma_f32_16x16x32_bf16 v[216:219], v[120:123], v[192:195], v[216:219]\n"
    "s_add_u32 s60, s42, s48\n"
    "s_addc_u32 s61, s43, 0\n"
    "v_mfma_f32_16x16x32_bf16 v[220:223], v[144:147], v[192:195], v[220:223]\n"
    "s_add_u32 m0, s54, 35584\n"
    "v_mfma_f32_16x16x32_bf16 v[216:219], v[124:127], v[196:199], v[216:219]\n"
    "global_load_lds_dwordx4 v230, s[60:61]\n"
    "s_add_u32 m0, s54, 37632\n"
    "v_mfma_f32_16x16x32_bf16 v[220:223], v[148:151], v[196:199], v[220:223]\n"
    "global_load_lds_dwordx4 v231, s[60:61]\n"
    "s_add_u32 m0, s54, 39680\n"
    "s_waitcnt lgkmcnt(1)\n"
    "global_load_lds_dwordx4 v232, s[60:61]\n"
    "s_add_u32 m0, s55, 35584\n"
    "s_nop 0\n"
    "global_load_lds_dwordx4 v233, s[60:61]\n"
    "s_add_u32 m0, s56, 35584\n"
    "s_nop 0\n"
    "global_load_lds_dwordx4 v234, s[60:61]\n"
    "s_add_u32 s60, s42, s49\n"
    "s_addc_u32 s61, s43, 0\n"
    "s_add_u32 m0, s54, 44480\n"
    "s_nop 0\n"
    "global_load_lds_dwordx4 v230, s[60:61]\n"
    "s_add_u32 m0, s54, 46528\n"
    "s_nop 0\n"
    "global_load_lds_dwordx4 v231, s[60:61]\n"
    "s_add_u32 m0, s54, 48576\n"
    "s_nop 0\n"
    "global_load_lds_dwordx4 v232, s[60:61]\n"
    "s_add_u32 m0, s55, 44480\n"
    "s_nop 0\n"
    "global_load_lds_dwordx4 v233, s[60:61]\n"
    "s_add_u32 m0, s56, 44480\n"
    "s_nop 0\n"
    "global_load_lds_dwordx4 v234, s[60:61]\n"
    "v_mfma_f32_16x16x32_bf16 v[200:203], v[8:11], v[152:155], 0\n"
    "v_cvt_pk_bf16_f32 v216, v216, v217\n"
    "v_cvt_pk_bf16_f32 v217, v218, v219\n"
    "v_mfma_f32_16x16x32_bf16 v[204:207], v[32:35], v[152:155], 0\n"
    "v_cvt_pk_bf16_f32 v218, v220, v221\n"
    "v_cvt_pk_bf16_f32 v219, v222, v223\n"
    "v_mfma_f32_16x16x32_bf16 v[200:203], v[12:15], v[156:159], v[200:203]\n"
    "ds_write_b128 v7, v[216:219] offset:2048\n"
    "v_mfma_f32_16x16x32_bf16 v[204:207], v[36:39], v[156:159], v[204:207]\n"
    "v_mov_b32 v227, 10\n"
    "v_mfma_f32_16x16x32_bf16 v[200:203], v[16:19], v[160:163], v[200:203]\n"
    "ds_write_b32 v225, v227 offset:16\n"
    "ds_read_b32 v228, v224 offset:240\n"
    "v_mfma_f32_16x16x32_bf16 v[204:207], v[40:43], v[160:163], v[204:207]\n"
    "s_waitcnt vmcnt(10)\n"
    "v_mov_b32 v227, 9\n"
    "v_mfma_f32_16x16x32_bf16 v[200:203], v[20:23], v[164:167], v[200:203]\n"
    "ds_write_b32 v226, v227 offset:0\n"
    "v_mfma_f32_16x16x32_bf16 v[204:207], v[44:47], v[164:167], v[204:207]\n"
    "v_mfma_f32_16x16x32_bf16 v[200:203], v[24:27], v[168:171], v[200:203]\n"
    "v_mfma_f32_16x16x32_bf16 v[204:207], v[48:51], v[168:171], v[204:207]\n"
    "v_mfma_f32_16x16x32_bf16 v[200:203], v[28:31], v[172:175], v[200:203]\n"
    "v_mfma_f32_16x16x32_bf16 v[204:207], v[52:55], v[172:175], v[204:207]\n"
    "v_mfma_f32_16x16x32_bf16 v[208:211], v[56:59], v[152:155], 0\n"
    "s_waitcnt lgkmcnt(1)\n"
    "v_cmp_gt_u32 vcc, 2, v228\n"
    "v_mfma_f32_16x16x32_bf16 v[212:215], v[80:83], v[152:155], 0\n"
    "v_mfma_f32_16x16x32_bf16 v[208:211], v[60:63], v[156:159], v[208:211]\n"
    "s_cbranch_vccnz .Lg22\n"
    ".Lg22b:\n"
    "v_mfma_f32_16x16x32_bf16 v[212:215], v[84:87], v[156:159], v[212:215]\n"
    "v_cvt_pk_bf16_f32 v200, v200, v201\n"
    "v_cvt_pk_bf16_f32 v201, v202, v203\n"
    "v_mfma_f32_16x16x32_bf16 v[208:211], v[64:67], v[160:163], v[208:211]\n"
    "v_cvt_pk_bf16_f32 v202, v204, v205\n"
    "v_cvt_pk_bf16_f32 v203, v206, v207\n"
    "v_mfma_f32_16x16x32_bf16 v[212:215], v[88:91], v[160:163], v[212:215]\n"
    "ds_write_b128 v7, v[200:203] offset:6144\n"
    "v_mfma_f32_16x16x32_bf16 v[208:211], v[68:71], v[164:167], v[208:211]\n"
    "v_mfma_f32_16x16x32_bf16 v[212:215], v[92:95], v[164:167], v[212:215]\n"
    "v_mfma_f32_16x16x32_bf16 v[208:211], v[72:75], v[168:171], v[208:211]\n"
    "v_mfma_f32_16x16x32_bf16 v[212:215], v[96:99], v[168:171], v[212:215]\n"
    "v_mfma_f32_16x16x32_bf16 v[208:211], v[76:79], v[172:175], v[208:211]\n"
    "v_mfma_f32_16x16x32_bf16 v[212:215], v[100:103], v[172:175], v[212:215]\n"
    "v_mfma_f32_16x16x32_bf16 v[216:219], v[104:107], v[152:155], 0\n"
    "v_add_u32 v5, v6, v5\n"
    "v_mfma_f32_16x16x32_bf16 v[220:223], v[128:131], v[152:155], 0\n"
    "ds_read_b128 v[176:179], v5 offset:0\n"
    "ds_read_b128 v[180:183], v5 offset:64\n"
    "v_mfma_f32_16x16x32_bf16 v[216:219], v[108:111], v[156:159], v[216:219]\n"
    "ds_read_b128 v[184:187], v5 offset:128\n"
    "ds_read_b128 v[188:191], v5 offset:192\n"
    "v_mfma_f32_16x16x32_bf16 v[220:223], v[132:135], v[156:159], v[220:223]\n"
    "ds_read_b128 v[192:195], v5 offset:256\n"
    "ds_read_b128 v[196:199], v5 offset:320\n"
    "v_mfma_f32_16x16x32_bf16 v[216:219], v[112:115], v[160:163], v[216:219]\n"
    "v_cvt_pk_bf16_f32 v208, v208, v209\n"
    "v_cvt_pk_bf16_f32 v209, v210, v211\n"
    "v_mfma_f32_16x16x32_bf16 v[220:223], v[136:139], v[160:163], v[220:223]\n"
    "v_cvt_pk_bf16_f32 v210, v212, v213\n"
    "v_cvt_pk_bf16_f32 v211, v214, v215\n"
    "v_mfma_f32_16x16x32_bf16 v[216:219], v[116:119], v[164:167], v[216:219]\n"
    "ds_write_b128 v7, v[208:211] offset:7168\n"
    "v_mfma_f32_16x16x32_bf16 v[220:223], v[140:143], v[164:167], v[220:223]\n"
    "v_mfma_f32_16x16x32_bf16 v[216:219], v[120:123], v[168:171], v[216:219]\n"
    "v_mfma_f32_16x16x32_bf16 v[220:223], v[144:147], v[168:171], v[220:223]\n"
    "v_mfma_f32_16x16x32_bf16 v[216:219], v[124:127], v[172:175], v[216:219]\n"
    "v_mfma_f32_16x16x32_bf16 v[220:223], v[148:151], v[172:175], v[220:223]\n"
    "s_waitcnt lgkmcnt(1)\n"
    "s_lshl1_add_u32 s53, s58, s50\n"
    "s_cmp_eq_u32 s53, 2\n"
    "s_cbranch_scc1 .Lg_e12a\n"
    "s_cmp_eq_u32 s53, 3\n"
    "s_cbranch_scc1 .Lg_e12b\n"
    "v_mfma_f32_16x16x32_bf16 v[200:203], v[8:11], v[176:179], 0\n"
    "v_cvt_pk_bf16_f32 v216, v216, v217\n"
    "v_cvt_pk_bf16_f32 v217, v218, v219\n"
    "v_mfma_f32_16x16x32_bf16 v[204:207], v[32:35], v[176:179], 0\n"
    "v_cvt_pk_bf16_f32 v218, v220, v221\n"
    "v_cvt_pk_bf16_f32 v219, v222, v223\n"
    "v_mfma_f32_16x16x32_bf16 v[200:203], v[12:15], v[180:183], v[200:203]\n"
    "ds_write_b128 v7, v[216:219] offset:8192\n"
    "v_mfma_f32_16x16x32_bf16 v[204:207], v[36:39], v[180:183], v[204:207]\n"
    "v_mov_b32 v227, 11\n"
    "v_mfma_f32_16x16x32_bf16 v[200:203], v[16:19], v[184:187], v[200:203]\n"
    "ds_write_b32 v225, v227 offset:16\n"
    "ds_read_b32 v228, v224 offset:192\n"
    "v_mfma_f32_16x16x32_bf16 v[204:207], v[40:43], v[184:187], v[204:207]\n"
    "v_mfma_f32_16x16x32_bf16 v[200:203], v[20:23], v[188:191], v[200:203]\n"
    "v_mfma_f32_16x16x32_bf16 v[204:207], v[44:47], v[188:191], v[204:207]\n"
    "v_mfma_f32_16x16x32_bf16 v[200:203], v[24:27], v[192:195], v[200:203]\n"
    "v_mfma_f32_16x16x32_bf16 v[204:207], v[48:51], v[192:195], v[204:207]\n"
    "v_mfma_f32_16x16x32_bf16 v[200:203], v[28:31], v[196:199], v[200:203]\n"
    "v_mfma_f32_16x16x32_bf16 v[204:207], v[52:55], v[196:199], v[204:207]\n"
    "v_mfma_f32_16x16x32_bf16 v[208:211], v[56:59], v[176:179], 0\n"
    "s_waitcnt lgkmcnt(0)\n"
    "v_cmp_gt_u32 vcc, 3, v228\n"
    "v_mfma_f32_16x16x32_bf16 v[212:215], v[80:83], v[176:179], 0\n"
    "ds_read_b32 v229, v224 offset:0\n"
    "s_cbranch_vccnz .Lg27\n"
    ".Lg27b:\n"
    "v_mfma_f32_16x16x32_bf16 v[208:211], v[60:63], v[180:183], v[208:211]\n"
    "v_cvt_pk_bf16_f32 v200, v200, v201\n"
    "v_cvt_pk_bf16_f32 v201, v202, v203\n"
    "v_mfma_f32_16x16x32_bf16 v[212:215], v[84:87], v[180:183], v[212:215]\n"
    "v_cvt_pk_bf16_f32 v202, v204, v205\n"
    "v_cvt_pk_bf16_f32 v203, v206, v207\n"
    "v_mfma_f32_16x16x32_bf16 v[208:211], v[64:67], v[184:187], v[208:211]\n"
    "ds_write_b128 v7, v[200:203] offset:12288\n"
    "v_mfma_f32_16x16x32_bf16 v[212:215], v[88:91], v[184:187], v[212:215]\n"
    "v_mfma_f32_16x16x32_bf16 v[208:211], v[68:71], v[188:191], v[208:211]\n"
    "v_mfma_f32_16x16x32_bf16 v[212:215], v[92:95], v[188:191], v[212:215]\n"
    "v_mfma_f32_16x16x32_bf16 v[208:211], v[72:75], v[192:195], v[208:211]\n"
    "v_mfma_f32_16x16x32_bf16 v[212:215], v[96:99], v[192:195], v[212:215]\n"
    "s_waitcnt lgkmcnt(1)\n"
    "v_cmp_gt_u32 vcc, 9, v229\n"
    "v_mfma_f32_16x16x32_bf16 v[208:211], v[76:79], v[196:199], v[208:211]\n"
    "v_mfma_f32_16x16x32_bf16 v[212:215], v[100:103], v[196:199], v[212:215]\n"
    "s_cbranch_vccnz .Lg26\n"
    ".Lg26b:\n"
    "v_mfma_f32_16x16x32_bf16 v[216:219], v[104:107], v[176:179], 0\n"
    "v_add_u32 v2, s84, v2\n"
    "v_mfma_f32_16x16x32_bf16 v[220:223], v[128:131], v[176:179], 0\n"
    "ds_read_b128 v[152:155], v2 offset:8896\n"
    "ds_read_b128 v[156:159], v2 offset:8960\n"
    "v_mfma_f32_16x16x32_bf16 v[216:219], v[108:111], v[180:183], v[216:219]\n"
    "ds_read_b128 v[160:163], v2 offset:9024\n"
    "ds_read_b128 v[164:167], v2 offset:9088\n"
    "v_mfma_f32_16x16x32_bf16 v[220:223], v[132:135], v[180:183], v[220:223]\n"
    "ds_read_b128 v[168:171], v2 offset:9152\n"
    "ds_read_b128 v[172:175], v2 offset:9216\n"
    "v_mfma_f32_16x16x32_bf16 v[216:219], v[112:115], v[184:187], v[216:219]\n"
    "v_cvt_pk_bf16_f32 v208, v208, v209\n"
    "v_cvt_pk_bf16_f32 v209, v210, v211\n"
    "v_mfma_f32_16x16x32_bf16 v[220:223], v[136:139], v[184:187], v[220:223]\n"
    "v_cvt_pk_bf16_f32 v210, v212, v213\n"
    "v_cvt_pk_bf16_f32 v211, v214, v215\n"
    "v_mfma_f32_16x16x32_bf16 v[216:219], v[116:119], v[188:191], v[216:219]\n"
    "ds_write_b128 v7, v[208:211] offset:13312\n"
    "v_mfma_f32_16x16x32_bf16 v[220:223], v[140:143], v[188:191], v[220:223]\n"
    "v_mfma_f32_16x16x32_bf16 v[216:219], v[120:123], v[192:195], v[216:219]\n"
    "v_mfma_f32_16x16x32_bf16 v[220:223], v[144:147], v[192:195], v[220:223]\n"
    "v_mfma_f32_16x16x32_bf16 v[216:219], v[124:127], v[196:199], v[216:219]\n"
    "v_mfma_f32_16x16x32_bf16 v[220:223], v[148:151], v[196:199], v[220:223]\n"
    "s_waitcnt lgkmcnt(1)\n"
    "s_cmp_eq_u32 s50, 0\n"
    "s_cbranch_scc1 .Lg_e13a\n"
    "v_mfma_f32_16x16x32_bf16 v[200:203], v[8:11], v[152:155], 0\n"
    "v_mov_b32 v227, 12\n"
    "v_mfma_f32_16x16x32_bf16 v[204:207], v[32:35], v[152:155], 0\n"
    "v_mfma_f32_16x16x32_bf16 v[200:203], v[12:15], v[156:159], v[200:203]\n"
    "v_cvt_pk_bf16_f32 v216, v216, v217\n"
    "v_cvt_pk_bf16_f32 v217, v218, v219\n"
    "v_mfma_f32_16x16x32_bf16 v[204:207], v[36:39], v[156:159], v[204:207]\n"
    "v_cvt_pk_bf16_f32 v218, v220, v221\n"
    "v_cvt_pk_bf16_f32 v219, v222, v223\n"
    "v_mfma_f32_16x16x32_bf16 v[200:203], v[16:19], v[160:163], v[200:203]\n"
    "ds_write_b128 v7, v[216:219] offset:14336\n"
    "v_mfma_f32_16x16x32_bf16 v[204:207], v[40:43], v[160:163], v[204:207]\n"
    "ds_write_b32 v225, v227 offset:16\n"
    "ds_read_b32 v228, v224 offset:208\n"
    "v_mfma_f32_16x16x32_bf16 v[200:203], v[20:23], v[164:167], v[200:203]\n"
    "s_waitcnt vmcnt(0)\n"
    "v_mov_b32 v227, 10\n"
    "v_mfma_f32_16x16x32_bf16 v[204:207], v[44:47], v[164:167], v[204:207]\n"
    "ds_write_b32 v226, v227 offset:0\n"
    "v_mfma_f32_16x16x32_bf16 v[200:203], v[24:27], v[168:171], v[200:203]\n"
    "v_mfma_f32_16x16x32_bf16 v[204:207], v[48:51], v[168:171], v[204:207]\n"
    "v_mfma_f32_16x16x32_bf16 v[200:203], v[28:31], v[172:175], v[200:203]\n"
    "v_mfma_f32_16x16x32_bf16 v[204:207], v[52:55], v[172:175], v[204:207]\n"
    "ds_read_b32 v239, v224 offset:272\n"
    "s_waitcnt lgkmcnt(0)\n"
    "v_cmp_gt_u32 vcc, 1, v239\n"
    "s_cbranch_vccnz .Lg30\n"
    ".Lg30b:\n"
    "v_mfma_f32_16x16x32_bf16 v[208:211], v[56:59], v[152:155], 0\n"
    "v_cmp_gt_u32 vcc, 3, v228\n"
    "v_mfma_f32_16x16x32_bf16 v[212:215], v[80:83], v[152:155], 0\n"
    "s_cbranch_vccnz .Lg29\n"
    ".Lg29b:\n"
    "v_cvt_pk_bf16_f32 v200, v200, v201\n"
    "v_mfma_f32_16x16x32_bf16 v[208:211], v[60:63], v[156:159], v[208:211]\n"
    "v_cvt_pk_bf16_f32 v201, v202, v203\n"
    "v_cvt_pk_bf16_f32 v202, v204, v205\n"
    "v_mfma_f32_16x16x32_bf16 v[212:215], v[84:87], v[156:159], v[212:215]\n"
    "v_cvt_pk_bf16_f32 v203, v206, v207\n"
    "v_mfma_f32_16x16x32_bf16 v[208:211], v[64:67], v[160:163], v[208:211]\n"
    "ds_write_b128 v7, v[200:203] offset:0\n"
    "v_mfma_f32_16x16x32_bf16 v[212:215], v[88:91], v[160:163], v[212:215]\n"
    "s_add_u32 s60, s40, 109568\n"
    "s_addc_u32 s61, s41, 0\n"
    "v_mfma_f32_16x16x32_bf16 v[208:211], v[68:71], v[164:167], v[208:211]\n"
    "s_add_u32 s66, s60, 6144\n"
    "s_addc_u32 s67, s61, 0\n"
    "v_mfma_f32_16x16x32_bf16 v[212:215], v[92:95], v[164:167], v[212:215]\n"
    "global_load_dwordx4 v[8:11], v235, s[60:61] offset:-3072 sc1\n"
    "global_load_dwordx4 v[32:35], v235, s[66:67] offset:-3072 sc1\n"
    "v_mfma_f32_16x16x32_bf16 v[208:211], v[72:75], v[168:171], v[208:211]\n"
    "global_load_dwordx4 v[12:15], v235, s[60:61] offset:-2048 sc1\n"
    "global_load_dwordx4 v[36:39], v235, s[66:67] offset:-2048 sc1\n"
    "v_mfma_f32_16x16x32_bf16 v[212:215], v[96:99], v[168:171], v[212:215]\n"
    "global_load_dwordx4 v[16:19], v235, s[60:61] offset:-1024 sc1\n"
    "global_load_dwordx4 v[40:43], v235, s[66:67] offset:-1024 sc1\n"
    "v_mfma_f32_16x16x32_bf16 v[208:211], v[76:79], v[172:175], v[208:211]\n"
    "global_load_dwordx4 v[20:23], v235, s[60:61] offset:0 sc1\n"
    "global_load_dwordx4 v[44:47], v235, s[66:67] offset:0 sc1\n"
    "v_mfma_f32_16x16x32_bf16 v[212:215], v[100:103], v[172:175], v[212:215]\n"
    "global_load_dwordx4 v[24:27], v235, s[60:61] offset:1024 sc1\n"
    "global_load_dwordx4 v[48:51], v235, s[66:67] offset:1024 sc1\n"
    "v_mfma_f32_16x16x32_bf16 v[216:219], v[104:107], v[152:155], 0\n"
    "global_load_dwordx4 v[28:31], v235, s[60:61] offset:2048 sc1\n"
    "global_load_dwordx4 v[52:55], v235, s[66:67] offset:2048 sc1\n"
    "v_mfma_f32_16x16x32_bf16 v[220:223], v[128:131], v[152:155], 0\n"
    "v_cvt_pk_bf16_f32 v208, v208, v209\n"
    "v_cvt_pk_bf16_f32 v209, v210, v211\n"
    "v_mfma_f32_16x16x32_bf16 v[216:219], v[108:111], v[156:159], v[216:219]\n"
    "v_cvt_pk_bf16_f32 v210, v212, v213\n"
    "v_cvt_pk_bf16_f32 v211, v214, v215\n"
    "v_mfma_f32_16x16x32_bf16 v[220:223], v[132:135], v[156:159], v[220:223]\n"
    "ds_write_b128 v7, v[208:211] offset:1024\n"
    "v_mfma_f32_16x16x32_bf16 v[216:219], v[112:115], v[160:163], v[216:219]\n"
    "s_add_u32 s60, s40, 146432\n"
    "s_addc_u32 s61, s41, 0\n"
    "v_mfma_f32_16x16x32_bf16 v[220:223], v[136:139], v[160:163], v[220:223]\n"
    "s_add_u32 s66, s60, 6144\n"
    "s_addc_u32 s67, s61, 0\n"
    "v_mfma_f32_16x16x32_bf16 v[216:219], v[116:119], v[164:167], v[216:219]\n"
    "global_load_dwordx4 v[56:59], v235, s[60:61] offset:-3072 sc1\n"
    "global_load_dwordx4 v[80:83], v235, s[66:67] offset:-3072 sc1\n"
    "v_mfma_f32_16x16x32_bf16 v[220:223], v[140:143], v[164:167], v[220:223]\n"
    "global_load_dwordx4 v[60:63], v235, s[60:61] offset:-2048 sc1\n"
    "global_load_dwordx4 v[84:87], v235, s[66:67] offset:-2048 sc1\n"
    "v_mfma_f32_16x16x32_bf16 v[216:219], v[120:123], v[168:171], v[216:219]\n"
    "global_load_dwordx4 v[64:67], v235, s[60:61] offset:-1024 sc1\n"
    "global_load_dwordx4 v[88:91], v235, s[66:67] offset:-1024 sc1\n"
    "v_mfma_f32_16x16x32_bf16 v[220:223], v[144:147], v[168:171], v[220:223]\n"
    "global_load_dwordx4 v[68:71], v235, s[60:61] offset:0 sc1\n"
    "global_load_dwordx4 v[92:95], v235, s[66:67] offset:0 sc1\n"
    "v_mfma_f32_16x16x32_bf16 v[216:219], v[124:127], v[172:175], v[216:219]\n"
    "global_load_dwordx4 v[72:75], v235, s[60:61] offset:1024 sc1\n"
    "global_load_dwordx4 v[96:99], v235, s[66:67] offset:1024 sc1\n"
    "v_mfma_f32_16x16x32_bf16 v[220:223], v[148:151], v[172:175], v[220:223]\n"
    "global_load_dwordx4 v[76:79], v235, s[60:61] offset:2048 sc1\n"
    "global_load_dwordx4 v[100:103], v235, s[66:67] offset:2048 sc1\n"
    "s_nop 2\n"
    "v_cvt_pk_bf16_f32 v216, v216, v217\n"
    "v_cvt_pk_bf16_f32 v217, v218, v219\n"
    "s_nop 0\n"
    "v_cvt_pk_bf16_f32 v218, v220, v221\n"
    "v_cvt_pk_bf16_f32 v219, v222, v223\n"
    "ds_write_b128 v7, v[216:219] offset:2048\n"
    "v_mov_b32 v227, 13\n"
    "ds_write_b32 v225, v227 offset:16\n"
    ".Lg_to:\n"
    "s_waitcnt vmcnt(12)\n"
    "v_sub_u32 v8, v8, v240\n"
    "v_sub_u32 v12, v12, v240\n"
    "v_sub_u32 v16, v16, v240\n"
    "v_sub_u32 v20, v20, v240\n"
    "v_sub_u32 v24, v24, v240\n"
    "v_sub_u32 v28, v28, v240\n"
    "v_sub_u32 v32, v32, v240\n"
    "v_sub_u32 v36, v36, v240\n"
    "v_sub_u32 v40, v40, v240\n"
    "v_sub_u32 v44, v44, v240\n"
    "v_sub_u32 v48, v48, v240\n"
    "v_sub_u32 v52, v52, v240\n"
    "s_waitcnt vmcnt(0)\n"
    "v_sub_u32 v56, v56, v240\n"
    "v_sub_u32 v60, v60, v240\n"
    "v_sub_u32 v64, v64, v240\n"
    "v_sub_u32 v68, v68, v240\n"
    "v_sub_u32 v72, v72, v240\n"
    "v_sub_u32 v76, v76, v240\n"
    "v_sub_u32 v80, v80, v240\n"
    "v_sub_u32 v84, v84, v240\n"
    "v_sub_u32 v88, v88, v240\n"
    "v_sub_u32 v92, v92, v240\n"
    "v_sub_u32 v96, v96, v240\n"
    "v_sub_u32 v100, v100, v240\n"
    "ds_read_b32 v228, v224 offset:144\n"
    "s_waitcnt lgkmcnt(0)\n"
    "v_cmp_gt_u32 vcc, 3, v228\n"
    "s_cbranch_vccnz .Lg31\n"
    ".Lg31b:\n"
    "ds_read_b64_tr_b16 v[176:177], v0 offset:18432\n"
    "ds_read_b64_tr_b16 v[178:179], v1 offset:18432\n"
    "ds_read_b64_tr_b16 v[180:181], v0 offset:19456\n"
    "ds_read_b64_tr_b16 v[182:183], v1 offset:19456\n"
    "ds_read_b64_tr_b16 v[184:185], v0 offset:20480\n"
    "ds_read_b64_tr_b16 v[186:187], v1 offset:20480\n"
    "ds_read_b64_tr_b16 v[188:189], v0 offset:21504\n"
    "ds_read_b64_tr_b16 v[190:191], v1 offset:21504\n"
    "ds_read_b64_tr_b16 v[192:193], v0 offset:22528\n"
    "ds_read_b64_tr_b16 v[194:195], v1 offset:22528\n"
    "ds_read_b64_tr_b16 v[196:197], v0 offset:23552\n"
    "ds_read_b64_tr_b16 v[198:199], v1 offset:23552\n"
    "ds_read_b32 v253, v238 offset:0\n"
    "s_waitcnt lgkmcnt(9)\n"
    "v_mfma_f32_16x16x32_bf16 v[200:203], v[8:11], v[176:179], 0\n"
    "v_mfma_f32_16x16x32_bf16 v[204:207], v[32:35], v[176:179], 0\n"
    "ds_read_b32 v228, v224 offset:160\n"
    "v_mfma_f32_16x16x32_bf16 v[200:203], v[12:15], v[180:183], v[200:203]\n"
    "v_mfma_f32_16x16x32_bf16 v[204:207], v[36:39], v[180:183], v[204:207]\n"
    "s_waitcnt lgkmcnt(8)\n"
    "v_mfma_f32_16x16x32_bf16 v[200:203], v[16:19], v[184:187], v[200:203]\n"
    "v_mfma_f32_16x16x32_bf16 v[204:207], v[40:43], v[184:187], v[204:207]\n"
    "s_waitcnt lgkmcnt(6)\n"
    "v_mfma_f32_16x16x32_bf16 v[200:203], v[20:23], v[188:191], v[200:203]\n"
    "v_mfma_f32_16x16x32_bf16 v[204:207], v[44:47], v[188:191], v[204:207]\n"
    "s_waitcnt lgkmcnt(4)\n"
    "v_mfma_f32_16x16x32_bf16 v[200:203], v[24:27], v[192:195], v[200:203]\n"
    "v_mfma_f32_16x16x32_bf16 v[204:207], v[48:51], v[192:195], v[204:207]\n"
    "s_waitcnt lgkmcnt(2)\n"
    "v_mfma_f32_16x16x32_bf16 v[200:203], v[28:31], v[196:199], v[200:203]\n"
    "s_waitcnt lgkmcnt(0)\n"
    "v_cmp_gt_u32 vcc, 3, v228\n"
    "v_mfma_f32_16x16x32_bf16 v[204:207], v[52:55], v[196:199], v[204:207]\n"
    "s_cbranch_vccnz .Lg32\n"
    "v_mfma_f32_16x16x32_bf16 v[208:211], v[56:59], v[176:179], 0\n"
    "ds_read_b64_tr_b16 v[152:153], v0 offset:6144\n"
    "ds_read_b64_tr_b16 v[154:155], v1 offset:6144\n"
    "v_mfma_f32_16x16x32_bf16 v[212:215], v[80:83], v[176:179], 0\n"
    "ds_read_b64_tr_b16 v[156:157], v0 offset:7168\n"
    "ds_read_b64_tr_b16 v[158:159], v1 offset:7168\n"
    "v_mfma_f32_16x16x32_bf16 v[208:211], v[60:63], v[180:183], v[208:211]\n"
    "ds_read_b64_tr_b16 v[160:161], v0 offset:8192\n"
    "ds_read_b64_tr_b16 v[162:163], v1 offset:8192\n"
    "v_mfma_f32_16x16x32_bf16 v[212:215], v[84:87], v[180:183], v[212:215]\n"
    "ds_read_b64_tr_b16 v[164:165], v0 offset:9216\n"
    "ds_read_b64_tr_b16 v[166:167], v1 offset:9216\n"
    "v_mfma_f32_16x16x32_bf16 v[208:211], v[64:67], v[184:187], v[208:211]\n"
    "v_mul_f32 v200, v200, v253\n"
    "v_mul_f32 v201, v201, v253\n"
    "v_mfma_f32_16x16x32_bf16 v[212:215], v[88:91], v[184:187], v[212:215]\n"
    "ds_read_b64_tr_b16 v[168:169], v0 offset:10240\n"
    "ds_read_b64_tr_b16 v[170:171], v1 offset:10240\n"
    "v_mfma_f32_16x16x32_bf16 v[208:211], v[68:71], v[188:191], v[208:211]\n"
    "v_mul_f32 v202, v202, v253\n"
    "v_mul_f32 v203, v203, v253\n"
    "v_mfma_f32_16x16x32_bf16 v[212:215], v[92:95], v[188:191], v[212:215]\n"
    "ds_read_b64_tr_b16 v[172:173], v0 offset:11264\n"
    "ds_read_b64_tr_b16 v[174:175], v1 offset:11264\n"
    "v_mfma_f32_16x16x32_bf16 v[208:211], v[72:75], v[192:195], v[208:211]\n"
    "v_mul_f32 v204, v204, v253\n"
    "v_mul_f32 v205, v205, v253\n"
    "v_mfma_f32_16x16x32_bf16 v[212:215], v[96:99], v[192:195], v[212:215]\n"
    "ds_read_b32 v252, v238 offset:256\n"
    "v_mul_f32 v206, v206, v253\n"
    "v_mfma_f32_16x16x32_bf16 v[208:211], v[76:79], v[196:199], v[208:211]\n"
    "v_mul_f32 v207, v207, v253\n"
    "v_cvt_pk_bf16_f32 v200, v200, v201\n"
    "v_mfma_f32_16x16x32_bf16 v[212:215], v[100:103], v[196:199], v[212:215]\n"
    "v_cvt_pk_bf16_f32 v201, v202, v203\n"
    "v_cvt_pk_bf16_f32 v202, v204, v205\n"
    "v_cvt_pk_bf16_f32 v203, v206, v207\n"
    "buffer_store_dwordx4 v[200:203], v249, s[80:83], 0 offen offset:128 sc1\n"
    ".Lg32j:\n"
    "s_waitcnt lgkmcnt(1)\n"
    "s_nop 0\n"
    "v_mfma_f32_16x16x32_bf16 v[200:203], v[8:11], v[152:155], 0\n"
    "v_mul_f32 v208, v208, v253\n"
    "v_mul_f32 v209, v209, v253\n"
    "v_mfma_f32_16x16x32_bf16 v[204:207], v[32:35], v[152:155], 0\n"
    "ds_read_b32 v228, v224 offset:176\n"
    "v_mul_f32 v210, v210, v253\n"
    "v_mfma_f32_16x16x32_bf16 v[200:203], v[12:15], v[156:159], v[200:203]\n"
    "v_mul_f32 v211, v211, v253\n"
    "v_mul_f32 v212, v212, v253\n"
    "v_mfma_f32_16x16x32_bf16 v[204:207], v[36:39], v[156:159], v[204:207]\n"
    "v_mul_f32 v213, v213, v253\n"
    "v_mul_f32 v214, v214, v253\n"
    "v_mfma_f32_16x16x32_bf16 v[200:203], v[16:19], v[160:163], v[200:203]\n"
    "v_mul_f32 v215, v215, v253\n"
    "v_cvt_pk_bf16_f32 v208, v208, v209\n"
    "v_mfma_f32_16x16x32_bf16 v[204:207], v[40:43], v[160:163], v[204:207]\n"
    "v_cvt_pk_bf16_f32 v209, v210, v211\n"
    "v_cvt_pk_bf16_f32 v210, v212, v213\n"
    "v_mfma_f32_16x16x32_bf16 v[200:203], v[20:23], v[164:167], v[200:203]\n"
    "v_cvt_pk_bf16_f32 v211, v214, v215\n"
    "v_mfma_f32_16x16x32_bf16 v[204:207], v[44:47], v[164:167], v[204:207]\n"
    "buffer_store_dwordx4 v[208:211], v249, s[80:83], 0 offen offset:320 sc1\n"
    "v_mfma_f32_16x16x32_bf16 v[200:203], v[24:27], v[168:171], v[200:203]\n"
    "v_mfma_f32_16x16x32_bf16 v[204:207], v[48:51], v[168:171], v[204:207]\n"
    "v_add_u32 v249, v251, v249\n"
    "v_mfma_f32_16x16x32_bf16 v[200:203], v[28:31], v[172:175], v[200:203]\n"
    "s_waitcnt lgkmcnt(0)\n"
    "v_cmp_gt_u32 vcc, 3, v228\n"
    "v_mfma_f32_16x16x32_bf16 v[204:207], v[52:55], v[172:175], v[204:207]\n"
    "s_cbranch_vccnz .Lg33\n"
    "v_mfma_f32_16x16x32_bf16 v[208:211], v[56:59], v[152:155], 0\n"
    "ds_read_b64_tr_b16 v[176:177], v0 offset:12288\n"
    "ds_read_b64_tr_b16 v[178:179], v1 offset:12288\n"
    "v_mfma_f32_16x16x32_bf16 v[212:215], v[80:83], v[152:155], 0\n"
    "ds_read_b64_tr_b16 v[180:181], v0 offset:13312\n"
    "ds_read_b64_tr_b16 v[182:183], v1 offset:13312\n"
    "v_mfma_f32_16x16x32_bf16 v[208:211], v[60:63], v[156:159], v[208:211]\n"
    "ds_read_b64_tr_b16 v[184:185], v0 offset:14336\n"
    "ds_read_b64_tr_b16 v[186:187], v1 offset:14336\n"
    "v_mfma_f32_16x16x32_bf16 v[212:215], v[84:87], v[156:159], v[212:215]\n"
    "ds_read_b64_tr_b16 v[188:189], v0 offset:15360\n"
    "ds_read_b64_tr_b16 v[190:191], v1 offset:15360\n"
    "v_mfma_f32_16x16x32_bf16 v[208:211], v[64:67], v[160:163], v[208:211]\n"
    "v_mul_f32 v200, v200, v252\n"
    "v_mul_f32 v201, v201, v252\n"
    "v_mfma_f32_16x16x32_bf16 v[212:215], v[88:91], v[160:163], v[212:215]\n"
    "ds_read_b64_tr_b16 v[192:193], v0 offset:16384\n"
    "ds_read_b64_tr_b16 v[194:195], v1 offset:16384\n"
    "v_mfma_f32_16x16x32_bf16 v[208:211], v[68:71], v[164:167], v[208:211]\n"
    "v_mul_f32 v202, v202, v252\n"
    "v_mul_f32 v203, v203, v252\n"
    "v_mfma_f32_16x16x32_bf16 v[212:215], v[92:95], v[164:167], v[212:215]\n"
    "ds_read_b64_tr_b16 v[196:197], v0 offset:17408\n"
    "ds_read_b64_tr_b16 v[198:199], v1 offset:17408\n"
    "v_mfma_f32_16x16x32_bf16 v[208:211], v[72:75], v[168:171], v[208:211]\n"
    "v_mul_f32 v204, v204, v252\n"
    "v_mul_f32 v205, v205, v252\n"
    "v_mfma_f32_16x16x32_bf16 v[212:215], v[96:99], v[168:171], v[212:215]\n"
    "ds_read_b32 v253, v238 offset:320\n"
    "v_mul_f32 v206, v206, v252\n"
    "v_mfma_f32_16x16x32_bf16 v[208:211], v[76:79], v[172:175], v[208:211]\n"
    "v_mul_f32 v207, v207, v252\n"
    "v_cvt_pk_bf16_f32 v200, v200, v201\n"
    "v_mfma_f32_16x16x32_bf16 v[212:215], v[100:103], v[172:175], v[212:215]\n"
    "v_cvt_pk_bf16_f32 v201, v202, v203\n"
    "v_cvt_pk_bf16_f32 v202, v204, v205\n"
    "v_cvt_pk_bf16_f32 v203, v206, v207\n"
    "buffer_store_dwordx4 v[200:203], v248, s[80:83], 0 offen offset:128 sc1\n"
    ".Lg33j:\n"
    "s_cmp_lg_u32 s58, 0\n"
    "s_cbranch_scc1 .Lg_t12\n"
    "s_waitcnt lgkmcnt(1)\n"
    "v_mfma_f32_16x16x32_bf16 v[200:203], v[8:11], v[176:179], 0\n"
    "v_mul_f32 v208, v208, v252\n"
    "v_mul_f32 v209, v209, v252\n"
    "v_mfma_f32_16x16x32_bf16 v[204:207], v[32:35], v[176:179], 0\n"
    "ds_read_b32 v228, v224 offset:832\n"
    "v_mul_f32 v210, v210, v252\n"
    "v_mfma_f32_16x16x32_bf16 v[200:203], v[12:15], v[180:183], v[200:203]\n"
    "v_mul_f32 v211, v211, v252\n"
    "v_mul_f32 v212, v212, v252\n"
    "v_mfma_f32_16x16x32_bf16 v[204:207], v[36:39], v[180:183], v[204:207]\n"
    "v_mul_f32 v213, v213, v252\n"
    "v_mul_f32 v214, v214, v252\n"
    "v_mfma_f32_16x16x32_bf16 v[200:203], v[16:19], v[184:187], v[200:203]\n"
    "v_mul_f32 v215, v215, v252\n"
    "v_cvt_pk_bf16_f32 v208, v208, v209\n"
    "v_mfma_f32_16x16x32_bf16 v[204:207], v[40:43], v[184:187], v[204:207]\n"
    "v_cvt_pk_bf16_f32 v209, v210, v211\n"
    "v_cvt_pk_bf16_f32 v210, v212, v213\n"
    "v_mfma_f32_16x16x32_bf16 v[200:203], v[20:23], v[188:191], v[200:203]\n"
    "v_cvt_pk_bf16_f32 v211, v214, v215\n"
    "v_mfma_f32_16x16x32_bf16 v[204:207], v[44:47], v[188:191], v[204:207]\n"
    "buffer_store_dwordx4 v[208:211], v248, s[80:83], 0 offen offset:320 sc1\n"
    "v_mfma_f32_16x16x32_bf16 v[200:203], v[24:27], v[192:195], v[200:203]\n"
    "v_mfma_f32_16x16x32_bf16 v[204:207], v[48:51], v[192:195], v[204:207]\n"
    "v_add_u32 v248, v255, v248\n"
    "v_mfma_f32_16x16x32_bf16 v[200:203], v[28:31], v[196:199], v[200:203]\n"
    "s_waitcnt lgkmcnt(0)\n"
    "v_cmp_gt_u32 vcc, 4, v228\n"
    "v_mfma_f32_16x16x32_bf16 v[204:207], v[52:55], v[196:199], v[204:207]\n"
    "s_cbranch_vccnz .Lg34\n"
    "v_mfma_f32_16x16x32_bf16 v[208:211], v[56:59], v[176:179], 0\n"
    "ds_read_b64_tr_b16 v[152:153], v0 offset:0\n"
    "ds_read_b64_tr_b16 v[154:155], v1 offset:0\n"
    "v_mfma_f32_16x16x32_bf16 v[212:215], v[80:83], v[176:179], 0\n"
    "ds_read_b64_tr_b16 v[156:157], v0 offset:1024\n"
    "ds_read_b64_tr_b16 v[158:159], v1 offset:1024\n"
    "v_mfma_f32_16x16x32_bf16 v[208:211], v[60:63], v[180:183], v[208:211]\n"
    "ds_read_b64_tr_b16 v[160:161], v0 offset:2048\n"
    "ds_read_b64_tr_b16 v[162:163], v1 offset:2048\n"
    "v_mfma_f32_16x16x32_bf16 v[212:215], v[84:87], v[180:183], v[212:215]\n"
    "ds_read_b64_tr_b16 v[164:165], v0 offset:3072\n"
    "ds_read_b64_tr_b16 v[166:167], v1 offset:3072\n"
    "v_mfma_f32_16x16x32_bf16 v[208:211], v[64:67], v[184:187], v[208:211]\n"
    "v_mul_f32 v200, v200, v253\n"
    "v_mul_f32 v201, v201, v253\n"
    "v_mfma_f32_16x16x32_bf16 v[212:215], v[88:91], v[184:187], v[212:215]\n"
    "ds_read_b64_tr_b16 v[168:169], v0 offset:4096\n"
    "ds_read_b64_tr_b16 v[170:171], v1 offset:4096\n"
    "v_mfma_f32_16x16x32_bf16 v[208:211], v[68:71], v[188:191], v[208:211]\n"
    "v_mul_f32 v202, v202, v253\n"
    "v_mul_f32 v203, v203, v253\n"
    "v_mfma_f32_16x16x32_bf16 v[212:215], v[92:95], v[188:191], v[212:215]\n"
    "ds_read_b64_tr_b16 v[172:173], v0 offset:5120\n"
    "ds_read_b64_tr_b16 v[174:175], v1 offset:5120\n"
    "v_mfma_f32_16x16x32_bf16 v[208:211], v[72:75], v[192:195], v[208:211]\n"
    "v_mul_f32 v204, v204, v253\n"
    "v_mul_f32 v205, v205, v253\n"
    "v_mfma_f32_16x16x32_bf16 v[212:215], v[96:99], v[192:195], v[212:215]\n"
    "ds_read_b32 v252, v238 offset:192\n"
    "v_mul_f32 v206, v206, v253\n"
    "v_mfma_f32_16x16x32_bf16 v[208:211], v[76:79], v[196:199], v[208:211]\n"
    "v_mul_f32 v207, v207, v253\n"
    "v_cvt_pk_bf16_f32 v200, v200, v201\n"
    "v_mfma_f32_16x16x32_bf16 v[212:215], v[100:103], v[196:199], v[212:215]\n"
    "v_cvt_pk_bf16_f32 v201, v202, v203\n"
    "v_cvt_pk_bf16_f32 v202, v204, v205\n"
    "v_cvt_pk_bf16_f32 v203, v206, v207\n"
    "buffer_store_dwordx4 v[200:203], v249, s[80:83], 0 offen offset:128 sc1\n"
    ".Lg34j:\n"
    "s_waitcnt lgkmcnt(1)\n"
    "s_nop 0\n"
    "v_mfma_f32_16x16x32_bf16 v[200:203], v[8:11], v[152:155], 0\n"
    "v_mul_f32 v208, v208, v253\n"
    "v_mul_f32 v209, v209, v253\n"
    "v_mfma_f32_16x16x32_bf16 v[204:207], v[32:35], v[152:155], 0\n"
    "v_mul_f32 v210, v210, v253\n"
    "v_mul_f32 v211, v211, v253\n"
    "v_mfma_f32_16x16x32_bf16 v[200:203], v[12:15], v[156:159], v[200:203]\n"
    "v_mul_f32 v212, v212, v253\n"
    "v_mul_f32 v213, v213, v253\n"
    "v_mfma_f32_16x16x32_bf16 v[204:207], v[36:39], v[156:159], v[204:207]\n"
    "v_mul_f32 v214, v214, v253\n"
    "v_mul_f32 v215, v215, v253\n"
    "v_mfma_f32_16x16x32_bf16 v[200:203], v[16:19], v[160:163], v[200:203]\n"
    "v_cvt_pk_bf16_f32 v208, v208, v209\n"
    "v_cvt_pk_bf16_f32 v209, v210, v211\n"
    "v_mfma_f32_16x16x32_bf16 v[204:207], v[40:43], v[160:163], v[204:207]\n"
    "v_cvt_pk_bf16_f32 v210, v212, v213\n"
    "v_cvt_pk_bf16_f32 v211, v214, v215\n"
    "v_mfma_f32_16x16x32_bf16 v[200:203], v[20:23], v[164:167], v[200:203]\n"
    "buffer_store_dwordx4 v[208:211], v249, s[80:83], 0 offen offset:320 sc1\n"
    "v_mfma_f32_16x16x32_bf16 v[204:207], v[44:47], v[164:167], v[204:207]\n"
    "v_mfma_f32_16x16x32_bf16 v[200:203], v[24:27], v[168:171], v[200:203]\n"
    "v_mfma_f32_16x16x32_bf16 v[204:207], v[48:51], v[168:171], v[204:207]\n"
    "v_mfma_f32_16x16x32_bf16 v[200:203], v[28:31], v[172:175], v[200:203]\n"
    "v_mfma_f32_16x16x32_bf16 v[204:207], v[52:55], v[172:175], v[204:207]\n"
    "v_mfma_f32_16x16x32_bf16 v[208:211], v[56:59], v[152:155], 0\n"
    "v_mfma_f32_16x16x32_bf16 v[212:215], v[80:83], v[152:155], 0\n"
    "v_mfma_f32_16x16x32_bf16 v[208:211], v[60:63], v[156:159], v[208:211]\n"
    "v_mfma_f32_16x16x32_bf16 v[212:215], v[84:87], v[156:159], v[212:215]\n"
    "v_mfma_f32_16x16x32_bf16 v[208:211], v[64:67], v[160:163], v[208:211]\n"
    "v_mfma_f32_16x16x32_bf16 v[212:215], v[88:91], v[160:163], v[212:215]\n"
    "s_waitcnt lgkmcnt(0)\n"
    "v_mul_f32 v200, v200, v252\n"
    "v_mfma_f32_16x16x32_bf16 v[208:211], v[68:71], v[164:167], v[208:211]\n"
    "v_mul_f32 v201, v201, v252\n"
    "v_mul_f32 v202, v202, v252\n"
    "v_mfma_f32_16x16x32_bf16 v[212:215], v[92:95], v[164:167], v[212:215]\n"
    "v_mul_f32 v203, v203, v252\n"
    "v_mul_f32 v204, v204, v252\n"
    "v_mfma_f32_16x16x32_bf16 v[208:211], v[72:75], v[168:171], v[208:211]\n"
    "v_mul_f32 v205, v205, v252\n"
    "v_mul_f32 v206, v206, v252\n"
    "v_mfma_f32_16x16x32_bf16 v[212:215], v[96:99], v[168:171], v[212:215]\n"
    "v_mul_f32 v207, v207, v252\n"
    "v_cvt_pk_bf16_f32 v200, v200, v201\n"
    "v_mfma_f32_16x16x32_bf16 v[208:211], v[76:79], v[172:175], v[208:211]\n"
    "v_cvt_pk_bf16_f32 v201, v202, v203\n"
    "v_cvt_pk_bf16_f32 v202, v204, v205\n"
    "v_mfma_f32_16x16x32_bf16 v[212:215], v[100:103], v[172:175], v[212:215]\n"
    "v_cvt_pk_bf16_f32 v203, v206, v207\n"
    "buffer_store_dwordx4 v[200:203], v248, s[80:83], 0 offen offset:128 sc1\n"
    "s_nop 2\n"
    "v_mul_f32 v208, v208, v252\n"
    "v_mul_f32 v209, v209, v252\n"
    "v_mul_f32 v210, v210, v252\n"
    "v_mul_f32 v211, v211, v252\n"
    "v_mul_f32 v212, v212, v252\n"
    "v_mul_f32 v213, v213, v252\n"
    "v_mul_f32 v214, v214, v252\n"
    "v_mul_f32 v215, v215, v252\n"
    "v_cvt_pk_bf16_f32 v208, v208, v209\n"
    "v_cvt_pk_bf16_f32 v209, v210, v211\n"
    "v_cvt_pk_bf16_f32 v210, v212, v213\n"
    "v_cvt_pk_bf16_f32 v211, v214, v215\n"
    "buffer_store_dwordx4 v[208:211], v248, s[80:83], 0 offen offset:320 sc1\n"
    ".Lg_end:\n"
    "s_waitcnt lgkmcnt(0)\n"
    "s_endpgm\n"
    ".Lg1:\n"
    "s_sub_u32 s52, s52, 1\n"
    "s_cmp_lt_i32 s52, 0\n"
    "s_cbranch_scc1 .Lg1b\n"
    "ds_read_b32 v228, v224 offset:48\n"
    "s_waitcnt lgkmcnt(0)\n"
    "v_cmp_gt_u32 vcc, 1, v228\n"
    "s_cbranch_vccnz .Lg1\n"
    "s_branch .Lg1b\n"
    ".Lg_poll:\n"
    "global_load_dwordx4 v[72:75], v235, s[68:69] offset:1024 sc1\n"
    "global_load_dwordx4 v[96:99], v235, s[70:71] offset:1024 sc1\n"
    "global_load_dwordx4 v[76:79], v235, s[68:69] offset:2048 sc1\n"
    "global_load_dwordx4 v[100:103], v235, s[70:71] offset:2048 sc1\n"
    "global_load_dwordx4 v[104:107], v235, s[72:73] offset:-3072 sc1\n"
    "global_load_dwordx4 v[128:131], v235, s[74:75] offset:-3072 sc1\n"
    "global_load_dwordx4 v[108:111], v235, s[72:73] offset:-2048 sc1\n"
    "global_load_dwordx4 v[132:135], v235, s[74:75] offset:-2048 sc1\n"
    "global_load_dwordx4 v[112:115], v235, s[72:73] offset:-1024 sc1\n"
    "global_load_dwordx4 v[136:139], v235, s[74:75] offset:-1024 sc1\n"
    "global_load_dwordx4 v[116:119], v235, s[72:73] offset:0 sc1\n"
    "global_load_dwordx4 v[140:143], v235, s[74:75] offset:0 sc1\n"
    "global_load_dwordx4 v[120:123], v235, s[72:73] offset:1024 sc1\n"
    "global_load_dwordx4 v[144:147], v235, s[74:75] offset:1024 sc1\n"
    "global_load_dwordx4 v[124:127], v235, s[72:73] offset:2048 sc1\n"
    "global_load_dwordx4 v[148:151], v235, s[74:75] offset:2048 sc1\n"
    "v_mov_b32 v227, 1\n"
    "ds_write_b32 v225, v227 offset:256\n"
    ".Lg_pl2:\n"
    "s_mov_b64 s[76:77], vcc\n"
    "v_mov_b64 v[244:245], v[240:241]\n"
    "v_mov_b64 v[246:247], 0\n"
    "v_mov_b64 v[242:243], 0\n"
    "s_mov_b32 exec_hi, 15\n"
    "global_load_dwordx4 v[244:247], v236, s[40:41] sc1\n"
    "s_mov_b64 exec, -1\n"
    "s_bitcmp1_b64 s[76:77], 0\n"
    "s_cbranch_scc0 2\n"
    "global_load_dwordx4 v[8:11], v235, s[64:65] offset:-3072 sc1\n"
    "s_bitcmp1_b64 s[76:77], 6\n"
    "s_cbranch_scc0 2\n"
    "global_load_dwordx4 v[32:35], v235, s[66:67] offset:-3072 sc1\n"
    "s_bitcmp1_b64 s[76:77], 1\n"
    "s_cbranch_scc0 2\n"
    "global_load_dwordx4 v[12:15], v235, s[64:65] offset:-2048 sc1\n"
    "s_bitcmp1_b64 s[76:77], 7\n"
    "s_cbranch_scc0 2\n"
    "global_load_dwordx4 v[36:39], v235, s[66:67] offset:-2048 sc1\n"
    "s_bitcmp1_b64 s[76:77], 2\n"
    "s_cbranch_scc0 2\n"
    "global_load_dwordx4 v[16:19], v235, s[64:65] offset:-1024 sc1\n"
    "s_bitcmp1_b64 s[76:77], 8\n"
    "s_cbranch_scc0 2\n"
    "global_load_dwordx4 v[40:43], v235, s[66:67] offset:-1024 sc1\n"
    "s_bitcmp1_b64 s[76:77], 3\n"
    "s_cbranch_scc0 2\n"
    "global_load_dwordx4 v[20:23], v235, s[64:65] offset:0 sc1\n"
    "s_bitcmp1_b64 s[76:77], 9\n"
    "s_cbranch_scc0 2\n"
    "global_load_dwordx4 v[44:47], v235, s[66:67] offset:0 sc1\n"
    "s_bitcmp1_b64 s[76:77], 4\n"
    "s_cbranch_scc0 2\n"
    "global_load_dwordx4 v[24:27], v235, s[64:65] offset:1024 sc1\n"
    "s_bitcmp1_b64 s[76:77], 10\n"
    "s_cbranch_scc0 2\n"
    "global_load_dwordx4 v[48:51], v235, s[66:67] offset:1024 sc1\n"
    "s_bitcmp1_b64 s[76:77], 5\n"
    "s_cbranch_scc0 2\n"
    "global_load_dwordx4 v[28:31], v235, s[64:65] offset:2048 sc1\n"
    "s_bitcmp1_b64 s[76:77], 11\n"
    "s_cbranch_scc0 2\n"
    "global_load_dwordx4 v[52:55], v235, s[66:67] offset:2048 sc1\n"
    "s_bitcmp1_b64 s[76:77], 12\n"
    "s_cbranch_scc0 2\n"
    "global_load_dwordx4 v[56:59], v235, s[68:69] offset:-3072 sc1\n"
    "s_bitcmp1_b64 s[76:77], 18\n"
    "s_cbranch_scc0 2\n"
    "global_load_dwordx4 v[80:83], v235, s[70:71] offset:-3072 sc1\n"
    "s_bitcmp1_b64 s[76:77], 13\n"
    "s_cbranch_scc0 2\n"
    "global_load_dwordx4 v[60:63], v235, s[68:69] offset:-2048 sc1\n"
    "s_bitcmp1_b64 s[76:77], 19\n"
    "s_cbranch_scc0 2\n"
    "global_load_dwordx4 v[84:87], v235, s[70:71] offset:-2048 sc1\n"
    "s_bitcmp1_b64 s[76:77], 14\n"
    "s_cbranch_scc0 2\n"
    "global_load_dwordx4 v[64:67], v235, s[68:69] offset:-1024 sc1\n"
    "s_bitcmp1_b64 s[76:77], 20\n"
    "s_cbranch_scc0 2\n"
    "global_load_dwordx4 v[88:91], v235, s[70:71] offset:-1024 sc1\n"
    "s_bitcmp1_b64 s[76:77], 15\n"
    "s_cbranch_scc0 2\n"
    "global_load_dwordx4 v[68:71], v235, s[68:69] offset:0 sc1\n"
    "s_bitcmp1_b64 s[76:77], 21\n"
    "s_cbranch_scc0 2\n"
    "global_load_dwordx4 v[92:95], v235, s[70:71] offset:0 sc1\n"
    "s_bitcmp1_b64 s[76:77], 16\n"
    "s_cbranch_scc0 2\n"
    "global_load_dwordx4 v[72:75], v235, s[68:69] offset:1024 sc1\n"
    "s_bitcmp1_b64 s[76:77], 22\n"
    "s_cbranch_scc0 2\n"
    "global_load_dwordx4 v[96:99], v235, s[70:71] offset:1024 sc1\n"
    "s_bitcmp1_b64 s[76:77], 17\n"
    "s_cbranch_scc0 2\n"
    "global_load_dwordx4 v[76:79], v235, s[68:69] offset:2048 sc1\n"
    "s_bitcmp1_b64 s[76:77], 23\n"
    "s_cbranch_scc0 2\n"
    "global_load_dwordx4 v[100:103], v235, s[70:71] offset:2048 sc1\n"
    "s_bitcmp1_b64 s[76:77], 24\n"
    "s_cbranch_scc0 2\n"
    "global_load_dwordx4 v[104:107], v235, s[72:73] offset:-3072 sc1\n"
    "s_bitcmp1_b64 s[76:77], 30\n"
    "s_cbranch_scc0 2\n"
    "global_load_dwordx4 v[128:131], v235, s[74:75] offset:-3072 sc1\n"
    "s_bitcmp1_b64 s[76:77], 25\n"
    "s_cbranch_scc0 2\n"
    "global_load_dwordx4 v[108:111], v235, s[72:73] offset:-2048 sc1\n"
    "s_bitcmp1_b64 s[76:77], 31\n"
    "s_cbranch_scc0 2\n"
    "global_load_dwordx4 v[132:135], v235, s[74:75] offset:-2048 sc1\n"
    "s_bitcmp1_b64 s[76:77], 26\n"
    "s_cbranch_scc0 2\n"
    "global_load_dwordx4 v[112:115], v235, s[72:73] offset:-1024 sc1\n"
    "s_bitcmp1_b64 s[76:77], 32\n"
    "s_cbranch_scc0 2\n"
    "global_load_dwordx4 v[136:139], v235, s[74:75] offset:-1024 sc1\n"
    "s_bitcmp1_b64 s[76:77], 27\n"
    "s_cbranch_scc0 2\n"
    "global_load_dwordx4 v[116:119], v235, s[72:73] offset:0 sc1\n"
    "s_bitcmp1_b64 s[76:77], 33\n"
    "s_cbranch_scc0 2\n"
    "global_load_dwordx4 v[140:143], v235, s[74:75] offset:0 sc1\n"
    "s_bitcmp1_b64 s[76:77], 28\n"
    "s_cbranch_scc0 2\n"
    "global_load_dwordx4 v[120:123], v235, s[72:73] offset:1024 sc1\n"
    "s_bitcmp1_b64 s[76:77], 34\n"
    "s_cbranch_scc0 2\n"
    "global_load_dwordx4 v[144:147], v235, s[74:75] offset:1024 sc1\n"
    "s_bitcmp1_b64 s[76:77], 29\n"
    "s_cbranch_scc0 2\n"
    "global_load_dwordx4 v[124:127], v235, s[72:73] offset:2048 sc1\n"
    "s_bitcmp1_b64 s[76:77], 35\n"
    "s_cbranch_scc0 2\n"
    "global_load_dwordx4 v[148:151], v235, s[74:75] offset:2048 sc1\n"
    "s_sub_u32 s51, s51, 1\n"
    "s_waitcnt vmcnt(0)\n"
    "v_cmp_ne_u64 vcc, v[240:241], v[244:245]\n"
    "s_cmp_gt_i32 s51, 0\n"
    "s_cbranch_scc0 .Lg_tb\n"
    "s_cbranch_vccnz .Lg_pl2\n"
    "s_branch .Lg_tb\n"
    ".Lg_fail:\n"
    "s_sub_u32 s51, s51, 1\n"
    "s_sleep 2\n"
    "s_cmp_gt_i32 s51, 0\n"
    "s_cbranch_scc1 .Lg_retry\n"
    "s_branch .Lg_ok\n"
    ".Lg2:\n"
    "s_sub_u32 s52, s52, 1\n"
    "s_cmp_lt_i32 s52, 0\n"
    "s_cbranch_scc1 .Lg2b\n"
    "ds_read_b32 v228, v224 offset:0\n"
    "s_waitcnt lgkmcnt(0)\n"
    "v_cmp_gt_u32 vcc, 1, v228\n"
    "s_cbranch_vccnz .Lg2\n"
    "s_branch .Lg2b\n"
    ".Lg_tb:\n"
    "ds_read_b32 v228, v224 offset:0\n"
    "s_waitcnt lgkmcnt(0)\n"
    "v_cmp_gt_u32 vcc, 1, v228\n"
    "s_cbranch_vccnz .Lg2\n"
    ".Lg2b:\n"
    "ds_read_b128 v[152:155], v2 offset:26688\n"
    "ds_read_b128 v[156:159], v2 offset:26752\n"
    "ds_read_b128 v[160:163], v2 offset:26816\n"
    "ds_read_b128 v[164:167], v2 offset:26880\n"
    "ds_read_b128 v[168:171], v2 offset:26944\n"
    "ds_read_b128 v[172:175], v2 offset:27008\n"
    "v_add3_u32 v242, v242, v8, v11\n"
    "v_sub_u32 v8, v8, v240\n"
    "v_add3_u32 v242, v242, v32, v35\n"
    "v_sub_u32 v32, v32, v240\n"
    "v_add3_u32 v242, v242, v12, v15\n"
    "v_sub_u32 v12, v12, v240\n"
    "s_waitcnt lgkmcnt(5)\n"
    "v_mfma_f32_16x16x32_bf16 v[200:203], v[8:11], v[152:155], 0\n"
    "v_add3_u32 v242, v242, v36, v39\n"
    "v_sub_u32 v36, v36, v240\n"
    "v_mfma_f32_16x16x32_bf16 v[204:207], v[32:35], v[152:155], 0\n"
    "v_add3_u32 v242, v242, v16, v19\n"
    "v_sub_u32 v16, v16, v240\n"
    "s_waitcnt lgkmcnt(4)\n"
    "v_mfma_f32_16x16x32_bf16 v[200:203], v[12:15], v[156:159], v[200:203]\n"
    "v_add3_u32 v242, v242, v40, v43\n"
    "v_sub_u32 v40, v40, v240\n"
    "v_mfma_f32_16x16x32_bf16 v[204:207], v[36:39], v[156:159], v[204:207]\n"
    "v_add3_u32 v242, v242, v20, v23\n"
    "v_sub_u32 v20, v20, v240\n"
    "s_waitcnt lgkmcnt(3)\n"
    "v_mfma_f32_16x16x32_bf16 v[200:203], v[16:19], v[160:163], v[200:203]\n"
    "v_add3_u32 v242, v242, v44, v47\n"
    "v_sub_u32 v44, v44, v240\n"
    "v_mfma_f32_16x16x32_bf16 v[204:207], v[40:43], v[160:163], v[204:207]\n"
    "v_add3_u32 v242, v242, v24, v27\n"
    "v_sub_u32 v24, v24, v240\n"
    "s_waitcnt lgkmcnt(2)\n"
    "v_mfma_f32_16x16x32_bf16 v[200:203], v[20:23], v[164:167], v[200:203]\n"
    "v_add3_u32 v242, v242, v48, v51\n"
    "v_sub_u32 v48, v48, v240\n"
    "v_mfma_f32_16x16x32_bf16 v[204:207], v[44:47], v[164:167], v[204:207]\n"
    "v_add3_u32 v242, v242, v28, v31\n"
    "v_sub_u32 v28, v28, v240\n"
    "s_waitcnt lgkmcnt(1)\n"
    "v_mfma_f32_16x16x32_bf16 v[200:203], v[24:27], v[168:171], v[200:203]\n"
    "v_add3_u32 v242, v242, v52, v55\n"
    "v_sub_u32 v52, v52, v240\n"
    "v_mfma_f32_16x16x32_bf16 v[204:207], v[48:51], v[168:171], v[204:207]\n"
    "s_waitcnt lgkmcnt(0)\n"
    "v_mfma_f32_16x16x32_bf16 v[200:203], v[28:31], v[172:175], v[200:203]\n"
    "v_mfma_f32_16x16x32_bf16 v[204:207], v[52:55], v[172:175], v[204:207]\n"
    "v_add3_u32 v242, v242, v56, v59\n"
    "v_sub_u32 v56, v56, v240\n"
    "v_add3_u32 v242, v242, v80, v83\n"
    "v_sub_u32 v80, v80, v240\n"
    "v_add3_u32 v242, v242, v60, v63\n"
    "v_sub_u32 v60, v60, v240\n"
    "v_mfma_f32_16x16x32_bf16 v[208:211], v[56:59], v[152:155], 0\n"
    "v_cvt_pk_bf16_f32 v200, v200, v201\n"
    "v_add3_u32 v242, v242, v84, v87\n"
    "v_sub_u32 v84, v84, v240\n"
    "v_mfma_f32_16x16x32_bf16 v[212:215], v[80:83], v[152:155], 0\n"
    "v_cvt_pk_bf16_f32 v201, v202, v203\n"
    "v_add3_u32 v242, v242, v64, v67\n"
    "v_sub_u32 v64, v64, v240\n"
    "v_mfma_f32_16x16x32_bf16 v[208:211], v[60:63], v[156:159], v[208:211]\n"
    "v_cvt_pk_bf16_f32 v202, v204, v205\n"
    "v_add3_u32 v242, v242, v88, v91\n"
    "v_sub_u32 v88, v88, v240\n"
    "v_mfma_f32_16x16x32_bf16 v[212:215], v[84:87], v[156:159], v[212:215]\n"
    "v_cvt_pk_bf16_f32 v203, v206, v207\n"
    "v_add3_u32 v242, v242, v68, v71\n"
    "v_sub_u32 v68, v68, v240\n"
    "v_mfma_f32_16x16x32_bf16 v[208:211], v[64:67], v[160:163], v[208:211]\n"
    "v_add3_u32 v242, v242, v92, v95\n"
    "v_sub_u32 v92, v92, v240\n"
    "v_mfma_f32_16x16x32_bf16 v[212:215], v[88:91], v[160:163], v[212:215]\n"
    "v_add3_u32 v242, v242, v72, v75\n"
    "v_sub_u32 v72, v72, v240\n"
    "v_mfma_f32_16x16x32_bf16 v[208:211], v[68:71], v[164:167], v[208:211]\n"
    "v_add3_u32 v242, v242, v96, v99\n"
    "v_sub_u32 v96, v96, v240\n"
    "v_mfma_f32_16x16x32_bf16 v[212:215], v[92:95], v[164:167], v[212:215]\n"
    "v_add3_u32 v242, v242, v76, v79\n"
    "v_sub_u32 v76, v76, v240\n"
    "v_mfma_f32_16x16x32_bf16 v[208:211], v[72:75], v[168:171], v[208:211]\n"
    "v_add3_u32 v242, v242, v100, v103\n"
    "v_sub_u32 v100, v100, v240\n"
    "v_mfma_f32_16x16x32_bf16 v[212:215], v[96:99], v[168:171], v[212:215]\n"
    "v_mfma_f32_16x16x32_bf16 v[208:211], v[76:79], v[172:175], v[208:211]\n"
    "ds_write_b128 v7, v[200:203] offset:0\n"
    "v_mfma_f32_16x16x32_bf16 v[212:215], v[100:103], v[172:175], v[212:215]\n"
    "v_add3_u32 v242, v242, v104, v107\n"
    "v_sub_u32 v104, v104, v240\n"
    "v_add3_u32 v242, v242, v128, v131\n"
    "v_sub_u32 v128, v128, v240\n"
    "v_add3_u32 v242, v242, v108, v111\n"
    "v_sub_u32 v108, v108, v240\n"
    "v_mfma_f32_16x16x32_bf16 v[216:219], v[104:107], v[152:155], 0\n"
    "ds_read_b128 v[176:179], v3 offset:26688\n"
    "v_add3_u32 v242, v242, v132, v135\n"
    "v_sub_u32 v132, v132, v240\n"
    "v_mfma_f32_16x16x32_bf16 v[220:223], v[128:131], v[152:155], 0\n"
    "ds_read_b128 v[180:183], v3 offset:26752\n"
    "v_add3_u32 v242, v242, v112, v115\n"
    "v_sub_u32 v112, v112, v240\n"
    "v_mfma_f32_16x16x32_bf16 v[216:219], v[108:111], v[156:159], v[216:219]\n"
    "ds_read_b128 v[184:187], v3 offset:26816\n"
    "v_add3_u32 v242, v242, v136, v139\n"
    "v_sub_u32 v136, v136, v240\n"
    "v_mfma_f32_16x16x32_bf16 v[220:223], v[132:135], v[156:159], v[220:223]\n"
    "ds_read_b128 v[188:191], v3 offset:26880\n"
    "v_add3_u32 v242, v242, v116, v119\n"
    "v_sub_u32 v116, v116, v240\n"
    "v_mfma_f32_16x16x32_bf16 v[216:219], v[112:115], v[160:163], v[216:219]\n"
    "ds_read_b128 v[192:195], v3 offset:26944\n"
    "v_add3_u32 v242, v242, v140, v143\n"
    "v_sub_u32 v140, v140, v240\n"
    "v_mfma_f32_16x16x32_bf16 v[220:223], v[136:139], v[160:163], v[220:223]\n"
    "ds_read_b128 v[196:199], v3 offset:27008\n"
    "v_add3_u32 v242, v242, v120, v123\n"
    "v_sub_u32 v120, v120, v240\n"
    "v_mfma_f32_16x16x32_bf16 v[216:219], v[116:119], v[164:167], v[216:219]\n"
    "v_cvt_pk_bf16_f32 v208, v208, v209\n"
    "v_add3_u32 v242, v242, v144, v147\n"
    "v_sub_u32 v144, v144, v240\n"
    "v_mfma_f32_16x16x32_bf16 v[220:223], v[140:143], v[164:167], v[220:223]\n"
    "v_cvt_pk_bf16_f32 v209, v210, v211\n"
    "v_add3_u32 v242, v242, v124, v127\n"
    "v_sub_u32 v124, v124, v240\n"
    "v_mfma_f32_16x16x32_bf16 v[216:219], v[120:123], v[168:171], v[216:219]\n"
    "v_cvt_pk_bf16_f32 v210, v212, v213\n"
    "v_add3_u32 v242, v242, v148, v151\n"
    "v_sub_u32 v148, v148, v240\n"
    "v_mfma_f32_16x16x32_bf16 v[220:223], v[144:147], v[168:171], v[220:223]\n"
    "v_cvt_pk_bf16_f32 v211, v214, v215\n"
    "v_mfma_f32_16x16x32_bf16 v[216:219], v[124:127], v[172:175], v[216:219]\n"
    "ds_write_b128 v7, v[208:211] offset:1024\n"
    "v_mfma_f32_16x16x32_bf16 v[220:223], v[148:151], v[172:175], v[220:223]\n"
    "v_cmp_ne_u64 s[76:77], v[240:241], v[244:245]\n"
    "v_sub_u32 v242, v242, v246\n"
    "v_mov_b32 v243, v242\n"
    "s_nop 1\n"
    "v_permlane32_swap_b32 v242, v243\n"
    "v_add_u32 v242, v242, v243\n"
    "v_cvt_pk_bf16_f32 v216, v216, v217\n"
    "v_cvt_pk_bf16_f32 v217, v218, v219\n"
    "v_add_u32_dpp v242, v242, v242 row_ror:8 row_mask:0xf bank_mask:0xf\n"
    "v_cvt_pk_bf16_f32 v218, v220, v221\n"
    "v_cvt_pk_bf16_f32 v219, v222, v223\n"
    "v_add_u32_dpp v242, v242, v242 row_ror:4 row_mask:0xf bank_mask:0xf\n"
    "ds_write_b128 v7, v[216:219] offset:2048\n"
    "s_nop 0\n"
    "v_add_u32_dpp v242, v242, v242 row_ror:2 row_mask:0xf bank_mask:0xf\n"
    "s_nop 1\n"
    "v_add_u32_dpp v242, v242, v242 row_ror:1 row_mask:0xf bank_mask:0xf\n"
    "v_mov_b32 v243, v242\n"
    "s_nop 1\n"
    "v_permlane16_swap_b32 v242, v243\n"
    "v_add_u32 v242, v242, v243\n"
    "v_cmp_ne_u32 vcc, 0, v242\n"
    "v_mov_b32 v227, 1\n"
    "s_cmp_lg_u64 vcc, 0\n"
    "s_cbranch_scc1 .Lg_fail\n"
    "s_cmp_lg_u64 s[76:77], 0\n"
    "s_cbranch_scc1 .Lg_fail\n"
    ".Lg_okb:\n"
    "ds_write_b32 v225, v227 offset:16\n"
    "v_mov_b32 v242, 0\n"
    "s_waitcnt lgkmcnt(3)\n"
    "s_branch .Lg_t1\n"
    ".Lg3:\n"
    "s_sub_u32 s52, s52, 1\n"
    "s_cmp_lt_i32 s52, 0\n"
    "s_cbranch_scc1 .Lg3b\n"
    "ds_read_b32 v228, v224 offset:0\n"
    "s_waitcnt lgkmcnt(0)\n"
    "v_cmp_gt_u32 vcc, 1, v228\n"
    "s_cbranch_vccnz .Lg3\n"
    "s_branch .Lg3b\n"
    ".Lg4:\n"
    "s_sub_u32 s52, s52, 1\n"
    "s_cmp_lt_i32 s52, 0\n"
    "s_cbranch_scc1 .Lg4b\n"
    "ds_read_b32 v229, v224 offset:0\n"
    "s_waitcnt lgkmcnt(0)\n"
    "v_cmp_gt_u32 vcc, 2, v229\n"
    "s_cbranch_vccnz .Lg4\n"
    "s_branch .Lg4b\n"
    ".Lg5:\n"
    "s_sub_u32 s52, s52, 1\n"
    "s_cmp_lt_i32 s52, 0\n"
    "s_cbranch_scc1 .Lg5b\n"
    "ds_read_b32 v229, v224 offset:0\n"
    "s_waitcnt lgkmcnt(0)\n"
    "v_cmp_gt_u32 vcc, 3, v229\n"
    "s_cbranch_vccnz .Lg5\n"
    "s_branch .Lg5b\n"
    ".Lg6:\n"
    "s_sub_u32 s52, s52, 1\n"
    "s_cmp_lt_i32 s52, 0\n"
    "s_cbranch_scc1 .Lg6b\n"
    "ds_read_b32 v228, v224 offset:192\n"
    "s_waitcnt lgkmcnt(0)\n"
    "v_cmp_gt_u32 vcc, 1, v228\n"
    "s_cbranch_vccnz .Lg6\n"
    "s_branch .Lg6b\n"
    ".Lg7:\n"
    "s_sub_u32 s52, s52, 1\n"
    "s_cmp_lt_i32 s52, 0\n"
    "s_cbranch_scc1 .Lg7b\n"
    "ds_read_b32 v228, v224 offset:208\n"
    "s_waitcnt lgkmcnt(0)\n"
    "v_cmp_gt_u32 vcc, 1, v228\n"
    "s_cbranch_vccnz .Lg7\n"
    "s_branch .Lg7b\n"
    ".Lg8:\n"
    "s_sub_u32 s52, s52, 1\n"
    "s_cmp_lt_i32 s52, 0\n"
    "s_cbranch_scc1 .Lg8b\n"
    "ds_read_b32 v239, v224 offset:64\n"
    "s_waitcnt lgkmcnt(0)\n"
    "v_cmp_gt_u32 vcc, 1, v239\n"
    "s_cbranch_vccnz .Lg8\n"
    "s_branch .Lg8b\n"
    ".Lg9:\n"
    "s_sub_u32 s52, s52, 1\n"
    "s_cmp_lt_i32 s52, 0\n"
    "s_cbranch_scc1 .Lg9b\n"
    "ds_read_b32 v229, v224 offset:0\n"
    "s_waitcnt lgkmcnt(0)\n"
    "v_cmp_gt_u32 vcc, 6, v229\n"
    "s_cbranch_vccnz .Lg9\n"
    "s_branch .Lg9b\n"
    ".Lg10:\n"
    "s_sub_u32 s52, s52, 1\n"
    "s_cmp_lt_i32 s52, 0\n"
    "s_cbranch_scc1 .Lg10b\n"
    "ds_read_b32 v228, v224 offset:224\n"
    "s_waitcnt lgkmcnt(0)\n"
    "v_cmp_gt_u32 vcc, 1, v228\n"
    "s_cbranch_vccnz .Lg10\n"
    "s_branch .Lg10b\n"
    ".Lg11:\n"
    "s_sub_u32 s52, s52, 1\n"
    "s_cmp_lt_i32 s52, 0\n"
    "s_cbranch_scc1 .Lg11b\n"
    "ds_read_b32 v239, v224 offset:80\n"
    "s_waitcnt lgkmcnt(0)\n"
    "v_cmp_gt_u32 vcc, 1, v239\n"
    "s_cbranch_vccnz .Lg11\n"
    "s_branch .Lg11b\n"
    ".Lg12:\n"
    "s_sub_u32 s52, s52, 1\n"
    "s_cmp_lt_i32 s52, 0\n"
    "s_cbranch_scc1 .Lg12b\n"
    "ds_read_b32 v228, v224 offset:240\n"
    "s_waitcnt lgkmcnt(0)\n"
    "v_cmp_gt_u32 vcc, 1, v228\n"
    "s_cbranch_vccnz .Lg12\n"
    "s_branch .Lg12b\n"
    ".Lg13:\n"
    "s_sub_u32 s52, s52, 1\n"
    "s_cmp_lt_i32 s52, 0\n"
    "s_cbranch_scc1 .Lg13b\n"
    "ds_read_b32 v239, v224 offset:96\n"
    "s_waitcnt lgkmcnt(0)\n"
    "v_cmp_gt_u32 vcc, 1, v239\n"
    "s_cbranch_vccnz .Lg13\n"
    "s_branch .Lg13b\n"
    ".Lg14:\n"
    "s_sub_u32 s52, s52, 1\n"
    "s_cmp_lt_i32 s52, 0\n"
    "s_cbranch_scc1 .Lg14b\n"
    "ds_read_b32 v229, v224 offset:0\n"
    "s_waitcnt lgkmcnt(0)\n"
    "v_cmp_gt_u32 vcc, 7, v229\n"
    "s_cbranch_vccnz .Lg14\n"
    "s_branch .Lg14b\n"
    ".Lg15:\n"
    "s_sub_u32 s52, s52, 1\n"
    "s_cmp_lt_i32 s52, 0\n"
    "s_cbranch_scc1 .Lg15b\n"
    "ds_read_b32 v228, v224 offset:192\n"
    "s_waitcnt lgkmcnt(0)\n"
    "v_cmp_gt_u32 vcc, 2, v228\n"
    "s_cbranch_vccnz .Lg15\n"
    "s_branch .Lg15b\n"
    ".Lg16:\n"
    "s_sub_u32 s52, s52, 1\n"
    "s_cmp_lt_i32 s52, 0\n"
    "s_cbranch_scc1 .Lg16b\n"
    "ds_read_b32 v239, v224 offset:112\n"
    "s_waitcnt lgkmcnt(0)\n"
    "v_cmp_gt_u32 vcc, 1, v239\n"
    "s_cbranch_vccnz .Lg16\n"
    "s_branch .Lg16b\n"
    ".Lg17:\n"
    "s_sub_u32 s52, s52, 1\n"
    "s_cmp_lt_i32 s52, 0\n"
    "s_cbranch_scc1 .Lg17b\n"
    "ds_read_b32 v228, v224 offset:208\n"
    "s_waitcnt lgkmcnt(0)\n"
    "v_cmp_gt_u32 vcc, 2, v228\n"
    "s_cbranch_vccnz .Lg17\n"
    "s_branch .Lg17b\n"
    ".Lg18:\n"
    "s_sub_u32 s52, s52, 1\n"
    "s_cmp_lt_i32 s52, 0\n"
    "s_cbranch_scc1 .Lg18b\n"
    "ds_read_b32 v239, v224 offset:64\n"
    "s_waitcnt lgkmcnt(0)\n"
    "v_cmp_gt_u32 vcc, 2, v239\n"
    "s_cbranch_vccnz .Lg18\n"
    "s_branch .Lg18b\n"
    ".Lg19:\n"
    "s_sub_u32 s52, s52, 1\n"
    "s_cmp_lt_i32 s52, 0\n"
    "s_cbranch_scc1 .Lg19b\n"
    "ds_read_b32 v229, v224 offset:0\n"
    "s_waitcnt lgkmcnt(0)\n"
    "v_cmp_gt_u32 vcc, 8, v229\n"
    "s_cbranch_vccnz .Lg19\n"
    "s_branch .Lg19b\n"
    ".Lg20:\n"
    "s_sub_u32 s52, s52, 1\n"
    "s_cmp_lt_i32 s52, 0\n"
    "s_cbranch_scc1 .Lg20b\n"
    "ds_read_b32 v228, v224 offset:224\n"
    "s_waitcnt lgkmcnt(0)\n"
    "v_cmp_gt_u32 vcc, 2, v228\n"
    "s_cbranch_vccnz .Lg20\n"
    "s_branch .Lg20b\n"
    ".Lg21:\n"
    "s_sub_u32 s52, s52, 1\n"
    "s_cmp_lt_i32 s52, 0\n"
    "s_cbranch_scc1 .Lg21b\n"
    "ds_read_b32 v239, v224 offset:80\n"
    "s_waitcnt lgkmcnt(0)\n"
    "v_cmp_gt_u32 vcc, 2, v239\n"
    "s_cbranch_vccnz .Lg21\n"
    "s_branch .Lg21b\n"
    ".Lg22:\n"
    "s_sub_u32 s52, s52, 1\n"
    "s_cmp_lt_i32 s52, 0\n"
    "s_cbranch_scc1 .Lg22b\n"
    "ds_read_b32 v228, v224 offset:240\n"
    "s_waitcnt lgkmcnt(0)\n"
    "v_cmp_gt_u32 vcc, 2, v228\n"
    "s_cbranch_vccnz .Lg22\n"
    "s_branch .Lg22b\n"
    ".Lg23:\n"
    "s_sub_u32 s52, s52, 1\n"
    "s_cmp_lt_i32 s52, 0\n"
    "s_cbranch_scc1 .Lg23b\n"
    "ds_read_b32 v228, v224 offset:192\n"
    "s_waitcnt lgkmcnt(0)\n"
    "v_cmp_gt_u32 vcc, 3, v228\n"
    "s_cbranch_vccnz .Lg23\n"
    "s_branch .Lg23b\n"
    ".Lg_e12a:\n"
    "v_mfma_f32_16x16x32_bf16 v[200:203], v[8:11], v[176:179], 0\n"
    "v_cvt_pk_bf16_f32 v216, v216, v217\n"
    "v_cvt_pk_bf16_f32 v217, v218, v219\n"
    "v_mfma_f32_16x16x32_bf16 v[204:207], v[32:35], v[176:179], 0\n"
    "v_cvt_pk_bf16_f32 v218, v220, v221\n"
    "v_cvt_pk_bf16_f32 v219, v222, v223\n"
    "v_mfma_f32_16x16x32_bf16 v[200:203], v[12:15], v[180:183], v[200:203]\n"
    "ds_write_b128 v7, v[216:219] offset:8192\n"
    "v_mfma_f32_16x16x32_bf16 v[204:207], v[36:39], v[180:183], v[204:207]\n"
    "v_mov_b32 v227, 11\n"
    "v_mfma_f32_16x16x32_bf16 v[200:203], v[16:19], v[184:187], v[200:203]\n"
    "ds_write_b32 v225, v227 offset:16\n"
    "ds_read_b32 v228, v224 offset:192\n"
    "v_mfma_f32_16x16x32_bf16 v[204:207], v[40:43], v[184:187], v[204:207]\n"
    "v_mfma_f32_16x16x32_bf16 v[200:203], v[20:23], v[188:191], v[200:203]\n"
    "v_mfma_f32_16x16x32_bf16 v[204:207], v[44:47], v[188:191], v[204:207]\n"
    "v_mfma_f32_16x16x32_bf16 v[200:203], v[24:27], v[192:195], v[200:203]\n"
    "v_mfma_f32_16x16x32_bf16 v[204:207], v[48:51], v[192:195], v[204:207]\n"
    "v_mfma_f32_16x16x32_bf16 v[200:203], v[28:31], v[196:199], v[200:203]\n"
    "v_mfma_f32_16x16x32_bf16 v[204:207], v[52:55], v[196:199], v[204:207]\n"
    "v_mfma_f32_16x16x32_bf16 v[208:211], v[56:59], v[176:179], 0\n"
    "s_waitcnt lgkmcnt(0)\n"
    "v_cmp_gt_u32 vcc, 3, v228\n"
    "v_mfma_f32_16x16x32_bf16 v[212:215], v[80:83], v[176:179], 0\n"
    "v_mfma_f32_16x16x32_bf16 v[208:211], v[60:63], v[180:183], v[208:211]\n"
    "s_cbranch_vccnz .Lg23\n"
    ".Lg23b:\n"
    "v_mfma_f32_16x16x32_bf16 v[212:215], v[84:87], v[180:183], v[212:215]\n"
    "v_cvt_pk_bf16_f32 v200, v200, v201\n"
    "v_cvt_pk_bf16_f32 v201, v202, v203\n"
    "v_mfma_f32_16x16x32_bf16 v[208:211], v[64:67], v[184:187], v[208:211]\n"
    "v_cvt_pk_bf16_f32 v202, v204, v205\n"
    "v_cvt_pk_bf16_f32 v203, v206, v207\n"
    "v_mfma_f32_16x16x32_bf16 v[212:215], v[88:91], v[184:187], v[212:215]\n"
    "ds_write_b128 v7, v[200:203] offset:12288\n"
    "v_mfma_f32_16x16x32_bf16 v[208:211], v[68:71], v[188:191], v[208:211]\n"
    "v_mfma_f32_16x16x32_bf16 v[212:215], v[92:95], v[188:191], v[212:215]\n"
    "v_mfma_f32_16x16x32_bf16 v[208:211], v[72:75], v[192:195], v[208:211]\n"
    "v_mfma_f32_16x16x32_bf16 v[212:215], v[96:99], v[192:195], v[212:215]\n"
    "v_mfma_f32_16x16x32_bf16 v[208:211], v[76:79], v[196:199], v[208:211]\n"
    "v_mfma_f32_16x16x32_bf16 v[212:215], v[100:103], v[196:199], v[212:215]\n"
    "v_mfma_f32_16x16x32_bf16 v[216:219], v[104:107], v[176:179], 0\n"
    "v_mfma_f32_16x16x32_bf16 v[220:223], v[128:131], v[176:179], 0\n"
    "v_mfma_f32_16x16x32_bf16 v[216:219], v[108:111], v[180:183], v[216:219]\n"
    "v_mfma_f32_16x16x32_bf16 v[220:223], v[132:135], v[180:183], v[220:223]\n"
    "v_mfma_f32_16x16x32_bf16 v[216:219], v[112:115], v[184:187], v[216:219]\n"
    "v_mfma_f32_16x16x32_bf16 v[220:223], v[136:139], v[184:187], v[220:223]\n"
    "v_mfma_f32_16x16x32_bf16 v[216:219], v[116:119], v[188:191], v[216:219]\n"
    "v_cvt_pk_bf16_f32 v208, v208, v209\n"
    "v_cvt_pk_bf16_f32 v209, v210, v211\n"
    "v_mfma_f32_16x16x32_bf16 v[220:223], v[140:143], v[188:191], v[220:223]\n"
    "v_cvt_pk_bf16_f32 v210, v212, v213\n"
    "v_cvt_pk_bf16_f32 v211, v214, v215\n"
    "v_mfma_f32_16x16x32_bf16 v[216:219], v[120:123], v[192:195], v[216:219]\n"
    "ds_write_b128 v7, v[208:211] offset:13312\n"
    "v_mfma_f32_16x16x32_bf16 v[220:223], v[144:147], v[192:195], v[220:223]\n"
    "v_mfma_f32_16x16x32_bf16 v[216:219], v[124:127], v[196:199], v[216:219]\n"
    "v_mfma_f32_16x16x32_bf16 v[220:223], v[148:151], v[196:199], v[220:223]\n"
    "s_nop 6\n"
    "v_cvt_pk_bf16_f32 v216, v216, v217\n"
    "v_cvt_pk_bf16_f32 v217, v218, v219\n"
    "v_cvt_pk_bf16_f32 v218, v220, v221\n"
    "v_cvt_pk_bf16_f32 v219, v222, v223\n"
    "ds_write_b128 v7, v[216:219] offset:14336\n"
    "v_mov_b32 v227, 12\n"
    "ds_write_b32 v225, v227 offset:16\n"
    "s_branch .Lg_end\n"
    ".Lg24:\n"
    "s_sub_u32 s52, s52, 1\n"
    "s_cmp_lt_i32 s52, 0\n"
    "s_cbranch_scc1 .Lg24b\n"
    "ds_read_b32 v228, v224 offset:192\n"
    "s_waitcnt lgkmcnt(0)\n"
    "v_cmp_gt_u32 vcc, 3, v228\n"
    "s_cbranch_vccnz .Lg24\n"
    "s_branch .Lg24b\n"
    ".Lg25:\n"
    "s_sub_u32 s52, s52, 1\n"
    "s_cmp_lt_i32 s52, 0\n"
    "s_cbranch_scc1 .Lg25b\n"
    "ds_read_b32 v239, v224 offset:272\n"
    "s_waitcnt lgkmcnt(0)\n"
    "v_cmp_gt_u32 vcc, 1, v239\n"
    "s_cbranch_vccnz .Lg25\n"
    "s_branch .Lg25b\n"
    ".Lg_e12b:\n"
    "v_mfma_f32_16x16x32_bf16 v[200:203], v[8:11], v[176:179], 0\n"
    "v_cvt_pk_bf16_f32 v216, v216, v217\n"
    "v_cvt_pk_bf16_f32 v217, v218, v219\n"
    "v_mfma_f32_16x16x32_bf16 v[204:207], v[32:35], v[176:179], 0\n"
    "v_cvt_pk_bf16_f32 v218, v220, v221\n"
    "v_cvt_pk_bf16_f32 v219, v222, v223\n"
    "v_mfma_f32_16x16x32_bf16 v[200:203], v[12:15], v[180:183], v[200:203]\n"
    "ds_write_b128 v7, v[216:219] offset:8192\n"
    "v_mfma_f32_16x16x32_bf16 v[204:207], v[36:39], v[180:183], v[204:207]\n"
    "v_mov_b32 v227, 11\n"
    "v_mfma_f32_16x16x32_bf16 v[200:203], v[16:19], v[184:187], v[200:203]\n"
    "ds_write_b32 v225, v227 offset:16\n"
    "ds_read_b32 v228, v224 offset:192\n"
    "v_mfma_f32_16x16x32_bf16 v[204:207], v[40:43], v[184:187], v[204:207]\n"
    "v_mfma_f32_16x16x32_bf16 v[200:203], v[20:23], v[188:191], v[200:203]\n"
    "v_mfma_f32_16x16x32_bf16 v[204:207], v[44:47], v[188:191], v[204:207]\n"
    "v_mfma_f32_16x16x32_bf16 v[200:203], v[24:27], v[192:195], v[200:203]\n"
    "v_mfma_f32_16x16x32_bf16 v[204:207], v[48:51], v[192:195], v[204:207]\n"
    "v_mfma_f32_16x16x32_bf16 v[200:203], v[28:31], v[196:199], v[200:203]\n"
    "v_mfma_f32_16x16x32_bf16 v[204:207], v[52:55], v[196:199], v[204:207]\n"
    "ds_read_b32 v239, v224 offset:272\n"
    "s_waitcnt lgkmcnt(0)\n"
    "v_cmp_gt_u32 vcc, 1, v239\n"
    "s_cbranch_vccnz .Lg25\n"
    ".Lg25b:\n"
    "v_mfma_f32_16x16x32_bf16 v[208:211], v[56:59], v[176:179], 0\n"
    "v_cmp_gt_u32 vcc, 3, v228\n"
    "v_mfma_f32_16x16x32_bf16 v[212:215], v[80:83], v[176:179], 0\n"
    "s_cbranch_vccnz .Lg24\n"
    ".Lg24b:\n"
    "v_cvt_pk_bf16_f32 v200, v200, v201\n"
    "v_mfma_f32_16x16x32_bf16 v[208:211], v[60:63], v[180:183], v[208:211]\n"
    "v_cvt_pk_bf16_f32 v201, v202, v203\n"
    "v_cvt_pk_bf16_f32 v202, v204, v205\n"
    "v_mfma_f32_16x16x32_bf16 v[212:215], v[84:87], v[180:183], v[212:215]\n"
    "v_cvt_pk_bf16_f32 v203, v206, v207\n"
    "v_mfma_f32_16x16x32_bf16 v[208:211], v[64:67], v[184:187], v[208:211]\n"
    "ds_write_b128 v7, v[200:203] offset:12288\n"
    "v_mfma_f32_16x16x32_bf16 v[212:215], v[88:91], v[184:187], v[212:215]\n"
    "s_add_u32 s60, s40, 109568\n"
    "s_addc_u32 s61, s41, 0\n"
    "v_mfma_f32_16x16x32_bf16 v[208:211], v[68:71], v[188:191], v[208:211]\n"
    "s_add_u32 s66, s60, 6144\n"
    "s_addc_u32 s67, s61, 0\n"
    "v_mfma_f32_16x16x32_bf16 v[212:215], v[92:95], v[188:191], v[212:215]\n"
    "global_load_dwordx4 v[8:11], v235, s[60:61] offset:-3072 sc1\n"
    "global_load_dwordx4 v[32:35], v235, s[66:67] offset:-3072 sc1\n"
    "v_mfma_f32_16x16x32_bf16 v[208:211], v[72:75], v[192:195], v[208:211]\n"
    "global_load_dwordx4 v[12:15], v235, s[60:61] offset:-2048 sc1\n"
    "global_load_dwordx4 v[36:39], v235, s[66:67] offset:-2048 sc1\n"
    "v_mfma_f32_16x16x32_bf16 v[212:215], v[96:99], v[192:195], v[212:215]\n"
    "global_load_dwordx4 v[16:19], v235, s[60:61] offset:-1024 sc1\n"
    "global_load_dwordx4 v[40:43], v235, s[66:67] offset:-1024 sc1\n"
    "v_mfma_f32_16x16x32_bf16 v[208:211], v[76:79], v[196:199], v[208:211]\n"
    "global_load_dwordx4 v[20:23], v235, s[60:61] offset:0 sc1\n"
    "global_load_dwordx4 v[44:47], v235, s[66:67] offset:0 sc1\n"
    "v_mfma_f32_16x16x32_bf16 v[212:215], v[100:103], v[196:199], v[212:215]\n"
    "global_load_dwordx4 v[24:27], v235, s[60:61] offset:1024 sc1\n"
    "global_load_dwordx4 v[48:51], v235, s[66:67] offset:1024 sc1\n"
    "v_mfma_f32_16x16x32_bf16 v[216:219], v[104:107], v[176:179], 0\n"
    "global_load_dwordx4 v[28:31], v235, s[60:61] offset:2048 sc1\n"
    "global_load_dwordx4 v[52:55], v235, s[66:67] offset:2048 sc1\n"
    "v_mfma_f32_16x16x32_bf16 v[220:223], v[128:131], v[176:179], 0\n"
    "v_cvt_pk_bf16_f32 v208, v208, v209\n"
    "v_cvt_pk_bf16_f32 v209, v210, v211\n"
    "v_mfma_f32_16x16x32_bf16 v[216:219], v[108:111], v[180:183], v[216:219]\n"
    "v_cvt_pk_bf16_f32 v210, v212, v213\n"
    "v_cvt_pk_bf16_f32 v211, v214, v215\n"
    "v_mfma_f32_16x16x32_bf16 v[220:223], v[132:135], v[180:183], v[220:223]\n"
    "ds_write_b128 v7, v[208:211] offset:13312\n"
    "v_mfma_f32_16x16x32_bf16 v[216:219], v[112:115], v[184:187], v[216:219]\n"
    "s_add_u32 s60, s40, 146432\n"
    "s_addc_u32 s61, s41, 0\n"
    "v_mfma_f32_16x16x32_bf16 v[220:223], v[136:139], v[184:187], v[220:223]\n"
    "s_add_u32 s66, s60, 6144\n"
    "s_addc_u32 s67, s61, 0\n"
    "v_mfma_f32_16x16x32_bf16 v[216:219], v[116:119], v[188:191], v[216:219]\n"
    "global_load_dwordx4 v[56:59], v235, s[60:61] offset:-3072 sc1\n"
    "global_load_dwordx4 v[80:83], v235, s[66:67] offset:-3072 sc1\n"
    "v_mfma_f32_16x16x32_bf16 v[220:223], v[140:143], v[188:191], v[220:223]\n"
    "global_load_dwordx4 v[60:63], v235, s[60:61] offset:-2048 sc1\n"
    "global_load_dwordx4 v[84:87], v235, s[66:67] offset:-2048 sc1\n"
    "v_mfma_f32_16x16x32_bf16 v[216:219], v[120:123], v[192:195], v[216:219]\n"
    "global_load_dwordx4 v[64:67], v235, s[60:61] offset:-1024 sc1\n"
    "global_load_dwordx4 v[88:91], v235, s[66:67] offset:-1024 sc1\n"
    "v_mfma_f32_16x16x32_bf16 v[220:223], v[144:147], v[192:195], v[220:223]\n"
    "global_load_dwordx4 v[68:71], v235, s[60:61] offset:0 sc1\n"
    "global_load_dwordx4 v[92:95], v235, s[66:67] offset:0 sc1\n"
    "v_mfma_f32_16x16x32_bf16 v[216:219], v[124:127], v[196:199], v[216:219]\n"
    "global_load_dwordx4 v[72:75], v235, s[60:61] offset:1024 sc1\n"
    "global_load_dwordx4 v[96:99], v235, s[66:67] offset:1024 sc1\n"
    "v_mfma_f32_16x16x32_bf16 v[220:223], v[148:151], v[196:199], v[220:223]\n"
    "global_load_dwordx4 v[76:79], v235, s[60:61] offset:2048 sc1\n"
    "global_load_dwordx4 v[100:103], v235, s[66:67] offset:2048 sc1\n"
    "s_nop 2\n"
    "v_cvt_pk_bf16_f32 v216, v216, v217\n"
    "v_cvt_pk_bf16_f32 v217, v218, v219\n"
    "s_nop 0\n"
    "v_cvt_pk_bf16_f32 v218, v220, v221\n"
    "v_cvt_pk_bf16_f32 v219, v222, v223\n"
    "ds_write_b128 v7, v[216:219] offset:14336\n"
    "v_mov_b32 v227, 12\n"
    "ds_write_b32 v225, v227 offset:16\n"
    "s_branch .Lg_to\n"
    ".Lg26:\n"
    "s_sub_u32 s52, s52, 1\n"
    "s_cmp_lt_i32 s52, 0\n"
    "s_cbranch_scc1 .Lg26b\n"
    "ds_read_b32 v229, v224 offset:0\n"
    "s_waitcnt lgkmcnt(0)\n"
    "v_cmp_gt_u32 vcc, 9, v229\n"
    "s_cbranch_vccnz .Lg26\n"
    "s_branch .Lg26b\n"
    ".Lg27:\n"
    "s_sub_u32 s52, s52, 1\n"
    "s_cmp_lt_i32 s52, 0\n"
    "s_cbranch_scc1 .Lg27b\n"
    "ds_read_b32 v228, v224 offset:192\n"
    "s_waitcnt lgkmcnt(0)\n"
    "v_cmp_gt_u32 vcc, 3, v228\n"
    "s_cbranch_vccnz .Lg27\n"
    "s_branch .Lg27b\n"
    ".Lg28:\n"
    "s_sub_u32 s52, s52, 1\n"
    "s_cmp_lt_i32 s52, 0\n"
    "s_cbranch_scc1 .Lg28b\n"
    "ds_read_b32 v228, v224 offset:208\n"
    "s_waitcnt lgkmcnt(0)\n"
    "v_cmp_gt_u32 vcc, 3, v228\n"
    "s_cbranch_vccnz .Lg28\n"
    "s_branch .Lg28b\n"
    ".Lg_e13a:\n"
    "s_setprio 2\n"
    "v_mfma_f32_16x16x32_bf16 v[200:203], v[8:11], v[152:155], 0\n"
    "v_mov_b32 v227, 12\n"
    "v_mfma_f32_16x16x32_bf16 v[204:207], v[32:35], v[152:155], 0\n"
    "v_cvt_pk_bf16_f32 v216, v216, v217\n"
    "v_cvt_pk_bf16_f32 v217, v218, v219\n"
    "v_mfma_f32_16x16x32_bf16 v[200:203], v[12:15], v[156:159], v[200:203]\n"
    "v_cvt_pk_bf16_f32 v218, v220, v221\n"
    "v_cvt_pk_bf16_f32 v219, v222, v223\n"
    "v_mfma_f32_16x16x32_bf16 v[204:207], v[36:39], v[156:159], v[204:207]\n"
    "ds_write_b128 v7, v[216:219] offset:14336\n"
    "v_mfma_f32_16x16x32_bf16 v[200:203], v[16:19], v[160:163], v[200:203]\n"
    "ds_write_b32 v225, v227 offset:16\n"
    "ds_read_b32 v228, v224 offset:208\n"
    "v_mfma_f32_16x16x32_bf16 v[204:207], v[40:43], v[160:163], v[204:207]\n"
    "s_waitcnt vmcnt(0)\n"
    "v_mov_b32 v227, 10\n"
    "v_mfma_f32_16x16x32_bf16 v[200:203], v[20:23], v[164:167], v[200:203]\n"
    "ds_write_b32 v226, v227 offset:0\n"
    "v_mfma_f32_16x16x32_bf16 v[204:207], v[44:47], v[164:167], v[204:207]\n"
    "v_mfma_f32_16x16x32_bf16 v[200:203], v[24:27], v[168:171], v[200:203]\n"
    "v_mfma_f32_16x16x32_bf16 v[204:207], v[48:51], v[168:171], v[204:207]\n"
    "v_mfma_f32_16x16x32_bf16 v[200:203], v[28:31], v[172:175], v[200:203]\n"
    "v_mfma_f32_16x16x32_bf16 v[204:207], v[52:55], v[172:175], v[204:207]\n"
    "v_mfma_f32_16x16x32_bf16 v[208:211], v[56:59], v[152:155], 0\n"
    "s_waitcnt lgkmcnt(1)\n"
    "v_cmp_gt_u32 vcc, 3, v228\n"
    "v_mfma_f32_16x16x32_bf16 v[212:215], v[80:83], v[152:155], 0\n"
    "v_mfma_f32_16x16x32_bf16 v[208:211], v[60:63], v[156:159], v[208:211]\n"
    "s_cbranch_vccnz .Lg28\n"
    ".Lg28b:\n"
    "v_mfma_f32_16x16x32_bf16 v[212:215], v[84:87], v[156:159], v[212:215]\n"
    "v_cvt_pk_bf16_f32 v200, v200, v201\n"
    "v_cvt_pk_bf16_f32 v201, v202, v203\n"
    "v_mfma_f32_16x16x32_bf16 v[208:211], v[64:67], v[160:163], v[208:211]\n"
    "v_cvt_pk_bf16_f32 v202, v204, v205\n"
    "v_cvt_pk_bf16_f32 v203, v206, v207\n"
    "v_mfma_f32_16x16x32_bf16 v[212:215], v[88:91], v[160:163], v[212:215]\n"
    "ds_write_b128 v7, v[200:203] offset:0\n"
    "v_mfma_f32_16x16x32_bf16 v[208:211], v[68:71], v[164:167], v[208:211]\n"
    "v_mfma_f32_16x16x32_bf16 v[212:215], v[92:95], v[164:167], v[212:215]\n"
    "v_mfma_f32_16x16x32_bf16 v[208:211], v[72:75], v[168:171], v[208:211]\n"
    "v_mfma_f32_16x16x32_bf16 v[212:215], v[96:99], v[168:171], v[212:215]\n"
    "v_mfma_f32_16x16x32_bf16 v[208:211], v[76:79], v[172:175], v[208:211]\n"
    "v_mfma_f32_16x16x32_bf16 v[212:215], v[100:103], v[172:175], v[212:215]\n"
    "v_mfma_f32_16x16x32_bf16 v[216:219], v[104:107], v[152:155], 0\n"
    "v_mfma_f32_16x16x32_bf16 v[220:223], v[128:131], v[152:155], 0\n"
    "v_mfma_f32_16x16x32_bf16 v[216:219], v[108:111], v[156:159], v[216:219]\n"
    "v_mfma_f32_16x16x32_bf16 v[220:223], v[132:135], v[156:159], v[220:223]\n"
    "v_mfma_f32_16x16x32_bf16 v[216:219], v[112:115], v[160:163], v[216:219]\n"
    "v_mfma_f32_16x16x32_bf16 v[220:223], v[136:139], v[160:163], v[220:223]\n"
    "v_mfma_f32_16x16x32_bf16 v[216:219], v[116:119], v[164:167], v[216:219]\n"
    "v_cvt_pk_bf16_f32 v208, v208, v209\n"
    "v_cvt_pk_bf16_f32 v209, v210, v211\n"
    "v_mfma_f32_16x16x32_bf16 v[220:223], v[140:143], v[164:167], v[220:223]\n"
    "v_cvt_pk_bf16_f32 v210, v212, v213\n"
    "v_cvt_pk_bf16_f32 v211, v214, v215\n"
    "v_mfma_f32_16x16x32_bf16 v[216:219], v[120:123], v[168:171], v[216:219]\n"
    "ds_write_b128 v7, v[208:211] offset:1024\n"
    "v_mfma_f32_16x16x32_bf16 v[220:223], v[144:147], v[168:171], v[220:223]\n"
    "v_mfma_f32_16x16x32_bf16 v[216:219], v[124:127], v[172:175], v[216:219]\n"
    "v_mfma_f32_16x16x32_bf16 v[220:223], v[148:151], v[172:175], v[220:223]\n"
    "s_nop 6\n"
    "v_cvt_pk_bf16_f32 v216, v216, v217\n"
    "v_cvt_pk_bf16_f32 v217, v218, v219\n"
    "v_cvt_pk_bf16_f32 v218, v220, v221\n"
    "v_cvt_pk_bf16_f32 v219, v222, v223\n"
    "ds_write_b128 v7, v[216:219] offset:2048\n"
    "v_mov_b32 v227, 13\n"
    "ds_write_b32 v225, v227 offset:16\n"
    "s_setprio 0\n"
    "s_branch .Lg_att\n"
    ".Lg29:\n"
    "s_sub_u32 s52, s52, 1\n"
    "s_cmp_lt_i32 s52, 0\n"
    "s_cbranch_scc1 .Lg29b\n"
    "ds_read_b32 v228, v224 offset:208\n"
    "s_waitcnt lgkmcnt(0)\n"
    "v_cmp_gt_u32 vcc, 3, v228\n"
    "s_cbranch_vccnz .Lg29\n"
    "s_branch .Lg29b\n"
    ".Lg30:\n"
    "s_sub_u32 s52, s52, 1\n"
    "s_cmp_lt_i32 s52, 0\n"
    "s_cbranch_scc1 .Lg30b\n"
    "ds_read_b32 v239, v224 offset:272\n"
    "s_waitcnt lgkmcnt(0)\n"
    "v_cmp_gt_u32 vcc, 1, v239\n"
    "s_cbranch_vccnz .Lg30\n"
    "s_branch .Lg30b\n"
    ".Lg_att:\n"
    "s_waitcnt vmcnt(0)\n"
    "v_mov_b32 v8, 0xff800000\n"
    "v_add_u32 v3, 124544, v235\n"
    "v_mov_b32 v6, v224\n"
    "v_lshrrev_b32 v9, 6, v235\n"
    "v_and_b32 v9, 12, v9\n"
    "v_lshlrev_b32 v9, 4, v9\n"
    "v_xor_b32 v4, v9, v235\n"
    "v_add_u32 v4, 124544, v4\n"
    "v_cmp_gt_u32 vcc, 64, v235\n"
    "v_add_u32 v7, 704, v224\n"
    "v_mov_b32 v9, 0x100000\n"
    "v_mov_b32 v0, v248\n"
    "v_cndmask_b32 v7, v9, v7, vcc\n"
    "v_mov_b32 v1, v249\n"
    "v_mov_b32 v2, v250\n"
    "v_and_b32 v5, 15, v251\n"
    "v_mul_u32_u24 v5, 0x1111, v5\n"
    "v_bfe_i32 v10, v251, 8, 1\n"
    "v_bfe_i32 v9, v251, 4, 1\n"
    "v_bfi_b32 v232, v9, 0, v8\n"
    "v_bfi_b32 v236, v10, v8, v232\n"
    "v_bfi_b32 v240, v10, v232, v8\n"
    "v_bfe_i32 v9, v251, 5, 1\n"
    "v_bfi_b32 v233, v9, 0, v8\n"
    "v_bfi_b32 v237, v10, v8, v233\n"
    "v_bfi_b32 v241, v10, v233, v8\n"
    "v_bfe_i32 v9, v251, 6, 1\n"
    "v_bfi_b32 v234, v9, 0, v8\n"
    "v_bfi_b32 v238, v10, v8, v234\n"
    "v_bfi_b32 v242, v10, v234, v8\n"
    "v_bfe_i32 v9, v251, 7, 1\n"
    "v_bfi_b32 v235, v9, 0, v8\n"
    "v_bfi_b32 v239, v10, v8, v235\n"
    "v_bfi_b32 v243, v10, v235, v8\n"
    "s_mov_b32 s45, 12\n"
    "s_mov_b32 s54, 10\n"
    "s_movk_i32 s60, 0x420c\n"
    "s_mov_b32 s61, 0\n"
    "s_mov_b32 s46, 0xbdd53b94\n"
    "s_mov_b32 s47, 0x3dd53b94\n"
    "s_mov_b32 s63, 161920\n"
    "s_mov_b32 s66, 0\n"
    "s_mov_b32 s56, -16\n"
    "s_branch .La_join\n"
    ".Lg31:\n"
    "s_sub_u32 s52, s52, 1\n"
    "s_cmp_lt_i32 s52, 0\n"
    "s_cbranch_scc1 .Lg31b\n"
    "ds_read_b32 v228, v224 offset:144\n"
    "s_waitcnt lgkmcnt(0)\n"
    "v_cmp_gt_u32 vcc, 3, v228\n"
    "s_cbranch_vccnz .Lg31\n"
    "s_branch .Lg31b\n"
    ".Lg32:\n"
    "v_mfma_f32_16x16x32_bf16 v[208:211], v[56:59], v[176:179], 0\n"
    "v_mfma_f32_16x16x32_bf16 v[212:215], v[80:83], v[176:179], 0\n"
    "v_mfma_f32_16x16x32_bf16 v[208:211], v[60:63], v[180:183], v[208:211]\n"
    "v_mfma_f32_16x16x32_bf16 v[212:215], v[84:87], v[180:183], v[212:215]\n"
    "v_mul_f32 v200, v200, v253\n"
    "v_mul_f32 v201, v201, v253\n"
    "v_mfma_f32_16x16x32_bf16 v[208:211], v[64:67], v[184:187], v[208:211]\n"
    "v_mul_f32 v202, v202, v253\n"
    "v_mul_f32 v203, v203, v253\n"
    "v_mfma_f32_16x16x32_bf16 v[212:215], v[88:91], v[184:187], v[212:215]\n"
    "ds_read_b32 v228, v224 offset:160\n"
    "v_mul_f32 v204, v204, v253\n"
    "v_mfma_f32_16x16x32_bf16 v[208:211], v[68:71], v[188:191], v[208:211]\n"
    "v_mul_f32 v205, v205, v253\n"
    "v_mul_f32 v206, v206, v253\n"
    "v_mfma_f32_16x16x32_bf16 v[212:215], v[92:95], v[188:191], v[212:215]\n"
    "v_mul_f32 v207, v207, v253\n"
    "v_cvt_pk_bf16_f32 v200, v200, v201\n"
    "v_mfma_f32_16x16x32_bf16 v[208:211], v[72:75], v[192:195], v[208:211]\n"
    "v_cvt_pk_bf16_f32 v201, v202, v203\n"
    "v_cvt_pk_bf16_f32 v202, v204, v205\n"
    "v_mfma_f32_16x16x32_bf16 v[212:215], v[96:99], v[192:195], v[212:215]\n"
    "v_cvt_pk_bf16_f32 v203, v206, v207\n"
    "v_mfma_f32_16x16x32_bf16 v[208:211], v[76:79], v[196:199], v[208:211]\n"
    "buffer_store_dwordx4 v[200:203], v249, s[80:83], 0 offen offset:128 sc1\n"
    "v_mfma_f32_16x16x32_bf16 v[212:215], v[100:103], v[196:199], v[212:215]\n"
    "s_waitcnt lgkmcnt(0)\n"
    "v_cmp_gt_u32 vcc, 3, v228\n"
    "s_cbranch_vccz .Lg32g\n"
    ".Lg32r:\n"
    "s_sub_u32 s52, s52, 1\n"
    "s_cmp_lt_i32 s52, 0\n"
    "s_cbranch_scc1 .Lg32g\n"
    "ds_read_b32 v228, v224 offset:160\n"
    "s_waitcnt lgkmcnt(0)\n"
    "v_cmp_gt_u32 vcc, 3, v228\n"
    "s_cbranch_vccnz .Lg32r\n"
    "s_branch .Lg32g\n"
    ".Lg32g:\n"
    "ds_read_b64_tr_b16 v[152:153], v0 offset:6144\n"
    "ds_read_b64_tr_b16 v[154:155], v1 offset:6144\n"
    "ds_read_b64_tr_b16 v[156:157], v0 offset:7168\n"
    "ds_read_b64_tr_b16 v[158:159], v1 offset:7168\n"
    "ds_read_b64_tr_b16 v[160:161], v0 offset:8192\n"
    "ds_read_b64_tr_b16 v[162:163], v1 offset:8192\n"
    "ds_read_b64_tr_b16 v[164:165], v0 offset:9216\n"
    "ds_read_b64_tr_b16 v[166:167], v1 offset:9216\n"
    "ds_read_b64_tr_b16 v[168:169], v0 offset:10240\n"
    "ds_read_b64_tr_b16 v[170:171], v1 offset:10240\n"
    "ds_read_b64_tr_b16 v[172:173], v0 offset:11264\n"
    "ds_read_b64_tr_b16 v[174:175], v1 offset:11264\n"
    "ds_read_b32 v252, v238 offset:256\n"
    "s_branch .Lg32j\n"
    ".Lg33:\n"
    "v_mfma_f32_16x16x32_bf16 v[208:211], v[56:59], v[152:155], 0\n"
    "v_mfma_f32_16x16x32_bf16 v[212:215], v[80:83], v[152:155], 0\n"
    "v_mfma_f32_16x16x32_bf16 v[208:211], v[60:63], v[156:159], v[208:211]\n"
    "v_mfma_f32_16x16x32_bf16 v[212:215], v[84:87], v[156:159], v[212:215]\n"
    "v_mul_f32 v200, v200, v252\n"
    "v_mul_f32 v201, v201, v252\n"
    "v_mfma_f32_16x16x32_bf16 v[208:211], v[64:67], v[160:163], v[208:211]\n"
    "v_mul_f32 v202, v202, v252\n"
    "v_mul_f32 v203, v203, v252\n"
    "v_mfma_f32_16x16x32_bf16 v[212:215], v[88:91], v[160:163], v[212:215]\n"
    "ds_read_b32 v228, v224 offset:176\n"
    "v_mul_f32 v204, v204, v252\n"
    "v_mfma_f32_16x16x32_bf16 v[208:211], v[68:71], v[164:167], v[208:211]\n"
    "v_mul_f32 v205, v205, v252\n"
    "v_mul_f32 v206, v206, v252\n"
    "v_mfma_f32_16x16x32_bf16 v[212:215], v[92:95], v[164:167], v[212:215]\n"
    "v_mul_f32 v207, v207, v252\n"
    "v_cvt_pk_bf16_f32 v200, v200, v201\n"
    "v_mfma_f32_16x16x32_bf16 v[208:211], v[72:75], v[168:171], v[208:211]\n"
    "v_cvt_pk_bf16_f32 v201, v202, v203\n"
    "v_cvt_pk_bf16_f32 v202, v204, v205\n"
    "v_mfma_f32_16x16x32_bf16 v[212:215], v[96:99], v[168:171], v[212:215]\n"
    "v_cvt_pk_bf16_f32 v203, v206, v207\n"
    "v_mfma_f32_16x16x32_bf16 v[208:211], v[76:79], v[172:175], v[208:211]\n"
    "buffer_store_dwordx4 v[200:203], v248, s[80:83], 0 offen offset:128 sc1\n"
    "v_mfma_f32_16x16x32_bf16 v[212:215], v[100:103], v[172:175], v[212:215]\n"
    "s_waitcnt lgkmcnt(0)\n"
    "v_cmp_gt_u32 vcc, 3, v228\n"
    "s_cbranch_vccz .Lg33g\n"
    ".Lg33r:\n"
    "s_sub_u32 s52, s52, 1\n"
    "s_cmp_lt_i32 s52, 0\n"
    "s_cbranch_scc1 .Lg33g\n"
    "ds_read_b32 v228, v224 offset:176\n"
    "s_waitcnt lgkmcnt(0)\n"
    "v_cmp_gt_u32 vcc, 3, v228\n"
    "s_cbranch_vccnz .Lg33r\n"
    "s_branch .Lg33g\n"
    ".Lg33g:\n"
    "ds_read_b64_tr_b16 v[176:177], v0 offset:12288\n"
    "ds_read_b64_tr_b16 v[178:179], v1 offset:12288\n"
    "ds_read_b64_tr_b16 v[180:181], v0 offset:13312\n"
    "ds_read_b64_tr_b16 v[182:183], v1 offset:13312\n"
    "ds_read_b64_tr_b16 v[184:185], v0 offset:14336\n"
    "ds_read_b64_tr_b16 v[186:187], v1 offset:14336\n"
    "ds_read_b64_tr_b16 v[188:189], v0 offset:15360\n"
    "ds_read_b64_tr_b16 v[190:191], v1 offset:15360\n"
    "ds_read_b64_tr_b16 v[192:193], v0 offset:16384\n"
    "ds_read_b64_tr_b16 v[194:195], v1 offset:16384\n"
    "ds_read_b64_tr_b16 v[196:197], v0 offset:17408\n"
    "ds_read_b64_tr_b16 v[198:199], v1 offset:17408\n"
    "ds_read_b32 v253, v238 offset:320\n"
    "s_branch .Lg33j\n"
    ".Lg_t12:\n"
    "s_waitcnt lgkmcnt(1)\n"
    "v_mfma_f32_16x16x32_bf16 v[200:203], v[8:11], v[176:179], 0\n"
    "v_mul_f32 v208, v208, v252\n"
    "v_mul_f32 v209, v209, v252\n"
    "v_mfma_f32_16x16x32_bf16 v[204:207], v[32:35], v[176:179], 0\n"
    "v_mul_f32 v210, v210, v252\n"
    "v_mul_f32 v211, v211, v252\n"
    "v_mfma_f32_16x16x32_bf16 v[200:203], v[12:15], v[180:183], v[200:203]\n"
    "v_mul_f32 v212, v212, v252\n"
    "v_mul_f32 v213, v213, v252\n"
    "v_mfma_f32_16x16x32_bf16 v[204:207], v[36:39], v[180:183], v[204:207]\n"
    "v_mul_f32 v214, v214, v252\n"
    "v_mul_f32 v215, v215, v252\n"
    "v_mfma_f32_16x16x32_bf16 v[200:203], v[16:19], v[184:187], v[200:203]\n"
    "v_cvt_pk_bf16_f32 v208, v208, v209\n"
    "v_cvt_pk_bf16_f32 v209, v210, v211\n"
    "v_mfma_f32_16x16x32_bf16 v[204:207], v[40:43], v[184:187], v[204:207]\n"
    "v_cvt_pk_bf16_f32 v210, v212, v213\n"
    "v_cvt_pk_bf16_f32 v211, v214, v215\n"
    "v_mfma_f32_16x16x32_bf16 v[200:203], v[20:23], v[188:191], v[200:203]\n"
    "buffer_store_dwordx4 v[208:211], v248, s[80:83], 0 offen offset:320 sc1\n"
    "v_mfma_f32_16x16x32_bf16 v[204:207], v[44:47], v[188:191], v[204:207]\n"
    "v_mfma_f32_16x16x32_bf16 v[200:203], v[24:27], v[192:195], v[200:203]\n"
    "v_add_u32 v248, v255, v248\n"
    "v_mfma_f32_16x16x32_bf16 v[204:207], v[48:51], v[192:195], v[204:207]\n"
    "v_mfma_f32_16x16x32_bf16 v[200:203], v[28:31], v[196:199], v[200:203]\n"
    "v_mfma_f32_16x16x32_bf16 v[204:207], v[52:55], v[196:199], v[204:207]\n"
    "v_mfma_f32_16x16x32_bf16 v[208:211], v[56:59], v[176:179], 0\n"
    "v_mfma_f32_16x16x32_bf16 v[212:215], v[80:83], v[176:179], 0\n"
    "v_mfma_f32_16x16x32_bf16 v[208:211], v[60:63], v[180:183], v[208:211]\n"
    "v_mfma_f32_16x16x32_bf16 v[212:215], v[84:87], v[180:183], v[212:215]\n"
    "v_mfma_f32_16x16x32_bf16 v[208:211], v[64:67], v[184:187], v[208:211]\n"
    "v_mfma_f32_16x16x32_bf16 v[212:215], v[88:91], v[184:187], v[212:215]\n"
    "s_waitcnt lgkmcnt(0)\n"
    "v_mul_f32 v200, v200, v253\n"
    "v_mfma_f32_16x16x32_bf16 v[208:211], v[68:71], v[188:191], v[208:211]\n"
    "v_mul_f32 v201, v201, v253\n"
    "v_mul_f32 v202, v202, v253\n"
    "v_mfma_f32_16x16x32_bf16 v[212:215], v[92:95], v[188:191], v[212:215]\n"
    "v_mul_f32 v203, v203, v253\n"
    "v_mul_f32 v204, v204, v253\n"
    "v_mfma_f32_16x16x32_bf16 v[208:211], v[72:75], v[192:195], v[208:211]\n"
    "v_mul_f32 v205, v205, v253\n"
    "v_mul_f32 v206, v206, v253\n"
    "v_mfma_f32_16x16x32_bf16 v[212:215], v[96:99], v[192:195], v[212:215]\n"
    "v_mul_f32 v207, v207, v253\n"
    "v_cvt_pk_bf16_f32 v200, v200, v201\n"
    "v_mfma_f32_16x16x32_bf16 v[208:211], v[76:79], v[196:199], v[208:211]\n"
    "v_cvt_pk_bf16_f32 v201, v202, v203\n"
    "v_cvt_pk_bf16_f32 v202, v204, v205\n"
    "v_mfma_f32_16x16x32_bf16 v[212:215], v[100:103], v[196:199], v[212:215]\n"
    "v_cvt_pk_bf16_f32 v203, v206, v207\n"
    "buffer_store_dwordx4 v[200:203], v249, s[80:83], 0 offen offset:128 sc1\n"
    "s_nop 2\n"
    "v_mul_f32 v208, v208, v253\n"
    "v_mul_f32 v209, v209, v253\n"
    "v_mul_f32 v210, v210, v253\n"
    "v_mul_f32 v211, v211, v253\n"
    "v_mul_f32 v212, v212, v253\n"
    "v_mul_f32 v213, v213, v253\n"
    "v_mul_f32 v214, v214, v253\n"
    "v_mul_f32 v215, v215, v253\n"
    "v_cvt_pk_bf16_f32 v208, v208, v209\n"
    "v_cvt_pk_bf16_f32 v209, v210, v211\n"
    "v_cvt_pk_bf16_f32 v210, v212, v213\n"
    "v_cvt_pk_bf16_f32 v211, v214, v215\n"
    "buffer_store_dwordx4 v[208:211], v249, s[80:83], 0 offen offset:320 sc1\n"
    "s_branch .Lg_end\n"
    ".Lg34:\n"
    "v_mfma_f32_16x16x32_bf16 v[208:211], v[56:59], v[176:179], 0\n"
    "v_mfma_f32_16x16x32_bf16 v[212:215], v[80:83], v[176:179], 0\n"
    "v_mfma_f32_16x16x32_bf16 v[208:211], v[60:63], v[180:183], v[208:211]\n"
    "v_mfma_f32_16x16x32_bf16 v[212:215], v[84:87], v[180:183], v[212:215]\n"
    "v_mul_f32 v200, v200, v253\n"
    "v_mul_f32 v201, v201, v253\n"
    "v_mfma_f32_16x16x32_bf16 v[208:211], v[64:67], v[184:187], v[208:211]\n"
    "v_mul_f32 v202, v202, v253\n"
    "v_mul_f32 v203, v203, v253\n"
    "v_mfma_f32_16x16x32_bf16 v[212:215], v[88:91], v[184:187], v[212:215]\n"
    "ds_read_b32 v228, v224 offset:832\n"
    "v_mul_f32 v204, v204, v253\n"
    "v_mfma_f32_16x16x32_bf16 v[208:211], v[68:71], v[188:191], v[208:211]\n"
    "v_mul_f32 v205, v205, v253\n"
    "v_mul_f32 v206, v206, v253\n"
    "v_mfma_f32_16x16x32_bf16 v[212:215], v[92:95], v[188:191], v[212:215]\n"
    "v_mul_f32 v207, v207, v253\n"
    "v_cvt_pk_bf16_f32 v200, v200, v201\n"
    "v_mfma_f32_16x16x32_bf16 v[208:211], v[72:75], v[192:195], v[208:211]\n"
    "v_cvt_pk_bf16_f32 v201, v202, v203\n"
    "v_cvt_pk_bf16_f32 v202, v204, v205\n"
    "v_mfma_f32_16x16x32_bf16 v[212:215], v[96:99], v[192:195], v[212:215]\n"
    "v_cvt_pk_bf16_f32 v203, v206, v207\n"
    "v_mfma_f32_16x16x32_bf16 v[208:211], v[76:79], v[196:199], v[208:211]\n"
    "buffer_store_dwordx4 v[200:203], v249, s[80:83], 0 offen offset:128 sc1\n"
    "v_mfma_f32_16x16x32_bf16 v[212:215], v[100:103], v[196:199], v[212:215]\n"
    "s_waitcnt lgkmcnt(0)\n"
    "v_cmp_gt_u32 vcc, 4, v228\n"
    "s_cbranch_vccz .Lg34g\n"
    ".Lg34r:\n"
    "s_sub_u32 s52, s52, 1\n"
    "s_cmp_lt_i32 s52, 0\n"
    "s_cbranch_scc1 .Lg34g\n"
    "ds_read_b32 v228, v224 offset:832\n"
    "s_waitcnt lgkmcnt(0)\n"
    "v_cmp_gt_u32 vcc, 4, v228\n"
    "s_cbranch_vccnz .Lg34r\n"
    "s_branch .Lg34g\n"
    ".Lg34g:\n"
    "ds_read_b64_tr_b16 v[152:153], v0 offset:0\n"
    "ds_read_b64_tr_b16 v[154:155], v1 offset:0\n"
    "ds_read_b64_tr_b16 v[156:157], v0 offset:1024\n"
    "ds_read_b64_tr_b16 v[158:159], v1 offset:1024\n"
    "ds_read_b64_tr_b16 v[160:161], v0 offset:2048\n"
    "ds_read_b64_tr_b16 v[162:163], v1 offset:2048\n"
    "ds_read_b64_tr_b16 v[164:165], v0 offset:3072\n"
    "ds_read_b64_tr_b16 v[166:167], v1 offset:3072\n"
    "ds_read_b64_tr_b16 v[168:169], v0 offset:4096\n"
    "ds_read_b64_tr_b16 v[170:171], v1 offset:4096\n"
    "ds_read_b64_tr_b16 v[172:173], v0 offset:5120\n"
    "ds_read_b64_tr_b16 v[174:175], v1 offset:5120\n"
    "ds_read_b32 v252, v238 offset:192\n"
    "s_branch .Lg34j\n"
    ".Lg_last:\n"
                :: "{v[0:1]}"(vz), "{v238}"(vi), "{v[248:251]}"(to), "{s[80:83]}"(srd), "{v[2:3]}"(va), "{v[6:7]}"(vb), "{v[224:227]}"(fl), "{v[230:233]}"(xo), "{v234}"(xo4), "{v235}"(lf[0]), "{v236}"(lf[1]),
                   "{s[40:41]}"(ws), "{s[42:43]}"(xb), "{s44}"(UNI(axis_at(rm, 14) * IROWB)), "{s45}"(UNI(axis_at(rm, 15) * IROWB)),
                   "{s46}"(UNI(axis_at(rm, 16) * IROWB)), "{s47}"(UNI(axis_at(rm, 17) * IROWB)), "{s48}"(UNI(axis_at(rm, t12 ? 4 : 18) * IROWB)),
                   "{s49}"(UNI(axis_at(rm, t12 ? 5 : 19) * IROWB)), "{s84}"(UNI(3200 * hs)), "{s85}"(UNI(2 * rm.d * IROWB)), "{v255}"(d56), "{s50}"(h), "{s58}"(UNI(t12)), "{s59}"(UNI(t0 + (bid & 4 ? 4400 : 5200)))
                : "memory", "v253", "s51", "s52", "s53", "s54", "s55", "s56", "s57", "s60", "s61", "s79", "s90", "s91", "s98", "s99", "s92", "s93", "s94", "s95", "vcc", "scc");
        } else {
            const unsigned r3 = axis_at(rm, 3 + e) * IROWB + 192 * h;
            const u32x4 vo = {r3 + c0, r3 + c1, dd[0], dd[1]};
            const u32x2 uu = {(unsigned)(pixad(3 + qj) + (3 + e) * RP + 16 * g), LQ + l16 + h * 3072 + 2048};
            u32x2 xp;
#pragma unroll
            for (int k = 0; k < 2; ++k) {
                const int i = 72 * h + min(lane + 64 * k, 71), r = i / 48, c = (i - 48 * r) / 3;
                xp[k] = axis_at(rm, 14 + r) * IROWB + axis_at(cm, 3 + c) * PIXB + 128 * (i - 48 * r - 3 * c);
            }
            u32x2 lf = {l16, (unsigned)(WS_FL + ((bid & (NREP - 1)) * 144 + 72 + nf) * 128)};
            asm volatile("s_nop 7\n"
    "s_mov_b32 s52, 0x40000\n"
    "s_load_dwordx2 s[78:79], s[40:41], 0x0\n"
    "s_mul_i32 s53, s50, 36864\n"
    "s_add_u32 s57, s53, 84992\n"
    "s_add_u32 s64, s40, s57\n"
    "s_addc_u32 s65, s41, 0\n"
    "s_add_u32 s57, s53, 91136\n"
    "s_add_u32 s66, s40, s57\n"
    "s_addc_u32 s67, s41, 0\n"
    "s_add_u32 s57, s53, 97280\n"
    "s_add_u32 s68, s40, s57\n"
    "s_addc_u32 s69, s41, 0\n"
    "s_add_u32 s57, s53, 103424\n"
    "s_add_u32 s70, s40, s57\n"
    "s_addc_u32 s71, s41, 0\n"
    "s_add_u32 s57, s53, 109568\n"
    "s_add_u32 s72, s40, s57\n"
    "s_addc_u32 s73, s41, 0\n"
    "s_add_u32 s57, s53, 115712\n"
    "s_add_u32 s74, s40, s57\n"
    "s_addc_u32 s75, s41, 0\n"
    "s_sub_u32 s88, s72, 73728\n"
    "s_subb_u32 s89, s73, 0\n"
    "s_sub_u32 s90, s74, 73728\n"
    "s_subb_u32 s91, s75, 0\n"
    "v_add_u32 v251, -6144, v236\n"
    "s_cmp_eq_u32 s50, 0\n"
    "s_cbranch_scc1 .Lh_x0\n"
    "v_mov_b32 v233, v234\n"
    ".Lh_x0:\n"
    "s_waitcnt lgkmcnt(0)\n"
    "s_add_u32 s78, s78, 0x7f4a7c15\n"
    "s_add_u32 s79, s79, 0x9e3779b9\n"
    "v_mov_b64 v[240:241], s[78:79]\n"
    "s_mov_b32 s51, 0x4000\n"
    "ds_read_b32 v228, v224 offset:288\n"
    "s_waitcnt lgkmcnt(0)\n"
    "v_cmp_gt_u32 vcc, 1, v228\n"
    "s_cbranch_vccnz .Lh1\n"
    ".Lh1b:\n"
    ".Lh_d2:\n"
    "s_sleep 1\n"
    "s_memtime s[76:77]\n"
    "s_waitcnt lgkmcnt(0)\n"
    "s_sub_u32 s76, s76, s87\n"
    "s_cmp_lt_i32 s76, 0\n"
    "s_cbranch_scc1 .Lh_d2\n"
    "s_mov_b32 s51, 0x4000\n"
    "v_mov_b64 v[244:245], v[240:241]\n"
    "v_mov_b64 v[246:247], 0\n"
    "v_mov_b64 v[242:243], 0\n"
    "s_mov_b32 exec_hi, 15\n"
    "global_load_dwordx4 v[244:247], v236, s[40:41] sc1\n"
    "s_mov_b64 exec, -1\n"
    "global_load_dwordx4 v[8:11], v235, s[64:65] offset:-3072 sc1\n"
    "global_load_dwordx4 v[32:35], v235, s[66:67] offset:-3072 sc1\n"
    "global_load_dwordx4 v[12:15], v235, s[64:65] offset:-2048 sc1\n"
    "global_load_dwordx4 v[36:39], v235, s[66:67] offset:-2048 sc1\n"
    "global_load_dwordx4 v[16:19], v235, s[64:65] offset:-1024 sc1\n"
    "global_load_dwordx4 v[40:43], v235, s[66:67] offset:-1024 sc1\n"
    "global_load_dwordx4 v[20:23], v235, s[64:65] offset:0 sc1\n"
    "global_load_dwordx4 v[44:47], v235, s[66:67] offset:0 sc1\n"
    "global_load_dwordx4 v[24:27], v235, s[64:65] offset:1024 sc1\n"
    "global_load_dwordx4 v[48:51], v235, s[66:67] offset:1024 sc1\n"
    "global_load_dwordx4 v[28:31], v235, s[64:65] offset:2048 sc1\n"
    "global_load_dwordx4 v[52:55], v235, s[66:67] offset:2048 sc1\n"
    "global_load_dwordx4 v[56:59], v235, s[68:69] offset:-3072 sc1\n"
    "global_load_dwordx4 v[80:83], v235, s[70:71] offset:-3072 sc1\n"
    "global_load_dwordx4 v[60:63], v235, s[68:69] offset:-2048 sc1\n"
    "global_load_dwordx4 v[84:87], v235, s[70:71] offset:-2048 sc1\n"
    "global_load_dwordx4 v[64:67], v235, s[68:69] offset:-1024 sc1\n"
    "global_load_dwordx4 v[88:91], v235, s[70:71] offset:-1024 sc1\n"
    "global_load_dwordx4 v[68:71], v235, s[68:69] offset:0 sc1\n"
    "global_load_dwordx4 v[92:95], v235, s[70:71] offset:0 sc1\n"
    "s_waitcnt vmcnt(20)\n"
    "v_cmp_ne_u64 vcc, v[240:241], v[244:245]\n"
    "s_cbranch_vccnz .Lh_pl\n"
    "global_load_dwordx4 v[72:75], v235, s[68:69] offset:1024 sc1\n"
    "global_load_dwordx4 v[96:99], v235, s[70:71] offset:1024 sc1\n"
    "global_load_dwordx4 v[76:79], v235, s[68:69] offset:2048 sc1\n"
    "global_load_dwordx4 v[100:103], v235, s[70:71] offset:2048 sc1\n"
    "global_load_dwordx4 v[104:107], v235, s[72:73] offset:-3072 sc1\n"
    "global_load_dwordx4 v[128:131], v235, s[74:75] offset:-3072 sc1\n"
    "global_load_dwordx4 v[108:111], v235, s[72:73] offset:-2048 sc1\n"
    "global_load_dwordx4 v[132:135], v235, s[74:75] offset:-2048 sc1\n"
    "global_load_dwordx4 v[112:115], v235, s[72:73] offset:-1024 sc1\n"
    "global_load_dwordx4 v[136:139], v235, s[74:75] offset:-1024 sc1\n"
    "global_load_dwordx4 v[116:119], v235, s[72:73] offset:0 sc1\n"
    "global_load_dwordx4 v[140:143], v235, s[74:75] offset:0 sc1\n"
    "global_load_dwordx4 v[120:123], v235, s[72:73] offset:1024 sc1\n"
    "global_load_dwordx4 v[144:147], v235, s[74:75] offset:1024 sc1\n"
    "global_load_dwordx4 v[124:127], v235, s[72:73] offset:2048 sc1\n"
    "global_load_dwordx4 v[148:151], v235, s[74:75] offset:2048 sc1\n"
    ".Lh_rs:\n"
    "s_lshl_b32 s54, s50, 10\n"
    "s_add_u32 s54, s54, 1024\n"
    "s_mul_i32 s56, s50, 7776\n"
    "s_max_u32 s55, s56, 7168\n"
    "s_add_u32 s60, s42, s84\n"
    "s_addc_u32 s61, s43, 0\n"
    "s_add_u32 m0, s54, 106752\n"
    "s_nop 0\n"
    "global_load_lds_dwordx4 v230, s[60:61]\n"
    "s_add_u32 m0, s54, 108800\n"
    "s_nop 0\n"
    "global_load_lds_dwordx4 v231, s[60:61]\n"
    "s_add_u32 m0, s54, 110848\n"
    "s_nop 0\n"
    "global_load_lds_dwordx4 v232, s[60:61]\n"
    "s_add_u32 m0, s55, 106752\n"
    "s_nop 0\n"
    "global_load_lds_dwordx4 v233, s[60:61]\n"
    "s_add_u32 m0, s56, 106752\n"
    "s_nop 0\n"
    "global_load_lds_dwordx4 v234, s[60:61]\n"
    "s_add_u32 s60, s42, s85\n"
    "s_addc_u32 s61, s43, 0\n"
    "s_add_u32 m0, s54, 115648\n"
    "s_nop 0\n"
    "global_load_lds_dwordx4 v230, s[60:61]\n"
    "s_add_u32 m0, s54, 117696\n"
    "s_nop 0\n"
    "global_load_lds_dwordx4 v231, s[60:61]\n"
    "s_add_u32 m0, s54, 119744\n"
    "s_nop 0\n"
    "global_load_lds_dwordx4 v232, s[60:61]\n"
    "s_add_u32 m0, s55, 115648\n"
    "s_nop 0\n"
    "global_load_lds_dwordx4 v233, s[60:61]\n"
    "s_add_u32 m0, s56, 115648\n"
    "s_nop 0\n"
    "global_load_lds_dwordx4 v234, s[60:61]\n"
    "global_load_dword v250, v238, s[42:43]\n"
    "global_load_dword v250, v239, s[42:43]\n"
    "s_waitcnt vmcnt(44)\n"
    "v_add3_u32 v242, v242, v8, v11\n"
    "v_sub_u32 v8, v8, v240\n"
    "v_add3_u32 v242, v242, v32, v35\n"
    "v_sub_u32 v32, v32, v240\n"
    "v_add3_u32 v242, v242, v12, v15\n"
    "v_sub_u32 v12, v12, v240\n"
    "v_add3_u32 v242, v242, v36, v39\n"
    "v_sub_u32 v36, v36, v240\n"
    "s_waitcnt vmcnt(40)\n"
    "v_add3_u32 v242, v242, v16, v19\n"
    "v_sub_u32 v16, v16, v240\n"
    "v_add3_u32 v242, v242, v40, v43\n"
    "v_sub_u32 v40, v40, v240\n"
    "v_add3_u32 v242, v242, v20, v23\n"
    "v_sub_u32 v20, v20, v240\n"
    "v_add3_u32 v242, v242, v44, v47\n"
    "v_sub_u32 v44, v44, v240\n"
    "s_waitcnt vmcnt(36)\n"
    "v_add3_u32 v242, v242, v24, v27\n"
    "v_sub_u32 v24, v24, v240\n"
    "v_add3_u32 v242, v242, v48, v51\n"
    "v_sub_u32 v48, v48, v240\n"
    "v_add3_u32 v242, v242, v28, v31\n"
    "v_sub_u32 v28, v28, v240\n"
    "v_add3_u32 v242, v242, v52, v55\n"
    "v_sub_u32 v52, v52, v240\n"
    "s_waitcnt vmcnt(32)\n"
    "v_add3_u32 v242, v242, v56, v59\n"
    "v_sub_u32 v56, v56, v240\n"
    "v_add3_u32 v242, v242, v80, v83\n"
    "v_sub_u32 v80, v80, v240\n"
    "v_add3_u32 v242, v242, v60, v63\n"
    "v_sub_u32 v60, v60, v240\n"
    "v_add3_u32 v242, v242, v84, v87\n"
    "v_sub_u32 v84, v84, v240\n"
    "s_waitcnt vmcnt(28)\n"
    "v_add3_u32 v242, v242, v64, v67\n"
    "v_sub_u32 v64, v64, v240\n"
    "v_add3_u32 v242, v242, v88, v91\n"
    "v_sub_u32 v88, v88, v240\n"
    "v_add3_u32 v242, v242, v68, v71\n"
    "v_sub_u32 v68, v68, v240\n"
    "v_add3_u32 v242, v242, v92, v95\n"
    "v_sub_u32 v92, v92, v240\n"
    "s_waitcnt vmcnt(24)\n"
    "v_add3_u32 v242, v242, v72, v75\n"
    "v_sub_u32 v72, v72, v240\n"
    "v_add3_u32 v242, v242, v96, v99\n"
    "v_sub_u32 v96, v96, v240\n"
    "v_add3_u32 v242, v242, v76, v79\n"
    "v_sub_u32 v76, v76, v240\n"
    "v_add3_u32 v242, v242, v100, v103\n"
    "v_sub_u32 v100, v100, v240\n"
    "s_waitcnt vmcnt(20)\n"
    "v_add3_u32 v242, v242, v104, v107\n"
    "v_sub_u32 v104, v104, v240\n"
    "v_add3_u32 v242, v242, v128, v131\n"
    "v_sub_u32 v128, v128, v240\n"
    "v_add3_u32 v242, v242, v108, v111\n"
    "v_sub_u32 v108, v108, v240\n"
    "v_add3_u32 v242, v242, v132, v135\n"
    "v_sub_u32 v132, v132, v240\n"
    "s_waitcnt vmcnt(16)\n"
    "v_add3_u32 v242, v242, v112, v115\n"
    "v_sub_u32 v112, v112, v240\n"
    "v_add3_u32 v242, v242, v136, v139\n"
    "v_sub_u32 v136, v136, v240\n"
    "v_add3_u32 v242, v242, v116, v119\n"
    "v_sub_u32 v116, v116, v240\n"
    "v_add3_u32 v242, v242, v140, v143\n"
    "v_sub_u32 v140, v140, v240\n"
    "s_waitcnt vmcnt(12)\n"
    "v_add3_u32 v242, v242, v120, v123\n"
    "v_sub_u32 v120, v120, v240\n"
    "v_add3_u32 v242, v242, v144, v147\n"
    "v_sub_u32 v144, v144, v240\n"
    "v_add3_u32 v242, v242, v124, v127\n"
    "v_sub_u32 v124, v124, v240\n"
    "v_add3_u32 v242, v242, v148, v151\n"
    "v_sub_u32 v148, v148, v240\n"
    ".Lh_ce:\n"
    "v_cmp_ne_u64 s[76:77], v[240:241], v[244:245]\n"
    "v_sub_u32 v242, v242, v246\n"
    "v_mov_b32 v243, v242\n"
    "s_nop 1\n"
    "v_permlane32_swap_b32 v242, v243\n"
    "v_add_u32 v242, v242, v243\n"
    "s_nop 1\n"
    "v_add_u32_dpp v242, v242, v242 row_ror:8 row_mask:0xf bank_mask:0xf\n"
    "s_nop 1\n"
    "v_add_u32_dpp v242, v242, v242 row_ror:4 row_mask:0xf bank_mask:0xf\n"
    "s_nop 1\n"
    "v_add_u32_dpp v242, v242, v242 row_ror:2 row_mask:0xf bank_mask:0xf\n"
    "s_nop 1\n"
    "v_add_u32_dpp v242, v242, v242 row_ror:1 row_mask:0xf bank_mask:0xf\n"
    "v_mov_b32 v243, v242\n"
    "s_nop 1\n"
    "v_permlane16_swap_b32 v242, v243\n"
    "v_add_u32 v242, v242, v243\n"
    "v_cmp_ne_u32 vcc, 0, v242\n"
    "s_nop 1\n"
    "s_or_b64 vcc, vcc, s[76:77]\n"
    "s_cbranch_scc1 .Lh_re\n"
    ".Lh_ok:\n"
    "v_mov_b32 v227, 1\n"
    "ds_write_b32 v225, v227 offset:272\n"
    "ds_read_b32 v228, v224 offset:0\n"
    "s_waitcnt lgkmcnt(0)\n"
    "v_cmp_gt_u32 vcc, 6, v228\n"
    "s_cbranch_vccnz .Lh2\n"
    ".Lh2b:\n"
    "s_waitcnt vmcnt(2)\n"
    "v_mov_b32 v227, 99\n"
    "ds_write_b32 v226, v227 offset:0\n"
    "s_setprio 0\n"
    "ds_read_b32 v228, v224 offset:128\n"
    "s_waitcnt lgkmcnt(0)\n"
    "v_cmp_gt_u32 vcc, 1, v228\n"
    "s_cbranch_vccnz .Lh3\n"
    ".Lh3b:\n"
    "ds_read_b64_tr_b16 v[152:153], v2 offset:18432\n"
    "ds_read_b64_tr_b16 v[154:155], v3 offset:18432\n"
    "ds_read_b64_tr_b16 v[156:157], v2 offset:19456\n"
    "ds_read_b64_tr_b16 v[158:159], v3 offset:19456\n"
    "ds_read_b64_tr_b16 v[160:161], v2 offset:20480\n"
    "ds_read_b64_tr_b16 v[162:163], v3 offset:20480\n"
    "ds_read_b64_tr_b16 v[164:165], v2 offset:21504\n"
    "ds_read_b64_tr_b16 v[166:167], v3 offset:21504\n"
    "ds_read_b64_tr_b16 v[168:169], v2 offset:22528\n"
    "ds_read_b64_tr_b16 v[170:171], v3 offset:22528\n"
    "ds_read_b64_tr_b16 v[172:173], v2 offset:23552\n"
    "ds_read_b64_tr_b16 v[174:175], v3 offset:23552\n"
    "ds_read_b32 v252, v1 offset:0\n"
    "v_mov_b32 v227, 1\n"
    "ds_write_b32 v226, v227 offset:32\n"
    "v_mov_b32 v242, v240\n"
    "v_mov_b32 v243, v241\n"
    "v_and_b32 v229, 0x70, v235\n"
    "v_add_u32 v229, s48, v229\n"
    "global_store_dwordx4 v229, v[240:243], s[40:41] sc1\n"
    "s_waitcnt lgkmcnt(10)\n"
    "v_mfma_f32_16x16x32_bf16 v[200:203], v[8:11], v[152:155], 0\n"
    "v_mfma_f32_16x16x32_bf16 v[204:207], v[32:35], v[152:155], 0\n"
    "v_mfma_f32_16x16x32_bf16 v[200:203], v[12:15], v[156:159], v[200:203]\n"
    "v_mfma_f32_16x16x32_bf16 v[204:207], v[36:39], v[156:159], v[204:207]\n"
    "s_waitcnt lgkmcnt(8)\n"
    "v_mfma_f32_16x16x32_bf16 v[200:203], v[16:19], v[160:163], v[200:203]\n"
    "v_mfma_f32_16x16x32_bf16 v[204:207], v[40:43], v[160:163], v[204:207]\n"
    "s_waitcnt lgkmcnt(6)\n"
    "v_mfma_f32_16x16x32_bf16 v[200:203], v[20:23], v[164:167], v[200:203]\n"
    "v_mfma_f32_16x16x32_bf16 v[204:207], v[44:47], v[164:167], v[204:207]\n"
    "s_waitcnt lgkmcnt(4)\n"
    "v_mfma_f32_16x16x32_bf16 v[200:203], v[24:27], v[168:171], v[200:203]\n"
    "v_mfma_f32_16x16x32_bf16 v[204:207], v[48:51], v[168:171], v[204:207]\n"
    "s_waitcnt lgkmcnt(2)\n"
    "v_mfma_f32_16x16x32_bf16 v[200:203], v[28:31], v[172:175], v[200:203]\n"
    "v_mfma_f32_16x16x32_bf16 v[204:207], v[52:55], v[172:175], v[204:207]\n"
    "v_mfma_f32_16x16x32_bf16 v[208:211], v[56:59], v[152:155], 0\n"
    "v_mfma_f32_16x16x32_bf16 v[212:215], v[80:83], v[152:155], 0\n"
    "ds_read_b32 v228, v224 offset:144\n"
    "v_mfma_f32_16x16x32_bf16 v[208:211], v[60:63], v[156:159], v[208:211]\n"
    "v_mfma_f32_16x16x32_bf16 v[212:215], v[84:87], v[156:159], v[212:215]\n"
    "v_mfma_f32_16x16x32_bf16 v[208:211], v[64:67], v[160:163], v[208:211]\n"
    "s_waitcnt lgkmcnt(2)\n"
    "v_mul_f32 v200, v200, v252\n"
    "v_mfma_f32_16x16x32_bf16 v[212:215], v[88:91], v[160:163], v[212:215]\n"
    "v_mul_f32 v201, v201, v252\n"
    "v_mul_f32 v202, v202, v252\n"
    "v_mfma_f32_16x16x32_bf16 v[208:211], v[68:71], v[164:167], v[208:211]\n"
    "v_mul_f32 v203, v203, v252\n"
    "v_mul_f32 v204, v204, v252\n"
    "v_mfma_f32_16x16x32_bf16 v[212:215], v[92:95], v[164:167], v[212:215]\n"
    "v_mul_f32 v205, v205, v252\n"
    "v_mul_f32 v206, v206, v252\n"
    "v_mfma_f32_16x16x32_bf16 v[208:211], v[72:75], v[168:171], v[208:211]\n"
    "v_mul_f32 v207, v207, v252\n"
    "v_cvt_pk_bf16_f32 v200, v200, v201\n"
    "v_mfma_f32_16x16x32_bf16 v[212:215], v[96:99], v[168:171], v[212:215]\n"
    "v_cvt_pk_bf16_f32 v201, v202, v203\n"
    "v_cvt_pk_bf16_f32 v202, v204, v205\n"
    "v_mfma_f32_16x16x32_bf16 v[208:211], v[76:79], v[172:175], v[208:211]\n"
    "s_waitcnt lgkmcnt(0)\n"
    "v_cmp_gt_u32 vcc, 1, v228\n"
    "v_mfma_f32_16x16x32_bf16 v[212:215], v[100:103], v[172:175], v[212:215]\n"
    "v_cvt_pk_bf16_f32 v203, v206, v207\n"
    "s_cbranch_vccnz .Lh4\n"
    "v_mfma_f32_16x16x32_bf16 v[216:219], v[104:107], v[152:155], 0\n"
    "ds_read_b64_tr_b16 v[176:177], v2 offset:24576\n"
    "ds_read_b64_tr_b16 v[178:179], v3 offset:24576\n"
    "v_mfma_f32_16x16x32_bf16 v[220:223], v[128:131], v[152:155], 0\n"
    "ds_read_b64_tr_b16 v[180:181], v2 offset:25600\n"
    "ds_read_b64_tr_b16 v[182:183], v3 offset:25600\n"
    "v_mfma_f32_16x16x32_bf16 v[216:219], v[108:111], v[156:159], v[216:219]\n"
    "ds_read_b64_tr_b16 v[184:185], v2 offset:26624\n"
    "ds_read_b64_tr_b16 v[186:187], v3 offset:26624\n"
    "v_mfma_f32_16x16x32_bf16 v[220:223], v[132:135], v[156:159], v[220:223]\n"
    "ds_read_b64_tr_b16 v[188:189], v2 offset:27648\n"
    "ds_read_b64_tr_b16 v[190:191], v3 offset:27648\n"
    "v_mfma_f32_16x16x32_bf16 v[216:219], v[112:115], v[160:163], v[216:219]\n"
    "buffer_store_dwordx4 v[200:203], v4, s[80:83], 0 offen offset:0 sc1\n"
    "v_mfma_f32_16x16x32_bf16 v[220:223], v[136:139], v[160:163], v[220:223]\n"
    "v_mul_f32 v208, v208, v252\n"
    "v_mul_f32 v209, v209, v252\n"
    "v_mfma_f32_16x16x32_bf16 v[216:219], v[116:119], v[164:167], v[216:219]\n"
    "ds_read_b64_tr_b16 v[192:193], v2 offset:28672\n"
    "ds_read_b64_tr_b16 v[194:195], v3 offset:28672\n"
    "v_mfma_f32_16x16x32_bf16 v[220:223], v[140:143], v[164:167], v[220:223]\n"
    "v_mul_f32 v210, v210, v252\n"
    "v_mul_f32 v211, v211, v252\n"
    "v_mfma_f32_16x16x32_bf16 v[216:219], v[120:123], v[168:171], v[216:219]\n"
    "ds_read_b64_tr_b16 v[196:197], v2 offset:29696\n"
    "ds_read_b64_tr_b16 v[198:199], v3 offset:29696\n"
    "v_mfma_f32_16x16x32_bf16 v[220:223], v[144:147], v[168:171], v[220:223]\n"
    "v_mul_f32 v212, v212, v252\n"
    "v_mov_b32 v227, 2\n"
    "v_mfma_f32_16x16x32_bf16 v[216:219], v[124:127], v[172:175], v[216:219]\n"
    "ds_read_b32 v253, v1 offset:64\n"
    "v_mul_f32 v213, v213, v252\n"
    "v_mfma_f32_16x16x32_bf16 v[220:223], v[148:151], v[172:175], v[220:223]\n"
    "v_mul_f32 v214, v214, v252\n"
    "v_mul_f32 v215, v215, v252\n"
    "ds_write_b32 v226, v227 offset:32\n"
    "v_cvt_pk_bf16_f32 v208, v208, v209\n"
    "v_cvt_pk_bf16_f32 v209, v210, v211\n"
    "v_cvt_pk_bf16_f32 v210, v212, v213\n"
    "v_cvt_pk_bf16_f32 v211, v214, v215\n"
    "buffer_store_dwordx4 v[208:211], v4, s[80:83], 0 offen offset:64 sc1\n"
    ".Lh4j:\n"
    "s_waitcnt lgkmcnt(2)\n"
    "v_mfma_f32_16x16x32_bf16 v[200:203], v[8:11], v[176:179], 0\n"
    "v_mul_f32 v216, v216, v252\n"
    "v_mul_f32 v217, v217, v252\n"
    "v_mfma_f32_16x16x32_bf16 v[204:207], v[32:35], v[176:179], 0\n"
    "v_mul_f32 v218, v218, v252\n"
    "v_mul_f32 v219, v219, v252\n"
    "v_mfma_f32_16x16x32_bf16 v[200:203], v[12:15], v[180:183], v[200:203]\n"
    "v_mul_f32 v220, v220, v252\n"
    "v_mul_f32 v221, v221, v252\n"
    "v_mfma_f32_16x16x32_bf16 v[204:207], v[36:39], v[180:183], v[204:207]\n"
    "v_mul_f32 v222, v222, v252\n"
    "v_mul_f32 v223, v223, v252\n"
    "v_mfma_f32_16x16x32_bf16 v[200:203], v[16:19], v[184:187], v[200:203]\n"
    "v_cvt_pk_bf16_f32 v216, v216, v217\n"
    "v_cvt_pk_bf16_f32 v217, v218, v219\n"
    "v_mfma_f32_16x16x32_bf16 v[204:207], v[40:43], v[184:187], v[204:207]\n"
    "v_cvt_pk_bf16_f32 v218, v220, v221\n"
    "v_cvt_pk_bf16_f32 v219, v222, v223\n"
    "v_mfma_f32_16x16x32_bf16 v[200:203], v[20:23], v[188:191], v[200:203]\n"
    "buffer_store_dwordx4 v[216:219], v4, s[80:83], 0 offen offset:128 sc1\n"
    "v_mfma_f32_16x16x32_bf16 v[204:207], v[44:47], v[188:191], v[204:207]\n"
    "v_mfma_f32_16x16x32_bf16 v[200:203], v[24:27], v[192:195], v[200:203]\n"
    "v_add_u32 v4, s46, v4\n"
    "v_mfma_f32_16x16x32_bf16 v[204:207], v[48:51], v[192:195], v[204:207]\n"
    "v_mfma_f32_16x16x32_bf16 v[200:203], v[28:31], v[196:199], v[200:203]\n"
    "v_mfma_f32_16x16x32_bf16 v[204:207], v[52:55], v[196:199], v[204:207]\n"
    "v_mfma_f32_16x16x32_bf16 v[208:211], v[56:59], v[176:179], 0\n"
    "v_mfma_f32_16x16x32_bf16 v[212:215], v[80:83], v[176:179], 0\n"
    "ds_read_b32 v228, v224 offset:160\n"
    "v_mfma_f32_16x16x32_bf16 v[208:211], v[60:63], v[180:183], v[208:211]\n"
    "v_mfma_f32_16x16x32_bf16 v[212:215], v[84:87], v[180:183], v[212:215]\n"
    "v_mfma_f32_16x16x32_bf16 v[208:211], v[64:67], v[184:187], v[208:211]\n"
    "s_waitcnt lgkmcnt(2)\n"
    "v_mul_f32 v200, v200, v253\n"
    "v_mfma_f32_16x16x32_bf16 v[212:215], v[88:91], v[184:187], v[212:215]\n"
    "v_mul_f32 v201, v201, v253\n"
    "v_mul_f32 v202, v202, v253\n"
    "v_mfma_f32_16x16x32_bf16 v[208:211], v[68:71], v[188:191], v[208:211]\n"
    "v_mul_f32 v203, v203, v253\n"
    "v_mul_f32 v204, v204, v253\n"
    "v_mfma_f32_16x16x32_bf16 v[212:215], v[92:95], v[188:191], v[212:215]\n"
    "v_mul_f32 v205, v205, v253\n"
    "v_mul_f32 v206, v206, v253\n"
    "v_mfma_f32_16x16x32_bf16 v[208:211], v[72:75], v[192:195], v[208:211]\n"
    "v_mul_f32 v207, v207, v253\n"
    "v_cvt_pk_bf16_f32 v200, v200, v201\n"
    "v_mfma_f32_16x16x32_bf16 v[212:215], v[96:99], v[192:195], v[212:215]\n"
    "v_cvt_pk_bf16_f32 v201, v202, v203\n"
    "v_cvt_pk_bf16_f32 v202, v204, v205\n"
    "v_mfma_f32_16x16x32_bf16 v[208:211], v[76:79], v[196:199], v[208:211]\n"
    "s_waitcnt lgkmcnt(0)\n"
    "v_cmp_gt_u32 vcc, 1, v228\n"
    "v_mfma_f32_16x16x32_bf16 v[212:215], v[100:103], v[196:199], v[212:215]\n"
    "v_cvt_pk_bf16_f32 v203, v206, v207\n"
    "s_cbranch_vccnz .Lh5\n"
    "v_mfma_f32_16x16x32_bf16 v[216:219], v[104:107], v[176:179], 0\n"
    "ds_read_b64_tr_b16 v[152:153], v2 offset:30720\n"
    "ds_read_b64_tr_b16 v[154:155], v3 offset:30720\n"
    "v_mfma_f32_16x16x32_bf16 v[220:223], v[128:131], v[176:179], 0\n"
    "ds_read_b64_tr_b16 v[156:157], v2 offset:31744\n"
    "ds_read_b64_tr_b16 v[158:159], v3 offset:31744\n"
    "v_mfma_f32_16x16x32_bf16 v[216:219], v[108:111], v[180:183], v[216:219]\n"
    "ds_read_b64_tr_b16 v[160:161], v2 offset:32768\n"
    "ds_read_b64_tr_b16 v[162:163], v3 offset:32768\n"
    "v_mfma_f32_16x16x32_bf16 v[220:223], v[132:135], v[180:183], v[220:223]\n"
    "ds_read_b64_tr_b16 v[164:165], v2 offset:33792\n"
    "ds_read_b64_tr_b16 v[166:167], v3 offset:33792\n"
    "v_mfma_f32_16x16x32_bf16 v[216:219], v[112:115], v[184:187], v[216:219]\n"
    "buffer_store_dwordx4 v[200:203], v5, s[80:83], 0 offen offset:0 sc1\n"
    "v_mfma_f32_16x16x32_bf16 v[220:223], v[136:139], v[184:187], v[220:223]\n"
    "v_mul_f32 v208, v208, v253\n"
    "v_mul_f32 v209, v209, v253\n"
    "v_mfma_f32_16x16x32_bf16 v[216:219], v[116:119], v[188:191], v[216:219]\n"
    "ds_read_b64_tr_b16 v[168:169], v2 offset:34816\n"
    "ds_read_b64_tr_b16 v[170:171], v3 offset:34816\n"
    "v_mfma_f32_16x16x32_bf16 v[220:223], v[140:143], v[188:191], v[220:223]\n"
    "v_mul_f32 v210, v210, v253\n"
    "v_mul_f32 v211, v211, v253\n"
    "v_mfma_f32_16x16x32_bf16 v[216:219], v[120:123], v[192:195], v[216:219]\n"
    "ds_read_b64_tr_b16 v[172:173], v2 offset:35840\n"
    "ds_read_b64_tr_b16 v[174:175], v3 offset:35840\n"
    "v_mfma_f32_16x16x32_bf16 v[220:223], v[144:147], v[192:195], v[220:223]\n"
    "v_mul_f32 v212, v212, v253\n"
    "v_mov_b32 v227, 3\n"
    "v_mfma_f32_16x16x32_bf16 v[216:219], v[124:127], v[196:199], v[216:219]\n"
    "ds_read_b32 v252, v1 offset:128\n"
    "v_mul_f32 v213, v213, v253\n"
    "v_mfma_f32_16x16x32_bf16 v[220:223], v[148:151], v[196:199], v[220:223]\n"
    "v_mul_f32 v214, v214, v253\n"
    "v_mul_f32 v215, v215, v253\n"
    "ds_write_b32 v226, v227 offset:32\n"
    "v_cvt_pk_bf16_f32 v208, v208, v209\n"
    "v_cvt_pk_bf16_f32 v209, v210, v211\n"
    "v_cvt_pk_bf16_f32 v210, v212, v213\n"
    "v_cvt_pk_bf16_f32 v211, v214, v215\n"
    "buffer_store_dwordx4 v[208:211], v5, s[80:83], 0 offen offset:64 sc1\n"
    ".Lh5j:\n"
    "s_waitcnt lgkmcnt(2)\n"
    "v_mfma_f32_16x16x32_bf16 v[200:203], v[8:11], v[152:155], 0\n"
    "v_mul_f32 v216, v216, v253\n"
    "v_mul_f32 v217, v217, v253\n"
    "v_mfma_f32_16x16x32_bf16 v[204:207], v[32:35], v[152:155], 0\n"
    "v_mul_f32 v218, v218, v253\n"
    "v_mul_f32 v219, v219, v253\n"
    "v_mfma_f32_16x16x32_bf16 v[200:203], v[12:15], v[156:159], v[200:203]\n"
    "v_mul_f32 v220, v220, v253\n"
    "v_mul_f32 v221, v221, v253\n"
    "v_mfma_f32_16x16x32_bf16 v[204:207], v[36:39], v[156:159], v[204:207]\n"
    "v_mul_f32 v222, v222, v253\n"
    "v_mul_f32 v223, v223, v253\n"
    "v_mfma_f32_16x16x32_bf16 v[200:203], v[16:19], v[160:163], v[200:203]\n"
    "v_cvt_pk_bf16_f32 v216, v216, v217\n"
    "v_cvt_pk_bf16_f32 v217, v218, v219\n"
    "v_mfma_f32_16x16x32_bf16 v[204:207], v[40:43], v[160:163], v[204:207]\n"
    "v_cvt_pk_bf16_f32 v218, v220, v221\n"
    "v_cvt_pk_bf16_f32 v219, v222, v223\n"
    "v_mfma_f32_16x16x32_bf16 v[200:203], v[20:23], v[164:167], v[200:203]\n"
    "buffer_store_dwordx4 v[216:219], v5, s[80:83], 0 offen offset:128 sc1\n"
    "v_mfma_f32_16x16x32_bf16 v[204:207], v[44:47], v[164:167], v[204:207]\n"
    "v_mfma_f32_16x16x32_bf16 v[200:203], v[24:27], v[168:171], v[200:203]\n"
    "v_add_u32 v5, s46, v5\n"
    "v_mfma_f32_16x16x32_bf16 v[204:207], v[48:51], v[168:171], v[204:207]\n"
    "v_mfma_f32_16x16x32_bf16 v[200:203], v[28:31], v[172:175], v[200:203]\n"
    "v_mfma_f32_16x16x32_bf16 v[204:207], v[52:55], v[172:175], v[204:207]\n"
    "v_mfma_f32_16x16x32_bf16 v[208:211], v[56:59], v[152:155], 0\n"
    "v_mfma_f32_16x16x32_bf16 v[212:215], v[80:83], v[152:155], 0\n"
    "ds_read_b32 v228, v224 offset:176\n"
    "v_mfma_f32_16x16x32_bf16 v[208:211], v[60:63], v[156:159], v[208:211]\n"
    "v_mfma_f32_16x16x32_bf16 v[212:215], v[84:87], v[156:159], v[212:215]\n"
    "v_mfma_f32_16x16x32_bf16 v[208:211], v[64:67], v[160:163], v[208:211]\n"
    "s_waitcnt lgkmcnt(2)\n"
    "v_mul_f32 v200, v200, v252\n"
    "v_mfma_f32_16x16x32_bf16 v[212:215], v[88:91], v[160:163], v[212:215]\n"
    "v_mul_f32 v201, v201, v252\n"
    "v_mul_f32 v202, v202, v252\n"
    "v_mfma_f32_16x16x32_bf16 v[208:211], v[68:71], v[164:167], v[208:211]\n"
    "v_mul_f32 v203, v203, v252\n"
    "v_mul_f32 v204, v204, v252\n"
    "v_mfma_f32_16x16x32_bf16 v[212:215], v[92:95], v[164:167], v[212:215]\n"
    "v_mul_f32 v205, v205, v252\n"
    "v_mul_f32 v206, v206, v252\n"
    "v_mfma_f32_16x16x32_bf16 v[208:211], v[72:75], v[168:171], v[208:211]\n"
    "v_mul_f32 v207, v207, v252\n"
    "v_cvt_pk_bf16_f32 v200, v200, v201\n"
    "v_mfma_f32_16x16x32_bf16 v[212:215], v[96:99], v[168:171], v[212:215]\n"
    "v_cvt_pk_bf16_f32 v201, v202, v203\n"
    "v_cvt_pk_bf16_f32 v202, v204, v205\n"
    "v_mfma_f32_16x16x32_bf16 v[208:211], v[76:79], v[172:175], v[208:211]\n"
    "s_waitcnt lgkmcnt(0)\n"
    "v_cmp_gt_u32 vcc, 1, v228\n"
    "v_mfma_f32_16x16x32_bf16 v[212:215], v[100:103], v[172:175], v[212:215]\n"
    "v_cvt_pk_bf16_f32 v203, v206, v207\n"
    "s_cbranch_vccnz .Lh6\n"
    "v_mfma_f32_16x16x32_bf16 v[216:219], v[104:107], v[152:155], 0\n"
    "ds_read_b64_tr_b16 v[176:177], v2 offset:18432\n"
    "ds_read_b64_tr_b16 v[178:179], v3 offset:18432\n"
    "v_mfma_f32_16x16x32_bf16 v[220:223], v[128:131], v[152:155], 0\n"
    "ds_read_b64_tr_b16 v[180:181], v2 offset:19456\n"
    "ds_read_b64_tr_b16 v[182:183], v3 offset:19456\n"
    "v_mfma_f32_16x16x32_bf16 v[216:219], v[108:111], v[156:159], v[216:219]\n"
    "ds_read_b64_tr_b16 v[184:185], v2 offset:20480\n"
    "ds_read_b64_tr_b16 v[186:187], v3 offset:20480\n"
    "v_mfma_f32_16x16x32_bf16 v[220:223], v[132:135], v[156:159], v[220:223]\n"
    "ds_read_b64_tr_b16 v[188:189], v2 offset:21504\n"
    "ds_read_b64_tr_b16 v[190:191], v3 offset:21504\n"
    "v_mfma_f32_16x16x32_bf16 v[216:219], v[112:115], v[160:163], v[216:219]\n"
    "buffer_store_dwordx4 v[200:203], v4, s[80:83], 0 offen offset:0 sc1\n"
    "v_mfma_f32_16x16x32_bf16 v[220:223], v[136:139], v[160:163], v[220:223]\n"
    "v_mul_f32 v208, v208, v252\n"
    "v_mul_f32 v209, v209, v252\n"
    "v_mfma_f32_16x16x32_bf16 v[216:219], v[116:119], v[164:167], v[216:219]\n"
    "ds_read_b64_tr_b16 v[192:193], v2 offset:22528\n"
    "ds_read_b64_tr_b16 v[194:195], v3 offset:22528\n"
    "v_mfma_f32_16x16x32_bf16 v[220:223], v[140:143], v[164:167], v[220:223]\n"
    "v_mul_f32 v210, v210, v252\n"
    "v_mul_f32 v211, v211, v252\n"
    "v_mfma_f32_16x16x32_bf16 v[216:219], v[120:123], v[168:171], v[216:219]\n"
    "ds_read_b64_tr_b16 v[196:197], v2 offset:23552\n"
    "ds_read_b64_tr_b16 v[198:199], v3 offset:23552\n"
    "v_mfma_f32_16x16x32_bf16 v[220:223], v[144:147], v[168:171], v[220:223]\n"
    "v_mul_f32 v212, v212, v252\n"
    "v_mov_b32 v227, 4\n"
    "v_mfma_f32_16x16x32_bf16 v[216:219], v[124:127], v[172:175], v[216:219]\n"
    "ds_read_b32 v253, v1 offset:0\n"
    "v_mul_f32 v213, v213, v252\n"
    "v_mfma_f32_16x16x32_bf16 v[220:223], v[148:151], v[172:175], v[220:223]\n"
    "v_mul_f32 v214, v214, v252\n"
    "v_mul_f32 v215, v215, v252\n"
    "ds_write_b32 v226, v227 offset:32\n"
    "v_cvt_pk_bf16_f32 v208, v208, v209\n"
    "v_cvt_pk_bf16_f32 v209, v210, v211\n"
    "v_cvt_pk_bf16_f32 v210, v212, v213\n"
    "v_cvt_pk_bf16_f32 v211, v214, v215\n"
    "buffer_store_dwordx4 v[208:211], v4, s[80:83], 0 offen offset:64 sc1\n"
    ".Lh6j:\n"
    "s_waitcnt lgkmcnt(2)\n"
    "v_mfma_f32_16x16x32_bf16 v[200:203], v[8:11], v[176:179], 0\n"
    "v_mul_f32 v216, v216, v252\n"
    "v_mul_f32 v217, v217, v252\n"
    "v_mfma_f32_16x16x32_bf16 v[204:207], v[32:35], v[176:179], 0\n"
    "v_mul_f32 v218, v218, v252\n"
    "v_mul_f32 v219, v219, v252\n"
    "v_mfma_f32_16x16x32_bf16 v[200:203], v[12:15], v[180:183], v[200:203]\n"
    "v_mul_f32 v220, v220, v252\n"
    "v_mul_f32 v221, v221, v252\n"
    "v_mfma_f32_16x16x32_bf16 v[204:207], v[36:39], v[180:183], v[204:207]\n"
    "v_mul_f32 v222, v222, v252\n"
    "v_mul_f32 v223, v223, v252\n"
    "v_mfma_f32_16x16x32_bf16 v[200:203], v[16:19], v[184:187], v[200:203]\n"
    "v_cvt_pk_bf16_f32 v216, v216, v217\n"
    "v_cvt_pk_bf16_f32 v217, v218, v219\n"
    "v_mfma_f32_16x16x32_bf16 v[204:207], v[40:43], v[184:187], v[204:207]\n"
    "v_cvt_pk_bf16_f32 v218, v220, v221\n"
    "v_cvt_pk_bf16_f32 v219, v222, v223\n"
    "v_mfma_f32_16x16x32_bf16 v[200:203], v[20:23], v[188:191], v[200:203]\n"
    "buffer_store_dwordx4 v[216:219], v4, s[80:83], 0 offen offset:128 sc1\n"
    "v_mfma_f32_16x16x32_bf16 v[204:207], v[44:47], v[188:191], v[204:207]\n"
    "v_mfma_f32_16x16x32_bf16 v[200:203], v[24:27], v[192:195], v[200:203]\n"
    "v_add_u32 v4, s46, v4\n"
    "v_mfma_f32_16x16x32_bf16 v[204:207], v[48:51], v[192:195], v[204:207]\n"
    "v_mfma_f32_16x16x32_bf16 v[200:203], v[28:31], v[196:199], v[200:203]\n"
    "v_mfma_f32_16x16x32_bf16 v[204:207], v[52:55], v[196:199], v[204:207]\n"
    "v_mfma_f32_16x16x32_bf16 v[208:211], v[56:59], v[176:179], 0\n"
    "v_mfma_f32_16x16x32_bf16 v[212:215], v[80:83], v[176:179], 0\n"
    "ds_read_b32 v228, v224 offset:128\n"
    "v_mfma_f32_16x16x32_bf16 v[208:211], v[60:63], v[180:183], v[208:211]\n"
    "v_mfma_f32_16x16x32_bf16 v[212:215], v[84:87], v[180:183], v[212:215]\n"
    "v_mfma_f32_16x16x32_bf16 v[208:211], v[64:67], v[184:187], v[208:211]\n"
    "s_waitcnt lgkmcnt(2)\n"
    "v_mul_f32 v200, v200, v253\n"
    "v_mfma_f32_16x16x32_bf16 v[212:215], v[88:91], v[184:187], v[212:215]\n"
    "v_mul_f32 v201, v201, v253\n"
    "v_mul_f32 v202, v202, v253\n"
    "v_mfma_f32_16x16x32_bf16 v[208:211], v[68:71], v[188:191], v[208:211]\n"
    "v_mul_f32 v203, v203, v253\n"
    "v_mul_f32 v204, v204, v253\n"
    "v_mfma_f32_16x16x32_bf16 v[212:215], v[92:95], v[188:191], v[212:215]\n"
    "v_mul_f32 v205, v205, v253\n"
    "v_mul_f32 v206, v206, v253\n"
    "v_mfma_f32_16x16x32_bf16 v[208:211], v[72:75], v[192:195], v[208:211]\n"
    "v_mul_f32 v207, v207, v253\n"
    "v_cvt_pk_bf16_f32 v200, v200, v201\n"
    "v_mfma_f32_16x16x32_bf16 v[212:215], v[96:99], v[192:195], v[212:215]\n"
    "v_cvt_pk_bf16_f32 v201, v202, v203\n"
    "v_cvt_pk_bf16_f32 v202, v204, v205\n"
    "v_mfma_f32_16x16x32_bf16 v[208:211], v[76:79], v[196:199], v[208:211]\n"
    "s_waitcnt lgkmcnt(0)\n"
    "v_cmp_gt_u32 vcc, 2, v228\n"
    "v_mfma_f32_16x16x32_bf16 v[212:215], v[100:103], v[196:199], v[212:215]\n"
    "v_cvt_pk_bf16_f32 v203, v206, v207\n"
    "s_cbranch_vccnz .Lh7\n"
    "v_mfma_f32_16x16x32_bf16 v[216:219], v[104:107], v[176:179], 0\n"
    "ds_read_b64_tr_b16 v[152:153], v2 offset:24576\n"
    "ds_read_b64_tr_b16 v[154:155], v3 offset:24576\n"
    "v_mfma_f32_16x16x32_bf16 v[220:223], v[128:131], v[176:179], 0\n"
    "ds_read_b64_tr_b16 v[156:157], v2 offset:25600\n"
    "ds_read_b64_tr_b16 v[158:159], v3 offset:25600\n"
    "v_mfma_f32_16x16x32_bf16 v[216:219], v[108:111], v[180:183], v[216:219]\n"
    "ds_read_b64_tr_b16 v[160:161], v2 offset:26624\n"
    "ds_read_b64_tr_b16 v[162:163], v3 offset:26624\n"
    "v_mfma_f32_16x16x32_bf16 v[220:223], v[132:135], v[180:183], v[220:223]\n"
    "ds_read_b64_tr_b16 v[164:165], v2 offset:27648\n"
    "ds_read_b64_tr_b16 v[166:167], v3 offset:27648\n"
    "v_mfma_f32_16x16x32_bf16 v[216:219], v[112:115], v[184:187], v[216:219]\n"
    "buffer_store_dwordx4 v[200:203], v5, s[80:83], 0 offen offset:0 sc1\n"
    "v_mfma_f32_16x16x32_bf16 v[220:223], v[136:139], v[184:187], v[220:223]\n"
    "v_mul_f32 v208, v208, v253\n"
    "v_mul_f32 v209, v209, v253\n"
    "v_mfma_f32_16x16x32_bf16 v[216:219], v[116:119], v[188:191], v[216:219]\n"
    "ds_read_b64_tr_b16 v[168:169], v2 offset:28672\n"
    "ds_read_b64_tr_b16 v[170:171], v3 offset:28672\n"
    "v_mfma_f32_16x16x32_bf16 v[220:223], v[140:143], v[188:191], v[220:223]\n"
    "v_mul_f32 v210, v210, v253\n"
    "v_mul_f32 v211, v211, v253\n"
    "v_mfma_f32_16x16x32_bf16 v[216:219], v[120:123], v[192:195], v[216:219]\n"
    "ds_read_b64_tr_b16 v[172:173], v2 offset:29696\n"
    "ds_read_b64_tr_b16 v[174:175], v3 offset:29696\n"
    "v_mfma_f32_16x16x32_bf16 v[220:223], v[144:147], v[192:195], v[220:223]\n"
    "v_mul_f32 v212, v212, v253\n"
    "v_mov_b32 v227, 5\n"
    "v_mfma_f32_16x16x32_bf16 v[216:219], v[124:127], v[196:199], v[216:219]\n"
    "ds_read_b32 v252, v1 offset:64\n"
    "v_mul_f32 v213, v213, v253\n"
    "v_mfma_f32_16x16x32_bf16 v[220:223], v[148:151], v[196:199], v[220:223]\n"
    "v_mul_f32 v214, v214, v253\n"
    "v_mul_f32 v215, v215, v253\n"
    "ds_write_b32 v226, v227 offset:32\n"
    "v_cvt_pk_bf16_f32 v208, v208, v209\n"
    "v_cvt_pk_bf16_f32 v209, v210, v211\n"
    "v_cvt_pk_bf16_f32 v210, v212, v213\n"
    "v_cvt_pk_bf16_f32 v211, v214, v215\n"
    "buffer_store_dwordx4 v[208:211], v5, s[80:83], 0 offen offset:64 sc1\n"
    ".Lh7j:\n"
    "s_waitcnt lgkmcnt(2)\n"
    "v_mfma_f32_16x16x32_bf16 v[200:203], v[8:11], v[152:155], 0\n"
    "v_mul_f32 v216, v216, v253\n"
    "v_mul_f32 v217, v217, v253\n"
    "v_mfma_f32_16x16x32_bf16 v[204:207], v[32:35], v[152:155], 0\n"
    "v_mul_f32 v218, v218, v253\n"
    "v_mul_f32 v219, v219, v253\n"
    "v_mfma_f32_16x16x32_bf16 v[200:203], v[12:15], v[156:159], v[200:203]\n"
    "v_mul_f32 v220, v220, v253\n"
    "v_mul_f32 v221, v221, v253\n"
    "v_mfma_f32_16x16x32_bf16 v[204:207], v[36:39], v[156:159], v[204:207]\n"
    "v_mul_f32 v222, v222, v253\n"
    "v_mul_f32 v223, v223, v253\n"
    "v_mfma_f32_16x16x32_bf16 v[200:203], v[16:19], v[160:163], v[200:203]\n"
    "v_cvt_pk_bf16_f32 v216, v216, v217\n"
    "v_cvt_pk_bf16_f32 v217, v218, v219\n"
    "v_mfma_f32_16x16x32_bf16 v[204:207], v[40:43], v[160:163], v[204:207]\n"
    "v_cvt_pk_bf16_f32 v218, v220, v221\n"
    "v_cvt_pk_bf16_f32 v219, v222, v223\n"
    "v_mfma_f32_16x16x32_bf16 v[200:203], v[20:23], v[164:167], v[200:203]\n"
    "buffer_store_dwordx4 v[216:219], v5, s[80:83], 0 offen offset:128 sc1\n"
    "v_mfma_f32_16x16x32_bf16 v[204:207], v[44:47], v[164:167], v[204:207]\n"
    "v_mfma_f32_16x16x32_bf16 v[200:203], v[24:27], v[168:171], v[200:203]\n"
    "v_add_u32 v5, s46, v5\n"
    "v_mfma_f32_16x16x32_bf16 v[204:207], v[48:51], v[168:171], v[204:207]\n"
    "v_mfma_f32_16x16x32_bf16 v[200:203], v[28:31], v[172:175], v[200:203]\n"
    "v_mfma_f32_16x16x32_bf16 v[204:207], v[52:55], v[172:175], v[204:207]\n"
    "v_mfma_f32_16x16x32_bf16 v[208:211], v[56:59], v[152:155], 0\n"
    "v_mfma_f32_16x16x32_bf16 v[212:215], v[80:83], v[152:155], 0\n"
    "ds_read_b32 v228, v224 offset:144\n"
    "v_mfma_f32_16x16x32_bf16 v[208:211], v[60:63], v[156:159], v[208:211]\n"
    "v_mfma_f32_16x16x32_bf16 v[212:215], v[84:87], v[156:159], v[212:215]\n"
    "v_mfma_f32_16x16x32_bf16 v[208:211], v[64:67], v[160:163], v[208:211]\n"
    "s_waitcnt lgkmcnt(2)\n"
    "v_mul_f32 v200, v200, v252\n"
    "v_mfma_f32_16x16x32_bf16 v[212:215], v[88:91], v[160:163], v[212:215]\n"
    "v_mul_f32 v201, v201, v252\n"
    "v_mul_f32 v202, v202, v252\n"
    "v_mfma_f32_16x16x32_bf16 v[208:211], v[68:71], v[164:167], v[208:211]\n"
    "v_mul_f32 v203, v203, v252\n"
    "v_mul_f32 v204, v204, v252\n"
    "v_mfma_f32_16x16x32_bf16 v[212:215], v[92:95], v[164:167], v[212:215]\n"
    "v_mul_f32 v205, v205, v252\n"
    "v_mul_f32 v206, v206, v252\n"
    "v_mfma_f32_16x16x32_bf16 v[208:211], v[72:75], v[168:171], v[208:211]\n"
    "v_mul_f32 v207, v207, v252\n"
    "v_cvt_pk_bf16_f32 v200, v200, v201\n"
    "v_mfma_f32_16x16x32_bf16 v[212:215], v[96:99], v[168:171], v[212:215]\n"
    "v_cvt_pk_bf16_f32 v201, v202, v203\n"
    "v_cvt_pk_bf16_f32 v202, v204, v205\n"
    "v_mfma_f32_16x16x32_bf16 v[208:211], v[76:79], v[172:175], v[208:211]\n"
    "s_waitcnt lgkmcnt(0)\n"
    "v_cmp_gt_u32 vcc, 2, v228\n"
    "v_mfma_f32_16x16x32_bf16 v[212:215], v[100:103], v[172:175], v[212:215]\n"
    "v_cvt_pk_bf16_f32 v203, v206, v207\n"
    "s_cbranch_vccnz .Lh8\n"
    "v_mfma_f32_16x16x32_bf16 v[216:219], v[104:107], v[152:155], 0\n"
    "ds_read_b64_tr_b16 v[176:177], v2 offset:30720\n"
    "ds_read_b64_tr_b16 v[178:179], v3 offset:30720\n"
    "v_mfma_f32_16x16x32_bf16 v[220:223], v[128:131], v[152:155], 0\n"
    "ds_read_b64_tr_b16 v[180:181], v2 offset:31744\n"
    "ds_read_b64_tr_b16 v[182:183], v3 offset:31744\n"
    "v_mfma_f32_16x16x32_bf16 v[216:219], v[108:111], v[156:159], v[216:219]\n"
    "ds_read_b64_tr_b16 v[184:185], v2 offset:32768\n"
    "ds_read_b64_tr_b16 v[186:187], v3 offset:32768\n"
    "v_mfma_f32_16x16x32_bf16 v[220:223], v[132:135], v[156:159], v[220:223]\n"
    "ds_read_b64_tr_b16 v[188:189], v2 offset:33792\n"
    "ds_read_b64_tr_b16 v[190:191], v3 offset:33792\n"
    "v_mfma_f32_16x16x32_bf16 v[216:219], v[112:115], v[160:163], v[216:219]\n"
    "buffer_store_dwordx4 v[200:203], v4, s[80:83], 0 offen offset:0 sc1\n"
    "v_mfma_f32_16x16x32_bf16 v[220:223], v[136:139], v[160:163], v[220:223]\n"
    "v_mul_f32 v208, v208, v252\n"
    "v_mul_f32 v209, v209, v252\n"
    "v_mfma_f32_16x16x32_bf16 v[216:219], v[116:119], v[164:167], v[216:219]\n"
    "ds_read_b64_tr_b16 v[192:193], v2 offset:34816\n"
    "ds_read_b64_tr_b16 v[194:195], v3 offset:34816\n"
    "v_mfma_f32_16x16x32_bf16 v[220:223], v[140:143], v[164:167], v[220:223]\n"
    "v_mul_f32 v210, v210, v252\n"
    "v_mul_f32 v211, v211, v252\n"
    "v_mfma_f32_16x16x32_bf16 v[216:219], v[120:123], v[168:171], v[216:219]\n"
    "ds_read_b64_tr_b16 v[196:197], v2 offset:35840\n"
    "ds_read_b64_tr_b16 v[198:199], v3 offset:35840\n"
    "v_mfma_f32_16x16x32_bf16 v[220:223], v[144:147], v[168:171], v[220:223]\n"
    "v_mul_f32 v212, v212, v252\n"
    "v_mov_b32 v227, 6\n"
    "v_mfma_f32_16x16x32_bf16 v[216:219], v[124:127], v[172:175], v[216:219]\n"
    "ds_read_b32 v253, v1 offset:128\n"
    "v_mul_f32 v213, v213, v252\n"
    "v_mfma_f32_16x16x32_bf16 v[220:223], v[148:151], v[172:175], v[220:223]\n"
    "v_mul_f32 v214, v214, v252\n"
    "v_mul_f32 v215, v215, v252\n"
    "ds_write_b32 v226, v227 offset:32\n"
    "v_cvt_pk_bf16_f32 v208, v208, v209\n"
    "v_cvt_pk_bf16_f32 v209, v210, v211\n"
    "v_cvt_pk_bf16_f32 v210, v212, v213\n"
    "v_cvt_pk_bf16_f32 v211, v214, v215\n"
    "buffer_store_dwordx4 v[208:211], v4, s[80:83], 0 offen offset:64 sc1\n"
    ".Lh8j:\n"
    "s_waitcnt lgkmcnt(2)\n"
    "v_mfma_f32_16x16x32_bf16 v[200:203], v[8:11], v[176:179], 0\n"
    "v_mul_f32 v216, v216, v252\n"
    "v_mul_f32 v217, v217, v252\n"
    "v_mfma_f32_16x16x32_bf16 v[204:207], v[32:35], v[176:179], 0\n"
    "v_mul_f32 v218, v218, v252\n"
    "v_mul_f32 v219, v219, v252\n"
    "v_mfma_f32_16x16x32_bf16 v[200:203], v[12:15], v[180:183], v[200:203]\n"
    "v_mul_f32 v220, v220, v252\n"
    "v_mul_f32 v221, v221, v252\n"
    "v_mfma_f32_16x16x32_bf16 v[204:207], v[36:39], v[180:183], v[204:207]\n"
    "v_mul_f32 v222, v222, v252\n"
    "v_mul_f32 v223, v223, v252\n"
    "v_mfma_f32_16x16x32_bf16 v[200:203], v[16:19], v[184:187], v[200:203]\n"
    "v_cvt_pk_bf16_f32 v216, v216, v217\n"
    "v_cvt_pk_bf16_f32 v217, v218, v219\n"
    "v_mfma_f32_16x16x32_bf16 v[204:207], v[40:43], v[184:187], v[204:207]\n"
    "v_cvt_pk_bf16_f32 v218, v220, v221\n"
    "v_cvt_pk_bf16_f32 v219, v222, v223\n"
    "v_mfma_f32_16x16x32_bf16 v[200:203], v[20:23], v[188:191], v[200:203]\n"
    "buffer_store_dwordx4 v[216:219], v4, s[80:83], 0 offen offset:128 sc1\n"
    "v_mfma_f32_16x16x32_bf16 v[204:207], v[44:47], v[188:191], v[204:207]\n"
    "v_mfma_f32_16x16x32_bf16 v[200:203], v[24:27], v[192:195], v[200:203]\n"
    "v_add_u32 v4, s46, v4\n"
    "v_mfma_f32_16x16x32_bf16 v[204:207], v[48:51], v[192:195], v[204:207]\n"
    "v_mfma_f32_16x16x32_bf16 v[200:203], v[28:31], v[196:199], v[200:203]\n"
    "v_mfma_f32_16x16x32_bf16 v[204:207], v[52:55], v[196:199], v[204:207]\n"
    "v_mfma_f32_16x16x32_bf16 v[208:211], v[56:59], v[176:179], 0\n"
    "v_mfma_f32_16x16x32_bf16 v[212:215], v[80:83], v[176:179], 0\n"
    "ds_read_b32 v228, v224 offset:160\n"
    "v_mfma_f32_16x16x32_bf16 v[208:211], v[60:63], v[180:183], v[208:211]\n"
    "v_mfma_f32_16x16x32_bf16 v[212:215], v[84:87], v[180:183], v[212:215]\n"
    "v_mfma_f32_16x16x32_bf16 v[208:211], v[64:67], v[184:187], v[208:211]\n"
    "s_waitcnt lgkmcnt(2)\n"
    "v_mul_f32 v200, v200, v253\n"
    "v_mfma_f32_16x16x32_bf16 v[212:215], v[88:91], v[184:187], v[212:215]\n"
    "v_mul_f32 v201, v201, v253\n"
    "v_mul_f32 v202, v202, v253\n"
    "v_mfma_f32_16x16x32_bf16 v[208:211], v[68:71], v[188:191], v[208:211]\n"
    "v_mul_f32 v203, v203, v253\n"
    "v_mul_f32 v204, v204, v253\n"
    "v_mfma_f32_16x16x32_bf16 v[212:215], v[92:95], v[188:191], v[212:215]\n"
    "v_mul_f32 v205, v205, v253\n"
    "v_mul_f32 v206, v206, v253\n"
    "v_mfma_f32_16x16x32_bf16 v[208:211], v[72:75], v[192:195], v[208:211]\n"
    "v_mul_f32 v207, v207, v253\n"
    "v_cvt_pk_bf16_f32 v200, v200, v201\n"
    "v_mfma_f32_16x16x32_bf16 v[212:215], v[96:99], v[192:195], v[212:215]\n"
    "v_cvt_pk_bf16_f32 v201, v202, v203\n"
    "v_cvt_pk_bf16_f32 v202, v204, v205\n"
    "v_mfma_f32_16x16x32_bf16 v[208:211], v[76:79], v[196:199], v[208:211]\n"
    "s_waitcnt lgkmcnt(0)\n"
    "v_cmp_gt_u32 vcc, 2, v228\n"
    "v_mfma_f32_16x16x32_bf16 v[212:215], v[100:103], v[196:199], v[212:215]\n"
    "v_cvt_pk_bf16_f32 v203, v206, v207\n"
    "s_cbranch_vccnz .Lh9\n"
    "v_mfma_f32_16x16x32_bf16 v[216:219], v[104:107], v[176:179], 0\n"
    "ds_read_b64_tr_b16 v[152:153], v2 offset:18432\n"
    "ds_read_b64_tr_b16 v[154:155], v3 offset:18432\n"
    "v_mfma_f32_16x16x32_bf16 v[220:223], v[128:131], v[176:179], 0\n"
    "ds_read_b64_tr_b16 v[156:157], v2 offset:19456\n"
    "ds_read_b64_tr_b16 v[158:159], v3 offset:19456\n"
    "v_mfma_f32_16x16x32_bf16 v[216:219], v[108:111], v[180:183], v[216:219]\n"
    "ds_read_b64_tr_b16 v[160:161], v2 offset:20480\n"
    "ds_read_b64_tr_b16 v[162:163], v3 offset:20480\n"
    "v_mfma_f32_16x16x32_bf16 v[220:223], v[132:135], v[180:183], v[220:223]\n"
    "ds_read_b64_tr_b16 v[164:165], v2 offset:21504\n"
    "ds_read_b64_tr_b16 v[166:167], v3 offset:21504\n"
    "v_mfma_f32_16x16x32_bf16 v[216:219], v[112:115], v[184:187], v[216:219]\n"
    "buffer_store_dwordx4 v[200:203], v5, s[80:83], 0 offen offset:0 sc1\n"
    "v_mfma_f32_16x16x32_bf16 v[220:223], v[136:139], v[184:187], v[220:223]\n"
    "v_mul_f32 v208, v208, v253\n"
    "v_mul_f32 v209, v209, v253\n"
    "v_mfma_f32_16x16x32_bf16 v[216:219], v[116:119], v[188:191], v[216:219]\n"
    "ds_read_b64_tr_b16 v[168:169], v2 offset:22528\n"
    "ds_read_b64_tr_b16 v[170:171], v3 offset:22528\n"
    "v_mfma_f32_16x16x32_bf16 v[220:223], v[140:143], v[188:191], v[220:223]\n"
    "v_mul_f32 v210, v210, v253\n"
    "v_mul_f32 v211, v211, v253\n"
    "v_mfma_f32_16x16x32_bf16 v[216:219], v[120:123], v[192:195], v[216:219]\n"
    "ds_read_b64_tr_b16 v[172:173], v2 offset:23552\n"
    "ds_read_b64_tr_b16 v[174:175], v3 offset:23552\n"
    "v_mfma_f32_16x16x32_bf16 v[220:223], v[144:147], v[192:195], v[220:223]\n"
    "v_mul_f32 v212, v212, v253\n"
    "v_mov_b32 v227, 7\n"
    "v_mfma_f32_16x16x32_bf16 v[216:219], v[124:127], v[196:199], v[216:219]\n"
    "ds_read_b32 v252, v1 offset:0\n"
    "v_mul_f32 v213, v213, v253\n"
    "v_mfma_f32_16x16x32_bf16 v[220:223], v[148:151], v[196:199], v[220:223]\n"
    "v_mul_f32 v214, v214, v253\n"
    "v_mul_f32 v215, v215, v253\n"
    "ds_write_b32 v226, v227 offset:32\n"
    "v_cvt_pk_bf16_f32 v208, v208, v209\n"
    "v_cvt_pk_bf16_f32 v209, v210, v211\n"
    "v_cvt_pk_bf16_f32 v210, v212, v213\n"
    "v_cvt_pk_bf16_f32 v211, v214, v215\n"
    "buffer_store_dwordx4 v[208:211], v5, s[80:83], 0 offen offset:64 sc1\n"
    ".Lh9j:\n"
    "s_waitcnt lgkmcnt(2)\n"
    "v_mfma_f32_16x16x32_bf16 v[200:203], v[8:11], v[152:155], 0\n"
    "v_mul_f32 v216, v216, v253\n"
    "v_mul_f32 v217, v217, v253\n"
    "v_mfma_f32_16x16x32_bf16 v[204:207], v[32:35], v[152:155], 0\n"
    "v_mul_f32 v218, v218, v253\n"
    "v_mul_f32 v219, v219, v253\n"
    "v_mfma_f32_16x16x32_bf16 v[200:203], v[12:15], v[156:159], v[200:203]\n"
    "v_mul_f32 v220, v220, v253\n"
    "v_mul_f32 v221, v221, v253\n"
    "v_mfma_f32_16x16x32_bf16 v[204:207], v[36:39], v[156:159], v[204:207]\n"
    "v_mul_f32 v222, v222, v253\n"
    "v_mul_f32 v223, v223, v253\n"
    "v_mfma_f32_16x16x32_bf16 v[200:203], v[16:19], v[160:163], v[200:203]\n"
    "v_cvt_pk_bf16_f32 v216, v216, v217\n"
    "v_cvt_pk_bf16_f32 v217, v218, v219\n"
    "v_mfma_f32_16x16x32_bf16 v[204:207], v[40:43], v[160:163], v[204:207]\n"
    "v_cvt_pk_bf16_f32 v218, v220, v221\n"
    "v_cvt_pk_bf16_f32 v219, v222, v223\n"
    "v_mfma_f32_16x16x32_bf16 v[200:203], v[20:23], v[164:167], v[200:203]\n"
    "buffer_store_dwordx4 v[216:219], v5, s[80:83], 0 offen offset:128 sc1\n"
    "v_mfma_f32_16x16x32_bf16 v[204:207], v[44:47], v[164:167], v[204:207]\n"
    "v_mfma_f32_16x16x32_bf16 v[200:203], v[24:27], v[168:171], v[200:203]\n"
    "v_add_u32 v5, s46, v5\n"
    "v_mfma_f32_16x16x32_bf16 v[204:207], v[48:51], v[168:171], v[204:207]\n"
    "v_mfma_f32_16x16x32_bf16 v[200:203], v[28:31], v[172:175], v[200:203]\n"
    "v_mfma_f32_16x16x32_bf16 v[204:207], v[52:55], v[172:175], v[204:207]\n"
    "v_mfma_f32_16x16x32_bf16 v[208:211], v[56:59], v[152:155], 0\n"
    "v_mfma_f32_16x16x32_bf16 v[212:215], v[80:83], v[152:155], 0\n"
    "ds_read_b32 v228, v224 offset:176\n"
    "v_mfma_f32_16x16x32_bf16 v[208:211], v[60:63], v[156:159], v[208:211]\n"
    "v_mfma_f32_16x16x32_bf16 v[212:215], v[84:87], v[156:159], v[212:215]\n"
    "v_mfma_f32_16x16x32_bf16 v[208:211], v[64:67], v[160:163], v[208:211]\n"
    "s_waitcnt lgkmcnt(2)\n"
    "v_mul_f32 v200, v200, v252\n"
    "v_mfma_f32_16x16x32_bf16 v[212:215], v[88:91], v[160:163], v[212:215]\n"
    "v_mul_f32 v201, v201, v252\n"
    "v_mul_f32 v202, v202, v252\n"
    "v_mfma_f32_16x16x32_bf16 v[208:211], v[68:71], v[164:167], v[208:211]\n"
    "v_mul_f32 v203, v203, v252\n"
    "v_mul_f32 v204, v204, v252\n"
    "v_mfma_f32_16x16x32_bf16 v[212:215], v[92:95], v[164:167], v[212:215]\n"
    "v_mul_f32 v205, v205, v252\n"
    "v_mul_f32 v206, v206, v252\n"
    "v_mfma_f32_16x16x32_bf16 v[208:211], v[72:75], v[168:171], v[208:211]\n"
    "v_mul_f32 v207, v207, v252\n"
    "v_cvt_pk_bf16_f32 v200, v200, v201\n"
    "v_mfma_f32_16x16x32_bf16 v[212:215], v[96:99], v[168:171], v[212:215]\n"
    "v_cvt_pk_bf16_f32 v201, v202, v203\n"
    "v_cvt_pk_bf16_f32 v202, v204, v205\n"
    "v_mfma_f32_16x16x32_bf16 v[208:211], v[76:79], v[172:175], v[208:211]\n"
    "s_waitcnt lgkmcnt(0)\n"
    "v_cmp_gt_u32 vcc, 2, v228\n"
    "v_mfma_f32_16x16x32_bf16 v[212:215], v[100:103], v[172:175], v[212:215]\n"
    "v_cvt_pk_bf16_f32 v203, v206, v207\n"
    "s_cbranch_vccnz .Lh10\n"
    "v_mfma_f32_16x16x32_bf16 v[216:219], v[104:107], v[152:155], 0\n"
    "ds_read_b64_tr_b16 v[176:177], v2 offset:24576\n"
    "ds_read_b64_tr_b16 v[178:179], v3 offset:24576\n"
    "v_mfma_f32_16x16x32_bf16 v[220:223], v[128:131], v[152:155], 0\n"
    "ds_read_b64_tr_b16 v[180:181], v2 offset:25600\n"
    "ds_read_b64_tr_b16 v[182:183], v3 offset:25600\n"
    "v_mfma_f32_16x16x32_bf16 v[216:219], v[108:111], v[156:159], v[216:219]\n"
    "ds_read_b64_tr_b16 v[184:185], v2 offset:26624\n"
    "ds_read_b64_tr_b16 v[186:187], v3 offset:26624\n"
    "v_mfma_f32_16x16x32_bf16 v[220:223], v[132:135], v[156:159], v[220:223]\n"
    "ds_read_b64_tr_b16 v[188:189], v2 offset:27648\n"
    "ds_read_b64_tr_b16 v[190:191], v3 offset:27648\n"
    "v_mfma_f32_16x16x32_bf16 v[216:219], v[112:115], v[160:163], v[216:219]\n"
    "buffer_store_dwordx4 v[200:203], v4, s[80:83], 0 offen offset:0 sc1\n"
    "v_mfma_f32_16x16x32_bf16 v[220:223], v[136:139], v[160:163], v[220:223]\n"
    "v_mul_f32 v208, v208, v252\n"
    "v_mul_f32 v209, v209, v252\n"
    "v_mfma_f32_16x16x32_bf16 v[216:219], v[116:119], v[164:167], v[216:219]\n"
    "ds_read_b64_tr_b16 v[192:193], v2 offset:28672\n"
    "ds_read_b64_tr_b16 v[194:195], v3 offset:28672\n"
    "v_mfma_f32_16x16x32_bf16 v[220:223], v[140:143], v[164:167], v[220:223]\n"
    "v_mul_f32 v210, v210, v252\n"
    "v_mul_f32 v211, v211, v252\n"
    "v_mfma_f32_16x16x32_bf16 v[216:219], v[120:123], v[168:171], v[216:219]\n"
    "ds_read_b64_tr_b16 v[196:197], v2 offset:29696\n"
    "ds_read_b64_tr_b16 v[198:199], v3 offset:29696\n"
    "v_mfma_f32_16x16x32_bf16 v[220:223], v[144:147], v[168:171], v[220:223]\n"
    "v_mul_f32 v212, v212, v252\n"
    "v_mul_f32 v213, v213, v252\n"
    "v_mfma_f32_16x16x32_bf16 v[216:219], v[124:127], v[172:175], v[216:219]\n"
    "ds_read_b32 v253, v1 offset:64\n"
    "v_mul_f32 v214, v214, v252\n"
    "v_mfma_f32_16x16x32_bf16 v[220:223], v[148:151], v[172:175], v[220:223]\n"
    "v_mul_f32 v215, v215, v252\n"
    "v_cvt_pk_bf16_f32 v208, v208, v209\n"
    "v_cvt_pk_bf16_f32 v209, v210, v211\n"
    "v_cvt_pk_bf16_f32 v210, v212, v213\n"
    "v_cvt_pk_bf16_f32 v211, v214, v215\n"
    "buffer_store_dwordx4 v[208:211], v4, s[80:83], 0 offen offset:64 sc1\n"
    ".Lh10j:\n"
    "s_waitcnt lgkmcnt(1)\n"
    "v_mfma_f32_16x16x32_bf16 v[200:203], v[8:11], v[176:179], 0\n"
    "v_mul_f32 v216, v216, v252\n"
    "v_mul_f32 v217, v217, v252\n"
    "v_mfma_f32_16x16x32_bf16 v[204:207], v[32:35], v[176:179], 0\n"
    "v_mul_f32 v218, v218, v252\n"
    "v_mul_f32 v219, v219, v252\n"
    "v_mfma_f32_16x16x32_bf16 v[200:203], v[12:15], v[180:183], v[200:203]\n"
    "v_mul_f32 v220, v220, v252\n"
    "v_mul_f32 v221, v221, v252\n"
    "v_mfma_f32_16x16x32_bf16 v[204:207], v[36:39], v[180:183], v[204:207]\n"
    "v_mul_f32 v222, v222, v252\n"
    "v_mul_f32 v223, v223, v252\n"
    "v_mfma_f32_16x16x32_bf16 v[200:203], v[16:19], v[184:187], v[200:203]\n"
    "v_cvt_pk_bf16_f32 v216, v216, v217\n"
    "v_cvt_pk_bf16_f32 v217, v218, v219\n"
    "v_mfma_f32_16x16x32_bf16 v[204:207], v[40:43], v[184:187], v[204:207]\n"
    "v_cvt_pk_bf16_f32 v218, v220, v221\n"
    "v_cvt_pk_bf16_f32 v219, v222, v223\n"
    "v_mfma_f32_16x16x32_bf16 v[200:203], v[20:23], v[188:191], v[200:203]\n"
    "buffer_store_dwordx4 v[216:219], v4, s[80:83], 0 offen offset:128 sc1\n"
    "v_mfma_f32_16x16x32_bf16 v[204:207], v[44:47], v[188:191], v[204:207]\n"
    "v_mfma_f32_16x16x32_bf16 v[200:203], v[24:27], v[192:195], v[200:203]\n"
    "v_add_u32 v4, v6, v4\n"
    "v_mfma_f32_16x16x32_bf16 v[204:207], v[48:51], v[192:195], v[204:207]\n"
    "v_mfma_f32_16x16x32_bf16 v[200:203], v[28:31], v[196:199], v[200:203]\n"
    "v_mfma_f32_16x16x32_bf16 v[204:207], v[52:55], v[196:199], v[204:207]\n"
    "v_mfma_f32_16x16x32_bf16 v[208:211], v[56:59], v[176:179], 0\n"
    "v_mfma_f32_16x16x32_bf16 v[212:215], v[80:83], v[176:179], 0\n"
    "ds_read_b32 v228, v224 offset:128\n"
    "v_mfma_f32_16x16x32_bf16 v[208:211], v[60:63], v[180:183], v[208:211]\n"
    "v_mfma_f32_16x16x32_bf16 v[212:215], v[84:87], v[180:183], v[212:215]\n"
    "v_mfma_f32_16x16x32_bf16 v[208:211], v[64:67], v[184:187], v[208:211]\n"
    "s_waitcnt lgkmcnt(1)\n"
    "v_mul_f32 v200, v200, v253\n"
    "v_mfma_f32_16x16x32_bf16 v[212:215], v[88:91], v[184:187], v[212:215]\n"
    "v_mul_f32 v201, v201, v253\n"
    "v_mul_f32 v202, v202, v253\n"
    "v_mfma_f32_16x16x32_bf16 v[208:211], v[68:71], v[188:191], v[208:211]\n"
    "v_mul_f32 v203, v203, v253\n"
    "v_mul_f32 v204, v204, v253\n"
    "v_mfma_f32_16x16x32_bf16 v[212:215], v[92:95], v[188:191], v[212:215]\n"
    "v_mul_f32 v205, v205, v253\n"
    "v_mul_f32 v206, v206, v253\n"
    "v_mfma_f32_16x16x32_bf16 v[208:211], v[72:75], v[192:195], v[208:211]\n"
    "v_mul_f32 v207, v207, v253\n"
    "v_cvt_pk_bf16_f32 v200, v200, v201\n"
    "v_mfma_f32_16x16x32_bf16 v[212:215], v[96:99], v[192:195], v[212:215]\n"
    "v_cvt_pk_bf16_f32 v201, v202, v203\n"
    "v_cvt_pk_bf16_f32 v202, v204, v205\n"
    "v_mfma_f32_16x16x32_bf16 v[208:211], v[76:79], v[196:199], v[208:211]\n"
    "s_waitcnt lgkmcnt(0)\n"
    "v_cmp_gt_u32 vcc, 3, v228\n"
    "v_mfma_f32_16x16x32_bf16 v[212:215], v[100:103], v[196:199], v[212:215]\n"
    "v_cvt_pk_bf16_f32 v203, v206, v207\n"
    "s_cbranch_vccnz .Lh11\n"
    "v_mfma_f32_16x16x32_bf16 v[216:219], v[104:107], v[176:179], 0\n"
    "ds_read_b64_tr_b16 v[152:153], v2 offset:30720\n"
    "ds_read_b64_tr_b16 v[154:155], v3 offset:30720\n"
    "v_mfma_f32_16x16x32_bf16 v[220:223], v[128:131], v[176:179], 0\n"
    "ds_read_b64_tr_b16 v[156:157], v2 offset:31744\n"
    "ds_read_b64_tr_b16 v[158:159], v3 offset:31744\n"
    "v_mfma_f32_16x16x32_bf16 v[216:219], v[108:111], v[180:183], v[216:219]\n"
    "ds_read_b64_tr_b16 v[160:161], v2 offset:32768\n"
    "ds_read_b64_tr_b16 v[162:163], v3 offset:32768\n"
    "v_mfma_f32_16x16x32_bf16 v[220:223], v[132:135], v[180:183], v[220:223]\n"
    "ds_read_b64_tr_b16 v[164:165], v2 offset:33792\n"
    "ds_read_b64_tr_b16 v[166:167], v3 offset:33792\n"
    "v_mfma_f32_16x16x32_bf16 v[216:219], v[112:115], v[184:187], v[216:219]\n"
    "buffer_store_dwordx4 v[200:203], v5, s[80:83], 0 offen offset:0 sc1\n"
    "v_mfma_f32_16x16x32_bf16 v[220:223], v[136:139], v[184:187], v[220:223]\n"
    "v_mul_f32 v208, v208, v253\n"
    "v_mul_f32 v209, v209, v253\n"
    "v_mfma_f32_16x16x32_bf16 v[216:219], v[116:119], v[188:191], v[216:219]\n"
    "ds_read_b64_tr_b16 v[168:169], v2 offset:34816\n"
    "ds_read_b64_tr_b16 v[170:171], v3 offset:34816\n"
    "v_mfma_f32_16x16x32_bf16 v[220:223], v[140:143], v[188:191], v[220:223]\n"
    "v_mul_f32 v210, v210, v253\n"
    "v_mul_f32 v211, v211, v253\n"
    "v_mfma_f32_16x16x32_bf16 v[216:219], v[120:123], v[192:195], v[216:219]\n"
    "ds_read_b64_tr_b16 v[172:173], v2 offset:35840\n"
    "ds_read_b64_tr_b16 v[174:175], v3 offset:35840\n"
    "v_mfma_f32_16x16x32_bf16 v[220:223], v[144:147], v[192:195], v[220:223]\n"
    "v_mul_f32 v212, v212, v253\n"
    "v_mul_f32 v213, v213, v253\n"
    "v_mfma_f32_16x16x32_bf16 v[216:219], v[124:127], v[196:199], v[216:219]\n"
    "ds_read_b32 v252, v1 offset:128\n"
    "v_mul_f32 v214, v214, v253\n"
    "v_mfma_f32_16x16x32_bf16 v[220:223], v[148:151], v[196:199], v[220:223]\n"
    "v_mul_f32 v215, v215, v253\n"
    "v_cvt_pk_bf16_f32 v208, v208, v209\n"
    "v_cvt_pk_bf16_f32 v209, v210, v211\n"
    "v_cvt_pk_bf16_f32 v210, v212, v213\n"
    "v_cvt_pk_bf16_f32 v211, v214, v215\n"
    "buffer_store_dwordx4 v[208:211], v5, s[80:83], 0 offen offset:64 sc1\n"
    ".Lh11j:\n"
    "s_waitcnt lgkmcnt(1)\n"
    "v_mfma_f32_16x16x32_bf16 v[200:203], v[8:11], v[152:155], 0\n"
    "v_mul_f32 v216, v216, v253\n"
    "v_mul_f32 v217, v217, v253\n"
    "v_mfma_f32_16x16x32_bf16 v[204:207], v[32:35], v[152:155], 0\n"
    "v_mul_f32 v218, v218, v253\n"
    "v_mul_f32 v219, v219, v253\n"
    "v_mfma_f32_16x16x32_bf16 v[200:203], v[12:15], v[156:159], v[200:203]\n"
    "v_mul_f32 v220, v220, v253\n"
    "v_mul_f32 v221, v221, v253\n"
    "v_mfma_f32_16x16x32_bf16 v[204:207], v[36:39], v[156:159], v[204:207]\n"
    "v_mul_f32 v222, v222, v253\n"
    "v_mul_f32 v223, v223, v253\n"
    "v_mfma_f32_16x16x32_bf16 v[200:203], v[16:19], v[160:163], v[200:203]\n"
    "v_cvt_pk_bf16_f32 v216, v216, v217\n"
    "v_cvt_pk_bf16_f32 v217, v218, v219\n"
    "v_mfma_f32_16x16x32_bf16 v[204:207], v[40:43], v[160:163], v[204:207]\n"
    "v_cvt_pk_bf16_f32 v218, v220, v221\n"
    "v_cvt_pk_bf16_f32 v219, v222, v223\n"
    "v_mfma_f32_16x16x32_bf16 v[200:203], v[20:23], v[164:167], v[200:203]\n"
    "buffer_store_dwordx4 v[216:219], v5, s[80:83], 0 offen offset:128 sc1\n"
    "v_mfma_f32_16x16x32_bf16 v[204:207], v[44:47], v[164:167], v[204:207]\n"
    "v_mfma_f32_16x16x32_bf16 v[200:203], v[24:27], v[168:171], v[200:203]\n"
    "v_add_u32 v5, v6, v5\n"
    "v_mfma_f32_16x16x32_bf16 v[204:207], v[48:51], v[168:171], v[204:207]\n"
    "v_mfma_f32_16x16x32_bf16 v[200:203], v[28:31], v[172:175], v[200:203]\n"
    "v_mfma_f32_16x16x32_bf16 v[204:207], v[52:55], v[172:175], v[204:207]\n"
    "v_mfma_f32_16x16x32_bf16 v[208:211], v[56:59], v[152:155], 0\n"
    "v_mfma_f32_16x16x32_bf16 v[212:215], v[80:83], v[152:155], 0\n"
    "ds_read_b32 v228, v224 offset:144\n"
    "v_mfma_f32_16x16x32_bf16 v[208:211], v[60:63], v[156:159], v[208:211]\n"
    "v_mfma_f32_16x16x32_bf16 v[212:215], v[84:87], v[156:159], v[212:215]\n"
    "v_mfma_f32_16x16x32_bf16 v[208:211], v[64:67], v[160:163], v[208:211]\n"
    "s_waitcnt lgkmcnt(1)\n"
    "v_mul_f32 v200, v200, v252\n"
    "v_mfma_f32_16x16x32_bf16 v[212:215], v[88:91], v[160:163], v[212:215]\n"
    "v_mul_f32 v201, v201, v252\n"
    "v_mul_f32 v202, v202, v252\n"
    "v_mfma_f32_16x16x32_bf16 v[208:211], v[68:71], v[164:167], v[208:211]\n"
    "v_mul_f32 v203, v203, v252\n"
    "v_mul_f32 v204, v204, v252\n"
    "v_mfma_f32_16x16x32_bf16 v[212:215], v[92:95], v[164:167], v[212:215]\n"
    "v_mul_f32 v205, v205, v252\n"
    "v_mul_f32 v206, v206, v252\n"
    "v_mfma_f32_16x16x32_bf16 v[208:211], v[72:75], v[168:171], v[208:211]\n"
    "v_mul_f32 v207, v207, v252\n"
    "v_cvt_pk_bf16_f32 v200, v200, v201\n"
    "v_mfma_f32_16x16x32_bf16 v[212:215], v[96:99], v[168:171], v[212:215]\n"
    "v_cvt_pk_bf16_f32 v201, v202, v203\n"
    "v_cvt_pk_bf16_f32 v202, v204, v205\n"
    "v_mfma_f32_16x16x32_bf16 v[208:211], v[76:79], v[172:175], v[208:211]\n"
    "s_waitcnt lgkmcnt(0)\n"
    "v_cmp_gt_u32 vcc, 3, v228\n"
    "v_mfma_f32_16x16x32_bf16 v[212:215], v[100:103], v[172:175], v[212:215]\n"
    "v_cvt_pk_bf16_f32 v203, v206, v207\n"
    "s_cbranch_vccnz .Lh12\n"
    "v_mfma_f32_16x16x32_bf16 v[216:219], v[104:107], v[152:155], 0\n"
    "ds_read_b64_tr_b16 v[176:177], v2 offset:18432\n"
    "ds_read_b64_tr_b16 v[178:179], v3 offset:18432\n"
    "v_mfma_f32_16x16x32_bf16 v[220:223], v[128:131], v[152:155], 0\n"
    "ds_read_b64_tr_b16 v[180:181], v2 offset:19456\n"
    "ds_read_b64_tr_b16 v[182:183], v3 offset:19456\n"
    "v_mfma_f32_16x16x32_bf16 v[216:219], v[108:111], v[156:159], v[216:219]\n"
    "ds_read_b64_tr_b16 v[184:185], v2 offset:20480\n"
    "ds_read_b64_tr_b16 v[186:187], v3 offset:20480\n"
    "v_mfma_f32_16x16x32_bf16 v[220:223], v[132:135], v[156:159], v[220:223]\n"
    "ds_read_b64_tr_b16 v[188:189], v2 offset:21504\n"
    "ds_read_b64_tr_b16 v[190:191], v3 offset:21504\n"
    "v_mfma_f32_16x16x32_bf16 v[216:219], v[112:115], v[160:163], v[216:219]\n"
    "buffer_store_dwordx4 v[200:203], v4, s[80:83], 0 offen offset:0 sc1\n"
    "v_mfma_f32_16x16x32_bf16 v[220:223], v[136:139], v[160:163], v[220:223]\n"
    "v_mul_f32 v208, v208, v252\n"
    "v_mul_f32 v209, v209, v252\n"
    "v_mfma_f32_16x16x32_bf16 v[216:219], v[116:119], v[164:167], v[216:219]\n"
    "ds_read_b64_tr_b16 v[192:193], v2 offset:22528\n"
    "ds_read_b64_tr_b16 v[194:195], v3 offset:22528\n"
    "v_mfma_f32_16x16x32_bf16 v[220:223], v[140:143], v[164:167], v[220:223]\n"
    "v_mul_f32 v210, v210, v252\n"
    "v_mul_f32 v211, v211, v252\n"
    "v_mfma_f32_16x16x32_bf16 v[216:219], v[120:123], v[168:171], v[216:219]\n"
    "ds_read_b64_tr_b16 v[196:197], v2 offset:23552\n"
    "ds_read_b64_tr_b16 v[198:199], v3 offset:23552\n"
    "v_mfma_f32_16x16x32_bf16 v[220:223], v[144:147], v[168:171], v[220:223]\n"
    "v_mul_f32 v212, v212, v252\n"
    "v_mul_f32 v213, v213, v252\n"
    "v_mfma_f32_16x16x32_bf16 v[216:219], v[124:127], v[172:175], v[216:219]\n"
    "ds_read_b32 v253, v1 offset:0\n"
    "v_mul_f32 v214, v214, v252\n"
    "v_mfma_f32_16x16x32_bf16 v[220:223], v[148:151], v[172:175], v[220:223]\n"
    "v_mul_f32 v215, v215, v252\n"
    "v_cvt_pk_bf16_f32 v208, v208, v209\n"
    "v_cvt_pk_bf16_f32 v209, v210, v211\n"
    "v_cvt_pk_bf16_f32 v210, v212, v213\n"
    "v_cvt_pk_bf16_f32 v211, v214, v215\n"
    "buffer_store_dwordx4 v[208:211], v4, s[80:83], 0 offen offset:64 sc1\n"
    ".Lh12j:\n"
    "s_waitcnt lgkmcnt(1)\n"
    "v_mfma_f32_16x16x32_bf16 v[200:203], v[8:11], v[176:179], 0\n"
    "v_mul_f32 v216, v216, v252\n"
    "v_mul_f32 v217, v217, v252\n"
    "v_mfma_f32_16x16x32_bf16 v[204:207], v[32:35], v[176:179], 0\n"
    "ds_read_b32 v228, v224 offset:160\n"
    "v_mul_f32 v218, v218, v252\n"
    "v_mfma_f32_16x16x32_bf16 v[200:203], v[12:15], v[180:183], v[200:203]\n"
    "v_mul_f32 v219, v219, v252\n"
    "v_mul_f32 v220, v220, v252\n"
    "v_mfma_f32_16x16x32_bf16 v[204:207], v[36:39], v[180:183], v[204:207]\n"
    "v_mul_f32 v221, v221, v252\n"
    "v_mul_f32 v222, v222, v252\n"
    "v_mfma_f32_16x16x32_bf16 v[200:203], v[16:19], v[184:187], v[200:203]\n"
    "v_mul_f32 v223, v223, v252\n"
    "v_cvt_pk_bf16_f32 v216, v216, v217\n"
    "v_mfma_f32_16x16x32_bf16 v[204:207], v[40:43], v[184:187], v[204:207]\n"
    "v_cvt_pk_bf16_f32 v217, v218, v219\n"
    "v_cvt_pk_bf16_f32 v218, v220, v221\n"
    "v_mfma_f32_16x16x32_bf16 v[200:203], v[20:23], v[188:191], v[200:203]\n"
    "v_cvt_pk_bf16_f32 v219, v222, v223\n"
    "v_mfma_f32_16x16x32_bf16 v[204:207], v[44:47], v[188:191], v[204:207]\n"
    "buffer_store_dwordx4 v[216:219], v4, s[80:83], 0 offen offset:128 sc1\n"
    "v_mfma_f32_16x16x32_bf16 v[200:203], v[24:27], v[192:195], v[200:203]\n"
    "v_mfma_f32_16x16x32_bf16 v[204:207], v[48:51], v[192:195], v[204:207]\n"
    "v_add_u32 v4, v7, v4\n"
    "v_mfma_f32_16x16x32_bf16 v[200:203], v[28:31], v[196:199], v[200:203]\n"
    "s_waitcnt lgkmcnt(0)\n"
    "v_cmp_gt_u32 vcc, 3, v228\n"
    "v_mfma_f32_16x16x32_bf16 v[204:207], v[52:55], v[196:199], v[204:207]\n"
    "s_cbranch_vccnz .Lh13\n"
    "v_mfma_f32_16x16x32_bf16 v[208:211], v[56:59], v[176:179], 0\n"
    "ds_read_b64_tr_b16 v[152:153], v2 offset:6144\n"
    "ds_read_b64_tr_b16 v[154:155], v3 offset:6144\n"
    "v_mfma_f32_16x16x32_bf16 v[212:215], v[80:83], v[176:179], 0\n"
    "ds_read_b64_tr_b16 v[156:157], v2 offset:7168\n"
    "ds_read_b64_tr_b16 v[158:159], v3 offset:7168\n"
    "v_mfma_f32_16x16x32_bf16 v[208:211], v[60:63], v[180:183], v[208:211]\n"
    "ds_read_b64_tr_b16 v[160:161], v2 offset:8192\n"
    "ds_read_b64_tr_b16 v[162:163], v3 offset:8192\n"
    "v_mfma_f32_16x16x32_bf16 v[212:215], v[84:87], v[180:183], v[212:215]\n"
    "ds_read_b64_tr_b16 v[164:165], v2 offset:9216\n"
    "ds_read_b64_tr_b16 v[166:167], v3 offset:9216\n"
    "v_mfma_f32_16x16x32_bf16 v[208:211], v[64:67], v[184:187], v[208:211]\n"
    "v_mul_f32 v200, v200, v253\n"
    "v_mul_f32 v201, v201, v253\n"
    "v_mfma_f32_16x16x32_bf16 v[212:215], v[88:91], v[184:187], v[212:215]\n"
    "ds_read_b64_tr_b16 v[168:169], v2 offset:10240\n"
    "ds_read_b64_tr_b16 v[170:171], v3 offset:10240\n"
    "v_mfma_f32_16x16x32_bf16 v[208:211], v[68:71], v[188:191], v[208:211]\n"
    "v_mul_f32 v202, v202, v253\n"
    "v_mul_f32 v203, v203, v253\n"
    "v_mfma_f32_16x16x32_bf16 v[212:215], v[92:95], v[188:191], v[212:215]\n"
    "ds_read_b64_tr_b16 v[172:173], v2 offset:11264\n"
    "ds_read_b64_tr_b16 v[174:175], v3 offset:11264\n"
    "v_mfma_f32_16x16x32_bf16 v[208:211], v[72:75], v[192:195], v[208:211]\n"
    "v_mul_f32 v204, v204, v253\n"
    "v_mul_f32 v205, v205, v253\n"
    "v_mfma_f32_16x16x32_bf16 v[212:215], v[96:99], v[192:195], v[212:215]\n"
    "ds_read_b32 v252, v1 offset:256\n"
    "v_mul_f32 v206, v206, v253\n"
    "v_mfma_f32_16x16x32_bf16 v[208:211], v[76:79], v[196:199], v[208:211]\n"
    "v_mul_f32 v207, v207, v253\n"
    "v_cvt_pk_bf16_f32 v200, v200, v201\n"
    "v_mfma_f32_16x16x32_bf16 v[212:215], v[100:103], v[196:199], v[212:215]\n"
    "v_cvt_pk_bf16_f32 v201, v202, v203\n"
    "v_cvt_pk_bf16_f32 v202, v204, v205\n"
    "v_cvt_pk_bf16_f32 v203, v206, v207\n"
    "buffer_store_dwordx4 v[200:203], v5, s[80:83], 0 offen offset:0 sc1\n"
    ".Lh13j:\n"
    "s_waitcnt lgkmcnt(1)\n"
    "s_nop 0\n"
    "v_mfma_f32_16x16x32_bf16 v[200:203], v[8:11], v[152:155], 0\n"
    "v_mul_f32 v208, v208, v253\n"
    "v_mul_f32 v209, v209, v253\n"
    "v_mfma_f32_16x16x32_bf16 v[204:207], v[32:35], v[152:155], 0\n"
    "ds_read_b32 v228, v224 offset:176\n"
    "v_mul_f32 v210, v210, v253\n"
    "v_mfma_f32_16x16x32_bf16 v[200:203], v[12:15], v[156:159], v[200:203]\n"
    "v_mul_f32 v211, v211, v253\n"
    "v_mul_f32 v212, v212, v253\n"
    "v_mfma_f32_16x16x32_bf16 v[204:207], v[36:39], v[156:159], v[204:207]\n"
    "v_mul_f32 v213, v213, v253\n"
    "v_mul_f32 v214, v214, v253\n"
    "v_mfma_f32_16x16x32_bf16 v[200:203], v[16:19], v[160:163], v[200:203]\n"
    "v_mul_f32 v215, v215, v253\n"
    "v_cvt_pk_bf16_f32 v208, v208, v209\n"
    "v_mfma_f32_16x16x32_bf16 v[204:207], v[40:43], v[160:163], v[204:207]\n"
    "v_cvt_pk_bf16_f32 v209, v210, v211\n"
    "v_cvt_pk_bf16_f32 v210, v212, v213\n"
    "v_mfma_f32_16x16x32_bf16 v[200:203], v[20:23], v[164:167], v[200:203]\n"
    "v_cvt_pk_bf16_f32 v211, v214, v215\n"
    "v_mfma_f32_16x16x32_bf16 v[204:207], v[44:47], v[164:167], v[204:207]\n"
    "buffer_store_dwordx4 v[208:211], v5, s[80:83], 0 offen offset:64 sc1\n"
    "v_mfma_f32_16x16x32_bf16 v[200:203], v[24:27], v[168:171], v[200:203]\n"
    "v_mfma_f32_16x16x32_bf16 v[204:207], v[48:51], v[168:171], v[204:207]\n"
    "v_add_u32 v5, v7, v5\n"
    "v_mfma_f32_16x16x32_bf16 v[200:203], v[28:31], v[172:175], v[200:203]\n"
    "s_waitcnt lgkmcnt(0)\n"
    "v_cmp_gt_u32 vcc, 3, v228\n"
    "v_mfma_f32_16x16x32_bf16 v[204:207], v[52:55], v[172:175], v[204:207]\n"
    "s_cbranch_vccnz .Lh14\n"
    "v_mfma_f32_16x16x32_bf16 v[208:211], v[56:59], v[152:155], 0\n"
    "ds_read_b64_tr_b16 v[176:177], v2 offset:12288\n"
    "ds_read_b64_tr_b16 v[178:179], v3 offset:12288\n"
    "v_mfma_f32_16x16x32_bf16 v[212:215], v[80:83], v[152:155], 0\n"
    "ds_read_b64_tr_b16 v[180:181], v2 offset:13312\n"
    "ds_read_b64_tr_b16 v[182:183], v3 offset:13312\n"
    "v_mfma_f32_16x16x32_bf16 v[208:211], v[60:63], v[156:159], v[208:211]\n"
    "ds_read_b64_tr_b16 v[184:185], v2 offset:14336\n"
    "ds_read_b64_tr_b16 v[186:187], v3 offset:14336\n"
    "v_mfma_f32_16x16x32_bf16 v[212:215], v[84:87], v[156:159], v[212:215]\n"
    "ds_read_b64_tr_b16 v[188:189], v2 offset:15360\n"
    "ds_read_b64_tr_b16 v[190:191], v3 offset:15360\n"
    "v_mfma_f32_16x16x32_bf16 v[208:211], v[64:67], v[160:163], v[208:211]\n"
    "v_mul_f32 v200, v200, v252\n"
    "v_mul_f32 v201, v201, v252\n"
    "v_mfma_f32_16x16x32_bf16 v[212:215], v[88:91], v[160:163], v[212:215]\n"
    "ds_read_b64_tr_b16 v[192:193], v2 offset:16384\n"
    "ds_read_b64_tr_b16 v[194:195], v3 offset:16384\n"
    "v_mfma_f32_16x16x32_bf16 v[208:211], v[68:71], v[164:167], v[208:211]\n"
    "v_mul_f32 v202, v202, v252\n"
    "v_mul_f32 v203, v203, v252\n"
    "v_mfma_f32_16x16x32_bf16 v[212:215], v[92:95], v[164:167], v[212:215]\n"
    "ds_read_b64_tr_b16 v[196:197], v2 offset:17408\n"
    "ds_read_b64_tr_b16 v[198:199], v3 offset:17408\n"
    "v_mfma_f32_16x16x32_bf16 v[208:211], v[72:75], v[168:171], v[208:211]\n"
    "v_mul_f32 v204, v204, v252\n"
    "v_mul_f32 v205, v205, v252\n"
    "v_mfma_f32_16x16x32_bf16 v[212:215], v[96:99], v[168:171], v[212:215]\n"
    "ds_read_b32 v253, v1 offset:320\n"
    "v_mul_f32 v206, v206, v252\n"
    "v_mfma_f32_16x16x32_bf16 v[208:211], v[76:79], v[172:175], v[208:211]\n"
    "v_mul_f32 v207, v207, v252\n"
    "v_cvt_pk_bf16_f32 v200, v200, v201\n"
    "v_mfma_f32_16x16x32_bf16 v[212:215], v[100:103], v[172:175], v[212:215]\n"
    "v_cvt_pk_bf16_f32 v201, v202, v203\n"
    "v_cvt_pk_bf16_f32 v202, v204, v205\n"
    "v_cvt_pk_bf16_f32 v203, v206, v207\n"
    "buffer_store_dwordx4 v[200:203], v4, s[80:83], 0 offen offset:0 sc1\n"
    ".Lh14j:\n"
    "s_cmp_lg_u32 s58, 0\n"
    "s_cbranch_scc1 .Lh_e12\n"
    "s_waitcnt lgkmcnt(1)\n"
    "v_mfma_f32_16x16x32_bf16 v[200:203], v[8:11], v[176:179], 0\n"
    "v_mul_f32 v208, v208, v252\n"
    "v_mul_f32 v209, v209, v252\n"
    "v_mfma_f32_16x16x32_bf16 v[204:207], v[32:35], v[176:179], 0\n"
    "ds_read_b32 v228, v224 offset:832\n"
    "v_mul_f32 v210, v210, v252\n"
    "v_mfma_f32_16x16x32_bf16 v[200:203], v[12:15], v[180:183], v[200:203]\n"
    "v_mul_f32 v211, v211, v252\n"
    "v_mul_f32 v212, v212, v252\n"
    "v_mfma_f32_16x16x32_bf16 v[204:207], v[36:39], v[180:183], v[204:207]\n"
    "v_mul_f32 v213, v213, v252\n"
    "v_mul_f32 v214, v214, v252\n"
    "v_mfma_f32_16x16x32_bf16 v[200:203], v[16:19], v[184:187], v[200:203]\n"
    "v_mul_f32 v215, v215, v252\n"
    "v_cvt_pk_bf16_f32 v208, v208, v209\n"
    "v_mfma_f32_16x16x32_bf16 v[204:207], v[40:43], v[184:187], v[204:207]\n"
    "v_cvt_pk_bf16_f32 v209, v210, v211\n"
    "v_cvt_pk_bf16_f32 v210, v212, v213\n"
    "v_mfma_f32_16x16x32_bf16 v[200:203], v[20:23], v[188:191], v[200:203]\n"
    "v_cvt_pk_bf16_f32 v211, v214, v215\n"
    "v_mfma_f32_16x16x32_bf16 v[204:207], v[44:47], v[188:191], v[204:207]\n"
    "buffer_store_dwordx4 v[208:211], v4, s[80:83], 0 offen offset:64 sc1\n"
    "v_mfma_f32_16x16x32_bf16 v[200:203], v[24:27], v[192:195], v[200:203]\n"
    "v_mfma_f32_16x16x32_bf16 v[204:207], v[48:51], v[192:195], v[204:207]\n"
    "v_add_u32 v4, v0, v4\n"
    "v_mfma_f32_16x16x32_bf16 v[200:203], v[28:31], v[196:199], v[200:203]\n"
    "s_waitcnt lgkmcnt(0)\n"
    "v_cmp_gt_u32 vcc, 4, v228\n"
    "v_mfma_f32_16x16x32_bf16 v[204:207], v[52:55], v[196:199], v[204:207]\n"
    "s_cbranch_vccnz .Lh15\n"
    "v_mfma_f32_16x16x32_bf16 v[208:211], v[56:59], v[176:179], 0\n"
    "ds_read_b64_tr_b16 v[152:153], v2 offset:0\n"
    "ds_read_b64_tr_b16 v[154:155], v3 offset:0\n"
    "v_mfma_f32_16x16x32_bf16 v[212:215], v[80:83], v[176:179], 0\n"
    "ds_read_b64_tr_b16 v[156:157], v2 offset:1024\n"
    "ds_read_b64_tr_b16 v[158:159], v3 offset:1024\n"
    "v_mfma_f32_16x16x32_bf16 v[208:211], v[60:63], v[180:183], v[208:211]\n"
    "ds_read_b64_tr_b16 v[160:161], v2 offset:2048\n"
    "ds_read_b64_tr_b16 v[162:163], v3 offset:2048\n"
    "v_mfma_f32_16x16x32_bf16 v[212:215], v[84:87], v[180:183], v[212:215]\n"
    "ds_read_b64_tr_b16 v[164:165], v2 offset:3072\n"
    "ds_read_b64_tr_b16 v[166:167], v3 offset:3072\n"
    "v_mfma_f32_16x16x32_bf16 v[208:211], v[64:67], v[184:187], v[208:211]\n"
    "v_mul_f32 v200, v200, v253\n"
    "v_mul_f32 v201, v201, v253\n"
    "v_mfma_f32_16x16x32_bf16 v[212:215], v[88:91], v[184:187], v[212:215]\n"
    "ds_read_b64_tr_b16 v[168:169], v2 offset:4096\n"
    "ds_read_b64_tr_b16 v[170:171], v3 offset:4096\n"
    "v_mfma_f32_16x16x32_bf16 v[208:211], v[68:71], v[188:191], v[208:211]\n"
    "v_mul_f32 v202, v202, v253\n"
    "v_mul_f32 v203, v203, v253\n"
    "v_mfma_f32_16x16x32_bf16 v[212:215], v[92:95], v[188:191], v[212:215]\n"
    "ds_read_b64_tr_b16 v[172:173], v2 offset:5120\n"
    "ds_read_b64_tr_b16 v[174:175], v3 offset:5120\n"
    "v_mfma_f32_16x16x32_bf16 v[208:211], v[72:75], v[192:195], v[208:211]\n"
    "v_mul_f32 v204, v204, v253\n"
    "v_mul_f32 v205, v205, v253\n"
    "v_mfma_f32_16x16x32_bf16 v[212:215], v[96:99], v[192:195], v[212:215]\n"
    "ds_read_b32 v252, v1 offset:192\n"
    "v_mul_f32 v206, v206, v253\n"
    "v_mfma_f32_16x16x32_bf16 v[208:211], v[76:79], v[196:199], v[208:211]\n"
    "v_mul_f32 v207, v207, v253\n"
    "v_cvt_pk_bf16_f32 v200, v200, v201\n"
    "v_mfma_f32_16x16x32_bf16 v[212:215], v[100:103], v[196:199], v[212:215]\n"
    "v_cvt_pk_bf16_f32 v201, v202, v203\n"
    "v_cvt_pk_bf16_f32 v202, v204, v205\n"
    "v_cvt_pk_bf16_f32 v203, v206, v207\n"
    "buffer_store_dwordx4 v[200:203], v5, s[80:83], 0 offen offset:0 sc1\n"
    ".Lh15j:\n"
    "s_waitcnt lgkmcnt(1)\n"
    "s_nop 0\n"
    "v_mfma_f32_16x16x32_bf16 v[200:203], v[8:11], v[152:155], 0\n"
    "v_mul_f32 v208, v208, v253\n"
    "v_mul_f32 v209, v209, v253\n"
    "v_mfma_f32_16x16x32_bf16 v[204:207], v[32:35], v[152:155], 0\n"
    "v_mul_f32 v210, v210, v253\n"
    "v_mul_f32 v211, v211, v253\n"
    "v_mfma_f32_16x16x32_bf16 v[200:203], v[12:15], v[156:159], v[200:203]\n"
    "v_mul_f32 v212, v212, v253\n"
    "v_mul_f32 v213, v213, v253\n"
    "v_mfma_f32_16x16x32_bf16 v[204:207], v[36:39], v[156:159], v[204:207]\n"
    "v_mul_f32 v214, v214, v253\n"
    "v_mul_f32 v215, v215, v253\n"
    "v_mfma_f32_16x16x32_bf16 v[200:203], v[16:19], v[160:163], v[200:203]\n"
    "v_cvt_pk_bf16_f32 v208, v208, v209\n"
    "v_cvt_pk_bf16_f32 v209, v210, v211\n"
    "v_mfma_f32_16x16x32_bf16 v[204:207], v[40:43], v[160:163], v[204:207]\n"
    "v_cvt_pk_bf16_f32 v210, v212, v213\n"
    "v_cvt_pk_bf16_f32 v211, v214, v215\n"
    "v_mfma_f32_16x16x32_bf16 v[200:203], v[20:23], v[164:167], v[200:203]\n"
    "buffer_store_dwordx4 v[208:211], v5, s[80:83], 0 offen offset:64 sc1\n"
    "v_mfma_f32_16x16x32_bf16 v[204:207], v[44:47], v[164:167], v[204:207]\n"
    "v_mfma_f32_16x16x32_bf16 v[200:203], v[24:27], v[168:171], v[200:203]\n"
    "v_mfma_f32_16x16x32_bf16 v[204:207], v[48:51], v[168:171], v[204:207]\n"
    "v_mfma_f32_16x16x32_bf16 v[200:203], v[28:31], v[172:175], v[200:203]\n"
    "v_mfma_f32_16x16x32_bf16 v[204:207], v[52:55], v[172:175], v[204:207]\n"
    "v_mfma_f32_16x16x32_bf16 v[208:211], v[56:59], v[152:155], 0\n"
    "v_mfma_f32_16x16x32_bf16 v[212:215], v[80:83], v[152:155], 0\n"
    "v_mfma_f32_16x16x32_bf16 v[208:211], v[60:63], v[156:159], v[208:211]\n"
    "v_mfma_f32_16x16x32_bf16 v[212:215], v[84:87], v[156:159], v[212:215]\n"
    "v_mfma_f32_16x16x32_bf16 v[208:211], v[64:67], v[160:163], v[208:211]\n"
    "v_mfma_f32_16x16x32_bf16 v[212:215], v[88:91], v[160:163], v[212:215]\n"
    "s_waitcnt lgkmcnt(0)\n"
    "v_mul_f32 v200, v200, v252\n"
    "v_mfma_f32_16x16x32_bf16 v[208:211], v[68:71], v[164:167], v[208:211]\n"
    "v_mul_f32 v201, v201, v252\n"
    "v_mul_f32 v202, v202, v252\n"
    "v_mfma_f32_16x16x32_bf16 v[212:215], v[92:95], v[164:167], v[212:215]\n"
    "v_mul_f32 v203, v203, v252\n"
    "v_mul_f32 v204, v204, v252\n"
    "v_mfma_f32_16x16x32_bf16 v[208:211], v[72:75], v[168:171], v[208:211]\n"
    "v_mul_f32 v205, v205, v252\n"
    "v_mul_f32 v206, v206, v252\n"
    "v_mfma_f32_16x16x32_bf16 v[212:215], v[96:99], v[168:171], v[212:215]\n"
    "v_mul_f32 v207, v207, v252\n"
    "v_cvt_pk_bf16_f32 v200, v200, v201\n"
    "v_mfma_f32_16x16x32_bf16 v[208:211], v[76:79], v[172:175], v[208:211]\n"
    "v_cvt_pk_bf16_f32 v201, v202, v203\n"
    "v_cvt_pk_bf16_f32 v202, v204, v205\n"
    "v_mfma_f32_16x16x32_bf16 v[212:215], v[100:103], v[172:175], v[212:215]\n"
    "v_cvt_pk_bf16_f32 v203, v206, v207\n"
    "buffer_store_dwordx4 v[200:203], v4, s[80:83], 0 offen offset:0 sc1\n"
    "s_nop 2\n"
    "v_mul_f32 v208, v208, v252\n"
    "v_mul_f32 v209, v209, v252\n"
    "v_mul_f32 v210, v210, v252\n"
    "v_mul_f32 v211, v211, v252\n"
    "v_mul_f32 v212, v212, v252\n"
    "v_mul_f32 v213, v213, v252\n"
    "v_mul_f32 v214, v214, v252\n"
    "v_mul_f32 v215, v215, v252\n"
    "v_cvt_pk_bf16_f32 v208, v208, v209\n"
    "v_cvt_pk_bf16_f32 v209, v210, v211\n"
    "v_cvt_pk_bf16_f32 v210, v212, v213\n"
    "v_cvt_pk_bf16_f32 v211, v214, v215\n"
    "buffer_store_dwordx4 v[208:211], v4, s[80:83], 0 offen offset:64 sc1\n"
    ".Lh_end:\n"
    "s_endpgm\n"
    ".Lh1:\n"
    "s_sub_u32 s52, s52, 1\n"
    "s_cmp_lt_i32 s52, 0\n"
    "s_cbranch_scc1 .Lh1b\n"
    "ds_read_b32 v228, v224 offset:288\n"
    "s_waitcnt lgkmcnt(0)\n"
    "v_cmp_gt_u32 vcc, 1, v228\n"
    "s_cbranch_vccnz .Lh1\n"
    "s_branch .Lh1b\n"
    ".Lh2:\n"
    "s_sub_u32 s52, s52, 1\n"
    "s_cmp_lt_i32 s52, 0\n"
    "s_cbranch_scc1 .Lh2b\n"
    "ds_read_b32 v228, v224 offset:0\n"
    "s_waitcnt lgkmcnt(0)\n"
    "v_cmp_gt_u32 vcc, 6, v228\n"
    "s_cbranch_vccnz .Lh2\n"
    "s_branch .Lh2b\n"
    ".Lh_re:\n"
    "s_sub_u32 s51, s51, 1\n"
    "s_cmp_le_i32 s51, 0\n"
    "s_cbranch_scc1 .Lh_ok\n"
    "s_sleep 2\n"
    "v_mov_b64 v[244:245], v[240:241]\n"
    "v_mov_b64 v[246:247], 0\n"
    "v_mov_b64 v[242:243], 0\n"
    "s_mov_b32 exec_hi, 15\n"
    "global_load_dwordx4 v[244:247], v236, s[40:41] sc1\n"
    "s_mov_b64 exec, -1\n"
    "global_load_dwordx4 v[8:11], v235, s[64:65] offset:-3072 sc1\n"
    "global_load_dwordx4 v[32:35], v235, s[66:67] offset:-3072 sc1\n"
    "global_load_dwordx4 v[12:15], v235, s[64:65] offset:-2048 sc1\n"
    "global_load_dwordx4 v[36:39], v235, s[66:67] offset:-2048 sc1\n"
    "global_load_dwordx4 v[16:19], v235, s[64:65] offset:-1024 sc1\n"
    "global_load_dwordx4 v[40:43], v235, s[66:67] offset:-1024 sc1\n"
    "global_load_dwordx4 v[20:23], v235, s[64:65] offset:0 sc1\n"
    "global_load_dwordx4 v[44:47], v235, s[66:67] offset:0 sc1\n"
    "global_load_dwordx4 v[24:27], v235, s[64:65] offset:1024 sc1\n"
    "global_load_dwordx4 v[48:51], v235, s[66:67] offset:1024 sc1\n"
    "global_load_dwordx4 v[28:31], v235, s[64:65] offset:2048 sc1\n"
    "global_load_dwordx4 v[52:55], v235, s[66:67] offset:2048 sc1\n"
    "global_load_dwordx4 v[56:59], v235, s[68:69] offset:-3072 sc1\n"
    "global_load_dwordx4 v[80:83], v235, s[70:71] offset:-3072 sc1\n"
    "global_load_dwordx4 v[60:63], v235, s[68:69] offset:-2048 sc1\n"
    "global_load_dwordx4 v[84:87], v235, s[70:71] offset:-2048 sc1\n"
    "global_load_dwordx4 v[64:67], v235, s[68:69] offset:-1024 sc1\n"
    "global_load_dwordx4 v[88:91], v235, s[70:71] offset:-1024 sc1\n"
    "global_load_dwordx4 v[68:71], v235, s[68:69] offset:0 sc1\n"
    "global_load_dwordx4 v[92:95], v235, s[70:71] offset:0 sc1\n"
    "global_load_dwordx4 v[72:75], v235, s[68:69] offset:1024 sc1\n"
    "global_load_dwordx4 v[96:99], v235, s[70:71] offset:1024 sc1\n"
    "global_load_dwordx4 v[76:79], v235, s[68:69] offset:2048 sc1\n"
    "global_load_dwordx4 v[100:103], v235, s[70:71] offset:2048 sc1\n"
    "global_load_dwordx4 v[104:107], v235, s[72:73] offset:-3072 sc1\n"
    "global_load_dwordx4 v[128:131], v235, s[74:75] offset:-3072 sc1\n"
    "global_load_dwordx4 v[108:111], v235, s[72:73] offset:-2048 sc1\n"
    "global_load_dwordx4 v[132:135], v235, s[74:75] offset:-2048 sc1\n"
    "global_load_dwordx4 v[112:115], v235, s[72:73] offset:-1024 sc1\n"
    "global_load_dwordx4 v[136:139], v235, s[74:75] offset:-1024 sc1\n"
    "global_load_dwordx4 v[116:119], v235, s[72:73] offset:0 sc1\n"
    "global_load_dwordx4 v[140:143], v235, s[74:75] offset:0 sc1\n"
    "global_load_dwordx4 v[120:123], v235, s[72:73] offset:1024 sc1\n"
    "global_load_dwordx4 v[144:147], v235, s[74:75] offset:1024 sc1\n"
    "global_load_dwordx4 v[124:127], v235, s[72:73] offset:2048 sc1\n"
    "global_load_dwordx4 v[148:151], v235, s[74:75] offset:2048 sc1\n"
    "s_waitcnt vmcnt(0)\n"
    "v_add3_u32 v242, v242, v8, v11\n"
    "v_sub_u32 v8, v8, v240\n"
    "v_add3_u32 v242, v242, v32, v35\n"
    "v_sub_u32 v32, v32, v240\n"
    "v_add3_u32 v242, v242, v12, v15\n"
    "v_sub_u32 v12, v12, v240\n"
    "v_add3_u32 v242, v242, v36, v39\n"
    "v_sub_u32 v36, v36, v240\n"
    "v_add3_u32 v242, v242, v16, v19\n"
    "v_sub_u32 v16, v16, v240\n"
    "v_add3_u32 v242, v242, v40, v43\n"
    "v_sub_u32 v40, v40, v240\n"
    "v_add3_u32 v242, v242, v20, v23\n"
    "v_sub_u32 v20, v20, v240\n"
    "v_add3_u32 v242, v242, v44, v47\n"
    "v_sub_u32 v44, v44, v240\n"
    "v_add3_u32 v242, v242, v24, v27\n"
    "v_sub_u32 v24, v24, v240\n"
    "v_add3_u32 v242, v242, v48, v51\n"
    "v_sub_u32 v48, v48, v240\n"
    "v_add3_u32 v242, v242, v28, v31\n"
    "v_sub_u32 v28, v28, v240\n"
    "v_add3_u32 v242, v242, v52, v55\n"
    "v_sub_u32 v52, v52, v240\n"
    "v_add3_u32 v242, v242, v56, v59\n"
    "v_sub_u32 v56, v56, v240\n"
    "v_add3_u32 v242, v242, v80, v83\n"
    "v_sub_u32 v80, v80, v240\n"
    "v_add3_u32 v242, v242, v60, v63\n"
    "v_sub_u32 v60, v60, v240\n"
    "v_add3_u32 v242, v242, v84, v87\n"
    "v_sub_u32 v84, v84, v240\n"
    "v_add3_u32 v242, v242, v64, v67\n"
    "v_sub_u32 v64, v64, v240\n"
    "v_add3_u32 v242, v242, v88, v91\n"
    "v_sub_u32 v88, v88, v240\n"
    "v_add3_u32 v242, v242, v68, v71\n"
    "v_sub_u32 v68, v68, v240\n"
    "v_add3_u32 v242, v242, v92, v95\n"
    "v_sub_u32 v92, v92, v240\n"
    "v_add3_u32 v242, v242, v72, v75\n"
    "v_sub_u32 v72, v72, v240\n"
    "v_add3_u32 v242, v242, v96, v99\n"
    "v_sub_u32 v96, v96, v240\n"
    "v_add3_u32 v242, v242, v76, v79\n"
    "v_sub_u32 v76, v76, v240\n"
    "v_add3_u32 v242, v242, v100, v103\n"
    "v_sub_u32 v100, v100, v240\n"
    "v_add3_u32 v242, v242, v104, v107\n"
    "v_sub_u32 v104, v104, v240\n"
    "v_add3_u32 v242, v242, v128, v131\n"
    "v_sub_u32 v128, v128, v240\n"
    "v_add3_u32 v242, v242, v108, v111\n"
    "v_sub_u32 v108, v108, v240\n"
    "v_add3_u32 v242, v242, v132, v135\n"
    "v_sub_u32 v132, v132, v240\n"
    "v_add3_u32 v242, v242, v112, v115\n"
    "v_sub_u32 v112, v112, v240\n"
    "v_add3_u32 v242, v242, v136, v139\n"
    "v_sub_u32 v136, v136, v240\n"
    "v_add3_u32 v242, v242, v116, v119\n"
    "v_sub_u32 v116, v116, v240\n"
    "v_add3_u32 v242, v242, v140, v143\n"
    "v_sub_u32 v140, v140, v240\n"
    "v_add3_u32 v242, v242, v120, v123\n"
    "v_sub_u32 v120, v120, v240\n"
    "v_add3_u32 v242, v242, v144, v147\n"
    "v_sub_u32 v144, v144, v240\n"
    "v_add3_u32 v242, v242, v124, v127\n"
    "v_sub_u32 v124, v124, v240\n"
    "v_add3_u32 v242, v242, v148, v151\n"
    "v_sub_u32 v148, v148, v240\n"
    "s_branch .Lh_ce\n"
    ".Lh_pl:\n"
    "s_sub_u32 s51, s51, 1\n"
    "s_cmp_le_i32 s51, 0\n"
    "s_cbranch_scc1 .Lh_ld\n"
    "s_sleep 4\n"
    "s_mov_b32 exec_hi, 15\n"
    "global_load_dwordx2 v[244:245], v236, s[40:41] sc1\n"
    "s_mov_b64 exec, -1\n"
    "s_waitcnt vmcnt(0)\n"
    "v_cmp_ne_u64 vcc, v[240:241], v[244:245]\n"
    "s_cbranch_vccnz .Lh_pl\n"
    "s_branch .Lh_ld\n"
    ".Lh_ld:\n"
    "v_mov_b64 v[244:245], v[240:241]\n"
    "v_mov_b64 v[246:247], 0\n"
    "v_mov_b64 v[242:243], 0\n"
    "s_mov_b32 exec_hi, 15\n"
    "global_load_dwordx4 v[244:247], v236, s[40:41] sc1\n"
    "s_mov_b64 exec, -1\n"
    "global_load_dwordx4 v[8:11], v235, s[64:65] offset:-3072 sc1\n"
    "global_load_dwordx4 v[32:35], v235, s[66:67] offset:-3072 sc1\n"
    "global_load_dwordx4 v[12:15], v235, s[64:65] offset:-2048 sc1\n"
    "global_load_dwordx4 v[36:39], v235, s[66:67] offset:-2048 sc1\n"
    "global_load_dwordx4 v[16:19], v235, s[64:65] offset:-1024 sc1\n"
    "global_load_dwordx4 v[40:43], v235, s[66:67] offset:-1024 sc1\n"
    "global_load_dwordx4 v[20:23], v235, s[64:65] offset:0 sc1\n"
    "global_load_dwordx4 v[44:47], v235, s[66:67] offset:0 sc1\n"
    "global_load_dwordx4 v[24:27], v235, s[64:65] offset:1024 sc1\n"
    "global_load_dwordx4 v[48:51], v235, s[66:67] offset:1024 sc1\n"
    "global_load_dwordx4 v[28:31], v235, s[64:65] offset:2048 sc1\n"
    "global_load_dwordx4 v[52:55], v235, s[66:67] offset:2048 sc1\n"
    "global_load_dwordx4 v[56:59], v235, s[68:69] offset:-3072 sc1\n"
    "global_load_dwordx4 v[80:83], v235, s[70:71] offset:-3072 sc1\n"
    "global_load_dwordx4 v[60:63], v235, s[68:69] offset:-2048 sc1\n"
    "global_load_dwordx4 v[84:87], v235, s[70:71] offset:-2048 sc1\n"
    "global_load_dwordx4 v[64:67], v235, s[68:69] offset:-1024 sc1\n"
    "global_load_dwordx4 v[88:91], v235, s[70:71] offset:-1024 sc1\n"
    "global_load_dwordx4 v[68:71], v235, s[68:69] offset:0 sc1\n"
    "global_load_dwordx4 v[92:95], v235, s[70:71] offset:0 sc1\n"
    "global_load_dwordx4 v[72:75], v235, s[68:69] offset:1024 sc1\n"
    "global_load_dwordx4 v[96:99], v235, s[70:71] offset:1024 sc1\n"
    "global_load_dwordx4 v[76:79], v235, s[68:69] offset:2048 sc1\n"
    "global_load_dwordx4 v[100:103], v235, s[70:71] offset:2048 sc1\n"
    "global_load_dwordx4 v[104:107], v235, s[72:73] offset:-3072 sc1\n"
    "global_load_dwordx4 v[128:131], v235, s[74:75] offset:-3072 sc1\n"
    "global_load_dwordx4 v[108:111], v235, s[72:73] offset:-2048 sc1\n"
    "global_load_dwordx4 v[132:135], v235, s[74:75] offset:-2048 sc1\n"
    "global_load_dwordx4 v[112:115], v235, s[72:73] offset:-1024 sc1\n"
    "global_load_dwordx4 v[136:139], v235, s[74:75] offset:-1024 sc1\n"
    "global_load_dwordx4 v[116:119], v235, s[72:73] offset:0 sc1\n"
    "global_load_dwordx4 v[140:143], v235, s[74:75] offset:0 sc1\n"
    "global_load_dwordx4 v[120:123], v235, s[72:73] offset:1024 sc1\n"
    "global_load_dwordx4 v[144:147], v235, s[74:75] offset:1024 sc1\n"
    "global_load_dwordx4 v[124:127], v235, s[72:73] offset:2048 sc1\n"
    "global_load_dwordx4 v[148:151], v235, s[74:75] offset:2048 sc1\n"
    "s_branch .Lh_rs\n"
    ".Lh3:\n"
    "s_sub_u32 s52, s52, 1\n"
    "s_cmp_lt_i32 s52, 0\n"
    "s_cbranch_scc1 .Lh3b\n"
    "ds_read_b32 v228, v224 offset:128\n"
    "s_waitcnt lgkmcnt(0)\n"
    "v_cmp_gt_u32 vcc, 1, v228\n"
    "s_cbranch_vccnz .Lh3\n"
    "s_branch .Lh3b\n"
    ".Lh4:\n"
    "v_mfma_f32_16x16x32_bf16 v[216:219], v[104:107], v[152:155], 0\n"
    "buffer_store_dwordx4 v[200:203], v4, s[80:83], 0 offen offset:0 sc1\n"
    "v_mfma_f32_16x16x32_bf16 v[220:223], v[128:131], v[152:155], 0\n"
    "v_mul_f32 v208, v208, v252\n"
    "v_mul_f32 v209, v209, v252\n"
    "v_mfma_f32_16x16x32_bf16 v[216:219], v[108:111], v[156:159], v[216:219]\n"
    "v_mul_f32 v210, v210, v252\n"
    "v_mul_f32 v211, v211, v252\n"
    "v_mfma_f32_16x16x32_bf16 v[220:223], v[132:135], v[156:159], v[220:223]\n"
    "v_mul_f32 v212, v212, v252\n"
    "v_mul_f32 v213, v213, v252\n"
    "v_mfma_f32_16x16x32_bf16 v[216:219], v[112:115], v[160:163], v[216:219]\n"
    "v_mul_f32 v214, v214, v252\n"
    "v_mul_f32 v215, v215, v252\n"
    "v_mfma_f32_16x16x32_bf16 v[220:223], v[136:139], v[160:163], v[220:223]\n"
    "ds_read_b32 v228, v224 offset:144\n"
    "v_cvt_pk_bf16_f32 v208, v208, v209\n"
    "v_mfma_f32_16x16x32_bf16 v[216:219], v[116:119], v[164:167], v[216:219]\n"
    "v_cvt_pk_bf16_f32 v209, v210, v211\n"
    "v_cvt_pk_bf16_f32 v210, v212, v213\n"
    "v_mfma_f32_16x16x32_bf16 v[220:223], v[140:143], v[164:167], v[220:223]\n"
    "v_cvt_pk_bf16_f32 v211, v214, v215\n"
    "v_mfma_f32_16x16x32_bf16 v[216:219], v[120:123], v[168:171], v[216:219]\n"
    "buffer_store_dwordx4 v[208:211], v4, s[80:83], 0 offen offset:64 sc1\n"
    "v_mfma_f32_16x16x32_bf16 v[220:223], v[144:147], v[168:171], v[220:223]\n"
    "v_mfma_f32_16x16x32_bf16 v[216:219], v[124:127], v[172:175], v[216:219]\n"
    "v_mfma_f32_16x16x32_bf16 v[220:223], v[148:151], v[172:175], v[220:223]\n"
    "s_waitcnt lgkmcnt(0)\n"
    "v_cmp_gt_u32 vcc, 1, v228\n"
    "s_cbranch_vccz .Lh4g\n"
    ".Lh4r:\n"
    "s_sub_u32 s52, s52, 1\n"
    "s_cmp_lt_i32 s52, 0\n"
    "s_cbranch_scc1 .Lh4g\n"
    "ds_read_b32 v228, v224 offset:144\n"
    "s_waitcnt lgkmcnt(0)\n"
    "v_cmp_gt_u32 vcc, 1, v228\n"
    "s_cbranch_vccnz .Lh4r\n"
    "s_branch .Lh4g\n"
    ".Lh4g:\n"
    "ds_read_b64_tr_b16 v[176:177], v2 offset:24576\n"
    "ds_read_b64_tr_b16 v[178:179], v3 offset:24576\n"
    "ds_read_b64_tr_b16 v[180:181], v2 offset:25600\n"
    "ds_read_b64_tr_b16 v[182:183], v3 offset:25600\n"
    "ds_read_b64_tr_b16 v[184:185], v2 offset:26624\n"
    "ds_read_b64_tr_b16 v[186:187], v3 offset:26624\n"
    "ds_read_b64_tr_b16 v[188:189], v2 offset:27648\n"
    "ds_read_b64_tr_b16 v[190:191], v3 offset:27648\n"
    "ds_read_b64_tr_b16 v[192:193], v2 offset:28672\n"
    "ds_read_b64_tr_b16 v[194:195], v3 offset:28672\n"
    "ds_read_b64_tr_b16 v[196:197], v2 offset:29696\n"
    "ds_read_b64_tr_b16 v[198:199], v3 offset:29696\n"
    "ds_read_b32 v253, v1 offset:64\n"
    "v_mov_b32 v227, 2\n"
    "ds_write_b32 v226, v227 offset:32\n"
    "s_branch .Lh4j\n"
    ".Lh5:\n"
    "v_mfma_f32_16x16x32_bf16 v[216:219], v[104:107], v[176:179], 0\n"
    "buffer_store_dwordx4 v[200:203], v5, s[80:83], 0 offen offset:0 sc1\n"
    "v_mfma_f32_16x16x32_bf16 v[220:223], v[128:131], v[176:179], 0\n"
    "v_mul_f32 v208, v208, v253\n"
    "v_mul_f32 v209, v209, v253\n"
    "v_mfma_f32_16x16x32_bf16 v[216:219], v[108:111], v[180:183], v[216:219]\n"
    "v_mul_f32 v210, v210, v253\n"
    "v_mul_f32 v211, v211, v253\n"
    "v_mfma_f32_16x16x32_bf16 v[220:223], v[132:135], v[180:183], v[220:223]\n"
    "v_mul_f32 v212, v212, v253\n"
    "v_mul_f32 v213, v213, v253\n"
    "v_mfma_f32_16x16x32_bf16 v[216:219], v[112:115], v[184:187], v[216:219]\n"
    "v_mul_f32 v214, v214, v253\n"
    "v_mul_f32 v215, v215, v253\n"
    "v_mfma_f32_16x16x32_bf16 v[220:223], v[136:139], v[184:187], v[220:223]\n"
    "ds_read_b32 v228, v224 offset:160\n"
    "v_cvt_pk_bf16_f32 v208, v208, v209\n"
    "v_mfma_f32_16x16x32_bf16 v[216:219], v[116:119], v[188:191], v[216:219]\n"
    "v_cvt_pk_bf16_f32 v209, v210, v211\n"
    "v_cvt_pk_bf16_f32 v210, v212, v213\n"
    "v_mfma_f32_16x16x32_bf16 v[220:223], v[140:143], v[188:191], v[220:223]\n"
    "v_cvt_pk_bf16_f32 v211, v214, v215\n"
    "v_mfma_f32_16x16x32_bf16 v[216:219], v[120:123], v[192:195], v[216:219]\n"
    "buffer_store_dwordx4 v[208:211], v5, s[80:83], 0 offen offset:64 sc1\n"
    "v_mfma_f32_16x16x32_bf16 v[220:223], v[144:147], v[192:195], v[220:223]\n"
    "v_mfma_f32_16x16x32_bf16 v[216:219], v[124:127], v[196:199], v[216:219]\n"
    "v_mfma_f32_16x16x32_bf16 v[220:223], v[148:151], v[196:199], v[220:223]\n"
    "s_waitcnt lgkmcnt(0)\n"
    "v_cmp_gt_u32 vcc, 1, v228\n"
    "s_cbranch_vccz .Lh5g\n"
    ".Lh5r:\n"
    "s_sub_u32 s52, s52, 1\n"
    "s_cmp_lt_i32 s52, 0\n"
    "s_cbranch_scc1 .Lh5g\n"
    "ds_read_b32 v228, v224 offset:160\n"
    "s_waitcnt lgkmcnt(0)\n"
    "v_cmp_gt_u32 vcc, 1, v228\n"
    "s_cbranch_vccnz .Lh5r\n"
    "s_branch .Lh5g\n"
    ".Lh5g:\n"
    "ds_read_b64_tr_b16 v[152:153], v2 offset:30720\n"
    "ds_read_b64_tr_b16 v[154:155], v3 offset:30720\n"
    "ds_read_b64_tr_b16 v[156:157], v2 offset:31744\n"
    "ds_read_b64_tr_b16 v[158:159], v3 offset:31744\n"
    "ds_read_b64_tr_b16 v[160:161], v2 offset:32768\n"
    "ds_read_b64_tr_b16 v[162:163], v3 offset:32768\n"
    "ds_read_b64_tr_b16 v[164:165], v2 offset:33792\n"
    "ds_read_b64_tr_b16 v[166:167], v3 offset:33792\n"
    "ds_read_b64_tr_b16 v[168:169], v2 offset:34816\n"
    "ds_read_b64_tr_b16 v[170:171], v3 offset:34816\n"
    "ds_read_b64_tr_b16 v[172:173], v2 offset:35840\n"
    "ds_read_b64_tr_b16 v[174:175], v3 offset:35840\n"
    "ds_read_b32 v252, v1 offset:128\n"
    "v_mov_b32 v227, 3\n"
    "ds_write_b32 v226, v227 offset:32\n"
    "s_branch .Lh5j\n"
    ".Lh6:\n"
    "v_mfma_f32_16x16x32_bf16 v[216:219], v[104:107], v[152:155], 0\n"
    "buffer_store_dwordx4 v[200:203], v4, s[80:83], 0 offen offset:0 sc1\n"
    "v_mfma_f32_16x16x32_bf16 v[220:223], v[128:131], v[152:155], 0\n"
    "v_mul_f32 v208, v208, v252\n"
    "v_mul_f32 v209, v209, v252\n"
    "v_mfma_f32_16x16x32_bf16 v[216:219], v[108:111], v[156:159], v[216:219]\n"
    "v_mul_f32 v210, v210, v252\n"
    "v_mul_f32 v211, v211, v252\n"
    "v_mfma_f32_16x16x32_bf16 v[220:223], v[132:135], v[156:159], v[220:223]\n"
    "v_mul_f32 v212, v212, v252\n"
    "v_mul_f32 v213, v213, v252\n"
    "v_mfma_f32_16x16x32_bf16 v[216:219], v[112:115], v[160:163], v[216:219]\n"
    "v_mul_f32 v214, v214, v252\n"
    "v_mul_f32 v215, v215, v252\n"
    "v_mfma_f32_16x16x32_bf16 v[220:223], v[136:139], v[160:163], v[220:223]\n"
    "ds_read_b32 v228, v224 offset:176\n"
    "v_cvt_pk_bf16_f32 v208, v208, v209\n"
    "v_mfma_f32_16x16x32_bf16 v[216:219], v[116:119], v[164:167], v[216:219]\n"
    "v_cvt_pk_bf16_f32 v209, v210, v211\n"
    "v_cvt_pk_bf16_f32 v210, v212, v213\n"
    "v_mfma_f32_16x16x32_bf16 v[220:223], v[140:143], v[164:167], v[220:223]\n"
    "v_cvt_pk_bf16_f32 v211, v214, v215\n"
    "v_mfma_f32_16x16x32_bf16 v[216:219], v[120:123], v[168:171], v[216:219]\n"
    "buffer_store_dwordx4 v[208:211], v4, s[80:83], 0 offen offset:64 sc1\n"
    "v_mfma_f32_16x16x32_bf16 v[220:223], v[144:147], v[168:171], v[220:223]\n"
    "v_mfma_f32_16x16x32_bf16 v[216:219], v[124:127], v[172:175], v[216:219]\n"
    "v_mfma_f32_16x16x32_bf16 v[220:223], v[148:151], v[172:175], v[220:223]\n"
    "s_waitcnt lgkmcnt(0)\n"
    "v_cmp_gt_u32 vcc, 1, v228\n"
    "s_cbranch_vccz .Lh6g\n"
    ".Lh6r:\n"
    "s_sub_u32 s52, s52, 1\n"
    "s_cmp_lt_i32 s52, 0\n"
    "s_cbranch_scc1 .Lh6g\n"
    "ds_read_b32 v228, v224 offset:176\n"
    "s_waitcnt lgkmcnt(0)\n"
    "v_cmp_gt_u32 vcc, 1, v228\n"
    "s_cbranch_vccnz .Lh6r\n"
    "s_branch .Lh6g\n"
    ".Lh6g:\n"
    "ds_read_b64_tr_b16 v[176:177], v2 offset:18432\n"
    "ds_read_b64_tr_b16 v[178:179], v3 offset:18432\n"
    "ds_read_b64_tr_b16 v[180:181], v2 offset:19456\n"
    "ds_read_b64_tr_b16 v[182:183], v3 offset:19456\n"
    "ds_read_b64_tr_b16 v[184:185], v2 offset:20480\n"
    "ds_read_b64_tr_b16 v[186:187], v3 offset:20480\n"
    "ds_read_b64_tr_b16 v[188:189], v2 offset:21504\n"
    "ds_read_b64_tr_b16 v[190:191], v3 offset:21504\n"
    "ds_read_b64_tr_b16 v[192:193], v2 offset:22528\n"
    "ds_read_b64_tr_b16 v[194:195], v3 offset:22528\n"
    "ds_read_b64_tr_b16 v[196:197], v2 offset:23552\n"
    "ds_read_b64_tr_b16 v[198:199], v3 offset:23552\n"
    "ds_read_b32 v253, v1 offset:0\n"
    "v_mov_b32 v227, 4\n"
    "ds_write_b32 v226, v227 offset:32\n"
    "s_branch .Lh6j\n"
    ".Lh7:\n"
    "v_mfma_f32_16x16x32_bf16 v[216:219], v[104:107], v[176:179], 0\n"
    "buffer_store_dwordx4 v[200:203], v5, s[80:83], 0 offen offset:0 sc1\n"
    "v_mfma_f32_16x16x32_bf16 v[220:223], v[128:131], v[176:179], 0\n"
    "v_mul_f32 v208, v208, v253\n"
    "v_mul_f32 v209, v209, v253\n"
    "v_mfma_f32_16x16x32_bf16 v[216:219], v[108:111], v[180:183], v[216:219]\n"
    "v_mul_f32 v210, v210, v253\n"
    "v_mul_f32 v211, v211, v253\n"
    "v_mfma_f32_16x16x32_bf16 v[220:223], v[132:135], v[180:183], v[220:223]\n"
    "v_mul_f32 v212, v212, v253\n"
    "v_mul_f32 v213, v213, v253\n"
    "v_mfma_f32_16x16x32_bf16 v[216:219], v[112:115], v[184:187], v[216:219]\n"
    "v_mul_f32 v214, v214, v253\n"
    "v_mul_f32 v215, v215, v253\n"
    "v_mfma_f32_16x16x32_bf16 v[220:223], v[136:139], v[184:187], v[220:223]\n"
    "ds_read_b32 v228, v224 offset:128\n"
    "v_cvt_pk_bf16_f32 v208, v208, v209\n"
    "v_mfma_f32_16x16x32_bf16 v[216:219], v[116:119], v[188:191], v[216:219]\n"
    "v_cvt_pk_bf16_f32 v209, v210, v211\n"
    "v_cvt_pk_bf16_f32 v210, v212, v213\n"
    "v_mfma_f32_16x16x32_bf16 v[220:223], v[140:143], v[188:191], v[220:223]\n"
    "v_cvt_pk_bf16_f32 v211, v214, v215\n"
    "v_mfma_f32_16x16x32_bf16 v[216:219], v[120:123], v[192:195], v[216:219]\n"
    "buffer_store_dwordx4 v[208:211], v5, s[80:83], 0 offen offset:64 sc1\n"
    "v_mfma_f32_16x16x32_bf16 v[220:223], v[144:147], v[192:195], v[220:223]\n"
    "v_mfma_f32_16x16x32_bf16 v[216:219], v[124:127], v[196:199], v[216:219]\n"
    "v_mfma_f32_16x16x32_bf16 v[220:223], v[148:151], v[196:199], v[220:223]\n"
    "s_waitcnt lgkmcnt(0)\n"
    "v_cmp_gt_u32 vcc, 2, v228\n"
    "s_cbranch_vccz .Lh7g\n"
    ".Lh7r:\n"
    "s_sub_u32 s52, s52, 1\n"
    "s_cmp_lt_i32 s52, 0\n"
    "s_cbranch_scc1 .Lh7g\n"
    "ds_read_b32 v228, v224 offset:128\n"
    "s_waitcnt lgkmcnt(0)\n"
    "v_cmp_gt_u32 vcc, 2, v228\n"
    "s_cbranch_vccnz .Lh7r\n"
    "s_branch .Lh7g\n"
    ".Lh7g:\n"
    "ds_read_b64_tr_b16 v[152:153], v2 offset:24576\n"
    "ds_read_b64_tr_b16 v[154:155], v3 offset:24576\n"
    "ds_read_b64_tr_b16 v[156:157], v2 offset:25600\n"
    "ds_read_b64_tr_b16 v[158:159], v3 offset:25600\n"
    "ds_read_b64_tr_b16 v[160:161], v2 offset:26624\n"
    "ds_read_b64_tr_b16 v[162:163], v3 offset:26624\n"
    "ds_read_b64_tr_b16 v[164:165], v2 offset:27648\n"
    "ds_read_b64_tr_b16 v[166:167], v3 offset:27648\n"
    "ds_read_b64_tr_b16 v[168:169], v2 offset:28672\n"
    "ds_read_b64_tr_b16 v[170:171], v3 offset:28672\n"
    "ds_read_b64_tr_b16 v[172:173], v2 offset:29696\n"
    "ds_read_b64_tr_b16 v[174:175], v3 offset:29696\n"
    "ds_read_b32 v252, v1 offset:64\n"
    "v_mov_b32 v227, 5\n"
    "ds_write_b32 v226, v227 offset:32\n"
    "s_branch .Lh7j\n"
    ".Lh8:\n"
    "v_mfma_f32_16x16x32_bf16 v[216:219], v[104:107], v[152:155], 0\n"
    "buffer_store_dwordx4 v[200:203], v4, s[80:83], 0 offen offset:0 sc1\n"
    "v_mfma_f32_16x16x32_bf16 v[220:223], v[128:131], v[152:155], 0\n"
    "v_mul_f32 v208, v208, v252\n"
    "v_mul_f32 v209, v209, v252\n"
    "v_mfma_f32_16x16x32_bf16 v[216:219], v[108:111], v[156:159], v[216:219]\n"
    "v_mul_f32 v210, v210, v252\n"
    "v_mul_f32 v211, v211, v252\n"
    "v_mfma_f32_16x16x32_bf16 v[220:223], v[132:135], v[156:159], v[220:223]\n"
    "v_mul_f32 v212, v212, v252\n"
    "v_mul_f32 v213, v213, v252\n"
    "v_mfma_f32_16x16x32_bf16 v[216:219], v[112:115], v[160:163], v[216:219]\n"
    "v_mul_f32 v214, v214, v252\n"
    "v_mul_f32 v215, v215, v252\n"
    "v_mfma_f32_16x16x32_bf16 v[220:223], v[136:139], v[160:163], v[220:223]\n"
    "ds_read_b32 v228, v224 offset:144\n"
    "v_cvt_pk_bf16_f32 v208, v208, v209\n"
    "v_mfma_f32_16x16x32_bf16 v[216:219], v[116:119], v[164:167], v[216:219]\n"
    "v_cvt_pk_bf16_f32 v209, v210, v211\n"
    "v_cvt_pk_bf16_f32 v210, v212, v213\n"
    "v_mfma_f32_16x16x32_bf16 v[220:223], v[140:143], v[164:167], v[220:223]\n"
    "v_cvt_pk_bf16_f32 v211, v214, v215\n"
    "v_mfma_f32_16x16x32_bf16 v[216:219], v[120:123], v[168:171], v[216:219]\n"
    "buffer_store_dwordx4 v[208:211], v4, s[80:83], 0 offen offset:64 sc1\n"
    "v_mfma_f32_16x16x32_bf16 v[220:223], v[144:147], v[168:171], v[220:223]\n"
    "v_mfma_f32_16x16x32_bf16 v[216:219], v[124:127], v[172:175], v[216:219]\n"
    "v_mfma_f32_16x16x32_bf16 v[220:223], v[148:151], v[172:175], v[220:223]\n"
    "s_waitcnt lgkmcnt(0)\n"
    "v_cmp_gt_u32 vcc, 2, v228\n"
    "s_cbranch_vccz .Lh8g\n"
    ".Lh8r:\n"
    "s_sub_u32 s52, s52, 1\n"
    "s_cmp_lt_i32 s52, 0\n"
    "s_cbranch_scc1 .Lh8g\n"
    "ds_read_b32 v228, v224 offset:144\n"
    "s_waitcnt lgkmcnt(0)\n"
    "v_cmp_gt_u32 vcc, 2, v228\n"
    "s_cbranch_vccnz .Lh8r\n"
    "s_branch .Lh8g\n"
    ".Lh8g:\n"
    "ds_read_b64_tr_b16 v[176:177], v2 offset:30720\n"
    "ds_read_b64_tr_b16 v[178:179], v3 offset:30720\n"
    "ds_read_b64_tr_b16 v[180:181], v2 offset:31744\n"
    "ds_read_b64_tr_b16 v[182:183], v3 offset:31744\n"
    "ds_read_b64_tr_b16 v[184:185], v2 offset:32768\n"
    "ds_read_b64_tr_b16 v[186:187], v3 offset:32768\n"
    "ds_read_b64_tr_b16 v[188:189], v2 offset:33792\n"
    "ds_read_b64_tr_b16 v[190:191], v3 offset:33792\n"
    "ds_read_b64_tr_b16 v[192:193], v2 offset:34816\n"
    "ds_read_b64_tr_b16 v[194:195], v3 offset:34816\n"
    "ds_read_b64_tr_b16 v[196:197], v2 offset:35840\n"
    "ds_read_b64_tr_b16 v[198:199], v3 offset:35840\n"
    "ds_read_b32 v253, v1 offset:128\n"
    "v_mov_b32 v227, 6\n"
    "ds_write_b32 v226, v227 offset:32\n"
    "s_branch .Lh8j\n"
    ".Lh9:\n"
    "v_mfma_f32_16x16x32_bf16 v[216:219], v[104:107], v[176:179], 0\n"
    "buffer_store_dwordx4 v[200:203], v5, s[80:83], 0 offen offset:0 sc1\n"
    "v_mfma_f32_16x16x32_bf16 v[220:223], v[128:131], v[176:179], 0\n"
    "v_mul_f32 v208, v208, v253\n"
    "v_mul_f32 v209, v209, v253\n"
    "v_mfma_f32_16x16x32_bf16 v[216:219], v[108:111], v[180:183], v[216:219]\n"
    "v_mul_f32 v210, v210, v253\n"
    "v_mul_f32 v211, v211, v253\n"
    "v_mfma_f32_16x16x32_bf16 v[220:223], v[132:135], v[180:183], v[220:223]\n"
    "v_mul_f32 v212, v212, v253\n"
    "v_mul_f32 v213, v213, v253\n"
    "v_mfma_f32_16x16x32_bf16 v[216:219], v[112:115], v[184:187], v[216:219]\n"
    "v_mul_f32 v214, v214, v253\n"
    "v_mul_f32 v215, v215, v253\n"
    "v_mfma_f32_16x16x32_bf16 v[220:223], v[136:139], v[184:187], v[220:223]\n"
    "ds_read_b32 v228, v224 offset:160\n"
    "v_cvt_pk_bf16_f32 v208, v208, v209\n"
    "v_mfma_f32_16x16x32_bf16 v[216:219], v[116:119], v[188:191], v[216:219]\n"
    "v_cvt_pk_bf16_f32 v209, v210, v211\n"
    "v_cvt_pk_bf16_f32 v210, v212, v213\n"
    "v_mfma_f32_16x16x32_bf16 v[220:223], v[140:143], v[188:191], v[220:223]\n"
    "v_cvt_pk_bf16_f32 v211, v214, v215\n"
    "v_mfma_f32_16x16x32_bf16 v[216:219], v[120:123], v[192:195], v[216:219]\n"
    "buffer_store_dwordx4 v[208:211], v5, s[80:83], 0 offen offset:64 sc1\n"
    "v_mfma_f32_16x16x32_bf16 v[220:223], v[144:147], v[192:195], v[220:223]\n"
    "v_mfma_f32_16x16x32_bf16 v[216:219], v[124:127], v[196:199], v[216:219]\n"
    "v_mfma_f32_16x16x32_bf16 v[220:223], v[148:151], v[196:199], v[220:223]\n"
    "s_waitcnt lgkmcnt(0)\n"
    "v_cmp_gt_u32 vcc, 2, v228\n"
    "s_cbranch_vccz .Lh9g\n"
    ".Lh9r:\n"
    "s_sub_u32 s52, s52, 1\n"
    "s_cmp_lt_i32 s52, 0\n"
    "s_cbranch_scc1 .Lh9g\n"
    "ds_read_b32 v228, v224 offset:160\n"
    "s_waitcnt lgkmcnt(0)\n"
    "v_cmp_gt_u32 vcc, 2, v228\n"
    "s_cbranch_vccnz .Lh9r\n"
    "s_branch .Lh9g\n"
    ".Lh9g:\n"
    "ds_read_b64_tr_b16 v[152:153], v2 offset:18432\n"
    "ds_read_b64_tr_b16 v[154:155], v3 offset:18432\n"
    "ds_read_b64_tr_b16 v[156:157], v2 offset:19456\n"
    "ds_read_b64_tr_b16 v[158:159], v3 offset:19456\n"
    "ds_read_b64_tr_b16 v[160:161], v2 offset:20480\n"
    "ds_read_b64_tr_b16 v[162:163], v3 offset:20480\n"
    "ds_read_b64_tr_b16 v[164:165], v2 offset:21504\n"
    "ds_read_b64_tr_b16 v[166:167], v3 offset:21504\n"
    "ds_read_b64_tr_b16 v[168:169], v2 offset:22528\n"
    "ds_read_b64_tr_b16 v[170:171], v3 offset:22528\n"
    "ds_read_b64_tr_b16 v[172:173], v2 offset:23552\n"
    "ds_read_b64_tr_b16 v[174:175], v3 offset:23552\n"
    "ds_read_b32 v252, v1 offset:0\n"
    "v_mov_b32 v227, 7\n"
    "ds_write_b32 v226, v227 offset:32\n"
    "s_branch .Lh9j\n"
    ".Lh10:\n"
    "v_mfma_f32_16x16x32_bf16 v[216:219], v[104:107], v[152:155], 0\n"
    "buffer_store_dwordx4 v[200:203], v4, s[80:83], 0 offen offset:0 sc1\n"
    "v_mfma_f32_16x16x32_bf16 v[220:223], v[128:131], v[152:155], 0\n"
    "v_mul_f32 v208, v208, v252\n"
    "v_mul_f32 v209, v209, v252\n"
    "v_mfma_f32_16x16x32_bf16 v[216:219], v[108:111], v[156:159], v[216:219]\n"
    "v_mul_f32 v210, v210, v252\n"
    "v_mul_f32 v211, v211, v252\n"
    "v_mfma_f32_16x16x32_bf16 v[220:223], v[132:135], v[156:159], v[220:223]\n"
    "v_mul_f32 v212, v212, v252\n"
    "v_mul_f32 v213, v213, v252\n"
    "v_mfma_f32_16x16x32_bf16 v[216:219], v[112:115], v[160:163], v[216:219]\n"
    "v_mul_f32 v214, v214, v252\n"
    "v_mul_f32 v215, v215, v252\n"
    "v_mfma_f32_16x16x32_bf16 v[220:223], v[136:139], v[160:163], v[220:223]\n"
    "ds_read_b32 v228, v224 offset:176\n"
    "v_cvt_pk_bf16_f32 v208, v208, v209\n"
    "v_mfma_f32_16x16x32_bf16 v[216:219], v[116:119], v[164:167], v[216:219]\n"
    "v_cvt_pk_bf16_f32 v209, v210, v211\n"
    "v_cvt_pk_bf16_f32 v210, v212, v213\n"
    "v_mfma_f32_16x16x32_bf16 v[220:223], v[140:143], v[164:167], v[220:223]\n"
    "v_cvt_pk_bf16_f32 v211, v214, v215\n"
    "v_mfma_f32_16x16x32_bf16 v[216:219], v[120:123], v[168:171], v[216:219]\n"
    "buffer_store_dwordx4 v[208:211], v4, s[80:83], 0 offen offset:64 sc1\n"
    "v_mfma_f32_16x16x32_bf16 v[220:223], v[144:147], v[168:171], v[220:223]\n"
    "v_mfma_f32_16x16x32_bf16 v[216:219], v[124:127], v[172:175], v[216:219]\n"
    "v_mfma_f32_16x16x32_bf16 v[220:223], v[148:151], v[172:175], v[220:223]\n"
    "s_waitcnt lgkmcnt(0)\n"
    "v_cmp_gt_u32 vcc, 2, v228\n"
    "s_cbranch_vccz .Lh10g\n"
    ".Lh10r:\n"
    "s_sub_u32 s52, s52, 1\n"
    "s_cmp_lt_i32 s52, 0\n"
    "s_cbranch_scc1 .Lh10g\n"
    "ds_read_b32 v228, v224 offset:176\n"
    "s_waitcnt lgkmcnt(0)\n"
    "v_cmp_gt_u32 vcc, 2, v228\n"
    "s_cbranch_vccnz .Lh10r\n"
    "s_branch .Lh10g\n"
    ".Lh10g:\n"
    "ds_read_b64_tr_b16 v[176:177], v2 offset:24576\n"
    "ds_read_b64_tr_b16 v[178:179], v3 offset:24576\n"
    "ds_read_b64_tr_b16 v[180:181], v2 offset:25600\n"
    "ds_read_b64_tr_b16 v[182:183], v3 offset:25600\n"
    "ds_read_b64_tr_b16 v[184:185], v2 offset:26624\n"
    "ds_read_b64_tr_b16 v[186:187], v3 offset:26624\n"
    "ds_read_b64_tr_b16 v[188:189], v2 offset:27648\n"
    "ds_read_b64_tr_b16 v[190:191], v3 offset:27648\n"
    "ds_read_b64_tr_b16 v[192:193], v2 offset:28672\n"
    "ds_read_b64_tr_b16 v[194:195], v3 offset:28672\n"
    "ds_read_b64_tr_b16 v[196:197], v2 offset:29696\n"
    "ds_read_b64_tr_b16 v[198:199], v3 offset:29696\n"
    "ds_read_b32 v253, v1 offset:64\n"
    "s_branch .Lh10j\n"
    ".Lh11:\n"
    "v_mfma_f32_16x16x32_bf16 v[216:219], v[104:107], v[176:179], 0\n"
    "buffer_store_dwordx4 v[200:203], v5, s[80:83], 0 offen offset:0 sc1\n"
    "v_mfma_f32_16x16x32_bf16 v[220:223], v[128:131], v[176:179], 0\n"
    "v_mul_f32 v208, v208, v253\n"
    "v_mul_f32 v209, v209, v253\n"
    "v_mfma_f32_16x16x32_bf16 v[216:219], v[108:111], v[180:183], v[216:219]\n"
    "v_mul_f32 v210, v210, v253\n"
    "v_mul_f32 v211, v211, v253\n"
    "v_mfma_f32_16x16x32_bf16 v[220:223], v[132:135], v[180:183], v[220:223]\n"
    "v_mul_f32 v212, v212, v253\n"
    "v_mul_f32 v213, v213, v253\n"
    "v_mfma_f32_16x16x32_bf16 v[216:219], v[112:115], v[184:187], v[216:219]\n"
    "v_mul_f32 v214, v214, v253\n"
    "v_mul_f32 v215, v215, v253\n"
    "v_mfma_f32_16x16x32_bf16 v[220:223], v[136:139], v[184:187], v[220:223]\n"
    "ds_read_b32 v228, v224 offset:128\n"
    "v_cvt_pk_bf16_f32 v208, v208, v209\n"
    "v_mfma_f32_16x16x32_bf16 v[216:219], v[116:119], v[188:191], v[216:219]\n"
    "v_cvt_pk_bf16_f32 v209, v210, v211\n"
    "v_cvt_pk_bf16_f32 v210, v212, v213\n"
    "v_mfma_f32_16x16x32_bf16 v[220:223], v[140:143], v[188:191], v[220:223]\n"
    "v_cvt_pk_bf16_f32 v211, v214, v215\n"
    "v_mfma_f32_16x16x32_bf16 v[216:219], v[120:123], v[192:195], v[216:219]\n"
    "buffer_store_dwordx4 v[208:211], v5, s[80:83], 0 offen offset:64 sc1\n"
    "v_mfma_f32_16x16x32_bf16 v[220:223], v[144:147], v[192:195], v[220:223]\n"
    "v_mfma_f32_16x16x32_bf16 v[216:219], v[124:127], v[196:199], v[216:219]\n"
    "v_mfma_f32_16x16x32_bf16 v[220:223], v[148:151], v[196:199], v[220:223]\n"
    "s_waitcnt lgkmcnt(0)\n"
    "v_cmp_gt_u32 vcc, 3, v228\n"
    "s_cbranch_vccz .Lh11g\n"
    ".Lh11r:\n"
    "s_sub_u32 s52, s52, 1\n"
    "s_cmp_lt_i32 s52, 0\n"
    "s_cbranch_scc1 .Lh11g\n"
    "ds_read_b32 v228, v224 offset:128\n"
    "s_waitcnt lgkmcnt(0)\n"
    "v_cmp_gt_u32 vcc, 3, v228\n"
    "s_cbranch_vccnz .Lh11r\n"
    "s_branch .Lh11g\n"
    ".Lh11g:\n"
    "ds_read_b64_tr_b16 v[152:153], v2 offset:30720\n"
    "ds_read_b64_tr_b16 v[154:155], v3 offset:30720\n"
    "ds_read_b64_tr_b16 v[156:157], v2 offset:31744\n"
    "ds_read_b64_tr_b16 v[158:159], v3 offset:31744\n"
    "ds_read_b64_tr_b16 v[160:161], v2 offset:32768\n"
    "ds_read_b64_tr_b16 v[162:163], v3 offset:32768\n"
    "ds_read_b64_tr_b16 v[164:165], v2 offset:33792\n"
    "ds_read_b64_tr_b16 v[166:167], v3 offset:33792\n"
    "ds_read_b64_tr_b16 v[168:169], v2 offset:34816\n"
    "ds_read_b64_tr_b16 v[170:171], v3 offset:34816\n"
    "ds_read_b64_tr_b16 v[172:173], v2 offset:35840\n"
    "ds_read_b64_tr_b16 v[174:175], v3 offset:35840\n"
    "ds_read_b32 v252, v1 offset:128\n"
    "s_branch .Lh11j\n"
    ".Lh12:\n"
    "v_mfma_f32_16x16x32_bf16 v[216:219], v[104:107], v[152:155], 0\n"
    "buffer_store_dwordx4 v[200:203], v4, s[80:83], 0 offen offset:0 sc1\n"
    "v_mfma_f32_16x16x32_bf16 v[220:223], v[128:131], v[152:155], 0\n"
    "v_mul_f32 v208, v208, v252\n"
    "v_mul_f32 v209, v209, v252\n"
    "v_mfma_f32_16x16x32_bf16 v[216:219], v[108:111], v[156:159], v[216:219]\n"
    "v_mul_f32 v210, v210, v252\n"
    "v_mul_f32 v211, v211, v252\n"
    "v_mfma_f32_16x16x32_bf16 v[220:223], v[132:135], v[156:159], v[220:223]\n"
    "v_mul_f32 v212, v212, v252\n"
    "v_mul_f32 v213, v213, v252\n"
    "v_mfma_f32_16x16x32_bf16 v[216:219], v[112:115], v[160:163], v[216:219]\n"
    "v_mul_f32 v214, v214, v252\n"
    "v_mul_f32 v215, v215, v252\n"
    "v_mfma_f32_16x16x32_bf16 v[220:223], v[136:139], v[160:163], v[220:223]\n"
    "ds_read_b32 v228, v224 offset:144\n"
    "v_cvt_pk_bf16_f32 v208, v208, v209\n"
    "v_mfma_f32_16x16x32_bf16 v[216:219], v[116:119], v[164:167], v[216:219]\n"
    "v_cvt_pk_bf16_f32 v209, v210, v211\n"
    "v_cvt_pk_bf16_f32 v210, v212, v213\n"
    "v_mfma_f32_16x16x32_bf16 v[220:223], v[140:143], v[164:167], v[220:223]\n"
    "v_cvt_pk_bf16_f32 v211, v214, v215\n"
    "v_mfma_f32_16x16x32_bf16 v[216:219], v[120:123], v[168:171], v[216:219]\n"
    "buffer_store_dwordx4 v[208:211], v4, s[80:83], 0 offen offset:64 sc1\n"
    "v_mfma_f32_16x16x32_bf16 v[220:223], v[144:147], v[168:171], v[220:223]\n"
    "v_mfma_f32_16x16x32_bf16 v[216:219], v[124:127], v[172:175], v[216:219]\n"
    "v_mfma_f32_16x16x32_bf16 v[220:223], v[148:151], v[172:175], v[220:223]\n"
    "s_waitcnt lgkmcnt(0)\n"
    "v_cmp_gt_u32 vcc, 3, v228\n"
    "s_cbranch_vccz .Lh12g\n"
    ".Lh12r:\n"
    "s_sub_u32 s52, s52, 1\n"
    "s_cmp_lt_i32 s52, 0\n"
    "s_cbranch_scc1 .Lh12g\n"
    "ds_read_b32 v228, v224 offset:144\n"
    "s_waitcnt lgkmcnt(0)\n"
    "v_cmp_gt_u32 vcc, 3, v228\n"
    "s_cbranch_vccnz .Lh12r\n"
    "s_branch .Lh12g\n"
    ".Lh12g:\n"
    "ds_read_b64_tr_b16 v[176:177], v2 offset:18432\n"
    "ds_read_b64_tr_b16 v[178:179], v3 offset:18432\n"
    "ds_read_b64_tr_b16 v[180:181], v2 offset:19456\n"
    "ds_read_b64_tr_b16 v[182:183], v3 offset:19456\n"
    "ds_read_b64_tr_b16 v[184:185], v2 offset:20480\n"
    "ds_read_b64_tr_b16 v[186:187], v3 offset:20480\n"
    "ds_read_b64_tr_b16 v[188:189], v2 offset:21504\n"
    "ds_read_b64_tr_b16 v[190:191], v3 offset:21504\n"
    "ds_read_b64_tr_b16 v[192:193], v2 offset:22528\n"
    "ds_read_b64_tr_b16 v[194:195], v3 offset:22528\n"
    "ds_read_b64_tr_b16 v[196:197], v2 offset:23552\n"
    "ds_read_b64_tr_b16 v[198:199], v3 offset:23552\n"
    "ds_read_b32 v253, v1 offset:0\n"
    "s_branch .Lh12j\n"
    ".Lh13:\n"
    "v_mfma_f32_16x16x32_bf16 v[208:211], v[56:59], v[176:179], 0\n"
    "v_mfma_f32_16x16x32_bf16 v[212:215], v[80:83], v[176:179], 0\n"
    "v_mfma_f32_16x16x32_bf16 v[208:211], v[60:63], v[180:183], v[208:211]\n"
    "v_mfma_f32_16x16x32_bf16 v[212:215], v[84:87], v[180:183], v[212:215]\n"
    "v_mul_f32 v200, v200, v253\n"
    "v_mul_f32 v201, v201, v253\n"
    "v_mfma_f32_16x16x32_bf16 v[208:211], v[64:67], v[184:187], v[208:211]\n"
    "v_mul_f32 v202, v202, v253\n"
    "v_mul_f32 v203, v203, v253\n"
    "v_mfma_f32_16x16x32_bf16 v[212:215], v[88:91], v[184:187], v[212:215]\n"
    "ds_read_b32 v228, v224 offset:160\n"
    "v_mul_f32 v204, v204, v253\n"
    "v_mfma_f32_16x16x32_bf16 v[208:211], v[68:71], v[188:191], v[208:211]\n"
    "v_mul_f32 v205, v205, v253\n"
    "v_mul_f32 v206, v206, v253\n"
    "v_mfma_f32_16x16x32_bf16 v[212:215], v[92:95], v[188:191], v[212:215]\n"
    "v_mul_f32 v207, v207, v253\n"
    "v_cvt_pk_bf16_f32 v200, v200, v201\n"
    "v_mfma_f32_16x16x32_bf16 v[208:211], v[72:75], v[192:195], v[208:211]\n"
    "v_cvt_pk_bf16_f32 v201, v202, v203\n"
    "v_cvt_pk_bf16_f32 v202, v204, v205\n"
    "v_mfma_f32_16x16x32_bf16 v[212:215], v[96:99], v[192:195], v[212:215]\n"
    "v_cvt_pk_bf16_f32 v203, v206, v207\n"
    "v_mfma_f32_16x16x32_bf16 v[208:211], v[76:79], v[196:199], v[208:211]\n"
    "buffer_store_dwordx4 v[200:203], v5, s[80:83], 0 offen offset:0 sc1\n"
    "v_mfma_f32_16x16x32_bf16 v[212:215], v[100:103], v[196:199], v[212:215]\n"
    "s_waitcnt lgkmcnt(0)\n"
    "v_cmp_gt_u32 vcc, 3, v228\n"
    "s_cbranch_vccz .Lh13g\n"
    ".Lh13r:\n"
    "s_sub_u32 s52, s52, 1\n"
    "s_cmp_lt_i32 s52, 0\n"
    "s_cbranch_scc1 .Lh13g\n"
    "ds_read_b32 v228, v224 offset:160\n"
    "s_waitcnt lgkmcnt(0)\n"
    "v_cmp_gt_u32 vcc, 3, v228\n"
    "s_cbranch_vccnz .Lh13r\n"
    "s_branch .Lh13g\n"
    ".Lh13g:\n"
    "ds_read_b64_tr_b16 v[152:153], v2 offset:6144\n"
    "ds_read_b64_tr_b16 v[154:155], v3 offset:6144\n"
    "ds_read_b64_tr_b16 v[156:157], v2 offset:7168\n"
    "ds_read_b64_tr_b16 v[158:159], v3 offset:7168\n"
    "ds_read_b64_tr_b16 v[160:161], v2 offset:8192\n"
    "ds_read_b64_tr_b16 v[162:163], v3 offset:8192\n"
    "ds_read_b64_tr_b16 v[164:165], v2 offset:9216\n"
    "ds_read_b64_tr_b16 v[166:167], v3 offset:9216\n"
    "ds_read_b64_tr_b16 v[168:169], v2 offset:10240\n"
    "ds_read_b64_tr_b16 v[170:171], v3 offset:10240\n"
    "ds_read_b64_tr_b16 v[172:173], v2 offset:11264\n"
    "ds_read_b64_tr_b16 v[174:175], v3 offset:11264\n"
    "ds_read_b32 v252, v1 offset:256\n"
    "s_branch .Lh13j\n"
    ".Lh14:\n"
    "v_mfma_f32_16x16x32_bf16 v[208:211], v[56:59], v[152:155], 0\n"
    "v_mfma_f32_16x16x32_bf16 v[212:215], v[80:83], v[152:155], 0\n"
    "v_mfma_f32_16x16x32_bf16 v[208:211], v[60:63], v[156:159], v[208:211]\n"
    "v_mfma_f32_16x16x32_bf16 v[212:215], v[84:87], v[156:159], v[212:215]\n"
    "v_mul_f32 v200, v200, v252\n"
    "v_mul_f32 v201, v201, v252\n"
    "v_mfma_f32_16x16x32_bf16 v[208:211], v[64:67], v[160:163], v[208:211]\n"
    "v_mul_f32 v202, v202, v252\n"
    "v_mul_f32 v203, v203, v252\n"
    "v_mfma_f32_16x16x32_bf16 v[212:215], v[88:91], v[160:163], v[212:215]\n"
    "ds_read_b32 v228, v224 offset:176\n"
    "v_mul_f32 v204, v204, v252\n"
    "v_mfma_f32_16x16x32_bf16 v[208:211], v[68:71], v[164:167], v[208:211]\n"
    "v_mul_f32 v205, v205, v252\n"
    "v_mul_f32 v206, v206, v252\n"
    "v_mfma_f32_16x16x32_bf16 v[212:215], v[92:95], v[164:167], v[212:215]\n"
    "v_mul_f32 v207, v207, v252\n"
    "v_cvt_pk_bf16_f32 v200, v200, v201\n"
    "v_mfma_f32_16x16x32_bf16 v[208:211], v[72:75], v[168:171], v[208:211]\n"
    "v_cvt_pk_bf16_f32 v201, v202, v203\n"
    "v_cvt_pk_bf16_f32 v202, v204, v205\n"
    "v_mfma_f32_16x16x32_bf16 v[212:215], v[96:99], v[168:171], v[212:215]\n"
    "v_cvt_pk_bf16_f32 v203, v206, v207\n"
    "v_mfma_f32_16x16x32_bf16 v[208:211], v[76:79], v[172:175], v[208:211]\n"
    "buffer_store_dwordx4 v[200:203], v4, s[80:83], 0 offen offset:0 sc1\n"
    "v_mfma_f32_16x16x32_bf16 v[212:215], v[100:103], v[172:175], v[212:215]\n"
    "s_waitcnt lgkmcnt(0)\n"
    "v_cmp_gt_u32 vcc, 3, v228\n"
    "s_cbranch_vccz .Lh14g\n"
    ".Lh14r:\n"
    "s_sub_u32 s52, s52, 1\n"
    "s_cmp_lt_i32 s52, 0\n"
    "s_cbranch_scc1 .Lh14g\n"
    "ds_read_b32 v228, v224 offset:176\n"
    "s_waitcnt lgkmcnt(0)\n"
    "v_cmp_gt_u32 vcc, 3, v228\n"
    "s_cbranch_vccnz .Lh14r\n"
    "s_branch .Lh14g\n"
    ".Lh14g:\n"
    "ds_read_b64_tr_b16 v[176:177], v2 offset:12288\n"
    "ds_read_b64_tr_b16 v[178:179], v3 offset:12288\n"
    "ds_read_b64_tr_b16 v[180:181], v2 offset:13312\n"
    "ds_read_b64_tr_b16 v[182:183], v3 offset:13312\n"
    "ds_read_b64_tr_b16 v[184:185], v2 offset:14336\n"
    "ds_read_b64_tr_b16 v[186:187], v3 offset:14336\n"
    "ds_read_b64_tr_b16 v[188:189], v2 offset:15360\n"
    "ds_read_b64_tr_b16 v[190:191], v3 offset:15360\n"
    "ds_read_b64_tr_b16 v[192:193], v2 offset:16384\n"
    "ds_read_b64_tr_b16 v[194:195], v3 offset:16384\n"
    "ds_read_b64_tr_b16 v[196:197], v2 offset:17408\n"
    "ds_read_b64_tr_b16 v[198:199], v3 offset:17408\n"
    "ds_read_b32 v253, v1 offset:320\n"
    "s_branch .Lh14j\n"
    ".Lh_e12:\n"
    "s_waitcnt lgkmcnt(1)\n"
    "v_mfma_f32_16x16x32_bf16 v[200:203], v[8:11], v[176:179], 0\n"
    "v_mul_f32 v208, v208, v252\n"
    "v_mul_f32 v209, v209, v252\n"
    "v_mfma_f32_16x16x32_bf16 v[204:207], v[32:35], v[176:179], 0\n"
    "v_mul_f32 v210, v210, v252\n"
    "v_mul_f32 v211, v211, v252\n"
    "v_mfma_f32_16x16x32_bf16 v[200:203], v[12:15], v[180:183], v[200:203]\n"
    "v_mul_f32 v212, v212, v252\n"
    "v_mul_f32 v213, v213, v252\n"
    "v_mfma_f32_16x16x32_bf16 v[204:207], v[36:39], v[180:183], v[204:207]\n"
    "v_mul_f32 v214, v214, v252\n"
    "v_mul_f32 v215, v215, v252\n"
    "v_mfma_f32_16x16x32_bf16 v[200:203], v[16:19], v[184:187], v[200:203]\n"
    "v_cvt_pk_bf16_f32 v208, v208, v209\n"
    "v_cvt_pk_bf16_f32 v209, v210, v211\n"
    "v_mfma_f32_16x16x32_bf16 v[204:207], v[40:43], v[184:187], v[204:207]\n"
    "v_cvt_pk_bf16_f32 v210, v212, v213\n"
    "v_cvt_pk_bf16_f32 v211, v214, v215\n"
    "v_mfma_f32_16x16x32_bf16 v[200:203], v[20:23], v[188:191], v[200:203]\n"
    "buffer_store_dwordx4 v[208:211], v4, s[80:83], 0 offen offset:64 sc1\n"
    "v_mfma_f32_16x16x32_bf16 v[204:207], v[44:47], v[188:191], v[204:207]\n"
    "v_mfma_f32_16x16x32_bf16 v[200:203], v[24:27], v[192:195], v[200:203]\n"
    "v_add_u32 v4, v0, v4\n"
    "v_mfma_f32_16x16x32_bf16 v[204:207], v[48:51], v[192:195], v[204:207]\n"
    "v_mfma_f32_16x16x32_bf16 v[200:203], v[28:31], v[196:199], v[200:203]\n"
    "v_mfma_f32_16x16x32_bf16 v[204:207], v[52:55], v[196:199], v[204:207]\n"
    "v_mfma_f32_16x16x32_bf16 v[208:211], v[56:59], v[176:179], 0\n"
    "v_mfma_f32_16x16x32_bf16 v[212:215], v[80:83], v[176:179], 0\n"
    "v_mfma_f32_16x16x32_bf16 v[208:211], v[60:63], v[180:183], v[208:211]\n"
    "v_mfma_f32_16x16x32_bf16 v[212:215], v[84:87], v[180:183], v[212:215]\n"
    "v_mfma_f32_16x16x32_bf16 v[208:211], v[64:67], v[184:187], v[208:211]\n"
    "v_mfma_f32_16x16x32_bf16 v[212:215], v[88:91], v[184:187], v[212:215]\n"
    "s_waitcnt lgkmcnt(0)\n"
    "v_mul_f32 v200, v200, v253\n"
    "v_mfma_f32_16x16x32_bf16 v[208:211], v[68:71], v[188:191], v[208:211]\n"
    "v_mul_f32 v201, v201, v253\n"
    "v_mul_f32 v202, v202, v253\n"
    "v_mfma_f32_16x16x32_bf16 v[212:215], v[92:95], v[188:191], v[212:215]\n"
    "v_mul_f32 v203, v203, v253\n"
    "v_mul_f32 v204, v204, v253\n"
    "v_mfma_f32_16x16x32_bf16 v[208:211], v[72:75], v[192:195], v[208:211]\n"
    "v_mul_f32 v205, v205, v253\n"
    "v_mul_f32 v206, v206, v253\n"
    "v_mfma_f32_16x16x32_bf16 v[212:215], v[96:99], v[192:195], v[212:215]\n"
    "v_mul_f32 v207, v207, v253\n"
    "v_cvt_pk_bf16_f32 v200, v200, v201\n"
    "v_mfma_f32_16x16x32_bf16 v[208:211], v[76:79], v[196:199], v[208:211]\n"
    "v_cvt_pk_bf16_f32 v201, v202, v203\n"
    "v_cvt_pk_bf16_f32 v202, v204, v205\n"
    "v_mfma_f32_16x16x32_bf16 v[212:215], v[100:103], v[196:199], v[212:215]\n"
    "v_cvt_pk_bf16_f32 v203, v206, v207\n"
    "buffer_store_dwordx4 v[200:203], v5, s[80:83], 0 offen offset:0 sc1\n"
    "s_nop 2\n"
    "v_mul_f32 v208, v208, v253\n"
    "v_mul_f32 v209, v209, v253\n"
    "v_mul_f32 v210, v210, v253\n"
    "v_mul_f32 v211, v211, v253\n"
    "v_mul_f32 v212, v212, v253\n"
    "v_mul_f32 v213, v213, v253\n"
    "v_mul_f32 v214, v214, v253\n"
    "v_mul_f32 v215, v215, v253\n"
    "v_cvt_pk_bf16_f32 v208, v208, v209\n"
    "v_cvt_pk_bf16_f32 v209, v210, v211\n"
    "v_cvt_pk_bf16_f32 v210, v212, v213\n"
    "v_cvt_pk_bf16_f32 v211, v214, v215\n"
    "buffer_store_dwordx4 v[208:211], v5, s[80:83], 0 offen offset:64 sc1\n"
    "s_branch .Lh_end\n"
    ".Lh15:\n"
    "v_mfma_f32_16x16x32_bf16 v[208:211], v[56:59], v[176:179], 0\n"
    "v_mfma_f32_16x16x32_bf16 v[212:215], v[80:83], v[176:179], 0\n"
    "v_mfma_f32_16x16x32_bf16 v[208:211], v[60:63], v[180:183], v[208:211]\n"
    "v_mfma_f32_16x16x32_bf16 v[212:215], v[84:87], v[180:183], v[212:215]\n"
    "v_mul_f32 v200, v200, v253\n"
    "v_mul_f32 v201, v201, v253\n"
    "v_mfma_f32_16x16x32_bf16 v[208:211], v[64:67], v[184:187], v[208:211]\n"
    "v_mul_f32 v202, v202, v253\n"
    "v_mul_f32 v203, v203, v253\n"
    "v_mfma_f32_16x16x32_bf16 v[212:215], v[88:91], v[184:187], v[212:215]\n"
    "ds_read_b32 v228, v224 offset:832\n"
    "v_mul_f32 v204, v204, v253\n"
    "v_mfma_f32_16x16x32_bf16 v[208:211], v[68:71], v[188:191], v[208:211]\n"
    "v_mul_f32 v205, v205, v253\n"
    "v_mul_f32 v206, v206, v253\n"
    "v_mfma_f32_16x16x32_bf16 v[212:215], v[92:95], v[188:191], v[212:215]\n"
    "v_mul_f32 v207, v207, v253\n"
    "v_cvt_pk_bf16_f32 v200, v200, v201\n"
    "v_mfma_f32_16x16x32_bf16 v[208:211], v[72:75], v[192:195], v[208:211]\n"
    "v_cvt_pk_bf16_f32 v201, v202, v203\n"
    "v_cvt_pk_bf16_f32 v202, v204, v205\n"
    "v_mfma_f32_16x16x32_bf16 v[212:215], v[96:99], v[192:195], v[212:215]\n"
    "v_cvt_pk_bf16_f32 v203, v206, v207\n"
    "v_mfma_f32_16x16x32_bf16 v[208:211], v[76:79], v[196:199], v[208:211]\n"
    "buffer_store_dwordx4 v[200:203], v5, s[80:83], 0 offen offset:0 sc1\n"
    "v_mfma_f32_16x16x32_bf16 v[212:215], v[100:103], v[196:199], v[212:215]\n"
    "s_waitcnt lgkmcnt(0)\n"
    "v_cmp_gt_u32 vcc, 4, v228\n"
    "s_cbranch_vccz .Lh15g\n"
    ".Lh15r:\n"
    "s_sub_u32 s52, s52, 1\n"
    "s_cmp_lt_i32 s52, 0\n"
    "s_cbranch_scc1 .Lh15g\n"
    "ds_read_b32 v228, v224 offset:832\n"
    "s_waitcnt lgkmcnt(0)\n"
    "v_cmp_gt_u32 vcc, 4, v228\n"
    "s_cbranch_vccnz .Lh15r\n"
    "s_branch .Lh15g\n"
    ".Lh15g:\n"
    "ds_read_b64_tr_b16 v[152:153], v2 offset:0\n"
    "ds_read_b64_tr_b16 v[154:155], v3 offset:0\n"
    "ds_read_b64_tr_b16 v[156:157], v2 offset:1024\n"
    "ds_read_b64_tr_b16 v[158:159], v3 offset:1024\n"
    "ds_read_b64_tr_b16 v[160:161], v2 offset:2048\n"
    "ds_read_b64_tr_b16 v[162:163], v3 offset:2048\n"
    "ds_read_b64_tr_b16 v[164:165], v2 offset:3072\n"
    "ds_read_b64_tr_b16 v[166:167], v3 offset:3072\n"
    "ds_read_b64_tr_b16 v[168:169], v2 offset:4096\n"
    "ds_read_b64_tr_b16 v[170:171], v3 offset:4096\n"
    "ds_read_b64_tr_b16 v[172:173], v2 offset:5120\n"
    "ds_read_b64_tr_b16 v[174:175], v3 offset:5120\n"
    "ds_read_b32 v252, v1 offset:192\n"
    "s_branch .Lh15j\n"
    ".Lh_last:\n"
                :: "{v1}"(vi), "{v[2:3]}"(vz), "{v[4:7]}"(vo), "{v[224:227]}"(fl), "{v235}"(lf[0]), "{v236}"(lf[1]),
                   "{v[238:239]}"(xp), "{s[40:41]}"(ws), "{s[42:43]}"(xb), "{v[230:233]}"(xo), "{v234}"(xo4),
                   "{s84}"(UNI(axis_at(rm, 12) * IROWB)), "{s85}"(UNI(axis_at(rm, 13) * IROWB)),
                   "{s46}"(UNI(2 * rm.d * IROWB)), "{v0}"(d56), "{s48}"(UNI(WS_ACK + (2 * bid + h) * 128)),
                   "{s50}"(h), "{s58}"(UNI(t12)), "{s59}"(UNI(t0 + (bid & 4 ? 4400 : 5200))),
                   "{s87}"(UNI(t0 + 7000 - (bid & 4 ? 5200 - 4400 : 0))), "{v[248:249]}"(uu), "{s[80:83]}"(srd)
                : "memory", "v255", "s51", "s52", "s53", "s54", "s55", "s56", "s57", "s60", "s61", "s76", "s77", "s79", "s88", "s89", "s90", "s91", "s98", "s99", "s92", "s93", "s94", "s95", "vcc", "scc");
        }
    } else {
        const int a = (0x3210 >> (4 * wave - 16)) & 3, H = a & 1, n0 = a, u0 = a >> 1;
        const AttC c = att_const(lane, H, cedge);
        f32x4 cbM, cbF, cbL;
        const float NEG = -__builtin_inff();
#pragma unroll
        for (int r = 0; r < 4; ++r) {
            const bool ok = (c.pk >> (4 + r)) & 1;
            cbM[r] = ok ? 0.f : NEG;
            cbF[r] = ok && e == 0 ? 0.f : NEG;
            cbL[r] = ok && e == 1 ? 0.f : NEG;
        }
        const int sl = n0 >= 3 ? n0 - 3 : n0;
        const u32x4 va = {c.sb, c.v0, c.v1, (unsigned)(LQ + sl * 6144 + lane * 16)};
        const u32x4 vb = {(unsigned)(LZ + sl * 6144 + (16 * g + (i16 ^ (4 * g))) * 16), (c.pk & 15) * 0x1111u, vf, lane < 4 ? vf + 16 * a : OOB};
        const u64 slots = (redge ? 0x975A86420ull : 0x420CA86420ull) >> (4 * u0);
        asm volatile("s_nop 7\n"
    "s_sub_u32 s56, s45, 2\n"
    "s_setprio 0\n"
    "s_cmp_lg_u32 s70, 0\n"
    "s_cbranch_scc1 .La_ld\n"
    ".La_join:\n"
    "s_mov_b32 s52, 0x40000\n"
    "s_bfe_u32 s48, s60, 0x40000\n"
    "s_bfe_u32 s49, s60, 0x40004\n"
    "s_bfe_u32 s50, s60, 0x40008\n"
    "s_bfe_u32 s51, s60, 0x4000c\n"
    "s_mul_i32 s48, s48, 8896\n"
    "s_mul_i32 s49, s49, 8896\n"
    "s_mul_i32 s50, s50, 8896\n"
    "s_mul_i32 s51, s51, 8896\n"
    "s_lshr_b64 s[60:61], s[60:61], 8\n"
    "v_add_u32 v244, s48, v0\n"
    "v_add_u32 v245, s49, v0\n"
    "v_add_u32 v246, s50, v0\n"
    "v_add_u32 v247, s51, v0\n"
    "v_add_u32 v200, s48, v1\n"
    "v_add_u32 v201, s49, v1\n"
    "v_add_u32 v202, s48, v2\n"
    "v_add_u32 v203, s49, v2\n"
    "s_mov_b32 s69, 1\n"
    "s_mov_b32 s53, 0\n"
    "s_add_u32 s55, s45, 1\n"
    "s_lshr_b32 s57, s45, 2\n"
    "v_mov_b32 v248, 0x3f803f80\n"
    "v_mov_b32 v249, 0x3f803f80\n"
    "v_mov_b32 v250, 0x3f803f80\n"
    "v_mov_b32 v251, 0x3f803f80\n"
    "s_mul_i32 s62, s57, 6\n"
    "ds_read_b32 v253, v6 offset:0\n"
    "s_waitcnt lgkmcnt(0)\n"
    "v_cmp_gt_u32 vcc, s54, v253\n"
    "s_cbranch_vccnz .La1\n"
    ".La1b:\n"
    "ds_read_b32 v254, v6 offset:16\n"
    "s_waitcnt lgkmcnt(0)\n"
    "v_cmp_gt_u32 vcc, s55, v254\n"
    "s_cbranch_vccz .La_nf\n"
    "s_mov_b32 s69, 0\n"
    "ds_read_b64_tr_b16 v[104:105], v200 offset:0\n"
    "ds_read_b64_tr_b16 v[106:107], v201 offset:0\n"
    "ds_read_b64_tr_b16 v[108:109], v202 offset:0\n"
    "ds_read_b64_tr_b16 v[110:111], v203 offset:0\n"
    "ds_read_b64_tr_b16 v[112:113], v200 offset:32\n"
    "ds_read_b64_tr_b16 v[114:115], v201 offset:32\n"
    "ds_read_b64_tr_b16 v[116:117], v202 offset:32\n"
    "ds_read_b64_tr_b16 v[118:119], v203 offset:32\n"
    "ds_read_b64_tr_b16 v[120:121], v200 offset:64\n"
    "ds_read_b64_tr_b16 v[122:123], v201 offset:64\n"
    "ds_read_b64_tr_b16 v[124:125], v202 offset:64\n"
    "ds_read_b64_tr_b16 v[126:127], v203 offset:64\n"
    "ds_read_b64_tr_b16 v[128:129], v200 offset:96\n"
    "ds_read_b64_tr_b16 v[130:131], v201 offset:96\n"
    "ds_read_b64_tr_b16 v[132:133], v202 offset:96\n"
    "ds_read_b64_tr_b16 v[134:135], v203 offset:96\n"
    "ds_read_b64_tr_b16 v[136:137], v200 offset:128\n"
    "ds_read_b64_tr_b16 v[138:139], v201 offset:128\n"
    "ds_read_b64_tr_b16 v[140:141], v202 offset:128\n"
    "ds_read_b64_tr_b16 v[142:143], v203 offset:128\n"
    "ds_read_b64_tr_b16 v[144:145], v200 offset:160\n"
    "ds_read_b64_tr_b16 v[146:147], v201 offset:160\n"
    "ds_read_b64_tr_b16 v[148:149], v202 offset:160\n"
    "ds_read_b64_tr_b16 v[150:151], v203 offset:160\n"
    "ds_read_b64_tr_b16 v[152:153], v200 offset:192\n"
    "ds_read_b64_tr_b16 v[154:155], v201 offset:192\n"
    "ds_read_b64_tr_b16 v[156:157], v202 offset:192\n"
    "ds_read_b64_tr_b16 v[158:159], v203 offset:192\n"
    "ds_read_b64_tr_b16 v[160:161], v200 offset:224\n"
    "ds_read_b64_tr_b16 v[162:163], v201 offset:224\n"
    "ds_read_b64_tr_b16 v[164:165], v202 offset:224\n"
    "ds_read_b64_tr_b16 v[166:167], v203 offset:224\n"
    "ds_read_b64_tr_b16 v[168:169], v200 offset:256\n"
    "ds_read_b64_tr_b16 v[170:171], v201 offset:256\n"
    "ds_read_b64_tr_b16 v[172:173], v202 offset:256\n"
    "ds_read_b64_tr_b16 v[174:175], v203 offset:256\n"
    "ds_read_b64_tr_b16 v[176:177], v200 offset:288\n"
    "ds_read_b64_tr_b16 v[178:179], v201 offset:288\n"
    "ds_read_b64_tr_b16 v[180:181], v202 offset:288\n"
    "ds_read_b64_tr_b16 v[182:183], v203 offset:288\n"
    "ds_read_b64_tr_b16 v[184:185], v200 offset:320\n"
    "ds_read_b64_tr_b16 v[186:187], v201 offset:320\n"
    "ds_read_b64_tr_b16 v[188:189], v202 offset:320\n"
    "ds_read_b64_tr_b16 v[190:191], v203 offset:320\n"
    "ds_read_b64_tr_b16 v[192:193], v200 offset:352\n"
    "ds_read_b64_tr_b16 v[194:195], v201 offset:352\n"
    "ds_read_b64_tr_b16 v[196:197], v202 offset:352\n"
    "ds_read_b64_tr_b16 v[198:199], v203 offset:352\n"
    "ds_read_b32 v254, v6 offset:16\n"
    "s_waitcnt lgkmcnt(0)\n"
    "v_cmp_gt_u32 vcc, s55, v254\n"
    "s_cbranch_vccnz .La2\n"
    ".La2b:\n"
    ".La_nf:\n"
    "ds_read_b128 v[8:11], v3 offset:0\n"
    "ds_read_b128 v[56:59], v244 offset:0\n"
    "ds_read_b128 v[12:15], v3 offset:1024\n"
    "ds_read_b128 v[60:63], v244 offset:64\n"
    "ds_read_b128 v[16:19], v3 offset:2048\n"
    "ds_read_b128 v[64:67], v244 offset:128\n"
    "ds_read_b128 v[20:23], v3 offset:3072\n"
    "ds_read_b128 v[68:71], v244 offset:192\n"
    "ds_read_b128 v[24:27], v3 offset:4096\n"
    "ds_read_b128 v[28:31], v3 offset:5120\n"
    "ds_read_b128 v[32:35], v244 offset:256\n"
    "ds_read_b128 v[36:39], v244 offset:320\n"
    "ds_read_b128 v[40:43], v245 offset:0\n"
    "ds_read_b128 v[44:47], v245 offset:64\n"
    ".La_loop:\n"
    "s_setprio 1\n"
    "s_add_u32 s57, s57, 1\n"
    "s_waitcnt lgkmcnt(8)\n"
    "v_mfma_f32_16x16x32_bf16 v[72:75], v[56:59], v[8:11], v[236:239]\n"
    "ds_read_b128 v[48:51], v245 offset:128\n"
    "v_mov_b32 v252, s57\n"
    "v_mfma_f32_16x16x32_bf16 v[72:75], v[60:63], v[12:15], v[72:75]\n"
    "ds_read_b128 v[52:55], v245 offset:192\n"
    "v_add_u32 v4, s53, v4\n"
    "v_mfma_f32_16x16x32_bf16 v[72:75], v[64:67], v[16:19], v[72:75]\n"
    "ds_read_b128 v[56:59], v245 offset:256\n"
    "s_waitcnt lgkmcnt(5)\n"
    "v_mfma_f32_16x16x32_bf16 v[72:75], v[68:71], v[20:23], v[72:75]\n"
    "ds_read_b128 v[60:63], v245 offset:320\n"
    "ds_write_b32 v7, v252 offset:192\n"
    "v_mfma_f32_16x16x32_bf16 v[72:75], v[32:35], v[24:27], v[72:75]\n"
    "ds_read_b128 v[64:67], v244 offset:8896\n"
    "ds_read_b128 v[208:211], v7 offset:32768\n"
    "v_mfma_f32_16x16x32_bf16 v[72:75], v[36:39], v[28:31], v[72:75]\n"
    "ds_read_b128 v[68:71], v244 offset:8960\n"
    "s_waitcnt lgkmcnt(7)\n"
    "v_mfma_f32_16x16x32_bf16 v[76:79], v[40:43], v[8:11], v[232:235]\n"
    "ds_read_b128 v[32:35], v244 offset:9024\n"
    "ds_read_b128 v[212:215], v7 offset:32768\n"
    "v_mfma_f32_16x16x32_bf16 v[76:79], v[44:47], v[12:15], v[76:79]\n"
    "ds_read_b128 v[36:39], v244 offset:9088\n"
    "ds_read_b128 v[216:219], v7 offset:32768\n"
    "v_mfma_f32_16x16x32_bf16 v[76:79], v[48:51], v[16:19], v[76:79]\n"
    "ds_read_b128 v[40:43], v244 offset:9152\n"
    "s_waitcnt lgkmcnt(9)\n"
    "v_mfma_f32_16x16x32_bf16 v[76:79], v[52:55], v[20:23], v[76:79]\n"
    "ds_read_b128 v[44:47], v244 offset:9216\n"
    "v_max_f32 v72, v72, v73\n"
    "v_mfma_f32_16x16x32_bf16 v[76:79], v[56:59], v[24:27], v[76:79]\n"
    "ds_read_b128 v[48:51], v245 offset:8896\n"
    "v_max_f32 v74, v74, v75\n"
    "v_mfma_f32_16x16x32_bf16 v[76:79], v[60:63], v[28:31], v[76:79]\n"
    "ds_read_b128 v[52:55], v245 offset:8960\n"
    "s_waitcnt lgkmcnt(7)\n"
    "v_mfma_f32_16x16x32_bf16 v[80:83], v[64:67], v[8:11], v[232:235]\n"
    "ds_read_b128 v[56:59], v245 offset:9024\n"
    "v_max_f32 v206, v72, v74\n"
    "v_mfma_f32_16x16x32_bf16 v[80:83], v[68:71], v[12:15], v[80:83]\n"
    "ds_read_b128 v[60:63], v245 offset:9088\n"
    "ds_read_b128 v[220:223], v7 offset:32768\n"
    "v_mfma_f32_16x16x32_bf16 v[80:83], v[32:35], v[16:19], v[80:83]\n"
    "ds_read_b128 v[64:67], v245 offset:9152\n"
    "s_waitcnt lgkmcnt(6)\n"
    "v_mfma_f32_16x16x32_bf16 v[80:83], v[36:39], v[20:23], v[80:83]\n"
    "ds_read_b128 v[68:71], v245 offset:9216\n"
    "v_max_f32 v76, v76, v77\n"
    "v_mfma_f32_16x16x32_bf16 v[80:83], v[40:43], v[24:27], v[80:83]\n"
    "ds_read_b128 v[32:35], v246 offset:0\n"
    "v_max_f32 v78, v78, v79\n"
    "v_mfma_f32_16x16x32_bf16 v[80:83], v[44:47], v[28:31], v[80:83]\n"
    "ds_read_b128 v[36:39], v246 offset:64\n"
    "s_waitcnt lgkmcnt(6)\n"
    "v_mfma_f32_16x16x32_bf16 v[84:87], v[48:51], v[8:11], v[232:235]\n"
    "ds_read_b128 v[40:43], v246 offset:128\n"
    "v_max3_f32 v206, v206, v76, v78\n"
    "v_mfma_f32_16x16x32_bf16 v[84:87], v[52:55], v[12:15], v[84:87]\n"
    "ds_read_b128 v[44:47], v246 offset:192\n"
    "ds_read_b128 v[224:227], v7 offset:32768\n"
    "v_mfma_f32_16x16x32_bf16 v[84:87], v[56:59], v[16:19], v[84:87]\n"
    "ds_read_b128 v[48:51], v246 offset:256\n"
    "s_waitcnt lgkmcnt(6)\n"
    "v_mfma_f32_16x16x32_bf16 v[84:87], v[60:63], v[20:23], v[84:87]\n"
    "ds_read_b128 v[52:55], v246 offset:320\n"
    "v_max_f32 v80, v80, v81\n"
    "v_mfma_f32_16x16x32_bf16 v[84:87], v[64:67], v[24:27], v[84:87]\n"
    "ds_read_b128 v[56:59], v247 offset:0\n"
    "v_max_f32 v82, v82, v83\n"
    "v_mfma_f32_16x16x32_bf16 v[84:87], v[68:71], v[28:31], v[84:87]\n"
    "ds_read_b128 v[60:63], v247 offset:64\n"
    "s_waitcnt lgkmcnt(6)\n"
    "v_mfma_f32_16x16x32_bf16 v[88:91], v[32:35], v[8:11], v[232:235]\n"
    "ds_read_b128 v[64:67], v247 offset:128\n"
    "v_max3_f32 v206, v206, v80, v82\n"
    "v_mfma_f32_16x16x32_bf16 v[88:91], v[36:39], v[12:15], v[88:91]\n"
    "ds_read_b128 v[68:71], v247 offset:192\n"
    "ds_read_b128 v[228:231], v7 offset:32768\n"
    "v_mfma_f32_16x16x32_bf16 v[88:91], v[40:43], v[16:19], v[88:91]\n"
    "ds_read_b128 v[32:35], v247 offset:256\n"
    "s_waitcnt lgkmcnt(6)\n"
    "v_mfma_f32_16x16x32_bf16 v[88:91], v[44:47], v[20:23], v[88:91]\n"
    "ds_read_b128 v[36:39], v247 offset:320\n"
    "v_max_f32 v84, v84, v85\n"
    "v_mfma_f32_16x16x32_bf16 v[88:91], v[48:51], v[24:27], v[88:91]\n"
    "ds_read_b128 v[40:43], v246 offset:8896\n"
    "v_max_f32 v86, v86, v87\n"
    "v_mfma_f32_16x16x32_bf16 v[88:91], v[52:55], v[28:31], v[88:91]\n"
    "ds_read_b128 v[44:47], v246 offset:8960\n"
    "s_waitcnt lgkmcnt(6)\n"
    "v_mfma_f32_16x16x32_bf16 v[92:95], v[56:59], v[8:11], v[232:235]\n"
    "ds_read_b128 v[48:51], v246 offset:9024\n"
    "v_max3_f32 v206, v206, v84, v86\n"
    "v_mfma_f32_16x16x32_bf16 v[92:95], v[60:63], v[12:15], v[92:95]\n"
    "ds_read_b128 v[52:55], v246 offset:9088\n"
    "v_mfma_f32_16x16x32_bf16 v[92:95], v[64:67], v[16:19], v[92:95]\n"
    "ds_read_b128 v[56:59], v246 offset:9152\n"
    "s_waitcnt lgkmcnt(5)\n"
    "v_mfma_f32_16x16x32_bf16 v[92:95], v[68:71], v[20:23], v[92:95]\n"
    "ds_read_b128 v[60:63], v246 offset:9216\n"
    "v_max_f32 v88, v88, v89\n"
    "v_mfma_f32_16x16x32_bf16 v[92:95], v[32:35], v[24:27], v[92:95]\n"
    "ds_read_b128 v[64:67], v247 offset:8896\n"
    "v_max_f32 v90, v90, v91\n"
    "v_mfma_f32_16x16x32_bf16 v[92:95], v[36:39], v[28:31], v[92:95]\n"
    "ds_read_b128 v[68:71], v247 offset:8960\n"
    "s_waitcnt lgkmcnt(5)\n"
    "v_mfma_f32_16x16x32_bf16 v[96:99], v[40:43], v[8:11], v[232:235]\n"
    "ds_read_b128 v[32:35], v247 offset:9024\n"
    "v_max3_f32 v206, v206, v88, v90\n"
    "v_mfma_f32_16x16x32_bf16 v[96:99], v[44:47], v[12:15], v[96:99]\n"
    "ds_read_b128 v[36:39], v247 offset:9088\n"
    "v_mfma_f32_16x16x32_bf16 v[96:99], v[48:51], v[16:19], v[96:99]\n"
    "ds_read_b128 v[40:43], v247 offset:9152\n"
    "s_waitcnt lgkmcnt(5)\n"
    "v_mfma_f32_16x16x32_bf16 v[96:99], v[52:55], v[20:23], v[96:99]\n"
    "ds_read_b128 v[44:47], v247 offset:9216\n"
    "v_max_f32 v92, v92, v93\n"
    "v_mfma_f32_16x16x32_bf16 v[96:99], v[56:59], v[24:27], v[96:99]\n"
    "v_add_u32 v244, s50, v1\n"
    "v_add_u32 v245, s51, v1\n"
    "v_mfma_f32_16x16x32_bf16 v[96:99], v[60:63], v[28:31], v[96:99]\n"
    "v_add_u32 v246, s50, v2\n"
    "s_waitcnt lgkmcnt(3)\n"
    "v_mfma_f32_16x16x32_bf16 v[100:103], v[64:67], v[8:11], v[240:243]\n"
    "v_add_u32 v247, s51, v2\n"
    "v_max_f32 v94, v94, v95\n"
    "v_mfma_f32_16x16x32_bf16 v[100:103], v[68:71], v[12:15], v[100:103]\n"
    "ds_read_b64_tr_b16 v[48:49], v244 offset:160\n"
    "v_max3_f32 v206, v206, v92, v94\n"
    "v_mfma_f32_16x16x32_bf16 v[100:103], v[32:35], v[16:19], v[100:103]\n"
    "ds_read_b64_tr_b16 v[50:51], v245 offset:160\n"
    "s_waitcnt lgkmcnt(2)\n"
    "v_mfma_f32_16x16x32_bf16 v[100:103], v[36:39], v[20:23], v[100:103]\n"
    "ds_read_b64_tr_b16 v[52:53], v246 offset:160\n"
    "v_max_f32 v96, v96, v97\n"
    "v_mfma_f32_16x16x32_bf16 v[100:103], v[40:43], v[24:27], v[100:103]\n"
    "ds_read_b64_tr_b16 v[54:55], v247 offset:160\n"
    "v_max_f32 v98, v98, v99\n"
    "v_mfma_f32_16x16x32_bf16 v[100:103], v[44:47], v[28:31], v[100:103]\n"
    "ds_read_b64_tr_b16 v[56:57], v244 offset:192\n"
    "ds_read_b64_tr_b16 v[58:59], v245 offset:192\n"
    "s_cmp_lg_u32 s69, 0\n"
    "s_cbranch_scc1 .La_fill\n"
    ".La_filled:\n"
    "ds_write_b32 v7, v252 offset:64\n"
    "s_setprio 0\n"
    "v_max3_f32 v206, v206, v96, v98\n"
    "ds_read_b64_tr_b16 v[60:61], v246 offset:192\n"
    "v_max_f32 v100, v100, v101\n"
    "v_max_f32 v102, v102, v103\n"
    "v_max3_f32 v206, v206, v100, v102\n"
    "v_mov_b32 v207, v206\n"
    "ds_read_b64_tr_b16 v[62:63], v247 offset:192\n"
    "ds_read_b64_tr_b16 v[64:65], v244 offset:224\n"
    "v_permlane16_swap_b32 v206, v207\n"
    "v_max_f32 v206, v206, v207\n"
    "v_mov_b32 v207, v206\n"
    "ds_read_b64_tr_b16 v[66:67], v245 offset:224\n"
    "ds_read_b64_tr_b16 v[68:69], v246 offset:224\n"
    "v_permlane32_swap_b32 v206, v207\n"
    "v_max_f32 v206, v206, v207\n"
    "v_mul_f32 v207, s46, v206\n"
    "v_fma_f32 v72, v72, s47, v207\n"
    "v_fma_f32 v74, v74, s47, v207\n"
    "v_fma_f32 v76, v76, s47, v207\n"
    "v_fma_f32 v78, v78, s47, v207\n"
    "v_fma_f32 v80, v80, s47, v207\n"
    "v_fma_f32 v82, v82, s47, v207\n"
    "v_fma_f32 v84, v84, s47, v207\n"
    "v_fma_f32 v86, v86, s47, v207\n"
    "v_fma_f32 v88, v88, s47, v207\n"
    "v_fma_f32 v90, v90, s47, v207\n"
    "v_fma_f32 v92, v92, s47, v207\n"
    "v_fma_f32 v94, v94, s47, v207\n"
    "v_fma_f32 v96, v96, s47, v207\n"
    "v_fma_f32 v98, v98, s47, v207\n"
    "v_fma_f32 v100, v100, s47, v207\n"
    "v_fma_f32 v102, v102, s47, v207\n"
    "ds_read_b64_tr_b16 v[70:71], v247 offset:224\n"
    "ds_read_b64_tr_b16 v[32:33], v244 offset:96\n"
    "ds_read_b64_tr_b16 v[34:35], v245 offset:96\n"
    "ds_read_b64_tr_b16 v[36:37], v246 offset:96\n"
    "ds_read_b64_tr_b16 v[38:39], v247 offset:96\n"
    "ds_read_b64_tr_b16 v[40:41], v244 offset:128\n"
    "ds_read_b64_tr_b16 v[42:43], v245 offset:128\n"
    "ds_read_b64_tr_b16 v[44:45], v246 offset:128\n"
    "ds_read_b64_tr_b16 v[46:47], v247 offset:128\n"
    "ds_read_b64_tr_b16 v[8:9], v244 offset:0\n"
    "ds_read_b64_tr_b16 v[10:11], v245 offset:0\n"
    "ds_read_b64_tr_b16 v[12:13], v246 offset:0\n"
    "ds_read_b64_tr_b16 v[14:15], v247 offset:0\n"
    "ds_read_b64_tr_b16 v[16:17], v244 offset:32\n"
    "ds_read_b64_tr_b16 v[18:19], v245 offset:32\n"
    "ds_read_b64_tr_b16 v[20:21], v246 offset:32\n"
    "ds_read_b64_tr_b16 v[22:23], v247 offset:32\n"
    "ds_read_b64_tr_b16 v[24:25], v244 offset:64\n"
    "ds_read_b64_tr_b16 v[26:27], v245 offset:64\n"
    "ds_read_b64_tr_b16 v[28:29], v246 offset:64\n"
    "ds_read_b64_tr_b16 v[30:31], v247 offset:64\n"
    "ds_read_b32 v253, v6 offset:32\n"
    "v_exp_f32 v72, v72\n"
    "s_bfe_u32 s48, s60, 0x40000\n"
    "v_exp_f32 v74, v74\n"
    "s_bfe_u32 s49, s60, 0x40004\n"
    "v_exp_f32 v76, v76\n"
    "s_bfe_u32 s50, s60, 0x40008\n"
    "v_exp_f32 v78, v78\n"
    "s_bfe_u32 s51, s60, 0x4000c\n"
    "v_exp_f32 v80, v80\n"
    "s_mul_i32 s48, s48, 8896\n"
    "v_exp_f32 v82, v82\n"
    "s_mul_i32 s49, s49, 8896\n"
    "v_exp_f32 v84, v84\n"
    "s_mul_i32 s50, s50, 8896\n"
    "v_exp_f32 v86, v86\n"
    "s_mul_i32 s51, s51, 8896\n"
    "v_exp_f32 v88, v88\n"
    "s_lshr_b64 s[60:61], s[60:61], 8\n"
    "v_exp_f32 v90, v90\n"
    "s_add_u32 s45, s45, 4\n"
    "v_exp_f32 v92, v92\n"
    "s_add_u32 s54, s54, 2\n"
    "v_exp_f32 v94, v94\n"
    "s_add_u32 s55, s55, 4\n"
    "v_exp_f32 v96, v96\n"
    "s_cmp_eq_u32 s66, 2\n"
    "s_cselect_b32 s67, -2, 1\n"
    "v_exp_f32 v98, v98\n"
    "s_add_u32 s66, s66, s67\n"
    "v_exp_f32 v100, v100\n"
    "s_mul_i32 s68, s67, 6144\n"
    "v_exp_f32 v102, v102\n"
    "v_cvt_pk_bf16_f32 v200, v72, v74\n"
    "v_cvt_pk_bf16_f32 v201, v76, v78\n"
    "v_cvt_pk_bf16_f32 v202, v80, v82\n"
    "v_cvt_pk_bf16_f32 v203, v84, v86\n"
    "v_cvt_pk_bf16_f32 v204, v88, v90\n"
    "v_cvt_pk_bf16_f32 v205, v92, v94\n"
    "v_cvt_pk_bf16_f32 v206, v96, v98\n"
    "v_cvt_pk_bf16_f32 v207, v100, v102\n"
    "ds_read_b64_tr_b16 v[72:73], v244 offset:256\n"
    "ds_read_b64_tr_b16 v[74:75], v245 offset:256\n"
    "ds_read_b64_tr_b16 v[76:77], v246 offset:256\n"
    "ds_read_b64_tr_b16 v[78:79], v247 offset:256\n"
    "ds_read_b64_tr_b16 v[80:81], v244 offset:288\n"
    "ds_read_b64_tr_b16 v[82:83], v245 offset:288\n"
    "ds_read_b64_tr_b16 v[84:85], v246 offset:288\n"
    "ds_read_b64_tr_b16 v[86:87], v247 offset:288\n"
    "s_waitcnt lgkmcnt(8)\n"
    "s_setprio 1\n"
    "v_mfma_f32_16x16x32_bf16 v[100:103], v[248:251], v[200:203], 0\n"
    "v_cmp_gt_i32 vcc, s56, v253\n"
    "v_mfma_f32_16x16x32_bf16 v[100:103], v[248:251], v[204:207], v[100:103]\n"
    "v_smfmac_f32_16x16x64_bf16 v[220:223], v[200:203], v[128:135], v5\n"
    "ds_read_b128 v[128:131], v7 offset:32768\n"
    "v_smfmac_f32_16x16x64_bf16 v[224:227], v[200:203], v[136:143], v5\n"
    "ds_read_b128 v[132:135], v7 offset:32768\n"
    "v_smfmac_f32_16x16x64_bf16 v[228:231], v[200:203], v[144:151], v5\n"
    "ds_read_b128 v[136:139], v7 offset:32768\n"
    "v_smfmac_f32_16x16x64_bf16 v[208:211], v[200:203], v[104:111], v5\n"
    "ds_read_b128 v[140:143], v7 offset:32768\n"
    "v_smfmac_f32_16x16x64_bf16 v[212:215], v[200:203], v[112:119], v5\n"
    "ds_read_b128 v[144:147], v7 offset:32768\n"
    "v_smfmac_f32_16x16x64_bf16 v[216:219], v[200:203], v[120:127], v5\n"
    "ds_read_b128 v[148:151], v7 offset:32768\n"
    "v_smfmac_f32_16x16x64_bf16 v[208:211], v[204:207], v[8:15], v5\n"
    "ds_read_b64_tr_b16 v[88:89], v244 offset:320\n"
    "v_rcp_f32 v100, v100\n"
    "v_smfmac_f32_16x16x64_bf16 v[212:215], v[204:207], v[16:23], v5\n"
    "ds_read_b64_tr_b16 v[90:91], v245 offset:320\n"
    "v_lshrrev_b32 v101, 2, v3\n"
    "v_smfmac_f32_16x16x64_bf16 v[216:219], v[204:207], v[24:31], v5\n"
    "ds_read_b64_tr_b16 v[92:93], v246 offset:320\n"
    "v_and_or_b32 v101, v101, 60, s63\n"
    "v_smfmac_f32_16x16x64_bf16 v[220:223], v[204:207], v[32:39], v5\n"
    "ds_read_b64_tr_b16 v[94:95], v247 offset:320\n"
    "s_cbranch_vccnz .La3\n"
    ".La3b:\n"
    "v_smfmac_f32_16x16x64_bf16 v[224:227], v[204:207], v[40:47], v5\n"
    "ds_read_b64_tr_b16 v[96:97], v244 offset:352\n"
    "ds_read_b64_tr_b16 v[98:99], v245 offset:352\n"
    "v_smfmac_f32_16x16x64_bf16 v[228:231], v[204:207], v[48:55], v5\n"
    "s_waitcnt lgkmcnt(6)\n"
    "v_smfmac_f32_16x16x64_bf16 v[128:131], v[200:203], v[152:159], v5\n"
    "ds_write_b32 v101, v100 offset:0\n"
    "ds_read_b64_tr_b16 v[100:101], v246 offset:352\n"
    "v_smfmac_f32_16x16x64_bf16 v[132:135], v[200:203], v[160:167], v5\n"
    "ds_read_b64_tr_b16 v[102:103], v247 offset:352\n"
    "v_smfmac_f32_16x16x64_bf16 v[136:139], v[200:203], v[168:175], v5\n"
    "v_cvt_pk_bf16_f32 v208, v208, v209\n"
    "v_cvt_pk_bf16_f32 v209, v210, v211\n"
    "v_smfmac_f32_16x16x64_bf16 v[140:143], v[200:203], v[176:183], v5\n"
    "v_cvt_pk_bf16_f32 v210, v212, v213\n"
    "v_cvt_pk_bf16_f32 v211, v214, v215\n"
    "v_smfmac_f32_16x16x64_bf16 v[144:147], v[200:203], v[184:191], v5\n"
    "ds_write_b128 v4, v[208:211] offset:0\n"
    "v_smfmac_f32_16x16x64_bf16 v[148:151], v[200:203], v[192:199], v5\n"
    "v_cvt_pk_bf16_f32 v216, v216, v217\n"
    "v_cvt_pk_bf16_f32 v217, v218, v219\n"
    "v_smfmac_f32_16x16x64_bf16 v[128:131], v[204:207], v[56:63], v5\n"
    "ds_read_b32 v254, v6 offset:16\n"
    "ds_read_b32 v200, v6 offset:0\n"
    "v_smfmac_f32_16x16x64_bf16 v[132:135], v[204:207], v[64:71], v5\n"
    "v_cvt_pk_bf16_f32 v218, v220, v221\n"
    "s_waitcnt lgkmcnt(8)\n"
    "v_smfmac_f32_16x16x64_bf16 v[136:139], v[204:207], v[72:79], v5\n"
    "v_cvt_pk_bf16_f32 v219, v222, v223\n"
    "v_smfmac_f32_16x16x64_bf16 v[140:143], v[204:207], v[80:87], v5\n"
    "ds_write_b128 v4, v[216:219] offset:1024\n"
    "v_smfmac_f32_16x16x64_bf16 v[144:147], v[204:207], v[88:95], v5\n"
    "v_cvt_pk_bf16_f32 v224, v224, v225\n"
    "s_waitcnt lgkmcnt(4)\n"
    "v_smfmac_f32_16x16x64_bf16 v[148:151], v[204:207], v[96:103], v5\n"
    "v_cvt_pk_bf16_f32 v225, v226, v227\n"
    "v_cvt_pk_bf16_f32 v226, v228, v229\n"
    "s_setprio 0\n"
    "v_add_u32 v244, s48, v0\n"
    "v_add_u32 v245, s49, v0\n"
    "v_add_u32 v246, s50, v0\n"
    "v_add_u32 v247, s51, v0\n"
    "v_add_u32 v3, s68, v3\n"
    "v_cvt_pk_bf16_f32 v227, v230, v231\n"
    "ds_write_b128 v4, v[224:227] offset:2048\n"
    "s_waitcnt lgkmcnt(3)\n"
    "v_cmp_gt_u32 vcc, s55, v254\n"
    "s_waitcnt lgkmcnt(2)\n"
    "v_cmp_gt_u32 s[58:59], s54, v200\n"
    "s_add_u32 s56, s56, 4\n"
    "s_lshl_b32 s67, s67, 6\n"
    "s_add_u32 s63, s63, s67\n"
    "s_sub_u32 s53, s68, 18432\n"
    "s_add_u32 s67, s63, 192\n"
    "s_cmp_ge_u32 s45, 10\n"
    "s_cselect_b32 s53, s53, s68\n"
    "s_cselect_b32 s63, s67, s63\n"
    "s_cselect_b32 s56, -16, s56\n"
    "s_cmp_ge_u32 s45, 12\n"
    "s_cbranch_scc1 .La4\n"
    "s_or_b64 vcc, vcc, s[58:59]\n"
    "s_cbranch_scc1 .La4\n"
    "ds_read_b128 v[104:107], v3 offset:0\n"
    "ds_read_b128 v[152:155], v244 offset:0\n"
    "ds_read_b128 v[108:111], v3 offset:1024\n"
    "ds_read_b128 v[156:159], v244 offset:64\n"
    "ds_read_b128 v[112:115], v3 offset:2048\n"
    "ds_read_b128 v[160:163], v244 offset:128\n"
    "ds_read_b128 v[116:119], v3 offset:3072\n"
    "ds_read_b128 v[164:167], v244 offset:192\n"
    "ds_read_b128 v[120:123], v3 offset:4096\n"
    "ds_read_b128 v[124:127], v3 offset:5120\n"
    "v_cvt_pk_bf16_f32 v128, v128, v129\n"
    "v_cvt_pk_bf16_f32 v129, v130, v131\n"
    "v_cvt_pk_bf16_f32 v130, v132, v133\n"
    "v_cvt_pk_bf16_f32 v131, v134, v135\n"
    "ds_write_b128 v4, v[128:131] offset:3072\n"
    "v_cvt_pk_bf16_f32 v136, v136, v137\n"
    "v_cvt_pk_bf16_f32 v137, v138, v139\n"
    "v_cvt_pk_bf16_f32 v138, v140, v141\n"
    "v_cvt_pk_bf16_f32 v139, v142, v143\n"
    "ds_write_b128 v4, v[136:139] offset:4096\n"
    "v_cvt_pk_bf16_f32 v144, v144, v145\n"
    "v_cvt_pk_bf16_f32 v145, v146, v147\n"
    "v_cvt_pk_bf16_f32 v146, v148, v149\n"
    "v_cvt_pk_bf16_f32 v147, v150, v151\n"
    "ds_write_b128 v4, v[144:147] offset:5120\n"
    "ds_write_b32 v7, v252 offset:128\n"
    "ds_read_b128 v[128:131], v244 offset:256\n"
    "ds_read_b128 v[132:135], v244 offset:320\n"
    "ds_read_b128 v[136:139], v245 offset:0\n"
    "ds_read_b128 v[140:143], v245 offset:64\n"
    ".La4j:\n"
    "s_setprio 1\n"
    "s_add_u32 s57, s57, 1\n"
    "s_waitcnt lgkmcnt(8)\n"
    "v_mfma_f32_16x16x32_bf16 v[168:171], v[152:155], v[104:107], v[236:239]\n"
    "ds_read_b128 v[144:147], v245 offset:128\n"
    "v_mov_b32 v252, s57\n"
    "v_mfma_f32_16x16x32_bf16 v[168:171], v[156:159], v[108:111], v[168:171]\n"
    "ds_read_b128 v[148:151], v245 offset:192\n"
    "v_add_u32 v4, s53, v4\n"
    "v_mfma_f32_16x16x32_bf16 v[168:171], v[160:163], v[112:115], v[168:171]\n"
    "ds_read_b128 v[152:155], v245 offset:256\n"
    "s_waitcnt lgkmcnt(5)\n"
    "v_mfma_f32_16x16x32_bf16 v[168:171], v[164:167], v[116:119], v[168:171]\n"
    "ds_read_b128 v[156:159], v245 offset:320\n"
    "ds_write_b32 v7, v252 offset:192\n"
    "v_mfma_f32_16x16x32_bf16 v[168:171], v[128:131], v[120:123], v[168:171]\n"
    "ds_read_b128 v[160:163], v244 offset:8896\n"
    "ds_read_b128 v[208:211], v7 offset:32768\n"
    "v_mfma_f32_16x16x32_bf16 v[168:171], v[132:135], v[124:127], v[168:171]\n"
    "ds_read_b128 v[164:167], v244 offset:8960\n"
    "s_waitcnt lgkmcnt(7)\n"
    "v_mfma_f32_16x16x32_bf16 v[172:175], v[136:139], v[104:107], v[232:235]\n"
    "ds_read_b128 v[128:131], v244 offset:9024\n"
    "ds_read_b128 v[212:215], v7 offset:32768\n"
    "v_mfma_f32_16x16x32_bf16 v[172:175], v[140:143], v[108:111], v[172:175]\n"
    "ds_read_b128 v[132:135], v244 offset:9088\n"
    "ds_read_b128 v[216:219], v7 offset:32768\n"
    "v_mfma_f32_16x16x32_bf16 v[172:175], v[144:147], v[112:115], v[172:175]\n"
    "ds_read_b128 v[136:139], v244 offset:9152\n"
    "s_waitcnt lgkmcnt(9)\n"
    "v_mfma_f32_16x16x32_bf16 v[172:175], v[148:151], v[116:119], v[172:175]\n"
    "ds_read_b128 v[140:143], v244 offset:9216\n"
    "v_max_f32 v168, v168, v169\n"
    "v_mfma_f32_16x16x32_bf16 v[172:175], v[152:155], v[120:123], v[172:175]\n"
    "ds_read_b128 v[144:147], v245 offset:8896\n"
    "v_max_f32 v170, v170, v171\n"
    "v_mfma_f32_16x16x32_bf16 v[172:175], v[156:159], v[124:127], v[172:175]\n"
    "ds_read_b128 v[148:151], v245 offset:8960\n"
    "s_waitcnt lgkmcnt(7)\n"
    "v_mfma_f32_16x16x32_bf16 v[176:179], v[160:163], v[104:107], v[232:235]\n"
    "ds_read_b128 v[152:155], v245 offset:9024\n"
    "v_max_f32 v206, v168, v170\n"
    "v_mfma_f32_16x16x32_bf16 v[176:179], v[164:167], v[108:111], v[176:179]\n"
    "ds_read_b128 v[156:159], v245 offset:9088\n"
    "ds_read_b128 v[220:223], v7 offset:32768\n"
    "v_mfma_f32_16x16x32_bf16 v[176:179], v[128:131], v[112:115], v[176:179]\n"
    "ds_read_b128 v[160:163], v245 offset:9152\n"
    "s_waitcnt lgkmcnt(6)\n"
    "v_mfma_f32_16x16x32_bf16 v[176:179], v[132:135], v[116:119], v[176:179]\n"
    "ds_read_b128 v[164:167], v245 offset:9216\n"
    "v_max_f32 v172, v172, v173\n"
    "v_mfma_f32_16x16x32_bf16 v[176:179], v[136:139], v[120:123], v[176:179]\n"
    "ds_read_b128 v[128:131], v246 offset:0\n"
    "v_max_f32 v174, v174, v175\n"
    "v_mfma_f32_16x16x32_bf16 v[176:179], v[140:143], v[124:127], v[176:179]\n"
    "ds_read_b128 v[132:135], v246 offset:64\n"
    "s_waitcnt lgkmcnt(6)\n"
    "v_mfma_f32_16x16x32_bf16 v[180:183], v[144:147], v[104:107], v[232:235]\n"
    "ds_read_b128 v[136:139], v246 offset:128\n"
    "v_max3_f32 v206, v206, v172, v174\n"
    "v_mfma_f32_16x16x32_bf16 v[180:183], v[148:151], v[108:111], v[180:183]\n"
    "ds_read_b128 v[140:143], v246 offset:192\n"
    "ds_read_b128 v[224:227], v7 offset:32768\n"
    "v_mfma_f32_16x16x32_bf16 v[180:183], v[152:155], v[112:115], v[180:183]\n"
    "ds_read_b128 v[144:147], v246 offset:256\n"
    "s_waitcnt lgkmcnt(6)\n"
    "v_mfma_f32_16x16x32_bf16 v[180:183], v[156:159], v[116:119], v[180:183]\n"
    "ds_read_b128 v[148:151], v246 offset:320\n"
    "v_max_f32 v176, v176, v177\n"
    "v_mfma_f32_16x16x32_bf16 v[180:183], v[160:163], v[120:123], v[180:183]\n"
    "ds_read_b128 v[152:155], v247 offset:0\n"
    "v_max_f32 v178, v178, v179\n"
    "v_mfma_f32_16x16x32_bf16 v[180:183], v[164:167], v[124:127], v[180:183]\n"
    "ds_read_b128 v[156:159], v247 offset:64\n"
    "s_waitcnt lgkmcnt(6)\n"
    "v_mfma_f32_16x16x32_bf16 v[184:187], v[128:131], v[104:107], v[232:235]\n"
    "ds_read_b128 v[160:163], v247 offset:128\n"
    "v_max3_f32 v206, v206, v176, v178\n"
    "v_mfma_f32_16x16x32_bf16 v[184:187], v[132:135], v[108:111], v[184:187]\n"
    "ds_read_b128 v[164:167], v247 offset:192\n"
    "ds_read_b128 v[228:231], v7 offset:32768\n"
    "v_mfma_f32_16x16x32_bf16 v[184:187], v[136:139], v[112:115], v[184:187]\n"
    "ds_read_b128 v[128:131], v247 offset:256\n"
    "s_waitcnt lgkmcnt(6)\n"
    "v_mfma_f32_16x16x32_bf16 v[184:187], v[140:143], v[116:119], v[184:187]\n"
    "ds_read_b128 v[132:135], v247 offset:320\n"
    "v_max_f32 v180, v180, v181\n"
    "v_mfma_f32_16x16x32_bf16 v[184:187], v[144:147], v[120:123], v[184:187]\n"
    "ds_read_b128 v[136:139], v246 offset:8896\n"
    "v_max_f32 v182, v182, v183\n"
    "v_mfma_f32_16x16x32_bf16 v[184:187], v[148:151], v[124:127], v[184:187]\n"
    "ds_read_b128 v[140:143], v246 offset:8960\n"
    "s_waitcnt lgkmcnt(6)\n"
    "v_mfma_f32_16x16x32_bf16 v[188:191], v[152:155], v[104:107], v[232:235]\n"
    "ds_read_b128 v[144:147], v246 offset:9024\n"
    "v_max3_f32 v206, v206, v180, v182\n"
    "v_mfma_f32_16x16x32_bf16 v[188:191], v[156:159], v[108:111], v[188:191]\n"
    "ds_read_b128 v[148:151], v246 offset:9088\n"
    "v_mfma_f32_16x16x32_bf16 v[188:191], v[160:163], v[112:115], v[188:191]\n"
    "ds_read_b128 v[152:155], v246 offset:9152\n"
    "s_waitcnt lgkmcnt(5)\n"
    "v_mfma_f32_16x16x32_bf16 v[188:191], v[164:167], v[116:119], v[188:191]\n"
    "ds_read_b128 v[156:159], v246 offset:9216\n"
    "v_max_f32 v184, v184, v185\n"
    "v_mfma_f32_16x16x32_bf16 v[188:191], v[128:131], v[120:123], v[188:191]\n"
    "ds_read_b128 v[160:163], v247 offset:8896\n"
    "v_max_f32 v186, v186, v187\n"
    "v_mfma_f32_16x16x32_bf16 v[188:191], v[132:135], v[124:127], v[188:191]\n"
    "ds_read_b128 v[164:167], v247 offset:8960\n"
    "s_waitcnt lgkmcnt(5)\n"
    "v_mfma_f32_16x16x32_bf16 v[192:195], v[136:139], v[104:107], v[232:235]\n"
    "ds_read_b128 v[128:131], v247 offset:9024\n"
    "v_max3_f32 v206, v206, v184, v186\n"
    "v_mfma_f32_16x16x32_bf16 v[192:195], v[140:143], v[108:111], v[192:195]\n"
    "ds_read_b128 v[132:135], v247 offset:9088\n"
    "v_mfma_f32_16x16x32_bf16 v[192:195], v[144:147], v[112:115], v[192:195]\n"
    "ds_read_b128 v[136:139], v247 offset:9152\n"
    "s_waitcnt lgkmcnt(5)\n"
    "v_mfma_f32_16x16x32_bf16 v[192:195], v[148:151], v[116:119], v[192:195]\n"
    "ds_read_b128 v[140:143], v247 offset:9216\n"
    "v_max_f32 v188, v188, v189\n"
    "v_mfma_f32_16x16x32_bf16 v[192:195], v[152:155], v[120:123], v[192:195]\n"
    "v_add_u32 v244, s50, v1\n"
    "v_add_u32 v245, s51, v1\n"
    "v_mfma_f32_16x16x32_bf16 v[192:195], v[156:159], v[124:127], v[192:195]\n"
    "v_add_u32 v246, s50, v2\n"
    "s_waitcnt lgkmcnt(3)\n"
    "v_mfma_f32_16x16x32_bf16 v[196:199], v[160:163], v[104:107], v[240:243]\n"
    "v_add_u32 v247, s51, v2\n"
    "v_max_f32 v190, v190, v191\n"
    "v_mfma_f32_16x16x32_bf16 v[196:199], v[164:167], v[108:111], v[196:199]\n"
    "ds_read_b64_tr_b16 v[144:145], v244 offset:160\n"
    "v_max3_f32 v206, v206, v188, v190\n"
    "v_mfma_f32_16x16x32_bf16 v[196:199], v[128:131], v[112:115], v[196:199]\n"
    "ds_read_b64_tr_b16 v[146:147], v245 offset:160\n"
    "s_waitcnt lgkmcnt(2)\n"
    "v_mfma_f32_16x16x32_bf16 v[196:199], v[132:135], v[116:119], v[196:199]\n"
    "ds_read_b64_tr_b16 v[148:149], v246 offset:160\n"
    "v_max_f32 v192, v192, v193\n"
    "v_mfma_f32_16x16x32_bf16 v[196:199], v[136:139], v[120:123], v[196:199]\n"
    "ds_read_b64_tr_b16 v[150:151], v247 offset:160\n"
    "v_max_f32 v194, v194, v195\n"
    "v_mfma_f32_16x16x32_bf16 v[196:199], v[140:143], v[124:127], v[196:199]\n"
    "ds_read_b64_tr_b16 v[152:153], v244 offset:192\n"
    "ds_read_b64_tr_b16 v[154:155], v245 offset:192\n"
    "ds_write_b32 v7, v252 offset:64\n"
    "s_setprio 0\n"
    "v_max3_f32 v206, v206, v192, v194\n"
    "ds_read_b64_tr_b16 v[156:157], v246 offset:192\n"
    "ds_read_b64_tr_b16 v[158:159], v247 offset:192\n"
    "ds_read_b64_tr_b16 v[160:161], v244 offset:224\n"
    "v_max_f32 v196, v196, v197\n"
    "v_max_f32 v198, v198, v199\n"
    "v_max3_f32 v206, v206, v196, v198\n"
    "v_mov_b32 v207, v206\n"
    "ds_read_b64_tr_b16 v[162:163], v245 offset:224\n"
    "ds_read_b64_tr_b16 v[164:165], v246 offset:224\n"
    "v_permlane16_swap_b32 v206, v207\n"
    "v_max_f32 v206, v206, v207\n"
    "v_mov_b32 v207, v206\n"
    "ds_read_b64_tr_b16 v[166:167], v247 offset:224\n"
    "ds_read_b64_tr_b16 v[128:129], v244 offset:96\n"
    "v_permlane32_swap_b32 v206, v207\n"
    "v_max_f32 v206, v206, v207\n"
    "v_mul_f32 v207, s46, v206\n"
    "v_fma_f32 v168, v168, s47, v207\n"
    "v_fma_f32 v170, v170, s47, v207\n"
    "v_fma_f32 v172, v172, s47, v207\n"
    "v_fma_f32 v174, v174, s47, v207\n"
    "v_fma_f32 v176, v176, s47, v207\n"
    "v_fma_f32 v178, v178, s47, v207\n"
    "v_fma_f32 v180, v180, s47, v207\n"
    "v_fma_f32 v182, v182, s47, v207\n"
    "v_fma_f32 v184, v184, s47, v207\n"
    "v_fma_f32 v186, v186, s47, v207\n"
    "v_fma_f32 v188, v188, s47, v207\n"
    "v_fma_f32 v190, v190, s47, v207\n"
    "v_fma_f32 v192, v192, s47, v207\n"
    "v_fma_f32 v194, v194, s47, v207\n"
    "v_fma_f32 v196, v196, s47, v207\n"
    "v_fma_f32 v198, v198, s47, v207\n"
    "ds_read_b64_tr_b16 v[130:131], v245 offset:96\n"
    "ds_read_b64_tr_b16 v[132:133], v246 offset:96\n"
    "ds_read_b64_tr_b16 v[134:135], v247 offset:96\n"
    "ds_read_b64_tr_b16 v[136:137], v244 offset:128\n"
    "ds_read_b64_tr_b16 v[138:139], v245 offset:128\n"
    "ds_read_b64_tr_b16 v[140:141], v246 offset:128\n"
    "ds_read_b64_tr_b16 v[142:143], v247 offset:128\n"
    "ds_read_b64_tr_b16 v[104:105], v244 offset:0\n"
    "ds_read_b64_tr_b16 v[106:107], v245 offset:0\n"
    "ds_read_b64_tr_b16 v[108:109], v246 offset:0\n"
    "ds_read_b64_tr_b16 v[110:111], v247 offset:0\n"
    "ds_read_b64_tr_b16 v[112:113], v244 offset:32\n"
    "ds_read_b64_tr_b16 v[114:115], v245 offset:32\n"
    "ds_read_b64_tr_b16 v[116:117], v246 offset:32\n"
    "ds_read_b64_tr_b16 v[118:119], v247 offset:32\n"
    "ds_read_b64_tr_b16 v[120:121], v244 offset:64\n"
    "ds_read_b64_tr_b16 v[122:123], v245 offset:64\n"
    "ds_read_b64_tr_b16 v[124:125], v246 offset:64\n"
    "ds_read_b64_tr_b16 v[126:127], v247 offset:64\n"
    "ds_read_b32 v253, v6 offset:32\n"
    "v_exp_f32 v168, v168\n"
    "s_bfe_u32 s48, s60, 0x40000\n"
    "v_exp_f32 v170, v170\n"
    "s_bfe_u32 s49, s60, 0x40004\n"
    "v_exp_f32 v172, v172\n"
    "s_bfe_u32 s50, s60, 0x40008\n"
    "v_exp_f32 v174, v174\n"
    "s_bfe_u32 s51, s60, 0x4000c\n"
    "v_exp_f32 v176, v176\n"
    "s_mul_i32 s48, s48, 8896\n"
    "v_exp_f32 v178, v178\n"
    "s_mul_i32 s49, s49, 8896\n"
    "v_exp_f32 v180, v180\n"
    "s_mul_i32 s50, s50, 8896\n"
    "v_exp_f32 v182, v182\n"
    "s_mul_i32 s51, s51, 8896\n"
    "v_exp_f32 v184, v184\n"
    "s_lshr_b64 s[60:61], s[60:61], 8\n"
    "v_exp_f32 v186, v186\n"
    "s_add_u32 s45, s45, 4\n"
    "v_exp_f32 v188, v188\n"
    "s_add_u32 s54, s54, 2\n"
    "v_exp_f32 v190, v190\n"
    "s_add_u32 s55, s55, 4\n"
    "v_exp_f32 v192, v192\n"
    "s_cmp_eq_u32 s66, 2\n"
    "s_cselect_b32 s67, -2, 1\n"
    "v_exp_f32 v194, v194\n"
    "s_add_u32 s66, s66, s67\n"
    "v_exp_f32 v196, v196\n"
    "s_mul_i32 s68, s67, 6144\n"
    "v_exp_f32 v198, v198\n"
    "v_cvt_pk_bf16_f32 v200, v168, v170\n"
    "v_cvt_pk_bf16_f32 v201, v172, v174\n"
    "v_cvt_pk_bf16_f32 v202, v176, v178\n"
    "v_cvt_pk_bf16_f32 v203, v180, v182\n"
    "v_cvt_pk_bf16_f32 v204, v184, v186\n"
    "v_cvt_pk_bf16_f32 v205, v188, v190\n"
    "v_cvt_pk_bf16_f32 v206, v192, v194\n"
    "v_cvt_pk_bf16_f32 v207, v196, v198\n"
    "ds_read_b64_tr_b16 v[168:169], v244 offset:256\n"
    "ds_read_b64_tr_b16 v[170:171], v245 offset:256\n"
    "ds_read_b64_tr_b16 v[172:173], v246 offset:256\n"
    "ds_read_b64_tr_b16 v[174:175], v247 offset:256\n"
    "ds_read_b64_tr_b16 v[176:177], v244 offset:288\n"
    "ds_read_b64_tr_b16 v[178:179], v245 offset:288\n"
    "ds_read_b64_tr_b16 v[180:181], v246 offset:288\n"
    "ds_read_b64_tr_b16 v[182:183], v247 offset:288\n"
    "s_waitcnt lgkmcnt(8)\n"
    "s_setprio 1\n"
    "v_mfma_f32_16x16x32_bf16 v[196:199], v[248:251], v[200:203], 0\n"
    "v_cmp_gt_i32 vcc, s56, v253\n"
    "v_mfma_f32_16x16x32_bf16 v[196:199], v[248:251], v[204:207], v[196:199]\n"
    "v_smfmac_f32_16x16x64_bf16 v[220:223], v[200:203], v[32:39], v5\n"
    "ds_read_b128 v[32:35], v7 offset:32768\n"
    "v_smfmac_f32_16x16x64_bf16 v[224:227], v[200:203], v[40:47], v5\n"
    "ds_read_b128 v[36:39], v7 offset:32768\n"
    "v_smfmac_f32_16x16x64_bf16 v[228:231], v[200:203], v[48:55], v5\n"
    "ds_read_b128 v[40:43], v7 offset:32768\n"
    "v_smfmac_f32_16x16x64_bf16 v[208:211], v[200:203], v[8:15], v5\n"
    "ds_read_b128 v[44:47], v7 offset:32768\n"
    "v_smfmac_f32_16x16x64_bf16 v[212:215], v[200:203], v[16:23], v5\n"
    "ds_read_b128 v[48:51], v7 offset:32768\n"
    "v_smfmac_f32_16x16x64_bf16 v[216:219], v[200:203], v[24:31], v5\n"
    "ds_read_b128 v[52:55], v7 offset:32768\n"
    "v_smfmac_f32_16x16x64_bf16 v[208:211], v[204:207], v[104:111], v5\n"
    "ds_read_b64_tr_b16 v[184:185], v244 offset:320\n"
    "v_rcp_f32 v196, v196\n"
    "v_smfmac_f32_16x16x64_bf16 v[212:215], v[204:207], v[112:119], v5\n"
    "ds_read_b64_tr_b16 v[186:187], v245 offset:320\n"
    "v_lshrrev_b32 v197, 2, v3\n"
    "v_smfmac_f32_16x16x64_bf16 v[216:219], v[204:207], v[120:127], v5\n"
    "ds_read_b64_tr_b16 v[188:189], v246 offset:320\n"
    "v_and_or_b32 v197, v197, 60, s63\n"
    "v_smfmac_f32_16x16x64_bf16 v[220:223], v[204:207], v[128:135], v5\n"
    "ds_read_b64_tr_b16 v[190:191], v247 offset:320\n"
    "s_cbranch_vccnz .La7\n"
    ".La7b:\n"
    "v_smfmac_f32_16x16x64_bf16 v[224:227], v[204:207], v[136:143], v5\n"
    "ds_read_b64_tr_b16 v[192:193], v244 offset:352\n"
    "ds_read_b64_tr_b16 v[194:195], v245 offset:352\n"
    "v_smfmac_f32_16x16x64_bf16 v[228:231], v[204:207], v[144:151], v5\n"
    "s_waitcnt lgkmcnt(6)\n"
    "v_smfmac_f32_16x16x64_bf16 v[32:35], v[200:203], v[56:63], v5\n"
    "ds_write_b32 v197, v196 offset:0\n"
    "ds_read_b64_tr_b16 v[196:197], v246 offset:352\n"
    "v_smfmac_f32_16x16x64_bf16 v[36:39], v[200:203], v[64:71], v5\n"
    "ds_read_b64_tr_b16 v[198:199], v247 offset:352\n"
    "v_smfmac_f32_16x16x64_bf16 v[40:43], v[200:203], v[72:79], v5\n"
    "v_cvt_pk_bf16_f32 v208, v208, v209\n"
    "v_cvt_pk_bf16_f32 v209, v210, v211\n"
    "v_smfmac_f32_16x16x64_bf16 v[44:47], v[200:203], v[80:87], v5\n"
    "v_cvt_pk_bf16_f32 v210, v212, v213\n"
    "v_cvt_pk_bf16_f32 v211, v214, v215\n"
    "v_smfmac_f32_16x16x64_bf16 v[48:51], v[200:203], v[88:95], v5\n"
    "ds_write_b128 v4, v[208:211] offset:0\n"
    "v_smfmac_f32_16x16x64_bf16 v[52:55], v[200:203], v[96:103], v5\n"
    "v_cvt_pk_bf16_f32 v216, v216, v217\n"
    "v_cvt_pk_bf16_f32 v217, v218, v219\n"
    "v_smfmac_f32_16x16x64_bf16 v[32:35], v[204:207], v[152:159], v5\n"
    "ds_read_b32 v254, v6 offset:16\n"
    "ds_read_b32 v200, v6 offset:0\n"
    "v_smfmac_f32_16x16x64_bf16 v[36:39], v[204:207], v[160:167], v5\n"
    "v_cvt_pk_bf16_f32 v218, v220, v221\n"
    "s_waitcnt lgkmcnt(8)\n"
    "v_smfmac_f32_16x16x64_bf16 v[40:43], v[204:207], v[168:175], v5\n"
    "v_cvt_pk_bf16_f32 v219, v222, v223\n"
    "v_smfmac_f32_16x16x64_bf16 v[44:47], v[204:207], v[176:183], v5\n"
    "ds_write_b128 v4, v[216:219] offset:1024\n"
    "v_smfmac_f32_16x16x64_bf16 v[48:51], v[204:207], v[184:191], v5\n"
    "v_cvt_pk_bf16_f32 v224, v224, v225\n"
    "s_waitcnt lgkmcnt(4)\n"
    "v_smfmac_f32_16x16x64_bf16 v[52:55], v[204:207], v[192:199], v5\n"
    "v_cvt_pk_bf16_f32 v225, v226, v227\n"
    "v_cvt_pk_bf16_f32 v226, v228, v229\n"
    "s_setprio 0\n"
    "v_add_u32 v244, s48, v0\n"
    "v_add_u32 v245, s49, v0\n"
    "v_add_u32 v246, s50, v0\n"
    "v_add_u32 v247, s51, v0\n"
    "v_add_u32 v3, s68, v3\n"
    "v_cvt_pk_bf16_f32 v227, v230, v231\n"
    "ds_write_b128 v4, v[224:227] offset:2048\n"
    "s_waitcnt lgkmcnt(3)\n"
    "v_cmp_gt_u32 vcc, s55, v254\n"
    "s_waitcnt lgkmcnt(2)\n"
    "v_cmp_gt_u32 s[58:59], s54, v200\n"
    "s_add_u32 s56, s56, 4\n"
    "s_lshl_b32 s67, s67, 6\n"
    "s_add_u32 s63, s63, s67\n"
    "s_sub_u32 s53, s68, 18432\n"
    "s_add_u32 s67, s63, 192\n"
    "s_cmp_ge_u32 s45, 10\n"
    "s_cselect_b32 s53, s53, s68\n"
    "s_cselect_b32 s63, s67, s63\n"
    "s_cselect_b32 s56, -16, s56\n"
    "s_cmp_ge_u32 s45, 12\n"
    "s_cbranch_scc1 .La8\n"
    "s_or_b64 vcc, vcc, s[58:59]\n"
    "s_cbranch_scc1 .La8\n"
    "ds_read_b128 v[8:11], v3 offset:0\n"
    "ds_read_b128 v[56:59], v244 offset:0\n"
    "ds_read_b128 v[12:15], v3 offset:1024\n"
    "ds_read_b128 v[60:63], v244 offset:64\n"
    "ds_read_b128 v[16:19], v3 offset:2048\n"
    "ds_read_b128 v[64:67], v244 offset:128\n"
    "ds_read_b128 v[20:23], v3 offset:3072\n"
    "ds_read_b128 v[68:71], v244 offset:192\n"
    "ds_read_b128 v[24:27], v3 offset:4096\n"
    "ds_read_b128 v[28:31], v3 offset:5120\n"
    "v_cvt_pk_bf16_f32 v32, v32, v33\n"
    "v_cvt_pk_bf16_f32 v33, v34, v35\n"
    "v_cvt_pk_bf16_f32 v34, v36, v37\n"
    "v_cvt_pk_bf16_f32 v35, v38, v39\n"
    "ds_write_b128 v4, v[32:35] offset:3072\n"
    "v_cvt_pk_bf16_f32 v40, v40, v41\n"
    "v_cvt_pk_bf16_f32 v41, v42, v43\n"
    "v_cvt_pk_bf16_f32 v42, v44, v45\n"
    "v_cvt_pk_bf16_f32 v43, v46, v47\n"
    "ds_write_b128 v4, v[40:43] offset:4096\n"
    "v_cvt_pk_bf16_f32 v48, v48, v49\n"
    "v_cvt_pk_bf16_f32 v49, v50, v51\n"
    "v_cvt_pk_bf16_f32 v50, v52, v53\n"
    "v_cvt_pk_bf16_f32 v51, v54, v55\n"
    "ds_write_b128 v4, v[48:51] offset:5120\n"
    "ds_write_b32 v7, v252 offset:128\n"
    "ds_read_b128 v[32:35], v244 offset:256\n"
    "ds_read_b128 v[36:39], v244 offset:320\n"
    "ds_read_b128 v[40:43], v245 offset:0\n"
    "ds_read_b128 v[44:47], v245 offset:64\n"
    ".La8j:\n"
    "s_branch .La_loop\n"
    ".La_end:\n"
    "s_waitcnt lgkmcnt(0)\n"
    "s_endpgm\n"
    ".La_ld:\n"
    "s_mov_b32 s52, 0x40000\n"
    "ds_read_b32 v253, v6 offset:256\n"
    "s_waitcnt lgkmcnt(0)\n"
    "v_cmp_gt_u32 vcc, 1, v253\n"
    "s_cbranch_vccnz .Lb1\n"
    ".Lb1b:\n"
    "s_cmp_eq_u32 s70, 2\n"
    "s_cbranch_scc1 .La_l0\n"
    "s_cmp_eq_u32 s70, 3\n"
    "s_cbranch_scc1 .La_l1\n"
    "s_cmp_eq_u32 s70, 1\n"
    "s_cbranch_scc1 .La_l7\n"
    "ds_read_b32 v253, v6 offset:304\n"
    "s_waitcnt lgkmcnt(0)\n"
    "v_cmp_gt_u32 vcc, 1, v253\n"
    "s_cbranch_vccnz .Lb2\n"
    ".Lb2b:\n"
    ".La_l7:\n"
    "v_cmp_gt_u32 vcc, 2, v18\n"
    "v_mov_b32 v19, 0x100000\n"
    "v_mov_b32 v252, 1\n"
    "v_cndmask_b32 v254, v19, v6, vcc\n"
    "s_mul_i32 s48, s75, 9\n"
    "s_add_u32 s48, s48, s74\n"
    "s_add_u32 s50, s72, s48\n"
    "s_addc_u32 s51, s73, 0\n"
    "s_mov_b32 m0, 81088\n"
    "s_nop 0\n"
    "global_load_lds_dwordx4 v13, s[50:51]\n"
    "s_mov_b32 m0, 82112\n"
    "s_nop 0\n"
    "global_load_lds_dwordx4 v8, s[50:51]\n"
    "s_mov_b32 m0, 83136\n"
    "s_nop 0\n"
    "global_load_lds_dwordx4 v14, s[50:51]\n"
    "s_mov_b32 m0, 84160\n"
    "s_nop 0\n"
    "global_load_lds_dwordx4 v9, s[50:51]\n"
    "s_mov_b32 m0, 85184\n"
    "s_nop 0\n"
    "global_load_lds_dwordx4 v15, s[50:51]\n"
    "s_mov_b32 m0, 86208\n"
    "s_nop 0\n"
    "global_load_lds_dwordx4 v10, s[50:51]\n"
    "s_mov_b32 m0, 87232\n"
    "s_nop 0\n"
    "global_load_lds_dwordx4 v16, s[50:51]\n"
    "s_mov_b32 m0, 80064\n"
    "s_nop 0\n"
    "global_load_lds_dwordx4 v17, s[50:51]\n"
    "s_mov_b32 m0, 87840\n"
    "s_nop 0\n"
    "global_load_lds_dwordx4 v12, s[50:51]\n"
    "s_mul_i32 s48, s75, 10\n"
    "s_add_u32 s48, s48, s74\n"
    "s_add_u32 s50, s72, s48\n"
    "s_addc_u32 s51, s73, 0\n"
    "s_mov_b32 m0, 89984\n"
    "s_nop 0\n"
    "global_load_lds_dwordx4 v13, s[50:51]\n"
    "s_mov_b32 m0, 91008\n"
    "s_nop 0\n"
    "global_load_lds_dwordx4 v8, s[50:51]\n"
    "s_mov_b32 m0, 92032\n"
    "s_nop 0\n"
    "global_load_lds_dwordx4 v14, s[50:51]\n"
    "s_mov_b32 m0, 93056\n"
    "s_nop 0\n"
    "global_load_lds_dwordx4 v9, s[50:51]\n"
    "s_mov_b32 m0, 94080\n"
    "s_nop 0\n"
    "global_load_lds_dwordx4 v15, s[50:51]\n"
    "s_mov_b32 m0, 95104\n"
    "s_nop 0\n"
    "global_load_lds_dwordx4 v10, s[50:51]\n"
    "s_mov_b32 m0, 96128\n"
    "s_nop 0\n"
    "global_load_lds_dwordx4 v16, s[50:51]\n"
    "s_mov_b32 m0, 88960\n"
    "s_nop 0\n"
    "global_load_lds_dwordx4 v17, s[50:51]\n"
    "s_mov_b32 m0, 96736\n"
    "s_nop 0\n"
    "global_load_lds_dwordx4 v12, s[50:51]\n"
    "s_mul_i32 s48, s75, 11\n"
    "s_add_u32 s48, s48, s74\n"
    "s_add_u32 s50, s72, s48\n"
    "s_addc_u32 s51, s73, 0\n"
    "s_mov_b32 m0, 98880\n"
    "s_nop 0\n"
    "global_load_lds_dwordx4 v13, s[50:51]\n"
    "s_mov_b32 m0, 99904\n"
    "s_nop 0\n"
    "global_load_lds_dwordx4 v8, s[50:51]\n"
    "s_mov_b32 m0, 100928\n"
    "s_nop 0\n"
    "global_load_lds_dwordx4 v14, s[50:51]\n"
    "s_mov_b32 m0, 101952\n"
    "s_nop 0\n"
    "global_load_lds_dwordx4 v9, s[50:51]\n"
    "s_mov_b32 m0, 102976\n"
    "s_nop 0\n"
    "global_load_lds_dwordx4 v15, s[50:51]\n"
    "s_mov_b32 m0, 104000\n"
    "s_nop 0\n"
    "global_load_lds_dwordx4 v10, s[50:51]\n"
    "s_mov_b32 m0, 105024\n"
    "s_nop 0\n"
    "global_load_lds_dwordx4 v16, s[50:51]\n"
    "s_mov_b32 m0, 97856\n"
    "s_nop 0\n"
    "global_load_lds_dwordx4 v17, s[50:51]\n"
    "s_mov_b32 m0, 105632\n"
    "s_nop 0\n"
    "global_load_lds_dwordx4 v12, s[50:51]\n"
    "ds_write_b32 v6, v252 offset:288\n"
    "ds_read_b32 v253, v6 offset:0\n"
    "s_waitcnt lgkmcnt(0)\n"
    "v_cmp_gt_u32 vcc, 4, v253\n"
    "s_cbranch_vccnz .Lb3\n"
    ".Lb3b:\n"
    "s_waitcnt vmcnt(18)\n"
    "v_mov_b32 v252, 5\n"
    "ds_write_b32 v254, v252 offset:0\n"
    "s_waitcnt vmcnt(0)\n"
    "v_mov_b32 v252, 6\n"
    "ds_write_b32 v254, v252 offset:0\n"
    "s_branch .La_join\n"
    ".Lb1:\n"
    "s_sub_u32 s52, s52, 1\n"
    "s_cmp_lt_i32 s52, 0\n"
    "s_cbranch_scc1 .Lb1b\n"
    "ds_read_b32 v253, v6 offset:256\n"
    "s_waitcnt lgkmcnt(0)\n"
    "v_cmp_gt_u32 vcc, 1, v253\n"
    "s_cbranch_vccnz .Lb1\n"
    "s_branch .Lb1b\n"
    ".La_l0:\n"
    "s_mul_i32 s48, s75, 2\n"
    "s_add_u32 s48, s48, s74\n"
    "s_add_u32 s50, s72, s48\n"
    "s_addc_u32 s51, s73, 0\n"
    "s_mov_b32 m0, 18816\n"
    "s_nop 0\n"
    "global_load_lds_dwordx4 v8, s[50:51]\n"
    "s_mov_b32 m0, 20864\n"
    "s_nop 0\n"
    "global_load_lds_dwordx4 v9, s[50:51]\n"
    "s_mov_b32 m0, 22912\n"
    "s_nop 0\n"
    "global_load_lds_dwordx4 v10, s[50:51]\n"
    "s_mov_b32 m0, 24960\n"
    "s_nop 0\n"
    "global_load_lds_dwordx4 v11, s[50:51]\n"
    "s_mov_b32 m0, 17792\n"
    "s_nop 0\n"
    "global_load_lds_dwordx4 v12, s[50:51]\n"
    "s_mul_i32 s48, s75, 1\n"
    "s_add_u32 s48, s48, s74\n"
    "s_add_u32 s50, s72, s48\n"
    "s_addc_u32 s51, s73, 0\n"
    "s_mov_b32 m0, 9920\n"
    "s_nop 0\n"
    "global_load_lds_dwordx4 v8, s[50:51]\n"
    "s_mov_b32 m0, 11968\n"
    "s_nop 0\n"
    "global_load_lds_dwordx4 v9, s[50:51]\n"
    "s_mov_b32 m0, 14016\n"
    "s_nop 0\n"
    "global_load_lds_dwordx4 v10, s[50:51]\n"
    "s_mov_b32 m0, 16064\n"
    "s_nop 0\n"
    "global_load_lds_dwordx4 v11, s[50:51]\n"
    "s_mov_b32 m0, 8896\n"
    "s_nop 0\n"
    "global_load_lds_dwordx4 v12, s[50:51]\n"
    "s_mul_i32 s48, s75, 0\n"
    "s_add_u32 s48, s48, s74\n"
    "s_add_u32 s50, s72, s48\n"
    "s_addc_u32 s51, s73, 0\n"
    "s_mov_b32 m0, 1024\n"
    "s_nop 0\n"
    "global_load_lds_dwordx4 v8, s[50:51]\n"
    "s_mov_b32 m0, 3072\n"
    "s_nop 0\n"
    "global_load_lds_dwordx4 v9, s[50:51]\n"
    "s_mov_b32 m0, 5120\n"
    "s_nop 0\n"
    "global_load_lds_dwordx4 v10, s[50:51]\n"
    "s_mov_b32 m0, 7168\n"
    "s_nop 0\n"
    "global_load_lds_dwordx4 v11, s[50:51]\n"
    "s_mov_b32 m0, 0\n"
    "s_nop 0\n"
    "global_load_lds_dwordx4 v12, s[50:51]\n"
    "s_mul_i32 s48, s75, 3\n"
    "s_add_u32 s48, s48, s74\n"
    "s_add_u32 s50, s72, s48\n"
    "s_addc_u32 s51, s73, 0\n"
    "s_mov_b32 m0, 26688\n"
    "s_nop 0\n"
    "global_load_lds_dwordx4 v12, s[50:51]\n"
    "s_mul_i32 s48, s75, 4\n"
    "s_add_u32 s48, s48, s74\n"
    "s_add_u32 s50, s72, s48\n"
    "s_addc_u32 s51, s73, 0\n"
    "s_mov_b32 m0, 35584\n"
    "s_nop 0\n"
    "global_load_lds_dwordx4 v12, s[50:51]\n"
    "s_mul_i32 s48, s75, 5\n"
    "s_add_u32 s48, s48, s74\n"
    "s_add_u32 s50, s72, s48\n"
    "s_addc_u32 s51, s73, 0\n"
    "s_mov_b32 m0, 44480\n"
    "s_nop 0\n"
    "global_load_lds_dwordx4 v12, s[50:51]\n"
    "s_mul_i32 s48, s75, 6\n"
    "s_add_u32 s48, s48, s74\n"
    "s_add_u32 s50, s72, s48\n"
    "s_addc_u32 s51, s73, 0\n"
    "s_mov_b32 m0, 53376\n"
    "s_nop 0\n"
    "global_load_lds_dwordx4 v12, s[50:51]\n"
    "s_mul_i32 s48, s75, 7\n"
    "s_add_u32 s48, s48, s74\n"
    "s_add_u32 s50, s72, s48\n"
    "s_addc_u32 s51, s73, 0\n"
    "s_mov_b32 m0, 62272\n"
    "s_nop 0\n"
    "global_load_lds_dwordx4 v12, s[50:51]\n"
    "s_mul_i32 s48, s75, 8\n"
    "s_add_u32 s48, s48, s74\n"
    "s_add_u32 s50, s72, s48\n"
    "s_addc_u32 s51, s73, 0\n"
    "s_mov_b32 m0, 71168\n"
    "s_nop 0\n"
    "global_load_lds_dwordx4 v12, s[50:51]\n"
    "v_lshlrev_b32 v19, 20, v18\n"
    "v_add_u32 v19, 161408, v19\n"
    "v_mov_b32 v252, 1\n"
    "ds_write_b32 v19, v252 offset:304\n"
    "ds_write_b32 v19, v252 offset:312\n"
    "s_waitcnt vmcnt(0)\n"
    "v_mov_b32 v252, 4\n"
    "ds_write_b32 v19, v252 offset:0\n"
    "s_branch .La_join\n"
    ".La_l1:\n"
    "s_mul_i32 s48, s75, 2\n"
    "s_add_u32 s48, s48, s74\n"
    "s_add_u32 s50, s72, s48\n"
    "s_addc_u32 s51, s73, 0\n"
    "s_mov_b32 m0, 19840\n"
    "s_nop 0\n"
    "global_load_lds_dwordx4 v8, s[50:51]\n"
    "s_mov_b32 m0, 21888\n"
    "s_nop 0\n"
    "global_load_lds_dwordx4 v9, s[50:51]\n"
    "s_mov_b32 m0, 23936\n"
    "s_nop 0\n"
    "global_load_lds_dwordx4 v10, s[50:51]\n"
    "s_mov_b32 m0, 25568\n"
    "s_nop 0\n"
    "global_load_lds_dwordx4 v12, s[50:51]\n"
    "s_mul_i32 s48, s75, 1\n"
    "s_add_u32 s48, s48, s74\n"
    "s_add_u32 s50, s72, s48\n"
    "s_addc_u32 s51, s73, 0\n"
    "s_mov_b32 m0, 10944\n"
    "s_nop 0\n"
    "global_load_lds_dwordx4 v8, s[50:51]\n"
    "s_mov_b32 m0, 12992\n"
    "s_nop 0\n"
    "global_load_lds_dwordx4 v9, s[50:51]\n"
    "s_mov_b32 m0, 15040\n"
    "s_nop 0\n"
    "global_load_lds_dwordx4 v10, s[50:51]\n"
    "s_mov_b32 m0, 16672\n"
    "s_nop 0\n"
    "global_load_lds_dwordx4 v12, s[50:51]\n"
    "s_mul_i32 s48, s75, 0\n"
    "s_add_u32 s48, s48, s74\n"
    "s_add_u32 s50, s72, s48\n"
    "s_addc_u32 s51, s73, 0\n"
    "s_mov_b32 m0, 2048\n"
    "s_nop 0\n"
    "global_load_lds_dwordx4 v8, s[50:51]\n"
    "s_mov_b32 m0, 4096\n"
    "s_nop 0\n"
    "global_load_lds_dwordx4 v9, s[50:51]\n"
    "s_mov_b32 m0, 6144\n"
    "s_nop 0\n"
    "global_load_lds_dwordx4 v10, s[50:51]\n"
    "s_mov_b32 m0, 7776\n"
    "s_nop 0\n"
    "global_load_lds_dwordx4 v12, s[50:51]\n"
    "s_mul_i32 s48, s75, 3\n"
    "s_add_u32 s48, s48, s74\n"
    "s_add_u32 s50, s72, s48\n"
    "s_addc_u32 s51, s73, 0\n"
    "s_mov_b32 m0, 34464\n"
    "s_nop 0\n"
    "global_load_lds_dwordx4 v12, s[50:51]\n"
    "s_mul_i32 s48, s75, 4\n"
    "s_add_u32 s48, s48, s74\n"
    "s_add_u32 s50, s72, s48\n"
    "s_addc_u32 s51, s73, 0\n"
    "s_mov_b32 m0, 43360\n"
    "s_nop 0\n"
    "global_load_lds_dwordx4 v12, s[50:51]\n"
    "s_mul_i32 s48, s75, 5\n"
    "s_add_u32 s48, s48, s74\n"
    "s_add_u32 s50, s72, s48\n"
    "s_addc_u32 s51, s73, 0\n"
    "s_mov_b32 m0, 52256\n"
    "s_nop 0\n"
    "global_load_lds_dwordx4 v12, s[50:51]\n"
    "s_mul_i32 s48, s75, 6\n"
    "s_add_u32 s48, s48, s74\n"
    "s_add_u32 s50, s72, s48\n"
    "s_addc_u32 s51, s73, 0\n"
    "s_mov_b32 m0, 61152\n"
    "s_nop 0\n"
    "global_load_lds_dwordx4 v12, s[50:51]\n"
    "s_mul_i32 s48, s75, 7\n"
    "s_add_u32 s48, s48, s74\n"
    "s_add_u32 s50, s72, s48\n"
    "s_addc_u32 s51, s73, 0\n"
    "s_mov_b32 m0, 70048\n"
    "s_nop 0\n"
    "global_load_lds_dwordx4 v12, s[50:51]\n"
    "s_mul_i32 s48, s75, 8\n"
    "s_add_u32 s48, s48, s74\n"
    "s_add_u32 s50, s72, s48\n"
    "s_addc_u32 s51, s73, 0\n"
    "s_mov_b32 m0, 78944\n"
    "s_nop 0\n"
    "global_load_lds_dwordx4 v12, s[50:51]\n"
    "v_lshlrev_b32 v19, 20, v18\n"
    "v_add_u32 v19, 161412, v19\n"
    "v_mov_b32 v252, 1\n"
    "ds_write_b32 v19, v252 offset:304\n"
    "ds_write_b32 v19, v252 offset:312\n"
    "s_waitcnt vmcnt(0)\n"
    "v_mov_b32 v252, 4\n"
    "ds_write_b32 v19, v252 offset:0\n"
    "s_branch .La_join\n"
    ".Lb2:\n"
    "s_sub_u32 s52, s52, 1\n"
    "s_cmp_lt_i32 s52, 0\n"
    "s_cbranch_scc1 .Lb2b\n"
    "ds_read_b32 v253, v6 offset:304\n"
    "s_waitcnt lgkmcnt(0)\n"
    "v_cmp_gt_u32 vcc, 1, v253\n"
    "s_cbranch_vccnz .Lb2\n"
    "s_branch .Lb2b\n"
    ".Lb3:\n"
    "s_sub_u32 s52, s52, 1\n"
    "s_cmp_lt_i32 s52, 0\n"
    "s_cbranch_scc1 .Lb3b\n"
    "ds_read_b32 v253, v6 offset:0\n"
    "s_waitcnt lgkmcnt(0)\n"
    "v_cmp_gt_u32 vcc, 4, v253\n"
    "s_cbranch_vccnz .Lb3\n"
    "s_branch .Lb3b\n"
    ".La1:\n"
    "s_sub_u32 s52, s52, 1\n"
    "s_cmp_lt_i32 s52, 0\n"
    "s_cbranch_scc1 .La1b\n"
    "ds_read_b32 v253, v6 offset:0\n"
    "s_waitcnt lgkmcnt(0)\n"
    "v_cmp_gt_u32 vcc, s54, v253\n"
    "s_cbranch_vccnz .La1\n"
    "s_branch .La1b\n"
    ".La_fill:\n"
    "s_mov_b32 s69, 0\n"
    "ds_read_b64_tr_b16 v[104:105], v200 offset:0\n"
    "ds_read_b64_tr_b16 v[106:107], v201 offset:0\n"
    "ds_read_b64_tr_b16 v[108:109], v202 offset:0\n"
    "ds_read_b64_tr_b16 v[110:111], v203 offset:0\n"
    "ds_read_b64_tr_b16 v[112:113], v200 offset:32\n"
    "ds_read_b64_tr_b16 v[114:115], v201 offset:32\n"
    "ds_read_b64_tr_b16 v[116:117], v202 offset:32\n"
    "ds_read_b64_tr_b16 v[118:119], v203 offset:32\n"
    "ds_read_b64_tr_b16 v[120:121], v200 offset:64\n"
    "ds_read_b64_tr_b16 v[122:123], v201 offset:64\n"
    "ds_read_b64_tr_b16 v[124:125], v202 offset:64\n"
    "ds_read_b64_tr_b16 v[126:127], v203 offset:64\n"
    "ds_read_b64_tr_b16 v[128:129], v200 offset:96\n"
    "ds_read_b64_tr_b16 v[130:131], v201 offset:96\n"
    "ds_read_b64_tr_b16 v[132:133], v202 offset:96\n"
    "ds_read_b64_tr_b16 v[134:135], v203 offset:96\n"
    "ds_read_b64_tr_b16 v[136:137], v200 offset:128\n"
    "ds_read_b64_tr_b16 v[138:139], v201 offset:128\n"
    "ds_read_b64_tr_b16 v[140:141], v202 offset:128\n"
    "ds_read_b64_tr_b16 v[142:143], v203 offset:128\n"
    "ds_read_b64_tr_b16 v[144:145], v200 offset:160\n"
    "ds_read_b64_tr_b16 v[146:147], v201 offset:160\n"
    "ds_read_b64_tr_b16 v[148:149], v202 offset:160\n"
    "ds_read_b64_tr_b16 v[150:151], v203 offset:160\n"
    "ds_read_b64_tr_b16 v[152:153], v200 offset:192\n"
    "ds_read_b64_tr_b16 v[154:155], v201 offset:192\n"
    "ds_read_b64_tr_b16 v[156:157], v202 offset:192\n"
    "ds_read_b64_tr_b16 v[158:159], v203 offset:192\n"
    "ds_read_b64_tr_b16 v[160:161], v200 offset:224\n"
    "ds_read_b64_tr_b16 v[162:163], v201 offset:224\n"
    "ds_read_b64_tr_b16 v[164:165], v202 offset:224\n"
    "ds_read_b64_tr_b16 v[166:167], v203 offset:224\n"
    "ds_read_b64_tr_b16 v[168:169], v200 offset:256\n"
    "ds_read_b64_tr_b16 v[170:171], v201 offset:256\n"
    "ds_read_b64_tr_b16 v[172:173], v202 offset:256\n"
    "ds_read_b64_tr_b16 v[174:175], v203 offset:256\n"
    "ds_read_b64_tr_b16 v[176:177], v200 offset:288\n"
    "ds_read_b64_tr_b16 v[178:179], v201 offset:288\n"
    "ds_read_b64_tr_b16 v[180:181], v202 offset:288\n"
    "ds_read_b64_tr_b16 v[182:183], v203 offset:288\n"
    "ds_read_b64_tr_b16 v[184:185], v200 offset:320\n"
    "ds_read_b64_tr_b16 v[186:187], v201 offset:320\n"
    "ds_read_b64_tr_b16 v[188:189], v202 offset:320\n"
    "ds_read_b64_tr_b16 v[190:191], v203 offset:320\n"
    "ds_read_b64_tr_b16 v[192:193], v200 offset:352\n"
    "ds_read_b64_tr_b16 v[194:195], v201 offset:352\n"
    "ds_read_b64_tr_b16 v[196:197], v202 offset:352\n"
    "ds_read_b64_tr_b16 v[198:199], v203 offset:352\n"
    "s_branch .La_filled\n"
    ".La2:\n"
    "s_sub_u32 s52, s52, 1\n"
    "s_cmp_lt_i32 s52, 0\n"
    "s_cbranch_scc1 .La2b\n"
    "ds_read_b32 v254, v6 offset:16\n"
    "s_waitcnt lgkmcnt(0)\n"
    "v_cmp_gt_u32 vcc, s55, v254\n"
    "s_cbranch_vccnz .La2\n"
    "s_branch .La2b\n"
    ".La3:\n"
    "s_sub_u32 s52, s52, 1\n"
    "s_cmp_lt_i32 s52, 0\n"
    "s_cbranch_scc1 .La3b\n"
    "ds_read_b32 v253, v6 offset:32\n"
    "s_waitcnt lgkmcnt(0)\n"
    "v_cmp_gt_i32 vcc, s56, v253\n"
    "s_cbranch_vccnz .La3\n"
    "s_branch .La3b\n"
    ".La5:\n"
    "s_sub_u32 s52, s52, 1\n"
    "s_cmp_lt_i32 s52, 0\n"
    "s_cbranch_scc1 .La5b\n"
    "ds_read_b32 v200, v6 offset:0\n"
    "s_waitcnt lgkmcnt(0)\n"
    "v_cmp_gt_u32 vcc, s54, v200\n"
    "s_cbranch_vccnz .La5\n"
    "s_branch .La5b\n"
    ".La6:\n"
    "s_sub_u32 s52, s52, 1\n"
    "s_cmp_lt_i32 s52, 0\n"
    "s_cbranch_scc1 .La6b\n"
    "ds_read_b32 v254, v6 offset:16\n"
    "s_waitcnt lgkmcnt(0)\n"
    "v_cmp_gt_u32 vcc, s55, v254\n"
    "s_cbranch_vccnz .La6\n"
    "s_branch .La6b\n"
    ".La4:\n"
    "v_cvt_pk_bf16_f32 v128, v128, v129\n"
    "v_cvt_pk_bf16_f32 v129, v130, v131\n"
    "v_cvt_pk_bf16_f32 v130, v132, v133\n"
    "v_cvt_pk_bf16_f32 v131, v134, v135\n"
    "ds_write_b128 v4, v[128:131] offset:3072\n"
    "v_cvt_pk_bf16_f32 v136, v136, v137\n"
    "v_cvt_pk_bf16_f32 v137, v138, v139\n"
    "v_cvt_pk_bf16_f32 v138, v140, v141\n"
    "v_cvt_pk_bf16_f32 v139, v142, v143\n"
    "ds_write_b128 v4, v[136:139] offset:4096\n"
    "v_cvt_pk_bf16_f32 v144, v144, v145\n"
    "v_cvt_pk_bf16_f32 v145, v146, v147\n"
    "v_cvt_pk_bf16_f32 v146, v148, v149\n"
    "v_cvt_pk_bf16_f32 v147, v150, v151\n"
    "ds_write_b128 v4, v[144:147] offset:5120\n"
    "ds_write_b32 v7, v252 offset:128\n"
    "s_cmp_ge_u32 s45, 12\n"
    "s_cbranch_scc1 .La_end\n"
    "ds_read_b32 v200, v6 offset:0\n"
    "s_waitcnt lgkmcnt(0)\n"
    "v_cmp_gt_u32 vcc, s54, v200\n"
    "s_cbranch_vccnz .La5\n"
    ".La5b:\n"
    "ds_read_b32 v254, v6 offset:16\n"
    "s_waitcnt lgkmcnt(0)\n"
    "v_cmp_gt_u32 vcc, s55, v254\n"
    "s_cbranch_vccnz .La6\n"
    ".La6b:\n"
    "ds_read_b128 v[104:107], v3 offset:0\n"
    "ds_read_b128 v[152:155], v244 offset:0\n"
    "ds_read_b128 v[108:111], v3 offset:1024\n"
    "ds_read_b128 v[156:159], v244 offset:64\n"
    "ds_read_b128 v[112:115], v3 offset:2048\n"
    "ds_read_b128 v[160:163], v244 offset:128\n"
    "ds_read_b128 v[116:119], v3 offset:3072\n"
    "ds_read_b128 v[164:167], v244 offset:192\n"
    "ds_read_b128 v[120:123], v3 offset:4096\n"
    "ds_read_b128 v[124:127], v3 offset:5120\n"
    "ds_read_b128 v[128:131], v244 offset:256\n"
    "ds_read_b128 v[132:135], v244 offset:320\n"
    "ds_read_b128 v[136:139], v245 offset:0\n"
    "ds_read_b128 v[140:143], v245 offset:64\n"
    "s_branch .La4j\n"
    ".La7:\n"
    "s_sub_u32 s52, s52, 1\n"
    "s_cmp_lt_i32 s52, 0\n"
    "s_cbranch_scc1 .La7b\n"
    "ds_read_b32 v253, v6 offset:32\n"
    "s_waitcnt lgkmcnt(0)\n"
    "v_cmp_gt_i32 vcc, s56, v253\n"
    "s_cbranch_vccnz .La7\n"
    "s_branch .La7b\n"
    ".La9:\n"
    "s_sub_u32 s52, s52, 1\n"
    "s_cmp_lt_i32 s52, 0\n"
    "s_cbranch_scc1 .La9b\n"
    "ds_read_b32 v200, v6 offset:0\n"
    "s_waitcnt lgkmcnt(0)\n"
    "v_cmp_gt_u32 vcc, s54, v200\n"
    "s_cbranch_vccnz .La9\n"
    "s_branch .La9b\n"
    ".La10:\n"
    "s_sub_u32 s52, s52, 1\n"
    "s_cmp_lt_i32 s52, 0\n"
    "s_cbranch_scc1 .La10b\n"
    "ds_read_b32 v254, v6 offset:16\n"
    "s_waitcnt lgkmcnt(0)\n"
    "v_cmp_gt_u32 vcc, s55, v254\n"
    "s_cbranch_vccnz .La10\n"
    "s_branch .La10b\n"
    ".La8:\n"
    "v_cvt_pk_bf16_f32 v32, v32, v33\n"
    "v_cvt_pk_bf16_f32 v33, v34, v35\n"
    "v_cvt_pk_bf16_f32 v34, v36, v37\n"
    "v_cvt_pk_bf16_f32 v35, v38, v39\n"
    "ds_write_b128 v4, v[32:35] offset:3072\n"
    "v_cvt_pk_bf16_f32 v40, v40, v41\n"
    "v_cvt_pk_bf16_f32 v41, v42, v43\n"
    "v_cvt_pk_bf16_f32 v42, v44, v45\n"
    "v_cvt_pk_bf16_f32 v43, v46, v47\n"
    "ds_write_b128 v4, v[40:43] offset:4096\n"
    "v_cvt_pk_bf16_f32 v48, v48, v49\n"
    "v_cvt_pk_bf16_f32 v49, v50, v51\n"
    "v_cvt_pk_bf16_f32 v50, v52, v53\n"
    "v_cvt_pk_bf16_f32 v51, v54, v55\n"
    "ds_write_b128 v4, v[48:51] offset:5120\n"
    "ds_write_b32 v7, v252 offset:128\n"
    "s_cmp_ge_u32 s45, 12\n"
    "s_cbranch_scc1 .La_end\n"
    "ds_read_b32 v200, v6 offset:0\n"
    "s_waitcnt lgkmcnt(0)\n"
    "v_cmp_gt_u32 vcc, s54, v200\n"
    "s_cbranch_vccnz .La9\n"
    ".La9b:\n"
    "ds_read_b32 v254, v6 offset:16\n"
    "s_waitcnt lgkmcnt(0)\n"
    "v_cmp_gt_u32 vcc, s55, v254\n"
    "s_cbranch_vccnz .La10\n"
    ".La10b:\n"
    "ds_read_b128 v[8:11], v3 offset:0\n"
    "ds_read_b128 v[56:59], v244 offset:0\n"
    "ds_read_b128 v[12:15], v3 offset:1024\n"
    "ds_read_b128 v[60:63], v244 offset:64\n"
    "ds_read_b128 v[16:19], v3 offset:2048\n"
    "ds_read_b128 v[64:67], v244 offset:128\n"
    "ds_read_b128 v[20:23], v3 offset:3072\n"
    "ds_read_b128 v[68:71], v244 offset:192\n"
    "ds_read_b128 v[24:27], v3 offset:4096\n"
    "ds_read_b128 v[28:31], v3 offset:5120\n"
    "ds_read_b128 v[32:35], v244 offset:256\n"
    "ds_read_b128 v[36:39], v244 offset:320\n"
    "ds_read_b128 v[40:43], v245 offset:0\n"
    "ds_read_b128 v[44:47], v245 offset:64\n"
    "s_branch .La8j\n"
    ".La_last:\n"
            :: "{v[8:11]}"(xo), "{v12}"(xo4), "{v13}"(xq[0]), "{v14}"(xq[1]), "{v15}"(xq[2]), "{v16}"(xq[3]), "{v17}"(xq4), "{v18}"(lane),
               "{s70}"(UNI(wave >= 5 && t12 && (1 == 2 || (1 == 1 && (bid & 4))) ? wave - 3 : wave == 7)), "{s[72:73]}"(xb), "{s74}"(UNI(rm.o * IROWB)), "{s75}"(UNI(rm.d * IROWB)),
               "{v[0:3]}"(va), "{v[4:7]}"(vb), "{v[232:235]}"(cbM), "{v[236:239]}"(cbF), "{v[240:243]}"(cbL),
               "{s60}"(UNI(slots)), "{s61}"(UNI(slots >> 32)), "{s45}"(UNI(n0)), "{s54}"(UNI(u0 + 4)), "{s63}"(UNI((unsigned)(size_t)smem + LF + 320 + 64 * sl)), "{s66}"(UNI(sl)),
               "{s46}"(-0.10411754112863712f), "{s47}"(0.10411754112863712f)
            : "memory", "v254", "s48", "s49", "s50", "s51", "s52", "s53", "s55", "s56", "s57", "s58", "s59", "s62", "s67", "s68", "s69", "s88", "s89", "s90", "s91", "s98", "s99", "s92", "s93", "s94", "s95", "vcc", "scc");
    }
}

extern "C" void kernel_launch(void* const* d_in, const int* in_sizes, int n_in,
                              void* d_out, int out_size, void* d_ws, size_t ws_size,
                              hipStream_t stream) {
    na_main<<<256, 512, 0, stream>>>((const u16*)d_in[0], (const u16*)d_in[1], (const u16*)d_in[2], (u16*)d_out, (char*)d_ws + 0);
}
```
